```python
import torch

def _softmax(x, axis=-1):
    return torch.softmax(x, dim=axis)
def _gelu(x):
    return torch.nn.functional.gelu(x, approximate="tanh")
def _silu(x):
    return torch.nn.functional.silu(x)
def _sigmoid(x):
    return torch.sigmoid(x)
def _layernorm(x, axis=-1, eps=1e-5):
    m = x.mean(dim=axis, keepdim=True)
    v = x.var(dim=axis, keepdim=True, unbiased=False)
    return (x - m) / torch.sqrt(v + eps)
def _scatter_add(tgt, idx, src, axis=0):
    return tgt.clone().index_add_(axis, idx, src)
def _segment_sum(src, seg, n):
    out = torch.zeros((n,) + src.shape[1:], dtype=src.dtype)
    return out.index_add_(0, seg, src)
def _pad(x, pw):
    flat = tuple(v for pair in reversed(pw) for v in pair)
    return torch.nn.functional.pad(x, flat)

kernel_name = 'standalone__molecular_angular_gemnet__graph'

def setup_inputs():

    _w = (lambda a: (lambda t: t.to(torch.bfloat16) if t.is_floating_point() else t)(torch.as_tensor(a)))
    N, deg, De, L = 12500, 64, 4, 1
    E = N * deg
    T = E * deg
    NR = 8
    dist = torch.rand(E).to(float)
    angle = (torch.rand(T) * torch.pi).to(float)
    centers = torch.linspace(0.0, 1.0, NR).to(float)
    ang_freq = torch.arange(1, L + 1).to(float)
    return {
        "m": _w((lambda *s: torch.randn(*s, dtype=torch.bfloat16) * 0.05)(E, De)),
        "idx_in": _w((lambda lo, hi, *s: torch.randint(lo, hi, s))(0, E, T).to(torch.int64)),
        "idx_out": _w((lambda lo, hi, *s: torch.randint(lo, hi, s))(0, E, T).to(torch.int64)),
        "dist": _w(dist),
        "angle": _w(angle),
        "centers": _w(centers),
        "ang_freq": _w(ang_freq),
        "w_rbf": _w((lambda *s: torch.randn(*s, dtype=torch.bfloat16) * 0.2)(NR, De)),
        "w_sbf": _w((lambda *s: torch.randn(*s, dtype=torch.bfloat16) * 0.2)(NR * L, De)),
        "w_down": _w((lambda *s: torch.randn(*s, dtype=torch.bfloat16) * 0.2)(De, De)),
        "w_up": _w((lambda *s: torch.randn(*s, dtype=torch.bfloat16) * 0.2)(De, De)),
    }

def reference(m, idx_in, idx_out, dist, angle, centers, ang_freq, w_rbf, w_sbf, w_down, w_up):
    E, De = m.shape[0], m.shape[1]
    NR = centers.shape[0]
    L = ang_freq.shape[0]
    rbf = torch.exp(-10.0 * (dist[:, None] - centers[None, :]) ** 2)
    cbf = torch.cos(ang_freq[None, :] * angle[:, None])
    rbf_t = rbf[idx_out]
    sbf = torch.reshape(rbf_t[:, :, None] * cbf[:, None, :], (-1, NR * L))
    x = (m[idx_in] @ w_down) * (sbf @ w_sbf)
    agg = _segment_sum(x, idx_out, E)
    return m + (agg @ w_up) * (rbf @ w_rbf)
```

```cpp
#include <hip/hip_runtime.h>
#include <hip/hip_bf16.h>
#include <stdint.h>
#pragma clang diagnostic ignored "-Winline-asm"

typedef unsigned int u32x4 __attribute__((ext_vector_type(4)));
typedef unsigned int u32x2 __attribute__((ext_vector_type(2)));
typedef unsigned short u16x2 __attribute__((ext_vector_type(2)));
typedef _Float16 f16x2 __attribute__((ext_vector_type(2)));
typedef __bf16 bf16x2 __attribute__((ext_vector_type(2)));
typedef float f32x2 __attribute__((ext_vector_type(2)));
typedef unsigned long long u64;

static __device__ __forceinline__ float bf2f(uint32_t bits16) { return __uint_as_float(bits16 << 16); }
static __device__ __forceinline__ uint32_t f2bf(float f) {
    uint32_t u = __float_as_uint(f);
    if ((u & 0x7fffffffu) > 0x7f800000u) return (u >> 16) | 0x40;
    return (u + 0x7fffu + ((u >> 16) & 1u)) >> 16;
}
static __device__ __forceinline__ float h2f(uint32_t bits16) { return (float)__builtin_bit_cast(_Float16, (unsigned short)bits16); }

#define NB 2048
#define WNULL 0x80000000u
#define CSTEP 0x2aabu
#define CINIT 205u
#define CHV (12u * CSTEP)
#define SLOTO 25088
#define SLOTI 12544
#define EMAX (SLOTO * 32)
#define NT 1024
#define NWG 256
#define KMAX 4
#define NTAIL 6
#define CST 1025u
#define TST 257u
#define TBASE (NB * CST)
#define TPL (NB * TST)
#define LBASE (TBASE + 3u * TPL)
#define GR 2048
#define NTS 5
#define M_DONE 0
#define M_CNT (32 * 64)
#define M_E0 (M_CNT + 2 * NWG)
#define M_WORDS (M_E0 + 64)
#define AUX_NT 2
#define AUX_SC1 16

static __device__ __forceinline__ uint32_t row_max(uint32_t x) {
    x = max(x, (uint32_t)__builtin_amdgcn_update_dpp(0, x, 0x111, 0xf, 0xf, false));
    x = max(x, (uint32_t)__builtin_amdgcn_update_dpp(0, x, 0x112, 0xf, 0xf, false));
    x = max(x, (uint32_t)__builtin_amdgcn_update_dpp(0, x, 0x114, 0xf, 0xf, false));
    return max(x, (uint32_t)__builtin_amdgcn_update_dpp(0, x, 0x118, 0xf, 0xf, false));
}
static __device__ __forceinline__ uint32_t wave_max(uint32_t x) {
    x = row_max(x);
    x = max(x, (uint32_t)__builtin_amdgcn_update_dpp(0, x, 0x142, 0xa, 0xf, false));
    x = max(x, (uint32_t)__builtin_amdgcn_update_dpp(0, x, 0x143, 0xc, 0xf, false));
    return __builtin_amdgcn_readlane(x, 63);
}
static __device__ __forceinline__ float row_sum(float x) {
    x += __int_as_float(__builtin_amdgcn_update_dpp(0, __float_as_int(x), 0x111, 0xf, 0xf, false));
    x += __int_as_float(__builtin_amdgcn_update_dpp(0, __float_as_int(x), 0x112, 0xf, 0xf, false));
    x += __int_as_float(__builtin_amdgcn_update_dpp(0, __float_as_int(x), 0x114, 0xf, 0xf, false));
    return x + __int_as_float(__builtin_amdgcn_update_dpp(0, __float_as_int(x), 0x118, 0xf, 0xf, false));
}
static __device__ __forceinline__ float wave_sum(float x) {
    x = row_sum(x);
    x += __int_as_float(__builtin_amdgcn_update_dpp(0, __float_as_int(x), 0x142, 0xa, 0xf, false));
    x += __int_as_float(__builtin_amdgcn_update_dpp(0, __float_as_int(x), 0x143, 0xc, 0xf, false));
    return __int_as_float(__builtin_amdgcn_readlane(__float_as_int(x), 63));
}
static __device__ __forceinline__ uint32_t bfi(uint32_t mask, uint32_t a, uint32_t b) {
    return __builtin_amdgcn_bitop3_b32(mask, a, b, 0xca);
}
static __device__ __forceinline__ void opaque(int& x) { asm volatile("" : "+v"(x)); }
template <uint32_t C> static __device__ __forceinline__ uint32_t vconst() {
    uint32_t r;
    asm volatile("v_mov_b32 %0, %1" : "=v"(r) : "n"(C));
    return r;
}
static __device__ __forceinline__ uint32_t pk_bf16(float a, float b) {
    const f32x2 v = {a, b};
    return __builtin_bit_cast(uint32_t, __builtin_convertvector(v, bf16x2));
}
static __device__ __forceinline__ void pin(uint32_t& x) { asm volatile("" : "+v"(x)); }

#define ST_DECL
#define ST(i)
#define ST_OUT(off)

static __device__ __forceinline__ void md_of(u32x2 v, const float* w, uint32_t* r) {
    float a0 = bf2f(v.x & 0xffff), a1 = bf2f(v.x >> 16), a2 = bf2f(v.y & 0xffff), a3 = bf2f(v.y >> 16);
#pragma unroll
    for (int j = 0; j < 4; j++) r[j] = f2bf(a0 * w[j] + a1 * w[4 + j] + a2 * w[8 + j] + a3 * w[12 + j]);
}
static __device__ __forceinline__ void load_w(const uint16_t* __restrict__ wdown, float* w) {
    u32x4 wv0 = *(const u32x4*)wdown, wv1 = *(const u32x4*)(wdown + 8);
#pragma unroll
    for (int i = 0; i < 4; i++) {
        w[2 * i] = bf2f(wv0[i] & 0xffffu); w[2 * i + 1] = bf2f(wv0[i] >> 16);
        w[8 + 2 * i] = bf2f(wv1[i] & 0xffffu); w[8 + 2 * i + 1] = bf2f(wv1[i] >> 16);
    }
}
#define LDS __attribute__((address_space(3)))
#define P_CNT 0
#define P_FLAG ((NB + 64) * 4)
#define P_LST (P_FLAG + 256)
#define LCAP 32
#define P_TAB (P_LST + 4 * LCAP * 16)
#define P_BUF (P_TAB + NB * 4)
#define P_LDS (P_BUF + NB * 64)
#define PD 2
#define LPOS 0xa98763
#define ABL 0
#define SETP(n)
#define RMAXREV 40.0f
#define HB 88
#define HREG(S, D) (HB + ((S) & 3) * 10 + (((S) >> 2) + (D)) * 2)

template <int N> struct IC { static constexpr int value = N; };
struct Raw { u32x4 o[2], i[2]; u32x2 a[2]; };
static __device__ __forceinline__ uint32_t lds_ld(uint32_t a) { return *(const LDS uint32_t*)(uintptr_t)a; }
static __device__ __forceinline__ void lds_st(uint32_t a, uint32_t v) { *(LDS uint32_t*)(uintptr_t)a = v; }
static __device__ __forceinline__ void lds_or(uint32_t a, uint32_t v) {
    __hip_atomic_fetch_or((LDS uint32_t*)(uintptr_t)a, v, __ATOMIC_RELAXED, __HIP_MEMORY_SCOPE_WORKGROUP);
}
static __device__ __forceinline__ uint32_t lds_add(uint32_t a, uint32_t v) {
    return __hip_atomic_fetch_add((LDS uint32_t*)(uintptr_t)a, v, __ATOMIC_RELAXED, __HIP_MEMORY_SCOPE_WORKGROUP);
}
static __device__ __forceinline__ uint32_t slot_w(uint32_t ca, uint32_t cv) {
    return (ca << 4) | __builtin_amdgcn_bitop3_b32(cv >> 11, ca, 0x3cu, 0x28);
}
static __device__ __forceinline__ uint32_t slot_x2(uint32_t aw, uint32_t ca) {
    return __builtin_amdgcn_bitop3_b32(aw, ca, 0xcu, 0x72);
}
static __device__ __forceinline__ uint32_t slot_x(uint32_t aw) { return slot_x2(aw, aw >> 4); }
template <int R> static __device__ __forceinline__ u32x2 hget() {
    u32x2 d;
    asm volatile("v_mov_b64 %0, v[%1:%2]" : "=v"(d) : "n"(R), "n"(R + 1));
    return d;
}
static __device__ __forceinline__ u32x2 hget_at(int r) {
    u32x2 d;
    asm volatile("s_set_gpr_idx_on %2, gpr_idx(SRC0)\n\ts_nop 1\n\tv_mov_b32 %0, v88\n\tv_mov_b32 %1, v89\n\ts_set_gpr_idx_off"
                 : "=&v"(d.x), "=&v"(d.y) : "s"(r));
    return d;
}
static __device__ __forceinline__ void hset_at(int r, u32x2 d) {
    asm volatile("s_set_gpr_idx_on %2, gpr_idx(DST)\n\ts_nop 1\n\tv_mov_b32 v88, %0\n\tv_mov_b32 v89, %1\n\ts_set_gpr_idx_off"
                 :: "v"(d.x), "v"(d.y), "s"(r));
}

template <int S, int D> static __device__ __forceinline__ uint32_t take(uint32_t V, uint32_t u16, uint32_t la, u32x2 nul) {
    const uint32_t a = __builtin_amdgcn_bitop3_b32(V, u16, 15u, 0x54);
    asm volatile("v_mov_b64 v[%4:%4+1], v[%3:%3+1]\n\tv_cmpx_ne_u32 vcc, -16, %0\n\tds_wrxchg_rtn_b64 v[%4:%4+1], %1, %2 offset:%5\n\ts_mov_b64 exec, -1"
                 :: "v"(a), "v"(la), "v"(nul), "n"(HREG(S, D)), "n"(HREG(S, 1 - D)), "n"(S * 256) : "memory", "vcc");
    return a;
}
#define SK0 "s_bitcmp1_b32 %3, %4\n\ts_cbranch_scc0 1f\n\t"
#define SK1 "\n1:"
template <int S, int W> static __device__ __forceinline__ void put(u32x4 rsA, uint32_t ad, uint32_t pm) {
    if (S >= 16 - NTS) asm volatile(SK0 "s_waitcnt lgkmcnt(%5)\n\tbuffer_store_dwordx4 v[%2:%2+3], %0, %1, 0 offen nt\n\ts_nop 1" SK1 :: "v"(ad), "s"(rsA), "n"(HREG(S, 0)), "s"(pm), "n"(2 * S), "n"(W) : "memory", "scc");
    else asm volatile(SK0 "s_waitcnt lgkmcnt(%5)\n\tbuffer_store_dwordx4 v[%2:%2+3], %0, %1, 0 offen sc1\n\ts_nop 1" SK1 :: "v"(ad), "s"(rsA), "n"(HREG(S, 0)), "s"(pm), "n"(2 * S), "n"(W) : "memory", "scc");
}

__global__ __launch_bounds__(NT) __attribute__((amdgpu_num_vgpr(HB / 2))) void part_kernel(const int* __restrict__ iin, const int* __restrict__ iout,
                                                  const uint16_t* __restrict__ ang, const uint16_t* __restrict__ freq,
                                                  const u32x2* __restrict__ m, const uint16_t* __restrict__ wdown,
                                                  u32x2* __restrict__ stat, uint32_t* __restrict__ mdc,
                                                  unsigned* __restrict__ meta, uint32_t* __restrict__ tcnt,
                                                  char* __restrict__ recs,
                                                  int T, int E, int nfull, int grem, int ks, int lcap) {
    ST_DECL
    const int tid0 = threadIdx.x;
    const int wv = __builtin_amdgcn_readfirstlane(tid0 >> 6);
    const int G = T >> 2;
    const int pos = (int)((blockIdx.x & 7) << 5) | (int)(blockIdx.x >> 3);
    float frr;
    const uint32_t gbase = (LBASE + (uint32_t)(NWG * lcap)) * 128u;
    const uint32_t rbytes = gbase + (uint32_t)(NWG * lcap) * 2u;
    const __amdgpu_buffer_rsrc_t rsR = __builtin_amdgcn_make_buffer_rsrc(recs, 0, (int)rbytes, 0x00020000);
    const u32x4 rsA = {(uint32_t)(uintptr_t)recs, (uint32_t)((uintptr_t)recs >> 32) & 0xffffu, (ABL & 2) ? 0u : rbytes, 0x00020000u};
    const int nround = nfull + (grem > 0 ? 1 : 0);
    const uint32_t g2 = 2u * (uint32_t)ks;
    const uint32_t spec0 = (LBASE + (blockIdx.x + 1u) * (uint32_t)lcap) * 128u - 4u;
    auto rstart = [&](int r) __attribute__((always_inline)) -> int { return r < nfull ? (r * NWG + ((pos + r * 37) & (NWG - 1))) * GR : nfull * NWG * GR + pos * grem; };
    auto rcount = [&](int r) __attribute__((always_inline)) -> int {
        return r < nfull ? GR : (r == nfull ? max(min(grem, G - (nfull * NWG * GR + pos * grem)), 0) : 0);
    };
    uint32_t t8 = (uint32_t)tid0 * 8u;
    auto load_one = [&](Raw& r, int st, int ng, int idx) __attribute__((always_inline)) {
        const int j = idx < 4 ? idx >> 1 : idx & 1;
        const int n = st + ng;
        const uint32_t g0 = (uint32_t)(st + j * NT);
        __builtin_amdgcn_sched_barrier(0);
        if (idx >= 4) r.a[j] = __builtin_amdgcn_raw_buffer_load_b64(__builtin_amdgcn_make_buffer_rsrc((void*)ang, 0, n * 8, 0x00020000), t8, g0 * 8u, (ABL & 1) ? 0 : AUX_NT);
        else if (idx & 1) r.i[j] = __builtin_amdgcn_raw_buffer_load_b128(__builtin_amdgcn_make_buffer_rsrc((void*)iin, 0, n * 16, 0x00020000), t8 + t8, g0 * 16u, (ABL & 1) ? 0 : AUX_NT);
        else r.o[j] = __builtin_amdgcn_raw_buffer_load_b128(__builtin_amdgcn_make_buffer_rsrc((void*)iout, 0, n * 16, 0x00020000), t8 + t8, g0 * 16u, (ABL & 1) ? 0 : AUX_NT);
        __builtin_amdgcn_sched_barrier(0);
    };
    uint32_t la, tw;
    u32x2 nul;
    uint32_t U[2];
    {
        const uint32_t t = (uint32_t)tid0;
        la = P_BUF + ((t >> 5) << 12) + ((t & 0x18u) << 3) + ((t & 3u) << 4) + ((t & 4u) << 1);
        tw = P_TAB + ((t >> 5) << 8) + ((t & 1u) << 7) + ((t & 0x1eu) << 1);
        const uint32_t q = ((t >> 3) ^ (t >> 1 & 2u)) & 3u;
        nul.x = (0x7u >> q) << 31; nul.y = (0xbu >> q) << 31;
        U[0] = ((((t << 6) & 0x7c0u) | (t >> 5)) * CST + blockIdx.x) * 128u + (0x80000000u - 16384u) + 8u - g2;
        U[1] = U[0] + 32u * CST * 128u;
    }
    auto log_line = [&](bool lg, uint32_t b) __attribute__((always_inline)) -> uint32_t {
        uint32_t lp = 0u;
        if (lg && (t8 & 0x38u) == 0u) {
            lp = blockIdx.x * (uint32_t)lcap + lds_add(vconst<P_FLAG + 4>(), vconst<1>());
            __builtin_amdgcn_raw_buffer_store_b16((uint16_t)b, rsR, gbase + lp * 2u, 0, 0);
        }
        return LBASE + (uint32_t)__builtin_amdgcn_ds_bpermute((int)((t8 >> 1) & 0xe0u), (int)lp);
    };
    auto general = [&](int D, bool all, u64 sm) __attribute__((always_inline)) {
        uint32_t todo = __builtin_amdgcn_readfirstlane((uint32_t)sm | (uint32_t)(sm >> 32));
#pragma unroll 1
        while (todo) {
            const uint32_t S = (uint32_t)__builtin_ctz(todo) >> 1;
            todo &= ~(3u << (2u * S));
            pin(t8); pin(la);
            const uint32_t V = lds_ld(P_TAB + (t8 & ~63u) + S * 4u), g = V & 15u;
            const uint32_t col = S * 128u + ((t8 >> 1) & 0x60u) + (t8 >> 8);
            const bool act = all ? V != 0xffffffffu : (V & 0x80000009u) == 9u;
            const int hr = HREG((int)S, D) - HB;
            uint32_t ln = log_line(act && g > 8u + NTAIL, col);
            if (act) {
                const u32x2 N = *(const LDS u32x2*)(uintptr_t)(la + S * 256u);
                *(LDS u32x2*)(uintptr_t)(la + S * 256u) = nul;
                lds_st(P_TAB + (t8 & ~63u) + S * 4u, vconst<0xffffffffu>());
                if (!(g & 1u)) hset_at(hr, N);
                else {
                    uint32_t a, st;
                    if (g < 8u) { a = (V & ~15u) + ((t8 & 0x38u) << 1); st = 8u; }
                    else {
                        if (g <= 8u + NTAIL) ln = TBASE + ((g - 8u) >> 1) * TPL + col * TST + blockIdx.x;
                        a = ln * 128u + (la & 0x38u); st = 64u;
                    }
                    __builtin_amdgcn_raw_buffer_store_b64(hget_at(hr), rsR, a, 0, AUX_SC1);
                    __builtin_amdgcn_raw_buffer_store_b64(N, rsR, a + st, 0, AUX_SC1);
                }
            }
        }
    };
    unsigned epoch = 0;
    uint32_t pm = 0u;
    u64 fm = 0ull, sm = 0ull;
    auto look = [&](u32x2 c) __attribute__((always_inline)) {
        uint32_t V[2];
        const bool late = __builtin_amdgcn_ballot_w64(((U[0] | U[1]) & 8u) != 0u) != 0ull;
        epoch++;
        asm volatile("v_mov_b32 %0, -1\n\tv_mov_b32 %1, -1\n\t"
                     "v_cmpx_le_u32 %6, %7, %2\n\tv_mov_b32 %0, %4\n\tv_add_u32 %4, 0x80004001, %4\n\tv_subrev_u32 %2, %8, %2\n\ts_mov_b64 exec, -1\n\t"
                     "v_cmpx_le_u32 vcc, %8, %3\n\tv_mov_b32 %1, %5\n\tv_add_u32 %5, 0x80004001, %5\n\tv_subrev_u32 %3, %8, %3\n\t"
                     "s_or_b64 %6, %6, exec\n\ts_mov_b64 exec, -1"
                     : "=&v"(V[0]), "=&v"(V[1]), "+v"(c.x), "+v"(c.y), "+v"(U[0]), "+v"(U[1]), "=&s"(fm) : "s"(CHV), "n"(CHV) : "vcc", "scc");
        *(LDS u32x2*)(uintptr_t)(P_CNT + t8) = c;
        lds_st(tw, V[0]);
        lds_st(tw + 64u, V[1]);
        if (wv == 0) lds_st(P_CNT + NB * 4 + (t8 >> 1), vconst<0x40000000u>());
        const u64 pb = __builtin_amdgcn_ballot_w64((int)(V[0] & V[1]) >= 0);
        pm = (uint32_t)pb | (uint32_t)(pb >> 32);
        pm |= pm >> 1;
        if (ABL & 4) pm = 0;
        sm = 0ull;
        if (__builtin_expect(late, 0)) {
            sm = __builtin_amdgcn_ballot_w64((V[0] & 0x80000009u) == 9u || (V[1] & 0x80000009u) == 9u);
#pragma unroll
            for (int e = 0; e < 2; e++)
                if ((V[e] & 0x8000000fu) == 9u + NTAIL) U[e] -= 0x00008002u;
        }
    };
    u32x4 Va, Vb;

    Raw raw[PD];
    {
        const int tid = tid0;
        asm volatile("" ::: "v88", "v127");
        const uint16_t fq = freq[0];
        const int st0 = rstart(0), ng0 = rcount(0), st1 = rstart(1), ng1 = rcount(1);
#pragma unroll
        for (int d = 0; d < 6; d++) load_one(raw[0], st0, ng0, d);
        frr = __uint_as_float(__builtin_amdgcn_readfirstlane(__float_as_uint(bf2f(fq) * 0.15915494309189535f)));
#pragma unroll
        for (int s = 0; s < 16; s++) *(LDS u32x2*)(uintptr_t)(la + s * 256) = nul;
        const u32x2 ci = {CINIT, CINIT};
        *(LDS u32x2*)(uintptr_t)(P_CNT + tid * 8) = ci;
        lds_st(tw, 0xffffffffu);
        lds_st(tw + 64u, 0xffffffffu);
#pragma unroll
        for (int d = 0; d < 6; d++)
            if (((LPOS >> (4 * d)) & 15) < 4) load_one(raw[1], st1, ng1, d);
        if (blockIdx.x < 32 && tid == 0) meta[M_DONE + blockIdx.x * 64] = 0u;
        const int nt3 = blockIdx.x == 0 ? (T & 3) : 0;
        if (tid < nt3) __builtin_amdgcn_raw_buffer_store_b32((uint32_t)((T & ~3) + tid), rsR, spec0 - (uint32_t)tid * 4u, 0, 0);
        if (tid < 64) lds_st(P_CNT + (NB + tid) * 4, 0x40000000u);
        if (tid < 8) lds_st(P_FLAG + tid * 4, tid == 2 ? (uint32_t)nt3 : tid == 0 ? 0xffffffffu : 0u);
        Va = (u32x4)(0xffffffffu); Vb = Va;
    }
    pin(la); pin(tw);
    __syncthreads();
    ST(0)

    uint32_t w[8], aw[8], xv[8];
    u64 nm[8];
#pragma unroll
    for (int k = 0; k < 8; k++) { w[k] = 0u; aw[k] = 0u; xv[k] = 0u; nm[k] = ~0ull; }
    auto later2 = [&](int k) __attribute__((always_inline)) {
        uint32_t x0, x1;
        asm volatile("v_lshrrev_b32 %0, 4, %2\n\tv_lshrrev_b32 %1, 4, %5\n\tv_bitop3_b32 %0, %2, %0, 12 bitop3:0x72\n\tv_bitop3_b32 %1, %5, %1, 12 bitop3:0x72\n\t"
                     "s_not_b64 exec, %8\n\tds_write_b32 %2, %3 offset:%10\n\tds_or_b32 %0, %4 offset:%10\n\t"
                     "s_not_b64 exec, %9\n\tds_write_b32 %5, %6 offset:%10\n\tds_or_b32 %1, %7 offset:%10\n\ts_mov_b64 exec, -1"
                     : "=&v"(x0), "=&v"(x1)
                     : "v"(aw[k]), "v"(w[k]), "v"(xv[k]), "v"(aw[k + 1]), "v"(w[k + 1]), "v"(xv[k + 1]), "s"(nm[k]), "s"(nm[k + 1]), "n"(P_BUF) : "memory", "scc");
    };
    auto drain = [&]() __attribute__((always_inline)) {
        if (wv == 1) {
            const uint32_t q = (epoch + 1u) & 3u;
            const uint32_t n = __builtin_amdgcn_readfirstlane(lds_ld(vconst<P_FLAG + 16>() + q * 4u));
            if (__builtin_expect(n != 0u, 0)) {
                pin(t8);
                const uint32_t l = (t8 >> 3) & 63u;
                if (l < min(n, (uint32_t)LCAP)) {
                    const u32x4 e = *(const LDS u32x4*)(uintptr_t)(P_LST + (q * LCAP + l) * 16u);
                    lds_st(P_BUF + e.x, e.y);
                    lds_or(P_BUF + slot_x(e.x), e.z);
                }
                lds_st(vconst<P_FLAG + 16>() + q * 4u, vconst<0>());
            }
        }
    };
    auto again = [&](int D) __attribute__((always_inline)) {
        do {
            general(D, true, fm);
            __syncthreads();
            later2(0); later2(2); later2(4); later2(6);
            drain();
#pragma unroll
            for (int k = 0; k < 8; k++) {
                pin(aw[k]);
                bool due = false;
                if (aw[k] & 0x80000000u) {
                    xv[k] -= CHV;
                    if (xv[k] < CHV) {
                        const uint32_t f = (aw[k] >> 16) & 0x3ffu;
                        aw[k] = slot_w(aw[k] & 0xffffu, xv[k]);
                        xv[k] = f << ((xv[k] >> 8) & 31u);
                        due = true;
                    } else lds_st(vconst<P_FLAG>(), epoch + 1u);
                }
                nm[k] = ~__builtin_amdgcn_ballot_w64(due);
            }
            look(*(const LDS u32x2*)(uintptr_t)(P_CNT + t8));
            asm volatile("s_waitcnt lgkmcnt(0)" ::: "memory");
            __syncthreads();
        } while (__builtin_amdgcn_readfirstlane(lds_ld(vconst<P_FLAG>())) == epoch);
    };
    auto round = [&](auto DD, auto LL, Raw& rc, Raw& rn, int rd) __attribute__((always_inline)) {
        constexpr int D = decltype(DD)::value;
        constexpr bool LAST = decltype(LL)::value;
        const int st2 = (ABL & 1) ? ((pos + rd * 5) & 31) * GR : rstart(rd + PD), ng2 = rcount(rd + PD);
        const int st1 = (ABL & 1) ? ((pos + rd * 5 - 5) & 31) * GR : rstart(rd + 1), ng1 = rcount(rd + 1);
        const int ng = rcount(rd);
        auto hook = [&](int p) __attribute__((always_inline)) {
#pragma unroll
            for (int q = 0; q < 6; q++)
            {
                constexpr int dummy = 0; (void)dummy;
                const int lp = (LPOS >> (4 * q)) & 15;
                if (!LAST && lp < 4 && lp == p) load_one(rc, st2, ng2, q);
                if (!LAST && lp >= 4 && (lp < 8 ? lp : lp - 8) == p) load_one(rn, st1, ng1, q);
            }
        };
        uint32_t wn[8], ca[8], px[4], cv[8];
        float mx;
        float cs[2];
        auto convA = [&](int q) __attribute__((always_inline)) {
            if (LAST) return;
            const uint32_t a = rc.a[q >> 1][q & 1];
            const f32x2 av = {__uint_as_float(a << 16), __uint_as_float(a & 0xffff0000u)};
            const f32x2 rv = av * frr;
            mx = __builtin_elementwise_maximum(__builtin_elementwise_maximum(__builtin_fabsf(rv.x), __builtin_fabsf(rv.y)), (q & 1) ? mx : 0.f);
            cs[0] = __builtin_amdgcn_cosf(rv.x); cs[1] = __builtin_amdgcn_cosf(rv.y);
            asm volatile("" : "+v"(mx), "+v"(cs[0]), "+v"(cs[1]));
            __builtin_amdgcn_sched_barrier(0);
        };
        auto convB = [&](int k) __attribute__((always_inline)) {
            if (LAST) return;
            const uint32_t o = rc.o[k >> 2][k & 3], i = rc.i[k >> 2][k & 3];
            wn[k] = (__builtin_amdgcn_alignbit(bfi(63u, o, o >> 5), __float_as_uint(cs[k & 1]), 30u) << 14) | (i >> 6);
            ca[k] = (bfi(63u, i, o) << 2) & 0x1ffcu;
            pin(wn[k]); pin(ca[k]);
            __builtin_amdgcn_sched_barrier(0);
        };
        auto convD = [&](int q) __attribute__((always_inline)) {
            if (LAST) return;
            asm volatile("v_cvt_pk_f16_f32 %0, |%1|, |%2|\n\tv_pk_mul_f16 %0, %0, %3 op_sel_hi:[1,0]" : "=v"(px[q]) : "v"(cs[0]), "v"(cs[1]), "s"(0x3ffu));
        };
        uint32_t cst;
        auto check = [&](int j) __attribute__((always_inline)) {
            if (LAST) return;
            if (__builtin_expect((__builtin_amdgcn_ballot_w64(!(mx <= RMAXREV)) | (u64)(uint32_t)(GR - ng)) != 0ull, 0)) {
                pin(t8);
                asm volatile("" : "+v"(rc.a[j]));
                const int lg = j * NT + (int)(t8 >> 3);
#pragma unroll
                for (int k = 0; k < 4; k++) {
                    const uint32_t a = (k & 1) ? (rc.a[j][k >> 1] >> 16) : (rc.a[j][k >> 1] & 0xffffu);
                    const bool nrm = __builtin_fabsf(frr * bf2f(a)) <= RMAXREV;
                    if (!(lg < ng && nrm)) ca[j * 4 + k] = NB * 4u + ((t8 >> 1) & 0xfcu);
                    if (lg < ng && !nrm) __builtin_amdgcn_raw_buffer_store_b32((uint32_t)((rstart(rd) + lg) * 4 + k), rsR, spec0 - lds_add(vconst<P_FLAG + 8>(), vconst<1>()) * 4u, 0, 0);
                }
            }
            cst = vconst<CSTEP>();
        };
        auto ticket = [&](int k) __attribute__((always_inline)) {
            if (LAST) return;
            cv[k] = lds_add(P_CNT + ca[k], cst);
            __builtin_amdgcn_sched_barrier(0);
        };
        auto insert = [&](int j) __attribute__((always_inline)) {
            const int k0 = 4 * j;
            const bool odd = __builtin_amdgcn_ballot_w64(max(max(cv[k0], cv[k0 + 1]), max(cv[k0 + 2], cv[k0 + 3])) >= 2u * CHV) != 0ull;
#pragma unroll
            for (int k = k0; k < k0 + 4; k += 2) {
                uint32_t x0, x1;
                asm volatile("v_lshrrev_b32 %6, 11, %8\n\tv_lshrrev_b32 %7, 11, %9\n\t"
                             "v_bitop3_b32 %6, %6, %10, 60 bitop3:0x28\n\tv_bitop3_b32 %7, %7, %11, 60 bitop3:0x28\n\t"
                             "v_lshl_or_b32 %0, %10, 4, %6\n\tv_lshl_or_b32 %1, %11, 4, %7\n\t"
                             "v_lshlrev_b32_sdwa %2, %8, %12 dst_sel:DWORD dst_unused:UNUSED_PAD src0_sel:BYTE_1 src1_sel:WORD_0\n\t"
                             "v_lshlrev_b32_sdwa %3, %9, %12 dst_sel:DWORD dst_unused:UNUSED_PAD src0_sel:BYTE_1 src1_sel:WORD_1\n\t"
                             "v_bitop3_b32 %6, %0, %10, 12 bitop3:0x72\n\tv_bitop3_b32 %7, %1, %11, 12 bitop3:0x72\n\t"
                             "v_cmpx_gt_u32 %4, %15, %8\n\tds_write_b32 %0, %13 offset:%16\n\tds_or_b32 %6, %2 offset:%16\n\ts_mov_b64 exec, -1\n\t"
                             "v_cmpx_gt_u32 %5, %15, %9\n\tds_write_b32 %1, %14 offset:%16\n\tds_or_b32 %7, %3 offset:%16\n\ts_mov_b64 exec, -1"
                             : "=&v"(aw[k]), "=&v"(aw[k + 1]), "=&v"(xv[k]), "=&v"(xv[k + 1]), "=&s"(nm[k]), "=&s"(nm[k + 1]), "=&v"(x0), "=&v"(x1)
                             : "v"(cv[k]), "v"(cv[k + 1]), "v"(ca[k]), "v"(ca[k + 1]), "v"(px[k >> 1]), "v"(wn[k]), "v"(wn[k + 1]), "s"(CHV), "n"(P_BUF) : "memory");
                w[k] = wn[k]; w[k + 1] = wn[k + 1];
                if (__builtin_expect(odd, 0)) {
#pragma unroll
                    for (int q = k; q < k + 2; q++) {
                        nm[q] |= __builtin_amdgcn_ballot_w64(cv[q] >= 2u * CHV);
                        if (cv[q] >= 2u * CHV && cv[q] < 0x40000000u) {
                            const uint32_t lq = (epoch + 2u) & 3u;
                            uint32_t p = LCAP;
                            if (cv[q] < 3u * CHV && rd + 1 < nround) p = lds_add(vconst<P_FLAG + 16>() + lq * 4u, vconst<1>());
                            if (p < LCAP) {
                                const u32x2 e = {aw[q], w[q]};
                                *(LDS u32x2*)(uintptr_t)(P_LST + (lq * LCAP + p) * 16u) = e;
                                lds_st(P_LST + (lq * LCAP + p) * 16u + 8u, xv[q]);
                            } else {
                                aw[q] = ca[q] | ((q & 1) ? (px[q >> 1] & 0xffff0000u) : (px[q >> 1] << 16)) | 0x80000000u;
                                xv[q] = cv[q] - CHV;
                                lds_st(vconst<P_FLAG>(), epoch);
                            }
                        }
                    }
                }
            }
        };
        constexpr int P0 = D ? 0 : 3, P1 = D ? 1 : 2, P2 = D ? 2 : 1, P3 = D ? 3 : 0;
        const uint32_t fl = lds_ld(vconst<P_FLAG>());
        if (!LAST) asm volatile("" : "+v"(rc.o[0]), "+v"(rc.o[1]), "+v"(rc.i[0]), "+v"(rc.i[1]), "+v"(rc.a[0]), "+v"(rc.a[1]));
        ST(3)
        pin(t8);
        const uint32_t tr = P_TAB + (t8 & ~63u), u16 = (t8 + t8) & 0x70u;
        convA(0);
        convB(0);
        const bool ag = __builtin_amdgcn_readfirstlane(fl) == epoch;
        if (__builtin_expect(LAST || ag || sm != 0ull, 0)) {
            if (ag) again(D);
            general(D, false, sm);
            Va = *(const LDS u32x4*)(uintptr_t)(P_TAB + (t8 & ~63u) + P0 * 16);
            Vb = *(const LDS u32x4*)(uintptr_t)(P_TAB + (t8 & ~63u) + P1 * 16);
            asm volatile("" : "+v"(Va), "+v"(Vb));
        }
        const uint32_t pq = __builtin_amdgcn_readfirstlane(pm);
        uint32_t ad[8];
        ad[0] = take<4 * P0 + 0, D>(Va[0], u16, la, nul);
        convB(1);
        ad[1] = take<4 * P0 + 1, D>(Va[1], u16, la, nul);
        convD(0);
        convA(1);
        ad[2] = take<4 * P0 + 2, D>(Va[2], u16, la, nul);
        convB(2);
        ad[3] = take<4 * P0 + 3, D>(Va[3], u16, la, nul);
        convB(3);
        put<4 * P0 + 0, LAST ? 3 : 3>(rsA, ad[0], pq);
        ad[4] = take<4 * P1 + 0, D>(Vb[0], u16, la, nul);
        convD(1);
        check(0);
        ticket(0);
        hook(4);
        put<4 * P0 + 1, LAST ? 3 : 4>(rsA, ad[1], pq);
        ad[5] = take<4 * P1 + 1, D>(Vb[1], u16, la, nul);
        convA(2);
        ticket(1);
        put<4 * P0 + 2, LAST ? 3 : 5>(rsA, ad[2], pq);
        ad[6] = take<4 * P1 + 2, D>(Vb[2], u16, la, nul);
        convB(4);
        ticket(2);
        put<4 * P0 + 3, LAST ? 3 : 6>(rsA, ad[3], pq);
        Va = *(const LDS u32x4*)(uintptr_t)(tr + P2 * 16);
        __builtin_amdgcn_sched_barrier(0);
        ad[7] = take<4 * P1 + 3, D>(Vb[3], u16, la, nul);
        convB(5);
        ticket(3);
        SETP(2);
        put<4 * P1 + 0, LAST ? 4 : 8>(rsA, ad[4], pq);
        ad[0] = take<4 * P2 + 0, D>(Va[0], u16, la, nul);
        convD(2);
        convA(3);
        hook(5);
        put<4 * P1 + 1, LAST ? 4 : 7>(rsA, ad[5], pq);
        ad[1] = take<4 * P2 + 1, D>(Va[1], u16, la, nul);
        convB(6);
        put<4 * P1 + 2, LAST ? 4 : 6>(rsA, ad[6], pq);
        ad[2] = take<4 * P2 + 2, D>(Va[2], u16, la, nul);
        convB(7);
        put<4 * P1 + 3, LAST ? 3 : 4>(rsA, ad[7], pq);
        Vb = *(const LDS u32x4*)(uintptr_t)(tr + P3 * 16);
        __builtin_amdgcn_sched_barrier(0);
        ad[3] = take<4 * P2 + 3, D>(Va[3], u16, la, nul);
        convD(3);
        check(1);
        ticket(4);
        SETP(1);
        hook(6);
        put<4 * P2 + 0, LAST ? 4 : 5>(rsA, ad[0], pq);
        ad[4] = take<4 * P3 + 0, D>(Vb[0], u16, la, nul);
        ticket(5);
        put<4 * P2 + 1, LAST ? 4 : 6>(rsA, ad[1], pq);
        ad[5] = take<4 * P3 + 1, D>(Vb[1], u16, la, nul);
        ticket(6);
        put<4 * P2 + 2, LAST ? 4 : 7>(rsA, ad[2], pq);
        ad[6] = take<4 * P3 + 2, D>(Vb[2], u16, la, nul);
        ticket(7);
        SETP(0);
        put<4 * P2 + 3, LAST ? 3 : 7>(rsA, ad[3], pq);
        ad[7] = take<4 * P3 + 3, D>(Vb[3], u16, la, nul);
        hook(7);
        ST(4)
        SETP(PRIO > 1 ? 3 : 0);
        asm volatile("s_waitcnt lgkmcnt(0)" ::: "memory");
        __syncthreads();
        ST(5)
        u32x2 cn;
        if (!LAST) cn = *(const LDS u32x2*)(uintptr_t)(P_CNT + t8);
        __builtin_amdgcn_sched_barrier(0);
        later2(0);
        put<4 * P3, 15>(rsA, ad[4], pq);
        later2(2);
        put<4 * P3 + 1, 15>(rsA, ad[5], pq);
        drain();
        if (!LAST) look(cn);
        hook(0);
        SETP(PRIO > 1 ? 2 : 0);
        later2(4);
        put<4 * P3 + 2, 15>(rsA, ad[6], pq);
        later2(6);
        put<4 * P3 + 3, 15>(rsA, ad[7], pq);
        if (!LAST) {
            hook(1);
            SETP(PRIO > 1 ? 1 : 0);
            insert(0);
            hook(2);
            SETP(0);
            pin(t8);
            Va = *(const LDS u32x4*)(uintptr_t)(P_TAB + (t8 & ~63u) + P3 * 16);
            Vb = *(const LDS u32x4*)(uintptr_t)(P_TAB + (t8 & ~63u) + P2 * 16);
            __builtin_amdgcn_sched_barrier(0);
            insert(1);
            hook(3);
            ST(1)
            SETP(3);
            asm volatile("s_waitcnt lgkmcnt(0)" ::: "memory");
            __syncthreads();
            ST(2)
        }
    };
#define HMV(A, B) "v_mov_b64 v[" #A ":" #A "+1], v[" #B ":" #B "+1]\n\t"
#define HMV4(A) HMV(A + 8, A + 6) HMV(A + 6, A + 4) HMV(A + 4, A + 2) HMV(A + 2, A)
    int rd = 0;
#pragma unroll 1
    for (; rd + 1 < nround; rd += 2) {
        round(IC<0>(), IC<0>(), raw[0], raw[1], rd);
        round(IC<1>(), IC<0>(), raw[1], raw[0], rd + 1);
    }
    if (rd >= nround) asm volatile(HMV4(88) HMV4(98) HMV4(108) HMV4(118));
    asm volatile("" : "+s"(rd));
    if (rd < nround) round(IC<0>(), IC<0>(), raw[0], raw[1], rd);
    const uint32_t il0 = (blockIdx.x >> 2) * 256u + (blockIdx.x & 3u) * 16u + (uint32_t)wv;
    u32x2 mv[4], ms;
    {
        const __amdgpu_buffer_rsrc_t rsE = __builtin_amdgcn_make_buffer_rsrc((void*)m, 0, E * 8, 0x00020000);
        ms = __builtin_amdgcn_raw_buffer_load_b64(rsE, (t8 & 511u) * (uint32_t)(E >> 6), 0, 0);
#pragma unroll
        for (int q = 0; q < 4; q++) mv[q] = __builtin_amdgcn_raw_buffer_load_b64(rsE, t8 & 511u, (il0 + 64u * q) * 512u, 0);
        __builtin_amdgcn_sched_barrier(0);
    }
    round(IC<1>(), IC<1>(), raw[1], raw[0], 0);
    asm volatile("" : "+v"(ms), "+v"(mv[0]), "+v"(mv[1]), "+v"(mv[2]), "+v"(mv[3]));
    {
        const u32x2 c = *(const LDS u32x2*)(uintptr_t)(P_CNT + t8);
#pragma unroll
        for (int e = 0; e < 2; e++) {
            const uint32_t g = U[e] & 15u, some = c[e] >= CSTEP ? 1u : 0u;
            const uint32_t col = ((t8 << 3) & 0x7c0u) + (t8 >> 8) + e * 32u;
            uint32_t en = ((U[e] + ((g & 1u) ? 0u : 16384u)) & 0x7fffff81u) | (((7u - g) << 1) & 12u);
            if (g >= 8u) {
                uint32_t ln = TBASE + ((g - 8u) >> 1) * TPL + col * TST + blockIdx.x;
                if (g >= 8u + NTAIL && ((g & 1u) | some)) {
                    const uint32_t lp = blockIdx.x * (uint32_t)lcap + lds_add(P_FLAG + 4, 1u);
                    __builtin_amdgcn_raw_buffer_store_b16((uint16_t)col, rsR, gbase + lp * 2u, 0, 0);
                    ln = LBASE + lp;
                }
                en = ((g & 1u) | some) ? ln * 128u + 2u + (g & 1u) : 0x80000000u;
            }
            lds_st(tw + e * 64u, en);
            *(LDS uint8_t*)(uintptr_t)(P_CNT + (t8 & ~511u) + (((tw - P_TAB) >> 2) & 127u) + e * 16u) = (uint8_t)(g < 8u ? 0u : min(g - 8u + some, (uint32_t)NTAIL));
        }
        asm volatile("s_waitcnt lgkmcnt(0)" ::: "memory");
        __syncthreads();
        const uint32_t u16 = (t8 + t8) & 0x70u, lu = (la & ~63u) | (u16 & 0x30u), sel = 3u - (u16 >> 6);
        u32x4 En[4];
#pragma unroll
        for (int p = 0; p < 4; p++) En[p] = *(const LDS u32x4*)(uintptr_t)(P_TAB + (t8 & ~63u) + p * 16u);
        if ((t8 & 0x1c0u) == 0u)
            *(u32x4*)(tcnt + blockIdx.x * 512u + wv * 32 + (u16 >> 2)) = *(const LDS u32x4*)(uintptr_t)(P_CNT + (t8 & ~511u) + u16);
        if (t8 < 16u) meta[M_CNT + blockIdx.x * 2 + (t8 >> 3)] = lds_ld(P_FLAG + 4 + (t8 >> 1));
        u32x2 N[16];
#pragma unroll
        for (int s = 0; s < 16; s++) N[s] = *(const LDS u32x2*)(uintptr_t)(la + s * 256u);
        u32x4 d[4];
        auto fetch = [&](auto SS) __attribute__((always_inline)) {
            constexpr int s = decltype(SS)::value;
            const uint32_t en = En[s >> 2][s & 3], la_ = la, lu_ = lu, sel_ = sel;
            const u32x2 Hr = hget<HREG(s, 0)>();
            const u32x2 Hs = (en & 1u) ? Hr : nul;
            u32x4 q = {Hs.x, Hs.y, N[s].x, N[s].y};
            asm volatile("v_cmpx_ne_u32 vcc, 0, %4\n\tds_read_b128 %0, %2 offset:%6\n\tds_write_b64 %1, %3 offset:%6\n\t"
                         "v_cmpx_eq_u32 vcc, %5, %7\n\tds_read_b128 %0, %2 offset:%6\n\ts_mov_b64 exec, -1"
                         : "+&v"(q) : "v"(la_), "v"(lu_), "v"(Hs), "v"(en & 2u), "v"(en & 3u), "n"(s * 256), "v"(sel_) : "memory", "vcc");
            d[s & 3] = q;
        };
        auto rest = [&](auto SS) __attribute__((always_inline)) {
            constexpr int s = decltype(SS)::value;
            const uint32_t en = En[s >> 2][s & 3];
            uint32_t a = __builtin_amdgcn_bitop3_b32(en, u16, 15u, 0x54);
            __builtin_amdgcn_raw_buffer_store_b128(d[s & 3], rsR, a, 0, s >= 16 - NTS ? AUX_NT : AUX_SC1);
            if (__builtin_expect(__builtin_amdgcn_ballot_w64((en & 12u) != 0u) != 0ull, 0)) {
                const u32x4 dn = {nul.x, nul.y, nul.x, nul.y};
#pragma unroll 1
                for (uint32_t k = (en >> 2) & 3u; k > 0u; k--) {
                    a += 256u * 128u;
                    __builtin_amdgcn_raw_buffer_store_b128(dn, rsR, a, 0, AUX_SC1);
                }
            }
        };
#define REST4(S) fetch(IC<S>()); fetch(IC<S + 1>()); fetch(IC<S + 2>()); fetch(IC<S + 3>()); \
        asm volatile("s_waitcnt lgkmcnt(0)" : "+v"(d[0]), "+v"(d[1]), "+v"(d[2]), "+v"(d[3]) :: "memory"); \
        rest(IC<S>()); rest(IC<S + 1>()); rest(IC<S + 2>()); rest(IC<S + 3>());
        REST4(0) REST4(4) REST4(8) REST4(12)
    }
    ST(6)
    {
        float w[16];
        load_w(wdown, w);
        auto dot = [&](u32x2 v, int c) __attribute__((always_inline)) -> float {
            return bf2f(v.x & 0xffffu) * w[c] + bf2f(v.x >> 16) * w[4 + c] + bf2f(v.y & 0xffffu) * w[8 + c] + bf2f(v.y >> 16) * w[12 + c];
        };
        int e0[4];
#pragma unroll
        for (int c = 0; c < 4; c++) {
            const int ex = (int)(wave_max(__float_as_uint(dot(ms, c)) & 0x7fffffffu) >> 23);
            e0[c] = __builtin_amdgcn_readfirstlane(ex == 0 ? 0 : min(max(ex - 129, -100), 100));
        }
#pragma unroll
        for (int k = 0; k < 16; k++)
            w[k] = __uint_as_float(__builtin_amdgcn_readfirstlane(__float_as_uint(w[k] * __uint_as_float((uint32_t)(127 - e0[k & 3]) << 23))));
        const uint32_t lane = (t8 >> 3) & 63u;
#pragma unroll
        for (int cp = 0; cp < 2; cp++) {
            u32x4 o;
            u16x2 mx = {0, 0};
            float sq[2] = {0.f, 0.f};
#pragma unroll
            for (int q = 0; q < 4; q++) {
                const uint32_t hb = pk_bf16(dot(mv[q], 2 * cp), dot(mv[q], 2 * cp + 1));
                const float x0 = __uint_as_float(hb << 16), x1 = __uint_as_float(hb & 0xffff0000u);
                o[q] = __builtin_bit_cast(uint32_t, __builtin_amdgcn_cvt_pkrtz(x0, x1));
                mx = __builtin_elementwise_max(mx, __builtin_bit_cast(u16x2, hb & 0x7fff7fffu));
                sq[0] += x0 * x0; sq[1] += x1 * x1;
            }
            if (il0 < SLOTI) *(u32x4*)(mdc + (cp * 64u + lane) * SLOTI + (il0 & ~255u) + (il0 & 63u) * 4u) = o;
#pragma unroll
            for (int e = 0; e < 2; e++) {
                const u32x2 st = {wave_max(mx[e]), __float_as_uint(wave_sum(sq[e]))};
                if (lane == 0u) *(LDS u32x2*)(uintptr_t)(P_LST + ((2 * cp + e) * 16 + wv) * 8) = st;
            }
        }
        __syncthreads();
        if (wv == 0) {
            const u32x2 st = *(const LDS u32x2*)(uintptr_t)(P_LST + t8);
            const u32x2 r = {row_max(st.x) | (row_max(st.x ? 0xffffu - st.x : 0u) << 16), __float_as_uint(row_sum(__uint_as_float(st.y)))};
            if ((lane & 15u) == 15u) stat[(lane >> 4) * NWG + blockIdx.x] = r;
            if (blockIdx.x == 0 && lane < 4u) meta[M_E0 + lane] = (uint32_t)(lane == 0u ? e0[0] : lane == 1u ? e0[1] : lane == 2u ? e0[2] : e0[3]);
        }
    }
    ST(7)
    ST_OUT(0)
}

#define A_MD 0
#define A_ACC (SLOTI * 4)
#define A_FLAG (A_ACC + SLOTO * 4)
#define A_LDS (A_FLAG + 512)
static_assert(SLOTI * 4 == 3 * NT * 16 + 1024, "md staging assumes three full vectors and 256 dwords");
#define BIAS 0x40004000u
#define NFAST 8
#define NLD 10
#define PRE0 3
#define SHARE (SLOTO / 8)
#define SPINCAP 4096u
#define ZSIG 9.0f
#define HSLOW 0x80008000u
#define GAS __attribute__((address_space(1)))

__global__ __launch_bounds__(NT) __attribute__((amdgpu_num_vgpr(53))) void acc_kernel(const char* __restrict__ recs,
                                                 const u32x4* __restrict__ tcnt,
                                                 const uint32_t* __restrict__ mdc, const u32x4* __restrict__ stat,
                                                 const int* __restrict__ iin, const int* __restrict__ iout, const uint16_t* __restrict__ ang,
                                                 const u32x2* __restrict__ m, const uint16_t* __restrict__ wdown,
                                                 const uint16_t* __restrict__ freq,
                                                 const uint16_t* __restrict__ dist, const uint16_t* __restrict__ centers,
                                                 const uint16_t* __restrict__ wrbf, const uint16_t* __restrict__ wsbf,
                                                 const uint16_t* __restrict__ wup,
                                                 uint32_t* partial, unsigned* meta, u32x2* __restrict__ out, int E, int ks, int lcap, float zf) {
    extern __shared__ __attribute__((aligned(16))) char smem[];
    ST_DECL
    unsigned* acc = (unsigned*)(smem + A_ACC);
    unsigned* s_flag = (unsigned*)(smem + A_FLAG);
    const int tid = threadIdx.x;
    const int lane = tid & 63;
    const int wv = __builtin_amdgcn_readfirstlane(tid >> 6);
    const int cp = (blockIdx.x >> 3) & 1;
    const int h = blockIdx.x & 3;
    const int j = ((blockIdx.x >> 2) & 1) * 16 + (blockIdx.x >> 4);
    u32x4 tc;
    const __amdgpu_buffer_rsrc_t rsM = __builtin_amdgcn_make_buffer_rsrc((void*)mdc, 0, (int)(128u * SLOTI * 4u), 0x00020000);

    u32x4 pm[4];
    u32x4 ring[NLD];
    auto load_md = [&](int ff) __attribute__((always_inline)) {
        const uint32_t so = (uint32_t)(cp * 64 + ((ABL & 64) ? 0 : ff * 4) + h) * (SLOTI * 4u);
#pragma unroll
        for (int v = 0; v < 3; v++) pm[v] = __builtin_amdgcn_raw_buffer_load_b128(rsM, (uint32_t)tid * 16u, so + (uint32_t)v * (NT * 16u), 0);
        if (wv == 0) pm[3] = __builtin_amdgcn_raw_buffer_load_b128(rsM, (uint32_t)tid * 16u, so + 3u * NT * 16u, 0);
        __builtin_amdgcn_sched_barrier(0);
    };
    const uint32_t lf = (uint32_t)(tid & ~31) * 16u + (uint32_t)(tid & 12) * 32u + (uint32_t)(tid & 16) * 4u + (uint32_t)(tid & 3) * 16u;
    auto load_main = [&](int i, int ff, bool nt) __attribute__((always_inline)) -> u32x4 {
        const uint32_t col = (ABL & 8) ? (uint32_t)((ff & 15) * 128 + h * 32) : (uint32_t)((ff & 15) * 128 + h * 32 + j);
        if (ABL & 8) nt = false;
        const __amdgpu_buffer_rsrc_t rs = __builtin_amdgcn_make_buffer_rsrc((void*)(recs + col * (CST * 128u)), 0, ff < 16 ? ks * 32768 : 0, 0x00020000);
        if (nt) return __builtin_amdgcn_raw_buffer_load_b128(rs, lf, i * 16384, AUX_NT);
        return __builtin_amdgcn_raw_buffer_load_b128(rs, lf, i * 16384, 0);
    };
    auto tail_rank = [&](uint32_t cnt, uint32_t t0, int& dst) __attribute__((always_inline)) -> int {
        const bool has = (uint32_t)(lane & 3) + t0 < (cnt & 0xffu);
        const u64 mask = __builtin_amdgcn_ballot_w64(has);
        const int n = __builtin_popcountll(mask);
        const int rank = __builtin_amdgcn_mbcnt_hi((uint32_t)(mask >> 32), __builtin_amdgcn_mbcnt_lo((uint32_t)mask, 0u));
        dst = (has ? rank : (n + lane - rank)) << 2;
        return n;
    };
    const uint32_t tof = (uint32_t)(lane & 2) * (TPL * 64u) + (uint32_t)(lane >> 2) * 128u + (uint32_t)(lane & 1) * 64u;
    auto load_tail = [&](int e, uint32_t at, int n, int ff, bool nt) __attribute__((always_inline)) -> u32x4 {
        const uint32_t col = (uint32_t)(ff * 128 + h * 32 + j);
        const __amdgpu_buffer_rsrc_t rs = __builtin_amdgcn_make_buffer_rsrc((void*)(recs + (TBASE + col * TST + (uint32_t)wv * 16u) * 128u), 0,
                                                                            (int)((2u * TPL + 16u) * 128u), 0x00020000);
        uint32_t vo = at + (uint32_t)(lane & 3) * 16u;
        if (e * 16 + (lane >> 2) >= n || (ABL & 32)) vo = 0xffffff00u;
        if (nt) return __builtin_amdgcn_raw_buffer_load_b128(rs, vo, 0, AUX_NT);
        return __builtin_amdgcn_raw_buffer_load_b128(rs, vo, 0, 0);
    };
    float rs[4][4], rg[4][4];
    u32x4 em[2];
    u32x2 dw2;
    const __amdgpu_buffer_rsrc_t rsE = __builtin_amdgcn_make_buffer_rsrc((void*)m, 0, E * 8, 0x00020000);
    const __amdgpu_buffer_rsrc_t rsD = __builtin_amdgcn_make_buffer_rsrc((void*)dist, 0, E * 2, 0x00020000);
    auto edge_loads = [&](int sh) __attribute__((always_inline)) {
        const int ol = sh * SHARE + tid * 4;
        const uint32_t e0 = (tid < SHARE / 4) ? (uint32_t)(((ol >> 6) << 11) | (j << 6) | (ol & 63)) : 0x0fffff00u;
        em[0] = __builtin_amdgcn_raw_buffer_load_b128(rsE, e0 * 8u, 0, 0);
        em[1] = __builtin_amdgcn_raw_buffer_load_b128(rsE, e0 * 8u + 16u, 0, 0);
        dw2 = __builtin_amdgcn_raw_buffer_load_b64(rsD, e0 * 2u, 0, 0);
    };
    auto factors = [&]() __attribute__((always_inline)) {
        const uint32_t dv[4] = {dw2.x & 0xffffu, dw2.x >> 16, dw2.y & 0xffffu, dw2.y >> 16};
#pragma unroll
        for (int q = 0; q < 4; q++) {
#pragma unroll
            for (int k = 0; k < 4; k++) { rs[q][k] = 0.f; rg[q][k] = 0.f; }
        }
#pragma unroll 2
        for (int r = 0; r < 8; r++) {
            const uint32_t cw = ((const uint32_t*)centers)[r >> 1];
            const float cn = bf2f((r & 1) ? (cw >> 16) : (cw & 0xffffu));
            const u32x2 s2 = ((const u32x2*)wsbf)[r], r2 = ((const u32x2*)wrbf)[r];
            const float ws[4] = {bf2f(s2.x & 0xffffu), bf2f(s2.x >> 16), bf2f(s2.y & 0xffffu), bf2f(s2.y >> 16)};
            const float wr[4] = {bf2f(r2.x & 0xffffu), bf2f(r2.x >> 16), bf2f(r2.y & 0xffffu), bf2f(r2.y >> 16)};
#pragma unroll
            for (int q = 0; q < 4; q++) {
                const float xx = bf2f(dv[q]) - cn;
                const float rb = __expf(-10.0f * xx * xx);
#pragma unroll
                for (int k = 0; k < 4; k++) { rs[q][k] += rb * ws[k]; rg[q][k] += rb * wr[k]; }
            }
        }
    };
#define ADT(V, O) "ds_write_addtid_b32 " V " offset:" #O "\n\t"
    auto store_md = [&]() __attribute__((always_inline)) {
        asm volatile("s_mov_b32 m0, %12\n\ts_nop 0\n\t" ADT("%0", 0) ADT("%1", 256) ADT("%2", 512) ADT("%3", 768) ADT("%4", 16384) ADT("%5", 16640) ADT("%6", 16896) ADT("%7", 17152)
                     ADT("%8", 32768) ADT("%9", 33024) ADT("%10", 33280) ADT("%11", 33536)
                     :: "v"(pm[0][0]), "v"(pm[0][1]), "v"(pm[0][2]), "v"(pm[0][3]), "v"(pm[1][0]), "v"(pm[1][1]), "v"(pm[1][2]), "v"(pm[1][3]),
                        "v"(pm[2][0]), "v"(pm[2][1]), "v"(pm[2][2]), "v"(pm[2][3]), "s"(wv * 1024) : "memory");
        if (wv == 0) asm volatile("s_mov_b32 m0, 0\n\ts_nop 0\n\t" ADT("%0", 49152) ADT("%1", 49408) ADT("%2", 49664) ADT("%3", 49920)
                                  :: "v"(pm[3][0]), "v"(pm[3][1]), "v"(pm[3][2]), "v"(pm[3][3]) : "memory");
    };
    const int me = h * 2 + cp;
    bool fast, more;
    uint32_t need, mg0, mg1;
    uint32_t guard = 0;
    uint32_t sgn = 0x8000u;
    pin(sgn);

#define GAT3(M) { uint32_t a0_, a1_, a2_; asm volatile( \
        "v_lshlrev_b16 %0, 2, %3\n\tv_lshlrev_b16 %1, 2, %4\n\tv_lshlrev_b16 %2, 2, %5\n\t" \
        "ds_read_b32 v[%6], %0\n\tds_read_b32 v[%6+1], %1\n\tds_read_b32 v[%6+2], %2" \
        : "=&v"(a0_), "=&v"(a1_), "=&v"(a2_) : "v"(w0), "v"(w1), "v"(w2), "n"(M) : "memory"); }
#define MIXL " op_sel_hi:[1,1,0]\n\t"
#define MIXH " op_sel:[0,1,0] op_sel_hi:[1,1,0]\n\t"
#define SDW " dst_sel:DWORD dst_unused:UNUSED_PAD src0_sel:DWORD src1_sel:WORD_1\n\t"
#define SCAT(PRE, POST, M) { uint32_t c0_, c1_, c2_, a0_, a1_, a2_; asm volatile( \
        "v_bitop3_b32 %6, %6, v[%15-9], v[%15-8] bitop3:0xfe\n\tv_and_b32 %0, 0x3ff, %7\n\tv_lshrrev_b32 %2, 22, %7\n\tv_or_b32 %6, v[%15-7], %6\n\t" PRE \
        "v_bitop3_b32 %0, %11, %8, %0 bitop3:0xea\n\tv_bitop3_b32 %2, %11, %10, %2 bitop3:0xea\n\tv_bfe_u32 %1, %7, 11, 10\n\t" \
        "v_fma_mix_f32 %3, %0, v[%15], %12" MIXL "v_bitop3_b32 %1, %11, %9, %1 bitop3:0xea\n\tv_fma_mix_f32 %0, %0, v[%15], %13" MIXH \
        "v_fma_mix_f32 %4, %1, v[%15+1], %12" MIXL "v_lshl_add_u32 %0, %0, 16, %3\n\tv_lshlrev_b32_sdwa %3, 2, %8" SDW \
        "v_fma_mix_f32 %1, %1, v[%15+1], %13" MIXH "ds_add_rtn_u32 v[%15-9], %3, %0 offset:%14\n\t" \
        "v_fma_mix_f32 %5, %2, v[%15+2], %12" MIXL "v_lshl_add_u32 %1, %1, 16, %4\n\tv_lshlrev_b32_sdwa %4, 2, %9" SDW \
        "v_fma_mix_f32 %2, %2, v[%15+2], %13" MIXH "ds_add_rtn_u32 v[%15-8], %4, %1 offset:%14\n\t" \
        "v_lshl_add_u32 %2, %2, 16, %5\n\tv_lshlrev_b32_sdwa %5, 2, %10" SDW \
        "ds_add_rtn_u32 v[%15-7], %5, %2 offset:%14" POST \
        : "=&v"(c0_), "=&v"(c1_), "=&v"(c2_), "=&v"(a0_), "=&v"(a1_), "=&v"(a2_), "+v"(guard) \
        : "v"(xw), "v"(w0), "v"(w1), "v"(w2), "v"(sgn), "s"(mg0), "s"(mg1), "n"(A_ACC), "n"(M), "s"(tm) : "memory"); }
    asm volatile("v_mov_b32 v110, 0\n\tv_mov_b32 v111, 0\n\tv_mov_b32 v112, 0\n\tv_mov_b32 v113, 0\n\tv_mov_b32 v114, 0\n\tv_mov_b32 v115, 0\n\t"
                 "v_mov_b32 v116, 0\n\tv_mov_b32 v117, 0\n\tv_mov_b32 v118, 0" ::: "v106", "v127");
    auto unmix = [&](int i) __attribute__((always_inline)) {
        const auto s0 = __builtin_amdgcn_permlane16_swap(ring[i][0], ring[i][2], false, false);
        const auto s1 = __builtin_amdgcn_permlane16_swap(ring[i][1], ring[i][3], false, false);
        ring[i][0] = s0[0]; ring[i][2] = s0[1]; ring[i][1] = s1[0]; ring[i][3] = s1[1];
    };
    auto gather = [&](int R, int i, int p) __attribute__((always_inline)) {
        if (i < NFAST) unmix(i);
        const uint32_t w0 = ring[i][0 ^ R], w1 = ring[i][1 ^ R], w2 = ring[i][2 ^ R];
        if (ABL & 16) { asm volatile("" :: "v"(w0), "v"(w1), "v"(w2)); return; }
        if (p == 0) GAT3(119) else if (p == 1) GAT3(122) else GAT3(125)
    };
    auto scatter = [&](int R, int i, int p, u64 tm) __attribute__((always_inline)) {
        const uint32_t xw = ring[i][3 ^ R], w0 = ring[i][0 ^ R], w1 = ring[i][1 ^ R], w2 = ring[i][2 ^ R];
        if (ABL & 16) { asm volatile("" :: "v"(w0), "v"(w1), "v"(w2), "v"(xw)); return; }
        if (i < NFAST) { if (p == 0) SCAT("", "", 119) else if (p == 1) SCAT("", "", 122) else SCAT("", "", 125) }
        else if (p == 0) SCAT("s_mov_b64 exec, %16\n\t", "\n\ts_mov_b64 exec, -1", 119)
        else SCAT("s_mov_b64 exec, %16\n\t", "\n\ts_mov_b64 exec, -1", 125)
    };
    auto lanes_below = [&](int n) __attribute__((always_inline)) -> u64 {
        return n >= 16 ? ~0ull : n <= 0 ? 0ull : (1ull << (4 * n)) - 1ull;
    };
#define WAIT_L(N) asm volatile("s_waitcnt lgkmcnt(" #N ")" ::: "memory")
    int ntl = 0, ntn;
    uint32_t ta[2];
    auto list1 = [&]() __attribute__((always_inline)) {
        int dst;
        ntn = tail_rank(tc.x, 0u, dst);
        asm volatile("ds_permute_b32 v106, %0, %1" :: "v"(dst), "v"(tof) : "memory");
    };
    auto list2 = [&]() __attribute__((always_inline)) {
        asm volatile("ds_bpermute_b32 v107, %0, v106\n\tds_bpermute_b32 v108, %0, v106 offset:64" :: "v"(lane & ~3) : "memory");
    };
    auto step = [&](auto RR, auto NN, auto NC, int ff) __attribute__((always_inline)) {
        constexpr int R = decltype(RR)::value;
        constexpr bool nt = decltype(NN)::value, nc = decltype(NC)::value;
        gather(R, 0, 0);
        scatter(R, NFAST, 2, lanes_below(ntl));
        ntl = ntn;
        asm volatile("v_mov_b32 %0, v107\n\tv_mov_b32 %1, v108" : "=v"(ta[0]), "=v"(ta[1]));
        ring[NFAST] = load_tail(0, ta[0], ntl, ff, nc);
        ring[NFAST + 1] = load_tail(1, ta[1], ntl, ff, nc);
        load_md(min(ff + 1, 15));
        gather(R, 1, 1);
        const uint32_t tcur = tc.x;
#pragma unroll
        for (int i = 0; i < NFAST - 1; i++) {
            gather(R, i + 2, (i + 2) % 3);
            if (i < 2) WAIT_L(9); else WAIT_L(12);
            scatter(R, i, i % 3, 0ull);
            ring[i] = load_main(i, ff + 1, nt);
            if (i == 0) {
                tc.x = __builtin_amdgcn_alignbit(tc.y, tc.x, 8u); tc.y = __builtin_amdgcn_alignbit(tc.z, tc.y, 8u);
                tc.z = __builtin_amdgcn_alignbit(tc.w, tc.z, 8u); tc.w >>= 8;
                list1();
            }
            if (i == 4) list2();
            __builtin_amdgcn_sched_barrier(0);
        }
        if (__builtin_expect(ntl > 16, 0)) {
            gather(R, NFAST + 1, 0);
            WAIT_L(0);
            scatter(R, NFAST + 1, 0, lanes_below(ntl - 16));
        }
        if (__builtin_expect((__builtin_amdgcn_ballot_w64((tcur & 0xffu) > 4u) | (u64)(ntl > 32 ? 1 : 0)) != 0ull, 0)) {
#pragma unroll 1
            for (int e = 2; e < 6; e++) {
                int dst;
                const int n = tail_rank(tcur, (uint32_t)e & 4u, dst);
                const int cmp = __builtin_amdgcn_ds_permute(dst, (int)(tof + ((uint32_t)e & 4u) * (TPL * 64u)));
                ring[NFAST + 1] = load_tail(e & 3, (uint32_t)__builtin_amdgcn_ds_bpermute((e & 3) * 64 + (lane & ~3), cmp), n, ff, false);
                gather(R, NFAST + 1, 0);
                WAIT_L(0);
                scatter(R, NFAST + 1, 0, lanes_below(n - (e & 3) * 16));
            }
        }
        WAIT_L(3);
        ST(1)
        if (ff < 15) {
            __builtin_amdgcn_s_barrier();
            ST(3)
            store_md();
        }
        scatter(R, NFAST - 1, 1, 0ull);
        ring[NFAST - 1] = load_main(NFAST - 1, ff + 1, nt);
        if (ff < 15) {
            WAIT_L(3);
            __builtin_amdgcn_s_barrier();
            ST(4)
        }
    };
    auto steps = [&](auto RR) __attribute__((always_inline)) {
        constexpr int R = decltype(RR)::value;
        tc = tcnt[(wv * 16 + (lane >> 2)) * 128 + j * 4 + h];
        u32x4 sv[2];
        uint32_t e0;
        if (wv < 4) { sv[0] = stat[tid * 2]; sv[1] = stat[tid * 2 + 1]; e0 = meta[M_E0 + wv]; }
        const uint32_t extra = wv >= 8 ? meta[M_CNT + tid - 512] : 0u;
        load_md(0);
#pragma unroll
        for (int i = 0; i < PRE0; i++) ring[i] = load_main(i, 0, false);
        __builtin_amdgcn_sched_barrier(0);
        {
            const u32x4 bi = {BIAS, BIAS, BIAS, BIAS};
            for (int i = tid; i < SLOTO / 4; i += NT) *(LDS u32x4*)(uintptr_t)(A_ACC + i * 16) = bi;
        }
        list1();
        if (wv < 4) {
            const uint32_t mxb = wave_max(max(max(sv[0].x & 0xffffu, sv[0].z & 0xffffu), max(sv[1].x & 0xffffu, sv[1].z & 0xffffu)));
            const uint32_t mnb = 0xffffu - (wave_max(max(max(sv[0].x, sv[0].z), max(sv[1].x, sv[1].z))) >> 16);
            const float s2 = wave_sum((__uint_as_float(sv[0].y) + __uint_as_float(sv[0].w)) + (__uint_as_float(sv[1].y) + __uint_as_float(sv[1].w)));
            const float rng = fmaxf(sqrtf(zf * s2), 2.0f * bf2f(mxb)) * 6.103515625e-05f;
            const uint32_t eb = mxb ? (__float_as_uint(rng) + 0x7fffffu) >> 23 : 127u;
            const bool ok = !mxb || (mxb < 0x4700u && eb >= 107u && eb <= 140u && mnb >= ((eb + 8u) << 7));
            if (lane == 0) {
                s_flag[8 + wv] = (eb & 0xffu) | (ok ? 0x100u : 0u) | (mxb ? 0x200u : 0u);
                s_flag[12 + wv] = __float_as_uint(__uint_as_float(((eb + e0) & 0xffu) << 23) * (1024.0f / 1023.0f));
                s_flag[wv] = 0u;
            }
        }
        if (wv >= 8) {
            const u64 xm = __builtin_amdgcn_ballot_w64(extra != 0u);
            if (lane == 0) { s_flag[64 + 2 * wv] = (uint32_t)xm; s_flag[65 + 2 * wv] = (uint32_t)(xm >> 32); }
        }
        WAIT_L(0);
        list2();
        store_md();
#pragma unroll
        for (int i = PRE0; i < NFAST; i++) ring[i] = load_main(i, 0, false);
        ring[NFAST] = (u32x4)(0u);
        WAIT_L(0);
        __syncthreads();
        const uint32_t q0 = __builtin_amdgcn_readfirstlane(s_flag[8 + 2 * cp]), q1 = __builtin_amdgcn_readfirstlane(s_flag[9 + 2 * cp]);
        fast = (q0 & q1 & 0x100u) != 0u;
        need = (q0 >> 9) | ((q1 >> 9) << 1);
        more = __builtin_amdgcn_ballot_w64(s_flag[80 + (lane & 15)] != 0u) != 0ull;
        mg0 = (((q0 & 0xffu) + 9u) << 23) | 0x400000u;
        mg1 = ((((q1 & 0xffu) + 9u) << 23) | 0x400000u) + ((0u - (mg0 >> 16)) & 0xffffu);
        if (!fast) return;
        ST(0)
        int ff = 0;
#pragma unroll 1
        for (; ff < 15 - NTS; ff++) step(RR, IC<0>(), IC<0>(), ff);
        step(RR, IC<1>(), IC<0>(), ff++);
#pragma unroll 1
        for (; ff < 16; ff++) step(RR, IC<1>(), IC<1>(), ff);
        scatter(R, NFAST, 2, lanes_below(ntl));
    };
    auto irregular = [&](auto f) __attribute__((always_inline)) {
        const float fr = bf2f(freq[0]);
        const uint16_t* tags = (const uint16_t*)(recs + (size_t)(LBASE + NWG * (uint32_t)lcap) * 128u);
#pragma unroll 1
        for (uint32_t sb = 0; sb < NWG; sb++) {
            if (!((s_flag[80 + (sb >> 4)] >> ((sb & 15u) * 2u)) & 3u)) continue;
            const uint32_t nl = meta[M_CNT + sb * 2u], ns = meta[M_CNT + sb * 2u + 1u];
            const char* lg = recs + (size_t)(LBASE + sb * (uint32_t)lcap) * 128u;
#pragma unroll 1
            for (uint32_t q = tid; q < nl * 8u; q += NT) {
                const uint32_t b = tags[(size_t)sb * lcap + (q >> 3)];
                if ((b & 127u) == (uint32_t)(h * 32 + j)) {
                    const uint32_t* un = (const uint32_t*)(lg + (size_t)q * 16u);
                    const uint32_t xw = un[3 ^ h];
#pragma unroll 1
                    for (int k = 0; k < 3; k++) {
                        const uint32_t ww = un[k ^ h];
                        const float c = (float)((xw >> (k * 11)) & 0x3ffu) * (1.0f / 1023.0f);
                        if (!(ww & WNULL)) f(ww >> 16, ((ww & 0x3fffu) << 6) | (b >> 5), (ww & 0x8000u) ? -c : c);
                    }
                }
            }
            const uint32_t* se = (const uint32_t*)(lg + (size_t)lcap * 128u);
#pragma unroll 1
            for (uint32_t q = tid; q < ns; q += NT) {
                const uint32_t t = se[-1 - (int)q];
                const uint32_t i = (uint32_t)iin[t], o = (uint32_t)iout[t];
                if (((o >> 6) & 31u) == (uint32_t)j && (i & 3u) == (uint32_t)h) f(((o >> 11) << 6) | (o & 63u), i, cosf(fr * bf2f(ang[t])));
            }
        }
    };

    if (h == 0) steps(IC<0>());
    else if (h == 1) steps(IC<1>());
    else if (h == 2) steps(IC<2>());
    else steps(IC<3>());
    if (fast) {
        if (more) irregular([&](uint32_t ol, uint32_t i, float cf) __attribute__((always_inline)) {
            const uint32_t md = mdc[(cp * 64u + (i & 63u)) * SLOTI + ((i >> 14) << 8) + ((i >> 4) & 0xfcu) + ((i >> 12) & 3u)];
            const float c = cf * (1023.0f / 16777216.0f);
            const uint32_t lo = __float_as_uint(__builtin_fmaf(c, h2f(md & 0xffffu), __uint_as_float(mg0)));
            const uint32_t hi = __float_as_uint(__builtin_fmaf(c, h2f(md >> 16), __uint_as_float(mg1)));
            if (cf == cf) guard |= atomicAdd(&acc[ol], (hi << 16) + lo);
            else guard |= HSLOW;
        });
    }
    edge_loads(me);
    WAIT_L(0);
    asm volatile("v_or3_b32 %0, %0, v110, v111\n\tv_or3_b32 %0, %0, v112, v113\n\tv_or3_b32 %0, %0, v114, v115\n\tv_or3_b32 %0, %0, v116, v117\n\tv_or_b32 %0, v118, %0" : "+v"(guard));
    ST(2)
    __syncthreads();
    const __amdgpu_buffer_rsrc_t rsP = __builtin_amdgcn_make_buffer_rsrc((void*)(partial + (size_t)j * 16 * SLOTO), 0, 16 * SLOTO * 4, 0x00020000);
    if (fast) {
        uint32_t big = 0u;
        u32x4 v[7];
#pragma unroll
        for (int k = 0; k < 6; k++) v[k] = *(const LDS u32x4*)(uintptr_t)(A_ACC + (k * NT + tid) * 16);
        v[6] = (u32x4)(BIAS);
        if (wv < 2) v[6] = *(const LDS u32x4*)(uintptr_t)(A_ACC + (6 * NT + tid) * 16);
#pragma unroll
        for (int k = 0; k < 6; k++) __builtin_amdgcn_raw_buffer_store_b128(v[k], rsP, (uint32_t)(tid * 16 + me * SLOTO * 4 + k * NT * 16), 0, AUX_SC1);
        if (wv < 2) __builtin_amdgcn_raw_buffer_store_b128(v[6], rsP, (uint32_t)(tid * 16 + me * SLOTO * 4 + 6 * NT * 16), 0, AUX_SC1);
        __builtin_amdgcn_sched_barrier(0);
#pragma unroll
        for (int k = 0; k < 28; k++) { guard |= v[k >> 2][k & 3]; big |= (v[k >> 2][k & 3] ^ (v[k >> 2][k & 3] >> 1)) ^ 0x20002000u; }
        if (guard & HSLOW) s_flag[0] = 1u;
        if (big & 0x3e00u) s_flag[2] = 1u;
        if (big & 0x3e000000u) s_flag[3] = 2u;
    }
    if (wv < (SHARE / 4 + 63) / 64) factors();
    asm volatile("s_waitcnt vmcnt(0)" ::: "memory");
    __syncthreads();
    ST(5)
    const bool slow = (ABL & 120) ? false : !fast || __builtin_amdgcn_readfirstlane(s_flag[0]) != 0u || (need & ~__builtin_amdgcn_readfirstlane(s_flag[2] | s_flag[3])) != 0u;
    if (slow) {
        float* fa = (float*)acc;
        float w[16];
        load_w(wdown, w);
#pragma unroll 1
        for (int comp = 2 * cp; comp < 2 * cp + 2; comp++) {
            __syncthreads();
            for (int i = tid; i < SLOTO; i += NT) fa[i] = 0.f;
            __syncthreads();
            auto add = [&](uint32_t ol, uint32_t i, float cf) __attribute__((always_inline)) {
                uint32_t rr[4];
                md_of(m[i], w, rr);
                const uint32_t rc = comp == 0 ? rr[0] : comp == 1 ? rr[1] : comp == 2 ? rr[2] : rr[3];
                atomicAdd(&fa[ol], cf * bf2f(rc));
            };
#pragma unroll 1
            for (uint32_t q = tid; q < 16u * NWG * ((uint32_t)ks * 8u + NTAIL * 4u); q += NT) {
                const uint32_t per = (uint32_t)ks * 8u + NTAIL * 4u;
                const uint32_t un = q & 3u, hf = (q % per) >> 2, sb = (q / per) & (NWG - 1u), ff = (q / per) / NWG;
                const uint32_t col = ff * 128u + (uint32_t)(h * 32 + j);
                u32x2 p0, p1;
                if (hf < 2u * (uint32_t)ks) {
                    const char* ln = recs + (size_t)(col * CST + (hf >> 1) * 256u + sb) * 128u + un * 16u + (hf & 1u) * 8u;
                    p0 = *(const u32x2*)ln; p1 = *(const u32x2*)(ln + 64);
                } else {
                    const uint32_t tl = hf - 2u * (uint32_t)ks;
                    if (tl >= ((const uint8_t*)tcnt)[(sb * 128u + (uint32_t)(j * 4 + h)) * 16u + ff]) continue;
                    const char* ln = recs + (size_t)(TBASE + (tl >> 1) * TPL + col * TST + sb) * 128u + (tl & 1u) * 64u + un * 16u;
                    p0 = *(const u32x2*)ln; p1 = *(const u32x2*)(ln + 8);
                }
                if (h & 2) { const u32x2 t = p0; p0 = p1; p1 = t; }
                const uint32_t ws[4] = {(h & 1) ? p0.y : p0.x, (h & 1) ? p0.x : p0.y, (h & 1) ? p1.y : p1.x, (h & 1) ? p1.x : p1.y};
#pragma unroll
                for (int k = 0; k < 3; k++) {
                    const float c = (float)((ws[3] >> (k * 11)) & 0x3ffu) * (1.0f / 1023.0f);
                    if (!(ws[k] & WNULL)) add(ws[k] >> 16, ((ws[k] & 0x3fffu) << 6) | (ff * 4u + (uint32_t)h), (ws[k] & 0x8000u) ? -c : c);
                }
            }
            if (more) irregular(add);
            __syncthreads();
            for (int i = tid; i < SLOTO / 4; i += NT)
                __builtin_amdgcn_raw_buffer_store_b128(*(const LDS u32x4*)(uintptr_t)(A_ACC + i * 16), rsP, (uint32_t)(((comp & 1) * 8 + me) * SLOTO * 4 + i * 16), 0, AUX_SC1);
        }
        asm volatile("s_waitcnt vmcnt(0)" ::: "memory");
        __syncthreads();
    }
    if (tid == 0) {
        GAS unsigned* dw = (GAS unsigned*)(meta + M_DONE + j * 64);
        const unsigned inc = 1u + (slow ? 0x10000u << me : 0u);
        unsigned v = __hip_atomic_fetch_add(dw, inc, __ATOMIC_RELAXED, __HIP_MEMORY_SCOPE_AGENT) + inc;
        unsigned todo = 1u << me;
        if ((v & 0xffu) == 8u) todo |= (v >> 8) & 0xffu;
        else {
            unsigned spins = 0;
            while (((v = __hip_atomic_load(dw, __ATOMIC_RELAXED, __HIP_MEMORY_SCOPE_AGENT)) & 0xffu) != 8u) {
                __builtin_amdgcn_s_sleep(2);
                if (++spins > SPINCAP) {
                    v = __hip_atomic_fetch_add(dw, 256u << me, __ATOMIC_RELAXED, __HIP_MEMORY_SCOPE_AGENT);
                    if ((v & 0xffu) != 8u) todo = 0u;
                    break;
                }
            }
        }
        s_flag[1] = todo | (v & 0xff0000u);
    }
    __syncthreads();
    ST(6)
    unsigned todo = __builtin_amdgcn_readfirstlane(s_flag[1]);
    const unsigned flt = todo >> 16;
    todo &= 0xffu;
    int sh = me;
#pragma unroll 1
    while (todo) {
        todo &= ~(1u << sh);
        if (wv < (SHARE / 4 + 63) / 64) {
            const int ol = sh * SHARE + tid * 4;
            const int e0 = ((ol >> 6) << 11) | (j << 6) | (ol & 63);
            const uint32_t po = tid < SHARE / 4 ? (uint32_t)ol * 4u : 0x7fffff00u;
            u32x4 p[8];
#pragma unroll
            for (int k = 0; k < 8; k++)
                p[k] = __builtin_amdgcn_raw_buffer_load_b128(__builtin_amdgcn_make_buffer_rsrc((void*)(partial + (size_t)j * 16 * SLOTO), 0,
                                                                                               ((flt >> k) & 1u) ? 0 : 16 * SLOTO * 4, 0x00020000), po, k * SLOTO * 4, AUX_SC1);
            float g[4][4];
#pragma unroll
            for (int c = 0; c < 2; c++) {
                const int nb = 0x10000 - 0x4000 * __builtin_popcount(flt & (0x55u << c));
                const float u0 = nb ? __uint_as_float(__builtin_amdgcn_readfirstlane(s_flag[12 + 2 * c])) : 0.f;
                const float u1 = nb ? __uint_as_float(__builtin_amdgcn_readfirstlane(s_flag[13 + 2 * c])) : 0.f;
#pragma unroll
                for (int q = 0; q < 4; q++) {
                    const uint32_t a = p[c][q] + p[2 + c][q], b = p[4 + c][q] + p[6 + c][q];
                    g[q][2 * c] = (float)((int)((a & 0xffffu) + (b & 0xffffu)) - nb) * u0;
                    g[q][2 * c + 1] = (float)((int)((a >> 16) + (b >> 16)) - nb) * u1;
                }
            }
            if (__builtin_expect(flt != 0u, 0)) {
#pragma unroll
                for (int c = 0; c < 2; c++) {
#pragma unroll 1
                    for (int k = c; k < 8; k += 2) {
                        if (!((flt >> k) & 1u)) continue;
                        const u32x4 f0 = __builtin_amdgcn_raw_buffer_load_b128(rsP, po, k * SLOTO * 4, AUX_SC1);
                        const u32x4 f1 = __builtin_amdgcn_raw_buffer_load_b128(rsP, po, (8 + k) * SLOTO * 4, AUX_SC1);
#pragma unroll
                        for (int q = 0; q < 4; q++) { g[q][2 * c] += __uint_as_float(f0[q]); g[q][2 * c + 1] += __uint_as_float(f1[q]); }
                    }
                }
            }
            float wu[16];
            load_w(wup, wu);
            u32x4 ov[2];
#pragma unroll
            for (int q = 0; q < 4; q++) {
#pragma unroll
                for (int k = 0; k < 4; k++) g[q][k] *= rs[q][k];
                const uint32_t m0 = em[q >> 1][(q & 1) * 2], m1 = em[q >> 1][(q & 1) * 2 + 1];
                const float mm[4] = {bf2f(m0 & 0xffff), bf2f(m0 >> 16), bf2f(m1 & 0xffff), bf2f(m1 >> 16)};
                float o[4];
#pragma unroll
                for (int k = 0; k < 4; k++) o[k] = mm[k] + (g[q][0] * wu[k] + g[q][1] * wu[4 + k] + g[q][2] * wu[8 + k] + g[q][3] * wu[12 + k]) * rg[q][k];
                ov[q >> 1][(q & 1) * 2] = pk_bf16(o[0], o[1]);
                ov[q >> 1][(q & 1) * 2 + 1] = pk_bf16(o[2], o[3]);
            }
            if (tid < SHARE / 4 && e0 < E) { *(u32x4*)(out + e0) = ov[0]; *(u32x4*)(out + e0 + 2) = ov[1]; }
        }
        if (todo) { sh = __builtin_ctz(todo); edge_loads(sh); factors(); }
    }
    ST(7)
    ST_OUT(8)
}

__global__ __launch_bounds__(256) void gen_scatter_kernel(const int* __restrict__ iin, const int* __restrict__ iout,
                                                          const uint16_t* __restrict__ ang, const uint16_t* __restrict__ freq,
                                                          const u32x2* __restrict__ m, const uint16_t* __restrict__ wdown,
                                                          float* __restrict__ agg, int T) {
    float w[16];
    load_w(wdown, w);
    const float fr = bf2f(freq[0]);
    for (long t = (long)blockIdx.x * 256 + threadIdx.x; t < T; t += (long)gridDim.x * 256) {
        const float c = cosf(fr * bf2f(ang[t]));
        uint32_t r[4];
        md_of(m[iin[t]], w, r);
        float* q = agg + (size_t)iout[t] * 4;
#pragma unroll
        for (int k = 0; k < 4; k++) atomicAdd(q + k, c * bf2f(r[k]));
    }
}
__global__ __launch_bounds__(256) void gen_final_kernel(const u32x2* __restrict__ m, const uint16_t* __restrict__ dist,
                                                        const uint16_t* __restrict__ centers, const uint16_t* __restrict__ wrbf,
                                                        const uint16_t* __restrict__ wsbf, const uint16_t* __restrict__ wup,
                                                        const float* __restrict__ agg, u32x2* __restrict__ out, int E) {
    const int e = blockIdx.x * 256 + threadIdx.x;
    if (e >= E) return;
    const float d = bf2f(dist[e]);
    float rs[4] = {0, 0, 0, 0}, rg[4] = {0, 0, 0, 0};
#pragma unroll 1
    for (int r = 0; r < 8; r++) {
        const float x = d - bf2f(centers[r]);
        const float rb = __expf(-10.0f * x * x);
#pragma unroll
        for (int k = 0; k < 4; k++) { rs[k] += rb * bf2f(wsbf[r * 4 + k]); rg[k] += rb * bf2f(wrbf[r * 4 + k]); }
    }
    float wu[16];
    load_w(wup, wu);
    const u32x2 mv = m[e];
    const float mm[4] = {bf2f(mv.x & 0xffff), bf2f(mv.x >> 16), bf2f(mv.y & 0xffff), bf2f(mv.y >> 16)};
    float g[4], o[4];
#pragma unroll
    for (int k = 0; k < 4; k++) g[k] = agg[(size_t)e * 4 + k] * rs[k];
#pragma unroll
    for (int k = 0; k < 4; k++) o[k] = mm[k] + (g[0] * wu[k] + g[1] * wu[4 + k] + g[2] * wu[8 + k] + g[3] * wu[12 + k]) * rg[k];
    const u32x2 ov = {f2bf(o[0]) | (f2bf(o[1]) << 16), f2bf(o[2]) | (f2bf(o[3]) << 16)};
    out[e] = ov;
}

extern "C" void kernel_launch(void* const* d_in, const int* in_sizes, int n_in,
                              void* d_out, int out_size, void* d_ws, size_t ws_size,
                              hipStream_t stream) {
    const u32x2* m = (const u32x2*)d_in[0];
    const int* iin = (const int*)d_in[1];
    const int* iout = (const int*)d_in[2];
    const uint16_t* dist = (const uint16_t*)d_in[3];
    const uint16_t* ang = (const uint16_t*)d_in[4];
    const uint16_t* centers = (const uint16_t*)d_in[5];
    const uint16_t* freq = (const uint16_t*)d_in[6];
    const uint16_t* wrbf = (const uint16_t*)d_in[7];
    const uint16_t* wsbf = (const uint16_t*)d_in[8];
    const uint16_t* wdown = (const uint16_t*)d_in[9];
    const uint16_t* wup = (const uint16_t*)d_in[10];
    const int E = in_sizes[3];
    const int T = in_sizes[1];
    char* ws = (char*)d_ws;
    const int G = T >> 2;
    const int nfull = G / (NWG * GR);
    const int grem = (G - nfull * NWG * GR + NWG - 1) / NWG;
    const int lcap = (nfull * GR + grem) / 6 + 2;
    size_t off = 0;
    unsigned* meta = (unsigned*)(ws + off); off += M_WORDS * 4;
    uint32_t* tcnt = (uint32_t*)(ws + off); off += (size_t)NWG * NB;
    u32x2* stat = (u32x2*)(ws + off); off += 4 * NWG * 8;
    uint32_t* mdc = (uint32_t*)(ws + off); off += (size_t)128 * SLOTI * 4;
    uint32_t* partial = (uint32_t*)(ws + off); off += (size_t)32 * 16 * SLOTO * 4;
    char* recs = ws + off;
    const size_t rbytes = (size_t)LBASE * 128 + (size_t)NWG * lcap * 130;
    off += rbytes;
    if (E > EMAX || (E & 3) || E < 1 || rbytes > 0x7f000000u || off > ws_size) {
        (void)hipMemsetAsync(ws, 0, (size_t)E * 16, stream);
        gen_scatter_kernel<<<4096, 256, 0, stream>>>(iin, iout, ang, freq, m, wdown, (float*)ws, T);
        gen_final_kernel<<<(E + 255) / 256, 256, 0, stream>>>(m, dist, centers, wrbf, wsbf, wup, (const float*)ws, (u32x2*)d_out, E);
        return;
    }
    int ks = T / (NWG * NB * 24);
    if (ks > KMAX) ks = KMAX;
    const float zf = ZSIG * ZSIG * (float)T / (8.0f * (float)E * (float)E);
    (void)hipFuncSetAttribute((const void*)part_kernel, hipFuncAttributeMaxDynamicSharedMemorySize, P_LDS);
    (void)hipFuncSetAttribute((const void*)acc_kernel, hipFuncAttributeMaxDynamicSharedMemorySize, A_LDS);
    part_kernel<<<NWG, NT, P_LDS, stream>>>(iin, iout, ang, freq, m, wdown, stat, mdc, meta, tcnt, recs, T, E, nfull, grem, ks, lcap);
    if (!(ABL & 7)) acc_kernel<<<NWG, NT, A_LDS, stream>>>(recs, (const u32x4*)tcnt, mdc, (const u32x4*)stat, iin, iout, ang, m, wdown, freq, dist, centers, wrbf, wsbf, wup,
                                           partial, meta, (u32x2*)d_out, E, ks, lcap, zf);
}
```

```cpp
#include <hip/hip_runtime.h>
#include <hip/hip_bf16.h>
#include <stdint.h>
#pragma clang diagnostic ignored "-Winline-asm"

typedef unsigned int u32x4 __attribute__((ext_vector_type(4)));
typedef unsigned int u32x2 __attribute__((ext_vector_type(2)));
typedef unsigned short u16x2 __attribute__((ext_vector_type(2)));
typedef _Float16 f16x2 __attribute__((ext_vector_type(2)));
typedef __bf16 bf16x2 __attribute__((ext_vector_type(2)));
typedef float f32x2 __attribute__((ext_vector_type(2)));
typedef unsigned long long u64;

static __device__ __forceinline__ float bf2f(uint32_t bits16) { return __uint_as_float(bits16 << 16); }
static __device__ __forceinline__ uint32_t f2bf(float f) {
    uint32_t u = __float_as_uint(f);
    if ((u & 0x7fffffffu) > 0x7f800000u) return (u >> 16) | 0x40;
    return (u + 0x7fffu + ((u >> 16) & 1u)) >> 16;
}
static __device__ __forceinline__ float h2f(uint32_t bits16) { return (float)__builtin_bit_cast(_Float16, (unsigned short)bits16); }

#define NB 2048
#define WNULL 0x80000000u
#define CSTEP 0x2aabu
#define CINIT 205u
#define CHV (12u * CSTEP)
#define SLOTO 25088
#define SLOTI 12544
#define EMAX (SLOTO * 32)
#define NT 1024
#define NWG 256
#define KMAX 4
#define NTAIL 4
#define LAY 3
#define SC 257u
#define SR 1u
#define SK (128u * SC)
#define SS (4u * SK)
#define MAINL (16u * SS)
#define UINC (0x80000001u + SK * 64u)
#define TST 257u
#define TBASE MAINL
#define TPL (NB * TST)
#define SBASE (TBASE + 2u * TPL)
#define LBASE (SBASE + 128u * 2u * NWG * 9u)
#define NDYN 3
#define GR 2048
#define NTS 5
#define M_DONE 0
#define M_TILE (32 * 64)
#define M_LOG (M_TILE + 64)
#define M_E0 (M_LOG + 64)
#define M_WORDS (M_E0 + 64)
#define TSC1 0
#define ALT 0
#define POS(ff) (ALT ? ((ff) * 11) & 15 : (ff))
#define AUX_NT 2
#define AUX_SC1 16

static __device__ __forceinline__ uint32_t row_max(uint32_t x) {
    x = max(x, (uint32_t)__builtin_amdgcn_update_dpp(0, x, 0x111, 0xf, 0xf, false));
    x = max(x, (uint32_t)__builtin_amdgcn_update_dpp(0, x, 0x112, 0xf, 0xf, false));
    x = max(x, (uint32_t)__builtin_amdgcn_update_dpp(0, x, 0x114, 0xf, 0xf, false));
    return max(x, (uint32_t)__builtin_amdgcn_update_dpp(0, x, 0x118, 0xf, 0xf, false));
}
static __device__ __forceinline__ uint32_t wave_max(uint32_t x) {
    x = row_max(x);
    x = max(x, (uint32_t)__builtin_amdgcn_update_dpp(0, x, 0x142, 0xa, 0xf, false));
    x = max(x, (uint32_t)__builtin_amdgcn_update_dpp(0, x, 0x143, 0xc, 0xf, false));
    return __builtin_amdgcn_readlane(x, 63);
}
static __device__ __forceinline__ float row_sum(float x) {
    x += __int_as_float(__builtin_amdgcn_update_dpp(0, __float_as_int(x), 0x111, 0xf, 0xf, false));
    x += __int_as_float(__builtin_amdgcn_update_dpp(0, __float_as_int(x), 0x112, 0xf, 0xf, false));
    x += __int_as_float(__builtin_amdgcn_update_dpp(0, __float_as_int(x), 0x114, 0xf, 0xf, false));
    return x + __int_as_float(__builtin_amdgcn_update_dpp(0, __float_as_int(x), 0x118, 0xf, 0xf, false));
}
static __device__ __forceinline__ float wave_sum(float x) {
    x = row_sum(x);
    x += __int_as_float(__builtin_amdgcn_update_dpp(0, __float_as_int(x), 0x142, 0xa, 0xf, false));
    x += __int_as_float(__builtin_amdgcn_update_dpp(0, __float_as_int(x), 0x143, 0xc, 0xf, false));
    return __int_as_float(__builtin_amdgcn_readlane(__float_as_int(x), 63));
}
static __device__ __forceinline__ uint32_t bfi(uint32_t mask, uint32_t a, uint32_t b) {
    return __builtin_amdgcn_bitop3_b32(mask, a, b, 0xca);
}
static __device__ __forceinline__ void opaque(int& x) { asm volatile("" : "+v"(x)); }
template <uint32_t C> static __device__ __forceinline__ uint32_t vconst() {
    uint32_t r;
    asm volatile("v_mov_b32 %0, %1" : "=v"(r) : "n"(C));
    return r;
}
static __device__ __forceinline__ uint32_t pk_bf16(float a, float b) {
    const f32x2 v = {a, b};
    return __builtin_bit_cast(uint32_t, __builtin_convertvector(v, bf16x2));
}
static __device__ __forceinline__ void pin(uint32_t& x) { asm volatile("" : "+v"(x)); }

#define RTS(k)
#define ST_DECL
#define ST(i)
#define ST_OUT(off)

static __device__ __forceinline__ void md_of(u32x2 v, const float* w, uint32_t* r) {
    float a0 = bf2f(v.x & 0xffff), a1 = bf2f(v.x >> 16), a2 = bf2f(v.y & 0xffff), a3 = bf2f(v.y >> 16);
#pragma unroll
    for (int j = 0; j < 4; j++) r[j] = f2bf(a0 * w[j] + a1 * w[4 + j] + a2 * w[8 + j] + a3 * w[12 + j]);
}
static __device__ __forceinline__ void load_w(const uint16_t* __restrict__ wdown, float* w) {
    u32x4 wv0 = *(const u32x4*)wdown, wv1 = *(const u32x4*)(wdown + 8);
#pragma unroll
    for (int i = 0; i < 4; i++) {
        w[2 * i] = bf2f(wv0[i] & 0xffffu); w[2 * i + 1] = bf2f(wv0[i] >> 16);
        w[8 + 2 * i] = bf2f(wv1[i] & 0xffffu); w[8 + 2 * i + 1] = bf2f(wv1[i] >> 16);
    }
}
#define LDS __attribute__((address_space(3)))
#define P_CNT 0
#define P_FLAG ((NB + 64) * 4)
#define P_LST (P_FLAG + 256)
#define LCAP 32
#define P_TAB (P_LST + 4 * LCAP * 16)
#define P_BUF (P_TAB + NB * 4)
#define P_LDS (P_BUF + NB * 64)
#define PD 2
#define LPOS 0xa98763
#define ABL 0
#define SETP(n)
#define RMAXREV 40.0f
#define HB 88
#define HREG(S, D) (HB + ((S) & 3) * 10 + (((S) >> 2) + (D)) * 2)

template <int N> struct IC { static constexpr int value = N; };
struct Raw { u32x4 o[2], i[2]; u32x2 a[2]; };
static __device__ __forceinline__ uint32_t lds_ld(uint32_t a) { return *(const LDS uint32_t*)(uintptr_t)a; }
static __device__ __forceinline__ void lds_st(uint32_t a, uint32_t v) { *(LDS uint32_t*)(uintptr_t)a = v; }
static __device__ __forceinline__ void lds_or(uint32_t a, uint32_t v) {
    __hip_atomic_fetch_or((LDS uint32_t*)(uintptr_t)a, v, __ATOMIC_RELAXED, __HIP_MEMORY_SCOPE_WORKGROUP);
}
static __device__ __forceinline__ uint32_t lds_add(uint32_t a, uint32_t v) {
    return __hip_atomic_fetch_add((LDS uint32_t*)(uintptr_t)a, v, __ATOMIC_RELAXED, __HIP_MEMORY_SCOPE_WORKGROUP);
}
static __device__ __forceinline__ uint32_t slot_w(uint32_t ca, uint32_t cv) {
    return (ca << 4) | __builtin_amdgcn_bitop3_b32(cv >> 11, ca, 0x3cu, 0x28);
}
static __device__ __forceinline__ uint32_t slot_x2(uint32_t aw, uint32_t ca) {
    return __builtin_amdgcn_bitop3_b32(aw, ca, 0xcu, 0x72);
}
static __device__ __forceinline__ uint32_t slot_x(uint32_t aw) { return slot_x2(aw, aw >> 4); }
template <int R> static __device__ __forceinline__ u32x2 hget() {
    u32x2 d;
    asm volatile("v_mov_b64 %0, v[%1:%2]" : "=v"(d) : "n"(R), "n"(R + 1));
    return d;
}
static __device__ __forceinline__ u32x2 hget_at(int r) {
    u32x2 d;
    asm volatile("s_set_gpr_idx_on %2, gpr_idx(SRC0)\n\ts_nop 1\n\tv_mov_b32 %0, v88\n\tv_mov_b32 %1, v89\n\ts_set_gpr_idx_off"
                 : "=&v"(d.x), "=&v"(d.y) : "s"(r));
    return d;
}
static __device__ __forceinline__ void hset_at(int r, u32x2 d) {
    asm volatile("s_set_gpr_idx_on %2, gpr_idx(DST)\n\ts_nop 1\n\tv_mov_b32 v88, %0\n\tv_mov_b32 v89, %1\n\ts_set_gpr_idx_off"
                 :: "v"(d.x), "v"(d.y), "s"(r));
}

template <int S, int D> static __device__ __forceinline__ uint32_t take(uint32_t V, uint32_t u16, uint32_t la, u32x2 nul) {
    const uint32_t a = __builtin_amdgcn_bitop3_b32(V, u16, 15u, 0x54);
    asm volatile("v_mov_b64 v[%4:%4+1], v[%3:%3+1]\n\tv_cmpx_ne_u32 vcc, -16, %0\n\tds_wrxchg_rtn_b64 v[%4:%4+1], %1, %2 offset:%5\n\ts_mov_b64 exec, -1"
                 :: "v"(a), "v"(la), "v"(nul), "n"(HREG(S, D)), "n"(HREG(S, 1 - D)), "n"(S * 256) : "memory", "vcc");
    return a;
}
#define SK0 "s_bitcmp1_b32 %3, %4\n\ts_cbranch_scc0 1f\n\t"
#define SK1 "\n1:"
template <int S, int W> static __device__ __forceinline__ void put(u32x4 rsA, uint32_t ad, uint32_t pm) {
    if (POS(S) >= 16 - NTS) asm volatile(SK0 "s_waitcnt lgkmcnt(%5)\n\tbuffer_store_dwordx4 v[%2:%2+3], %0, %1, 0 offen nt\n\ts_nop 1" SK1 :: "v"(ad), "s"(rsA), "n"(HREG(S, 0)), "s"(pm), "n"(2 * S), "n"(W) : "memory", "scc");
    else asm volatile(SK0 "s_waitcnt lgkmcnt(%5)\n\tbuffer_store_dwordx4 v[%2:%2+3], %0, %1, 0 offen sc1\n\ts_nop 1" SK1 :: "v"(ad), "s"(rsA), "n"(HREG(S, 0)), "s"(pm), "n"(2 * S), "n"(W) : "memory", "scc");
}

__global__ __launch_bounds__(NT) __attribute__((amdgpu_num_vgpr(HB / 2))) void part_kernel(const int* __restrict__ iin, const int* __restrict__ iout,
                                                  const uint16_t* __restrict__ ang, const uint16_t* __restrict__ freq,
                                                  const u32x2* __restrict__ m, const uint16_t* __restrict__ wdown,
                                                  u32x2* __restrict__ stat, uint32_t* __restrict__ mdc,
                                                  unsigned* __restrict__ meta, uint32_t* __restrict__ tcnt,
                                                  char* __restrict__ recs,
                                                  int T, int E, int ntile, int nstat, int ks, int lcap) {
    ST_DECL
    RTS(0)
    const int tid0 = threadIdx.x;
    const int wv = __builtin_amdgcn_readfirstlane(tid0 >> 6);
    const int G = T >> 2;
    const int pos = (int)((blockIdx.x & 7) << 5) | (int)(blockIdx.x >> 3);
    float frr;
    const uint32_t gbase = (LBASE + (uint32_t)lcap) * 128u;
    const uint32_t rbytes = gbase + (uint32_t)lcap * 2u;
    const __amdgpu_buffer_rsrc_t rsR = __builtin_amdgcn_make_buffer_rsrc(recs, 0, (int)rbytes, 0x00020000);
    const u32x4 rsA = {(uint32_t)(uintptr_t)recs, (uint32_t)((uintptr_t)recs >> 32) & 0xffffu, (ABL & 2) ? 0u : rbytes, 0x00020000u};
    const uint32_t g2 = 2u * (uint32_t)ks;
    const uint32_t spec0 = gbase - 4u;
    auto fixed_tile = [&](int r) __attribute__((always_inline)) -> int { return r * NWG + ((pos + r * 37) & (NWG - 1)); };
    int tl0 = fixed_tile(0), tl1 = fixed_tile(1), tl2;
    uint32_t tk = 0u;
    const __amdgpu_buffer_rsrc_t rsO = __builtin_amdgcn_make_buffer_rsrc((void*)iout, 0, G * 16, 0x00020000);
    const __amdgpu_buffer_rsrc_t rsI = __builtin_amdgcn_make_buffer_rsrc((void*)iin, 0, G * 16, 0x00020000);
    const __amdgpu_buffer_rsrc_t rsG = __builtin_amdgcn_make_buffer_rsrc((void*)ang, 0, G * 8, 0x00020000);
    uint32_t t8 = (uint32_t)tid0 * 8u;
    auto load_one = [&](Raw& r, int tl, int idx) __attribute__((always_inline)) {
        const int j = idx < 4 ? idx >> 1 : idx & 1;
        const uint32_t g0 = (uint32_t)(((ABL & 1) ? tl & 31 : tl) * GR + j * NT);
        __builtin_amdgcn_sched_barrier(0);
        if (idx >= 4) r.a[j] = __builtin_amdgcn_raw_buffer_load_b64(rsG, t8, g0 * 8u, (ABL & 1) ? 0 : AUX_NT);
        else if (idx & 1) r.i[j] = __builtin_amdgcn_raw_buffer_load_b128(rsI, t8 + t8, g0 * 16u, (ABL & 1) ? 0 : AUX_NT);
        else r.o[j] = __builtin_amdgcn_raw_buffer_load_b128(rsO, t8 + t8, g0 * 16u, (ABL & 1) ? 0 : AUX_NT);
        __builtin_amdgcn_sched_barrier(0);
    };
    auto draw = [&](uint32_t at) __attribute__((always_inline)) -> uint32_t {
        return __hip_atomic_fetch_add((unsigned*)((char*)meta + at), vconst<1>(), __ATOMIC_RELAXED, __HIP_MEMORY_SCOPE_AGENT);
    };
    uint32_t la, tw;
    u32x2 nul;
    uint32_t U[2];
    {
        const uint32_t t = (uint32_t)tid0;
        la = P_BUF + ((t >> 5) << 12) + ((t & 0x18u) << 3) + ((t & 3u) << 4) + ((t & 4u) << 1);
        tw = P_TAB + ((t >> 5) << 8) + ((t & 1u) << 7) + ((t & 0x1eu) << 1);
        const uint32_t q = ((t >> 3) ^ (t >> 1 & 2u)) & 3u;
        nul.x = (0x7u >> q) << 31; nul.y = (0xbu >> q) << 31;
        U[0] = (POS((t >> 1) & 15u) * SS + ((t & 1u) * 64u + (t >> 5)) * SC + blockIdx.x * SR) * 128u + (0x80000000u - SK * 64u) + 8u - g2;
        U[1] = U[0] + 32u * SC * 128u;
    }
    auto log_line = [&](bool lg, uint32_t b) __attribute__((always_inline)) -> uint32_t {
        uint32_t lp = 0u;
        if (lg && (t8 & 0x38u) == 0u) {
            lp = draw(vconst<M_LOG * 4>());
            __builtin_amdgcn_raw_buffer_store_b16((uint16_t)b, rsR, gbase + lp * 2u, 0, 0);
        }
        return LBASE + (uint32_t)__builtin_amdgcn_ds_bpermute((int)((t8 >> 1) & 0xe0u), (int)lp);
    };
    auto general = [&](int D, bool all, u64 sm) __attribute__((always_inline)) {
        uint32_t todo = __builtin_amdgcn_readfirstlane((uint32_t)sm | (uint32_t)(sm >> 32));
#pragma unroll 1
        while (todo) {
            const uint32_t S = (uint32_t)__builtin_ctz(todo) >> 1;
            todo &= ~(3u << (2u * S));
            pin(t8); pin(la);
            const uint32_t V = lds_ld(P_TAB + (t8 & ~63u) + S * 4u), g = V & 15u;
            const uint32_t col = POS(S) * 128u + ((t8 >> 1) & 0x60u) + (t8 >> 8);
            const bool act = all ? V != 0xffffffffu : (V & 0x80000009u) == 9u;
            const int hr = HREG((int)S, D) - HB;
            uint32_t ln = log_line(act && g > 8u + NTAIL, col);
            if (act) {
                const u32x2 N = *(const LDS u32x2*)(uintptr_t)(la + S * 256u);
                *(LDS u32x2*)(uintptr_t)(la + S * 256u) = nul;
                lds_st(P_TAB + (t8 & ~63u) + S * 4u, vconst<0xffffffffu>());
                if (!(g & 1u)) hset_at(hr, N);
                else {
                    uint32_t a, st;
                    if (g < 8u) { a = (V & ~15u) + ((t8 & 0x38u) << 1); st = 8u; }
                    else {
                        if (g <= 8u + NTAIL) ln = TBASE + ((g - 8u) >> 1) * TPL + col * TST + blockIdx.x;
                        a = ln * 128u + (la & 0x38u); st = 64u;
                    }
                    __builtin_amdgcn_raw_buffer_store_b64(hget_at(hr), rsR, a, 0, AUX_SC1);
                    __builtin_amdgcn_raw_buffer_store_b64(N, rsR, a + st, 0, AUX_SC1);
                }
            }
        }
    };
    unsigned epoch = 0;
    uint32_t pm = 0u;
    u64 fm = 0ull, sm = 0ull;
    auto look = [&](u32x2 c) __attribute__((always_inline)) {
        uint32_t V[2];
        const bool late = __builtin_amdgcn_ballot_w64(((U[0] | U[1]) & 8u) != 0u) != 0ull;
        epoch++;
        asm volatile("v_mov_b32 %0, -1\n\tv_mov_b32 %1, -1\n\t"
                     "v_cmpx_le_u32 %6, %7, %2\n\tv_mov_b32 %0, %4\n\tv_add_u32 %4, %9, %4\n\tv_subrev_u32 %2, %8, %2\n\ts_mov_b64 exec, -1\n\t"
                     "v_cmpx_le_u32 vcc, %8, %3\n\tv_mov_b32 %1, %5\n\tv_add_u32 %5, %9, %5\n\tv_subrev_u32 %3, %8, %3\n\t"
                     "s_or_b64 %6, %6, exec\n\ts_mov_b64 exec, -1"
                     : "=&v"(V[0]), "=&v"(V[1]), "+v"(c.x), "+v"(c.y), "+v"(U[0]), "+v"(U[1]), "=&s"(fm) : "s"(CHV), "n"(CHV), "n"(UINC) : "vcc", "scc");
        *(LDS u32x2*)(uintptr_t)(P_CNT + t8) = c;
        lds_st(tw, V[0]);
        lds_st(tw + 64u, V[1]);
        if (wv == 0) lds_st(P_CNT + NB * 4 + (t8 >> 1), vconst<0x40000000u>());
        const u64 pb = __builtin_amdgcn_ballot_w64((int)(V[0] & V[1]) >= 0);
        pm = (uint32_t)pb | (uint32_t)(pb >> 32);
        pm |= pm >> 1;
        if (ABL & 4) pm = 0;
        sm = 0ull;
        if (__builtin_expect(late, 0)) {
            sm = __builtin_amdgcn_ballot_w64((V[0] & 0x80000009u) == 9u || (V[1] & 0x80000009u) == 9u);
#pragma unroll
            for (int e = 0; e < 2; e++)
                if ((V[e] & 0x8000000fu) == 9u + NTAIL) U[e] -= 2u * UINC;
        }
    };
    u32x4 Va, Vb;

    Raw raw[PD];
    {
        const int tid = tid0;
        asm volatile("" ::: "v88", "v127");
        const uint16_t fq = freq[0];
#pragma unroll
        for (int d = 0; d < 6; d++) load_one(raw[0], tl0, d);
        asm("s_nop 0\n\tv_readfirstlane_b32 %0, %1" : "=s"(frr) : "v"(bf2f(fq) * 0.15915494309189535f));
#pragma unroll
        for (int s = 0; s < 16; s++) *(LDS u32x2*)(uintptr_t)(la + s * 256) = nul;
        const u32x2 ci = {CINIT, CINIT};
        *(LDS u32x2*)(uintptr_t)(P_CNT + tid * 8) = ci;
        lds_st(tw, 0xffffffffu);
        lds_st(tw + 64u, 0xffffffffu);
#pragma unroll
        for (int d = 0; d < 6; d++)
            if (((LPOS >> (4 * d)) & 15) < 4) load_one(raw[1], tl1, d);
        if (tid < 64) lds_st(P_CNT + (NB + tid) * 4, 0x40000000u);
        if (tid < 8) lds_st(P_FLAG + tid * 4, tid == 0 ? 0xffffffffu : 0u);
        Va = (u32x4)(0xffffffffu); Vb = Va;
    }
    pin(la); pin(tw);
    __syncthreads();
    ST(0)

    uint32_t w[8], aw[8], xv[8];
    u64 nm[8];
#pragma unroll
    for (int k = 0; k < 8; k++) { w[k] = 0u; aw[k] = 0u; xv[k] = 0u; nm[k] = ~0ull; }
    auto later2 = [&](int k) __attribute__((always_inline)) {
        uint32_t x0, x1;
        asm volatile("v_lshrrev_b32 %0, 4, %2\n\tv_lshrrev_b32 %1, 4, %5\n\tv_bitop3_b32 %0, %2, %0, 12 bitop3:0x72\n\tv_bitop3_b32 %1, %5, %1, 12 bitop3:0x72\n\t"
                     "s_not_b64 exec, %8\n\tds_write_b32 %2, %3 offset:%10\n\tds_or_b32 %0, %4 offset:%10\n\t"
                     "s_not_b64 exec, %9\n\tds_write_b32 %5, %6 offset:%10\n\tds_or_b32 %1, %7 offset:%10\n\ts_mov_b64 exec, -1"
                     : "=&v"(x0), "=&v"(x1)
                     : "v"(aw[k]), "v"(w[k]), "v"(xv[k]), "v"(aw[k + 1]), "v"(w[k + 1]), "v"(xv[k + 1]), "s"(nm[k]), "s"(nm[k + 1]), "n"(P_BUF) : "memory", "scc");
    };
    auto drain = [&]() __attribute__((always_inline)) {
        if (wv == 1) {
            const uint32_t q = (epoch + 1u) & 3u;
            const uint32_t n = __builtin_amdgcn_readfirstlane(lds_ld(vconst<P_FLAG + 16>() + q * 4u));
            if (__builtin_expect(n != 0u, 0)) {
                pin(t8);
                const uint32_t l = (t8 >> 3) & 63u;
                if (l < min(n, (uint32_t)LCAP)) {
                    const u32x4 e = *(const LDS u32x4*)(uintptr_t)(P_LST + (q * LCAP + l) * 16u);
                    lds_st(P_BUF + e.x, e.y);
                    lds_or(P_BUF + slot_x(e.x), e.z);
                }
                lds_st(vconst<P_FLAG + 16>() + q * 4u, vconst<0>());
            }
        }
    };
    auto again = [&](int D) __attribute__((always_inline)) {
        do {
            general(D, true, fm);
            __syncthreads();
            later2(0); later2(2); later2(4); later2(6);
            drain();
#pragma unroll
            for (int k = 0; k < 8; k++) {
                pin(aw[k]);
                bool due = false;
                if (aw[k] & 0x80000000u) {
                    xv[k] -= CHV;
                    if (xv[k] < CHV) {
                        const uint32_t f = (aw[k] >> 16) & 0x3ffu;
                        aw[k] = slot_w(aw[k] & 0xffffu, xv[k]);
                        xv[k] = f << ((xv[k] >> 8) & 31u);
                        due = true;
                    } else lds_st(vconst<P_FLAG>(), epoch + 1u);
                }
                nm[k] = ~__builtin_amdgcn_ballot_w64(due);
            }
            look(*(const LDS u32x2*)(uintptr_t)(P_CNT + t8));
            asm volatile("s_waitcnt lgkmcnt(0)" ::: "memory");
            __syncthreads();
        } while (__builtin_amdgcn_readfirstlane(lds_ld(vconst<P_FLAG>())) == epoch);
    };
    auto round = [&](auto DD, auto LL, Raw& rc, Raw& rn, int rd) __attribute__((always_inline)) {
        constexpr int D = decltype(DD)::value;
        constexpr bool LAST = decltype(LL)::value;
        const int ng = min(GR, G - tl0 * GR);
        auto hook = [&](int p) __attribute__((always_inline)) {
#pragma unroll
            for (int q = 0; q < 6; q++)
            {
                constexpr int dummy = 0; (void)dummy;
                const int lp = (LPOS >> (4 * q)) & 15;
                if (!LAST && lp < 4 && lp == p) load_one(rc, tl2, q);
                if (!LAST && lp >= 4 && (lp < 8 ? lp : lp - 8) == p) load_one(rn, tl1, q);
            }
        };
        uint32_t wn[8], ca[8], px[4], cv[8];
        float mx;
        float cs[2];
        auto convA = [&](int q) __attribute__((always_inline)) {
            if (LAST) return;
            const uint32_t a = rc.a[q >> 1][q & 1];
            const f32x2 av = {__uint_as_float(a << 16), __uint_as_float(a & 0xffff0000u)};
            f32x2 rv;
            asm("v_pk_mul_f32 %0, %1, %2 op_sel_hi:[1,0]" : "=v"(rv) : "v"(av), "s"((u64)__float_as_uint(frr)));
            mx = __builtin_elementwise_maximum(__builtin_elementwise_maximum(__builtin_fabsf(rv.x), __builtin_fabsf(rv.y)), (q & 1) ? mx : 0.f);
            cs[0] = __builtin_amdgcn_cosf(rv.x); cs[1] = __builtin_amdgcn_cosf(rv.y);
            asm volatile("" : "+v"(mx), "+v"(cs[0]), "+v"(cs[1]));
            __builtin_amdgcn_sched_barrier(0);
        };
        auto convB = [&](int k) __attribute__((always_inline)) {
            if (LAST) return;
            const uint32_t o = rc.o[k >> 2][k & 3], i = rc.i[k >> 2][k & 3];
            wn[k] = (__builtin_amdgcn_alignbit(bfi(63u, o, o >> 5), __float_as_uint(cs[k & 1]), 30u) << 14) | (i >> 6);
            ca[k] = (bfi(63u, i, o) << 2) & 0x1ffcu;
            pin(wn[k]); pin(ca[k]);
            __builtin_amdgcn_sched_barrier(0);
        };
        auto convD = [&](int q) __attribute__((always_inline)) {
            if (LAST) return;
            asm volatile("v_cvt_pk_f16_f32 %0, |%1|, |%2|\n\tv_pk_mul_f16 %0, %0, %3 op_sel_hi:[1,0]" : "=v"(px[q]) : "v"(cs[0]), "v"(cs[1]), "s"(0x3ffu));
        };
        uint32_t cst;
        auto check = [&](int j) __attribute__((always_inline)) {
            if (LAST) return;
            if (__builtin_expect((__builtin_amdgcn_ballot_w64(!(mx <= RMAXREV)) | (u64)(uint32_t)(GR - ng)) != 0ull, 0)) {
                pin(t8);
                asm volatile("" : "+v"(rc.a[j]));
                const int lg = j * NT + (int)(t8 >> 3);
#pragma unroll
                for (int k = 0; k < 4; k++) {
                    const uint32_t a = (k & 1) ? (rc.a[j][k >> 1] >> 16) : (rc.a[j][k >> 1] & 0xffffu);
                    const bool nrm = __builtin_fabsf(frr * bf2f(a)) <= RMAXREV;
                    if (!(lg < ng && nrm)) ca[j * 4 + k] = NB * 4u + ((t8 >> 1) & 0xfcu);
                    if (lg < ng && !nrm) __builtin_amdgcn_raw_buffer_store_b32((uint32_t)((tl0 * GR + lg) * 4 + k), rsR, spec0 - draw(vconst<M_LOG * 4 + 4>()) * 4u, 0, 0);
                }
            }
            cst = vconst<CSTEP>();
        };
        auto ticket = [&](int k) __attribute__((always_inline)) {
            if (LAST) return;
            cv[k] = lds_add(P_CNT + ca[k], cst);
            __builtin_amdgcn_sched_barrier(0);
        };
        auto insert = [&](int j) __attribute__((always_inline)) {
            const int k0 = 4 * j;
            const bool odd = __builtin_amdgcn_ballot_w64(max(max(cv[k0], cv[k0 + 1]), max(cv[k0 + 2], cv[k0 + 3])) >= 2u * CHV) != 0ull;
#pragma unroll
            for (int k = k0; k < k0 + 4; k += 2) {
                uint32_t x0, x1;
                asm volatile("v_lshrrev_b32 %6, 11, %8\n\tv_lshrrev_b32 %7, 11, %9\n\t"
                             "v_bitop3_b32 %6, %6, %10, 60 bitop3:0x28\n\tv_bitop3_b32 %7, %7, %11, 60 bitop3:0x28\n\t"
                             "v_lshl_or_b32 %0, %10, 4, %6\n\tv_lshl_or_b32 %1, %11, 4, %7\n\t"
                             "v_lshlrev_b32_sdwa %2, %8, %12 dst_sel:DWORD dst_unused:UNUSED_PAD src0_sel:BYTE_1 src1_sel:WORD_0\n\t"
                             "v_lshlrev_b32_sdwa %3, %9, %12 dst_sel:DWORD dst_unused:UNUSED_PAD src0_sel:BYTE_1 src1_sel:WORD_1\n\t"
                             "v_bitop3_b32 %6, %0, %10, 12 bitop3:0x72\n\tv_bitop3_b32 %7, %1, %11, 12 bitop3:0x72\n\t"
                             "v_cmpx_gt_u32 %4, %15, %8\n\tds_write_b32 %0, %13 offset:%16\n\tds_or_b32 %6, %2 offset:%16\n\ts_mov_b64 exec, -1\n\t"
                             "v_cmpx_gt_u32 %5, %15, %9\n\tds_write_b32 %1, %14 offset:%16\n\tds_or_b32 %7, %3 offset:%16\n\ts_mov_b64 exec, -1"
                             : "=&v"(aw[k]), "=&v"(aw[k + 1]), "=&v"(xv[k]), "=&v"(xv[k + 1]), "=&s"(nm[k]), "=&s"(nm[k + 1]), "=&v"(x0), "=&v"(x1)
                             : "v"(cv[k]), "v"(cv[k + 1]), "v"(ca[k]), "v"(ca[k + 1]), "v"(px[k >> 1]), "v"(wn[k]), "v"(wn[k + 1]), "s"(CHV), "n"(P_BUF) : "memory");
                w[k] = wn[k]; w[k + 1] = wn[k + 1];
                if (__builtin_expect(odd, 0)) {
#pragma unroll
                    for (int q = k; q < k + 2; q++) {
                        nm[q] |= __builtin_amdgcn_ballot_w64(cv[q] >= 2u * CHV);
                        if (cv[q] >= 2u * CHV && cv[q] < 0x40000000u) {
                            const uint32_t lq = (epoch + 2u) & 3u;
                            uint32_t p = LCAP;
                            if (cv[q] < 3u * CHV) p = lds_add(vconst<P_FLAG + 16>() + lq * 4u, vconst<1>());
                            if (p < LCAP) {
                                const u32x2 e = {aw[q], w[q]};
                                *(LDS u32x2*)(uintptr_t)(P_LST + (lq * LCAP + p) * 16u) = e;
                                lds_st(P_LST + (lq * LCAP + p) * 16u + 8u, xv[q]);
                            } else {
                                aw[q] = ca[q] | ((q & 1) ? (px[q >> 1] & 0xffff0000u) : (px[q >> 1] << 16)) | 0x80000000u;
                                xv[q] = cv[q] - CHV;
                                lds_st(vconst<P_FLAG>(), epoch);
                            }
                        }
                    }
                }
            }
        };
        constexpr int P0 = D ? 0 : 3, P1 = D ? 1 : 2, P2 = D ? 2 : 1, P3 = D ? 3 : 0;
        const uint32_t fl = lds_ld(vconst<P_FLAG>());
        if (!LAST) asm volatile("" : "+v"(rc.o[0]), "+v"(rc.o[1]), "+v"(rc.i[0]), "+v"(rc.i[1]), "+v"(rc.a[0]), "+v"(rc.a[1]));
        if (!LAST && rd + 3 >= nstat && wv == 2) {
            pin(tk);
            if ((t8 & 511u) == 0u) {
                lds_st(vconst<P_FLAG + 4>(), tk);
                if ((int)tk < ntile - nstat * NWG) tk = draw(vconst<M_TILE * 4>());
            }
        }
        ST(3)
        pin(t8);
        const uint32_t tr = P_TAB + (t8 & ~63u), u16 = (t8 + t8) & 0x70u;
        convA(0);
        convB(0);
        const bool ag = __builtin_amdgcn_readfirstlane(fl) == epoch;
        if (__builtin_expect(LAST || ag || sm != 0ull, 0)) {
            if (ag) again(D);
            general(D, false, sm);
            Va = *(const LDS u32x4*)(uintptr_t)(P_TAB + (t8 & ~63u) + P0 * 16);
            Vb = *(const LDS u32x4*)(uintptr_t)(P_TAB + (t8 & ~63u) + P1 * 16);
            asm volatile("" : "+v"(Va), "+v"(Vb));
        }
        const uint32_t pq = __builtin_amdgcn_readfirstlane(pm);
        uint32_t ad[8];
        ad[0] = take<4 * P0 + 0, D>(Va[0], u16, la, nul);
        convB(1);
        ad[1] = take<4 * P0 + 1, D>(Va[1], u16, la, nul);
        convD(0);
        convA(1);
        ad[2] = take<4 * P0 + 2, D>(Va[2], u16, la, nul);
        convB(2);
        ad[3] = take<4 * P0 + 3, D>(Va[3], u16, la, nul);
        convB(3);
        put<4 * P0 + 0, LAST ? 3 : 3>(rsA, ad[0], pq);
        ad[4] = take<4 * P1 + 0, D>(Vb[0], u16, la, nul);
        convD(1);
        check(0);
        ticket(0);
        hook(4);
        put<4 * P0 + 1, LAST ? 3 : 4>(rsA, ad[1], pq);
        ad[5] = take<4 * P1 + 1, D>(Vb[1], u16, la, nul);
        convA(2);
        ticket(1);
        put<4 * P0 + 2, LAST ? 3 : 5>(rsA, ad[2], pq);
        ad[6] = take<4 * P1 + 2, D>(Vb[2], u16, la, nul);
        convB(4);
        ticket(2);
        put<4 * P0 + 3, LAST ? 3 : 6>(rsA, ad[3], pq);
        Va = *(const LDS u32x4*)(uintptr_t)(tr + P2 * 16);
        __builtin_amdgcn_sched_barrier(0);
        ad[7] = take<4 * P1 + 3, D>(Vb[3], u16, la, nul);
        convB(5);
        ticket(3);
        SETP(2);
        put<4 * P1 + 0, LAST ? 4 : 8>(rsA, ad[4], pq);
        ad[0] = take<4 * P2 + 0, D>(Va[0], u16, la, nul);
        convD(2);
        convA(3);
        hook(5);
        put<4 * P1 + 1, LAST ? 4 : 7>(rsA, ad[5], pq);
        ad[1] = take<4 * P2 + 1, D>(Va[1], u16, la, nul);
        convB(6);
        put<4 * P1 + 2, LAST ? 4 : 6>(rsA, ad[6], pq);
        ad[2] = take<4 * P2 + 2, D>(Va[2], u16, la, nul);
        convB(7);
        put<4 * P1 + 3, LAST ? 3 : 4>(rsA, ad[7], pq);
        Vb = *(const LDS u32x4*)(uintptr_t)(tr + P3 * 16);
        __builtin_amdgcn_sched_barrier(0);
        ad[3] = take<4 * P2 + 3, D>(Va[3], u16, la, nul);
        convD(3);
        check(1);
        ticket(4);
        SETP(1);
        hook(6);
        put<4 * P2 + 0, LAST ? 4 : 5>(rsA, ad[0], pq);
        ad[4] = take<4 * P3 + 0, D>(Vb[0], u16, la, nul);
        ticket(5);
        put<4 * P2 + 1, LAST ? 4 : 6>(rsA, ad[1], pq);
        ad[5] = take<4 * P3 + 1, D>(Vb[1], u16, la, nul);
        ticket(6);
        put<4 * P2 + 2, LAST ? 4 : 7>(rsA, ad[2], pq);
        ad[6] = take<4 * P3 + 2, D>(Vb[2], u16, la, nul);
        ticket(7);
        SETP(0);
        put<4 * P2 + 3, LAST ? 3 : 7>(rsA, ad[3], pq);
        ad[7] = take<4 * P3 + 3, D>(Vb[3], u16, la, nul);
        hook(7);
        ST(4)
        SETP(PRIO > 1 ? 3 : 0);
        asm volatile("s_waitcnt lgkmcnt(0)" ::: "memory");
        __syncthreads();
        ST(5)
        u32x2 cn;
        uint32_t tv;
        if (!LAST) { cn = *(const LDS u32x2*)(uintptr_t)(P_CNT + t8); tv = lds_ld(vconst<P_FLAG + 4>()); }
        __builtin_amdgcn_sched_barrier(0);
        later2(0);
        put<4 * P3, 15>(rsA, ad[4], pq);
        later2(2);
        put<4 * P3 + 1, 15>(rsA, ad[5], pq);
        drain();
        if (!LAST) {
            tl2 = rd + 2 < nstat ? fixed_tile(rd + 2) : nstat * NWG + (int)__builtin_amdgcn_readfirstlane(tv);
            look(cn);
        }
        hook(0);
        SETP(PRIO > 1 ? 2 : 0);
        later2(4);
        put<4 * P3 + 2, 15>(rsA, ad[6], pq);
        later2(6);
        put<4 * P3 + 3, 15>(rsA, ad[7], pq);
        if (!LAST) {
            hook(1);
            SETP(PRIO > 1 ? 1 : 0);
            insert(0);
            hook(2);
            SETP(0);
            pin(t8);
            Va = *(const LDS u32x4*)(uintptr_t)(P_TAB + (t8 & ~63u) + P3 * 16);
            Vb = *(const LDS u32x4*)(uintptr_t)(P_TAB + (t8 & ~63u) + P2 * 16);
            __builtin_amdgcn_sched_barrier(0);
            insert(1);
            hook(3);
            ST(1)
            SETP(3);
            asm volatile("s_waitcnt lgkmcnt(0)" ::: "memory");
            __syncthreads();
            ST(2)
            tl0 = tl1; tl1 = tl2;
        }
    };
#define HMV(A, B) "v_mov_b64 v[" #A ":" #A "+1], v[" #B ":" #B "+1]\n\t"
#define HMV4(A) HMV(A + 8, A + 6) HMV(A + 6, A + 4) HMV(A + 4, A + 2) HMV(A + 2, A)
    int rd = 0;
#pragma unroll 1
    for (; tl1 < ntile; rd += 2) {
        round(IC<0>(), IC<0>(), raw[0], raw[1], rd);
        round(IC<1>(), IC<0>(), raw[1], raw[0], rd + 1);
    }
    if (tl0 >= ntile) asm volatile(HMV4(88) HMV4(98) HMV4(108) HMV4(118));
    asm volatile("" : "+s"(tl0));
    if (tl0 < ntile) round(IC<0>(), IC<0>(), raw[0], raw[1], rd);
    RTS(1)
    const uint32_t mi = ntile > nstat * NWG ? (uint32_t)(tl0 - ntile) : blockIdx.x;
    const uint32_t il0 = (mi >> 2) * 256u + (mi & 3u) * 16u + (uint32_t)wv;
    u32x2 mv[4], ms;
    {
        const __amdgpu_buffer_rsrc_t rsE = __builtin_amdgcn_make_buffer_rsrc((void*)m, 0, E * 8, 0x00020000);
        ms = __builtin_amdgcn_raw_buffer_load_b64(rsE, (t8 & 511u) * (uint32_t)(E >> 6), 0, 0);
#pragma unroll
        for (int q = 0; q < 4; q++) mv[q] = __builtin_amdgcn_raw_buffer_load_b64(rsE, t8 & 511u, (il0 + 64u * q) * 512u, AUX_NT);
        __builtin_amdgcn_sched_barrier(0);
    }
    round(IC<1>(), IC<1>(), raw[1], raw[0], 0);
    if (__builtin_amdgcn_readfirstlane(lds_ld(P_FLAG + 16 + ((epoch + 2u) & 3u) * 4u)) != 0u) {
        asm volatile("s_waitcnt lgkmcnt(0)" ::: "memory");
        __syncthreads();
        look(*(const LDS u32x2*)(uintptr_t)(P_CNT + t8));
        general(0, true, fm);
        __syncthreads();
        drain();
    }
    asm volatile("" : "+v"(ms), "+v"(mv[0]), "+v"(mv[1]), "+v"(mv[2]), "+v"(mv[3]));
    {
        const u32x2 c = *(const LDS u32x2*)(uintptr_t)(P_CNT + t8);
#pragma unroll
        for (int e = 0; e < 2; e++) {
            const uint32_t g = U[e] & 15u;
            lds_st(tw + e * 64u, g >= 8u ? 0x80000000u : ((U[e] + ((g & 1u) ? 0u : SK * 64u)) & 0x7fffff81u) | (((7u - g) << 1) & 12u));
            *(LDS uint8_t*)(uintptr_t)(P_CNT + (t8 & ~511u) + (((tw - P_TAB) >> 2) & 127u) + e * 16u) =
                (uint8_t)(g < 8u ? 0u : ((g - 8u) >> 1) + (((c[e] + 2u * CSTEP) >> 15) << 2) + (g & 1u) * 0x50u);
        }
        asm volatile("s_waitcnt lgkmcnt(0)" ::: "memory");
        __syncthreads();
        const uint32_t u16 = (t8 + t8) & 0x70u;
        u32x4 En[4];
#pragma unroll
        for (int p = 0; p < 4; p++) En[p] = *(const LDS u32x4*)(uintptr_t)(P_TAB + (t8 & ~63u) + p * 16u);
        const u32x4 cb = *(const LDS u32x4*)(uintptr_t)(P_CNT + (t8 & ~511u) + ((t8 >> 2) & 0x70u));
        u32x2 N[16];
#pragma unroll
        for (int s = 0; s < 16; s++) N[s] = *(const LDS u32x2*)(uintptr_t)(la + s * 256u);
        if (u16 == 0u) *(u32x4*)(tcnt + blockIdx.x * 512u + (t8 >> 4)) = cb;
        const uint32_t sg = P_BUF + ((t8 & ~63u) << 4);
        uint32_t ps;
        auto rest = [&](auto SL) __attribute__((always_inline)) {
            constexpr int s = decltype(SL)::value;
            const uint32_t en = En[s >> 2][s & 3], by = cb[s >> 2] >> ((s & 3) * 8);
            const u32x2 Hr = hget<HREG(s, 0)>();
            const uint32_t at = ps + ((la & 0x38u) ^ ((s & 3) << 4));
            *(LDS u32x2*)(uintptr_t)at = Hr;
            *(LDS u32x2*)(uintptr_t)(at + (by & 0x40u)) = N[s];
            ps += (by & 0x3cu) << 2;
            const u32x2 Hs = (en & 1u) ? Hr : nul;
            const u32x4 d = {Hs.x, Hs.y, N[s].x, N[s].y};
            uint32_t a = __builtin_amdgcn_bitop3_b32(en, u16, 15u, 0x54);
            __builtin_amdgcn_raw_buffer_store_b128(d, rsR, a, 0, POS(s) >= 16 - NTS ? AUX_NT : AUX_SC1);
            if (__builtin_expect(__builtin_amdgcn_ballot_w64((en & 12u) != 0u) != 0ull, 0)) {
                const u32x4 dn = {nul.x, nul.y, nul.x, nul.y};
#pragma unroll 1
                for (uint32_t k = (en >> 2) & 3u; k > 0u; k--) {
                    a += SK * 128u;
                    __builtin_amdgcn_raw_buffer_store_b128(dn, rsR, a, 0, AUX_SC1);
                }
            }
        };
        auto pack = [&](int p) __attribute__((always_inline)) {
            const uint32_t a = (SBASE + ((((t8 >> 1) & 0x60u) + (t8 >> 8)) * 2u + p) * (NWG * 9u) + blockIdx.x * 9u) * 128u + u16;
            asm volatile("" ::: "memory");
#pragma unroll 1
            for (uint32_t q = 0; __builtin_amdgcn_ballot_w64(sg + q < ps) != 0ull; q += 256u) {
                const u32x4 d0 = *(const LDS u32x4*)(uintptr_t)(sg + q + u16), d1 = *(const LDS u32x4*)(uintptr_t)(sg + q + 128u + u16);
                __builtin_amdgcn_raw_buffer_store_b128(d0, rsR, sg + q < ps ? a + q : 0xffffff00u, 0, AUX_SC1);
                __builtin_amdgcn_raw_buffer_store_b128(d1, rsR, sg + q + 128u < ps ? a + q + 128u : 0xffffff00u, 0, AUX_SC1);
            }
            asm volatile("" ::: "memory");
        };
        asm volatile("" : "+v"(N[0]), "+v"(N[1]), "+v"(N[2]), "+v"(N[3]), "+v"(N[4]), "+v"(N[5]), "+v"(N[6]), "+v"(N[7]) :: "memory");
        asm volatile("" : "+v"(N[8]), "+v"(N[9]), "+v"(N[10]), "+v"(N[11]), "+v"(N[12]), "+v"(N[13]), "+v"(N[14]), "+v"(N[15]) :: "memory");
        ps = sg;
        rest(IC<0>()); rest(IC<1>()); rest(IC<2>()); rest(IC<3>()); rest(IC<4>()); rest(IC<5>()); rest(IC<6>()); rest(IC<7>());
        pack(0);
        ps = sg;
        rest(IC<8>()); rest(IC<9>()); rest(IC<10>()); rest(IC<11>()); rest(IC<12>()); rest(IC<13>()); rest(IC<14>()); rest(IC<15>());
        pack(1);
    }
    RTS(2)
    ST(6)
    if (mi < SLOTI / 64) {
        float w[16];
        load_w(wdown, w);
        auto dot = [&](u32x2 v, int c) __attribute__((always_inline)) -> float {
            return bf2f(v.x & 0xffffu) * w[c] + bf2f(v.x >> 16) * w[4 + c] + bf2f(v.y & 0xffffu) * w[8 + c] + bf2f(v.y >> 16) * w[12 + c];
        };
        int e0[4];
#pragma unroll
        for (int c = 0; c < 4; c++) {
            const int ex = (int)(wave_max(__float_as_uint(dot(ms, c)) & 0x7fffffffu) >> 23);
            e0[c] = __builtin_amdgcn_readfirstlane(ex == 0 ? 0 : min(max(ex - 129, -100), 100));
        }
#pragma unroll
        for (int k = 0; k < 16; k++)
            w[k] = __uint_as_float(__builtin_amdgcn_readfirstlane(__float_as_uint(w[k] * __uint_as_float((uint32_t)(127 - e0[k & 3]) << 23))));
        const uint32_t lane = (t8 >> 3) & 63u;
#pragma unroll
        for (int cp = 0; cp < 2; cp++) {
            u32x4 o;
            u16x2 mx = {0, 0};
            float sq[2] = {0.f, 0.f};
#pragma unroll
            for (int q = 0; q < 4; q++) {
                const uint32_t hb = pk_bf16(dot(mv[q], 2 * cp), dot(mv[q], 2 * cp + 1));
                const float x0 = __uint_as_float(hb << 16), x1 = __uint_as_float(hb & 0xffff0000u);
                o[q] = __builtin_bit_cast(uint32_t, __builtin_amdgcn_cvt_pkrtz(x0, x1));
                mx = __builtin_elementwise_max(mx, __builtin_bit_cast(u16x2, hb & 0x7fff7fffu));
                sq[0] += x0 * x0; sq[1] += x1 * x1;
            }
            if (cp == 0) __syncthreads();
            *(LDS u32x4*)(uintptr_t)(P_BUF + (cp * 64u + lane) * 272u + wv * 16) = o;
#pragma unroll
            for (int e = 0; e < 2; e++) {
                const u32x2 st = {wave_max(mx[e]), __float_as_uint(wave_sum(sq[e]))};
                if (lane == 0u) *(LDS u32x2*)(uintptr_t)(P_LST + ((2 * cp + e) * 16 + wv) * 8) = st;
            }
        }
        __syncthreads();
        {
            const __amdgpu_buffer_rsrc_t rsM = __builtin_amdgcn_make_buffer_rsrc((void*)mdc, 0, (int)(128u * SLOTI * 4u), 0x00020000);
#pragma unroll
            for (int cp = 0; cp < 2; cp++) {
                const uint32_t row = cp * 64u + (t8 >> 7), c16 = (t8 + t8) & 0xf0u;
                __builtin_amdgcn_raw_buffer_store_b128(*(const LDS u32x4*)(uintptr_t)(P_BUF + row * 272u + c16), rsM, row * (SLOTI * 4u) + c16 + mi * 256u, 0, AUX_SC1);
            }
        }
        if (wv == 0) {
            const u32x2 st = *(const LDS u32x2*)(uintptr_t)(P_LST + t8);
            const u32x2 r = {row_max(st.x) | (row_max(st.x ? 0xffffu - st.x : 0u) << 16), __float_as_uint(row_sum(__uint_as_float(st.y)))};
            if ((lane & 15u) == 15u) stat[(lane >> 4) * NWG + mi] = r;
            if (mi == 0u && lane < 4u) meta[M_E0 + lane] = (uint32_t)(lane == 0u ? e0[0] : lane == 1u ? e0[1] : lane == 2u ? e0[2] : e0[3]);
        }
    }
    RTS(3)
    RTS(4)
    ST(7)
    ST_OUT(0)
}

#define A_MD 0
#define A_ACC (SLOTI * 4)
#define A_FLAG (A_ACC + SLOTO * 4)
#define A_LDS (A_FLAG + 512)
static_assert(SLOTI * 4 == 3 * NT * 16 + 1024, "md staging assumes three full vectors and 256 dwords");
#define BIAS 0x40004000u
#define NFAST 8
#define NLD 10
#define PRE0 3
#define SHARE (SLOTO / 8)
#define SPINCAP 4096u
#define ZSIG 9.0f
#define HSLOW 0x80008000u
#define GAS __attribute__((address_space(1)))

__global__ __launch_bounds__(NT) __attribute__((amdgpu_num_vgpr(53))) void acc_kernel(const char* __restrict__ recs,
                                                 const u32x4* __restrict__ tcnt,
                                                 const uint32_t* __restrict__ mdc, const u32x4* __restrict__ stat,
                                                 const int* __restrict__ iin, const int* __restrict__ iout, const uint16_t* __restrict__ ang,
                                                 const u32x2* __restrict__ m, const uint16_t* __restrict__ wdown,
                                                 const uint16_t* __restrict__ freq,
                                                 const uint16_t* __restrict__ dist, const uint16_t* __restrict__ centers,
                                                 const uint16_t* __restrict__ wrbf, const uint16_t* __restrict__ wsbf,
                                                 const uint16_t* __restrict__ wup,
                                                 uint32_t* partial, unsigned* meta, u32x2* __restrict__ out, int T, int E, int ks, int lcap, float zf) {
    extern __shared__ __attribute__((aligned(16))) char smem[];
    ST_DECL
    RTS(8)
    unsigned* acc = (unsigned*)(smem + A_ACC);
    unsigned* s_flag = (unsigned*)(smem + A_FLAG);
    const int tid = threadIdx.x;
    const int lane = tid & 63;
    const int wv = __builtin_amdgcn_readfirstlane(tid >> 6);
    const int cp = (blockIdx.x >> 3) & 1;
    const int h = blockIdx.x & 3;
    const int j = ((blockIdx.x >> 2) & 1) * 16 + (blockIdx.x >> 4);
    u32x4 tc;
    const __amdgpu_buffer_rsrc_t rsM = __builtin_amdgcn_make_buffer_rsrc((void*)mdc, 0, (int)(128u * SLOTI * 4u), 0x00020000);

    u32x4 pm[4];
    u32x4 ring[NLD];
    auto load_md = [&](int ff) __attribute__((always_inline)) {
        const uint32_t so = (uint32_t)(cp * 64 + ((ABL & 64) ? 0 : ff * 4) + h) * (SLOTI * 4u);
#pragma unroll
        for (int v = 0; v < 3; v++) pm[v] = __builtin_amdgcn_raw_buffer_load_b128(rsM, (uint32_t)tid * 16u, so + (uint32_t)v * (NT * 16u), 0);
        if (wv == 0) pm[3] = __builtin_amdgcn_raw_buffer_load_b128(rsM, (uint32_t)tid * 16u, so + 3u * NT * 16u, 0);
        __builtin_amdgcn_sched_barrier(0);
    };
    const uint32_t lf = ((uint32_t)(tid & ~31) * 16u + (uint32_t)(tid & 12) * 32u) * SR + (uint32_t)(tid & 16) * 4u + (uint32_t)(tid & 3) * 16u;
    auto load_main = [&](int i, int ff, bool nt) __attribute__((always_inline)) -> u32x4 {
        const uint32_t col = (uint32_t)(POS(ff) * 128 + h * 32 + ((ABL & 8) ? 0 : j));
        if (ABL & 8) nt = false;
        const __amdgpu_buffer_rsrc_t rs = __builtin_amdgcn_make_buffer_rsrc((void*)(recs + ((col >> 7) * SS + (col & 127u) * SC) * 128u), 0, ks * (int)(SK * 128u), 0x00020000);
        const int so = (int)(((i >> 1) * SK + (i & 1) * 128u * SR) * 128u);
        if (nt) return __builtin_amdgcn_raw_buffer_load_b128(rs, lf, so, AUX_NT);
        return __builtin_amdgcn_raw_buffer_load_b128(rs, lf, so, 0);
    };
    const __amdgpu_buffer_rsrc_t rsT = __builtin_amdgcn_make_buffer_rsrc((void*)recs, 0, (int)(LBASE * 128u), 0x00020000);
    const uint32_t tof = (uint32_t)(lane & 2) * (TPL * 64u) + (uint32_t)(tid >> 2) * 128u + (uint32_t)(lane & 1) * 64u + 4u;
    const uint32_t sp0 = (SBASE + (uint32_t)(h * 32 + j) * (2u * NWG * 9u) + (uint32_t)(tid >> 2) * 9u) * 128u;
    auto tail_rank = [&](uint32_t c, uint32_t sp, int ff, uint32_t t0, int& dst, uint32_t& en) __attribute__((always_inline)) -> int {
        const int k4 = (int)(((uint32_t)(lane & 3) + t0) * 4u) - (int)((c & 3u) * 8u);
        const int left = (int)((c >> 2) & 15u) - k4;
        const bool has = left > 0;
        en = k4 < 0 ? tof + (TBASE + (uint32_t)(POS(ff) * 128 + h * 32 + j) * TST) * 128u : sp + (uint32_t)(k4 * 16 + min(max(left, 0), 4));
        const u64 mask = __builtin_amdgcn_ballot_w64(has);
        const int n = __builtin_popcountll(mask);
        const int rank = __builtin_amdgcn_mbcnt_hi((uint32_t)(mask >> 32), __builtin_amdgcn_mbcnt_lo((uint32_t)mask, 0u));
        dst = (has ? rank : (n + lane - rank)) << 2;
        return n;
    };
    auto load_tail = [&](uint32_t en, u64& tm) __attribute__((always_inline)) -> u32x4 {
        const bool on = (uint32_t)(lane & 3) < (en & 7u) && !(ABL & 32);
        tm = __builtin_amdgcn_ballot_w64(on);
        return __builtin_amdgcn_raw_buffer_load_b128(rsT, on ? (en & ~15u) + (uint32_t)(lane & 3) * 16u : 0xffffff00u, 0, 0);
    };
    float rs[4][4], rg[4][4];
    u32x4 em[2];
    u32x2 dw2;
    const __amdgpu_buffer_rsrc_t rsE = __builtin_amdgcn_make_buffer_rsrc((void*)m, 0, E * 8, 0x00020000);
    const __amdgpu_buffer_rsrc_t rsD = __builtin_amdgcn_make_buffer_rsrc((void*)dist, 0, E * 2, 0x00020000);
    auto edge_loads = [&](int sh) __attribute__((always_inline)) {
        const int ol = sh * SHARE + tid * 4;
        const uint32_t e0 = (tid < SHARE / 4) ? (uint32_t)(((ol >> 6) << 11) | (j << 6) | (ol & 63)) : 0x0fffff00u;
        em[0] = __builtin_amdgcn_raw_buffer_load_b128(rsE, e0 * 8u, 0, AUX_NT);
        em[1] = __builtin_amdgcn_raw_buffer_load_b128(rsE, e0 * 8u + 16u, 0, AUX_NT);
        dw2 = __builtin_amdgcn_raw_buffer_load_b64(rsD, e0 * 2u, 0, AUX_NT);
    };
    auto factors = [&]() __attribute__((always_inline)) {
        const float sq = 3.7982826f;
        const f32x2 d01 = {bf2f(dw2.x & 0xffffu), bf2f(dw2.x >> 16)}, d23 = {bf2f(dw2.y & 0xffffu), bf2f(dw2.y >> 16)};
        f32x2 a[4][4];
#pragma unroll
        for (int q = 0; q < 16; q++) a[q >> 2][q & 3] = (f32x2)(0.f);
#pragma unroll 2
        for (int r = 0; r < 8; r++) {
            const uint32_t cw = ((const uint32_t*)centers)[r >> 1];
            const float cn = bf2f((r & 1) ? (cw >> 16) : (cw & 0xffffu));
            const u32x2 s2 = ((const u32x2*)wsbf)[r], r2 = ((const u32x2*)wrbf)[r];
            const f32x2 w[4] = {{bf2f(s2.x & 0xffffu), bf2f(s2.x >> 16)}, {bf2f(s2.y & 0xffffu), bf2f(s2.y >> 16)},
                                {bf2f(r2.x & 0xffffu), bf2f(r2.x >> 16)}, {bf2f(r2.y & 0xffffu), bf2f(r2.y >> 16)}};
            const f32x2 y01 = (d01 - cn) * sq, y23 = (d23 - cn) * sq;
            const f32x2 t01 = -y01 * y01, t23 = -y23 * y23;
            const float rb[4] = {__builtin_amdgcn_exp2f(t01.x), __builtin_amdgcn_exp2f(t01.y), __builtin_amdgcn_exp2f(t23.x), __builtin_amdgcn_exp2f(t23.y)};
#pragma unroll
            for (int q = 0; q < 4; q++) {
#pragma unroll
                for (int k = 0; k < 4; k++) a[q][k] = __builtin_elementwise_fma(w[k], (f32x2)(rb[q]), a[q][k]);
            }
        }
#pragma unroll
        for (int q = 0; q < 4; q++) {
#pragma unroll
            for (int k = 0; k < 4; k++) { rs[q][k] = a[q][k >> 1][k & 1]; rg[q][k] = a[q][2 + (k >> 1)][k & 1]; }
        }
    };
#define ADT(V, O) "ds_write_addtid_b32 " V " offset:" #O "\n\t"
    auto store_md = [&]() __attribute__((always_inline)) {
        asm volatile("s_mov_b32 m0, %12\n\ts_nop 0\n\t" ADT("%0", 0) ADT("%1", 256) ADT("%2", 512) ADT("%3", 768) ADT("%4", 16384) ADT("%5", 16640) ADT("%6", 16896) ADT("%7", 17152)
                     ADT("%8", 32768) ADT("%9", 33024) ADT("%10", 33280) ADT("%11", 33536)
                     :: "v"(pm[0][0]), "v"(pm[0][1]), "v"(pm[0][2]), "v"(pm[0][3]), "v"(pm[1][0]), "v"(pm[1][1]), "v"(pm[1][2]), "v"(pm[1][3]),
                        "v"(pm[2][0]), "v"(pm[2][1]), "v"(pm[2][2]), "v"(pm[2][3]), "s"(wv * 1024) : "memory");
        if (wv == 0) asm volatile("s_mov_b32 m0, 0\n\ts_nop 0\n\t" ADT("%0", 49152) ADT("%1", 49408) ADT("%2", 49664) ADT("%3", 49920)
                                  :: "v"(pm[3][0]), "v"(pm[3][1]), "v"(pm[3][2]), "v"(pm[3][3]) : "memory");
    };
    const int me = h * 2 + cp;
    bool fast, more;
    uint32_t need, mg0, mg1;
    uint32_t guard = 0;
    uint32_t sgn = 0x8000u;
    pin(sgn);

#define GAT3(M) { uint32_t a0_, a1_, a2_; asm volatile( \
        "v_lshlrev_b16 %0, 2, %3\n\tv_lshlrev_b16 %1, 2, %4\n\tv_lshlrev_b16 %2, 2, %5\n\t" \
        "ds_read_b32 v[%6], %0\n\tds_read_b32 v[%6+1], %1\n\tds_read_b32 v[%6+2], %2" \
        : "=&v"(a0_), "=&v"(a1_), "=&v"(a2_) : "v"(w0), "v"(w1), "v"(w2), "n"(M) : "memory"); }
#define MIXL " op_sel_hi:[1,1,0]\n\t"
#define MIXH " op_sel:[0,1,0] op_sel_hi:[1,1,0]\n\t"
#define SDW " dst_sel:DWORD dst_unused:UNUSED_PAD src0_sel:DWORD src1_sel:WORD_1\n\t"
#define SCAT(PRE, POST, M) { uint32_t c0_, c1_, c2_, a0_, a1_, a2_; asm volatile( \
        "v_bitop3_b32 %6, %6, v[%15-9], v[%15-8] bitop3:0xfe\n\tv_and_b32 %0, 0x3ff, %7\n\tv_lshrrev_b32 %2, 22, %7\n\tv_or_b32 %6, v[%15-7], %6\n\t" PRE \
        "v_bitop3_b32 %0, %11, %8, %0 bitop3:0xea\n\tv_bitop3_b32 %2, %11, %10, %2 bitop3:0xea\n\tv_bfe_u32 %1, %7, 11, 10\n\t" \
        "v_fma_mix_f32 %3, %0, v[%15], %12" MIXL "v_bitop3_b32 %1, %11, %9, %1 bitop3:0xea\n\tv_fma_mix_f32 %0, %0, v[%15], %13" MIXH \
        "v_fma_mix_f32 %4, %1, v[%15+1], %12" MIXL "v_lshl_add_u32 %0, %0, 16, %3\n\tv_lshlrev_b32_sdwa %3, 2, %8" SDW \
        "v_fma_mix_f32 %1, %1, v[%15+1], %13" MIXH "ds_add_rtn_u32 v[%15-9], %3, %0 offset:%14\n\t" \
        "v_fma_mix_f32 %5, %2, v[%15+2], %12" MIXL "v_lshl_add_u32 %1, %1, 16, %4\n\tv_lshlrev_b32_sdwa %4, 2, %9" SDW \
        "v_fma_mix_f32 %2, %2, v[%15+2], %13" MIXH "ds_add_rtn_u32 v[%15-8], %4, %1 offset:%14\n\t" \
        "v_lshl_add_u32 %2, %2, 16, %5\n\tv_lshlrev_b32_sdwa %5, 2, %10" SDW \
        "ds_add_rtn_u32 v[%15-7], %5, %2 offset:%14" POST \
        : "=&v"(c0_), "=&v"(c1_), "=&v"(c2_), "=&v"(a0_), "=&v"(a1_), "=&v"(a2_), "+v"(guard) \
        : "v"(xw), "v"(w0), "v"(w1), "v"(w2), "v"(sgn), "s"(mg0), "s"(mg1), "n"(A_ACC), "n"(M), "s"(tm) : "memory"); }
    asm volatile("v_mov_b32 v110, 0\n\tv_mov_b32 v111, 0\n\tv_mov_b32 v112, 0\n\tv_mov_b32 v113, 0\n\tv_mov_b32 v114, 0\n\tv_mov_b32 v115, 0\n\t"
                 "v_mov_b32 v116, 0\n\tv_mov_b32 v117, 0\n\tv_mov_b32 v118, 0" ::: "v106", "v127");
    auto unmix = [&](int i) __attribute__((always_inline)) {
        const auto s0 = __builtin_amdgcn_permlane16_swap(ring[i][0], ring[i][2], false, false);
        const auto s1 = __builtin_amdgcn_permlane16_swap(ring[i][1], ring[i][3], false, false);
        ring[i][0] = s0[0]; ring[i][2] = s0[1]; ring[i][1] = s1[0]; ring[i][3] = s1[1];
    };
    auto gather = [&](int R, int i, int p) __attribute__((always_inline)) {
        if (i < NFAST) unmix(i);
        const uint32_t w0 = ring[i][0 ^ R], w1 = ring[i][1 ^ R], w2 = ring[i][2 ^ R];
        if (ABL & 16) { asm volatile("" :: "v"(w0), "v"(w1), "v"(w2)); return; }
        if (p == 0) GAT3(119) else if (p == 1) GAT3(122) else GAT3(125)
    };
    auto scatter = [&](int R, int i, int p, u64 tm) __attribute__((always_inline)) {
        const uint32_t xw = ring[i][3 ^ R], w0 = ring[i][0 ^ R], w1 = ring[i][1 ^ R], w2 = ring[i][2 ^ R];
        if (ABL & 16) { asm volatile("" :: "v"(w0), "v"(w1), "v"(w2), "v"(xw)); return; }
        if (i < NFAST) { if (p == 0) SCAT("", "", 119) else if (p == 1) SCAT("", "", 122) else SCAT("", "", 125) }
        else if (p == 0) SCAT("s_mov_b64 exec, %16\n\t", "\n\ts_mov_b64 exec, -1", 119)
        else SCAT("s_mov_b64 exec, %16\n\t", "\n\ts_mov_b64 exec, -1", 125)
    };
#define WAIT_L(N) asm volatile("s_waitcnt lgkmcnt(" #N ")" ::: "memory")
    int ntn;
    u64 tm0 = 0ull, tm1;
    uint32_t ta[2], sp = sp0;
    auto list1 = [&](int ff) __attribute__((always_inline)) {
        int dst;
        uint32_t en;
        ntn = tail_rank(tc.x, sp, ff, 0u, dst, en);
        asm volatile("ds_permute_b32 v106, %0, %1" :: "v"(dst), "v"(en) : "memory");
    };
    auto list2 = [&]() __attribute__((always_inline)) {
        asm volatile("ds_bpermute_b32 v107, %0, v106\n\tds_bpermute_b32 v108, %0, v106 offset:64" :: "v"(lane & ~3) : "memory");
    };
    auto step = [&](auto RR, auto NN, int ff) __attribute__((always_inline)) {
        constexpr int R = decltype(RR)::value;
        constexpr bool nt = decltype(NN)::value == 1, last = decltype(NN)::value == 2;
        gather(R, 0, 0);
        scatter(R, NFAST, 2, tm0);
        const int ntl = ntn;
        asm volatile("v_mov_b32 %0, v107\n\tv_mov_b32 %1, v108" : "=v"(ta[0]), "=v"(ta[1]));
        ring[NFAST] = load_tail(ta[0], tm0);
        ring[NFAST + 1] = load_tail(ta[1], tm1);
        if (last) edge_loads(me);
        else load_md(ff + 1);
        gather(R, 1, 1);
        const uint32_t tcur = tc.x, spc = sp;
#pragma unroll
        for (int i = 0; i < NFAST - 1; i++) {
            gather(R, i + 2, (i + 2) % 3);
            if (i < 2) WAIT_L(9); else WAIT_L(12);
            scatter(R, i, i % 3, 0ull);
            if (!last) ring[i] = load_main(i, ff + 1, nt);
            if (i == 0 && !last) {
                tc.x = __builtin_amdgcn_alignbit(tc.y, tc.x, 8u); tc.y = __builtin_amdgcn_alignbit(tc.z, tc.y, 8u);
                tc.z = __builtin_amdgcn_alignbit(tc.w, tc.z, 8u); tc.w >>= 8;
                sp = ff == 7 ? sp0 + NWG * 9u * 128u : sp + ((tcur & 0x3cu) << 2);
                list1(ff + 1);
            }
            if (i == 4 && !last) list2();
            __builtin_amdgcn_sched_barrier(0);
        }
        if (__builtin_expect(ntl > 16, 0)) {
            gather(R, NFAST + 1, 0);
            WAIT_L(0);
            scatter(R, NFAST + 1, 0, tm1);
        }
        if (__builtin_expect((__builtin_amdgcn_ballot_w64((tcur & 2u) != 0u) | (u64)(ntl > 32 ? 1 : 0)) != 0ull, 0)) {
#pragma unroll 1
            for (int e = 2; e < 6; e++) {
                int dst;
                uint32_t en;
                if (tail_rank(tcur, spc, ff, (uint32_t)e & 4u, dst, en) <= (e & 3) * 16) continue;
                const int cmp = __builtin_amdgcn_ds_permute(dst, (int)en);
                ring[NFAST + 1] = load_tail((uint32_t)__builtin_amdgcn_ds_bpermute((e & 3) * 64 + (lane & ~3), cmp), tm1);
                gather(R, NFAST + 1, 0);
                WAIT_L(0);
                scatter(R, NFAST + 1, 0, tm1);
            }
        }
        WAIT_L(3);
        ST(1)
        if (!last) {
            __builtin_amdgcn_s_barrier();
            ST(3)
            store_md();
        }
        scatter(R, NFAST - 1, 1, 0ull);
        if (!last) {
            ring[NFAST - 1] = load_main(NFAST - 1, ff + 1, nt);
            WAIT_L(3);
            __builtin_amdgcn_s_barrier();
            ST(4)
        }
    };
    auto steps = [&](auto RR) __attribute__((always_inline)) {
        constexpr int R = decltype(RR)::value;
        tc = tcnt[(wv * 16 + (lane >> 2)) * 128 + j * 4 + h];
        u32x4 sv[2];
        uint32_t e0;
        if (wv < 4) {
            sv[0] = (u32x4)(0u); sv[1] = sv[0];
            if (lane < SLOTI / 256) { sv[0] = stat[tid * 2]; sv[1] = stat[tid * 2 + 1]; }
            e0 = meta[M_E0 + wv];
        }
        const u32x2 extra = *(const u32x2*)(meta + M_LOG);
        load_md(0);
#pragma unroll
        for (int i = 0; i < PRE0; i++) ring[i] = load_main(i, 0, false);
        __builtin_amdgcn_sched_barrier(0);
        {
            const u32x4 bi = {BIAS, BIAS, BIAS, BIAS};
            for (int i = tid; i < SLOTO / 4; i += NT) *(LDS u32x4*)(uintptr_t)(A_ACC + i * 16) = bi;
        }
        asm volatile("" :: "s"(*(const uint32_t*)centers ^ *(const uint32_t*)wsbf ^ *(const uint32_t*)wrbf ^ *(const uint32_t*)wup));
        list1(0);
        if (wv < 4) {
            const uint32_t mxb = wave_max(max(max(sv[0].x & 0xffffu, sv[0].z & 0xffffu), max(sv[1].x & 0xffffu, sv[1].z & 0xffffu)));
            const uint32_t mnb = 0xffffu - (wave_max(max(max(sv[0].x, sv[0].z), max(sv[1].x, sv[1].z))) >> 16);
            const float s2 = wave_sum((__uint_as_float(sv[0].y) + __uint_as_float(sv[0].w)) + (__uint_as_float(sv[1].y) + __uint_as_float(sv[1].w)));
            const float rng = fmaxf(sqrtf(zf * s2), 2.0f * bf2f(mxb)) * 6.103515625e-05f;
            const uint32_t eb = mxb ? (__float_as_uint(rng) + 0x7fffffu) >> 23 : 127u;
            const bool ok = !mxb || (mxb < 0x4700u && eb >= 107u && eb <= 140u && mnb >= ((eb + 8u) << 7));
            if (lane == 0) {
                s_flag[8 + wv] = (eb & 0xffu) | (ok ? 0x100u : 0u) | (mxb ? 0x200u : 0u);
                s_flag[12 + wv] = __float_as_uint(__uint_as_float(((eb + e0) & 0xffu) << 23) * (1024.0f / 1023.0f));
                s_flag[wv] = 0u;
            }
        }
        WAIT_L(0);
        list2();
        store_md();
#pragma unroll
        for (int i = PRE0; i < NFAST; i++) ring[i] = load_main(i, 0, false);
        ring[NFAST] = (u32x4)(0u);
        WAIT_L(0);
        __syncthreads();
        const uint32_t q0 = __builtin_amdgcn_readfirstlane(s_flag[8 + 2 * cp]), q1 = __builtin_amdgcn_readfirstlane(s_flag[9 + 2 * cp]);
        fast = (q0 & q1 & 0x100u) != 0u;
        need = (q0 >> 9) | ((q1 >> 9) << 1);
        more = (__builtin_amdgcn_readfirstlane(extra.x | extra.y) | (uint32_t)(T & 3)) != 0u;
        mg0 = (((q0 & 0xffu) + 9u) << 23) | 0x400000u;
        mg1 = ((((q1 & 0xffu) + 9u) << 23) | 0x400000u) + ((0u - (mg0 >> 16)) & 0xffffu);
        if (!fast) return;
        ST(0)
        RTS(9)
        int ff = 0;
#pragma unroll 1
        for (; ff < 15 - NTS; ff++) step(RR, IC<0>(), ff);
#pragma unroll 1
        for (; ff < 15; ff++) step(RR, IC<1>(), ff);
        step(RR, IC<2>(), 15);
        scatter(R, NFAST, 2, tm0);
    };
    auto irregular = [&](auto f) __attribute__((always_inline)) {
        const float fr = bf2f(freq[0]);
        const char* lg = recs + (size_t)LBASE * 128u;
        const uint16_t* tags = (const uint16_t*)(lg + (size_t)lcap * 128u);
        const uint32_t nl = meta[M_LOG], ns = meta[M_LOG + 1];
#pragma unroll 1
        for (uint32_t q = tid; q < nl * 8u; q += NT) {
            const uint32_t b = tags[q >> 3];
            if ((b & 127u) == (uint32_t)(h * 32 + j)) {
                const uint32_t* un = (const uint32_t*)(lg + (size_t)q * 16u);
                const uint32_t xw = un[3 ^ h];
#pragma unroll 1
                for (int k = 0; k < 3; k++) {
                    const uint32_t ww = un[k ^ h];
                    const float c = (float)((xw >> (k * 11)) & 0x3ffu) * (1.0f / 1023.0f);
                    if (!(ww & WNULL)) f(ww >> 16, ((ww & 0x3fffu) << 6) | ((ALT ? ((b >> 7) * 3u) & 15u : b >> 7) << 2) | (uint32_t)h, (ww & 0x8000u) ? -c : c);
                }
            }
        }
#pragma unroll 1
        for (uint32_t q = tid; q < ns + (uint32_t)(T & 3); q += NT) {
            const uint32_t t = q < ns ? ((const uint32_t*)tags)[-1 - (int)q] : (uint32_t)(T & ~3) + (q - ns);
            const uint32_t i = (uint32_t)iin[t], o = (uint32_t)iout[t];
            if (((o >> 6) & 31u) == (uint32_t)j && (i & 3u) == (uint32_t)h) f(((o >> 11) << 6) | (o & 63u), i, cosf(fr * bf2f(ang[t])));
        }
    };

    if (h == 0) steps(IC<0>());
    else if (h == 1) steps(IC<1>());
    else if (h == 2) steps(IC<2>());
    else steps(IC<3>());
    if (fast) {
        if (more) irregular([&](uint32_t ol, uint32_t i, float cf) __attribute__((always_inline)) {
            const uint32_t md = mdc[(cp * 64u + (i & 63u)) * SLOTI + ((i >> 14) << 8) + ((i >> 4) & 0xfcu) + ((i >> 12) & 3u)];
            const float c = cf * (1023.0f / 16777216.0f);
            const uint32_t lo = __float_as_uint(__builtin_fmaf(c, h2f(md & 0xffffu), __uint_as_float(mg0)));
            const uint32_t hi = __float_as_uint(__builtin_fmaf(c, h2f(md >> 16), __uint_as_float(mg1)));
            if (cf == cf) guard |= atomicAdd(&acc[ol], (hi << 16) + lo);
            else guard |= HSLOW;
        });
    }
    GAS unsigned* dw = (GAS unsigned*)(meta + M_DONE + j * 64);
    volatile LDS unsigned* seen = (volatile LDS unsigned*)(uintptr_t)(A_FLAG + 4);
    if (tid == NT - 64) *seen = __hip_atomic_load(dw, __ATOMIC_RELAXED, __HIP_MEMORY_SCOPE_AGENT) & 0xff00ffu;
    WAIT_L(0);
    asm volatile("v_or3_b32 %0, %0, v110, v111\n\tv_or3_b32 %0, %0, v112, v113\n\tv_or3_b32 %0, %0, v114, v115\n\tv_or3_b32 %0, %0, v116, v117\n\tv_or_b32 %0, v118, %0" : "+v"(guard));
    ST(2)
    __syncthreads();
    RTS(10)
    const __amdgpu_buffer_rsrc_t rsP = __builtin_amdgcn_make_buffer_rsrc((void*)(partial + (size_t)j * 16 * SLOTO), 0, 16 * SLOTO * 4, 0x00020000);
    const bool fin = wv < (SHARE / 4 + 63) / 64;
    u32x4 p[8];
    unsigned have = 0u;
    auto fetch = [&](unsigned v, int sh) __attribute__((always_inline)) {
        const uint32_t po = tid < SHARE / 4 ? (uint32_t)(sh * SHARE + tid * 4) * 4u : 0x7fffff00u;
        const unsigned nw = v & ~have;
#pragma unroll
        for (int r = 0; r < 8; r++)
            if ((nw >> (me ^ r)) & 1u)
                p[r] = __builtin_amdgcn_raw_buffer_load_b128(rsP, po, ((v >> (16 + (me ^ r))) & 1u) ? 16 * SLOTO * 4 : (me ^ r) * SLOTO * 4, AUX_SC1);
        have |= v;
    };
    if (fast) {
        uint32_t big = 0u;
        u32x4 v[7];
#pragma unroll
        for (int k = 0; k < 6; k++) v[k] = *(const LDS u32x4*)(uintptr_t)(A_ACC + (k * NT + tid) * 16);
        v[6] = (u32x4)(BIAS);
        if (wv < 2) v[6] = *(const LDS u32x4*)(uintptr_t)(A_ACC + (6 * NT + tid) * 16);
#pragma unroll
        for (int k = 0; k < 6; k++) __builtin_amdgcn_raw_buffer_store_b128(v[k], rsP, (uint32_t)(tid * 16 + me * SLOTO * 4 + k * NT * 16), 0, AUX_SC1);
        if (wv < 2) __builtin_amdgcn_raw_buffer_store_b128(v[6], rsP, (uint32_t)(tid * 16 + me * SLOTO * 4 + 6 * NT * 16), 0, AUX_SC1);
        __builtin_amdgcn_sched_barrier(0);
        p[0] = *(const LDS u32x4*)(uintptr_t)(A_ACC + (me * SHARE + min(tid, SHARE / 4 - 1) * 4) * 4);
        have = 1u << me;
        if (fin) fetch(__builtin_amdgcn_readfirstlane(*seen), me);
#pragma unroll
        for (int k = 0; k < 28; k++) { guard |= v[k >> 2][k & 3]; big |= v[k >> 2][k & 3] + 0x42004200u; }
        if (guard & HSLOW) s_flag[0] = 1u;
        if (big & 0x7c00u) s_flag[2] = 1u;
        if (big & 0x7c000000u) s_flag[3] = 2u;
        if (fin) factors();
    }
    asm volatile("s_waitcnt vmcnt(0)" ::: "memory");
    __syncthreads();
    RTS(11)
    ST(5)
    const bool slow = (ABL & 120) ? false : !fast || __builtin_amdgcn_readfirstlane(s_flag[0]) != 0u || (need & ~__builtin_amdgcn_readfirstlane(s_flag[2] | s_flag[3])) != 0u;
    if (slow) {
        float* fa = (float*)acc;
        float w[16];
        load_w(wdown, w);
#pragma unroll 1
        for (int comp = 2 * cp; comp < 2 * cp + 2; comp++) {
            __syncthreads();
            for (int i = tid; i < SLOTO; i += NT) fa[i] = 0.f;
            __syncthreads();
            auto add = [&](uint32_t ol, uint32_t i, float cf) __attribute__((always_inline)) {
                uint32_t rr[4];
                md_of(m[i], w, rr);
                const uint32_t rc = comp == 0 ? rr[0] : comp == 1 ? rr[1] : comp == 2 ? rr[2] : rr[3];
                atomicAdd(&fa[ol], cf * bf2f(rc));
            };
#pragma unroll 1
            for (uint32_t q = tid; q < 16u * NWG * ((uint32_t)ks * 8u + 24u); q += NT) {
                const uint32_t per = (uint32_t)ks * 8u + 24u;
                const uint32_t un = q & 3u, hf = (q % per) >> 2, sb = (q / per) & (NWG - 1u), ff = (q / per) / NWG;
                const uint32_t col = POS(ff) * 128u + (uint32_t)(h * 32 + j);
                u32x2 p0, p1;
                if (hf < 2u * (uint32_t)ks) {
                    const char* ln = recs + (size_t)((col >> 7) * SS + (col & 127u) * SC + (hf >> 1) * SK + sb * SR) * 128u + un * 16u + (hf & 1u) * 8u;
                    p0 = *(const u32x2*)ln; p1 = *(const u32x2*)(ln + 64);
                } else {
                    const uint32_t tl = hf - 2u * (uint32_t)ks;
                    const uint8_t* cb = (const uint8_t*)tcnt + (sb * 128u + (uint32_t)(j * 4 + h)) * 16u;
                    const char* ln = recs + (size_t)(TBASE + (tl >> 1) * TPL + col * TST + sb) * 128u + (tl & 1u) * 64u + un * 16u;
                    if (tl < 4u) { if (tl >= (cb[ff] & 3u) * 2u) continue; }
                    else {
                        uint32_t at = (tl - 4u) * 4u + un;
                        if (at >= ((cb[ff] >> 2) & 15u)) continue;
#pragma unroll 1
                        for (uint32_t t = ff & 8u; t < ff; t++) at += (cb[t] >> 2) & 15u;
                        ln = recs + (size_t)(SBASE + (((uint32_t)(h * 32 + j) * 2u + (ff >> 3)) * NWG + sb) * 9u) * 128u + at * 16u;
                    }
                    p0 = *(const u32x2*)ln; p1 = *(const u32x2*)(ln + 8);
                }
                if (h & 2) { const u32x2 t = p0; p0 = p1; p1 = t; }
                const uint32_t ws[4] = {(h & 1) ? p0.y : p0.x, (h & 1) ? p0.x : p0.y, (h & 1) ? p1.y : p1.x, (h & 1) ? p1.x : p1.y};
#pragma unroll
                for (int k = 0; k < 3; k++) {
                    const float c = (float)((ws[3] >> (k * 11)) & 0x3ffu) * (1.0f / 1023.0f);
                    if (!(ws[k] & WNULL)) add(ws[k] >> 16, ((ws[k] & 0x3fffu) << 6) | (ff * 4u + (uint32_t)h), (ws[k] & 0x8000u) ? -c : c);
                }
            }
            if (more) irregular(add);
            __syncthreads();
            for (int i = tid; i < SLOTO / 4; i += NT)
                __builtin_amdgcn_raw_buffer_store_b128(*(const LDS u32x4*)(uintptr_t)(A_ACC + i * 16), rsP, (uint32_t)(((comp & 1) * 8 + me) * SLOTO * 4 + i * 16), 0, AUX_SC1);
        }
        edge_loads(me);
        factors();
#pragma unroll
        for (int k = 0; k < 8; k++) p[k] = (u32x4)(0u);
        have = 0u;
        asm volatile("s_waitcnt vmcnt(0)" ::: "memory");
        __syncthreads();
    }
    if (tid == NT - 64) {
        const unsigned inc = (slow ? 0x10001u : 1u) << me;
        unsigned v = __hip_atomic_fetch_add(dw, inc, __ATOMIC_RELAXED, __HIP_MEMORY_SCOPE_AGENT) + inc;
        if ((v & 0xffu) != 0xffu) {
            unsigned spins = 0;
            do {
                *seen = v & 0xff00ffu;
                __builtin_amdgcn_s_sleep(1);
                v = __hip_atomic_load(dw, __ATOMIC_RELAXED, __HIP_MEMORY_SCOPE_AGENT);
                if (++spins > SPINCAP && (v & 0xffu) != 0xffu) {
                    v = __hip_atomic_fetch_add(dw, 256u << me, __ATOMIC_RELAXED, __HIP_MEMORY_SCOPE_AGENT);
                    if ((v & 0xffu) != 0xffu) v = 0x800000ffu;
                }
            } while ((v & 0xffu) != 0xffu);
            v &= 0x80ff00ffu;
        }
        *seen = v;
    }
    if (!fin) return;
    uint32_t un[4];
#pragma unroll
    for (int c = 0; c < 4; c++) un[c] = __builtin_amdgcn_readfirstlane(s_flag[12 + c]);
    unsigned sv;
    {
        const uintptr_t pb = (uintptr_t)(partial + (size_t)j * 16 * SLOTO);
        const u32x4 rsQ = {(uint32_t)pb, (uint32_t)(pb >> 32) & 0xffffu, 16u * SLOTO * 4u, 0x00020000u};
        const uint32_t po = tid < SHARE / 4 ? (uint32_t)(me * SHARE + tid * 4) * 4u : 0x7fffff00u;
        do {
            sv = __builtin_amdgcn_readfirstlane(*seen);
            uint32_t t0, t1;
#define FET(R) "s_xor_b32 %8, %12, " #R "\n\ts_bitcmp1_b32 %13, %8\n\ts_cbranch_scc0 1f\n\ts_mul_i32 %9, %8, %15\n\ts_bitcmp1_b32 %14, %8\n\t" \
               "s_cselect_b32 %9, %16, %9\n\tbuffer_load_dwordx4 %" #R ", %10, %11, %9 offen sc1\n1:\n\t"
            asm volatile("s_and_b32 %8, %13, 0xff\n\ts_cbranch_scc0 2f\n\ts_nop 2\n\t" FET(0) FET(1) FET(2) FET(3) FET(4) FET(5) FET(6) FET(7) "\n2:\n\ts_sleep 2\n\ts_waitcnt vmcnt(0)"
                         : "+v"(p[0]), "+v"(p[1]), "+v"(p[2]), "+v"(p[3]), "+v"(p[4]), "+v"(p[5]), "+v"(p[6]), "+v"(p[7]), "=&s"(t0), "=&s"(t1)
                         : "v"(po), "s"(rsQ), "s"(me), "s"((int)sv < 0 ? 0u : sv & ~have), "s"(sv >> 16), "n"(SLOTO * 4), "n"(16 * SLOTO * 4) : "memory", "scc");
            have |= sv;
        } while ((sv & 0xffu) != 0xffu);
    }
    if ((int)sv < 0) return;
    RTS(12)
    ST(6)
    const unsigned flt = (sv >> 16) & 0xffu;
    auto finish = [&](int sh) __attribute__((always_inline)) {
        {
            const int ol = sh * SHARE + tid * 4;
            const int e0 = ((ol >> 6) << 11) | (j << 6) | (ol & 63);
            const uint32_t po = tid < SHARE / 4 ? (uint32_t)ol * 4u : 0x7fffff00u;
            float g[4][4];
            u32x4 a[2] = {p[0] + p[2], p[1] + p[3]}, b[2] = {p[4] + p[6], p[5] + p[7]};
            if (cp) { const u32x4 t = a[0], u = b[0]; a[0] = a[1]; b[0] = b[1]; a[1] = t; b[1] = u; }
#pragma unroll
            for (int c = 0; c < 2; c++) {
                const int nb = 0x10000 - 0x4000 * __builtin_popcount(flt & (0x55u << c));
                const float u0 = __uint_as_float(nb ? un[2 * c] : 0u), u1 = __uint_as_float(nb ? un[2 * c + 1] : 0u);
#pragma unroll
                for (int q = 0; q < 4; q++) {
                    g[q][2 * c] = (float)((int)((a[c][q] & 0xffffu) + (b[c][q] & 0xffffu)) - nb) * u0;
                    g[q][2 * c + 1] = (float)((int)((a[c][q] >> 16) + (b[c][q] >> 16)) - nb) * u1;
                }
            }
            if (__builtin_expect(flt != 0u, 0)) {
#pragma unroll
                for (int c = 0; c < 2; c++) {
#pragma unroll 1
                    for (int k = c; k < 8; k += 2) {
                        if (!((flt >> k) & 1u)) continue;
                        const u32x4 f0 = __builtin_amdgcn_raw_buffer_load_b128(rsP, po, k * SLOTO * 4, AUX_SC1);
                        const u32x4 f1 = __builtin_amdgcn_raw_buffer_load_b128(rsP, po, (8 + k) * SLOTO * 4, AUX_SC1);
#pragma unroll
                        for (int q = 0; q < 4; q++) { g[q][2 * c] += __uint_as_float(f0[q]); g[q][2 * c + 1] += __uint_as_float(f1[q]); }
                    }
                }
            }
            float wu[16];
            load_w(wup, wu);
            u32x4 ov[2];
#pragma unroll
            for (int q = 0; q < 4; q++) {
#pragma unroll
                for (int k = 0; k < 4; k++) g[q][k] *= rs[q][k];
                const uint32_t m0 = em[q >> 1][(q & 1) * 2], m1 = em[q >> 1][(q & 1) * 2 + 1];
                const float mm[4] = {bf2f(m0 & 0xffff), bf2f(m0 >> 16), bf2f(m1 & 0xffff), bf2f(m1 >> 16)};
                float o[4];
#pragma unroll
                for (int k = 0; k < 4; k++) o[k] = mm[k] + (g[q][0] * wu[k] + g[q][1] * wu[4 + k] + g[q][2] * wu[8 + k] + g[q][3] * wu[12 + k]) * rg[q][k];
                ov[q >> 1][(q & 1) * 2] = pk_bf16(o[0], o[1]);
                ov[q >> 1][(q & 1) * 2 + 1] = pk_bf16(o[2], o[3]);
            }
            if (tid < SHARE / 4 && e0 < E) { *(u32x4*)(out + e0) = ov[0]; *(u32x4*)(out + e0 + 2) = ov[1]; }
        }
    };
    finish(me);
#pragma unroll 1
    for (unsigned todo = (sv >> 8) & 0xffu; todo; todo &= todo - 1u) {
        const int sh = __builtin_ctz(todo);
        edge_loads(sh);
        factors();
        have = 0u;
        fetch(sv, sh);
        finish(sh);
    }
    RTS(13)
    ST(7)
    ST_OUT(8)
}

__global__ __launch_bounds__(256) void gen_scatter_kernel(const int* __restrict__ iin, const int* __restrict__ iout,
                                                          const uint16_t* __restrict__ ang, const uint16_t* __restrict__ freq,
                                                          const u32x2* __restrict__ m, const uint16_t* __restrict__ wdown,
                                                          float* __restrict__ agg, int T) {
    float w[16];
    load_w(wdown, w);
    const float fr = bf2f(freq[0]);
    for (long t = (long)blockIdx.x * 256 + threadIdx.x; t < T; t += (long)gridDim.x * 256) {
        const float c = cosf(fr * bf2f(ang[t]));
        uint32_t r[4];
        md_of(m[iin[t]], w, r);
        float* q = agg + (size_t)iout[t] * 4;
#pragma unroll
        for (int k = 0; k < 4; k++) atomicAdd(q + k, c * bf2f(r[k]));
    }
}
__global__ __launch_bounds__(256) void gen_final_kernel(const u32x2* __restrict__ m, const uint16_t* __restrict__ dist,
                                                        const uint16_t* __restrict__ centers, const uint16_t* __restrict__ wrbf,
                                                        const uint16_t* __restrict__ wsbf, const uint16_t* __restrict__ wup,
                                                        const float* __restrict__ agg, u32x2* __restrict__ out, int E) {
    const int e = blockIdx.x * 256 + threadIdx.x;
    if (e >= E) return;
    const float d = bf2f(dist[e]);
    float rs[4] = {0, 0, 0, 0}, rg[4] = {0, 0, 0, 0};
#pragma unroll 1
    for (int r = 0; r < 8; r++) {
        const float x = d - bf2f(centers[r]);
        const float rb = __expf(-10.0f * x * x);
#pragma unroll
        for (int k = 0; k < 4; k++) { rs[k] += rb * bf2f(wsbf[r * 4 + k]); rg[k] += rb * bf2f(wrbf[r * 4 + k]); }
    }
    float wu[16];
    load_w(wup, wu);
    const u32x2 mv = m[e];
    const float mm[4] = {bf2f(mv.x & 0xffff), bf2f(mv.x >> 16), bf2f(mv.y & 0xffff), bf2f(mv.y >> 16)};
    float g[4], o[4];
#pragma unroll
    for (int k = 0; k < 4; k++) g[k] = agg[(size_t)e * 4 + k] * rs[k];
#pragma unroll
    for (int k = 0; k < 4; k++) o[k] = mm[k] + (g[0] * wu[k] + g[1] * wu[4 + k] + g[2] * wu[8 + k] + g[3] * wu[12 + k]) * rg[k];
    const u32x2 ov = {f2bf(o[0]) | (f2bf(o[1]) << 16), f2bf(o[2]) | (f2bf(o[3]) << 16)};
    out[e] = ov;
}

extern "C" void kernel_launch(void* const* d_in, const int* in_sizes, int n_in,
                              void* d_out, int out_size, void* d_ws, size_t ws_size,
                              hipStream_t stream) {
    const u32x2* m = (const u32x2*)d_in[0];
    const int* iin = (const int*)d_in[1];
    const int* iout = (const int*)d_in[2];
    const uint16_t* dist = (const uint16_t*)d_in[3];
    const uint16_t* ang = (const uint16_t*)d_in[4];
    const uint16_t* centers = (const uint16_t*)d_in[5];
    const uint16_t* freq = (const uint16_t*)d_in[6];
    const uint16_t* wrbf = (const uint16_t*)d_in[7];
    const uint16_t* wsbf = (const uint16_t*)d_in[8];
    const uint16_t* wdown = (const uint16_t*)d_in[9];
    const uint16_t* wup = (const uint16_t*)d_in[10];
    const int E = in_sizes[3];
    const int T = in_sizes[1];
    char* ws = (char*)d_ws;
    const int G = T >> 2;
    const int ntile = (G + GR - 1) / GR;
    const int nstat = ntile / NWG - NDYN > 3 ? ntile / NWG - NDYN : 3;
    const int lcap = G / 6 + 2;
    size_t off = 0;
    unsigned* meta = (unsigned*)(ws + off); off += M_WORDS * 4;
    uint32_t* tcnt = (uint32_t*)(ws + off); off += (size_t)NWG * NB;
    u32x2* stat = (u32x2*)(ws + off); off += 4 * NWG * 8;
    uint32_t* mdc = (uint32_t*)(ws + off); off += (size_t)128 * SLOTI * 4;
    uint32_t* partial = (uint32_t*)(ws + off); off += (size_t)32 * 16 * SLOTO * 4;
    char* recs = ws + off;
    const size_t rbytes = (size_t)LBASE * 128 + (size_t)lcap * 130;
    off += rbytes;
    if (E > EMAX || (E & 3) || E < 1 || rbytes > 0x70000000u || off > ws_size) {
        (void)hipMemsetAsync(ws, 0, (size_t)E * 16, stream);
        gen_scatter_kernel<<<4096, 256, 0, stream>>>(iin, iout, ang, freq, m, wdown, (float*)ws, T);
        gen_final_kernel<<<(E + 255) / 256, 256, 0, stream>>>(m, dist, centers, wrbf, wsbf, wup, (const float*)ws, (u32x2*)d_out, E);
        return;
    }
    int ks = T / (NWG * NB * 24);
    if (ks > KMAX) ks = KMAX;
    const float zf = ZSIG * ZSIG * (float)T / (8.0f * (float)E * (float)E);
    (void)hipFuncSetAttribute((const void*)part_kernel, hipFuncAttributeMaxDynamicSharedMemorySize, P_LDS);
    (void)hipFuncSetAttribute((const void*)acc_kernel, hipFuncAttributeMaxDynamicSharedMemorySize, A_LDS);
    (void)hipMemsetAsync(ws, 0, M_E0 * 4, stream);
    part_kernel<<<NWG, NT, P_LDS, stream>>>(iin, iout, ang, freq, m, wdown, stat, mdc, meta, tcnt, recs, T, E, ntile, nstat, ks, lcap);
    if (!(ABL & 7)) acc_kernel<<<NWG, NT, A_LDS, stream>>>(recs, (const u32x4*)tcnt, mdc, (const u32x4*)stat, iin, iout, ang, m, wdown, freq, dist, centers, wrbf, wsbf, wup,
                                           partial, meta, (u32x2*)d_out, T, E, ks, lcap, zf);
}
```

```cpp
#include <hip/hip_runtime.h>
#include <hip/hip_bf16.h>
#include <stdint.h>
#pragma clang diagnostic ignored "-Winline-asm"

typedef unsigned int u32x4 __attribute__((ext_vector_type(4)));
typedef unsigned int u32x2 __attribute__((ext_vector_type(2)));
typedef unsigned short u16x2 __attribute__((ext_vector_type(2)));
typedef _Float16 f16x2 __attribute__((ext_vector_type(2)));
typedef __bf16 bf16x2 __attribute__((ext_vector_type(2)));
typedef float f32x2 __attribute__((ext_vector_type(2)));
typedef unsigned long long u64;

static __device__ __forceinline__ float bf2f(uint32_t bits16) { return __uint_as_float(bits16 << 16); }
static __device__ __forceinline__ uint32_t f2bf(float f) {
    uint32_t u = __float_as_uint(f);
    if ((u & 0x7fffffffu) > 0x7f800000u) return (u >> 16) | 0x40;
    return (u + 0x7fffu + ((u >> 16) & 1u)) >> 16;
}
static __device__ __forceinline__ float h2f(uint32_t bits16) { return (float)__builtin_bit_cast(_Float16, (unsigned short)bits16); }

#define NB 2048
#define WNULL 0x80000000u
#define CSTEP 0x2aabu
#define CINIT 205u
#define CHV (12u * CSTEP)
#define SLOTO 25088
#define SLOTI 12544
#define EMAX (SLOTO * 32)
#define NT 1024
#define NWG 256
#define KMAX 4
#define NTAIL 4
#define LAY 3
#define SC 257u
#define SR 1u
#define SK (128u * SC)
#define SS (4u * SK)
#define MAINL (16u * SS)
#define UINC (0x80000001u + SK * 64u)
#define TST 257u
#define TBASE MAINL
#define TPL (NB * TST)
#define SBASE (TBASE + 2u * TPL)
#define LBASE (SBASE + 128u * 2u * NWG * 9u)
#define NDYN 3
#define GR 2048
#define NTS 5
#define M_DONE 0
#define M_TILE (32 * 64)
#define M_LOG (M_TILE + 64)
#define M_MD (M_LOG + 64)
#define M_BEAT (M_MD + 64)
#define M_E0 (M_BEAT + 64)
#define M_WORDS (M_E0 + 64)
#define W_TCNT (M_WORDS * 4)
#define W_STAT (W_TCNT + NWG * NB)
#define W_MDC (W_STAT + 4 * NWG * 8)
#define W_PART (W_MDC + 128 * SLOTI * 4)
#define W_RECS (W_PART + 32 * 16 * SLOTO * 4)
#define TSC1 0
#define ALT 0
#define POS(ff) (ALT ? ((ff) * 11) & 15 : (ff))
#define AUX_NT 2
#define AUX_SC1 16

static __device__ __forceinline__ uint32_t row_max(uint32_t x) {
    x = max(x, (uint32_t)__builtin_amdgcn_update_dpp(0, x, 0x111, 0xf, 0xf, false));
    x = max(x, (uint32_t)__builtin_amdgcn_update_dpp(0, x, 0x112, 0xf, 0xf, false));
    x = max(x, (uint32_t)__builtin_amdgcn_update_dpp(0, x, 0x114, 0xf, 0xf, false));
    return max(x, (uint32_t)__builtin_amdgcn_update_dpp(0, x, 0x118, 0xf, 0xf, false));
}
static __device__ __forceinline__ uint32_t wave_max(uint32_t x) {
    x = row_max(x);
    x = max(x, (uint32_t)__builtin_amdgcn_update_dpp(0, x, 0x142, 0xa, 0xf, false));
    x = max(x, (uint32_t)__builtin_amdgcn_update_dpp(0, x, 0x143, 0xc, 0xf, false));
    return __builtin_amdgcn_readlane(x, 63);
}
static __device__ __forceinline__ float row_sum(float x) {
    x += __int_as_float(__builtin_amdgcn_update_dpp(0, __float_as_int(x), 0x111, 0xf, 0xf, false));
    x += __int_as_float(__builtin_amdgcn_update_dpp(0, __float_as_int(x), 0x112, 0xf, 0xf, false));
    x += __int_as_float(__builtin_amdgcn_update_dpp(0, __float_as_int(x), 0x114, 0xf, 0xf, false));
    return x + __int_as_float(__builtin_amdgcn_update_dpp(0, __float_as_int(x), 0x118, 0xf, 0xf, false));
}
static __device__ __forceinline__ float wave_sum(float x) {
    x = row_sum(x);
    x += __int_as_float(__builtin_amdgcn_update_dpp(0, __float_as_int(x), 0x142, 0xa, 0xf, false));
    x += __int_as_float(__builtin_amdgcn_update_dpp(0, __float_as_int(x), 0x143, 0xc, 0xf, false));
    return __int_as_float(__builtin_amdgcn_readlane(__float_as_int(x), 63));
}
static __device__ __forceinline__ uint32_t bfi(uint32_t mask, uint32_t a, uint32_t b) {
    return __builtin_amdgcn_bitop3_b32(mask, a, b, 0xca);
}
static __device__ __forceinline__ void opaque(int& x) { asm volatile("" : "+v"(x)); }
template <uint32_t C> static __device__ __forceinline__ uint32_t vconst() {
    uint32_t r;
    asm volatile("v_mov_b32 %0, %1" : "=v"(r) : "n"(C));
    return r;
}
static __device__ __forceinline__ uint32_t pk_bf16(float a, float b) {
    const f32x2 v = {a, b};
    return __builtin_bit_cast(uint32_t, __builtin_convertvector(v, bf16x2));
}
static __device__ __forceinline__ void pin(uint32_t& x) { asm volatile("" : "+v"(x)); }

#define RTS(k)
#define ST_DECL
#define ST(i)
#define ST_OUT(off)

static __device__ __forceinline__ void md_of(u32x2 v, const float* w, uint32_t* r) {
    float a0 = bf2f(v.x & 0xffff), a1 = bf2f(v.x >> 16), a2 = bf2f(v.y & 0xffff), a3 = bf2f(v.y >> 16);
#pragma unroll
    for (int j = 0; j < 4; j++) r[j] = f2bf(a0 * w[j] + a1 * w[4 + j] + a2 * w[8 + j] + a3 * w[12 + j]);
}
static __device__ __forceinline__ void load_w(const uint16_t* __restrict__ wdown, float* w) {
    u32x4 wv0 = *(const u32x4*)wdown, wv1 = *(const u32x4*)(wdown + 8);
#pragma unroll
    for (int i = 0; i < 4; i++) {
        w[2 * i] = bf2f(wv0[i] & 0xffffu); w[2 * i + 1] = bf2f(wv0[i] >> 16);
        w[8 + 2 * i] = bf2f(wv1[i] & 0xffffu); w[8 + 2 * i + 1] = bf2f(wv1[i] >> 16);
    }
}
#define LDS __attribute__((address_space(3)))
#define P_CNT 0
#define P_FLAG ((NB + 64) * 4)
#define P_LST (P_FLAG + 256)
#define LCAP 32
#define P_TAB (P_LST + 4 * LCAP * 16)
#define P_BUF (P_TAB + NB * 4)
#define P_LDS (P_BUF + NB * 64)
#define PD 2
#define LPOS 0x448763
#define ABL 0
#define PRIO 2
#define SETP(n) __builtin_amdgcn_s_setprio(n)
#define RMAXREV 40.0f
#define HB 88
#define HREG(S, D) (HB + ((S) & 3) * 10 + (((S) >> 2) + (D)) * 2)

template <int N> struct IC { static constexpr int value = N; };
struct Raw { u32x4 o[2], i[2]; u32x2 a[2]; };
static __device__ __forceinline__ uint32_t lds_ld(uint32_t a) { return *(const LDS uint32_t*)(uintptr_t)a; }
static __device__ __forceinline__ void lds_st(uint32_t a, uint32_t v) { *(LDS uint32_t*)(uintptr_t)a = v; }
static __device__ __forceinline__ void lds_or(uint32_t a, uint32_t v) {
    __hip_atomic_fetch_or((LDS uint32_t*)(uintptr_t)a, v, __ATOMIC_RELAXED, __HIP_MEMORY_SCOPE_WORKGROUP);
}
static __device__ __forceinline__ uint32_t lds_add(uint32_t a, uint32_t v) {
    return __hip_atomic_fetch_add((LDS uint32_t*)(uintptr_t)a, v, __ATOMIC_RELAXED, __HIP_MEMORY_SCOPE_WORKGROUP);
}
static __device__ __forceinline__ uint32_t slot_w(uint32_t ca, uint32_t cv) {
    return (ca << 4) | __builtin_amdgcn_bitop3_b32(cv >> 11, ca, 0x3cu, 0x28);
}
static __device__ __forceinline__ uint32_t slot_x2(uint32_t aw, uint32_t ca) {
    return __builtin_amdgcn_bitop3_b32(aw, ca, 0xcu, 0x72);
}
static __device__ __forceinline__ uint32_t slot_x(uint32_t aw) { return slot_x2(aw, aw >> 4); }
template <int R> static __device__ __forceinline__ u32x2 hget() {
    u32x2 d;
    asm volatile("v_mov_b64 %0, v[%1:%2]" : "=v"(d) : "n"(R), "n"(R + 1));
    return d;
}
static __device__ __forceinline__ u32x2 hget_at(int r) {
    u32x2 d;
    asm volatile("s_set_gpr_idx_on %2, gpr_idx(SRC0)\n\ts_nop 1\n\tv_mov_b32 %0, v88\n\tv_mov_b32 %1, v89\n\ts_set_gpr_idx_off"
                 : "=&v"(d.x), "=&v"(d.y) : "s"(r));
    return d;
}
static __device__ __forceinline__ void hset_at(int r, u32x2 d) {
    asm volatile("s_set_gpr_idx_on %2, gpr_idx(DST)\n\ts_nop 1\n\tv_mov_b32 v88, %0\n\tv_mov_b32 v89, %1\n\ts_set_gpr_idx_off"
                 :: "v"(d.x), "v"(d.y), "s"(r));
}

template <int S, int D> static __device__ __forceinline__ uint32_t take(uint32_t V, uint32_t u16, uint32_t la, u32x2 nul) {
    const uint32_t a = __builtin_amdgcn_bitop3_b32(V, u16, 15u, 0x54);
    asm volatile("v_mov_b64 v[%4:%4+1], v[%3:%3+1]\n\tv_cmpx_ne_u32 vcc, -16, %0\n\tds_wrxchg_rtn_b64 v[%4:%4+1], %1, %2 offset:%5\n\ts_mov_b64 exec, -1"
                 :: "v"(a), "v"(la), "v"(nul), "n"(HREG(S, D)), "n"(HREG(S, 1 - D)), "n"(S * 256) : "memory", "vcc");
    return a;
}
#define SK0 "s_bitcmp1_b32 %3, %4\n\ts_cbranch_scc0 1f\n\t"
#define SK1 "\n1:"
template <int S, int W> static __device__ __forceinline__ void put(u32x4 rsA, uint32_t ad, uint32_t pm) {
    if (POS(S) >= 16 - NTS) asm volatile(SK0 "s_waitcnt lgkmcnt(%5)\n\tbuffer_store_dwordx4 v[%2:%2+3], %0, %1, 0 offen nt\n\ts_nop 1" SK1 :: "v"(ad), "s"(rsA), "n"(HREG(S, 0)), "s"(pm), "n"(2 * S), "n"(W) : "memory", "scc");
    else asm volatile(SK0 "s_waitcnt lgkmcnt(%5)\n\tbuffer_store_dwordx4 v[%2:%2+3], %0, %1, 0 offen sc1\n\ts_nop 1" SK1 :: "v"(ad), "s"(rsA), "n"(HREG(S, 0)), "s"(pm), "n"(2 * S), "n"(W) : "memory", "scc");
}

__global__ __launch_bounds__(NT) __attribute__((amdgpu_num_vgpr(HB / 2))) void part_kernel(const int* __restrict__ iin, const int* __restrict__ iout,
                                                  const uint16_t* __restrict__ ang, const uint16_t* __restrict__ freq,
                                                  const u32x2* __restrict__ m, const uint16_t* __restrict__ wdown,
                                                  char* __restrict__ recs,
                                                  int T, int E, int ntile, int nstat, int ks, int lcap) {
    ST_DECL
    RTS(0)
    const int tid0 = threadIdx.x;
    const int wv = __builtin_amdgcn_readfirstlane(tid0 >> 6);
    const int G = T >> 2;
    const int pos = (int)((blockIdx.x & 7) << 5) | (int)(blockIdx.x >> 3);
    float frr;
    unsigned* meta = (unsigned*)(recs - W_RECS);
    const uint32_t gbase = (LBASE + (uint32_t)lcap) * 128u;
    const uint32_t rbytes = gbase + (uint32_t)lcap * 2u;
    const __amdgpu_buffer_rsrc_t rsR = __builtin_amdgcn_make_buffer_rsrc(recs, 0, (int)rbytes, 0x00020000);
    const u32x4 rsA = {(uint32_t)(uintptr_t)recs, (uint32_t)((uintptr_t)recs >> 32) & 0xffffu, (ABL & 2) ? 0u : rbytes, 0x00020000u};
    const uint32_t g2 = 2u * (uint32_t)ks;
    const uint32_t spec0 = gbase - 4u;
    auto fixed_tile = [&](int r) __attribute__((always_inline)) -> int { return r * NWG + ((pos + r * 37) & (NWG - 1)); };
    int tl0 = fixed_tile(0), tl1 = fixed_tile(1), tl2;
    uint32_t tk = 0u;
    const __amdgpu_buffer_rsrc_t rsO = __builtin_amdgcn_make_buffer_rsrc((void*)iout, 0, G * 16, 0x00020000);
    const __amdgpu_buffer_rsrc_t rsI = __builtin_amdgcn_make_buffer_rsrc((void*)iin, 0, G * 16, 0x00020000);
    const __amdgpu_buffer_rsrc_t rsG = __builtin_amdgcn_make_buffer_rsrc((void*)ang, 0, G * 8, 0x00020000);
    uint32_t t8 = (uint32_t)tid0 * 8u;
    auto load_one = [&](Raw& r, int tl, int idx) __attribute__((always_inline)) {
        const int j = idx < 4 ? idx >> 1 : idx & 1;
        const uint32_t g0 = (uint32_t)(((ABL & 1) ? tl & 31 : tl) * GR + j * NT);
        __builtin_amdgcn_sched_barrier(0);
        if (idx >= 4) r.a[j] = __builtin_amdgcn_raw_buffer_load_b64(rsG, t8, g0 * 8u, (ABL & 1) ? 0 : AUX_NT);
        else if (idx & 1) r.i[j] = __builtin_amdgcn_raw_buffer_load_b128(rsI, t8 + t8, g0 * 16u, (ABL & 1) ? 0 : AUX_NT);
        else r.o[j] = __builtin_amdgcn_raw_buffer_load_b128(rsO, t8 + t8, g0 * 16u, (ABL & 1) ? 0 : AUX_NT);
        __builtin_amdgcn_sched_barrier(0);
    };
    auto peek = [&](uint32_t at) __attribute__((always_inline)) -> uint32_t {
        return __hip_atomic_load((unsigned*)((char*)meta + at), __ATOMIC_RELAXED, __HIP_MEMORY_SCOPE_AGENT);
    };
    auto ready = [&]() __attribute__((always_inline)) {
        if (blockIdx.x != 0) {
            const uint32_t x = lds_ld(vconst<P_FLAG + 8>());
            while (peek(vconst<M_BEAT * 4>()) == x) __builtin_amdgcn_s_sleep(8);
        }
    };
    auto beat = [&]() __attribute__((always_inline)) {
        (void)__hip_atomic_fetch_add((unsigned*)((char*)meta + vconst<M_BEAT * 4>()), vconst<1>(), __ATOMIC_RELAXED, __HIP_MEMORY_SCOPE_AGENT);
    };
    auto draw = [&](uint32_t at) __attribute__((always_inline)) -> uint32_t {
        return __hip_atomic_fetch_add((unsigned*)((char*)meta + at), vconst<1>(), __ATOMIC_RELAXED, __HIP_MEMORY_SCOPE_AGENT);
    };
    uint32_t la;
    auto tab_at = [&]() __attribute__((always_inline)) -> uint32_t {
        return P_TAB + bfi(0xffu, bfi(0x3cu, t8 >> 2, t8 << 4), t8);
    };
    u32x2 nul;
    uint32_t U[2];
    {
        const uint32_t t = (uint32_t)tid0;
        la = P_BUF + ((t >> 5) << 12) + ((t & 0x18u) << 3) + ((t & 3u) << 4) + ((t & 4u) << 1);
        const uint32_t q = ((t >> 3) ^ (t >> 1 & 2u)) & 3u;
        nul.x = (0x7u >> q) << 31; nul.y = (0xbu >> q) << 31;
        U[0] = (POS((t >> 1) & 15u) * SS + ((t & 1u) * 64u + (t >> 5)) * SC + blockIdx.x * SR) * 128u + (0x80000000u - SK * 64u) + 8u - g2;
        U[1] = U[0] + 32u * SC * 128u;
    }
    auto log_line = [&](bool lg, uint32_t b) __attribute__((always_inline)) -> uint32_t {
        uint32_t lp = 0u;
        if (lg && (t8 & 0x38u) == 0u) {
            ready();
            lp = draw(vconst<M_LOG * 4>());
            __builtin_amdgcn_raw_buffer_store_b16((uint16_t)b, rsR, gbase + lp * 2u, 0, 0);
        }
        return LBASE + (uint32_t)__builtin_amdgcn_ds_bpermute((int)((t8 >> 1) & 0xe0u), (int)lp);
    };
    auto general = [&](int D, bool all, u64 sm) __attribute__((always_inline)) {
        uint32_t todo = __builtin_amdgcn_readfirstlane((uint32_t)sm | (uint32_t)(sm >> 32));
#pragma unroll 1
        while (todo) {
            const uint32_t S = (uint32_t)__builtin_ctz(todo) >> 1;
            todo &= ~(3u << (2u * S));
            pin(t8); pin(la);
            const uint32_t V = lds_ld(P_TAB + (t8 & ~63u) + S * 4u), g = V & 15u;
            const uint32_t col = POS(S) * 128u + ((t8 >> 1) & 0x60u) + (t8 >> 8);
            const bool act = all ? V != 0xffffffffu : (V & 0x80000009u) == 9u;
            const int hr = HREG((int)S, D) - HB;
            uint32_t ln = log_line(act && g > 8u + NTAIL, col);
            if (act) {
                const u32x2 N = *(const LDS u32x2*)(uintptr_t)(la + S * 256u);
                *(LDS u32x2*)(uintptr_t)(la + S * 256u) = nul;
                lds_st(P_TAB + (t8 & ~63u) + S * 4u, vconst<0xffffffffu>());
                if (!(g & 1u)) hset_at(hr, N);
                else {
                    uint32_t a, st;
                    if (g < 8u) { a = (V & ~15u) + ((t8 & 0x38u) << 1); st = 8u; }
                    else {
                        if (g <= 8u + NTAIL) ln = TBASE + ((g - 8u) >> 1) * TPL + col * TST + blockIdx.x;
                        a = ln * 128u + (la & 0x38u); st = 64u;
                    }
                    __builtin_amdgcn_raw_buffer_store_b64(hget_at(hr), rsR, a, 0, AUX_SC1);
                    __builtin_amdgcn_raw_buffer_store_b64(N, rsR, a + st, 0, AUX_SC1);
                }
            }
        }
    };
    unsigned epoch = 0;
    u64 pb2 = 0ull;
    u64 fm = 0ull, sm = 0ull;
    auto look = [&](u32x2 c) __attribute__((always_inline)) {
        uint32_t V[2];
        const bool late = __builtin_amdgcn_ballot_w64(((U[0] | U[1]) & 8u) != 0u) != 0ull;
        epoch++;
        asm volatile("v_mov_b32 %0, -1\n\tv_mov_b32 %1, -1\n\t"
                     "v_cmpx_le_u32 %6, %7, %2\n\tv_mov_b32 %0, %4\n\tv_add_u32 %4, %9, %4\n\tv_subrev_u32 %2, %8, %2\n\ts_mov_b64 exec, -1\n\t"
                     "v_cmpx_le_u32 vcc, %8, %3\n\tv_mov_b32 %1, %5\n\tv_add_u32 %5, %9, %5\n\tv_subrev_u32 %3, %8, %3\n\t"
                     "s_or_b64 %6, %6, exec\n\ts_mov_b64 exec, -1"
                     : "=&v"(V[0]), "=&v"(V[1]), "+v"(c.x), "+v"(c.y), "+v"(U[0]), "+v"(U[1]), "=&s"(fm) : "s"(CHV), "n"(CHV), "n"(UINC) : "vcc", "scc");
        *(LDS u32x2*)(uintptr_t)(P_CNT + t8) = c;
        pin(t8);
        const uint32_t tw = tab_at();
        lds_st(tw, V[0]);
        lds_st(tw + 64u, V[1]);
        if (wv == 1) lds_st(P_CNT + NB * 4 - 256 + (t8 >> 1), vconst<0x40000000u>());
        const u64 pb = __builtin_amdgcn_ballot_w64((int)(V[0] & V[1]) >= 0);
        pb2 = (ABL & 4) ? 0ull : pb;
        sm = 0ull;
        if (__builtin_expect(late, 0)) {
            sm = __builtin_amdgcn_ballot_w64((V[0] & 0x80000009u) == 9u || (V[1] & 0x80000009u) == 9u);
#pragma unroll
            for (int e = 0; e < 2; e++)
                if ((V[e] & 0x8000000fu) == 9u + NTAIL) U[e] -= 2u * UINC;
        }
    };
    u32x4 Va, Vb;

    Raw raw[PD];
    {
        const int tid = tid0;
        asm volatile("" ::: "v88", "v127");
        const uint16_t fq = freq[0];
        if (tid == 128) tk = peek(vconst<M_BEAT * 4>());
        if (blockIdx.x == 0 && wv == 3 && (tid & 63) < M_BEAT / 64) {
            __hip_atomic_store(meta + (tid & 63) * 64, 0u, __ATOMIC_RELAXED, __HIP_MEMORY_SCOPE_AGENT);
            __hip_atomic_store(meta + (tid & 63) * 64 + 1, 0u, __ATOMIC_RELAXED, __HIP_MEMORY_SCOPE_AGENT);
        }
#pragma unroll
        for (int d = 0; d < 6; d++) load_one(raw[0], tl0, d);
        asm("s_nop 0\n\tv_readfirstlane_b32 %0, %1" : "=s"(frr) : "v"(bf2f(fq) * 0.15915494309189535f));
#pragma unroll
        for (int s = 0; s < 16; s++) *(LDS u32x2*)(uintptr_t)(la + s * 256) = nul;
        const u32x2 ci = {CINIT, CINIT};
        *(LDS u32x2*)(uintptr_t)(P_CNT + tid * 8) = ci;
        *(LDS u32x2*)(uintptr_t)(P_TAB + t8) = (u32x2)(0xffffffffu);
#pragma unroll
        for (int d = 0; d < 6; d++)
            if (((LPOS >> (4 * d)) & 15) < 4) load_one(raw[1], tl1, d);
        if (tid < 64) lds_st(P_CNT + (NB + tid) * 4, 0x40000000u);
        if (tid < 8 && tid != 2) lds_st(P_FLAG + tid * 4, tid == 0 ? 0xffffffffu : 0u);
        if (tid == 128) lds_st(P_FLAG + 8, tk);
        if (blockIdx.x == 0 && wv == 3) asm volatile("s_waitcnt vmcnt(0)" ::: "memory");
        Va = (u32x4)(0xffffffffu); Vb = Va;
    }
    pin(la);
    __syncthreads();
    ST(0)

    uint32_t w[8], aw[8], xv[8];
    u64 nm[8];
#pragma unroll
    for (int k = 0; k < 8; k++) { w[k] = 0u; aw[k] = 0u; xv[k] = 0u; nm[k] = ~0ull; }
    auto later2 = [&](int k) __attribute__((always_inline)) {
        uint32_t x0, x1;
        asm volatile("v_lshrrev_b32 %0, 4, %2\n\tv_lshrrev_b32 %1, 4, %5\n\tv_bitop3_b32 %0, %2, %0, 12 bitop3:0x72\n\tv_bitop3_b32 %1, %5, %1, 12 bitop3:0x72\n\t"
                     "s_not_b64 exec, %8\n\tds_write_b32 %2, %3 offset:%10\n\tds_or_b32 %0, %4 offset:%10\n\t"
                     "s_not_b64 exec, %9\n\tds_write_b32 %5, %6 offset:%10\n\tds_or_b32 %1, %7 offset:%10\n\ts_mov_b64 exec, -1"
                     : "=&v"(x0), "=&v"(x1)
                     : "v"(aw[k]), "v"(w[k]), "v"(xv[k]), "v"(aw[k + 1]), "v"(w[k + 1]), "v"(xv[k + 1]), "s"(nm[k]), "s"(nm[k + 1]), "n"(P_BUF) : "memory", "scc");
    };
    auto drain = [&]() __attribute__((always_inline)) {
        if (wv == 1) {
            const uint32_t q = (epoch + 1u) & 3u;
            const uint32_t n = __builtin_amdgcn_readfirstlane(lds_ld(vconst<P_FLAG + 16>() + q * 4u));
            if (__builtin_expect(n != 0u, 0)) {
                pin(t8);
                const uint32_t l = (t8 >> 3) & 63u;
                if (l < min(n, (uint32_t)LCAP)) {
                    const u32x4 e = *(const LDS u32x4*)(uintptr_t)(P_LST + (q * LCAP + l) * 16u);
                    lds_st(P_BUF + e.x, e.y);
                    lds_or(P_BUF + slot_x(e.x), e.z);
                }
                lds_st(vconst<P_FLAG + 16>() + q * 4u, vconst<0>());
            }
        }
    };
    auto again = [&](int D) __attribute__((always_inline)) {
        do {
            general(D, true, fm);
            __syncthreads();
            later2(0); later2(2); later2(4); later2(6);
            drain();
#pragma unroll
            for (int k = 0; k < 8; k++) {
                pin(aw[k]);
                bool due = false;
                if (aw[k] & 0x80000000u) {
                    xv[k] -= CHV;
                    if (xv[k] < CHV) {
                        const uint32_t f = (aw[k] >> 16) & 0x3ffu;
                        aw[k] = slot_w(aw[k] & 0xffffu, xv[k]);
                        xv[k] = f << ((xv[k] >> 8) & 31u);
                        due = true;
                    } else lds_st(vconst<P_FLAG>(), epoch + 1u);
                }
                nm[k] = ~__builtin_amdgcn_ballot_w64(due);
            }
            look(*(const LDS u32x2*)(uintptr_t)(P_CNT + t8));
            asm volatile("s_waitcnt lgkmcnt(0)" ::: "memory");
            __syncthreads();
        } while (__builtin_amdgcn_readfirstlane(lds_ld(vconst<P_FLAG>())) == epoch);
    };
    uint32_t mi;
    u32x2 mv[4], ms;
    auto round = [&](auto DD, auto LL, Raw& rc, Raw& rn, int rd) __attribute__((always_inline)) {
        constexpr int D = decltype(DD)::value;
        constexpr bool LAST = decltype(LL)::value;
        const int ng = min(GR, G - tl0 * GR);
        auto hook = [&](int p) __attribute__((always_inline)) {
#pragma unroll
            for (int q = 0; q < 6; q++)
            {
                constexpr int dummy = 0; (void)dummy;
                const int lp = (LPOS >> (4 * q)) & 15;
                if (!LAST && lp < 4 && lp == p) load_one(rc, tl2, q);
                if (!LAST && lp >= 4 && (lp < 8 || lp >= 12 ? lp : lp - 8) == p) load_one(rn, tl1, q);
            }
        };
        uint32_t wn[8], ca[8], px[4], cv[8];
        float mx;
        float cs[2];
        auto convA = [&](int q) __attribute__((always_inline)) {
            if (LAST) return;
            const uint32_t a = rc.a[q >> 1][q & 1];
            const f32x2 av = {__uint_as_float(a << 16), __uint_as_float(a & 0xffff0000u)};
            f32x2 rv;
            asm("v_pk_mul_f32 %0, %1, %2 op_sel_hi:[1,0]" : "=v"(rv) : "v"(av), "s"((u64)__float_as_uint(frr)));
            mx = __builtin_elementwise_maximum(__builtin_elementwise_maximum(__builtin_fabsf(rv.x), __builtin_fabsf(rv.y)), (q & 1) ? mx : 0.f);
            cs[0] = __builtin_amdgcn_cosf(rv.x); cs[1] = __builtin_amdgcn_cosf(rv.y);
            asm volatile("" : "+v"(mx), "+v"(cs[0]), "+v"(cs[1]));
            __builtin_amdgcn_sched_barrier(0);
        };
        auto convB = [&](int k) __attribute__((always_inline)) {
            if (LAST) return;
            const uint32_t o = rc.o[k >> 2][k & 3], i = rc.i[k >> 2][k & 3];
            wn[k] = (__builtin_amdgcn_alignbit(bfi(63u, o, o >> 5), __float_as_uint(cs[k & 1]), 30u) << 14) | (i >> 6);
            ca[k] = (bfi(63u, i, o) << 2) & 0x1ffcu;
            pin(wn[k]); pin(ca[k]);
            __builtin_amdgcn_sched_barrier(0);
        };
        auto convD = [&](int q) __attribute__((always_inline)) {
            if (LAST) return;
            asm volatile("v_cvt_pk_f16_f32 %0, |%1|, |%2|\n\tv_pk_mul_f16 %0, %0, %3 op_sel_hi:[1,0]" : "=v"(px[q]) : "v"(cs[0]), "v"(cs[1]), "s"(0x3ffu));
        };
        uint32_t cst;
        auto check = [&](int j) __attribute__((always_inline)) {
            if (LAST) return;
            if (__builtin_expect((__builtin_amdgcn_ballot_w64(!(mx <= RMAXREV)) | (u64)(uint32_t)(GR - ng)) != 0ull, 0)) {
                pin(t8);
                asm volatile("" : "+v"(rc.a[j]));
                const int lg = j * NT + (int)(t8 >> 3);
#pragma unroll
                for (int k = 0; k < 4; k++) {
                    const uint32_t a = (k & 1) ? (rc.a[j][k >> 1] >> 16) : (rc.a[j][k >> 1] & 0xffffu);
                    const bool nrm = __builtin_fabsf(frr * bf2f(a)) <= RMAXREV;
                    if (!(lg < ng && nrm)) ca[j * 4 + k] = NB * 4u + ((t8 >> 1) & 0xfcu);
                    if (lg < ng && !nrm) {
                        ready();
                        __builtin_amdgcn_raw_buffer_store_b32((uint32_t)((tl0 * GR + lg) * 4 + k), rsR, spec0 - draw(vconst<M_LOG * 4 + 4>()) * 4u, 0, 0);
                    }
                }
            }
            cst = vconst<CSTEP>();
        };
        auto ticket = [&](int k) __attribute__((always_inline)) {
            if (LAST) return;
            cv[k] = lds_add(P_CNT + ca[k], cst);
            __builtin_amdgcn_sched_barrier(0);
        };
        auto insert = [&](int j) __attribute__((always_inline)) {
            const int k0 = 4 * j;
            const bool odd = __builtin_amdgcn_ballot_w64(max(max(cv[k0], cv[k0 + 1]), max(cv[k0 + 2], cv[k0 + 3])) >= 2u * CHV) != 0ull;
#pragma unroll
            for (int k = k0; k < k0 + 4; k += 2) {
                uint32_t x0, x1;
                asm volatile("v_lshrrev_b32 %6, 11, %8\n\tv_lshrrev_b32 %7, 11, %9\n\t"
                             "v_bitop3_b32 %6, %6, %10, 60 bitop3:0x28\n\tv_bitop3_b32 %7, %7, %11, 60 bitop3:0x28\n\t"
                             "v_lshl_or_b32 %0, %10, 4, %6\n\tv_lshl_or_b32 %1, %11, 4, %7\n\t"
                             "v_lshlrev_b32_sdwa %2, %8, %12 dst_sel:DWORD dst_unused:UNUSED_PAD src0_sel:BYTE_1 src1_sel:WORD_0\n\t"
                             "v_lshlrev_b32_sdwa %3, %9, %12 dst_sel:DWORD dst_unused:UNUSED_PAD src0_sel:BYTE_1 src1_sel:WORD_1\n\t"
                             "v_bitop3_b32 %6, %0, %10, 12 bitop3:0x72\n\tv_bitop3_b32 %7, %1, %11, 12 bitop3:0x72\n\t"
                             "v_cmpx_gt_u32 %4, %15, %8\n\tds_write_b32 %0, %13 offset:%16\n\tds_or_b32 %6, %2 offset:%16\n\ts_mov_b64 exec, -1\n\t"
                             "v_cmpx_gt_u32 %5, %15, %9\n\tds_write_b32 %1, %14 offset:%16\n\tds_or_b32 %7, %3 offset:%16\n\ts_mov_b64 exec, -1"
                             : "=&v"(aw[k]), "=&v"(aw[k + 1]), "=&v"(xv[k]), "=&v"(xv[k + 1]), "=&s"(nm[k]), "=&s"(nm[k + 1]), "=&v"(x0), "=&v"(x1)
                             : "v"(cv[k]), "v"(cv[k + 1]), "v"(ca[k]), "v"(ca[k + 1]), "v"(px[k >> 1]), "v"(wn[k]), "v"(wn[k + 1]), "s"(CHV), "n"(P_BUF) : "memory");
                w[k] = wn[k]; w[k + 1] = wn[k + 1];
                if (__builtin_expect(odd, 0)) {
#pragma unroll
                    for (int q = k; q < k + 2; q++) {
                        nm[q] |= __builtin_amdgcn_ballot_w64(cv[q] >= 2u * CHV);
                        if (cv[q] >= 2u * CHV && cv[q] < 0x40000000u) {
                            const uint32_t lq = (epoch + 2u) & 3u;
                            uint32_t p = LCAP;
                            if (cv[q] < 3u * CHV) p = lds_add(vconst<P_FLAG + 16>() + lq * 4u, vconst<1>());
                            if (p < LCAP) {
                                const u32x2 e = {aw[q], w[q]};
                                *(LDS u32x2*)(uintptr_t)(P_LST + (lq * LCAP + p) * 16u) = e;
                                lds_st(P_LST + (lq * LCAP + p) * 16u + 8u, xv[q]);
                            } else {
                                aw[q] = ca[q] | ((q & 1) ? (px[q >> 1] & 0xffff0000u) : (px[q >> 1] << 16)) | 0x80000000u;
                                xv[q] = cv[q] - CHV;
                                lds_st(vconst<P_FLAG>(), epoch);
                            }
                        }
                    }
                }
            }
        };
        constexpr int P0 = D ? 0 : 3, P1 = D ? 1 : 2, P2 = D ? 2 : 1, P3 = D ? 3 : 0;
        const uint32_t fl = lds_ld(vconst<P_FLAG>());
        if (!LAST) asm volatile("" : "+v"(rc.o[0]), "+v"(rc.o[1]), "+v"(rc.i[0]), "+v"(rc.i[1]), "+v"(rc.a[0]), "+v"(rc.a[1]));
        if (!LAST && (rd + 4 >= nstat || tl1 >= ntile) && wv == 2) {
            pin(tk);
            if ((t8 & 511u) == 0u) {
                if (tl1 >= ntile) {
                    if (rd + 3 <= nstat) ready();
                    tk = draw(vconst<M_MD * 4>());
                } else if (rd + 4 == nstat) tk = peek(vconst<M_BEAT * 4>());
                else {
                    if (rd + 3 == nstat) {
                        if (tk == lds_ld(vconst<P_FLAG + 8>())) ready();
                        tk = 0u;
                    }
                    lds_st(vconst<P_FLAG + 4>(), tk);
                    if ((int)tk < ntile - nstat * NWG) tk = draw(vconst<M_TILE * 4>());
                }
            }
        }
        if (!LAST && blockIdx.x == 0 && wv == 3 && (t8 & 511u) == 0u) beat();
        hook(12);
        ST(3)
        pin(t8);
        const uint32_t tr = P_TAB + (t8 & ~63u), u16 = (t8 + t8) & 0x70u;
        convA(0);
        convB(0);
        const bool ag = __builtin_amdgcn_readfirstlane(fl) == epoch;
        if (__builtin_expect(LAST || ag || sm != 0ull, 0)) {
            if (ag) again(D);
            general(D, false, sm);
            Va = *(const LDS u32x4*)(uintptr_t)(P_TAB + (t8 & ~63u) + P0 * 16);
            Vb = *(const LDS u32x4*)(uintptr_t)(P_TAB + (t8 & ~63u) + P1 * 16);
            asm volatile("" : "+v"(Va), "+v"(Vb));
        }
        uint32_t pq = (uint32_t)pb2 | (uint32_t)(pb2 >> 32);
        pq = __builtin_amdgcn_readfirstlane(pq | (pq >> 1));
        uint32_t ad[8];
        ad[0] = take<4 * P0 + 0, D>(Va[0], u16, la, nul);
        convB(1);
        ad[1] = take<4 * P0 + 1, D>(Va[1], u16, la, nul);
        convD(0);
        convA(1);
        ad[2] = take<4 * P0 + 2, D>(Va[2], u16, la, nul);
        convB(2);
        hook(13);
        ad[3] = take<4 * P0 + 3, D>(Va[3], u16, la, nul);
        convB(3);
        put<4 * P0 + 0, LAST ? 3 : 3>(rsA, ad[0], pq);
        ad[4] = take<4 * P1 + 0, D>(Vb[0], u16, la, nul);
        convD(1);
        check(0);
        ticket(0);
        hook(4);
        put<4 * P0 + 1, LAST ? 3 : 4>(rsA, ad[1], pq);
        ad[5] = take<4 * P1 + 1, D>(Vb[1], u16, la, nul);
        convA(2);
        ticket(1);
        put<4 * P0 + 2, LAST ? 3 : 5>(rsA, ad[2], pq);
        ad[6] = take<4 * P1 + 2, D>(Vb[2], u16, la, nul);
        convB(4);
        ticket(2);
        put<4 * P0 + 3, LAST ? 3 : 6>(rsA, ad[3], pq);
        Va = *(const LDS u32x4*)(uintptr_t)(tr + P2 * 16);
        __builtin_amdgcn_sched_barrier(0);
        ad[7] = take<4 * P1 + 3, D>(Vb[3], u16, la, nul);
        convB(5);
        ticket(3);
        SETP(2);
        put<4 * P1 + 0, LAST ? 4 : 8>(rsA, ad[4], pq);
        ad[0] = take<4 * P2 + 0, D>(Va[0], u16, la, nul);
        convD(2);
        convA(3);
        hook(5);
        put<4 * P1 + 1, LAST ? 4 : 7>(rsA, ad[5], pq);
        ad[1] = take<4 * P2 + 1, D>(Va[1], u16, la, nul);
        convB(6);
        put<4 * P1 + 2, LAST ? 4 : 6>(rsA, ad[6], pq);
        ad[2] = take<4 * P2 + 2, D>(Va[2], u16, la, nul);
        convB(7);
        put<4 * P1 + 3, LAST ? 3 : 4>(rsA, ad[7], pq);
        Vb = *(const LDS u32x4*)(uintptr_t)(tr + P3 * 16);
        __builtin_amdgcn_sched_barrier(0);
        ad[3] = take<4 * P2 + 3, D>(Va[3], u16, la, nul);
        convD(3);
        check(1);
        ticket(4);
        SETP(1);
        hook(6);
        put<4 * P2 + 0, LAST ? 4 : 5>(rsA, ad[0], pq);
        ad[4] = take<4 * P3 + 0, D>(Vb[0], u16, la, nul);
        ticket(5);
        put<4 * P2 + 1, LAST ? 4 : 6>(rsA, ad[1], pq);
        ad[5] = take<4 * P3 + 1, D>(Vb[1], u16, la, nul);
        ticket(6);
        put<4 * P2 + 2, LAST ? 4 : 7>(rsA, ad[2], pq);
        ad[6] = take<4 * P3 + 2, D>(Vb[2], u16, la, nul);
        ticket(7);
        SETP(0);
        put<4 * P2 + 3, LAST ? 3 : 7>(rsA, ad[3], pq);
        ad[7] = take<4 * P3 + 3, D>(Vb[3], u16, la, nul);
        hook(7);
        ST(4)
        SETP(PRIO > 1 ? 3 : 0);
        asm volatile("s_waitcnt lgkmcnt(0)" ::: "memory");
        __syncthreads();
        ST(5)
        u32x2 cn;
        if (LAST) {
            mi = __builtin_amdgcn_readfirstlane(lds_ld(P_FLAG + 4));
            {
                const __amdgpu_buffer_rsrc_t rsE = __builtin_amdgcn_make_buffer_rsrc((void*)m, 0, E * 8, 0x00020000);
                ms = __builtin_amdgcn_raw_buffer_load_b64(rsE, (t8 & 511u) * (uint32_t)(E >> 6), 0, 0);
#pragma unroll
                for (int q = 0; q < 4; q++) mv[q] = __builtin_amdgcn_raw_buffer_load_b64(rsE, t8 & 511u, ((mi >> 2) * 256u + (mi & 3u) * 16u + (uint32_t)wv + 64u * q) * 512u, AUX_NT);
                __builtin_amdgcn_sched_barrier(0);
            }
        }
        uint32_t tv;
        if (!LAST) { cn = *(const LDS u32x2*)(uintptr_t)(P_CNT + t8); tv = lds_ld(vconst<P_FLAG + 4>()); }
        __builtin_amdgcn_sched_barrier(0);
        later2(0);
        put<4 * P3, 15>(rsA, ad[4], pq);
        later2(2);
        put<4 * P3 + 1, 15>(rsA, ad[5], pq);
        drain();
        if (!LAST) {
            tl2 = rd + 2 < nstat ? fixed_tile(rd + 2) : nstat * NWG + (int)__builtin_amdgcn_readfirstlane(tv);
            look(cn);
        }
        hook(0);
        SETP(PRIO > 1 ? 2 : 0);
        later2(4);
        put<4 * P3 + 2, 15>(rsA, ad[6], pq);
        later2(6);
        put<4 * P3 + 3, 15>(rsA, ad[7], pq);
        if (!LAST) {
            hook(1);
            SETP(PRIO > 1 ? 1 : 0);
            insert(0);
            hook(2);
            SETP(0);
            pin(t8);
            Va = *(const LDS u32x4*)(uintptr_t)(P_TAB + (t8 & ~63u) + P3 * 16);
            Vb = *(const LDS u32x4*)(uintptr_t)(P_TAB + (t8 & ~63u) + P2 * 16);
            __builtin_amdgcn_sched_barrier(0);
            insert(1);
            hook(3);
            ST(1)
            SETP(3);
            asm volatile("s_waitcnt lgkmcnt(0)" ::: "memory");
            __syncthreads();
            ST(2)
            tl0 = tl1; tl1 = tl2;
        }
    };
#define HMV(A, B) "v_mov_b64 v[" #A ":" #A "+1], v[" #B ":" #B "+1]\n\t"
#define HMV4(A) HMV(A + 8, A + 6) HMV(A + 6, A + 4) HMV(A + 4, A + 2) HMV(A + 2, A)
    int rd = 0;
#pragma unroll 1
    for (; tl1 < ntile; rd += 2) {
        round(IC<0>(), IC<0>(), raw[0], raw[1], rd);
        round(IC<1>(), IC<0>(), raw[1], raw[0], rd + 1);
    }
    if (tl0 >= ntile) asm volatile(HMV4(88) HMV4(98) HMV4(108) HMV4(118));
    asm volatile("" : "+s"(tl0));
    if (tl0 < ntile) round(IC<0>(), IC<0>(), raw[0], raw[1], rd);
    RTS(1)
    if (t8 == 1024u) {
        if (fixed_tile(0) >= ntile) { ready(); tk = draw(vconst<M_MD * 4>()); }
        lds_st(P_FLAG + 4, tk);
    }
    round(IC<1>(), IC<1>(), raw[1], raw[0], 0);
    if (__builtin_amdgcn_readfirstlane(lds_ld(P_FLAG + 16 + ((epoch + 2u) & 3u) * 4u)) != 0u) {
        asm volatile("s_waitcnt lgkmcnt(0)" ::: "memory");
        __syncthreads();
        look(*(const LDS u32x2*)(uintptr_t)(P_CNT + t8));
        general(0, true, fm);
        __syncthreads();
        drain();
    }
    {
        uint32_t* tcnt = meta + W_TCNT / 4;
        const u32x2 c = *(const LDS u32x2*)(uintptr_t)(P_CNT + t8);
        const uint32_t tw = tab_at();
#pragma unroll
        for (int e = 0; e < 2; e++) {
            const uint32_t g = U[e] & 15u;
            lds_st(tw + e * 64u, g >= 8u ? 0x80000000u : ((U[e] + ((g & 1u) ? 0u : SK * 64u)) & 0x7fffff81u) | (((7u - g) << 1) & 12u));
            *(LDS uint8_t*)(uintptr_t)(P_CNT + (t8 & ~511u) + (((tw - P_TAB) >> 2) & 127u) + e * 16u) =
                (uint8_t)(g < 8u ? 0u : ((g - 8u) >> 1) + (((c[e] + 2u * CSTEP) >> 15) << 2) + (g & 1u) * 0x50u);
        }
        asm volatile("s_waitcnt lgkmcnt(0)" ::: "memory");
        __syncthreads();
        const uint32_t u16 = (t8 + t8) & 0x70u;
        u32x4 En[4];
#pragma unroll
        for (int p = 0; p < 4; p++) En[p] = *(const LDS u32x4*)(uintptr_t)(P_TAB + (t8 & ~63u) + p * 16u);
        const u32x4 cb = *(const LDS u32x4*)(uintptr_t)(P_CNT + (t8 & ~511u) + ((t8 >> 2) & 0x70u));
        u32x2 N[16];
#pragma unroll
        for (int s = 0; s < 16; s++) N[s] = *(const LDS u32x2*)(uintptr_t)(la + s * 256u);
        if (u16 == 0u) *(u32x4*)(tcnt + blockIdx.x * 512u + (t8 >> 4)) = cb;
        const uint32_t sg = P_BUF + ((t8 & ~63u) << 4);
        uint32_t ps;
        auto rest = [&](auto SL) __attribute__((always_inline)) {
            constexpr int s = decltype(SL)::value;
            const uint32_t en = En[s >> 2][s & 3], by = cb[s >> 2] >> ((s & 3) * 8);
            const u32x2 Hr = hget<HREG(s, 0)>();
            const uint32_t at = ps + ((la & 0x38u) ^ ((s & 3) << 4));
            *(LDS u32x2*)(uintptr_t)at = Hr;
            *(LDS u32x2*)(uintptr_t)(at + (by & 0x40u)) = N[s];
            ps += (by & 0x3cu) << 2;
            const u32x2 Hs = (en & 1u) ? Hr : nul;
            const u32x4 d = {Hs.x, Hs.y, N[s].x, N[s].y};
            uint32_t a = __builtin_amdgcn_bitop3_b32(en, u16, 15u, 0x54);
            __builtin_amdgcn_raw_buffer_store_b128(d, rsR, a, 0, POS(s) >= 16 - NTS ? AUX_NT : AUX_SC1);
            if (__builtin_expect(__builtin_amdgcn_ballot_w64((en & 12u) != 0u) != 0ull, 0)) {
                const u32x4 dn = {nul.x, nul.y, nul.x, nul.y};
#pragma unroll 1
                for (uint32_t k = (en >> 2) & 3u; k > 0u; k--) {
                    a += SK * 128u;
                    __builtin_amdgcn_raw_buffer_store_b128(dn, rsR, a, 0, AUX_SC1);
                }
            }
        };
        auto pack = [&](int p) __attribute__((always_inline)) {
            const uint32_t a = (SBASE + ((((t8 >> 1) & 0x60u) + (t8 >> 8)) * 2u + p) * (NWG * 9u) + blockIdx.x * 9u) * 128u + u16;
            asm volatile("" ::: "memory");
#pragma unroll 1
            for (uint32_t q = 0; __builtin_amdgcn_ballot_w64(sg + q < ps) != 0ull; q += 256u) {
                const u32x4 d0 = *(const LDS u32x4*)(uintptr_t)(sg + q + u16), d1 = *(const LDS u32x4*)(uintptr_t)(sg + q + 128u + u16);
                __builtin_amdgcn_raw_buffer_store_b128(d0, rsR, sg + q < ps ? a + q : 0xffffff00u, 0, AUX_SC1);
                __builtin_amdgcn_raw_buffer_store_b128(d1, rsR, sg + q + 128u < ps ? a + q + 128u : 0xffffff00u, 0, AUX_SC1);
            }
            asm volatile("" ::: "memory");
        };
        asm volatile("" : "+v"(N[0]), "+v"(N[1]), "+v"(N[2]), "+v"(N[3]), "+v"(N[4]), "+v"(N[5]), "+v"(N[6]), "+v"(N[7]) :: "memory");
        asm volatile("" : "+v"(N[8]), "+v"(N[9]), "+v"(N[10]), "+v"(N[11]), "+v"(N[12]), "+v"(N[13]), "+v"(N[14]), "+v"(N[15]) :: "memory");
        ps = sg;
        rest(IC<0>()); rest(IC<1>()); rest(IC<2>()); rest(IC<3>()); rest(IC<4>()); rest(IC<5>()); rest(IC<6>()); rest(IC<7>());
        pack(0);
        ps = sg;
        rest(IC<8>()); rest(IC<9>()); rest(IC<10>()); rest(IC<11>()); rest(IC<12>()); rest(IC<13>()); rest(IC<14>()); rest(IC<15>());
        pack(1);
    }
    RTS(2)
    ST(6)
    if (mi < SLOTI / 64) {
        uint32_t* mdc = meta + W_MDC / 4;
        u32x2* stat = (u32x2*)(meta + W_STAT / 4);
        float w[16];
        load_w(wdown, w);
        auto dot = [&](u32x2 v, int c) __attribute__((always_inline)) -> float {
            return bf2f(v.x & 0xffffu) * w[c] + bf2f(v.x >> 16) * w[4 + c] + bf2f(v.y & 0xffffu) * w[8 + c] + bf2f(v.y >> 16) * w[12 + c];
        };
        int e0[4];
#pragma unroll
        for (int c = 0; c < 4; c++) {
            const int ex = (int)(wave_max(__float_as_uint(dot(ms, c)) & 0x7fffffffu) >> 23);
            e0[c] = __builtin_amdgcn_readfirstlane(ex == 0 ? 0 : min(max(ex - 129, -100), 100));
        }
#pragma unroll
        for (int k = 0; k < 16; k++)
            w[k] = __uint_as_float(__builtin_amdgcn_readfirstlane(__float_as_uint(w[k] * __uint_as_float((uint32_t)(127 - e0[k & 3]) << 23))));
        const uint32_t lane = (t8 >> 3) & 63u;
#pragma unroll
        for (int cp = 0; cp < 2; cp++) {
            u32x4 o;
            u16x2 mx = {0, 0};
            float sq[2] = {0.f, 0.f};
#pragma unroll
            for (int q = 0; q < 4; q++) {
                const uint32_t hb = pk_bf16(dot(mv[q], 2 * cp), dot(mv[q], 2 * cp + 1));
                const float x0 = __uint_as_float(hb << 16), x1 = __uint_as_float(hb & 0xffff0000u);
                o[q] = __builtin_bit_cast(uint32_t, __builtin_amdgcn_cvt_pkrtz(x0, x1));
                mx = __builtin_elementwise_max(mx, __builtin_bit_cast(u16x2, hb & 0x7fff7fffu));
                sq[0] += x0 * x0; sq[1] += x1 * x1;
            }
            if (cp == 0) __syncthreads();
            *(LDS u32x4*)(uintptr_t)(P_BUF + (cp * 64u + lane) * 272u + wv * 16) = o;
#pragma unroll
            for (int e = 0; e < 2; e++) {
                const u32x2 st = {wave_max(mx[e]), __float_as_uint(wave_sum(sq[e]))};
                if (lane == 0u) *(LDS u32x2*)(uintptr_t)(P_LST + ((2 * cp + e) * 16 + wv) * 8) = st;
            }
        }
        __syncthreads();
        {
            const __amdgpu_buffer_rsrc_t rsM = __builtin_amdgcn_make_buffer_rsrc((void*)mdc, 0, (int)(128u * SLOTI * 4u), 0x00020000);
#pragma unroll
            for (int cp = 0; cp < 2; cp++) {
                const uint32_t row = cp * 64u + (t8 >> 7), c16 = (t8 + t8) & 0xf0u;
                __builtin_amdgcn_raw_buffer_store_b128(*(const LDS u32x4*)(uintptr_t)(P_BUF + row * 272u + c16), rsM, row * (SLOTI * 4u) + c16 + mi * 256u, 0, AUX_SC1);
            }
        }
        if (wv == 0) {
            const u32x2 st = *(const LDS u32x2*)(uintptr_t)(P_LST + t8);
            const u32x2 r = {row_max(st.x) | (row_max(st.x ? 0xffffu - st.x : 0u) << 16), __float_as_uint(row_sum(__uint_as_float(st.y)))};
            if ((lane & 15u) == 15u) stat[(lane >> 4) * NWG + mi] = r;
            if (mi == 0u && lane < 4u) meta[M_E0 + lane] = (uint32_t)(lane == 0u ? e0[0] : lane == 1u ? e0[1] : lane == 2u ? e0[2] : e0[3]);
        }
    }
    if (blockIdx.x == 0 && t8 == 1536u) {
        do {
            beat();
            __builtin_amdgcn_s_sleep(8);
        } while (peek(vconst<M_MD * 4>()) < NWG);
    }
    RTS(3)
    RTS(4)
    ST(7)
    ST_OUT(0)
}

#define A_MD 0
#define A_ACC (SLOTI * 4)
#define A_FLAG (A_ACC + SLOTO * 4)
#define A_LDS (A_FLAG + 512)
static_assert(SLOTI * 4 == 3 * NT * 16 + 1024, "md staging assumes three full vectors and 256 dwords");
#define BIAS 0x40004000u
#define NFAST 8
#define NLD 10
#define PRE0 3
#define SHARE (SLOTO / 8)
#define SPINCAP 4096u
#define ZSIG 9.0f
#define HSLOW 0x80008000u
#define GAS __attribute__((address_space(1)))

__global__ __launch_bounds__(NT) __attribute__((amdgpu_num_vgpr(53))) void acc_kernel(const char* __restrict__ recs,
                                                 const u32x4* __restrict__ tcnt,
                                                 const uint32_t* __restrict__ mdc, const u32x4* __restrict__ stat,
                                                 const int* __restrict__ iin, const int* __restrict__ iout, const uint16_t* __restrict__ ang,
                                                 const u32x2* __restrict__ m, const uint16_t* __restrict__ wdown,
                                                 const uint16_t* __restrict__ freq,
                                                 const uint16_t* __restrict__ dist, const uint16_t* __restrict__ centers,
                                                 const uint16_t* __restrict__ wrbf, const uint16_t* __restrict__ wsbf,
                                                 const uint16_t* __restrict__ wup,
                                                 uint32_t* partial, unsigned* meta, u32x2* __restrict__ out, int T, int E, int ks, int lcap, float zf) {
    extern __shared__ __attribute__((aligned(16))) char smem[];
    ST_DECL
    RTS(8)
    unsigned* acc = (unsigned*)(smem + A_ACC);
    unsigned* s_flag = (unsigned*)(smem + A_FLAG);
    const int tid = threadIdx.x;
    const int lane = tid & 63;
    const int wv = __builtin_amdgcn_readfirstlane(tid >> 6);
    const int cp = (blockIdx.x >> 3) & 1;
    const int h = blockIdx.x & 3;
    const int j = ((blockIdx.x >> 2) & 1) * 16 + (blockIdx.x >> 4);
    u32x4 tc;
    const __amdgpu_buffer_rsrc_t rsM = __builtin_amdgcn_make_buffer_rsrc((void*)mdc, 0, (int)(128u * SLOTI * 4u), 0x00020000);

    u32x4 pm[4];
    u32x4 ring[NLD];
    auto load_md = [&](int ff) __attribute__((always_inline)) {
        const uint32_t so = (uint32_t)(cp * 64 + ((ABL & 64) ? 0 : ff * 4) + h) * (SLOTI * 4u);
#pragma unroll
        for (int v = 0; v < 3; v++) pm[v] = __builtin_amdgcn_raw_buffer_load_b128(rsM, (uint32_t)tid * 16u, so + (uint32_t)v * (NT * 16u), 0);
        if (wv == 0) pm[3] = __builtin_amdgcn_raw_buffer_load_b128(rsM, (uint32_t)tid * 16u, so + 3u * NT * 16u, 0);
        __builtin_amdgcn_sched_barrier(0);
    };
    const uint32_t lf = ((uint32_t)(tid & ~31) * 16u + (uint32_t)(tid & 12) * 32u) * SR + (uint32_t)(tid & 16) * 4u + (uint32_t)(tid & 3) * 16u;
    auto load_main = [&](int i, int ff, bool nt) __attribute__((always_inline)) -> u32x4 {
        const uint32_t col = (uint32_t)(POS(ff) * 128 + h * 32 + ((ABL & 8) ? 0 : j));
        if (ABL & 8) nt = false;
        const __amdgpu_buffer_rsrc_t rs = __builtin_amdgcn_make_buffer_rsrc((void*)(recs + ((col >> 7) * SS + (col & 127u) * SC) * 128u), 0, ks * (int)(SK * 128u), 0x00020000);
        const int so = (int)(((i >> 1) * SK + (i & 1) * 128u * SR) * 128u);
        if (nt) return __builtin_amdgcn_raw_buffer_load_b128(rs, lf, so, AUX_NT);
        return __builtin_amdgcn_raw_buffer_load_b128(rs, lf, so, 0);
    };
    const __amdgpu_buffer_rsrc_t rsT = __builtin_amdgcn_make_buffer_rsrc((void*)recs, 0, (int)(LBASE * 128u), 0x00020000);
    const uint32_t tof = (uint32_t)(lane & 2) * (TPL * 64u) + (uint32_t)(tid >> 2) * 128u + (uint32_t)(lane & 1) * 64u + 4u;
    const uint32_t sp0 = (SBASE + (uint32_t)(h * 32 + j) * (2u * NWG * 9u) + (uint32_t)(tid >> 2) * 9u) * 128u;
    auto tail_rank = [&](uint32_t c, uint32_t sp, int ff, uint32_t t0, int& dst, uint32_t& en) __attribute__((always_inline)) -> int {
        const int k4 = (int)(((uint32_t)(lane & 3) + t0) * 4u) - (int)((c & 3u) * 8u);
        const int left = (int)((c >> 2) & 15u) - k4;
        const bool has = left > 0;
        en = k4 < 0 ? tof + (TBASE + (uint32_t)(POS(ff) * 128 + h * 32 + j) * TST) * 128u : sp + (uint32_t)(k4 * 16 + min(max(left, 0), 4));
        const u64 mask = __builtin_amdgcn_ballot_w64(has);
        const int n = __builtin_popcountll(mask);
        const int rank = __builtin_amdgcn_mbcnt_hi((uint32_t)(mask >> 32), __builtin_amdgcn_mbcnt_lo((uint32_t)mask, 0u));
        dst = (has ? rank : (n + lane - rank)) << 2;
        return n;
    };
    auto load_tail = [&](uint32_t en, u64& tm) __attribute__((always_inline)) -> u32x4 {
        const bool on = (uint32_t)(lane & 3) < (en & 7u) && !(ABL & 32);
        tm = __builtin_amdgcn_ballot_w64(on);
        return __builtin_amdgcn_raw_buffer_load_b128(rsT, on ? (en & ~15u) + (uint32_t)(lane & 3) * 16u : 0xffffff00u, 0, 0);
    };
    float rs[4][4], rg[4][4];
    u32x4 em[2];
    u32x2 dw2;
    const __amdgpu_buffer_rsrc_t rsE = __builtin_amdgcn_make_buffer_rsrc((void*)m, 0, E * 8, 0x00020000);
    const __amdgpu_buffer_rsrc_t rsD = __builtin_amdgcn_make_buffer_rsrc((void*)dist, 0, E * 2, 0x00020000);
    auto edge_loads = [&](int sh) __attribute__((always_inline)) {
        const int ol = sh * SHARE + tid * 4;
        const uint32_t e0 = (tid < SHARE / 4) ? (uint32_t)(((ol >> 6) << 11) | (j << 6) | (ol & 63)) : 0x0fffff00u;
        em[0] = __builtin_amdgcn_raw_buffer_load_b128(rsE, e0 * 8u, 0, AUX_NT);
        em[1] = __builtin_amdgcn_raw_buffer_load_b128(rsE, e0 * 8u + 16u, 0, AUX_NT);
        dw2 = __builtin_amdgcn_raw_buffer_load_b64(rsD, e0 * 2u, 0, AUX_NT);
    };
    auto factors = [&]() __attribute__((always_inline)) {
        const float sq = 3.7982826f;
        const f32x2 d01 = {bf2f(dw2.x & 0xffffu), bf2f(dw2.x >> 16)}, d23 = {bf2f(dw2.y & 0xffffu), bf2f(dw2.y >> 16)};
        f32x2 a[4][4];
#pragma unroll
        for (int q = 0; q < 16; q++) a[q >> 2][q & 3] = (f32x2)(0.f);
#pragma unroll 2
        for (int r = 0; r < 8; r++) {
            const uint32_t cw = ((const uint32_t*)centers)[r >> 1];
            const float cn = bf2f((r & 1) ? (cw >> 16) : (cw & 0xffffu));
            const u32x2 s2 = ((const u32x2*)wsbf)[r], r2 = ((const u32x2*)wrbf)[r];
            const f32x2 w[4] = {{bf2f(s2.x & 0xffffu), bf2f(s2.x >> 16)}, {bf2f(s2.y & 0xffffu), bf2f(s2.y >> 16)},
                                {bf2f(r2.x & 0xffffu), bf2f(r2.x >> 16)}, {bf2f(r2.y & 0xffffu), bf2f(r2.y >> 16)}};
            const f32x2 y01 = (d01 - cn) * sq, y23 = (d23 - cn) * sq;
            const f32x2 t01 = -y01 * y01, t23 = -y23 * y23;
            const float rb[4] = {__builtin_amdgcn_exp2f(t01.x), __builtin_amdgcn_exp2f(t01.y), __builtin_amdgcn_exp2f(t23.x), __builtin_amdgcn_exp2f(t23.y)};
#pragma unroll
            for (int q = 0; q < 4; q++) {
#pragma unroll
                for (int k = 0; k < 4; k++) a[q][k] = __builtin_elementwise_fma(w[k], (f32x2)(rb[q]), a[q][k]);
            }
        }
#pragma unroll
        for (int q = 0; q < 4; q++) {
#pragma unroll
            for (int k = 0; k < 4; k++) { rs[q][k] = a[q][k >> 1][k & 1]; rg[q][k] = a[q][2 + (k >> 1)][k & 1]; }
        }
    };
#define ADT(V, O) "ds_write_addtid_b32 " V " offset:" #O "\n\t"
    auto store_md = [&]() __attribute__((always_inline)) {
        asm volatile("s_mov_b32 m0, %12\n\ts_nop 0\n\t" ADT("%0", 0) ADT("%1", 256) ADT("%2", 512) ADT("%3", 768) ADT("%4", 16384) ADT("%5", 16640) ADT("%6", 16896) ADT("%7", 17152)
                     ADT("%8", 32768) ADT("%9", 33024) ADT("%10", 33280) ADT("%11", 33536)
                     :: "v"(pm[0][0]), "v"(pm[0][1]), "v"(pm[0][2]), "v"(pm[0][3]), "v"(pm[1][0]), "v"(pm[1][1]), "v"(pm[1][2]), "v"(pm[1][3]),
                        "v"(pm[2][0]), "v"(pm[2][1]), "v"(pm[2][2]), "v"(pm[2][3]), "s"(wv * 1024) : "memory");
        if (wv == 0) asm volatile("s_mov_b32 m0, 0\n\ts_nop 0\n\t" ADT("%0", 49152) ADT("%1", 49408) ADT("%2", 49664) ADT("%3", 49920)
                                  :: "v"(pm[3][0]), "v"(pm[3][1]), "v"(pm[3][2]), "v"(pm[3][3]) : "memory");
    };
    const int me = h * 2 + cp;
    bool fast, more;
    uint32_t need, mg0, mg1;
    uint32_t guard = 0;
    uint32_t sgn = 0x8000u;
    pin(sgn);

#define GAT3(M) { uint32_t a0_, a1_, a2_; asm volatile( \
        "v_lshlrev_b16 %0, 2, %3\n\tv_lshlrev_b16 %1, 2, %4\n\tv_lshlrev_b16 %2, 2, %5\n\t" \
        "ds_read_b32 v[%6], %0\n\tds_read_b32 v[%6+1], %1\n\tds_read_b32 v[%6+2], %2" \
        : "=&v"(a0_), "=&v"(a1_), "=&v"(a2_) : "v"(w0), "v"(w1), "v"(w2), "n"(M) : "memory"); }
#define MIXL " op_sel_hi:[1,1,0]\n\t"
#define MIXH " op_sel:[0,1,0] op_sel_hi:[1,1,0]\n\t"
#define SDW " dst_sel:DWORD dst_unused:UNUSED_PAD src0_sel:DWORD src1_sel:WORD_1\n\t"
#define SCAT(PRE, POST, M) { uint32_t c0_, c1_, c2_, a0_, a1_, a2_; asm volatile( \
        "v_bitop3_b32 %6, %6, v[%15-9], v[%15-8] bitop3:0xfe\n\tv_and_b32 %0, 0x3ff, %7\n\tv_lshrrev_b32 %2, 22, %7\n\tv_or_b32 %6, v[%15-7], %6\n\t" PRE \
        "v_bitop3_b32 %0, %11, %8, %0 bitop3:0xea\n\tv_bitop3_b32 %2, %11, %10, %2 bitop3:0xea\n\tv_bfe_u32 %1, %7, 11, 10\n\t" \
        "v_fma_mix_f32 %3, %0, v[%15], %12" MIXL "v_bitop3_b32 %1, %11, %9, %1 bitop3:0xea\n\tv_fma_mix_f32 %0, %0, v[%15], %13" MIXH \
        "v_fma_mix_f32 %4, %1, v[%15+1], %12" MIXL "v_lshl_add_u32 %0, %0, 16, %3\n\tv_lshlrev_b32_sdwa %3, 2, %8" SDW \
        "v_fma_mix_f32 %1, %1, v[%15+1], %13" MIXH "ds_add_rtn_u32 v[%15-9], %3, %0 offset:%14\n\t" \
        "v_fma_mix_f32 %5, %2, v[%15+2], %12" MIXL "v_lshl_add_u32 %1, %1, 16, %4\n\tv_lshlrev_b32_sdwa %4, 2, %9" SDW \
        "v_fma_mix_f32 %2, %2, v[%15+2], %13" MIXH "ds_add_rtn_u32 v[%15-8], %4, %1 offset:%14\n\t" \
        "v_lshl_add_u32 %2, %2, 16, %5\n\tv_lshlrev_b32_sdwa %5, 2, %10" SDW \
        "ds_add_rtn_u32 v[%15-7], %5, %2 offset:%14" POST \
        : "=&v"(c0_), "=&v"(c1_), "=&v"(c2_), "=&v"(a0_), "=&v"(a1_), "=&v"(a2_), "+v"(guard) \
        : "v"(xw), "v"(w0), "v"(w1), "v"(w2), "v"(sgn), "s"(mg0), "s"(mg1), "n"(A_ACC), "n"(M), "s"(tm) : "memory"); }
    asm volatile("v_mov_b32 v110, 0\n\tv_mov_b32 v111, 0\n\tv_mov_b32 v112, 0\n\tv_mov_b32 v113, 0\n\tv_mov_b32 v114, 0\n\tv_mov_b32 v115, 0\n\t"
                 "v_mov_b32 v116, 0\n\tv_mov_b32 v117, 0\n\tv_mov_b32 v118, 0" ::: "v106", "v127");
    auto unmix = [&](int i) __attribute__((always_inline)) {
        const auto s0 = __builtin_amdgcn_permlane16_swap(ring[i][0], ring[i][2], false, false);
        const auto s1 = __builtin_amdgcn_permlane16_swap(ring[i][1], ring[i][3], false, false);
        ring[i][0] = s0[0]; ring[i][2] = s0[1]; ring[i][1] = s1[0]; ring[i][3] = s1[1];
    };
    auto gather = [&](int R, int i, int p) __attribute__((always_inline)) {
        if (i < NFAST) unmix(i);
        const uint32_t w0 = ring[i][0 ^ R], w1 = ring[i][1 ^ R], w2 = ring[i][2 ^ R];
        if (ABL & 16) { asm volatile("" :: "v"(w0), "v"(w1), "v"(w2)); return; }
        if (p == 0) GAT3(119) else if (p == 1) GAT3(122) else GAT3(125)
    };
    auto scatter = [&](int R, int i, int p, u64 tm) __attribute__((always_inline)) {
        const uint32_t xw = ring[i][3 ^ R], w0 = ring[i][0 ^ R], w1 = ring[i][1 ^ R], w2 = ring[i][2 ^ R];
        if (ABL & 16) { asm volatile("" :: "v"(w0), "v"(w1), "v"(w2), "v"(xw)); return; }
        if (i < NFAST) { if (p == 0) SCAT("", "", 119) else if (p == 1) SCAT("", "", 122) else SCAT("", "", 125) }
        else if (p == 0) SCAT("s_mov_b64 exec, %16\n\t", "\n\ts_mov_b64 exec, -1", 119)
        else SCAT("s_mov_b64 exec, %16\n\t", "\n\ts_mov_b64 exec, -1", 125)
    };
#define WAIT_L(N) asm volatile("s_waitcnt lgkmcnt(" #N ")" ::: "memory")
    int ntn;
    u64 tm0 = 0ull, tm1;
    uint32_t ta[2], sp = sp0;
    auto list1 = [&](int ff) __attribute__((always_inline)) {
        int dst;
        uint32_t en;
        ntn = tail_rank(tc.x, sp, ff, 0u, dst, en);
        asm volatile("ds_permute_b32 v106, %0, %1" :: "v"(dst), "v"(en) : "memory");
    };
    auto list2 = [&]() __attribute__((always_inline)) {
        asm volatile("ds_bpermute_b32 v107, %0, v106\n\tds_bpermute_b32 v108, %0, v106 offset:64" :: "v"(lane & ~3) : "memory");
    };
    auto step = [&](auto RR, auto NN, int ff) __attribute__((always_inline)) {
        constexpr int R = decltype(RR)::value;
        constexpr bool nt = decltype(NN)::value == 1, last = decltype(NN)::value == 2;
        gather(R, 0, 0);
        scatter(R, NFAST, 2, tm0);
        const int ntl = ntn;
        asm volatile("v_mov_b32 %0, v107\n\tv_mov_b32 %1, v108" : "=v"(ta[0]), "=v"(ta[1]));
        ring[NFAST] = load_tail(ta[0], tm0);
        ring[NFAST + 1] = load_tail(ta[1], tm1);
        if (last) edge_loads(me);
        else load_md(ff + 1);
        gather(R, 1, 1);
        const uint32_t tcur = tc.x, spc = sp;
#pragma unroll
        for (int i = 0; i < NFAST - 1; i++) {
            gather(R, i + 2, (i + 2) % 3);
            if (i < 2) WAIT_L(9); else WAIT_L(12);
            scatter(R, i, i % 3, 0ull);
            if (!last) ring[i] = load_main(i, ff + 1, nt);
            if (i == 0 && !last) {
                tc.x = __builtin_amdgcn_alignbit(tc.y, tc.x, 8u); tc.y = __builtin_amdgcn_alignbit(tc.z, tc.y, 8u);
                tc.z = __builtin_amdgcn_alignbit(tc.w, tc.z, 8u); tc.w >>= 8;
                sp = ff == 7 ? sp0 + NWG * 9u * 128u : sp + ((tcur & 0x3cu) << 2);
                list1(ff + 1);
            }
            if (i == 4 && !last) list2();
            __builtin_amdgcn_sched_barrier(0);
        }
        if (__builtin_expect(ntl > 16, 0)) {
            gather(R, NFAST + 1, 0);
            WAIT_L(0);
            scatter(R, NFAST + 1, 0, tm1);
        }
        if (__builtin_expect((__builtin_amdgcn_ballot_w64((tcur & 2u) != 0u) | (u64)(ntl > 32 ? 1 : 0)) != 0ull, 0)) {
#pragma unroll 1
            for (int e = 2; e < 6; e++) {
                int dst;
                uint32_t en;
                if (tail_rank(tcur, spc, ff, (uint32_t)e & 4u, dst, en) <= (e & 3) * 16) continue;
                const int cmp = __builtin_amdgcn_ds_permute(dst, (int)en);
                ring[NFAST + 1] = load_tail((uint32_t)__builtin_amdgcn_ds_bpermute((e & 3) * 64 + (lane & ~3), cmp), tm1);
                gather(R, NFAST + 1, 0);
                WAIT_L(0);
                scatter(R, NFAST + 1, 0, tm1);
            }
        }
        WAIT_L(3);
        ST(1)
        if (!last) {
            __builtin_amdgcn_s_barrier();
            ST(3)
            store_md();
        }
        scatter(R, NFAST - 1, 1, 0ull);
        if (!last) {
            ring[NFAST - 1] = load_main(NFAST - 1, ff + 1, nt);
            WAIT_L(3);
            __builtin_amdgcn_s_barrier();
            ST(4)
        }
    };
    auto steps = [&](auto RR) __attribute__((always_inline)) {
        constexpr int R = decltype(RR)::value;
        tc = tcnt[(wv * 16 + (lane >> 2)) * 128 + j * 4 + h];
        u32x4 sv[2];
        uint32_t e0;
        if (wv < 4) {
            sv[0] = (u32x4)(0u); sv[1] = sv[0];
            if (lane < SLOTI / 256) { sv[0] = stat[tid * 2]; sv[1] = stat[tid * 2 + 1]; }
            e0 = meta[M_E0 + wv];
        }
        const u32x2 extra = *(const u32x2*)(meta + M_LOG);
        load_md(0);
#pragma unroll
        for (int i = 0; i < PRE0; i++) ring[i] = load_main(i, 0, false);
        __builtin_amdgcn_sched_barrier(0);
        {
            const u32x4 bi = {BIAS, BIAS, BIAS, BIAS};
            for (int i = tid; i < SLOTO / 4; i += NT) *(LDS u32x4*)(uintptr_t)(A_ACC + i * 16) = bi;
        }
        asm volatile("" :: "s"(*(const uint32_t*)centers ^ *(const uint32_t*)wsbf ^ *(const uint32_t*)wrbf ^ *(const uint32_t*)wup));
        list1(0);
        if (wv < 4) {
            const uint32_t mxb = wave_max(max(max(sv[0].x & 0xffffu, sv[0].z & 0xffffu), max(sv[1].x & 0xffffu, sv[1].z & 0xffffu)));
            const uint32_t mnb = 0xffffu - (wave_max(max(max(sv[0].x, sv[0].z), max(sv[1].x, sv[1].z))) >> 16);
            const float s2 = wave_sum((__uint_as_float(sv[0].y) + __uint_as_float(sv[0].w)) + (__uint_as_float(sv[1].y) + __uint_as_float(sv[1].w)));
            const float rng = fmaxf(sqrtf(zf * s2), 2.0f * bf2f(mxb)) * 6.103515625e-05f;
            const uint32_t eb = mxb ? (__float_as_uint(rng) + 0x7fffffu) >> 23 : 127u;
            const bool ok = !mxb || (mxb < 0x4700u && eb >= 107u && eb <= 140u && mnb >= ((eb + 8u) << 7));
            if (lane == 0) {
                s_flag[8 + wv] = (eb & 0xffu) | (ok ? 0x100u : 0u) | (mxb ? 0x200u : 0u);
                s_flag[12 + wv] = __float_as_uint(__uint_as_float(((eb + e0) & 0xffu) << 23) * (1024.0f / 1023.0f));
                s_flag[wv] = 0u;
            }
        }
        WAIT_L(0);
        list2();
        store_md();
#pragma unroll
        for (int i = PRE0; i < NFAST; i++) ring[i] = load_main(i, 0, false);
        ring[NFAST] = (u32x4)(0u);
        WAIT_L(0);
        __syncthreads();
        const uint32_t q0 = __builtin_amdgcn_readfirstlane(s_flag[8 + 2 * cp]), q1 = __builtin_amdgcn_readfirstlane(s_flag[9 + 2 * cp]);
        fast = (q0 & q1 & 0x100u) != 0u;
        need = (q0 >> 9) | ((q1 >> 9) << 1);
        more = (__builtin_amdgcn_readfirstlane(extra.x | extra.y) | (uint32_t)(T & 3)) != 0u;
        mg0 = (((q0 & 0xffu) + 9u) << 23) | 0x400000u;
        mg1 = ((((q1 & 0xffu) + 9u) << 23) | 0x400000u) + ((0u - (mg0 >> 16)) & 0xffffu);
        if (!fast) return;
        ST(0)
        RTS(9)
        int ff = 0;
#pragma unroll 1
        for (; ff < 15 - NTS; ff++) step(RR, IC<0>(), ff);
#pragma unroll 1
        for (; ff < 15; ff++) step(RR, IC<1>(), ff);
        step(RR, IC<2>(), 15);
        scatter(R, NFAST, 2, tm0);
    };
    auto irregular = [&](auto f) __attribute__((always_inline)) {
        const float fr = bf2f(freq[0]);
        const char* lg = recs + (size_t)LBASE * 128u;
        const uint16_t* tags = (const uint16_t*)(lg + (size_t)lcap * 128u);
        const uint32_t nl = meta[M_LOG], ns = meta[M_LOG + 1];
#pragma unroll 1
        for (uint32_t q = tid; q < nl * 8u; q += NT) {
            const uint32_t b = tags[q >> 3];
            if ((b & 127u) == (uint32_t)(h * 32 + j)) {
                const uint32_t* un = (const uint32_t*)(lg + (size_t)q * 16u);
                const uint32_t xw = un[3 ^ h];
#pragma unroll 1
                for (int k = 0; k < 3; k++) {
                    const uint32_t ww = un[k ^ h];
                    const float c = (float)((xw >> (k * 11)) & 0x3ffu) * (1.0f / 1023.0f);
                    if (!(ww & WNULL)) f(ww >> 16, ((ww & 0x3fffu) << 6) | ((ALT ? ((b >> 7) * 3u) & 15u : b >> 7) << 2) | (uint32_t)h, (ww & 0x8000u) ? -c : c);
                }
            }
        }
#pragma unroll 1
        for (uint32_t q = tid; q < ns + (uint32_t)(T & 3); q += NT) {
            const uint32_t t = q < ns ? ((const uint32_t*)tags)[-1 - (int)q] : (uint32_t)(T & ~3) + (q - ns);
            const uint32_t i = (uint32_t)iin[t], o = (uint32_t)iout[t];
            if (((o >> 6) & 31u) == (uint32_t)j && (i & 3u) == (uint32_t)h) f(((o >> 11) << 6) | (o & 63u), i, cosf(fr * bf2f(ang[t])));
        }
    };

    if (h == 0) steps(IC<0>());
    else if (h == 1) steps(IC<1>());
    else if (h == 2) steps(IC<2>());
    else steps(IC<3>());
    if (fast) {
        if (more) irregular([&](uint32_t ol, uint32_t i, float cf) __attribute__((always_inline)) {
            const uint32_t md = mdc[(cp * 64u + (i & 63u)) * SLOTI + ((i >> 14) << 8) + ((i >> 4) & 0xfcu) + ((i >> 12) & 3u)];
            const float c = cf * (1023.0f / 16777216.0f);
            const uint32_t lo = __float_as_uint(__builtin_fmaf(c, h2f(md & 0xffffu), __uint_as_float(mg0)));
            const uint32_t hi = __float_as_uint(__builtin_fmaf(c, h2f(md >> 16), __uint_as_float(mg1)));
            if (cf == cf) guard |= atomicAdd(&acc[ol], (hi << 16) + lo);
            else guard |= HSLOW;
        });
    }
    GAS unsigned* dw = (GAS unsigned*)(meta + M_DONE + j * 64);
    volatile LDS unsigned* seen = (volatile LDS unsigned*)(uintptr_t)(A_FLAG + 4);
    if (tid == NT - 64) *seen = __hip_atomic_load(dw, __ATOMIC_RELAXED, __HIP_MEMORY_SCOPE_AGENT) & 0xff00ffu;
    WAIT_L(0);
    asm volatile("v_or3_b32 %0, %0, v110, v111\n\tv_or3_b32 %0, %0, v112, v113\n\tv_or3_b32 %0, %0, v114, v115\n\tv_or3_b32 %0, %0, v116, v117\n\tv_or_b32 %0, v118, %0" : "+v"(guard));
    ST(2)
    __syncthreads();
    RTS(10)
    const __amdgpu_buffer_rsrc_t rsP = __builtin_amdgcn_make_buffer_rsrc((void*)(partial + (size_t)j * 16 * SLOTO), 0, 16 * SLOTO * 4, 0x00020000);
    const bool fin = wv < (SHARE / 4 + 63) / 64;
    u32x4 p[8];
    unsigned have = 0u;
    auto fetch = [&](unsigned v, int sh) __attribute__((always_inline)) {
        const uint32_t po = tid < SHARE / 4 ? (uint32_t)(sh * SHARE + tid * 4) * 4u : 0x7fffff00u;
        const unsigned nw = v & ~have;
#pragma unroll
        for (int r = 0; r < 8; r++)
            if ((nw >> (me ^ r)) & 1u)
                p[r] = __builtin_amdgcn_raw_buffer_load_b128(rsP, po, ((v >> (16 + (me ^ r))) & 1u) ? 16 * SLOTO * 4 : (me ^ r) * SLOTO * 4, AUX_SC1);
        have |= v;
    };
    if (fast) {
        uint32_t big = 0u;
        u32x4 v[7];
#pragma unroll
        for (int k = 0; k < 6; k++) v[k] = *(const LDS u32x4*)(uintptr_t)(A_ACC + (k * NT + tid) * 16);
        v[6] = (u32x4)(BIAS);
        if (wv < 2) v[6] = *(const LDS u32x4*)(uintptr_t)(A_ACC + (6 * NT + tid) * 16);
#pragma unroll
        for (int k = 0; k < 6; k++) __builtin_amdgcn_raw_buffer_store_b128(v[k], rsP, (uint32_t)(tid * 16 + me * SLOTO * 4 + k * NT * 16), 0, AUX_SC1);
        if (wv < 2) __builtin_amdgcn_raw_buffer_store_b128(v[6], rsP, (uint32_t)(tid * 16 + me * SLOTO * 4 + 6 * NT * 16), 0, AUX_SC1);
        __builtin_amdgcn_sched_barrier(0);
        p[0] = *(const LDS u32x4*)(uintptr_t)(A_ACC + (me * SHARE + min(tid, SHARE / 4 - 1) * 4) * 4);
        have = 1u << me;
        if (fin) fetch(__builtin_amdgcn_readfirstlane(*seen), me);
#pragma unroll
        for (int k = 0; k < 28; k++) { guard |= v[k >> 2][k & 3]; big |= v[k >> 2][k & 3] + 0x42004200u; }
        if (guard & HSLOW) s_flag[0] = 1u;
        if (big & 0x7c00u) s_flag[2] = 1u;
        if (big & 0x7c000000u) s_flag[3] = 2u;
        if (fin) factors();
    }
    asm volatile("s_waitcnt vmcnt(0)" ::: "memory");
    __syncthreads();
    RTS(11)
    ST(5)
    const bool slow = (ABL & 120) ? false : !fast || __builtin_amdgcn_readfirstlane(s_flag[0]) != 0u || (need & ~__builtin_amdgcn_readfirstlane(s_flag[2] | s_flag[3])) != 0u;
    if (slow) {
        float* fa = (float*)acc;
        float w[16];
        load_w(wdown, w);
#pragma unroll 1
        for (int comp = 2 * cp; comp < 2 * cp + 2; comp++) {
            __syncthreads();
            for (int i = tid; i < SLOTO; i += NT) fa[i] = 0.f;
            __syncthreads();
            auto add = [&](uint32_t ol, uint32_t i, float cf) __attribute__((always_inline)) {
                uint32_t rr[4];
                md_of(m[i], w, rr);
                const uint32_t rc = comp == 0 ? rr[0] : comp == 1 ? rr[1] : comp == 2 ? rr[2] : rr[3];
                atomicAdd(&fa[ol], cf * bf2f(rc));
            };
#pragma unroll 1
            for (uint32_t q = tid; q < 16u * NWG * ((uint32_t)ks * 8u + 24u); q += NT) {
                const uint32_t per = (uint32_t)ks * 8u + 24u;
                const uint32_t un = q & 3u, hf = (q % per) >> 2, sb = (q / per) & (NWG - 1u), ff = (q / per) / NWG;
                const uint32_t col = POS(ff) * 128u + (uint32_t)(h * 32 + j);
                u32x2 p0, p1;
                if (hf < 2u * (uint32_t)ks) {
                    const char* ln = recs + (size_t)((col >> 7) * SS + (col & 127u) * SC + (hf >> 1) * SK + sb * SR) * 128u + un * 16u + (hf & 1u) * 8u;
                    p0 = *(const u32x2*)ln; p1 = *(const u32x2*)(ln + 64);
                } else {
                    const uint32_t tl = hf - 2u * (uint32_t)ks;
                    const uint8_t* cb = (const uint8_t*)tcnt + (sb * 128u + (uint32_t)(j * 4 + h)) * 16u;
                    const char* ln = recs + (size_t)(TBASE + (tl >> 1) * TPL + col * TST + sb) * 128u + (tl & 1u) * 64u + un * 16u;
                    if (tl < 4u) { if (tl >= (cb[ff] & 3u) * 2u) continue; }
                    else {
                        uint32_t at = (tl - 4u) * 4u + un;
                        if (at >= ((cb[ff] >> 2) & 15u)) continue;
#pragma unroll 1
                        for (uint32_t t = ff & 8u; t < ff; t++) at += (cb[t] >> 2) & 15u;
                        ln = recs + (size_t)(SBASE + (((uint32_t)(h * 32 + j) * 2u + (ff >> 3)) * NWG + sb) * 9u) * 128u + at * 16u;
                    }
                    p0 = *(const u32x2*)ln; p1 = *(const u32x2*)(ln + 8);
                }
                if (h & 2) { const u32x2 t = p0; p0 = p1; p1 = t; }
                const uint32_t ws[4] = {(h & 1) ? p0.y : p0.x, (h & 1) ? p0.x : p0.y, (h & 1) ? p1.y : p1.x, (h & 1) ? p1.x : p1.y};
#pragma unroll
                for (int k = 0; k < 3; k++) {
                    const float c = (float)((ws[3] >> (k * 11)) & 0x3ffu) * (1.0f / 1023.0f);
                    if (!(ws[k] & WNULL)) add(ws[k] >> 16, ((ws[k] & 0x3fffu) << 6) | (ff * 4u + (uint32_t)h), (ws[k] & 0x8000u) ? -c : c);
                }
            }
            if (more) irregular(add);
            __syncthreads();
            for (int i = tid; i < SLOTO / 4; i += NT)
                __builtin_amdgcn_raw_buffer_store_b128(*(const LDS u32x4*)(uintptr_t)(A_ACC + i * 16), rsP, (uint32_t)(((comp & 1) * 8 + me) * SLOTO * 4 + i * 16), 0, AUX_SC1);
        }
        edge_loads(me);
        factors();
#pragma unroll
        for (int k = 0; k < 8; k++) p[k] = (u32x4)(0u);
        have = 0u;
        asm volatile("s_waitcnt vmcnt(0)" ::: "memory");
        __syncthreads();
    }
    if (tid == NT - 64) {
        const unsigned inc = (slow ? 0x10001u : 1u) << me;
        unsigned v = __hip_atomic_fetch_add(dw, inc, __ATOMIC_RELAXED, __HIP_MEMORY_SCOPE_AGENT) + inc;
        if ((v & 0xffu) != 0xffu) {
            unsigned spins = 0;
            do {
                *seen = v & 0xff00ffu;
                __builtin_amdgcn_s_sleep(1);
                v = __hip_atomic_load(dw, __ATOMIC_RELAXED, __HIP_MEMORY_SCOPE_AGENT);
                if (++spins > SPINCAP && (v & 0xffu) != 0xffu) {
                    v = __hip_atomic_fetch_add(dw, 256u << me, __ATOMIC_RELAXED, __HIP_MEMORY_SCOPE_AGENT);
                    if ((v & 0xffu) != 0xffu) v = 0x800000ffu;
                }
            } while ((v & 0xffu) != 0xffu);
            v &= 0x80ff00ffu;
        }
        *seen = v;
    }
    if (!fin) return;
    uint32_t un[4];
#pragma unroll
    for (int c = 0; c < 4; c++) un[c] = __builtin_amdgcn_readfirstlane(s_flag[12 + c]);
    unsigned sv;
    {
        const uintptr_t pb = (uintptr_t)(partial + (size_t)j * 16 * SLOTO);
        const u32x4 rsQ = {(uint32_t)pb, (uint32_t)(pb >> 32) & 0xffffu, 16u * SLOTO * 4u, 0x00020000u};
        const uint32_t po = tid < SHARE / 4 ? (uint32_t)(me * SHARE + tid * 4) * 4u : 0x7fffff00u;
        do {
            sv = __builtin_amdgcn_readfirstlane(*seen);
            uint32_t t0, t1;
#define FET(R) "s_xor_b32 %8, %12, " #R "\n\ts_bitcmp1_b32 %13, %8\n\ts_cbranch_scc0 1f\n\ts_mul_i32 %9, %8, %15\n\ts_bitcmp1_b32 %14, %8\n\t" \
               "s_cselect_b32 %9, %16, %9\n\tbuffer_load_dwordx4 %" #R ", %10, %11, %9 offen sc1\n1:\n\t"
            asm volatile("s_and_b32 %8, %13, 0xff\n\ts_cbranch_scc0 2f\n\ts_nop 2\n\t" FET(0) FET(1) FET(2) FET(3) FET(4) FET(5) FET(6) FET(7) "\n2:\n\ts_sleep 2\n\ts_waitcnt vmcnt(0)"
                         : "+v"(p[0]), "+v"(p[1]), "+v"(p[2]), "+v"(p[3]), "+v"(p[4]), "+v"(p[5]), "+v"(p[6]), "+v"(p[7]), "=&s"(t0), "=&s"(t1)
                         : "v"(po), "s"(rsQ), "s"(me), "s"((int)sv < 0 ? 0u : sv & ~have), "s"(sv >> 16), "n"(SLOTO * 4), "n"(16 * SLOTO * 4) : "memory", "scc");
            have |= sv;
        } while ((sv & 0xffu) != 0xffu);
    }
    if ((int)sv < 0) return;
    RTS(12)
    ST(6)
    const unsigned flt = (sv >> 16) & 0xffu;
    auto finish = [&](int sh) __attribute__((always_inline)) {
        {
            const int ol = sh * SHARE + tid * 4;
            const int e0 = ((ol >> 6) << 11) | (j << 6) | (ol & 63);
            const uint32_t po = tid < SHARE / 4 ? (uint32_t)ol * 4u : 0x7fffff00u;
            float g[4][4];
            u32x4 a[2] = {p[0] + p[2], p[1] + p[3]}, b[2] = {p[4] + p[6], p[5] + p[7]};
            if (cp) { const u32x4 t = a[0], u = b[0]; a[0] = a[1]; b[0] = b[1]; a[1] = t; b[1] = u; }
#pragma unroll
            for (int c = 0; c < 2; c++) {
                const int nb = 0x10000 - 0x4000 * __builtin_popcount(flt & (0x55u << c));
                const float u0 = __uint_as_float(nb ? un[2 * c] : 0u), u1 = __uint_as_float(nb ? un[2 * c + 1] : 0u);
#pragma unroll
                for (int q = 0; q < 4; q++) {
                    g[q][2 * c] = (float)((int)((a[c][q] & 0xffffu) + (b[c][q] & 0xffffu)) - nb) * u0;
                    g[q][2 * c + 1] = (float)((int)((a[c][q] >> 16) + (b[c][q] >> 16)) - nb) * u1;
                }
            }
            if (__builtin_expect(flt != 0u, 0)) {
#pragma unroll
                for (int c = 0; c < 2; c++) {
#pragma unroll 1
                    for (int k = c; k < 8; k += 2) {
                        if (!((flt >> k) & 1u)) continue;
                        const u32x4 f0 = __builtin_amdgcn_raw_buffer_load_b128(rsP, po, k * SLOTO * 4, AUX_SC1);
                        const u32x4 f1 = __builtin_amdgcn_raw_buffer_load_b128(rsP, po, (8 + k) * SLOTO * 4, AUX_SC1);
#pragma unroll
                        for (int q = 0; q < 4; q++) { g[q][2 * c] += __uint_as_float(f0[q]); g[q][2 * c + 1] += __uint_as_float(f1[q]); }
                    }
                }
            }
            float wu[16];
            load_w(wup, wu);
            u32x4 ov[2];
#pragma unroll
            for (int q = 0; q < 4; q++) {
#pragma unroll
                for (int k = 0; k < 4; k++) g[q][k] *= rs[q][k];
                const uint32_t m0 = em[q >> 1][(q & 1) * 2], m1 = em[q >> 1][(q & 1) * 2 + 1];
                const float mm[4] = {bf2f(m0 & 0xffff), bf2f(m0 >> 16), bf2f(m1 & 0xffff), bf2f(m1 >> 16)};
                float o[4];
#pragma unroll
                for (int k = 0; k < 4; k++) o[k] = mm[k] + (g[q][0] * wu[k] + g[q][1] * wu[4 + k] + g[q][2] * wu[8 + k] + g[q][3] * wu[12 + k]) * rg[q][k];
                ov[q >> 1][(q & 1) * 2] = pk_bf16(o[0], o[1]);
                ov[q >> 1][(q & 1) * 2 + 1] = pk_bf16(o[2], o[3]);
            }
            if (tid < SHARE / 4 && e0 < E) { *(u32x4*)(out + e0) = ov[0]; *(u32x4*)(out + e0 + 2) = ov[1]; }
        }
    };
    finish(me);
#pragma unroll 1
    for (unsigned todo = (sv >> 8) & 0xffu; todo; todo &= todo - 1u) {
        const int sh = __builtin_ctz(todo);
        edge_loads(sh);
        factors();
        have = 0u;
        fetch(sv, sh);
        finish(sh);
    }
    RTS(13)
    ST(7)
    ST_OUT(8)
}

__global__ __launch_bounds__(256) void gen_scatter_kernel(const int* __restrict__ iin, const int* __restrict__ iout,
                                                          const uint16_t* __restrict__ ang, const uint16_t* __restrict__ freq,
                                                          const u32x2* __restrict__ m, const uint16_t* __restrict__ wdown,
                                                          float* __restrict__ agg, int T) {
    float w[16];
    load_w(wdown, w);
    const float fr = bf2f(freq[0]);
    for (long t = (long)blockIdx.x * 256 + threadIdx.x; t < T; t += (long)gridDim.x * 256) {
        const float c = cosf(fr * bf2f(ang[t]));
        uint32_t r[4];
        md_of(m[iin[t]], w, r);
        float* q = agg + (size_t)iout[t] * 4;
#pragma unroll
        for (int k = 0; k < 4; k++) atomicAdd(q + k, c * bf2f(r[k]));
    }
}
__global__ __launch_bounds__(256) void gen_final_kernel(const u32x2* __restrict__ m, const uint16_t* __restrict__ dist,
                                                        const uint16_t* __restrict__ centers, const uint16_t* __restrict__ wrbf,
                                                        const uint16_t* __restrict__ wsbf, const uint16_t* __restrict__ wup,
                                                        const float* __restrict__ agg, u32x2* __restrict__ out, int E) {
    const int e = blockIdx.x * 256 + threadIdx.x;
    if (e >= E) return;
    const float d = bf2f(dist[e]);
    float rs[4] = {0, 0, 0, 0}, rg[4] = {0, 0, 0, 0};
#pragma unroll 1
    for (int r = 0; r < 8; r++) {
        const float x = d - bf2f(centers[r]);
        const float rb = __expf(-10.0f * x * x);
#pragma unroll
        for (int k = 0; k < 4; k++) { rs[k] += rb * bf2f(wsbf[r * 4 + k]); rg[k] += rb * bf2f(wrbf[r * 4 + k]); }
    }
    float wu[16];
    load_w(wup, wu);
    const u32x2 mv = m[e];
    const float mm[4] = {bf2f(mv.x & 0xffff), bf2f(mv.x >> 16), bf2f(mv.y & 0xffff), bf2f(mv.y >> 16)};
    float g[4], o[4];
#pragma unroll
    for (int k = 0; k < 4; k++) g[k] = agg[(size_t)e * 4 + k] * rs[k];
#pragma unroll
    for (int k = 0; k < 4; k++) o[k] = mm[k] + (g[0] * wu[k] + g[1] * wu[4 + k] + g[2] * wu[8 + k] + g[3] * wu[12 + k]) * rg[k];
    const u32x2 ov = {f2bf(o[0]) | (f2bf(o[1]) << 16), f2bf(o[2]) | (f2bf(o[3]) << 16)};
    out[e] = ov;
}

extern "C" void kernel_launch(void* const* d_in, const int* in_sizes, int n_in,
                              void* d_out, int out_size, void* d_ws, size_t ws_size,
                              hipStream_t stream) {
    const u32x2* m = (const u32x2*)d_in[0];
    const int* iin = (const int*)d_in[1];
    const int* iout = (const int*)d_in[2];
    const uint16_t* dist = (const uint16_t*)d_in[3];
    const uint16_t* ang = (const uint16_t*)d_in[4];
    const uint16_t* centers = (const uint16_t*)d_in[5];
    const uint16_t* freq = (const uint16_t*)d_in[6];
    const uint16_t* wrbf = (const uint16_t*)d_in[7];
    const uint16_t* wsbf = (const uint16_t*)d_in[8];
    const uint16_t* wdown = (const uint16_t*)d_in[9];
    const uint16_t* wup = (const uint16_t*)d_in[10];
    const int E = in_sizes[3];
    const int T = in_sizes[1];
    char* ws = (char*)d_ws;
    const int G = T >> 2;
    const int ntile = (G + GR - 1) / GR;
    const int nstat = ntile / NWG - NDYN > 4 ? ntile / NWG - NDYN : 4;
    const int lcap = G / 6 + 2;
    unsigned* meta = (unsigned*)ws;
    uint32_t* tcnt = (uint32_t*)(ws + W_TCNT);
    u32x2* stat = (u32x2*)(ws + W_STAT);
    uint32_t* mdc = (uint32_t*)(ws + W_MDC);
    uint32_t* partial = (uint32_t*)(ws + W_PART);
    char* recs = ws + W_RECS;
    size_t off = W_RECS;
    const size_t rbytes = (size_t)LBASE * 128 + (size_t)lcap * 130;
    off += rbytes;
    if (E > EMAX || (E & 3) || E < 1 || rbytes > 0x70000000u || off > ws_size) {
        (void)hipMemsetAsync(ws, 0, (size_t)E * 16, stream);
        gen_scatter_kernel<<<4096, 256, 0, stream>>>(iin, iout, ang, freq, m, wdown, (float*)ws, T);
        gen_final_kernel<<<(E + 255) / 256, 256, 0, stream>>>(m, dist, centers, wrbf, wsbf, wup, (const float*)ws, (u32x2*)d_out, E);
        return;
    }
    int ks = T / (NWG * NB * 24);
    if (ks > KMAX) ks = KMAX;
    const float zf = ZSIG * ZSIG * (float)T / (8.0f * (float)E * (float)E);
    (void)hipFuncSetAttribute((const void*)part_kernel, hipFuncAttributeMaxDynamicSharedMemorySize, P_LDS);
    (void)hipFuncSetAttribute((const void*)acc_kernel, hipFuncAttributeMaxDynamicSharedMemorySize, A_LDS);
    part_kernel<<<NWG, NT, P_LDS, stream>>>(iin, iout, ang, freq, m, wdown, recs, T, E, ntile, nstat, ks, lcap);
    if (!(ABL & 7)) acc_kernel<<<NWG, NT, A_LDS, stream>>>(recs, (const u32x4*)tcnt, mdc, (const u32x4*)stat, iin, iout, ang, m, wdown, freq, dist, centers, wrbf, wsbf, wup,
                                           partial, meta, (u32x2*)d_out, T, E, ks, lcap, zf);
}
```

```cpp
#include <hip/hip_runtime.h>
#include <hip/hip_bf16.h>
#include <stdint.h>
#pragma clang diagnostic ignored "-Winline-asm"

typedef unsigned int u32x4 __attribute__((ext_vector_type(4)));
typedef unsigned int u32x2 __attribute__((ext_vector_type(2)));
typedef unsigned short u16x2 __attribute__((ext_vector_type(2)));
typedef _Float16 f16x2 __attribute__((ext_vector_type(2)));
typedef __bf16 bf16x2 __attribute__((ext_vector_type(2)));
typedef float f32x2 __attribute__((ext_vector_type(2)));
typedef unsigned long long u64;

static __device__ __forceinline__ float bf2f(uint32_t bits16) { return __uint_as_float(bits16 << 16); }
static __device__ __forceinline__ uint32_t f2bf(float f) {
    uint32_t u = __float_as_uint(f);
    if ((u & 0x7fffffffu) > 0x7f800000u) return (u >> 16) | 0x40;
    return (u + 0x7fffu + ((u >> 16) & 1u)) >> 16;
}
static __device__ __forceinline__ float h2f(uint32_t bits16) { return (float)__builtin_bit_cast(_Float16, (unsigned short)bits16); }

#define NB 2048
#define WNULL 0x80000000u
#define CSTEP 0x2aabu
#define CINIT 205u
#define CHV (12u * CSTEP)
#define SLOTO 25088
#define SLOTI 12544
#define EMAX (SLOTO * 32)
#define NT 1024
#define NWG 256
#define KMAX 4
#define NTAIL 4
#define LAY 3
#define SC 257u
#define SR 1u
#define SK (128u * SC)
#define SS (4u * SK)
#define MAINL (16u * SS)
#define UINC (0x80000001u + SK * 64u)
#define TST 257u
#define TBASE MAINL
#define TPL (NB * TST)
#define SBASE (TBASE + 2u * TPL)
#define LBASE (SBASE + 128u * 2u * NWG * 9u)
#define NDYN 3
#define GR 2048
#define NTS 5
#define M_DONE 0
#define M_TILE (32 * 64)
#define M_LOG (M_TILE + 64)
#define M_MD (M_LOG + 64)
#define M_BEAT (M_MD + 64)
#define M_E0 (M_BEAT + 64)
#define M_WORDS (M_E0 + 64)
#define W_TCNT (M_WORDS * 4)
#define W_STAT (W_TCNT + NWG * NB)
#define W_MDC (W_STAT + 4 * NWG * 8)
#define W_PART (W_MDC + 128 * SLOTI * 4)
#define W_RECS (W_PART + 32 * 16 * SLOTO * 4)
#define TSC1 0
#define ALT 0
#define POS(ff) (ALT ? ((ff) * 11) & 15 : (ff))
#define AUX_NT 2
#define AUX_SC1 16

static __device__ __forceinline__ uint32_t row_max(uint32_t x) {
    x = max(x, (uint32_t)__builtin_amdgcn_update_dpp(0, x, 0x111, 0xf, 0xf, false));
    x = max(x, (uint32_t)__builtin_amdgcn_update_dpp(0, x, 0x112, 0xf, 0xf, false));
    x = max(x, (uint32_t)__builtin_amdgcn_update_dpp(0, x, 0x114, 0xf, 0xf, false));
    return max(x, (uint32_t)__builtin_amdgcn_update_dpp(0, x, 0x118, 0xf, 0xf, false));
}
static __device__ __forceinline__ uint32_t wave_max(uint32_t x) {
    x = row_max(x);
    x = max(x, (uint32_t)__builtin_amdgcn_update_dpp(0, x, 0x142, 0xa, 0xf, false));
    x = max(x, (uint32_t)__builtin_amdgcn_update_dpp(0, x, 0x143, 0xc, 0xf, false));
    return __builtin_amdgcn_readlane(x, 63);
}
static __device__ __forceinline__ float row_sum(float x) {
    x += __int_as_float(__builtin_amdgcn_update_dpp(0, __float_as_int(x), 0x111, 0xf, 0xf, false));
    x += __int_as_float(__builtin_amdgcn_update_dpp(0, __float_as_int(x), 0x112, 0xf, 0xf, false));
    x += __int_as_float(__builtin_amdgcn_update_dpp(0, __float_as_int(x), 0x114, 0xf, 0xf, false));
    return x + __int_as_float(__builtin_amdgcn_update_dpp(0, __float_as_int(x), 0x118, 0xf, 0xf, false));
}
static __device__ __forceinline__ float wave_sum(float x) {
    x = row_sum(x);
    x += __int_as_float(__builtin_amdgcn_update_dpp(0, __float_as_int(x), 0x142, 0xa, 0xf, false));
    x += __int_as_float(__builtin_amdgcn_update_dpp(0, __float_as_int(x), 0x143, 0xc, 0xf, false));
    return __int_as_float(__builtin_amdgcn_readlane(__float_as_int(x), 63));
}
static __device__ __forceinline__ uint32_t bfi(uint32_t mask, uint32_t a, uint32_t b) {
    return __builtin_amdgcn_bitop3_b32(mask, a, b, 0xca);
}
static __device__ __forceinline__ void opaque(int& x) { asm volatile("" : "+v"(x)); }
template <uint32_t C> static __device__ __forceinline__ uint32_t vconst() {
    uint32_t r;
    asm volatile("v_mov_b32 %0, %1" : "=v"(r) : "n"(C));
    return r;
}
static __device__ __forceinline__ uint32_t pk_bf16(float a, float b) {
    const f32x2 v = {a, b};
    return __builtin_bit_cast(uint32_t, __builtin_convertvector(v, bf16x2));
}
static __device__ __forceinline__ void pin(uint32_t& x) { asm volatile("" : "+v"(x)); }

#define RTS(k)
#define ST_DECL
#define ST(i)
#define ST_OUT(off)

static __device__ __forceinline__ void md_of(u32x2 v, const float* w, uint32_t* r) {
    float a0 = bf2f(v.x & 0xffff), a1 = bf2f(v.x >> 16), a2 = bf2f(v.y & 0xffff), a3 = bf2f(v.y >> 16);
#pragma unroll
    for (int j = 0; j < 4; j++) r[j] = f2bf(a0 * w[j] + a1 * w[4 + j] + a2 * w[8 + j] + a3 * w[12 + j]);
}
static __device__ __forceinline__ void load_w(const uint16_t* __restrict__ wdown, float* w) {
    u32x4 wv0 = *(const u32x4*)wdown, wv1 = *(const u32x4*)(wdown + 8);
#pragma unroll
    for (int i = 0; i < 4; i++) {
        w[2 * i] = bf2f(wv0[i] & 0xffffu); w[2 * i + 1] = bf2f(wv0[i] >> 16);
        w[8 + 2 * i] = bf2f(wv1[i] & 0xffffu); w[8 + 2 * i + 1] = bf2f(wv1[i] >> 16);
    }
}
#define LDS __attribute__((address_space(3)))
#define P_CNT 0
#define P_FLAG ((NB + 64) * 4)
#define P_LST (P_FLAG + 256)
#define LCAP 32
#define P_TAB (P_LST + 4 * LCAP * 16)
#define P_BUF (P_TAB + NB * 4)
#define P_LDS (P_BUF + NB * 64)
#define PD 2
#define LPOS 0x448763
#define ABL 0
#define PRIO 2
#define SETP(n) __builtin_amdgcn_s_setprio(n)
#define MDL 3
#define RMAXREV 40.0f
#define HB 88
#define HREG(S, D) (HB + ((S) & 3) * 10 + (((S) >> 2) + (D)) * 2)

template <int N> struct IC { static constexpr int value = N; };
struct Raw { u32x4 o[2], i[2]; u32x2 a[2]; };
static __device__ __forceinline__ uint32_t lds_ld(uint32_t a) { return *(const LDS uint32_t*)(uintptr_t)a; }
static __device__ __forceinline__ void lds_st(uint32_t a, uint32_t v) { *(LDS uint32_t*)(uintptr_t)a = v; }
static __device__ __forceinline__ void lds_or(uint32_t a, uint32_t v) {
    __hip_atomic_fetch_or((LDS uint32_t*)(uintptr_t)a, v, __ATOMIC_RELAXED, __HIP_MEMORY_SCOPE_WORKGROUP);
}
static __device__ __forceinline__ uint32_t lds_add(uint32_t a, uint32_t v) {
    return __hip_atomic_fetch_add((LDS uint32_t*)(uintptr_t)a, v, __ATOMIC_RELAXED, __HIP_MEMORY_SCOPE_WORKGROUP);
}
static __device__ __forceinline__ uint32_t slot_w(uint32_t ca, uint32_t cv) {
    return (ca << 4) | __builtin_amdgcn_bitop3_b32(cv >> 11, ca, 0x3cu, 0x28);
}
static __device__ __forceinline__ uint32_t slot_x2(uint32_t aw, uint32_t ca) {
    return __builtin_amdgcn_bitop3_b32(aw, ca, 0xcu, 0x72);
}
static __device__ __forceinline__ uint32_t slot_x(uint32_t aw) { return slot_x2(aw, aw >> 4); }
template <int R> static __device__ __forceinline__ u32x2 hget() {
    u32x2 d;
    asm volatile("v_mov_b64 %0, v[%1:%2]" : "=v"(d) : "n"(R), "n"(R + 1));
    return d;
}
static __device__ __forceinline__ u32x2 hget_at(int r) {
    u32x2 d;
    asm volatile("s_set_gpr_idx_on %2, gpr_idx(SRC0)\n\ts_nop 1\n\tv_mov_b32 %0, v88\n\tv_mov_b32 %1, v89\n\ts_set_gpr_idx_off"
                 : "=&v"(d.x), "=&v"(d.y) : "s"(r));
    return d;
}
static __device__ __forceinline__ void hset_at(int r, u32x2 d) {
    asm volatile("s_set_gpr_idx_on %2, gpr_idx(DST)\n\ts_nop 1\n\tv_mov_b32 v88, %0\n\tv_mov_b32 v89, %1\n\ts_set_gpr_idx_off"
                 :: "v"(d.x), "v"(d.y), "s"(r));
}

template <int S, int D> static __device__ __forceinline__ uint32_t take(uint32_t V, uint32_t u16, uint32_t la, u32x2 nul) {
    const uint32_t a = __builtin_amdgcn_bitop3_b32(V, u16, 15u, 0x54);
    asm volatile("v_mov_b64 v[%4:%4+1], v[%3:%3+1]\n\tv_cmpx_ne_u32 vcc, -16, %0\n\tds_wrxchg_rtn_b64 v[%4:%4+1], %1, %2 offset:%5\n\ts_mov_b64 exec, -1"
                 :: "v"(a), "v"(la), "v"(nul), "n"(HREG(S, D)), "n"(HREG(S, 1 - D)), "n"(S * 256) : "memory", "vcc");
    return a;
}
#define SK0 "s_bitcmp1_b32 %3, %4\n\ts_cbranch_scc0 1f\n\t"
#define SK1 "\n1:"
template <int S, int W> static __device__ __forceinline__ void put(u32x4 rsA, uint32_t ad, uint32_t pm) {
    if (POS(S) >= 16 - NTS) asm volatile(SK0 "s_waitcnt lgkmcnt(%5)\n\tbuffer_store_dwordx4 v[%2:%2+3], %0, %1, 0 offen nt\n\ts_nop 1" SK1 :: "v"(ad), "s"(rsA), "n"(HREG(S, 0)), "s"(pm), "n"(2 * S), "n"(W) : "memory", "scc");
    else asm volatile(SK0 "s_waitcnt lgkmcnt(%5)\n\tbuffer_store_dwordx4 v[%2:%2+3], %0, %1, 0 offen sc1\n\ts_nop 1" SK1 :: "v"(ad), "s"(rsA), "n"(HREG(S, 0)), "s"(pm), "n"(2 * S), "n"(W) : "memory", "scc");
}

__global__ __launch_bounds__(NT) __attribute__((amdgpu_num_vgpr(HB / 2))) void part_kernel(const int* __restrict__ iin, const int* __restrict__ iout,
                                                  const uint16_t* __restrict__ ang, const uint16_t* __restrict__ freq,
                                                  const u32x2* __restrict__ m, const uint16_t* __restrict__ wdown,
                                                  char* __restrict__ recs,
                                                  int T, int E, int ntile, int nstat, int ks, int lcap) {
    ST_DECL
    RTS(0)
    const int tid0 = threadIdx.x;
    const int wv = __builtin_amdgcn_readfirstlane(tid0 >> 6);
    const int G = T >> 2;
    const int pos = (int)((blockIdx.x & 7) << 5) | (int)(blockIdx.x >> 3);
    float frr;
    unsigned* meta = (unsigned*)(recs - W_RECS);
    const uint32_t gbase = (LBASE + (uint32_t)lcap) * 128u;
    const uint32_t rbytes = gbase + (uint32_t)lcap * 2u;
    const __amdgpu_buffer_rsrc_t rsR = __builtin_amdgcn_make_buffer_rsrc(recs, 0, (int)rbytes, 0x00020000);
    const u32x4 rsA = {(uint32_t)(uintptr_t)recs, (uint32_t)((uintptr_t)recs >> 32) & 0xffffu, (ABL & 2) ? 0u : rbytes, 0x00020000u};
    const uint32_t g2 = 2u * (uint32_t)ks;
    const uint32_t spec0 = gbase - 4u;
    auto fixed_tile = [&](int r) __attribute__((always_inline)) -> int { return r * NWG + ((pos + r * 37) & (NWG - 1)); };
    int tl0 = fixed_tile(0), tl1 = fixed_tile(1), tl2;
    uint32_t tk = 0u;
    const __amdgpu_buffer_rsrc_t rsO = __builtin_amdgcn_make_buffer_rsrc((void*)iout, 0, G * 16, 0x00020000);
    const __amdgpu_buffer_rsrc_t rsI = __builtin_amdgcn_make_buffer_rsrc((void*)iin, 0, G * 16, 0x00020000);
    const __amdgpu_buffer_rsrc_t rsG = __builtin_amdgcn_make_buffer_rsrc((void*)ang, 0, G * 8, 0x00020000);
    uint32_t t8 = (uint32_t)tid0 * 8u;
    auto load_one = [&](Raw& r, int tl, int idx) __attribute__((always_inline)) {
        const int j = idx < 4 ? idx >> 1 : idx & 1;
        const uint32_t g0 = (uint32_t)(((ABL & 1) ? tl & 31 : tl) * GR + j * NT);
        __builtin_amdgcn_sched_barrier(0);
        if (idx >= 4) r.a[j] = __builtin_amdgcn_raw_buffer_load_b64(rsG, t8, g0 * 8u, (ABL & 1) ? 0 : AUX_NT);
        else if (idx & 1) r.i[j] = __builtin_amdgcn_raw_buffer_load_b128(rsI, t8 + t8, g0 * 16u, (ABL & 1) ? 0 : AUX_NT);
        else r.o[j] = __builtin_amdgcn_raw_buffer_load_b128(rsO, t8 + t8, g0 * 16u, (ABL & 1) ? 0 : AUX_NT);
        __builtin_amdgcn_sched_barrier(0);
    };
    auto peek = [&](uint32_t at) __attribute__((always_inline)) -> uint32_t {
        return __hip_atomic_load((unsigned*)((char*)meta + at), __ATOMIC_RELAXED, __HIP_MEMORY_SCOPE_AGENT);
    };
    auto ready = [&]() __attribute__((always_inline)) {
        if (blockIdx.x != 0) {
            const uint32_t x = lds_ld(vconst<P_FLAG + 8>());
            while (peek(vconst<M_BEAT * 4>()) == x) __builtin_amdgcn_s_sleep(8);
        }
    };
    auto beat = [&]() __attribute__((always_inline)) {
        (void)__hip_atomic_fetch_add((unsigned*)((char*)meta + vconst<M_BEAT * 4>()), vconst<1>(), __ATOMIC_RELAXED, __HIP_MEMORY_SCOPE_AGENT);
    };
    auto draw = [&](uint32_t at) __attribute__((always_inline)) -> uint32_t {
        return __hip_atomic_fetch_add((unsigned*)((char*)meta + at), vconst<1>(), __ATOMIC_RELAXED, __HIP_MEMORY_SCOPE_AGENT);
    };
    uint32_t la;
    auto tab_at = [&]() __attribute__((always_inline)) -> uint32_t {
        return P_TAB + bfi(0xffu, bfi(0x3cu, t8 >> 2, t8 << 4), t8);
    };
    u32x2 nul;
    uint32_t U[2];
    {
        const uint32_t t = (uint32_t)tid0;
        la = P_BUF + ((t >> 5) << 12) + ((t & 0x18u) << 3) + ((t & 3u) << 4) + ((t & 4u) << 1);
        const uint32_t q = ((t >> 3) ^ (t >> 1 & 2u)) & 3u;
        nul.x = (0x7u >> q) << 31; nul.y = (0xbu >> q) << 31;
        U[0] = (POS((t >> 1) & 15u) * SS + ((t & 1u) * 64u + (t >> 5)) * SC + blockIdx.x * SR) * 128u + (0x80000000u - SK * 64u) + 8u - g2;
        U[1] = U[0] + 32u * SC * 128u;
    }
    auto log_line = [&](bool lg, uint32_t b) __attribute__((always_inline)) -> uint32_t {
        uint32_t lp = 0u;
        if (lg && (t8 & 0x38u) == 0u) {
            ready();
            lp = draw(vconst<M_LOG * 4>());
            __builtin_amdgcn_raw_buffer_store_b16((uint16_t)b, rsR, gbase + lp * 2u, 0, 0);
        }
        return LBASE + (uint32_t)__builtin_amdgcn_ds_bpermute((int)((t8 >> 1) & 0xe0u), (int)lp);
    };
    auto general = [&](int D, bool all, u64 sm) __attribute__((always_inline)) {
        uint32_t todo = __builtin_amdgcn_readfirstlane((uint32_t)sm | (uint32_t)(sm >> 32));
#pragma unroll 1
        while (todo) {
            const uint32_t S = (uint32_t)__builtin_ctz(todo) >> 1;
            todo &= ~(3u << (2u * S));
            pin(t8); pin(la);
            const uint32_t V = lds_ld(P_TAB + (t8 & ~63u) + S * 4u), g = V & 15u;
            const uint32_t col = POS(S) * 128u + ((t8 >> 1) & 0x60u) + (t8 >> 8);
            const bool act = all ? V != 0xffffffffu : (V & 0x80000009u) == 9u;
            const int hr = HREG((int)S, D) - HB;
            uint32_t ln = log_line(act && g > 8u + NTAIL, col);
            if (act) {
                const u32x2 N = *(const LDS u32x2*)(uintptr_t)(la + S * 256u);
                *(LDS u32x2*)(uintptr_t)(la + S * 256u) = nul;
                lds_st(P_TAB + (t8 & ~63u) + S * 4u, vconst<0xffffffffu>());
                if (!(g & 1u)) hset_at(hr, N);
                else {
                    uint32_t a, st;
                    if (g < 8u) { a = (V & ~15u) + ((t8 & 0x38u) << 1); st = 8u; }
                    else {
                        if (g <= 8u + NTAIL) ln = TBASE + ((g - 8u) >> 1) * TPL + col * TST + blockIdx.x;
                        a = ln * 128u + (la & 0x38u); st = 64u;
                    }
                    __builtin_amdgcn_raw_buffer_store_b64(hget_at(hr), rsR, a, 0, AUX_SC1);
                    __builtin_amdgcn_raw_buffer_store_b64(N, rsR, a + st, 0, AUX_SC1);
                }
            }
        }
    };
    unsigned epoch = 0;
    u64 pb2 = 0ull;
    u64 fm = 0ull, sm = 0ull;
    auto look = [&](u32x2 c) __attribute__((always_inline)) {
        uint32_t V[2];
        const bool late = __builtin_amdgcn_ballot_w64(((U[0] | U[1]) & 8u) != 0u) != 0ull;
        epoch++;
        asm volatile("v_mov_b32 %0, -1\n\tv_mov_b32 %1, -1\n\t"
                     "v_cmpx_le_u32 %6, %7, %2\n\tv_mov_b32 %0, %4\n\tv_add_u32 %4, %9, %4\n\tv_subrev_u32 %2, %8, %2\n\ts_mov_b64 exec, -1\n\t"
                     "v_cmpx_le_u32 vcc, %8, %3\n\tv_mov_b32 %1, %5\n\tv_add_u32 %5, %9, %5\n\tv_subrev_u32 %3, %8, %3\n\t"
                     "s_or_b64 %6, %6, exec\n\ts_mov_b64 exec, -1"
                     : "=&v"(V[0]), "=&v"(V[1]), "+v"(c.x), "+v"(c.y), "+v"(U[0]), "+v"(U[1]), "=&s"(fm) : "s"(CHV), "n"(CHV), "n"(UINC) : "vcc", "scc");
        *(LDS u32x2*)(uintptr_t)(P_CNT + t8) = c;
        pin(t8);
        const uint32_t tw = tab_at();
        lds_st(tw, V[0]);
        lds_st(tw + 64u, V[1]);
        if (wv == 1) lds_st(P_CNT + NB * 4 - 256 + (t8 >> 1), vconst<0x40000000u>());
        const u64 pb = __builtin_amdgcn_ballot_w64((int)(V[0] & V[1]) >= 0);
        pb2 = (ABL & 4) ? 0ull : pb;
        sm = 0ull;
        if (__builtin_expect(late, 0)) {
            sm = __builtin_amdgcn_ballot_w64((V[0] & 0x80000009u) == 9u || (V[1] & 0x80000009u) == 9u);
#pragma unroll
            for (int e = 0; e < 2; e++)
                if ((V[e] & 0x8000000fu) == 9u + NTAIL) U[e] -= 2u * UINC;
        }
    };
    u32x4 Va, Vb;

    Raw raw[PD];
    {
        const int tid = tid0;
        asm volatile("" ::: "v88", "v127");
        const uint16_t fq = freq[0];
        if (tid == 128) tk = peek(vconst<M_BEAT * 4>());
        if (blockIdx.x == 0 && wv == 3 && (tid & 63) < M_BEAT / 64) {
            __hip_atomic_store(meta + (tid & 63) * 64, 0u, __ATOMIC_RELAXED, __HIP_MEMORY_SCOPE_AGENT);
            __hip_atomic_store(meta + (tid & 63) * 64 + 1, 0u, __ATOMIC_RELAXED, __HIP_MEMORY_SCOPE_AGENT);
        }
        const __amdgpu_buffer_rsrc_t rsE = __builtin_amdgcn_make_buffer_rsrc((void*)m, 0, E * 8, 0x00020000);
        u32x2 mv[5];
        mv[4] = __builtin_amdgcn_raw_buffer_load_b64(rsE, (t8 & 511u) * (uint32_t)(E >> 6), 0, 0);
#pragma unroll
        for (int q = 0; q < 4; q++)
            mv[q] = __builtin_amdgcn_raw_buffer_load_b64(rsE, t8 & 511u, ((blockIdx.x >> 2) * 256u + (blockIdx.x & 3u) * 16u + (uint32_t)wv + 64u * q) * 512u, AUX_NT);
#pragma unroll
        for (int d = 0; d < MDL; d++) load_one(raw[1], tl0, d);
        const u32x2 ci = {CINIT, CINIT};
        *(LDS u32x2*)(uintptr_t)(P_CNT + tid * 8) = ci;
        if (tid < 64) lds_st(P_CNT + (NB + tid) * 4, 0x40000000u);
        if (tid < 8 && tid != 2) lds_st(P_FLAG + tid * 4, tid == 0 ? 0xffffffffu : 0u);
        if (tid == 128) lds_st(P_FLAG + 8, tk);
        asm("s_nop 0\n\tv_readfirstlane_b32 %0, %1" : "=s"(frr) : "v"(bf2f(fq) * 0.15915494309189535f));
        const uint32_t lane = (uint32_t)tid & 63u;
        int e0[4];
        if (blockIdx.x < SLOTI / 64) {
            float w[16];
            load_w(wdown, w);
            auto dot = [&](u32x2 v, int c) __attribute__((always_inline)) -> f32x2 {
                const f32x2 w0 = {w[c], w[c + 1]}, w1 = {w[4 + c], w[5 + c]}, w2 = {w[8 + c], w[9 + c]}, w3 = {w[12 + c], w[13 + c]};
                return w0 * bf2f(v.x & 0xffffu) + w1 * bf2f(v.x >> 16) + w2 * bf2f(v.y & 0xffffu) + w3 * bf2f(v.y >> 16);
            };
#pragma unroll
            for (int cp = 0; cp < 2; cp++) {
                const f32x2 sd = dot(mv[4], 2 * cp);
#pragma unroll
                for (int e = 0; e < 2; e++) {
                    const int ex = (int)(wave_max(__float_as_uint(sd[e]) & 0x7fffffffu) >> 23);
                    e0[2 * cp + e] = __builtin_amdgcn_readfirstlane(ex == 0 ? 0 : min(max(ex - 129, -100), 100));
                }
                const f32x2 sc = {__uint_as_float((uint32_t)(127 - e0[2 * cp]) << 23), __uint_as_float((uint32_t)(127 - e0[2 * cp + 1]) << 23)};
                u32x4 o;
                u16x2 mx = {0, 0};
                f32x2 sq = {0.f, 0.f};
#pragma unroll
                for (int q = 0; q < 4; q++) {
                    const f32x2 y = dot(mv[q], 2 * cp) * sc;
                    const uint32_t hb = pk_bf16(y.x, y.y);
                    const f32x2 x = {__uint_as_float(hb << 16), __uint_as_float(hb & 0xffff0000u)};
                    o[q] = __builtin_bit_cast(uint32_t, __builtin_amdgcn_cvt_pkrtz(x.x, x.y));
                    mx = __builtin_elementwise_max(mx, __builtin_bit_cast(u16x2, hb & 0x7fff7fffu));
                    sq += x * x;
                }
                *(LDS u32x4*)(uintptr_t)(P_BUF + (cp * 64u + lane) * 272u + wv * 16) = o;
#pragma unroll
                for (int e = 0; e < 2; e++) {
                    const u32x2 st = {wave_max(mx[e]), __float_as_uint(wave_sum(sq[e]))};
                    if (lane == 0u) *(LDS u32x2*)(uintptr_t)(P_LST + ((2 * cp + e) * 16 + wv) * 8) = st;
                }
            }
        }
#pragma unroll
        for (int d = MDL; d < 6; d++) load_one(raw[1], tl0, d);
#pragma unroll
        for (int d = 0; d < 6; d++)
            if (((LPOS >> (4 * d)) & 15) < 4) load_one(raw[0], tl1, d);
        __syncthreads();
        u32x4 mo[2];
#pragma unroll
        for (int cp = 0; cp < 2; cp++) mo[cp] = *(const LDS u32x4*)(uintptr_t)(P_BUF + (cp * 64u + (t8 >> 7)) * 272u + ((t8 + t8) & 0xf0u));
        if (blockIdx.x == 0 && wv == 3) asm volatile("s_waitcnt vmcnt(0)" ::: "memory");
        __syncthreads();
#pragma unroll
        for (int s = 0; s < 16; s++) *(LDS u32x2*)(uintptr_t)(la + s * 256) = nul;
        asm volatile("" : "+v"(raw[1].o[0]), "+v"(raw[1].o[1]), "+v"(raw[1].i[0]), "+v"(raw[1].i[1]), "+v"(raw[1].a[0]), "+v"(raw[1].a[1]));
        if (blockIdx.x < SLOTI / 64) {
            const __amdgpu_buffer_rsrc_t rsM = __builtin_amdgcn_make_buffer_rsrc((void*)(meta + W_MDC / 4), 0, (int)(128u * SLOTI * 4u), 0x00020000);
#pragma unroll
            for (int cp = 0; cp < 2; cp++) {
                const uint32_t row = cp * 64u + (t8 >> 7), c16 = (t8 + t8) & 0xf0u;
                __builtin_amdgcn_raw_buffer_store_b128(mo[cp], rsM, row * (SLOTI * 4u) + c16 + blockIdx.x * 256u, 0, AUX_SC1);
            }
            if (wv == 0) {
                const u32x2 st = *(const LDS u32x2*)(uintptr_t)(P_LST + t8);
                const u32x2 r = {row_max(st.x) | (row_max(st.x ? 0xffffu - st.x : 0u) << 16), __float_as_uint(row_sum(__uint_as_float(st.y)))};
                if ((lane & 15u) == 15u) ((u32x2*)(meta + W_STAT / 4))[(lane >> 4) * NWG + blockIdx.x] = r;
                if (blockIdx.x == 0 && lane < 4u) meta[M_E0 + lane] = (uint32_t)(lane == 0u ? e0[0] : lane == 1u ? e0[1] : lane == 2u ? e0[2] : e0[3]);
            }
        }
    }
    pin(la);
    ST(0)
    RTS(5)

    uint32_t w[8], aw[8], xv[8];
    u64 nm[8];
    auto later2 = [&](int k) __attribute__((always_inline)) {
        uint32_t x0, x1;
        asm volatile("v_lshrrev_b32 %0, 4, %2\n\tv_lshrrev_b32 %1, 4, %5\n\tv_bitop3_b32 %0, %2, %0, 12 bitop3:0x72\n\tv_bitop3_b32 %1, %5, %1, 12 bitop3:0x72\n\t"
                     "s_not_b64 exec, %8\n\tds_write_b32 %2, %3 offset:%10\n\tds_or_b32 %0, %4 offset:%10\n\t"
                     "s_not_b64 exec, %9\n\tds_write_b32 %5, %6 offset:%10\n\tds_or_b32 %1, %7 offset:%10\n\ts_mov_b64 exec, -1"
                     : "=&v"(x0), "=&v"(x1)
                     : "v"(aw[k]), "v"(w[k]), "v"(xv[k]), "v"(aw[k + 1]), "v"(w[k + 1]), "v"(xv[k + 1]), "s"(nm[k]), "s"(nm[k + 1]), "n"(P_BUF) : "memory", "scc");
    };
    auto drain = [&]() __attribute__((always_inline)) {
        if (wv == 1) {
            const uint32_t q = (epoch + 1u) & 3u;
            const uint32_t n = __builtin_amdgcn_readfirstlane(lds_ld(vconst<P_FLAG + 16>() + q * 4u));
            if (__builtin_expect(n != 0u, 0)) {
                pin(t8);
                const uint32_t l = (t8 >> 3) & 63u;
                if (l < min(n, (uint32_t)LCAP)) {
                    const u32x4 e = *(const LDS u32x4*)(uintptr_t)(P_LST + (q * LCAP + l) * 16u);
                    lds_st(P_BUF + e.x, e.y);
                    lds_or(P_BUF + slot_x(e.x), e.z);
                }
                lds_st(vconst<P_FLAG + 16>() + q * 4u, vconst<0>());
            }
        }
    };
    auto again = [&](int D) __attribute__((always_inline)) {
        do {
            general(D, true, fm);
            __syncthreads();
            later2(0); later2(2); later2(4); later2(6);
            drain();
#pragma unroll
            for (int k = 0; k < 8; k++) {
                pin(aw[k]);
                bool due = false;
                if (aw[k] & 0x80000000u) {
                    xv[k] -= CHV;
                    if (xv[k] < CHV) {
                        const uint32_t f = (aw[k] >> 16) & 0x3ffu;
                        aw[k] = slot_w(aw[k] & 0xffffu, xv[k]);
                        xv[k] = f << ((xv[k] >> 8) & 31u);
                        due = true;
                    } else lds_st(vconst<P_FLAG>(), epoch + 1u);
                }
                nm[k] = ~__builtin_amdgcn_ballot_w64(due);
            }
            look(*(const LDS u32x2*)(uintptr_t)(P_CNT + t8));
            asm volatile("s_waitcnt lgkmcnt(0)" ::: "memory");
            __syncthreads();
        } while (__builtin_amdgcn_readfirstlane(lds_ld(vconst<P_FLAG>())) == epoch);
    };
    auto round = [&](auto DD, auto LL, Raw& rc, Raw& rn, int rd) __attribute__((always_inline)) {
        constexpr int D = decltype(DD)::value;
        constexpr bool LAST = decltype(LL)::value == 1, FIRST = decltype(LL)::value == 2;
        const int ng = min(GR, G - tl0 * GR);
        auto hook = [&](int p) __attribute__((always_inline)) {
#pragma unroll
            for (int q = 0; q < 6; q++)
            {
                constexpr int dummy = 0; (void)dummy;
                const int lp = (LPOS >> (4 * q)) & 15;
                if (!LAST && lp < 4 && lp == p) load_one(rc, tl2, q);
                if (!LAST && lp >= 4 && (lp < 8 || lp >= 12 ? lp : lp - 8) == p) load_one(rn, tl1, q);
            }
        };
        uint32_t wn[8], ca[8], px[4], cv[8];
        float mx;
        float cs[2];
        auto convA = [&](int q) __attribute__((always_inline)) {
            if (LAST) return;
            const uint32_t a = rc.a[q >> 1][q & 1];
            const f32x2 av = {__uint_as_float(a << 16), __uint_as_float(a & 0xffff0000u)};
            f32x2 rv;
            asm("v_pk_mul_f32 %0, %1, %2 op_sel_hi:[1,0]" : "=v"(rv) : "v"(av), "s"((u64)__float_as_uint(frr)));
            mx = __builtin_elementwise_maximum(__builtin_elementwise_maximum(__builtin_fabsf(rv.x), __builtin_fabsf(rv.y)), (q & 1) ? mx : 0.f);
            cs[0] = __builtin_amdgcn_cosf(rv.x); cs[1] = __builtin_amdgcn_cosf(rv.y);
            asm volatile("" : "+v"(mx), "+v"(cs[0]), "+v"(cs[1]));
            __builtin_amdgcn_sched_barrier(0);
        };
        auto convB = [&](int k) __attribute__((always_inline)) {
            if (LAST) return;
            const uint32_t o = rc.o[k >> 2][k & 3], i = rc.i[k >> 2][k & 3];
            wn[k] = (__builtin_amdgcn_alignbit(bfi(63u, o, o >> 5), __float_as_uint(cs[k & 1]), 30u) << 14) | (i >> 6);
            ca[k] = (bfi(63u, i, o) << 2) & 0x1ffcu;
            pin(wn[k]); pin(ca[k]);
            __builtin_amdgcn_sched_barrier(0);
        };
        auto convD = [&](int q) __attribute__((always_inline)) {
            if (LAST) return;
            asm volatile("v_cvt_pk_f16_f32 %0, |%1|, |%2|\n\tv_pk_mul_f16 %0, %0, %3 op_sel_hi:[1,0]" : "=v"(px[q]) : "v"(cs[0]), "v"(cs[1]), "s"(0x3ffu));
        };
        uint32_t cst;
        auto check = [&](int j) __attribute__((always_inline)) {
            if (LAST) return;
            if (__builtin_expect((__builtin_amdgcn_ballot_w64(!(mx <= RMAXREV)) | (u64)(uint32_t)(GR - ng)) != 0ull, 0)) {
                pin(t8);
                asm volatile("" : "+v"(rc.a[j]));
                const int lg = j * NT + (int)(t8 >> 3);
#pragma unroll
                for (int k = 0; k < 4; k++) {
                    const uint32_t a = (k & 1) ? (rc.a[j][k >> 1] >> 16) : (rc.a[j][k >> 1] & 0xffffu);
                    const bool nrm = __builtin_fabsf(frr * bf2f(a)) <= RMAXREV;
                    if (!(lg < ng && nrm)) ca[j * 4 + k] = NB * 4u + ((t8 >> 1) & 0xfcu);
                    if (lg < ng && !nrm) {
                        ready();
                        __builtin_amdgcn_raw_buffer_store_b32((uint32_t)((tl0 * GR + lg) * 4 + k), rsR, spec0 - draw(vconst<M_LOG * 4 + 4>()) * 4u, 0, 0);
                    }
                }
            }
            cst = vconst<CSTEP>();
        };
        auto ticket = [&](int k) __attribute__((always_inline)) {
            if (LAST) return;
            cv[k] = lds_add(P_CNT + ca[k], cst);
            __builtin_amdgcn_sched_barrier(0);
        };
        auto insert = [&](int j) __attribute__((always_inline)) {
            const int k0 = 4 * j;
            const bool odd = __builtin_amdgcn_ballot_w64(max(max(cv[k0], cv[k0 + 1]), max(cv[k0 + 2], cv[k0 + 3])) >= 2u * CHV) != 0ull;
#pragma unroll
            for (int k = k0; k < k0 + 4; k += 2) {
                uint32_t x0, x1;
                asm volatile("v_lshrrev_b32 %6, 11, %8\n\tv_lshrrev_b32 %7, 11, %9\n\t"
                             "v_bitop3_b32 %6, %6, %10, 60 bitop3:0x28\n\tv_bitop3_b32 %7, %7, %11, 60 bitop3:0x28\n\t"
                             "v_lshl_or_b32 %0, %10, 4, %6\n\tv_lshl_or_b32 %1, %11, 4, %7\n\t"
                             "v_lshlrev_b32_sdwa %2, %8, %12 dst_sel:DWORD dst_unused:UNUSED_PAD src0_sel:BYTE_1 src1_sel:WORD_0\n\t"
                             "v_lshlrev_b32_sdwa %3, %9, %12 dst_sel:DWORD dst_unused:UNUSED_PAD src0_sel:BYTE_1 src1_sel:WORD_1\n\t"
                             "v_bitop3_b32 %6, %0, %10, 12 bitop3:0x72\n\tv_bitop3_b32 %7, %1, %11, 12 bitop3:0x72\n\t"
                             "v_cmpx_gt_u32 %4, %15, %8\n\tds_write_b32 %0, %13 offset:%16\n\tds_or_b32 %6, %2 offset:%16\n\ts_mov_b64 exec, -1\n\t"
                             "v_cmpx_gt_u32 %5, %15, %9\n\tds_write_b32 %1, %14 offset:%16\n\tds_or_b32 %7, %3 offset:%16\n\ts_mov_b64 exec, -1"
                             : "=&v"(aw[k]), "=&v"(aw[k + 1]), "=&v"(xv[k]), "=&v"(xv[k + 1]), "=&s"(nm[k]), "=&s"(nm[k + 1]), "=&v"(x0), "=&v"(x1)
                             : "v"(cv[k]), "v"(cv[k + 1]), "v"(ca[k]), "v"(ca[k + 1]), "v"(px[k >> 1]), "v"(wn[k]), "v"(wn[k + 1]), "s"(CHV), "n"(P_BUF) : "memory");
                w[k] = wn[k]; w[k + 1] = wn[k + 1];
                if (__builtin_expect(odd, 0)) {
#pragma unroll
                    for (int q = k; q < k + 2; q++) {
                        nm[q] |= __builtin_amdgcn_ballot_w64(cv[q] >= 2u * CHV);
                        if (cv[q] >= 2u * CHV && cv[q] < 0x40000000u) {
                            const uint32_t lq = (epoch + 2u) & 3u;
                            uint32_t p = LCAP;
                            if (cv[q] < 3u * CHV) p = lds_add(vconst<P_FLAG + 16>() + lq * 4u, vconst<1>());
                            if (p < LCAP) {
                                const u32x2 e = {aw[q], w[q]};
                                *(LDS u32x2*)(uintptr_t)(P_LST + (lq * LCAP + p) * 16u) = e;
                                lds_st(P_LST + (lq * LCAP + p) * 16u + 8u, xv[q]);
                            } else {
                                aw[q] = ca[q] | ((q & 1) ? (px[q >> 1] & 0xffff0000u) : (px[q >> 1] << 16)) | 0x80000000u;
                                xv[q] = cv[q] - CHV;
                                lds_st(vconst<P_FLAG>(), epoch);
                            }
                        }
                    }
                }
            }
        };
        constexpr int P0 = D ? 0 : 3, P1 = D ? 1 : 2, P2 = D ? 2 : 1, P3 = D ? 3 : 0;
        const uint32_t fl = lds_ld(vconst<P_FLAG>());
        if (!LAST) asm volatile("" : "+v"(rc.o[0]), "+v"(rc.o[1]), "+v"(rc.i[0]), "+v"(rc.i[1]), "+v"(rc.a[0]), "+v"(rc.a[1]));
        if (!LAST && (rd + 4 >= nstat || tl1 >= ntile) && wv == 2) {
            pin(tk);
            if ((t8 & 511u) == 0u) {
                if (tl1 >= ntile) {
                    if (rd + 3 <= nstat) ready();
                    (void)draw(vconst<M_MD * 4>());
                } else if (rd + 4 == nstat) tk = peek(vconst<M_BEAT * 4>());
                else {
                    if (rd + 3 == nstat) {
                        if (tk == lds_ld(vconst<P_FLAG + 8>())) ready();
                        tk = 0u;
                    }
                    lds_st(vconst<P_FLAG + 4>(), tk);
                    if ((int)tk < ntile - nstat * NWG) tk = draw(vconst<M_TILE * 4>());
                }
            }
        }
        if (!LAST && blockIdx.x == 0 && wv == 3 && (t8 & 511u) == 0u) beat();
        hook(12);
        ST(3)
        pin(t8);
        const uint32_t tr = P_TAB + (t8 & ~63u), u16 = (t8 + t8) & 0x70u;
        convA(0);
        convB(0);
        const bool ag = __builtin_amdgcn_readfirstlane(fl) == epoch;
        if (!FIRST && __builtin_expect(LAST || ag || sm != 0ull, 0)) {
            if (ag) again(D);
            general(D, false, sm);
            Va = *(const LDS u32x4*)(uintptr_t)(P_TAB + (t8 & ~63u) + P0 * 16);
            Vb = *(const LDS u32x4*)(uintptr_t)(P_TAB + (t8 & ~63u) + P1 * 16);
            asm volatile("" : "+v"(Va), "+v"(Vb));
        }
        uint32_t pq = (uint32_t)pb2 | (uint32_t)(pb2 >> 32);
        pq = __builtin_amdgcn_readfirstlane(pq | (pq >> 1));
        uint32_t ad[8];
        if (!FIRST) ad[0] = take<4 * P0 + 0, D>(Va[0], u16, la, nul);
        convB(1);
        if (!FIRST) ad[1] = take<4 * P0 + 1, D>(Va[1], u16, la, nul);
        convD(0);
        convA(1);
        if (!FIRST) ad[2] = take<4 * P0 + 2, D>(Va[2], u16, la, nul);
        convB(2);
        hook(13);
        if (!FIRST) ad[3] = take<4 * P0 + 3, D>(Va[3], u16, la, nul);
        convB(3);
        if (!FIRST) put<4 * P0 + 0, LAST ? 3 : 3>(rsA, ad[0], pq);
        if (!FIRST) ad[4] = take<4 * P1 + 0, D>(Vb[0], u16, la, nul);
        convD(1);
        check(0);
        ticket(0);
        hook(4);
        if (!FIRST) put<4 * P0 + 1, LAST ? 3 : 4>(rsA, ad[1], pq);
        if (!FIRST) ad[5] = take<4 * P1 + 1, D>(Vb[1], u16, la, nul);
        convA(2);
        ticket(1);
        if (!FIRST) put<4 * P0 + 2, LAST ? 3 : 5>(rsA, ad[2], pq);
        if (!FIRST) ad[6] = take<4 * P1 + 2, D>(Vb[2], u16, la, nul);
        convB(4);
        ticket(2);
        if (!FIRST) put<4 * P0 + 3, LAST ? 3 : 6>(rsA, ad[3], pq);
        if (!FIRST) Va = *(const LDS u32x4*)(uintptr_t)(tr + P2 * 16);
        __builtin_amdgcn_sched_barrier(0);
        if (!FIRST) ad[7] = take<4 * P1 + 3, D>(Vb[3], u16, la, nul);
        convB(5);
        ticket(3);
        SETP(2);
        if (!FIRST) put<4 * P1 + 0, LAST ? 4 : 8>(rsA, ad[4], pq);
        if (!FIRST) ad[0] = take<4 * P2 + 0, D>(Va[0], u16, la, nul);
        convD(2);
        convA(3);
        hook(5);
        if (!FIRST) put<4 * P1 + 1, LAST ? 4 : 7>(rsA, ad[5], pq);
        if (!FIRST) ad[1] = take<4 * P2 + 1, D>(Va[1], u16, la, nul);
        convB(6);
        if (!FIRST) put<4 * P1 + 2, LAST ? 4 : 6>(rsA, ad[6], pq);
        if (!FIRST) ad[2] = take<4 * P2 + 2, D>(Va[2], u16, la, nul);
        convB(7);
        if (!FIRST) put<4 * P1 + 3, LAST ? 3 : 4>(rsA, ad[7], pq);
        if (!FIRST) Vb = *(const LDS u32x4*)(uintptr_t)(tr + P3 * 16);
        __builtin_amdgcn_sched_barrier(0);
        if (!FIRST) ad[3] = take<4 * P2 + 3, D>(Va[3], u16, la, nul);
        convD(3);
        check(1);
        ticket(4);
        SETP(1);
        hook(6);
        if (!FIRST) put<4 * P2 + 0, LAST ? 4 : 5>(rsA, ad[0], pq);
        if (!FIRST) ad[4] = take<4 * P3 + 0, D>(Vb[0], u16, la, nul);
        ticket(5);
        if (!FIRST) put<4 * P2 + 1, LAST ? 4 : 6>(rsA, ad[1], pq);
        if (!FIRST) ad[5] = take<4 * P3 + 1, D>(Vb[1], u16, la, nul);
        ticket(6);
        if (!FIRST) put<4 * P2 + 2, LAST ? 4 : 7>(rsA, ad[2], pq);
        if (!FIRST) ad[6] = take<4 * P3 + 2, D>(Vb[2], u16, la, nul);
        ticket(7);
        SETP(0);
        if (!FIRST) put<4 * P2 + 3, LAST ? 3 : 7>(rsA, ad[3], pq);
        if (!FIRST) ad[7] = take<4 * P3 + 3, D>(Vb[3], u16, la, nul);
        hook(7);
        ST(4)
        SETP(PRIO > 1 ? 3 : 0);
        asm volatile("s_waitcnt lgkmcnt(0)" ::: "memory");
        __syncthreads();
        ST(5)
        u32x2 cn;
        uint32_t tv;
        if (!LAST) { cn = *(const LDS u32x2*)(uintptr_t)(P_CNT + t8); tv = lds_ld(vconst<P_FLAG + 4>()); }
        __builtin_amdgcn_sched_barrier(0);
        if (!FIRST) later2(0);
        if (!FIRST) put<4 * P3, 15>(rsA, ad[4], pq);
        if (!FIRST) later2(2);
        if (!FIRST) put<4 * P3 + 1, 15>(rsA, ad[5], pq);
        if (!FIRST) drain();
        if (!LAST) {
            tl2 = rd + 2 < nstat ? fixed_tile(rd + 2) : nstat * NWG + (int)__builtin_amdgcn_readfirstlane(tv);
            look(cn);
        }
        hook(0);
        SETP(PRIO > 1 ? 2 : 0);
        if (!FIRST) later2(4);
        if (!FIRST) put<4 * P3 + 2, 15>(rsA, ad[6], pq);
        if (!FIRST) later2(6);
        if (!FIRST) put<4 * P3 + 3, 15>(rsA, ad[7], pq);
        if (!LAST) {
            hook(1);
            SETP(PRIO > 1 ? 1 : 0);
            insert(0);
            hook(2);
            SETP(0);
            pin(t8);
            Va = *(const LDS u32x4*)(uintptr_t)(P_TAB + (t8 & ~63u) + P3 * 16);
            Vb = *(const LDS u32x4*)(uintptr_t)(P_TAB + (t8 & ~63u) + P2 * 16);
            __builtin_amdgcn_sched_barrier(0);
            insert(1);
            hook(3);
            ST(1)
            SETP(3);
            asm volatile("s_waitcnt lgkmcnt(0)" ::: "memory");
            __syncthreads();
            ST(2)
            tl0 = tl1; tl1 = tl2;
        }
    };
#define HMV(A, B) "v_mov_b64 v[" #A ":" #A "+1], v[" #B ":" #B "+1]\n\t"
#define HMV4(A) HMV(A + 8, A + 6) HMV(A + 6, A + 4) HMV(A + 4, A + 2) HMV(A + 2, A)
    round(IC<1>(), IC<2>(), raw[1], raw[0], 0);
    int rd = 1;
#pragma unroll 1
    for (; tl1 < ntile; rd += 2) {
        round(IC<0>(), IC<0>(), raw[0], raw[1], rd);
        round(IC<1>(), IC<0>(), raw[1], raw[0], rd + 1);
    }
    if (tl0 >= ntile) asm volatile(HMV4(88) HMV4(98) HMV4(108) HMV4(118));
    asm volatile("" : "+s"(tl0));
    if (tl0 < ntile) round(IC<0>(), IC<0>(), raw[0], raw[1], rd);
    RTS(1)
    round(IC<1>(), IC<1>(), raw[1], raw[0], 0);
    if (__builtin_amdgcn_readfirstlane(lds_ld(P_FLAG + 16 + ((epoch + 2u) & 3u) * 4u)) != 0u) {
        asm volatile("s_waitcnt lgkmcnt(0)" ::: "memory");
        __syncthreads();
        look(*(const LDS u32x2*)(uintptr_t)(P_CNT + t8));
        general(0, true, fm);
        __syncthreads();
        drain();
    }
    {
        uint32_t* tcnt = meta + W_TCNT / 4;
        const u32x2 c = *(const LDS u32x2*)(uintptr_t)(P_CNT + t8);
        const uint32_t tw = tab_at();
#pragma unroll
        for (int e = 0; e < 2; e++) {
            const uint32_t g = U[e] & 15u;
            lds_st(tw + e * 64u, g >= 8u ? 0x80000000u : ((U[e] + ((g & 1u) ? 0u : SK * 64u)) & 0x7fffff81u) | (((7u - g) << 1) & 12u));
            *(LDS uint8_t*)(uintptr_t)(P_CNT + (t8 & ~511u) + (((tw - P_TAB) >> 2) & 127u) + e * 16u) =
                (uint8_t)(g < 8u ? 0u : ((g - 8u) >> 1) + (((c[e] + 2u * CSTEP) >> 15) << 2) + (g & 1u) * 0x50u);
        }
        asm volatile("s_waitcnt lgkmcnt(0)" ::: "memory");
        __syncthreads();
        const uint32_t u16 = (t8 + t8) & 0x70u;
        u32x4 En[4];
#pragma unroll
        for (int p = 0; p < 4; p++) En[p] = *(const LDS u32x4*)(uintptr_t)(P_TAB + (t8 & ~63u) + p * 16u);
        const u32x4 cb = *(const LDS u32x4*)(uintptr_t)(P_CNT + (t8 & ~511u) + ((t8 >> 2) & 0x70u));
        u32x2 N[16];
#pragma unroll
        for (int s = 0; s < 16; s++) N[s] = *(const LDS u32x2*)(uintptr_t)(la + s * 256u);
        if (u16 == 0u) *(u32x4*)(tcnt + blockIdx.x * 512u + (t8 >> 4)) = cb;
        const uint32_t sg = P_BUF + ((t8 & ~63u) << 4);
        uint32_t ps;
        auto rest = [&](auto SL) __attribute__((always_inline)) {
            constexpr int s = decltype(SL)::value;
            const uint32_t en = En[s >> 2][s & 3], by = cb[s >> 2] >> ((s & 3) * 8);
            const u32x2 Hr = hget<HREG(s, 0)>();
            const uint32_t at = ps + ((la & 0x38u) ^ ((s & 3) << 4));
            *(LDS u32x2*)(uintptr_t)at = Hr;
            *(LDS u32x2*)(uintptr_t)(at + (by & 0x40u)) = N[s];
            ps += (by & 0x3cu) << 2;
            const u32x2 Hs = (en & 1u) ? Hr : nul;
            const u32x4 d = {Hs.x, Hs.y, N[s].x, N[s].y};
            uint32_t a = __builtin_amdgcn_bitop3_b32(en, u16, 15u, 0x54);
            __builtin_amdgcn_raw_buffer_store_b128(d, rsR, a, 0, POS(s) >= 16 - NTS ? AUX_NT : AUX_SC1);
            if (__builtin_expect(__builtin_amdgcn_ballot_w64((en & 12u) != 0u) != 0ull, 0)) {
                const u32x4 dn = {nul.x, nul.y, nul.x, nul.y};
#pragma unroll 1
                for (uint32_t k = (en >> 2) & 3u; k > 0u; k--) {
                    a += SK * 128u;
                    __builtin_amdgcn_raw_buffer_store_b128(dn, rsR, a, 0, AUX_SC1);
                }
            }
        };
        auto pack = [&](int p) __attribute__((always_inline)) {
            const uint32_t a = (SBASE + ((((t8 >> 1) & 0x60u) + (t8 >> 8)) * 2u + p) * (NWG * 9u) + blockIdx.x * 9u) * 128u + u16;
            asm volatile("" ::: "memory");
#pragma unroll 1
            for (uint32_t q = 0; __builtin_amdgcn_ballot_w64(sg + q < ps) != 0ull; q += 256u) {
                const u32x4 d0 = *(const LDS u32x4*)(uintptr_t)(sg + q + u16), d1 = *(const LDS u32x4*)(uintptr_t)(sg + q + 128u + u16);
                __builtin_amdgcn_raw_buffer_store_b128(d0, rsR, sg + q < ps ? a + q : 0xffffff00u, 0, AUX_SC1);
                __builtin_amdgcn_raw_buffer_store_b128(d1, rsR, sg + q + 128u < ps ? a + q + 128u : 0xffffff00u, 0, AUX_SC1);
            }
            asm volatile("" ::: "memory");
        };
        asm volatile("" : "+v"(N[0]), "+v"(N[1]), "+v"(N[2]), "+v"(N[3]), "+v"(N[4]), "+v"(N[5]), "+v"(N[6]), "+v"(N[7]) :: "memory");
        asm volatile("" : "+v"(N[8]), "+v"(N[9]), "+v"(N[10]), "+v"(N[11]), "+v"(N[12]), "+v"(N[13]), "+v"(N[14]), "+v"(N[15]) :: "memory");
        ps = sg;
        rest(IC<0>()); rest(IC<1>()); rest(IC<2>()); rest(IC<3>()); rest(IC<4>()); rest(IC<5>()); rest(IC<6>()); rest(IC<7>());
        pack(0);
        ps = sg;
        rest(IC<8>()); rest(IC<9>()); rest(IC<10>()); rest(IC<11>()); rest(IC<12>()); rest(IC<13>()); rest(IC<14>()); rest(IC<15>());
        pack(1);
    }
    RTS(2)
    ST(6)
    if (blockIdx.x == 0 && t8 == 1536u) {
        do {
            beat();
            __builtin_amdgcn_s_sleep(8);
        } while (peek(vconst<M_MD * 4>()) < NWG);
    }
    RTS(3)
    RTS(4)
    ST(7)
    ST_OUT(0)
}

#define A_MD 0
#define A_ACC (SLOTI * 4)
#define A_FLAG (A_ACC + SLOTO * 4)
#define A_LDS (A_FLAG + 512)
static_assert(SLOTI * 4 == 3 * NT * 16 + 1024, "md staging assumes three full vectors and 256 dwords");
#define BIAS 0x40004000u
#define NFAST 8
#define NLD 10
#define PRE0 4
#define MDP 0xaaaaa
#define MDQ 0x88888
#define UNM 1
#define APRIO 0x543
#define SHARE (SLOTO / 8)
#define SPINCAP 4096u
#define ZSIG 9.0f
#define HSLOW 0x80008000u
#define GAS __attribute__((address_space(1)))

__global__ __launch_bounds__(NT) __attribute__((amdgpu_num_vgpr(53))) void acc_kernel(const char* __restrict__ recs,
                                                 const u32x4* __restrict__ tcnt,
                                                 const uint32_t* __restrict__ mdc, const u32x4* __restrict__ stat,
                                                 const int* __restrict__ iin, const int* __restrict__ iout, const uint16_t* __restrict__ ang,
                                                 const u32x2* __restrict__ m, const uint16_t* __restrict__ wdown,
                                                 const uint16_t* __restrict__ freq,
                                                 const uint16_t* __restrict__ dist, const uint16_t* __restrict__ centers,
                                                 const uint16_t* __restrict__ wrbf, const uint16_t* __restrict__ wsbf,
                                                 const uint16_t* __restrict__ wup,
                                                 uint32_t* partial, unsigned* meta, u32x2* __restrict__ out, int T, int E, int ks, int lcap, float zf) {
    extern __shared__ __attribute__((aligned(16))) char smem[];
    ST_DECL
    RTS(8)
    unsigned* acc = (unsigned*)(smem + A_ACC);
    unsigned* s_flag = (unsigned*)(smem + A_FLAG);
    const int tid = threadIdx.x;
    const int lane = tid & 63;
    const int wv = __builtin_amdgcn_readfirstlane(tid >> 6);
    const int cp = (blockIdx.x >> 3) & 1;
    const int h = blockIdx.x & 3;
    const int j = ((blockIdx.x >> 2) & 1) * 16 + (blockIdx.x >> 4);
    u32x4 tc;
    const __amdgpu_buffer_rsrc_t rsM = __builtin_amdgcn_make_buffer_rsrc((void*)mdc, 0, (int)(128u * SLOTI * 4u), 0x00020000);

    u32x4 pm[4];
    u32x4 ring[NLD];
    auto load_md = [&](int ff, int pl, int mdp) __attribute__((always_inline)) {
        const uint32_t so = (uint32_t)(cp * 64 + ((ABL & 64) ? 0 : ff * 4) + h) * (SLOTI * 4u);
#pragma unroll
        for (int v = 0; v < 3; v++)
            if (pl < 0 || pl == ((mdp >> (4 * v)) & 15)) pm[v] = __builtin_amdgcn_raw_buffer_load_b128(rsM, (uint32_t)tid * 16u, so + (uint32_t)v * (NT * 16u), 0);
        if ((pl < 0 || pl == (mdp & 15)) && wv == 0) pm[3] = __builtin_amdgcn_raw_buffer_load_b128(rsM, (uint32_t)tid * 16u, so + 3u * NT * 16u, 0);
        __builtin_amdgcn_sched_barrier(0);
    };
    const uint32_t lf = ((uint32_t)(tid & ~31) * 16u + (uint32_t)(tid & 12) * 32u) * SR + (uint32_t)(tid & 16) * 4u + (uint32_t)(tid & 3) * 16u;
    const uint32_t lp = (uint32_t)(tid >> 2) * (128u * SR) + (uint32_t)(tid & 3) * 16u;
    auto load_main = [&](int i, int ff, bool nt) __attribute__((always_inline)) -> u32x4 {
        const uint32_t col = (uint32_t)(POS(ff) * 128 + h * 32 + ((ABL & 8) ? 0 : j));
        if (ABL & 8) nt = false;
        const __amdgpu_buffer_rsrc_t rs = __builtin_amdgcn_make_buffer_rsrc((void*)(recs + ((col >> 7) * SS + (col & 127u) * SC) * 128u), 0, ks * (int)(SK * 128u), 0x00020000);
        const int so = (int)(((i >> 1) * SK + (i & 1) * 128u * SR) * 128u);
        if (nt) return __builtin_amdgcn_raw_buffer_load_b128(rs, lf, so, AUX_NT);
        if (UNM) return __builtin_amdgcn_raw_buffer_load_b128(rs, lp + (uint32_t)(i & 1) * 64u, (int)((i >> 1) * SK * 128u), 0);
        return __builtin_amdgcn_raw_buffer_load_b128(rs, lf, so, 0);
    };
    const __amdgpu_buffer_rsrc_t rsT = __builtin_amdgcn_make_buffer_rsrc((void*)recs, 0, (int)(LBASE * 128u), 0x00020000);
    const uint32_t tof = (uint32_t)(lane & 2) * (TPL * 64u) + (uint32_t)(tid >> 2) * 128u + (uint32_t)(lane & 1) * 64u + 4u;
    const uint32_t sp0 = (SBASE + (uint32_t)(h * 32 + j) * (2u * NWG * 9u) + (uint32_t)(tid >> 2) * 9u) * 128u;
    auto tail_rank = [&](uint32_t c, uint32_t sp, int ff, uint32_t t0, int& dst, uint32_t& en) __attribute__((always_inline)) -> int {
        const int k4 = (int)(((uint32_t)(lane & 3) + t0) * 4u) - (int)((c & 3u) * 8u);
        const int left = (int)((c >> 2) & 15u) - k4;
        const bool has = left > 0;
        en = k4 < 0 ? tof + (TBASE + (uint32_t)(POS(ff) * 128 + h * 32 + j) * TST) * 128u : sp + (uint32_t)(k4 * 16 + min(max(left, 0), 4));
        const u64 mask = __builtin_amdgcn_ballot_w64(has);
        const int n = __builtin_popcountll(mask);
        const int rank = __builtin_amdgcn_mbcnt_hi((uint32_t)(mask >> 32), __builtin_amdgcn_mbcnt_lo((uint32_t)mask, 0u));
        const int r = has ? rank : (n + lane - rank);
        dst = ((r & 15) << 4) | ((r >> 4) << 2);
        return n;
    };
    auto load_tail = [&](uint32_t en, u64& tm) __attribute__((always_inline)) -> u32x4 {
        const bool on = (uint32_t)(lane & 3) < (en & 7u) && !(ABL & 32);
        tm = __builtin_amdgcn_ballot_w64(on);
        return __builtin_amdgcn_raw_buffer_load_b128(rsT, on ? (en & ~15u) + (uint32_t)(lane & 3) * 16u : 0xffffff00u, 0, 0);
    };
    float rs[4][4], rg[4][4];
    u32x4 em[2];
    u32x2 dw2;
    const __amdgpu_buffer_rsrc_t rsE = __builtin_amdgcn_make_buffer_rsrc((void*)m, 0, E * 8, 0x00020000);
    const __amdgpu_buffer_rsrc_t rsD = __builtin_amdgcn_make_buffer_rsrc((void*)dist, 0, E * 2, 0x00020000);
    auto edge_loads = [&](int sh) __attribute__((always_inline)) {
        const int ol = sh * SHARE + tid * 4;
        const uint32_t e0 = (tid < SHARE / 4) ? (uint32_t)(((ol >> 6) << 11) | (j << 6) | (ol & 63)) : 0x0fffff00u;
        em[0] = __builtin_amdgcn_raw_buffer_load_b128(rsE, e0 * 8u, 0, AUX_NT);
        em[1] = __builtin_amdgcn_raw_buffer_load_b128(rsE, e0 * 8u + 16u, 0, AUX_NT);
        dw2 = __builtin_amdgcn_raw_buffer_load_b64(rsD, e0 * 2u, 0, AUX_NT);
    };
    auto factors = [&]() __attribute__((always_inline)) {
        const float sq = 3.7982826f;
        const f32x2 d01 = {bf2f(dw2.x & 0xffffu), bf2f(dw2.x >> 16)}, d23 = {bf2f(dw2.y & 0xffffu), bf2f(dw2.y >> 16)};
        f32x2 a[4][4];
#pragma unroll
        for (int q = 0; q < 16; q++) a[q >> 2][q & 3] = (f32x2)(0.f);
#pragma unroll 2
        for (int r = 0; r < 8; r++) {
            const uint32_t cw = ((const uint32_t*)centers)[r >> 1];
            const float cn = bf2f((r & 1) ? (cw >> 16) : (cw & 0xffffu));
            const u32x2 s2 = ((const u32x2*)wsbf)[r], r2 = ((const u32x2*)wrbf)[r];
            const f32x2 w[4] = {{bf2f(s2.x & 0xffffu), bf2f(s2.x >> 16)}, {bf2f(s2.y & 0xffffu), bf2f(s2.y >> 16)},
                                {bf2f(r2.x & 0xffffu), bf2f(r2.x >> 16)}, {bf2f(r2.y & 0xffffu), bf2f(r2.y >> 16)}};
            const f32x2 y01 = (d01 - cn) * sq, y23 = (d23 - cn) * sq;
            const f32x2 t01 = -y01 * y01, t23 = -y23 * y23;
            const float rb[4] = {__builtin_amdgcn_exp2f(t01.x), __builtin_amdgcn_exp2f(t01.y), __builtin_amdgcn_exp2f(t23.x), __builtin_amdgcn_exp2f(t23.y)};
#pragma unroll
            for (int q = 0; q < 4; q++) {
#pragma unroll
                for (int k = 0; k < 4; k++) a[q][k] = __builtin_elementwise_fma(w[k], (f32x2)(rb[q]), a[q][k]);
            }
        }
#pragma unroll
        for (int q = 0; q < 4; q++) {
#pragma unroll
            for (int k = 0; k < 4; k++) { rs[q][k] = a[q][k >> 1][k & 1]; rg[q][k] = a[q][2 + (k >> 1)][k & 1]; }
        }
    };
#define ADT(V, O) "ds_write_addtid_b32 " V " offset:" #O "\n\t"
    auto store_md = [&]() __attribute__((always_inline)) {
        asm volatile("s_mov_b32 m0, %12\n\ts_nop 0\n\t" ADT("%0", 0) ADT("%1", 256) ADT("%2", 512) ADT("%3", 768) ADT("%4", 16384) ADT("%5", 16640) ADT("%6", 16896) ADT("%7", 17152)
                     ADT("%8", 32768) ADT("%9", 33024) ADT("%10", 33280) ADT("%11", 33536)
                     :: "v"(pm[0][0]), "v"(pm[0][1]), "v"(pm[0][2]), "v"(pm[0][3]), "v"(pm[1][0]), "v"(pm[1][1]), "v"(pm[1][2]), "v"(pm[1][3]),
                        "v"(pm[2][0]), "v"(pm[2][1]), "v"(pm[2][2]), "v"(pm[2][3]), "s"(wv * 1024) : "memory");
        if (wv == 0) asm volatile("s_mov_b32 m0, 0\n\ts_nop 0\n\t" ADT("%0", 49152) ADT("%1", 49408) ADT("%2", 49664) ADT("%3", 49920)
                                  :: "v"(pm[3][0]), "v"(pm[3][1]), "v"(pm[3][2]), "v"(pm[3][3]) : "memory");
    };
    const int me = h * 2 + cp;
    bool fast, more;
    uint32_t need, mg0, mg1;
    uint32_t guard = 0;
    uint32_t sgn = 0x8000u;
    pin(sgn);

#define GAT3(M) { uint32_t a0_, a1_, a2_; asm volatile( \
        "v_lshlrev_b16 %0, 2, %3\n\tv_lshlrev_b16 %1, 2, %4\n\tv_lshlrev_b16 %2, 2, %5\n\t" \
        "ds_read_b32 v[%6], %0\n\tds_read_b32 v[%6+1], %1\n\tds_read_b32 v[%6+2], %2" \
        : "=&v"(a0_), "=&v"(a1_), "=&v"(a2_) : "v"(w0), "v"(w1), "v"(w2), "n"(M) : "memory"); }
#define MIXL " op_sel_hi:[1,1,0]\n\t"
#define MIXH " op_sel:[0,1,0] op_sel_hi:[1,1,0]\n\t"
#define SDW " dst_sel:DWORD dst_unused:UNUSED_PAD src0_sel:DWORD src1_sel:WORD_1\n\t"
#define SCAT(PRE, POST, M) { uint32_t c0_, c1_, c2_, a0_, a1_, a2_; asm volatile( \
        "v_bitop3_b32 %6, %6, v[%15-9], v[%15-8] bitop3:0xfe\n\tv_and_b32 %0, 0x3ff, %7\n\tv_lshrrev_b32 %2, 22, %7\n\tv_or_b32 %6, v[%15-7], %6\n\t" PRE \
        "v_bitop3_b32 %0, %11, %8, %0 bitop3:0xea\n\tv_bitop3_b32 %2, %11, %10, %2 bitop3:0xea\n\tv_bfe_u32 %1, %7, 11, 10\n\t" \
        "v_fma_mix_f32 %3, %0, v[%15], %12" MIXL "v_bitop3_b32 %1, %11, %9, %1 bitop3:0xea\n\tv_fma_mix_f32 %0, %0, v[%15], %13" MIXH \
        "v_fma_mix_f32 %4, %1, v[%15+1], %12" MIXL "v_lshl_add_u32 %0, %0, 16, %3\n\tv_lshlrev_b32_sdwa %3, 2, %8" SDW \
        "v_fma_mix_f32 %1, %1, v[%15+1], %13" MIXH "ds_add_rtn_u32 v[%15-9], %3, %0 offset:%14\n\t" \
        "v_fma_mix_f32 %5, %2, v[%15+2], %12" MIXL "v_lshl_add_u32 %1, %1, 16, %4\n\tv_lshlrev_b32_sdwa %4, 2, %9" SDW \
        "v_fma_mix_f32 %2, %2, v[%15+2], %13" MIXH "ds_add_rtn_u32 v[%15-8], %4, %1 offset:%14\n\t" \
        "v_lshl_add_u32 %2, %2, 16, %5\n\tv_lshlrev_b32_sdwa %5, 2, %10" SDW \
        "ds_add_rtn_u32 v[%15-7], %5, %2 offset:%14" POST \
        : "=&v"(c0_), "=&v"(c1_), "=&v"(c2_), "=&v"(a0_), "=&v"(a1_), "=&v"(a2_), "+v"(guard) \
        : "v"(xw), "v"(w0), "v"(w1), "v"(w2), "v"(sgn), "s"(mg0), "s"(mg1), "n"(A_ACC), "n"(M), "s"(tm) : "memory"); }
    asm volatile("v_mov_b32 v110, 0\n\tv_mov_b32 v111, 0\n\tv_mov_b32 v112, 0\n\tv_mov_b32 v113, 0\n\tv_mov_b32 v114, 0\n\tv_mov_b32 v115, 0\n\t"
                 "v_mov_b32 v116, 0\n\tv_mov_b32 v117, 0\n\tv_mov_b32 v118, 0" ::: "v106", "v127");
    auto unmix = [&](int i) __attribute__((always_inline)) {
        const auto s0 = __builtin_amdgcn_permlane16_swap(ring[i][0], ring[i][2], false, false);
        const auto s1 = __builtin_amdgcn_permlane16_swap(ring[i][1], ring[i][3], false, false);
        ring[i][0] = s0[0]; ring[i][2] = s0[1]; ring[i][1] = s1[0]; ring[i][3] = s1[1];
    };
    auto word = [&](int i, int k, bool pr) __attribute__((always_inline)) -> uint32_t {
        return pr && i < NFAST ? ring[(i & ~1) | (k >> 1)][(i & 1) * 2 + (k & 1)] : ring[i][k];
    };
    auto gather = [&](int R, int i, int p, bool pr) __attribute__((always_inline)) {
        if (i < NFAST && !pr) unmix(i);
        const uint32_t w0 = word(i, 0 ^ R, pr), w1 = word(i, 1 ^ R, pr), w2 = word(i, 2 ^ R, pr);
        if (ABL & 16) { asm volatile("" :: "v"(w0), "v"(w1), "v"(w2)); return; }
        if (p == 0) GAT3(119) else if (p == 1) GAT3(122) else GAT3(125)
    };
    auto scatter = [&](int R, int i, int p, u64 tm, bool pr) __attribute__((always_inline)) {
        const uint32_t xw = word(i, 3 ^ R, pr), w0 = word(i, 0 ^ R, pr), w1 = word(i, 1 ^ R, pr), w2 = word(i, 2 ^ R, pr);
        if (ABL & 16) { asm volatile("" :: "v"(w0), "v"(w1), "v"(w2), "v"(xw)); return; }
        if (i < NFAST) { if (p == 0) SCAT("", "", 119) else if (p == 1) SCAT("", "", 122) else SCAT("", "", 125) }
        else if (p == 0) SCAT("s_mov_b64 exec, %16\n\t", "\n\ts_mov_b64 exec, -1", 119)
        else SCAT("s_mov_b64 exec, %16\n\t", "\n\ts_mov_b64 exec, -1", 125)
    };
#define WAIT_L(N) asm volatile("s_waitcnt lgkmcnt(" #N ")" ::: "memory")
    int ntn;
    u64 tm0 = 0ull, tm1;
    uint32_t ta[2], sp = sp0;
    auto list1 = [&](int ff) __attribute__((always_inline)) {
        int dst;
        uint32_t en;
        ntn = tail_rank(tc.x, sp, ff, 0u, dst, en);
        asm volatile("ds_permute_b32 v106, %0, %1" :: "v"(dst), "v"(en) : "memory");
    };
    auto step = [&](auto RR, auto NN, auto NC, int ff) __attribute__((always_inline)) {
        constexpr int R = decltype(RR)::value;
        constexpr bool nt = decltype(NN)::value == 1, last = decltype(NN)::value == 2, nc = decltype(NC)::value;
        constexpr bool pr = UNM && !nc;
        constexpr int mdp = (nt || nc) ? MDQ : MDP;
        gather(R, 0, 0, pr);
        scatter(R, NFAST, 2, tm0, pr);
        const int ntl = ntn;
        asm volatile("s_nop 1\n\tv_mov_b32_dpp %0, v106 quad_perm:[0,0,0,0] row_mask:0xf bank_mask:0xf\n\tv_mov_b32_dpp %1, v106 quad_perm:[1,1,1,1] row_mask:0xf bank_mask:0xf" : "=v"(ta[0]), "=v"(ta[1]));
        auto side = [&](int pl) __attribute__((always_inline)) {
            if (pl == ((mdp >> 12) & 15)) ring[NFAST] = load_tail(ta[0], tm0);
            if (pl == ((mdp >> 16) & 15)) ring[NFAST + 1] = load_tail(ta[1], tm1);
            if (!last) load_md(ff + 1, pl, mdp);
            else if (pl == (mdp & 15)) edge_loads(me);
            __builtin_amdgcn_sched_barrier(0);
        };
        side(8);
        gather(R, 1, 1, pr);
        side(9);
        const uint32_t tcur = tc.x, spc = sp;
#pragma unroll
        for (int i = 0; i < NFAST - 1; i++) {
            gather(R, i + 2, (i + 2) % 3, pr);
            if (i == 0) side(10);
            if (i < 2) WAIT_L(9); else WAIT_L(12);
            scatter(R, i, i % 3, 0ull, pr);
            if (!last && pr && (i & 1)) ring[i - 1] = load_main(i - 1, ff + 1, nt);
            if (!last && (!pr || (i & 1))) ring[i] = load_main(i, ff + 1, nt);
            side(i);
            if (APRIO && i == (APRIO & 15)) __builtin_amdgcn_s_setprio(2);
            if (APRIO && i == ((APRIO >> 4) & 15)) __builtin_amdgcn_s_setprio(1);
            if (APRIO && i == ((APRIO >> 8) & 15)) __builtin_amdgcn_s_setprio(0);
            if (i == 0 && !last) {
                tc.x = __builtin_amdgcn_alignbit(tc.y, tc.x, 8u); tc.y = __builtin_amdgcn_alignbit(tc.z, tc.y, 8u);
                tc.z = __builtin_amdgcn_alignbit(tc.w, tc.z, 8u); tc.w >>= 8;
                sp = ff == 7 ? sp0 + NWG * 9u * 128u : sp + ((tcur & 0x3cu) << 2);
                list1(ff + 1);
            }
            __builtin_amdgcn_sched_barrier(0);
        }
        if (__builtin_expect(ntl > 16, 0)) {
            gather(R, NFAST + 1, 0, pr);
            WAIT_L(0);
            scatter(R, NFAST + 1, 0, tm1, pr);
        }
        if (__builtin_expect((__builtin_amdgcn_ballot_w64((tcur & 2u) != 0u) | (u64)(ntl > 32 ? 1 : 0)) != 0ull, 0)) {
#pragma unroll 1
            for (int e = 2; e < 6; e++) {
                int dst;
                uint32_t en;
                if (tail_rank(tcur, spc, ff, (uint32_t)e & 4u, dst, en) <= (e & 3) * 16) continue;
                const int cmp = __builtin_amdgcn_ds_permute(dst, (int)en);
                ring[NFAST + 1] = load_tail((uint32_t)__builtin_amdgcn_ds_bpermute(((lane & ~3) | (e & 3)) << 2, cmp), tm1);
                gather(R, NFAST + 1, 0, pr);
                WAIT_L(0);
                scatter(R, NFAST + 1, 0, tm1, pr);
            }
        }
        if (APRIO) __builtin_amdgcn_s_setprio(3);
        WAIT_L(3);
        ST(1)
        if (!last) {
            __builtin_amdgcn_s_barrier();
            ST(3)
            store_md();
        }
        scatter(R, NFAST - 1, 1, 0ull, pr);
        if (!last) {
            if (pr) ring[NFAST - 2] = load_main(NFAST - 2, ff + 1, nt);
            ring[NFAST - 1] = load_main(NFAST - 1, ff + 1, nt);
            WAIT_L(3);
            __builtin_amdgcn_s_barrier();
            ST(4)
        }
    };
    auto steps = [&](auto RR) __attribute__((always_inline)) {
        constexpr int R = decltype(RR)::value;
        tc = tcnt[(wv * 16 + (lane >> 2)) * 128 + j * 4 + h];
        u32x4 sv[2];
        uint32_t e0;
        if (wv < 4) {
            sv[0] = (u32x4)(0u); sv[1] = sv[0];
            if (lane < SLOTI / 256) { sv[0] = stat[tid * 2]; sv[1] = stat[tid * 2 + 1]; }
            e0 = meta[M_E0 + wv];
        }
        const u32x2 extra = *(const u32x2*)(meta + M_LOG);
        load_md(0, -1, 0);
#pragma unroll
        for (int i = 0; i < PRE0; i++) ring[i] = load_main(i, 0, false);
        __builtin_amdgcn_sched_barrier(0);
        {
            const u32x4 bi = {BIAS, BIAS, BIAS, BIAS};
            for (int i = tid; i < SLOTO / 4; i += NT) *(LDS u32x4*)(uintptr_t)(A_ACC + i * 16) = bi;
        }
        asm volatile("" :: "s"(*(const uint32_t*)centers ^ *(const uint32_t*)wsbf ^ *(const uint32_t*)wrbf ^ *(const uint32_t*)wup));
        list1(0);
        if (wv < 4) {
            const uint32_t mxb = wave_max(max(max(sv[0].x & 0xffffu, sv[0].z & 0xffffu), max(sv[1].x & 0xffffu, sv[1].z & 0xffffu)));
            const uint32_t mnb = 0xffffu - (wave_max(max(max(sv[0].x, sv[0].z), max(sv[1].x, sv[1].z))) >> 16);
            const float s2 = wave_sum((__uint_as_float(sv[0].y) + __uint_as_float(sv[0].w)) + (__uint_as_float(sv[1].y) + __uint_as_float(sv[1].w)));
            const float rng = fmaxf(sqrtf(zf * s2), 2.0f * bf2f(mxb)) * 6.103515625e-05f;
            const uint32_t eb = mxb ? (__float_as_uint(rng) + 0x7fffffu) >> 23 : 127u;
            const bool ok = !mxb || (mxb < 0x4700u && eb >= 107u && eb <= 140u && mnb >= ((eb + 8u) << 7));
            if (lane == 0) {
                s_flag[8 + wv] = (eb & 0xffu) | (ok ? 0x100u : 0u) | (mxb ? 0x200u : 0u);
                s_flag[12 + wv] = __float_as_uint(__uint_as_float(((eb + e0) & 0xffu) << 23) * (1024.0f / 1023.0f));
                s_flag[wv] = 0u;
            }
        }
        store_md();
#pragma unroll
        for (int i = PRE0; i < NFAST; i++) ring[i] = load_main(i, 0, false);
        ring[NFAST] = (u32x4)(0u);
        WAIT_L(0);
        __syncthreads();
        const uint32_t q0 = __builtin_amdgcn_readfirstlane(s_flag[8 + 2 * cp]), q1 = __builtin_amdgcn_readfirstlane(s_flag[9 + 2 * cp]);
        fast = (q0 & q1 & 0x100u) != 0u;
        need = (q0 >> 9) | ((q1 >> 9) << 1);
        more = (__builtin_amdgcn_readfirstlane(extra.x | extra.y) | (uint32_t)(T & 3)) != 0u;
        mg0 = (((q0 & 0xffu) + 9u) << 23) | 0x400000u;
        mg1 = ((((q1 & 0xffu) + 9u) << 23) | 0x400000u) + ((0u - (mg0 >> 16)) & 0xffffu);
        if (!fast) return;
        ST(0)
        RTS(9)
        int ff = 0;
#pragma unroll 1
        for (; ff < 15 - NTS; ff++) step(RR, IC<0>(), IC<0>(), ff);
        step(RR, IC<1>(), IC<0>(), ff++);
#pragma unroll 1
        for (; ff < 15; ff++) step(RR, IC<1>(), IC<1>(), ff);
        step(RR, IC<2>(), IC<1>(), 15);
        scatter(R, NFAST, 2, tm0, false);
    };
    auto irregular = [&](auto f) __attribute__((always_inline)) {
        const float fr = bf2f(freq[0]);
        const char* lg = recs + (size_t)LBASE * 128u;
        const uint16_t* tags = (const uint16_t*)(lg + (size_t)lcap * 128u);
        const uint32_t nl = meta[M_LOG], ns = meta[M_LOG + 1];
#pragma unroll 1
        for (uint32_t q = tid; q < nl * 8u; q += NT) {
            const uint32_t b = tags[q >> 3];
            if ((b & 127u) == (uint32_t)(h * 32 + j)) {
                const uint32_t* un = (const uint32_t*)(lg + (size_t)q * 16u);
                const uint32_t xw = un[3 ^ h];
#pragma unroll 1
                for (int k = 0; k < 3; k++) {
                    const uint32_t ww = un[k ^ h];
                    const float c = (float)((xw >> (k * 11)) & 0x3ffu) * (1.0f / 1023.0f);
                    if (!(ww & WNULL)) f(ww >> 16, ((ww & 0x3fffu) << 6) | ((ALT ? ((b >> 7) * 3u) & 15u : b >> 7) << 2) | (uint32_t)h, (ww & 0x8000u) ? -c : c);
                }
            }
        }
#pragma unroll 1
        for (uint32_t q = tid; q < ns + (uint32_t)(T & 3); q += NT) {
            const uint32_t t = q < ns ? ((const uint32_t*)tags)[-1 - (int)q] : (uint32_t)(T & ~3) + (q - ns);
            const uint32_t i = (uint32_t)iin[t], o = (uint32_t)iout[t];
            if (((o >> 6) & 31u) == (uint32_t)j && (i & 3u) == (uint32_t)h) f(((o >> 11) << 6) | (o & 63u), i, cosf(fr * bf2f(ang[t])));
        }
    };

    if (h == 0) steps(IC<0>());
    else if (h == 1) steps(IC<1>());
    else if (h == 2) steps(IC<2>());
    else steps(IC<3>());
    if (fast) {
        if (more) irregular([&](uint32_t ol, uint32_t i, float cf) __attribute__((always_inline)) {
            const uint32_t md = mdc[(cp * 64u + (i & 63u)) * SLOTI + ((i >> 14) << 8) + ((i >> 4) & 0xfcu) + ((i >> 12) & 3u)];
            const float c = cf * (1023.0f / 16777216.0f);
            const uint32_t lo = __float_as_uint(__builtin_fmaf(c, h2f(md & 0xffffu), __uint_as_float(mg0)));
            const uint32_t hi = __float_as_uint(__builtin_fmaf(c, h2f(md >> 16), __uint_as_float(mg1)));
            if (cf == cf) guard |= atomicAdd(&acc[ol], (hi << 16) + lo);
            else guard |= HSLOW;
        });
    }
    GAS unsigned* dw = (GAS unsigned*)(meta + M_DONE + j * 64);
    volatile LDS unsigned* seen = (volatile LDS unsigned*)(uintptr_t)(A_FLAG + 4);
    if (tid == NT - 64) *seen = __hip_atomic_load(dw, __ATOMIC_RELAXED, __HIP_MEMORY_SCOPE_AGENT) & 0xff00ffu;
    WAIT_L(0);
    asm volatile("v_or3_b32 %0, %0, v110, v111\n\tv_or3_b32 %0, %0, v112, v113\n\tv_or3_b32 %0, %0, v114, v115\n\tv_or3_b32 %0, %0, v116, v117\n\tv_or_b32 %0, v118, %0" : "+v"(guard));
    ST(2)
    __syncthreads();
    RTS(10)
    const __amdgpu_buffer_rsrc_t rsP = __builtin_amdgcn_make_buffer_rsrc((void*)(partial + (size_t)j * 16 * SLOTO), 0, 16 * SLOTO * 4, 0x00020000);
    const bool fin = wv < (SHARE / 4 + 63) / 64;
    u32x4 p[8];
    unsigned have = 0u;
    auto fetch = [&](unsigned v, int sh) __attribute__((always_inline)) {
        const uint32_t po = tid < SHARE / 4 ? (uint32_t)(sh * SHARE + tid * 4) * 4u : 0x7fffff00u;
        const unsigned nw = v & ~have;
#pragma unroll
        for (int r = 0; r < 8; r++)
            if ((nw >> (me ^ r)) & 1u)
                p[r] = __builtin_amdgcn_raw_buffer_load_b128(rsP, po, ((v >> (16 + (me ^ r))) & 1u) ? 16 * SLOTO * 4 : (me ^ r) * SLOTO * 4, AUX_SC1);
        have |= v;
    };
    if (fast) {
        uint32_t big = 0u;
        u32x4 v[7];
#pragma unroll
        for (int k = 0; k < 6; k++) v[k] = *(const LDS u32x4*)(uintptr_t)(A_ACC + (k * NT + tid) * 16);
        v[6] = (u32x4)(BIAS);
        if (wv < 2) v[6] = *(const LDS u32x4*)(uintptr_t)(A_ACC + (6 * NT + tid) * 16);
#pragma unroll
        for (int k = 0; k < 6; k++) __builtin_amdgcn_raw_buffer_store_b128(v[k], rsP, (uint32_t)(tid * 16 + me * SLOTO * 4 + k * NT * 16), 0, AUX_SC1);
        if (wv < 2) __builtin_amdgcn_raw_buffer_store_b128(v[6], rsP, (uint32_t)(tid * 16 + me * SLOTO * 4 + 6 * NT * 16), 0, AUX_SC1);
        __builtin_amdgcn_sched_barrier(0);
        p[0] = *(const LDS u32x4*)(uintptr_t)(A_ACC + (me * SHARE + min(tid, SHARE / 4 - 1) * 4) * 4);
        have = 1u << me;
        if (fin) fetch(__builtin_amdgcn_readfirstlane(*seen), me);
#pragma unroll
        for (int k = 0; k < 28; k++) { guard |= v[k >> 2][k & 3]; big |= v[k >> 2][k & 3] + 0x42004200u; }
        if (guard & HSLOW) s_flag[0] = 1u;
        if (big & 0x7c00u) s_flag[2] = 1u;
        if (big & 0x7c000000u) s_flag[3] = 2u;
        if (fin) factors();
    }
    asm volatile("s_waitcnt vmcnt(0)" ::: "memory");
    __syncthreads();
    RTS(11)
    ST(5)
    const bool slow = (ABL & 120) ? false : !fast || __builtin_amdgcn_readfirstlane(s_flag[0]) != 0u || (need & ~__builtin_amdgcn_readfirstlane(s_flag[2] | s_flag[3])) != 0u;
    if (slow) {
        float* fa = (float*)acc;
        float w[16];
        load_w(wdown, w);
#pragma unroll 1
        for (int comp = 2 * cp; comp < 2 * cp + 2; comp++) {
            __syncthreads();
            for (int i = tid; i < SLOTO; i += NT) fa[i] = 0.f;
            __syncthreads();
            auto add = [&](uint32_t ol, uint32_t i, float cf) __attribute__((always_inline)) {
                uint32_t rr[4];
                md_of(m[i], w, rr);
                const uint32_t rc = comp == 0 ? rr[0] : comp == 1 ? rr[1] : comp == 2 ? rr[2] : rr[3];
                atomicAdd(&fa[ol], cf * bf2f(rc));
            };
#pragma unroll 1
            for (uint32_t q = tid; q < 16u * NWG * ((uint32_t)ks * 8u + 24u); q += NT) {
                const uint32_t per = (uint32_t)ks * 8u + 24u;
                const uint32_t un = q & 3u, hf = (q % per) >> 2, sb = (q / per) & (NWG - 1u), ff = (q / per) / NWG;
                const uint32_t col = POS(ff) * 128u + (uint32_t)(h * 32 + j);
                u32x2 p0, p1;
                if (hf < 2u * (uint32_t)ks) {
                    const char* ln = recs + (size_t)((col >> 7) * SS + (col & 127u) * SC + (hf >> 1) * SK + sb * SR) * 128u + un * 16u + (hf & 1u) * 8u;
                    p0 = *(const u32x2*)ln; p1 = *(const u32x2*)(ln + 64);
                } else {
                    const uint32_t tl = hf - 2u * (uint32_t)ks;
                    const uint8_t* cb = (const uint8_t*)tcnt + (sb * 128u + (uint32_t)(j * 4 + h)) * 16u;
                    const char* ln = recs + (size_t)(TBASE + (tl >> 1) * TPL + col * TST + sb) * 128u + (tl & 1u) * 64u + un * 16u;
                    if (tl < 4u) { if (tl >= (cb[ff] & 3u) * 2u) continue; }
                    else {
                        uint32_t at = (tl - 4u) * 4u + un;
                        if (at >= ((cb[ff] >> 2) & 15u)) continue;
#pragma unroll 1
                        for (uint32_t t = ff & 8u; t < ff; t++) at += (cb[t] >> 2) & 15u;
                        ln = recs + (size_t)(SBASE + (((uint32_t)(h * 32 + j) * 2u + (ff >> 3)) * NWG + sb) * 9u) * 128u + at * 16u;
                    }
                    p0 = *(const u32x2*)ln; p1 = *(const u32x2*)(ln + 8);
                }
                if (h & 2) { const u32x2 t = p0; p0 = p1; p1 = t; }
                const uint32_t ws[4] = {(h & 1) ? p0.y : p0.x, (h & 1) ? p0.x : p0.y, (h & 1) ? p1.y : p1.x, (h & 1) ? p1.x : p1.y};
#pragma unroll
                for (int k = 0; k < 3; k++) {
                    const float c = (float)((ws[3] >> (k * 11)) & 0x3ffu) * (1.0f / 1023.0f);
                    if (!(ws[k] & WNULL)) add(ws[k] >> 16, ((ws[k] & 0x3fffu) << 6) | (ff * 4u + (uint32_t)h), (ws[k] & 0x8000u) ? -c : c);
                }
            }
            if (more) irregular(add);
            __syncthreads();
            for (int i = tid; i < SLOTO / 4; i += NT)
                __builtin_amdgcn_raw_buffer_store_b128(*(const LDS u32x4*)(uintptr_t)(A_ACC + i * 16), rsP, (uint32_t)(((comp & 1) * 8 + me) * SLOTO * 4 + i * 16), 0, AUX_SC1);
        }
        edge_loads(me);
        factors();
#pragma unroll
        for (int k = 0; k < 8; k++) p[k] = (u32x4)(0u);
        have = 0u;
        asm volatile("s_waitcnt vmcnt(0)" ::: "memory");
        __syncthreads();
    }
    if (tid == NT - 64) {
        const unsigned inc = (slow ? 0x10001u : 1u) << me;
        unsigned v = __hip_atomic_fetch_add(dw, inc, __ATOMIC_RELAXED, __HIP_MEMORY_SCOPE_AGENT) + inc;
        if ((v & 0xffu) != 0xffu) {
            unsigned spins = 0;
            do {
                *seen = v & 0xff00ffu;
                __builtin_amdgcn_s_sleep(1);
                v = __hip_atomic_load(dw, __ATOMIC_RELAXED, __HIP_MEMORY_SCOPE_AGENT);
                if (++spins > SPINCAP && (v & 0xffu) != 0xffu) {
                    v = __hip_atomic_fetch_add(dw, 256u << me, __ATOMIC_RELAXED, __HIP_MEMORY_SCOPE_AGENT);
                    if ((v & 0xffu) != 0xffu) v = 0x800000ffu;
                }
            } while ((v & 0xffu) != 0xffu);
            v &= 0x80ff00ffu;
        }
        *seen = v;
    }
    if (!fin) return;
    uint32_t un[4];
#pragma unroll
    for (int c = 0; c < 4; c++) un[c] = __builtin_amdgcn_readfirstlane(s_flag[12 + c]);
    unsigned sv;
    {
        const uintptr_t pb = (uintptr_t)(partial + (size_t)j * 16 * SLOTO);
        const u32x4 rsQ = {(uint32_t)pb, (uint32_t)(pb >> 32) & 0xffffu, 16u * SLOTO * 4u, 0x00020000u};
        const uint32_t po = tid < SHARE / 4 ? (uint32_t)(me * SHARE + tid * 4) * 4u : 0x7fffff00u;
        do {
            sv = __builtin_amdgcn_readfirstlane(*seen);
            uint32_t t0, t1;
#define FET(R) "s_xor_b32 %8, %12, " #R "\n\ts_bitcmp1_b32 %13, %8\n\ts_cbranch_scc0 1f\n\ts_mul_i32 %9, %8, %15\n\ts_bitcmp1_b32 %14, %8\n\t" \
               "s_cselect_b32 %9, %16, %9\n\tbuffer_load_dwordx4 %" #R ", %10, %11, %9 offen sc1\n1:\n\t"
            asm volatile("s_and_b32 %8, %13, 0xff\n\ts_cbranch_scc0 2f\n\ts_nop 2\n\t" FET(0) FET(1) FET(2) FET(3) FET(4) FET(5) FET(6) FET(7) "\n2:\n\ts_sleep 2\n\ts_waitcnt vmcnt(0)"
                         : "+v"(p[0]), "+v"(p[1]), "+v"(p[2]), "+v"(p[3]), "+v"(p[4]), "+v"(p[5]), "+v"(p[6]), "+v"(p[7]), "=&s"(t0), "=&s"(t1)
                         : "v"(po), "s"(rsQ), "s"(me), "s"((int)sv < 0 ? 0u : sv & ~have), "s"(sv >> 16), "n"(SLOTO * 4), "n"(16 * SLOTO * 4) : "memory", "scc");
            have |= sv;
        } while ((sv & 0xffu) != 0xffu);
    }
    if ((int)sv < 0) return;
    RTS(12)
    ST(6)
    const unsigned flt = (sv >> 16) & 0xffu;
    auto finish = [&](int sh) __attribute__((always_inline)) {
        {
            const int ol = sh * SHARE + tid * 4;
            const int e0 = ((ol >> 6) << 11) | (j << 6) | (ol & 63);
            const uint32_t po = tid < SHARE / 4 ? (uint32_t)ol * 4u : 0x7fffff00u;
            float g[4][4];
            u32x4 a[2] = {p[0] + p[2], p[1] + p[3]}, b[2] = {p[4] + p[6], p[5] + p[7]};
            if (cp) { const u32x4 t = a[0], u = b[0]; a[0] = a[1]; b[0] = b[1]; a[1] = t; b[1] = u; }
#pragma unroll
            for (int c = 0; c < 2; c++) {
                const int nb = 0x10000 - 0x4000 * __builtin_popcount(flt & (0x55u << c));
                const float u0 = __uint_as_float(nb ? un[2 * c] : 0u), u1 = __uint_as_float(nb ? un[2 * c + 1] : 0u);
#pragma unroll
                for (int q = 0; q < 4; q++) {
                    g[q][2 * c] = (float)((int)((a[c][q] & 0xffffu) + (b[c][q] & 0xffffu)) - nb) * u0;
                    g[q][2 * c + 1] = (float)((int)((a[c][q] >> 16) + (b[c][q] >> 16)) - nb) * u1;
                }
            }
            if (__builtin_expect(flt != 0u, 0)) {
#pragma unroll
                for (int c = 0; c < 2; c++) {
#pragma unroll 1
                    for (int k = c; k < 8; k += 2) {
                        if (!((flt >> k) & 1u)) continue;
                        const u32x4 f0 = __builtin_amdgcn_raw_buffer_load_b128(rsP, po, k * SLOTO * 4, AUX_SC1);
                        const u32x4 f1 = __builtin_amdgcn_raw_buffer_load_b128(rsP, po, (8 + k) * SLOTO * 4, AUX_SC1);
#pragma unroll
                        for (int q = 0; q < 4; q++) { g[q][2 * c] += __uint_as_float(f0[q]); g[q][2 * c + 1] += __uint_as_float(f1[q]); }
                    }
                }
            }
            float wu[16];
            load_w(wup, wu);
            u32x4 ov[2];
#pragma unroll
            for (int q = 0; q < 4; q++) {
#pragma unroll
                for (int k = 0; k < 4; k++) g[q][k] *= rs[q][k];
                const uint32_t m0 = em[q >> 1][(q & 1) * 2], m1 = em[q >> 1][(q & 1) * 2 + 1];
                const float mm[4] = {bf2f(m0 & 0xffff), bf2f(m0 >> 16), bf2f(m1 & 0xffff), bf2f(m1 >> 16)};
                float o[4];
#pragma unroll
                for (int k = 0; k < 4; k++) o[k] = mm[k] + (g[q][0] * wu[k] + g[q][1] * wu[4 + k] + g[q][2] * wu[8 + k] + g[q][3] * wu[12 + k]) * rg[q][k];
                ov[q >> 1][(q & 1) * 2] = pk_bf16(o[0], o[1]);
                ov[q >> 1][(q & 1) * 2 + 1] = pk_bf16(o[2], o[3]);
            }
            if (tid < SHARE / 4 && e0 < E) { *(u32x4*)(out + e0) = ov[0]; *(u32x4*)(out + e0 + 2) = ov[1]; }
        }
    };
    finish(me);
#pragma unroll 1
    for (unsigned todo = (sv >> 8) & 0xffu; todo; todo &= todo - 1u) {
        const int sh = __builtin_ctz(todo);
        edge_loads(sh);
        factors();
        have = 0u;
        fetch(sv, sh);
        finish(sh);
    }
    RTS(13)
    ST(7)
    ST_OUT(8)
}

__global__ __launch_bounds__(256) void gen_scatter_kernel(const int* __restrict__ iin, const int* __restrict__ iout,
                                                          const uint16_t* __restrict__ ang, const uint16_t* __restrict__ freq,
                                                          const u32x2* __restrict__ m, const uint16_t* __restrict__ wdown,
                                                          float* __restrict__ agg, int T) {
    float w[16];
    load_w(wdown, w);
    const float fr = bf2f(freq[0]);
    for (long t = (long)blockIdx.x * 256 + threadIdx.x; t < T; t += (long)gridDim.x * 256) {
        const float c = cosf(fr * bf2f(ang[t]));
        uint32_t r[4];
        md_of(m[iin[t]], w, r);
        float* q = agg + (size_t)iout[t] * 4;
#pragma unroll
        for (int k = 0; k < 4; k++) atomicAdd(q + k, c * bf2f(r[k]));
    }
}
__global__ __launch_bounds__(256) void gen_final_kernel(const u32x2* __restrict__ m, const uint16_t* __restrict__ dist,
                                                        const uint16_t* __restrict__ centers, const uint16_t* __restrict__ wrbf,
                                                        const uint16_t* __restrict__ wsbf, const uint16_t* __restrict__ wup,
                                                        const float* __restrict__ agg, u32x2* __restrict__ out, int E) {
    const int e = blockIdx.x * 256 + threadIdx.x;
    if (e >= E) return;
    const float d = bf2f(dist[e]);
    float rs[4] = {0, 0, 0, 0}, rg[4] = {0, 0, 0, 0};
#pragma unroll 1
    for (int r = 0; r < 8; r++) {
        const float x = d - bf2f(centers[r]);
        const float rb = __expf(-10.0f * x * x);
#pragma unroll
        for (int k = 0; k < 4; k++) { rs[k] += rb * bf2f(wsbf[r * 4 + k]); rg[k] += rb * bf2f(wrbf[r * 4 + k]); }
    }
    float wu[16];
    load_w(wup, wu);
    const u32x2 mv = m[e];
    const float mm[4] = {bf2f(mv.x & 0xffff), bf2f(mv.x >> 16), bf2f(mv.y & 0xffff), bf2f(mv.y >> 16)};
    float g[4], o[4];
#pragma unroll
    for (int k = 0; k < 4; k++) g[k] = agg[(size_t)e * 4 + k] * rs[k];
#pragma unroll
    for (int k = 0; k < 4; k++) o[k] = mm[k] + (g[0] * wu[k] + g[1] * wu[4 + k] + g[2] * wu[8 + k] + g[3] * wu[12 + k]) * rg[k];
    const u32x2 ov = {f2bf(o[0]) | (f2bf(o[1]) << 16), f2bf(o[2]) | (f2bf(o[3]) << 16)};
    out[e] = ov;
}

extern "C" void kernel_launch(void* const* d_in, const int* in_sizes, int n_in,
                              void* d_out, int out_size, void* d_ws, size_t ws_size,
                              hipStream_t stream) {
    const u32x2* m = (const u32x2*)d_in[0];
    const int* iin = (const int*)d_in[1];
    const int* iout = (const int*)d_in[2];
    const uint16_t* dist = (const uint16_t*)d_in[3];
    const uint16_t* ang = (const uint16_t*)d_in[4];
    const uint16_t* centers = (const uint16_t*)d_in[5];
    const uint16_t* freq = (const uint16_t*)d_in[6];
    const uint16_t* wrbf = (const uint16_t*)d_in[7];
    const uint16_t* wsbf = (const uint16_t*)d_in[8];
    const uint16_t* wdown = (const uint16_t*)d_in[9];
    const uint16_t* wup = (const uint16_t*)d_in[10];
    const int E = in_sizes[3];
    const int T = in_sizes[1];
    char* ws = (char*)d_ws;
    const int G = T >> 2;
    const int ntile = (G + GR - 1) / GR;
    const int nstat = ntile / NWG - NDYN > 4 ? ntile / NWG - NDYN : 4;
    const int lcap = G / 6 + 2;
    unsigned* meta = (unsigned*)ws;
    uint32_t* tcnt = (uint32_t*)(ws + W_TCNT);
    u32x2* stat = (u32x2*)(ws + W_STAT);
    uint32_t* mdc = (uint32_t*)(ws + W_MDC);
    uint32_t* partial = (uint32_t*)(ws + W_PART);
    char* recs = ws + W_RECS;
    size_t off = W_RECS;
    const size_t rbytes = (size_t)LBASE * 128 + (size_t)lcap * 130;
    off += rbytes;
    if (E > EMAX || (E & 3) || E < 1 || rbytes > 0x70000000u || off > ws_size) {
        (void)hipMemsetAsync(ws, 0, (size_t)E * 16, stream);
        gen_scatter_kernel<<<4096, 256, 0, stream>>>(iin, iout, ang, freq, m, wdown, (float*)ws, T);
        gen_final_kernel<<<(E + 255) / 256, 256, 0, stream>>>(m, dist, centers, wrbf, wsbf, wup, (const float*)ws, (u32x2*)d_out, E);
        return;
    }
    int ks = T / (NWG * NB * 24);
    if (ks > KMAX) ks = KMAX;
    const float zf = ZSIG * ZSIG * (float)T / (8.0f * (float)E * (float)E);
    (void)hipFuncSetAttribute((const void*)part_kernel, hipFuncAttributeMaxDynamicSharedMemorySize, P_LDS);
    (void)hipFuncSetAttribute((const void*)acc_kernel, hipFuncAttributeMaxDynamicSharedMemorySize, A_LDS);
    part_kernel<<<NWG, NT, P_LDS, stream>>>(iin, iout, ang, freq, m, wdown, recs, T, E, ntile, nstat, ks, lcap);
    if (!(ABL & 7)) acc_kernel<<<NWG, NT, A_LDS, stream>>>(recs, (const u32x4*)tcnt, mdc, (const u32x4*)stat, iin, iout, ang, m, wdown, freq, dist, centers, wrbf, wsbf, wup,
                                           partial, meta, (u32x2*)d_out, T, E, ks, lcap, zf);
}
```

```cpp
#include <hip/hip_runtime.h>
#include <hip/hip_bf16.h>
#include <stdint.h>
#pragma clang diagnostic ignored "-Winline-asm"

typedef unsigned int u32x4 __attribute__((ext_vector_type(4)));
typedef unsigned int u32x2 __attribute__((ext_vector_type(2)));
typedef unsigned short u16x2 __attribute__((ext_vector_type(2)));
typedef _Float16 f16x2 __attribute__((ext_vector_type(2)));
typedef __bf16 bf16x2 __attribute__((ext_vector_type(2)));
typedef float f32x2 __attribute__((ext_vector_type(2)));
typedef unsigned long long u64;

static __device__ __forceinline__ float bf2f(uint32_t bits16) { return __uint_as_float(bits16 << 16); }
static __device__ __forceinline__ uint32_t f2bf(float f) {
    uint32_t u = __float_as_uint(f);
    if ((u & 0x7fffffffu) > 0x7f800000u) return (u >> 16) | 0x40;
    return (u + 0x7fffu + ((u >> 16) & 1u)) >> 16;
}
static __device__ __forceinline__ float h2f(uint32_t bits16) { return (float)__builtin_bit_cast(_Float16, (unsigned short)bits16); }

#define NB 2048
#define WNULL 0x80000000u
#define CSTEP 0x2aabu
#define CINIT 205u
#define CHV (12u * CSTEP)
#define SLOTO 25088
#define SLOTI 12544
#define EMAX (SLOTO * 32)
#define NT 1024
#define NWG 256
#define KMAX 4
#define NTAIL 4
#define LAY 3
#define SC 257u
#define SR 1u
#define SK (128u * SC)
#define SS (4u * SK)
#define MAINL (16u * SS)
#define UINC (0x80000001u + SK * 64u)
#define TST 257u
#define TBASE MAINL
#define TPL (NB * TST)
#define SBASE (TBASE + 2u * TPL)
#define LBASE (SBASE + 128u * 2u * NWG * 9u)
#define NDYN 3
#define GR 2048
#define NTS 4
#define M_DONE 0
#define M_TILE (32 * 64)
#define M_LOG (M_TILE + 64)
#define M_MD (M_LOG + 64)
#define M_BEAT (M_MD + 64)
#define M_E0 (M_BEAT + 64)
#define M_WORDS (M_E0 + 64)
#define W_TCNT (M_WORDS * 4)
#define W_STAT (W_TCNT + NWG * NB)
#define W_MDC (W_STAT + 4 * NWG * 8)
#define W_PART (W_MDC + 128 * SLOTI * 4)
#define W_RECS (W_PART + 32 * 16 * SLOTO * 4)
#define TSC1 0
#define ALT 0
#define POS(ff) (ALT ? ((ff) * 11) & 15 : (ff))
#define AUX_NT 2
#define AUX_SC1 16

static __device__ __forceinline__ uint32_t row_max(uint32_t x) {
    x = max(x, (uint32_t)__builtin_amdgcn_update_dpp(0, x, 0x111, 0xf, 0xf, false));
    x = max(x, (uint32_t)__builtin_amdgcn_update_dpp(0, x, 0x112, 0xf, 0xf, false));
    x = max(x, (uint32_t)__builtin_amdgcn_update_dpp(0, x, 0x114, 0xf, 0xf, false));
    return max(x, (uint32_t)__builtin_amdgcn_update_dpp(0, x, 0x118, 0xf, 0xf, false));
}
static __device__ __forceinline__ uint32_t wave_max(uint32_t x) {
    x = row_max(x);
    x = max(x, (uint32_t)__builtin_amdgcn_update_dpp(0, x, 0x142, 0xa, 0xf, false));
    x = max(x, (uint32_t)__builtin_amdgcn_update_dpp(0, x, 0x143, 0xc, 0xf, false));
    return __builtin_amdgcn_readlane(x, 63);
}
static __device__ __forceinline__ float row_sum(float x) {
    x += __int_as_float(__builtin_amdgcn_update_dpp(0, __float_as_int(x), 0x111, 0xf, 0xf, false));
    x += __int_as_float(__builtin_amdgcn_update_dpp(0, __float_as_int(x), 0x112, 0xf, 0xf, false));
    x += __int_as_float(__builtin_amdgcn_update_dpp(0, __float_as_int(x), 0x114, 0xf, 0xf, false));
    return x + __int_as_float(__builtin_amdgcn_update_dpp(0, __float_as_int(x), 0x118, 0xf, 0xf, false));
}
static __device__ __forceinline__ float wave_sum(float x) {
    x = row_sum(x);
    x += __int_as_float(__builtin_amdgcn_update_dpp(0, __float_as_int(x), 0x142, 0xa, 0xf, false));
    x += __int_as_float(__builtin_amdgcn_update_dpp(0, __float_as_int(x), 0x143, 0xc, 0xf, false));
    return __int_as_float(__builtin_amdgcn_readlane(__float_as_int(x), 63));
}
static __device__ __forceinline__ uint32_t bfi(uint32_t mask, uint32_t a, uint32_t b) {
    return __builtin_amdgcn_bitop3_b32(mask, a, b, 0xca);
}
static __device__ __forceinline__ void opaque(int& x) { asm volatile("" : "+v"(x)); }
template <uint32_t C> static __device__ __forceinline__ uint32_t vconst() {
    uint32_t r;
    asm volatile("v_mov_b32 %0, %1" : "=v"(r) : "n"(C));
    return r;
}
static __device__ __forceinline__ uint32_t pk_bf16(float a, float b) {
    const f32x2 v = {a, b};
    return __builtin_bit_cast(uint32_t, __builtin_convertvector(v, bf16x2));
}
static __device__ __forceinline__ void pin(uint32_t& x) { asm volatile("" : "+v"(x)); }

#define RTS(k)
#define ST_DECL
#define ST(i)
#define ST_OUT(off)

static __device__ __forceinline__ void md_of(u32x2 v, const float* w, uint32_t* r) {
    float a0 = bf2f(v.x & 0xffff), a1 = bf2f(v.x >> 16), a2 = bf2f(v.y & 0xffff), a3 = bf2f(v.y >> 16);
#pragma unroll
    for (int j = 0; j < 4; j++) r[j] = f2bf(a0 * w[j] + a1 * w[4 + j] + a2 * w[8 + j] + a3 * w[12 + j]);
}
static __device__ __forceinline__ void load_w(const uint16_t* __restrict__ wdown, float* w) {
    u32x4 wv0 = *(const u32x4*)wdown, wv1 = *(const u32x4*)(wdown + 8);
#pragma unroll
    for (int i = 0; i < 4; i++) {
        w[2 * i] = bf2f(wv0[i] & 0xffffu); w[2 * i + 1] = bf2f(wv0[i] >> 16);
        w[8 + 2 * i] = bf2f(wv1[i] & 0xffffu); w[8 + 2 * i + 1] = bf2f(wv1[i] >> 16);
    }
}
#define LDS __attribute__((address_space(3)))
#define P_CNT 0
#define P_FLAG ((NB + 64) * 4)
#define P_LST (P_FLAG + 256)
#define LCAP 32
#define P_TAB (P_LST + 4 * LCAP * 16)
#define P_BUF (P_TAB + NB * 4)
#define P_LDS (P_BUF + NB * 64)
#define PD 2
#define LPOS 0x448763
#define ABL 0
#define PRIO 2
#define SETP(n) __builtin_amdgcn_s_setprio(n)
#define MDL 3
#define RMAXREV 40.0f
#define HB 88
#define HREG(S, D) (HB + ((S) & 3) * 10 + (((S) >> 2) + (D)) * 2)

template <int N> struct IC { static constexpr int value = N; };
struct Raw { u32x4 o[2], i[2]; u32x2 a[2]; };
static __device__ __forceinline__ uint32_t lds_ld(uint32_t a) { return *(const LDS uint32_t*)(uintptr_t)a; }
static __device__ __forceinline__ void lds_st(uint32_t a, uint32_t v) { *(LDS uint32_t*)(uintptr_t)a = v; }
static __device__ __forceinline__ void lds_or(uint32_t a, uint32_t v) {
    __hip_atomic_fetch_or((LDS uint32_t*)(uintptr_t)a, v, __ATOMIC_RELAXED, __HIP_MEMORY_SCOPE_WORKGROUP);
}
static __device__ __forceinline__ uint32_t lds_add(uint32_t a, uint32_t v) {
    return __hip_atomic_fetch_add((LDS uint32_t*)(uintptr_t)a, v, __ATOMIC_RELAXED, __HIP_MEMORY_SCOPE_WORKGROUP);
}
static __device__ __forceinline__ uint32_t slot_w(uint32_t ca, uint32_t cv) {
    return (ca << 4) | __builtin_amdgcn_bitop3_b32(cv >> 11, ca, 0x3cu, 0x28);
}
static __device__ __forceinline__ uint32_t slot_x2(uint32_t aw, uint32_t ca) {
    return __builtin_amdgcn_bitop3_b32(aw, ca, 0xcu, 0x72);
}
static __device__ __forceinline__ uint32_t slot_x(uint32_t aw) { return slot_x2(aw, aw >> 4); }
template <int R> static __device__ __forceinline__ u32x2 hget() {
    u32x2 d;
    asm volatile("v_mov_b64 %0, v[%1:%2]" : "=v"(d) : "n"(R), "n"(R + 1));
    return d;
}
static __device__ __forceinline__ u32x2 hget_at(int r) {
    u32x2 d;
    asm volatile("s_set_gpr_idx_on %2, gpr_idx(SRC0)\n\ts_nop 1\n\tv_mov_b32 %0, v88\n\tv_mov_b32 %1, v89\n\ts_set_gpr_idx_off"
                 : "=&v"(d.x), "=&v"(d.y) : "s"(r));
    return d;
}
static __device__ __forceinline__ void hset_at(int r, u32x2 d) {
    asm volatile("s_set_gpr_idx_on %2, gpr_idx(DST)\n\ts_nop 1\n\tv_mov_b32 v88, %0\n\tv_mov_b32 v89, %1\n\ts_set_gpr_idx_off"
                 :: "v"(d.x), "v"(d.y), "s"(r));
}

template <int S, int D> static __device__ __forceinline__ uint32_t take(uint32_t V, uint32_t u16, uint32_t la, u32x2 nul) {
    const uint32_t a = __builtin_amdgcn_bitop3_b32(V, u16, 15u, 0x54);
    asm volatile("v_mov_b64 v[%4:%4+1], v[%3:%3+1]\n\tv_cmpx_ne_u32 vcc, -16, %0\n\tds_wrxchg_rtn_b64 v[%4:%4+1], %1, %2 offset:%5\n\ts_mov_b64 exec, -1"
                 :: "v"(a), "v"(la), "v"(nul), "n"(HREG(S, D)), "n"(HREG(S, 1 - D)), "n"(S * 256) : "memory", "vcc");
    return a;
}
#define SK0 "s_bitcmp1_b32 %3, %4\n\ts_cbranch_scc0 1f\n\t"
#define SK1 "\n1:"
template <int S, int W> static __device__ __forceinline__ void put(u32x4 rsA, uint32_t ad, uint32_t pm) {
    if (POS(S) >= 16 - NTS) asm volatile(SK0 "s_waitcnt lgkmcnt(%5)\n\tbuffer_store_dwordx4 v[%2:%2+3], %0, %1, 0 offen nt\n\ts_nop 1" SK1 :: "v"(ad), "s"(rsA), "n"(HREG(S, 0)), "s"(pm), "n"(2 * S), "n"(W) : "memory", "scc");
    else asm volatile(SK0 "s_waitcnt lgkmcnt(%5)\n\tbuffer_store_dwordx4 v[%2:%2+3], %0, %1, 0 offen sc1\n\ts_nop 1" SK1 :: "v"(ad), "s"(rsA), "n"(HREG(S, 0)), "s"(pm), "n"(2 * S), "n"(W) : "memory", "scc");
}

__global__ __launch_bounds__(NT) __attribute__((amdgpu_num_vgpr(HB / 2))) void part_kernel(const int* __restrict__ iin, const int* __restrict__ iout,
                                                  const uint16_t* __restrict__ ang, const uint16_t* __restrict__ freq,
                                                  const u32x2* __restrict__ m, const uint16_t* __restrict__ wdown,
                                                  char* __restrict__ recs,
                                                  int T, int E, int ntile, int nstat, int ks, int lcap) {
    ST_DECL
    RTS(0)
    const int tid0 = threadIdx.x;
    const int wv = __builtin_amdgcn_readfirstlane(tid0 >> 6);
    const int G = T >> 2;
    const int pos = (int)((blockIdx.x & 7) << 5) | (int)(blockIdx.x >> 3);
    float frr;
    unsigned* meta = (unsigned*)(recs - W_RECS);
    const uint32_t gbase = (LBASE + (uint32_t)lcap) * 128u;
    const uint32_t rbytes = gbase + (uint32_t)lcap * 2u;
    const __amdgpu_buffer_rsrc_t rsR = __builtin_amdgcn_make_buffer_rsrc(recs, 0, (int)rbytes, 0x00020000);
    const u32x4 rsA = {(uint32_t)(uintptr_t)recs, (uint32_t)((uintptr_t)recs >> 32) & 0xffffu, (ABL & 2) ? 0u : rbytes, 0x00020000u};
    const uint32_t g2 = 2u * (uint32_t)ks;
    const uint32_t spec0 = gbase - 4u;
    auto fixed_tile = [&](int r) __attribute__((always_inline)) -> int { return r * NWG + ((pos + r * 37) & (NWG - 1)); };
    int tl0 = fixed_tile(0), tl1 = fixed_tile(1), tl2;
    uint32_t tk = 0u;
    const __amdgpu_buffer_rsrc_t rsO = __builtin_amdgcn_make_buffer_rsrc((void*)iout, 0, G * 16, 0x00020000);
    const __amdgpu_buffer_rsrc_t rsI = __builtin_amdgcn_make_buffer_rsrc((void*)iin, 0, G * 16, 0x00020000);
    const __amdgpu_buffer_rsrc_t rsG = __builtin_amdgcn_make_buffer_rsrc((void*)ang, 0, G * 8, 0x00020000);
    uint32_t t8 = (uint32_t)tid0 * 8u;
    auto load_one = [&](Raw& r, int tl, int idx) __attribute__((always_inline)) {
        const int j = idx < 4 ? idx >> 1 : idx & 1;
        const uint32_t g0 = (uint32_t)(((ABL & 1) ? tl & 31 : tl) * GR + j * NT);
        __builtin_amdgcn_sched_barrier(0);
        if (idx >= 4) r.a[j] = __builtin_amdgcn_raw_buffer_load_b64(rsG, t8, g0 * 8u, (ABL & 1) ? 0 : AUX_NT);
        else if (idx & 1) r.i[j] = __builtin_amdgcn_raw_buffer_load_b128(rsI, t8 + t8, g0 * 16u, (ABL & 1) ? 0 : AUX_NT);
        else r.o[j] = __builtin_amdgcn_raw_buffer_load_b128(rsO, t8 + t8, g0 * 16u, (ABL & 1) ? 0 : AUX_NT);
        __builtin_amdgcn_sched_barrier(0);
    };
    auto peek = [&](uint32_t at) __attribute__((always_inline)) -> uint32_t {
        return __hip_atomic_load((unsigned*)((char*)meta + at), __ATOMIC_RELAXED, __HIP_MEMORY_SCOPE_AGENT);
    };
    auto ready = [&]() __attribute__((always_inline)) {
        if (blockIdx.x != 0) {
            const uint32_t x = lds_ld(vconst<P_FLAG + 8>());
            while (peek(vconst<M_BEAT * 4>()) == x) __builtin_amdgcn_s_sleep(8);
        }
    };
    auto beat = [&]() __attribute__((always_inline)) {
        (void)__hip_atomic_fetch_add((unsigned*)((char*)meta + vconst<M_BEAT * 4>()), vconst<1>(), __ATOMIC_RELAXED, __HIP_MEMORY_SCOPE_AGENT);
    };
    auto draw = [&](uint32_t at) __attribute__((always_inline)) -> uint32_t {
        return __hip_atomic_fetch_add((unsigned*)((char*)meta + at), vconst<1>(), __ATOMIC_RELAXED, __HIP_MEMORY_SCOPE_AGENT);
    };
    uint32_t la;
    auto tab_at = [&]() __attribute__((always_inline)) -> uint32_t {
        return P_TAB + bfi(0xffu, bfi(0x3cu, t8 >> 2, t8 << 4), t8);
    };
    u32x2 nul;
    uint32_t U[2];
    {
        const uint32_t t = (uint32_t)tid0;
        la = P_BUF + ((t >> 5) << 12) + ((t & 0x18u) << 3) + ((t & 3u) << 4) + ((t & 4u) << 1);
        const uint32_t q = ((t >> 3) ^ (t >> 1 & 2u)) & 3u;
        nul.x = (0x7u >> q) << 31; nul.y = (0xbu >> q) << 31;
        U[0] = (POS((t >> 1) & 15u) * SS + ((t & 1u) * 64u + (t >> 5)) * SC + blockIdx.x * SR) * 128u + (0x80000000u - SK * 64u) + 8u - g2;
        U[1] = U[0] + 32u * SC * 128u;
    }
    auto log_line = [&](bool lg, uint32_t b) __attribute__((always_inline)) -> uint32_t {
        uint32_t lp = 0u;
        if (lg && (t8 & 0x38u) == 0u) {
            ready();
            lp = draw(vconst<M_LOG * 4>());
            __builtin_amdgcn_raw_buffer_store_b16((uint16_t)b, rsR, gbase + lp * 2u, 0, 0);
        }
        return LBASE + (uint32_t)__builtin_amdgcn_ds_bpermute((int)((t8 >> 1) & 0xe0u), (int)lp);
    };
    auto general = [&](int D, bool all, u64 sm) __attribute__((always_inline)) {
        uint32_t todo = __builtin_amdgcn_readfirstlane((uint32_t)sm | (uint32_t)(sm >> 32));
#pragma unroll 1
        while (todo) {
            const uint32_t S = (uint32_t)__builtin_ctz(todo) >> 1;
            todo &= ~(3u << (2u * S));
            pin(t8); pin(la);
            const uint32_t V = lds_ld(P_TAB + (t8 & ~63u) + S * 4u), g = V & 15u;
            const uint32_t col = POS(S) * 128u + ((t8 >> 1) & 0x60u) + (t8 >> 8);
            const bool act = all ? V != 0xffffffffu : (V & 0x80000009u) == 9u;
            const int hr = HREG((int)S, D) - HB;
            uint32_t ln = log_line(act && g > 8u + NTAIL, col);
            if (act) {
                const u32x2 N = *(const LDS u32x2*)(uintptr_t)(la + S * 256u);
                *(LDS u32x2*)(uintptr_t)(la + S * 256u) = nul;
                lds_st(P_TAB + (t8 & ~63u) + S * 4u, vconst<0xffffffffu>());
                if (!(g & 1u)) hset_at(hr, N);
                else {
                    uint32_t a, st;
                    if (g < 8u) { a = (V & ~15u) + ((t8 & 0x38u) << 1); st = 8u; }
                    else {
                        if (g <= 8u + NTAIL) ln = TBASE + ((g - 8u) >> 1) * TPL + col * TST + blockIdx.x;
                        a = ln * 128u + (la & 0x38u); st = 64u;
                    }
                    __builtin_amdgcn_raw_buffer_store_b64(hget_at(hr), rsR, a, 0, AUX_SC1);
                    __builtin_amdgcn_raw_buffer_store_b64(N, rsR, a + st, 0, AUX_SC1);
                }
            }
        }
    };
    unsigned epoch = 0;
    u64 pb2 = 0ull;
    u64 fm = 0ull, sm = 0ull;
    auto look = [&](u32x2 c) __attribute__((always_inline)) {
        uint32_t V[2];
        const bool late = __builtin_amdgcn_ballot_w64(((U[0] | U[1]) & 8u) != 0u) != 0ull;
        epoch++;
        asm volatile("v_mov_b32 %0, -1\n\tv_mov_b32 %1, -1\n\t"
                     "v_cmpx_le_u32 %6, %7, %2\n\tv_mov_b32 %0, %4\n\tv_add_u32 %4, %9, %4\n\tv_subrev_u32 %2, %8, %2\n\ts_mov_b64 exec, -1\n\t"
                     "v_cmpx_le_u32 vcc, %8, %3\n\tv_mov_b32 %1, %5\n\tv_add_u32 %5, %9, %5\n\tv_subrev_u32 %3, %8, %3\n\t"
                     "s_or_b64 %6, %6, exec\n\ts_mov_b64 exec, -1"
                     : "=&v"(V[0]), "=&v"(V[1]), "+v"(c.x), "+v"(c.y), "+v"(U[0]), "+v"(U[1]), "=&s"(fm) : "s"(CHV), "n"(CHV), "n"(UINC) : "vcc", "scc");
        *(LDS u32x2*)(uintptr_t)(P_CNT + t8) = c;
        pin(t8);
        const uint32_t tw = tab_at();
        lds_st(tw, V[0]);
        lds_st(tw + 64u, V[1]);
        if (wv == 1) lds_st(P_CNT + NB * 4 - 256 + (t8 >> 1), vconst<0x40000000u>());
        const u64 pb = __builtin_amdgcn_ballot_w64((int)(V[0] & V[1]) >= 0);
        pb2 = (ABL & 4) ? 0ull : pb;
        sm = 0ull;
        if (__builtin_expect(late, 0)) {
            sm = __builtin_amdgcn_ballot_w64((V[0] & 0x80000009u) == 9u || (V[1] & 0x80000009u) == 9u);
#pragma unroll
            for (int e = 0; e < 2; e++)
                if ((V[e] & 0x8000000fu) == 9u + NTAIL) U[e] -= 2u * UINC;
        }
    };
    u32x4 Va, Vb;

    Raw raw[PD];
    {
        const int tid = tid0;
        asm volatile("" ::: "v88", "v127");
        const uint16_t fq = freq[0];
        if (tid == 128) tk = peek(vconst<M_BEAT * 4>());
        if (blockIdx.x == 0 && wv == 3 && (tid & 63) < M_BEAT / 64) {
            __hip_atomic_store(meta + (tid & 63) * 64, 0u, __ATOMIC_RELAXED, __HIP_MEMORY_SCOPE_AGENT);
            __hip_atomic_store(meta + (tid & 63) * 64 + 1, 0u, __ATOMIC_RELAXED, __HIP_MEMORY_SCOPE_AGENT);
        }
        const __amdgpu_buffer_rsrc_t rsE = __builtin_amdgcn_make_buffer_rsrc((void*)m, 0, E * 8, 0x00020000);
        u32x2 mv[5];
        mv[4] = __builtin_amdgcn_raw_buffer_load_b64(rsE, (t8 & 511u) * (uint32_t)(E >> 6), 0, 0);
#pragma unroll
        for (int q = 0; q < 4; q++)
            mv[q] = __builtin_amdgcn_raw_buffer_load_b64(rsE, t8 & 511u, ((blockIdx.x >> 2) * 256u + (blockIdx.x & 3u) * 16u + (uint32_t)wv + 64u * q) * 512u, AUX_NT);
#pragma unroll
        for (int d = 0; d < MDL; d++) load_one(raw[1], tl0, d);
        const u32x2 ci = {CINIT, CINIT};
        *(LDS u32x2*)(uintptr_t)(P_CNT + tid * 8) = ci;
        if (tid < 64) lds_st(P_CNT + (NB + tid) * 4, 0x40000000u);
        if (tid < 8 && tid != 2) lds_st(P_FLAG + tid * 4, tid == 0 ? 0xffffffffu : 0u);
        if (tid == 128) lds_st(P_FLAG + 8, tk);
        asm("s_nop 0\n\tv_readfirstlane_b32 %0, %1" : "=s"(frr) : "v"(bf2f(fq) * 0.15915494309189535f));
        const uint32_t lane = (uint32_t)tid & 63u;
        int e0[4];
        if (blockIdx.x < SLOTI / 64) {
            float w[16];
            load_w(wdown, w);
            auto dot = [&](u32x2 v, int c) __attribute__((always_inline)) -> f32x2 {
                const f32x2 w0 = {w[c], w[c + 1]}, w1 = {w[4 + c], w[5 + c]}, w2 = {w[8 + c], w[9 + c]}, w3 = {w[12 + c], w[13 + c]};
                return w0 * bf2f(v.x & 0xffffu) + w1 * bf2f(v.x >> 16) + w2 * bf2f(v.y & 0xffffu) + w3 * bf2f(v.y >> 16);
            };
#pragma unroll
            for (int cp = 0; cp < 2; cp++) {
                const f32x2 sd = dot(mv[4], 2 * cp);
#pragma unroll
                for (int e = 0; e < 2; e++) {
                    const int ex = (int)(wave_max(__float_as_uint(sd[e]) & 0x7fffffffu) >> 23);
                    e0[2 * cp + e] = __builtin_amdgcn_readfirstlane(ex == 0 ? 0 : min(max(ex - 129, -100), 100));
                }
                const f32x2 sc = {__uint_as_float((uint32_t)(127 - e0[2 * cp]) << 23), __uint_as_float((uint32_t)(127 - e0[2 * cp + 1]) << 23)};
                u32x4 o;
                u16x2 mx = {0, 0};
                f32x2 sq = {0.f, 0.f};
#pragma unroll
                for (int q = 0; q < 4; q++) {
                    const f32x2 y = dot(mv[q], 2 * cp) * sc;
                    const uint32_t hb = pk_bf16(y.x, y.y);
                    const f32x2 x = {__uint_as_float(hb << 16), __uint_as_float(hb & 0xffff0000u)};
                    o[q] = __builtin_bit_cast(uint32_t, __builtin_amdgcn_cvt_pkrtz(x.x, x.y));
                    mx = __builtin_elementwise_max(mx, __builtin_bit_cast(u16x2, hb & 0x7fff7fffu));
                    sq += x * x;
                }
                *(LDS u32x4*)(uintptr_t)(P_BUF + (cp * 64u + lane) * 272u + wv * 16) = o;
#pragma unroll
                for (int e = 0; e < 2; e++) {
                    const u32x2 st = {wave_max(mx[e]), __float_as_uint(wave_sum(sq[e]))};
                    if (lane == 0u) *(LDS u32x2*)(uintptr_t)(P_LST + ((2 * cp + e) * 16 + wv) * 8) = st;
                }
            }
        }
#pragma unroll
        for (int d = MDL; d < 6; d++) load_one(raw[1], tl0, d);
#pragma unroll
        for (int d = 0; d < 6; d++)
            if (((LPOS >> (4 * d)) & 15) < 4) load_one(raw[0], tl1, d);
        __syncthreads();
        u32x4 mo[2];
#pragma unroll
        for (int cp = 0; cp < 2; cp++) mo[cp] = *(const LDS u32x4*)(uintptr_t)(P_BUF + (cp * 64u + (t8 >> 7)) * 272u + ((t8 + t8) & 0xf0u));
        if (blockIdx.x == 0 && wv == 3) asm volatile("s_waitcnt vmcnt(0)" ::: "memory");
        __syncthreads();
#pragma unroll
        for (int s = 0; s < 16; s++) *(LDS u32x2*)(uintptr_t)(la + s * 256) = nul;
        asm volatile("" : "+v"(raw[1].o[0]), "+v"(raw[1].o[1]), "+v"(raw[1].i[0]), "+v"(raw[1].i[1]), "+v"(raw[1].a[0]), "+v"(raw[1].a[1]));
        if (blockIdx.x < SLOTI / 64) {
            const __amdgpu_buffer_rsrc_t rsM = __builtin_amdgcn_make_buffer_rsrc((void*)(meta + W_MDC / 4), 0, (int)(128u * SLOTI * 4u), 0x00020000);
#pragma unroll
            for (int cp = 0; cp < 2; cp++) {
                const uint32_t row = cp * 64u + (t8 >> 7), c16 = (t8 + t8) & 0xf0u;
                __builtin_amdgcn_raw_buffer_store_b128(mo[cp], rsM, row * (SLOTI * 4u) + c16 + blockIdx.x * 256u, 0, AUX_SC1);
            }
            if (wv == 0) {
                const u32x2 st = *(const LDS u32x2*)(uintptr_t)(P_LST + t8);
                const u32x2 r = {row_max(st.x) | (row_max(st.x ? 0xffffu - st.x : 0u) << 16), __float_as_uint(row_sum(__uint_as_float(st.y)))};
                if ((lane & 15u) == 15u) ((u32x2*)(meta + W_STAT / 4))[(lane >> 4) * NWG + blockIdx.x] = r;
                if (blockIdx.x == 0 && lane < 4u) meta[M_E0 + lane] = (uint32_t)(lane == 0u ? e0[0] : lane == 1u ? e0[1] : lane == 2u ? e0[2] : e0[3]);
            }
        }
    }
    pin(la);
    ST(0)
    RTS(5)

    uint32_t w[8], aw[8], xv[8];
    u64 nm[8];
    auto later2 = [&](int k) __attribute__((always_inline)) {
        uint32_t x0, x1;
        asm volatile("v_lshrrev_b32 %0, 4, %2\n\tv_lshrrev_b32 %1, 4, %5\n\tv_bitop3_b32 %0, %2, %0, 12 bitop3:0x72\n\tv_bitop3_b32 %1, %5, %1, 12 bitop3:0x72\n\t"
                     "s_not_b64 exec, %8\n\tds_write_b32 %2, %3 offset:%10\n\tds_or_b32 %0, %4 offset:%10\n\t"
                     "s_not_b64 exec, %9\n\tds_write_b32 %5, %6 offset:%10\n\tds_or_b32 %1, %7 offset:%10\n\ts_mov_b64 exec, -1"
                     : "=&v"(x0), "=&v"(x1)
                     : "v"(aw[k]), "v"(w[k]), "v"(xv[k]), "v"(aw[k + 1]), "v"(w[k + 1]), "v"(xv[k + 1]), "s"(nm[k]), "s"(nm[k + 1]), "n"(P_BUF) : "memory", "scc");
    };
    auto drain = [&]() __attribute__((always_inline)) {
        if (wv == 1) {
            const uint32_t q = (epoch + 1u) & 3u;
            const uint32_t n = __builtin_amdgcn_readfirstlane(lds_ld(vconst<P_FLAG + 16>() + q * 4u));
            if (__builtin_expect(n != 0u, 0)) {
                pin(t8);
                const uint32_t l = (t8 >> 3) & 63u;
                if (l < min(n, (uint32_t)LCAP)) {
                    const u32x4 e = *(const LDS u32x4*)(uintptr_t)(P_LST + (q * LCAP + l) * 16u);
                    lds_st(P_BUF + e.x, e.y);
                    lds_or(P_BUF + slot_x(e.x), e.z);
                }
                lds_st(vconst<P_FLAG + 16>() + q * 4u, vconst<0>());
            }
        }
    };
    auto again = [&](int D) __attribute__((always_inline)) {
        do {
            general(D, true, fm);
            __syncthreads();
            later2(0); later2(2); later2(4); later2(6);
            drain();
#pragma unroll
            for (int k = 0; k < 8; k++) {
                pin(aw[k]);
                bool due = false;
                if (aw[k] & 0x80000000u) {
                    xv[k] -= CHV;
                    if (xv[k] < CHV) {
                        const uint32_t f = (aw[k] >> 16) & 0x3ffu;
                        aw[k] = slot_w(aw[k] & 0xffffu, xv[k]);
                        xv[k] = f << ((xv[k] >> 8) & 31u);
                        due = true;
                    } else lds_st(vconst<P_FLAG>(), epoch + 1u);
                }
                nm[k] = ~__builtin_amdgcn_ballot_w64(due);
            }
            look(*(const LDS u32x2*)(uintptr_t)(P_CNT + t8));
            asm volatile("s_waitcnt lgkmcnt(0)" ::: "memory");
            __syncthreads();
        } while (__builtin_amdgcn_readfirstlane(lds_ld(vconst<P_FLAG>())) == epoch);
    };
    auto round = [&](auto DD, auto LL, Raw& rc, Raw& rn, int rd) __attribute__((always_inline)) {
        constexpr int D = decltype(DD)::value;
        constexpr bool LAST = decltype(LL)::value == 1, FIRST = decltype(LL)::value == 2;
        const int ng = min(GR, G - tl0 * GR);
        auto hook = [&](int p) __attribute__((always_inline)) {
#pragma unroll
            for (int q = 0; q < 6; q++)
            {
                constexpr int dummy = 0; (void)dummy;
                const int lp = (LPOS >> (4 * q)) & 15;
                if (!LAST && lp < 4 && lp == p) load_one(rc, tl2, q);
                if (!LAST && lp >= 4 && (lp < 8 || lp >= 12 ? lp : lp - 8) == p) load_one(rn, tl1, q);
            }
        };
        uint32_t wn[8], ca[8], px[4], cv[8];
        float mx;
        float cs[2];
        auto convA = [&](int q) __attribute__((always_inline)) {
            if (LAST) return;
            const uint32_t a = rc.a[q >> 1][q & 1];
            const f32x2 av = {__uint_as_float(a << 16), __uint_as_float(a & 0xffff0000u)};
            f32x2 rv;
            asm("v_pk_mul_f32 %0, %1, %2 op_sel_hi:[1,0]" : "=v"(rv) : "v"(av), "s"((u64)__float_as_uint(frr)));
            mx = __builtin_elementwise_maximum(__builtin_elementwise_maximum(__builtin_fabsf(rv.x), __builtin_fabsf(rv.y)), (q & 1) ? mx : 0.f);
            cs[0] = __builtin_amdgcn_cosf(rv.x); cs[1] = __builtin_amdgcn_cosf(rv.y);
            asm volatile("" : "+v"(mx), "+v"(cs[0]), "+v"(cs[1]));
            __builtin_amdgcn_sched_barrier(0);
        };
        auto convB = [&](int k) __attribute__((always_inline)) {
            if (LAST) return;
            const uint32_t o = rc.o[k >> 2][k & 3], i = rc.i[k >> 2][k & 3];
            wn[k] = (__builtin_amdgcn_alignbit(bfi(63u, o, o >> 5), __float_as_uint(cs[k & 1]), 30u) << 14) | (i >> 6);
            ca[k] = (bfi(63u, i, o) << 2) & 0x1ffcu;
            pin(wn[k]); pin(ca[k]);
            __builtin_amdgcn_sched_barrier(0);
        };
        auto convD = [&](int q) __attribute__((always_inline)) {
            if (LAST) return;
            asm volatile("v_cvt_pk_f16_f32 %0, |%1|, |%2|\n\tv_pk_mul_f16 %0, %0, %3 op_sel_hi:[1,0]" : "=v"(px[q]) : "v"(cs[0]), "v"(cs[1]), "s"(0x3ffu));
        };
        uint32_t cst;
        auto check = [&](int j) __attribute__((always_inline)) {
            if (LAST) return;
            if (__builtin_expect((__builtin_amdgcn_ballot_w64(!(mx <= RMAXREV)) | (u64)(uint32_t)(GR - ng)) != 0ull, 0)) {
                pin(t8);
                asm volatile("" : "+v"(rc.a[j]));
                const int lg = j * NT + (int)(t8 >> 3);
#pragma unroll
                for (int k = 0; k < 4; k++) {
                    const uint32_t a = (k & 1) ? (rc.a[j][k >> 1] >> 16) : (rc.a[j][k >> 1] & 0xffffu);
                    const bool nrm = __builtin_fabsf(frr * bf2f(a)) <= RMAXREV;
                    if (!(lg < ng && nrm)) ca[j * 4 + k] = NB * 4u + ((t8 >> 1) & 0xfcu);
                    if (lg < ng && !nrm) {
                        ready();
                        __builtin_amdgcn_raw_buffer_store_b32((uint32_t)((tl0 * GR + lg) * 4 + k), rsR, spec0 - draw(vconst<M_LOG * 4 + 4>()) * 4u, 0, 0);
                    }
                }
            }
            cst = vconst<CSTEP>();
        };
        auto ticket = [&](int k) __attribute__((always_inline)) {
            if (LAST) return;
            cv[k] = lds_add(P_CNT + ca[k], cst);
            __builtin_amdgcn_sched_barrier(0);
        };
        auto insert = [&](int j) __attribute__((always_inline)) {
            const int k0 = 4 * j;
            const bool odd = __builtin_amdgcn_ballot_w64(max(max(cv[k0], cv[k0 + 1]), max(cv[k0 + 2], cv[k0 + 3])) >= 2u * CHV) != 0ull;
#pragma unroll
            for (int k = k0; k < k0 + 4; k += 2) {
                uint32_t x0, x1;
                asm volatile("v_lshrrev_b32 %6, 11, %8\n\tv_lshrrev_b32 %7, 11, %9\n\t"
                             "v_bitop3_b32 %6, %6, %10, 60 bitop3:0x28\n\tv_bitop3_b32 %7, %7, %11, 60 bitop3:0x28\n\t"
                             "v_lshl_or_b32 %0, %10, 4, %6\n\tv_lshl_or_b32 %1, %11, 4, %7\n\t"
                             "v_lshlrev_b32_sdwa %2, %8, %12 dst_sel:DWORD dst_unused:UNUSED_PAD src0_sel:BYTE_1 src1_sel:WORD_0\n\t"
                             "v_lshlrev_b32_sdwa %3, %9, %12 dst_sel:DWORD dst_unused:UNUSED_PAD src0_sel:BYTE_1 src1_sel:WORD_1\n\t"
                             "v_bitop3_b32 %6, %0, %10, 12 bitop3:0x72\n\tv_bitop3_b32 %7, %1, %11, 12 bitop3:0x72\n\t"
                             "v_cmpx_gt_u32 %4, %15, %8\n\tds_write_b32 %0, %13 offset:%16\n\tds_or_b32 %6, %2 offset:%16\n\ts_mov_b64 exec, -1\n\t"
                             "v_cmpx_gt_u32 %5, %15, %9\n\tds_write_b32 %1, %14 offset:%16\n\tds_or_b32 %7, %3 offset:%16\n\ts_mov_b64 exec, -1"
                             : "=&v"(aw[k]), "=&v"(aw[k + 1]), "=&v"(xv[k]), "=&v"(xv[k + 1]), "=&s"(nm[k]), "=&s"(nm[k + 1]), "=&v"(x0), "=&v"(x1)
                             : "v"(cv[k]), "v"(cv[k + 1]), "v"(ca[k]), "v"(ca[k + 1]), "v"(px[k >> 1]), "v"(wn[k]), "v"(wn[k + 1]), "s"(CHV), "n"(P_BUF) : "memory");
                w[k] = wn[k]; w[k + 1] = wn[k + 1];
                if (__builtin_expect(odd, 0)) {
#pragma unroll
                    for (int q = k; q < k + 2; q++) {
                        nm[q] |= __builtin_amdgcn_ballot_w64(cv[q] >= 2u * CHV);
                        if (cv[q] >= 2u * CHV && cv[q] < 0x40000000u) {
                            const uint32_t lq = (epoch + 2u) & 3u;
                            uint32_t p = LCAP;
                            if (cv[q] < 3u * CHV) p = lds_add(vconst<P_FLAG + 16>() + lq * 4u, vconst<1>());
                            if (p < LCAP) {
                                const u32x2 e = {aw[q], w[q]};
                                *(LDS u32x2*)(uintptr_t)(P_LST + (lq * LCAP + p) * 16u) = e;
                                lds_st(P_LST + (lq * LCAP + p) * 16u + 8u, xv[q]);
                            } else {
                                aw[q] = ca[q] | ((q & 1) ? (px[q >> 1] & 0xffff0000u) : (px[q >> 1] << 16)) | 0x80000000u;
                                xv[q] = cv[q] - CHV;
                                lds_st(vconst<P_FLAG>(), epoch);
                            }
                        }
                    }
                }
            }
        };
        constexpr int P0 = D ? 0 : 3, P1 = D ? 1 : 2, P2 = D ? 2 : 1, P3 = D ? 3 : 0;
        const uint32_t fl = lds_ld(vconst<P_FLAG>());
        if (!LAST) asm volatile("" : "+v"(rc.o[0]), "+v"(rc.o[1]), "+v"(rc.i[0]), "+v"(rc.i[1]), "+v"(rc.a[0]), "+v"(rc.a[1]));
        if (!LAST && (rd + 4 >= nstat || tl1 >= ntile) && wv == 2) {
            pin(tk);
            if ((t8 & 511u) == 0u) {
                if (tl1 >= ntile) {
                    if (rd + 3 <= nstat) ready();
                    (void)draw(vconst<M_MD * 4>());
                } else if (rd + 4 == nstat) tk = peek(vconst<M_BEAT * 4>());
                else {
                    if (rd + 3 == nstat) {
                        if (tk == lds_ld(vconst<P_FLAG + 8>())) ready();
                        tk = 0u;
                    }
                    lds_st(vconst<P_FLAG + 4>(), tk);
                    if ((int)tk < ntile - nstat * NWG) tk = draw(vconst<M_TILE * 4>());
                }
            }
        }
        if (!LAST && blockIdx.x == 0 && wv == 3 && (t8 & 511u) == 0u) beat();
        hook(12);
        ST(3)
        pin(t8);
        const uint32_t tr = P_TAB + (t8 & ~63u), u16 = (t8 + t8) & 0x70u;
        convA(0);
        convB(0);
        const bool ag = __builtin_amdgcn_readfirstlane(fl) == epoch;
        if (!FIRST && __builtin_expect(LAST || ag || sm != 0ull, 0)) {
            if (ag) again(D);
            general(D, false, sm);
            Va = *(const LDS u32x4*)(uintptr_t)(P_TAB + (t8 & ~63u) + P0 * 16);
            Vb = *(const LDS u32x4*)(uintptr_t)(P_TAB + (t8 & ~63u) + P1 * 16);
            asm volatile("" : "+v"(Va), "+v"(Vb));
        }
        uint32_t pq = (uint32_t)pb2 | (uint32_t)(pb2 >> 32);
        pq = __builtin_amdgcn_readfirstlane(pq | (pq >> 1));
        uint32_t ad[8];
        if (!FIRST) ad[0] = take<4 * P0 + 0, D>(Va[0], u16, la, nul);
        convB(1);
        if (!FIRST) ad[1] = take<4 * P0 + 1, D>(Va[1], u16, la, nul);
        convD(0);
        convA(1);
        if (!FIRST) ad[2] = take<4 * P0 + 2, D>(Va[2], u16, la, nul);
        convB(2);
        hook(13);
        if (!FIRST) ad[3] = take<4 * P0 + 3, D>(Va[3], u16, la, nul);
        convB(3);
        if (!FIRST) put<4 * P0 + 0, LAST ? 3 : 3>(rsA, ad[0], pq);
        if (!FIRST) ad[4] = take<4 * P1 + 0, D>(Vb[0], u16, la, nul);
        convD(1);
        check(0);
        ticket(0);
        hook(4);
        if (!FIRST) put<4 * P0 + 1, LAST ? 3 : 4>(rsA, ad[1], pq);
        if (!FIRST) ad[5] = take<4 * P1 + 1, D>(Vb[1], u16, la, nul);
        convA(2);
        ticket(1);
        if (!FIRST) put<4 * P0 + 2, LAST ? 3 : 5>(rsA, ad[2], pq);
        if (!FIRST) ad[6] = take<4 * P1 + 2, D>(Vb[2], u16, la, nul);
        convB(4);
        ticket(2);
        if (!FIRST) put<4 * P0 + 3, LAST ? 3 : 6>(rsA, ad[3], pq);
        if (!FIRST) Va = *(const LDS u32x4*)(uintptr_t)(tr + P2 * 16);
        __builtin_amdgcn_sched_barrier(0);
        if (!FIRST) ad[7] = take<4 * P1 + 3, D>(Vb[3], u16, la, nul);
        convB(5);
        ticket(3);
        SETP(2);
        if (!FIRST) put<4 * P1 + 0, LAST ? 4 : 8>(rsA, ad[4], pq);
        if (!FIRST) ad[0] = take<4 * P2 + 0, D>(Va[0], u16, la, nul);
        convD(2);
        convA(3);
        hook(5);
        if (!FIRST) put<4 * P1 + 1, LAST ? 4 : 7>(rsA, ad[5], pq);
        if (!FIRST) ad[1] = take<4 * P2 + 1, D>(Va[1], u16, la, nul);
        convB(6);
        if (!FIRST) put<4 * P1 + 2, LAST ? 4 : 6>(rsA, ad[6], pq);
        if (!FIRST) ad[2] = take<4 * P2 + 2, D>(Va[2], u16, la, nul);
        convB(7);
        if (!FIRST) put<4 * P1 + 3, LAST ? 3 : 4>(rsA, ad[7], pq);
        if (!FIRST) Vb = *(const LDS u32x4*)(uintptr_t)(tr + P3 * 16);
        __builtin_amdgcn_sched_barrier(0);
        if (!FIRST) ad[3] = take<4 * P2 + 3, D>(Va[3], u16, la, nul);
        convD(3);
        check(1);
        ticket(4);
        SETP(1);
        hook(6);
        if (!FIRST) put<4 * P2 + 0, LAST ? 4 : 5>(rsA, ad[0], pq);
        if (!FIRST) ad[4] = take<4 * P3 + 0, D>(Vb[0], u16, la, nul);
        ticket(5);
        if (!FIRST) put<4 * P2 + 1, LAST ? 4 : 6>(rsA, ad[1], pq);
        if (!FIRST) ad[5] = take<4 * P3 + 1, D>(Vb[1], u16, la, nul);
        ticket(6);
        if (!FIRST) put<4 * P2 + 2, LAST ? 4 : 7>(rsA, ad[2], pq);
        if (!FIRST) ad[6] = take<4 * P3 + 2, D>(Vb[2], u16, la, nul);
        ticket(7);
        SETP(0);
        if (!FIRST) put<4 * P2 + 3, LAST ? 3 : 7>(rsA, ad[3], pq);
        if (!FIRST) ad[7] = take<4 * P3 + 3, D>(Vb[3], u16, la, nul);
        hook(7);
        ST(4)
        SETP(PRIO > 1 ? 3 : 0);
        asm volatile("s_waitcnt lgkmcnt(0)" ::: "memory");
        __syncthreads();
        ST(5)
        u32x2 cn;
        uint32_t tv;
        if (!LAST) { cn = *(const LDS u32x2*)(uintptr_t)(P_CNT + t8); tv = lds_ld(vconst<P_FLAG + 4>()); }
        __builtin_amdgcn_sched_barrier(0);
        if (!FIRST) later2(0);
        if (!FIRST) put<4 * P3, 15>(rsA, ad[4], pq);
        if (!FIRST) later2(2);
        if (!FIRST) put<4 * P3 + 1, 15>(rsA, ad[5], pq);
        if (!FIRST) drain();
        if (!LAST) {
            tl2 = rd + 2 < nstat ? fixed_tile(rd + 2) : nstat * NWG + (int)__builtin_amdgcn_readfirstlane(tv);
            look(cn);
        }
        hook(0);
        SETP(PRIO > 1 ? 2 : 0);
        if (!FIRST) later2(4);
        if (!FIRST) put<4 * P3 + 2, 15>(rsA, ad[6], pq);
        if (!FIRST) later2(6);
        if (!FIRST) put<4 * P3 + 3, 15>(rsA, ad[7], pq);
        if (!LAST) {
            hook(1);
            SETP(PRIO > 1 ? 1 : 0);
            insert(0);
            hook(2);
            SETP(0);
            pin(t8);
            Va = *(const LDS u32x4*)(uintptr_t)(P_TAB + (t8 & ~63u) + P3 * 16);
            Vb = *(const LDS u32x4*)(uintptr_t)(P_TAB + (t8 & ~63u) + P2 * 16);
            __builtin_amdgcn_sched_barrier(0);
            insert(1);
            hook(3);
            ST(1)
            SETP(3);
            asm volatile("s_waitcnt lgkmcnt(0)" ::: "memory");
            __syncthreads();
            ST(2)
            tl0 = tl1; tl1 = tl2;
        }
    };
#define HMV(A, B) "v_mov_b64 v[" #A ":" #A "+1], v[" #B ":" #B "+1]\n\t"
#define HMV4(A) HMV(A + 8, A + 6) HMV(A + 6, A + 4) HMV(A + 4, A + 2) HMV(A + 2, A)
    round(IC<1>(), IC<2>(), raw[1], raw[0], 0);
    int rd = 1;
#pragma unroll 1
    for (; tl1 < ntile; rd += 2) {
        round(IC<0>(), IC<0>(), raw[0], raw[1], rd);
        round(IC<1>(), IC<0>(), raw[1], raw[0], rd + 1);
    }
    if (tl0 >= ntile) asm volatile(HMV4(88) HMV4(98) HMV4(108) HMV4(118));
    asm volatile("" : "+s"(tl0));
    if (tl0 < ntile) round(IC<0>(), IC<0>(), raw[0], raw[1], rd);
    RTS(1)
    round(IC<1>(), IC<1>(), raw[1], raw[0], 0);
    if (__builtin_amdgcn_readfirstlane(lds_ld(P_FLAG + 16 + ((epoch + 2u) & 3u) * 4u)) != 0u) {
        asm volatile("s_waitcnt lgkmcnt(0)" ::: "memory");
        __syncthreads();
        look(*(const LDS u32x2*)(uintptr_t)(P_CNT + t8));
        general(0, true, fm);
        __syncthreads();
        drain();
    }
    {
        uint32_t* tcnt = meta + W_TCNT / 4;
        const u32x2 c = *(const LDS u32x2*)(uintptr_t)(P_CNT + t8);
        const uint32_t tw = tab_at();
#pragma unroll
        for (int e = 0; e < 2; e++) {
            const uint32_t g = U[e] & 15u;
            lds_st(tw + e * 64u, g >= 8u ? 0x80000000u : ((U[e] + ((g & 1u) ? 0u : SK * 64u)) & 0x7fffff81u) | (((7u - g) << 1) & 12u));
            *(LDS uint8_t*)(uintptr_t)(P_CNT + (t8 & ~511u) + (((tw - P_TAB) >> 2) & 127u) + e * 16u) =
                (uint8_t)(g < 8u ? 0u : ((g - 8u) >> 1) + (((c[e] + 2u * CSTEP) >> 15) << 2) + (g & 1u) * 0x50u);
        }
        asm volatile("s_waitcnt lgkmcnt(0)" ::: "memory");
        __syncthreads();
        const uint32_t u16 = (t8 + t8) & 0x70u;
        u32x4 En[4];
#pragma unroll
        for (int p = 0; p < 4; p++) En[p] = *(const LDS u32x4*)(uintptr_t)(P_TAB + (t8 & ~63u) + p * 16u);
        const u32x4 cb = *(const LDS u32x4*)(uintptr_t)(P_CNT + (t8 & ~511u) + ((t8 >> 2) & 0x70u));
        u32x2 N[16];
#pragma unroll
        for (int s = 0; s < 16; s++) N[s] = *(const LDS u32x2*)(uintptr_t)(la + s * 256u);
        if (u16 == 0u) *(u32x4*)(tcnt + blockIdx.x * 512u + (t8 >> 4)) = cb;
        const uint32_t sg = P_BUF + ((t8 & ~63u) << 4);
        uint32_t ps;
        auto rest = [&](auto SL) __attribute__((always_inline)) {
            constexpr int s = decltype(SL)::value;
            const uint32_t en = En[s >> 2][s & 3], by = cb[s >> 2] >> ((s & 3) * 8);
            const u32x2 Hr = hget<HREG(s, 0)>();
            const uint32_t at = ps + ((la & 0x38u) ^ ((s & 3) << 4));
            *(LDS u32x2*)(uintptr_t)at = Hr;
            *(LDS u32x2*)(uintptr_t)(at + (by & 0x40u)) = N[s];
            ps += (by & 0x3cu) << 2;
            const u32x2 Hs = (en & 1u) ? Hr : nul;
            const u32x4 d = {Hs.x, Hs.y, N[s].x, N[s].y};
            uint32_t a = __builtin_amdgcn_bitop3_b32(en, u16, 15u, 0x54);
            __builtin_amdgcn_raw_buffer_store_b128(d, rsR, a, 0, POS(s) >= 16 - NTS ? AUX_NT : AUX_SC1);
            if (__builtin_expect(__builtin_amdgcn_ballot_w64((en & 12u) != 0u) != 0ull, 0)) {
                const u32x4 dn = {nul.x, nul.y, nul.x, nul.y};
#pragma unroll 1
                for (uint32_t k = (en >> 2) & 3u; k > 0u; k--) {
                    a += SK * 128u;
                    __builtin_amdgcn_raw_buffer_store_b128(dn, rsR, a, 0, AUX_SC1);
                }
            }
        };
        auto pack = [&](int p) __attribute__((always_inline)) {
            const uint32_t a = (SBASE + ((((t8 >> 1) & 0x60u) + (t8 >> 8)) * 2u + p) * (NWG * 9u) + blockIdx.x * 9u) * 128u + u16;
            asm volatile("" ::: "memory");
#pragma unroll 1
            for (uint32_t q = 0; __builtin_amdgcn_ballot_w64(sg + q < ps) != 0ull; q += 256u) {
                const u32x4 d0 = *(const LDS u32x4*)(uintptr_t)(sg + q + u16), d1 = *(const LDS u32x4*)(uintptr_t)(sg + q + 128u + u16);
                __builtin_amdgcn_raw_buffer_store_b128(d0, rsR, sg + q < ps ? a + q : 0xffffff00u, 0, AUX_SC1);
                __builtin_amdgcn_raw_buffer_store_b128(d1, rsR, sg + q + 128u < ps ? a + q + 128u : 0xffffff00u, 0, AUX_SC1);
            }
            asm volatile("" ::: "memory");
        };
        asm volatile("" : "+v"(N[0]), "+v"(N[1]), "+v"(N[2]), "+v"(N[3]), "+v"(N[4]), "+v"(N[5]), "+v"(N[6]), "+v"(N[7]) :: "memory");
        asm volatile("" : "+v"(N[8]), "+v"(N[9]), "+v"(N[10]), "+v"(N[11]), "+v"(N[12]), "+v"(N[13]), "+v"(N[14]), "+v"(N[15]) :: "memory");
        ps = sg;
        rest(IC<0>()); rest(IC<1>()); rest(IC<2>()); rest(IC<3>()); rest(IC<4>()); rest(IC<5>()); rest(IC<6>()); rest(IC<7>());
        pack(0);
        ps = sg;
        rest(IC<8>()); rest(IC<9>()); rest(IC<10>()); rest(IC<11>()); rest(IC<12>()); rest(IC<13>()); rest(IC<14>()); rest(IC<15>());
        pack(1);
    }
    RTS(2)
    ST(6)
    if (blockIdx.x == 0 && t8 == 1536u) {
        do {
            beat();
            __builtin_amdgcn_s_sleep(8);
        } while (peek(vconst<M_MD * 4>()) < NWG);
    }
    RTS(3)
    RTS(4)
    ST(7)
    ST_OUT(0)
}

#define A_MD 0
#define A_ACC (SLOTI * 4)
#define A_FLAG (A_ACC + SLOTO * 4)
#define A_LDS (A_FLAG + 512)
static_assert(SLOTI * 4 == 3 * NT * 16 + 1024, "md staging assumes three full vectors and 256 dwords");
#define BIAS 0x40004000u
#define NFAST 8
#define NLD 10
#define PRE0 2
#define PRE1 4
#define MDF 0x0a11333
#define MDP 0xaaaaa
#define ROTH 0
#define ROTP 0
#define HAUX AUX_NT
#define OAUX 2
#define MDQ 0x00888
#define UNM 1
#define APRIO 0x543
#define SHARE (SLOTO / 8)
#define SPINCAP 4096u
#define ZSIG 9.0f
#define HSLOW 0x80008000u
#define GAS __attribute__((address_space(1)))

__global__ __launch_bounds__(NT) __attribute__((amdgpu_num_vgpr(53))) void acc_kernel(char* ws, int T, int E, int ks, int lcap, float zf, int pad,
                                                 const uint16_t* __restrict__ centers, const uint16_t* __restrict__ wrbf,
                                                 const uint16_t* __restrict__ wsbf, const uint16_t* __restrict__ wup,
                                                 const u32x2* __restrict__ m, const uint16_t* __restrict__ dist, u32x2* __restrict__ out,
                                                 const int* __restrict__ iin, const int* __restrict__ iout, const uint16_t* __restrict__ ang,
                                                 const uint16_t* __restrict__ wdown, const uint16_t* __restrict__ freq) {
    const char* recs = ws + W_RECS;
    const u32x4* tcnt = (const u32x4*)(ws + W_TCNT);
    const uint32_t* mdc = (const uint32_t*)(ws + W_MDC);
    const u32x4* stat = (const u32x4*)(ws + W_STAT);
    uint32_t* partial = (uint32_t*)(ws + W_PART);
    unsigned* meta = (unsigned*)ws;
    extern __shared__ __attribute__((aligned(16))) char smem[];
    ST_DECL
    RTS(8)
    unsigned* acc = (unsigned*)(smem + A_ACC);
    unsigned* s_flag = (unsigned*)(smem + A_FLAG);
    const int tid = threadIdx.x;
    const int lane = tid & 63;
    const int wv = __builtin_amdgcn_readfirstlane(tid >> 6);
    const int cp = (blockIdx.x >> 3) & 1;
    const int h = blockIdx.x & 3;
    const int j = ((blockIdx.x >> 2) & 1) * 16 + (blockIdx.x >> 4);
    u32x4 tc;
    const __amdgpu_buffer_rsrc_t rsM = __builtin_amdgcn_make_buffer_rsrc((void*)mdc, 0, (int)(128u * SLOTI * 4u), 0x00020000);

    u32x4 pm[4];
    u32x4 ring[NLD];
    auto load_md = [&](int ff, int pl, int mdp) __attribute__((always_inline)) {
        const uint32_t so = (uint32_t)(cp * 64 + ((ABL & 64) ? 0 : ff * 4) + h) * (SLOTI * 4u);
#pragma unroll
        for (int v = 0; v < 3; v++)
            if (pl < 0 || pl == ((mdp >> (4 * v)) & 15)) pm[v] = __builtin_amdgcn_raw_buffer_load_b128(rsM, (uint32_t)tid * 16u, so + (uint32_t)v * (NT * 16u), 0);
        if ((pl < 0 || pl == (mdp & 15)) && wv == 0) pm[3] = __builtin_amdgcn_raw_buffer_load_b128(rsM, (uint32_t)tid * 16u, so + 3u * NT * 16u, 0);
        __builtin_amdgcn_sched_barrier(0);
    };
    const uint32_t lf = ((uint32_t)(tid & ~31) * 16u + (uint32_t)(tid & 12) * 32u) * SR + (uint32_t)(tid & 16) * 4u + (uint32_t)(tid & 3) * 16u;
    const uint32_t lp = (uint32_t)(tid >> 2) * (128u * SR) + (uint32_t)(tid & 3) * 16u;
    auto load_main = [&](int i, int ff, bool nt) __attribute__((always_inline)) -> u32x4 {
        if (ABL & 8) nt = false;
        const __amdgpu_buffer_rsrc_t rs = __builtin_amdgcn_make_buffer_rsrc((void*)(recs + ((uint32_t)POS(ff) * SS + (uint32_t)(h * 32 + ((ABL & 8) ? 0 : j)) * SC) * 128u), 0, ks * (int)(SK * 128u), 0x00020000);
        const int kk = (i >> 1) ^ (cp * (nt ? ROTH : ROTP));
        const int so = (int)((kk * SK + (i & 1) * 128u * SR) * 128u);
        if (nt) return __builtin_amdgcn_raw_buffer_load_b128(rs, lf, so, HAUX);
        if (UNM) return __builtin_amdgcn_raw_buffer_load_b128(rs, lp + (uint32_t)(i & 1) * 64u, (int)(kk * SK * 128u), 0);
        return __builtin_amdgcn_raw_buffer_load_b128(rs, lf, so, 0);
    };
    const __amdgpu_buffer_rsrc_t rsT = __builtin_amdgcn_make_buffer_rsrc((void*)recs, 0, (int)(LBASE * 128u), 0x00020000);
    const uint32_t tof = (uint32_t)(lane & 2) * (TPL * 64u) + (uint32_t)(tid >> 2) * 128u + (uint32_t)(lane & 1) * 64u + 4u + (TBASE + (uint32_t)(h * 32 + j) * TST) * 128u;
    const uint32_t sp0 = (SBASE + (uint32_t)(h * 32 + j) * (2u * NWG * 9u) + (uint32_t)(tid >> 2) * 9u) * 128u;
    auto tail_rank = [&](uint32_t c, uint32_t sp, int ff, uint32_t t0, int& dst, uint32_t& en) __attribute__((always_inline)) -> int {
        const int k4 = (int)(((uint32_t)(lane & 3) + t0) * 4u) - (int)((c & 3u) * 8u);
        const int left = (int)((c >> 2) & 15u) - k4;
        const bool has = left > 0;
        uint32_t ea = tof + (uint32_t)POS(ff) * (128u * TST * 128u), eb = sp + (uint32_t)(k4 * 16 + min(max(left, 0), 4));
        asm volatile("" : "+v"(ea), "+v"(eb));
        en = k4 < 0 ? ea : eb;
        const u64 mask = __builtin_amdgcn_ballot_w64(has);
        const int n = __builtin_popcountll(mask);
        const int rank = __builtin_amdgcn_mbcnt_hi((uint32_t)(mask >> 32), __builtin_amdgcn_mbcnt_lo((uint32_t)mask, 0u));
        const int r = has ? rank : (n + lane - rank);
        dst = ((r & 15) << 4) | ((r >> 4) << 2);
        return n;
    };
    auto load_tail = [&](uint32_t en, u64& tm) __attribute__((always_inline)) -> u32x4 {
        const bool on = (uint32_t)(lane & 3) < (en & 7u) && !(ABL & 32);
        tm = __builtin_amdgcn_ballot_w64(on);
        return __builtin_amdgcn_raw_buffer_load_b128(rsT, on ? (en & ~15u) + (uint32_t)(lane & 3) * 16u : 0xffffff00u, 0, 0);
    };
    float rs[4][4], rg[4][4];
    u32x4 em[2];
    u32x2 dw2;
    const __amdgpu_buffer_rsrc_t rsE = __builtin_amdgcn_make_buffer_rsrc((void*)m, 0, E * 8, 0x00020000);
    const __amdgpu_buffer_rsrc_t rsD = __builtin_amdgcn_make_buffer_rsrc((void*)dist, 0, E * 2, 0x00020000);
    auto edge_loads = [&](int sh) __attribute__((always_inline)) {
        const int ol = sh * SHARE + tid * 4;
        const uint32_t e0 = (tid < SHARE / 4) ? (uint32_t)(((ol >> 6) << 11) | (j << 6) | (ol & 63)) : 0x0fffff00u;
        em[0] = __builtin_amdgcn_raw_buffer_load_b128(rsE, e0 * 8u, 0, AUX_NT);
        em[1] = __builtin_amdgcn_raw_buffer_load_b128(rsE, e0 * 8u + 16u, 0, AUX_NT);
        dw2 = __builtin_amdgcn_raw_buffer_load_b64(rsD, e0 * 2u, 0, AUX_NT);
    };
    auto factors = [&]() __attribute__((always_inline)) {
        const float sq = 3.7982826f;
        const f32x2 d01 = {bf2f(dw2.x & 0xffffu), bf2f(dw2.x >> 16)}, d23 = {bf2f(dw2.y & 0xffffu), bf2f(dw2.y >> 16)};
        f32x2 a[4][4];
#pragma unroll
        for (int q = 0; q < 16; q++) a[q >> 2][q & 3] = (f32x2)(0.f);
#pragma unroll 2
        for (int r = 0; r < 8; r++) {
            const uint32_t cw = ((const uint32_t*)centers)[r >> 1];
            const float cn = bf2f((r & 1) ? (cw >> 16) : (cw & 0xffffu));
            const u32x2 s2 = ((const u32x2*)wsbf)[r], r2 = ((const u32x2*)wrbf)[r];
            const f32x2 w[4] = {{bf2f(s2.x & 0xffffu), bf2f(s2.x >> 16)}, {bf2f(s2.y & 0xffffu), bf2f(s2.y >> 16)},
                                {bf2f(r2.x & 0xffffu), bf2f(r2.x >> 16)}, {bf2f(r2.y & 0xffffu), bf2f(r2.y >> 16)}};
            const f32x2 y01 = (d01 - cn) * sq, y23 = (d23 - cn) * sq;
            const f32x2 t01 = -y01 * y01, t23 = -y23 * y23;
            const float rb[4] = {__builtin_amdgcn_exp2f(t01.x), __builtin_amdgcn_exp2f(t01.y), __builtin_amdgcn_exp2f(t23.x), __builtin_amdgcn_exp2f(t23.y)};
#pragma unroll
            for (int q = 0; q < 4; q++) {
#pragma unroll
                for (int k = 0; k < 4; k++) a[q][k] = __builtin_elementwise_fma(w[k], (f32x2)(rb[q]), a[q][k]);
            }
        }
#pragma unroll
        for (int q = 0; q < 4; q++) {
#pragma unroll
            for (int k = 0; k < 4; k++) { rs[q][k] = a[q][k >> 1][k & 1]; rg[q][k] = a[q][2 + (k >> 1)][k & 1]; }
        }
    };
#define ADT(V, O) "ds_write_addtid_b32 " V " offset:" #O "\n\t"
    auto store_md = [&]() __attribute__((always_inline)) {
        asm volatile("s_mov_b32 m0, %12\n\ts_nop 0\n\t" ADT("%0", 0) ADT("%1", 256) ADT("%2", 512) ADT("%3", 768) ADT("%4", 16384) ADT("%5", 16640) ADT("%6", 16896) ADT("%7", 17152)
                     ADT("%8", 32768) ADT("%9", 33024) ADT("%10", 33280) ADT("%11", 33536)
                     :: "v"(pm[0][0]), "v"(pm[0][1]), "v"(pm[0][2]), "v"(pm[0][3]), "v"(pm[1][0]), "v"(pm[1][1]), "v"(pm[1][2]), "v"(pm[1][3]),
                        "v"(pm[2][0]), "v"(pm[2][1]), "v"(pm[2][2]), "v"(pm[2][3]), "s"(wv * 1024) : "memory");
        if (wv == 0) asm volatile("s_mov_b32 m0, 0\n\ts_nop 0\n\t" ADT("%0", 49152) ADT("%1", 49408) ADT("%2", 49664) ADT("%3", 49920)
                                  :: "v"(pm[3][0]), "v"(pm[3][1]), "v"(pm[3][2]), "v"(pm[3][3]) : "memory");
    };
    const int me = h * 2 + cp;
    bool fast, more;
    uint32_t need, mg0, mg1;
    uint32_t guard = 0;
    uint32_t sgn = 0x8000u;
    pin(sgn);

#define GAT3(M) { uint32_t a0_, a1_, a2_; asm volatile( \
        "v_lshlrev_b16 %0, 2, %3\n\tv_lshlrev_b16 %1, 2, %4\n\tv_lshlrev_b16 %2, 2, %5\n\t" \
        "ds_read_b32 v[%6], %0\n\tds_read_b32 v[%6+1], %1\n\tds_read_b32 v[%6+2], %2" \
        : "=&v"(a0_), "=&v"(a1_), "=&v"(a2_) : "v"(w0), "v"(w1), "v"(w2), "n"(M) : "memory"); }
#define MIXL " op_sel_hi:[1,1,0]\n\t"
#define MIXH " op_sel:[0,1,0] op_sel_hi:[1,1,0]\n\t"
#define SDW " dst_sel:DWORD dst_unused:UNUSED_PAD src0_sel:DWORD src1_sel:WORD_1\n\t"
#define SCAT(PRE, POST, M) { uint32_t c0_, c1_, c2_, a0_, a1_, a2_; asm volatile( \
        "v_bitop3_b32 %6, %6, v[%15-9], v[%15-8] bitop3:0xfe\n\tv_and_b32 %0, 0x3ff, %7\n\tv_lshrrev_b32 %2, 22, %7\n\tv_or_b32 %6, v[%15-7], %6\n\t" PRE \
        "v_bitop3_b32 %0, %11, %8, %0 bitop3:0xea\n\tv_bitop3_b32 %2, %11, %10, %2 bitop3:0xea\n\tv_bfe_u32 %1, %7, 11, 10\n\t" \
        "v_fma_mix_f32 %3, %0, v[%15], %12" MIXL "v_bitop3_b32 %1, %11, %9, %1 bitop3:0xea\n\tv_fma_mix_f32 %0, %0, v[%15], %13" MIXH \
        "v_fma_mix_f32 %4, %1, v[%15+1], %12" MIXL "v_lshl_add_u32 %0, %0, 16, %3\n\tv_lshlrev_b32_sdwa %3, 2, %8" SDW \
        "v_fma_mix_f32 %1, %1, v[%15+1], %13" MIXH "ds_add_rtn_u32 v[%15-9], %3, %0 offset:%14\n\t" \
        "v_fma_mix_f32 %5, %2, v[%15+2], %12" MIXL "v_lshl_add_u32 %1, %1, 16, %4\n\tv_lshlrev_b32_sdwa %4, 2, %9" SDW \
        "v_fma_mix_f32 %2, %2, v[%15+2], %13" MIXH "ds_add_rtn_u32 v[%15-8], %4, %1 offset:%14\n\t" \
        "v_lshl_add_u32 %2, %2, 16, %5\n\tv_lshlrev_b32_sdwa %5, 2, %10" SDW \
        "ds_add_rtn_u32 v[%15-7], %5, %2 offset:%14" POST \
        : "=&v"(c0_), "=&v"(c1_), "=&v"(c2_), "=&v"(a0_), "=&v"(a1_), "=&v"(a2_), "+v"(guard) \
        : "v"(xw), "v"(w0), "v"(w1), "v"(w2), "v"(sgn), "s"(mg0), "s"(mg1), "n"(A_ACC), "n"(M), "s"(tm) : "memory"); }
    asm volatile("v_mov_b32 v110, 0\n\tv_mov_b32 v111, 0\n\tv_mov_b32 v112, 0\n\tv_mov_b32 v113, 0\n\tv_mov_b32 v114, 0\n\tv_mov_b32 v115, 0\n\t"
                 "v_mov_b32 v116, 0\n\tv_mov_b32 v117, 0\n\tv_mov_b32 v118, 0" ::: "v106", "v127");
    auto unmix = [&](int i) __attribute__((always_inline)) {
        const auto s0 = __builtin_amdgcn_permlane16_swap(ring[i][0], ring[i][2], false, false);
        const auto s1 = __builtin_amdgcn_permlane16_swap(ring[i][1], ring[i][3], false, false);
        ring[i][0] = s0[0]; ring[i][2] = s0[1]; ring[i][1] = s1[0]; ring[i][3] = s1[1];
    };
    auto word = [&](int i, int k, bool pr) __attribute__((always_inline)) -> uint32_t {
        return pr && i < NFAST ? ring[(i & ~1) | (k >> 1)][(i & 1) * 2 + (k & 1)] : ring[i][k];
    };
    auto gather = [&](int R, int i, int p, bool pr) __attribute__((always_inline)) {
        if (i < NFAST && !pr) unmix(i);
        const uint32_t w0 = word(i, 0 ^ R, pr), w1 = word(i, 1 ^ R, pr), w2 = word(i, 2 ^ R, pr);
        if (ABL & 16) { asm volatile("" :: "v"(w0), "v"(w1), "v"(w2)); return; }
        if (p == 0) GAT3(119) else if (p == 1) GAT3(122) else GAT3(125)
    };
    auto scatter = [&](int R, int i, int p, u64 tm, bool pr) __attribute__((always_inline)) {
        const uint32_t xw = word(i, 3 ^ R, pr), w0 = word(i, 0 ^ R, pr), w1 = word(i, 1 ^ R, pr), w2 = word(i, 2 ^ R, pr);
        if (ABL & 16) { asm volatile("" :: "v"(w0), "v"(w1), "v"(w2), "v"(xw)); return; }
        if (i < NFAST) { if (p == 0) SCAT("", "", 119) else if (p == 1) SCAT("", "", 122) else SCAT("", "", 125) }
        else if (p == 0) SCAT("s_mov_b64 exec, %16\n\t", "\n\ts_mov_b64 exec, -1", 119)
        else SCAT("s_mov_b64 exec, %16\n\t", "\n\ts_mov_b64 exec, -1", 125)
    };
#define WAIT_L(N) asm volatile("s_waitcnt lgkmcnt(" #N ")" ::: "memory")
    int ntn;
    u64 tm0, tm1;
    uint32_t ta[2], sp = sp0;
    auto list1 = [&](int ff) __attribute__((always_inline)) {
        int dst;
        uint32_t en;
        ntn = tail_rank(tc.x, sp, ff, 0u, dst, en);
        asm volatile("ds_permute_b32 v106, %0, %1" :: "v"(dst), "v"(en) : "memory");
    };
    auto step = [&](auto RR, auto NN, auto NC, int ff) __attribute__((always_inline)) {
        constexpr int R = decltype(RR)::value;
        constexpr bool nt = decltype(NN)::value == 1, last = decltype(NN)::value == 2, nc = decltype(NC)::value == 1, first = decltype(NC)::value == 2;
        constexpr bool pr = UNM && !nc;
        constexpr int mdp = first ? MDF : (nt || nc) ? MDQ : MDP;
        gather(R, 0, 0, pr);
        if (!first) scatter(R, NFAST, 2, tm0, pr);
        const int ntl = ntn;
        asm volatile("s_nop 1\n\tv_mov_b32_dpp %0, v106 quad_perm:[0,0,0,0] row_mask:0xf bank_mask:0xf\n\tv_mov_b32_dpp %1, v106 quad_perm:[1,1,1,1] row_mask:0xf bank_mask:0xf" : "=v"(ta[0]), "=v"(ta[1]));
        auto side = [&](int pl) __attribute__((always_inline)) {
            if (first && PRE1 <= 4 && pl == ((mdp >> 20) & 15)) { ring[4] = load_main(4, ff, false); ring[5] = load_main(5, ff, false); }
            if (first && PRE1 <= 6 && pl == ((mdp >> 24) & 15)) { ring[6] = load_main(6, ff, false); ring[7] = load_main(7, ff, false); }
            if (pl == ((mdp >> 12) & 15)) ring[NFAST] = load_tail(ta[0], tm0);
            if (pl == ((mdp >> 16) & 15)) ring[NFAST + 1] = load_tail(ta[1], tm1);
            __builtin_amdgcn_sched_barrier(0);
            if (!last) load_md(ff + 1, pl, mdp);
            else if (pl == (mdp & 15)) edge_loads(me);
            __builtin_amdgcn_sched_barrier(0);
        };
        side(8);
        gather(R, 1, 1, pr);
        side(9);
        const uint32_t tcur = tc.x, spc = sp;
#pragma unroll
        for (int i = 0; i < NFAST - 1; i++) {
            gather(R, i + 2, (i + 2) % 3, pr);
            if (i == 0) side(10);
            if (i == 0 && first) WAIT_L(6); else if (i < 2) WAIT_L(9); else WAIT_L(12);
            scatter(R, i, i % 3, 0ull, pr);
            if (!last && pr && (i & 1)) ring[i - 1] = load_main(i - 1, ff + 1, nt);
            if (!last && (!pr || (i & 1))) ring[i] = load_main(i, ff + 1, nt);
            side(i);
            if (APRIO && i == (APRIO & 15)) __builtin_amdgcn_s_setprio(2);
            if (APRIO && i == ((APRIO >> 4) & 15)) __builtin_amdgcn_s_setprio(1);
            if (APRIO && i == ((APRIO >> 8) & 15)) __builtin_amdgcn_s_setprio(0);
            if (i == 0 && !last) {
                tc.x = __builtin_amdgcn_alignbit(tc.y, tc.x, 8u); tc.y = __builtin_amdgcn_alignbit(tc.z, tc.y, 8u);
                tc.z = __builtin_amdgcn_alignbit(tc.w, tc.z, 8u); tc.w >>= 8;
                sp = ff == 7 ? sp0 + NWG * 9u * 128u : sp + ((tcur & 0x3cu) << 2);
                list1(ff + 1);
            }
            __builtin_amdgcn_sched_barrier(0);
        }
        if (__builtin_expect(ntl > 16, 0)) {
            gather(R, NFAST + 1, 0, pr);
            WAIT_L(0);
            scatter(R, NFAST + 1, 0, tm1, pr);
        }
        if (__builtin_expect((__builtin_amdgcn_ballot_w64((tcur & 2u) != 0u) | (u64)(ntl > 32 ? 1 : 0)) != 0ull, 0)) {
#pragma unroll 1
            for (int e = 2; e < 6; e++) {
                int dst;
                uint32_t en;
                if (tail_rank(tcur, spc, ff, (uint32_t)e & 4u, dst, en) <= (e & 3) * 16) continue;
                const int cmp = __builtin_amdgcn_ds_permute(dst, (int)en);
                ring[NFAST + 1] = load_tail((uint32_t)__builtin_amdgcn_ds_bpermute(((lane & ~3) | (e & 3)) << 2, cmp), tm1);
                gather(R, NFAST + 1, 0, pr);
                WAIT_L(0);
                scatter(R, NFAST + 1, 0, tm1, pr);
            }
        }
        if (APRIO) __builtin_amdgcn_s_setprio(3);
        WAIT_L(3);
        ST(1)
        if (!last) {
            __builtin_amdgcn_s_barrier();
            ST(3)
            store_md();
        }
        scatter(R, NFAST - 1, 1, 0ull, pr);
        if (!last) {
            if (pr) ring[NFAST - 2] = load_main(NFAST - 2, ff + 1, nt);
            ring[NFAST - 1] = load_main(NFAST - 1, ff + 1, nt);
            WAIT_L(3);
            __builtin_amdgcn_s_barrier();
            ST(4)
        }
    };
    auto steps = [&](auto RR) __attribute__((always_inline)) {
        constexpr int R = decltype(RR)::value;
        tc = tcnt[(wv * 16 + (lane >> 2)) * 128 + j * 4 + h];
        u32x4 sv[2];
        uint32_t e0;
        if (wv < 4) {
            sv[0] = (u32x4)(0u); sv[1] = sv[0];
            if (lane < SLOTI / 256) { sv[0] = stat[tid * 2]; sv[1] = stat[tid * 2 + 1]; }
            e0 = meta[M_E0 + wv];
        }
        const u32x2 extra = *(const u32x2*)(meta + M_LOG);
        load_md(0, -1, 0);
        {
            const u32x4 bi = {BIAS, BIAS, BIAS, BIAS};
#pragma unroll
            for (int k = 0; k < 6; k++) *(LDS u32x4*)(uintptr_t)(A_ACC + (k * NT + threadIdx.x) * 16) = bi;
            if (threadIdx.x < 128) *(LDS u32x4*)(uintptr_t)(A_ACC + (6 * NT + threadIdx.x) * 16) = bi;
            __builtin_amdgcn_sched_barrier(0);
        }
#pragma unroll
        for (int i = 0; i < PRE0; i++) ring[i] = load_main(i, 0, false);
        __builtin_amdgcn_sched_barrier(0);
        if (wv == 15) asm volatile("" :: "s"(*(const uint32_t*)centers ^ *(const uint32_t*)wsbf ^ *(const uint32_t*)wrbf ^ *(const uint32_t*)wup));
        list1(0);
        if (wv < 4) {
            const uint32_t mxb = wave_max(max(max(sv[0].x & 0xffffu, sv[0].z & 0xffffu), max(sv[1].x & 0xffffu, sv[1].z & 0xffffu)));
            const uint32_t mnb = 0xffffu - (wave_max(max(max(sv[0].x, sv[0].z), max(sv[1].x, sv[1].z))) >> 16);
            const float s2 = wave_sum((__uint_as_float(sv[0].y) + __uint_as_float(sv[0].w)) + (__uint_as_float(sv[1].y) + __uint_as_float(sv[1].w)));
            const float rng = fmaxf(sqrtf(zf * s2), 2.0f * bf2f(mxb)) * 6.103515625e-05f;
            const uint32_t eb = mxb ? (__float_as_uint(rng) + 0x7fffffu) >> 23 : 127u;
            const bool ok = !mxb || (mxb < 0x4700u && eb >= 107u && eb <= 140u && mnb >= ((eb + 8u) << 7));
            if (lane == 0) {
                s_flag[8 + wv] = (eb & 0xffu) | (ok ? 0x100u : 0u) | (mxb ? 0x200u : 0u);
                s_flag[12 + wv] = __float_as_uint(__uint_as_float(((eb + e0) & 0xffu) << 23) * (1024.0f / 1023.0f));
                s_flag[wv] = 0u;
            }
        }
        store_md();
#pragma unroll
        for (int i = PRE0; i < PRE1; i++) ring[i] = load_main(i, 0, false);
        WAIT_L(0);
        __syncthreads();
        const uint32_t q0 = __builtin_amdgcn_readfirstlane(s_flag[8 + 2 * cp]), q1 = __builtin_amdgcn_readfirstlane(s_flag[9 + 2 * cp]);
        fast = (q0 & q1 & 0x100u) != 0u;
        need = (q0 >> 9) | ((q1 >> 9) << 1);
        more = (__builtin_amdgcn_readfirstlane(extra.x | extra.y) | (uint32_t)(T & 3)) != 0u;
        mg0 = (((q0 & 0xffu) + 9u) << 23) | 0x400000u;
        mg1 = ((((q1 & 0xffu) + 9u) << 23) | 0x400000u) + ((0u - (mg0 >> 16)) & 0xffffu);
        if (!fast) return;
        ST(0)
        RTS(9)
        step(RR, IC<0>(), IC<2>(), 0);
        int ff = 1;
#pragma unroll 1
        for (; ff < 15 - NTS; ff++) step(RR, IC<0>(), IC<0>(), ff);
        step(RR, IC<1>(), IC<0>(), ff++);
#pragma unroll 1
        for (; ff < 15; ff++) step(RR, IC<1>(), IC<1>(), ff);
        step(RR, IC<2>(), IC<1>(), 15);
        scatter(R, NFAST, 2, tm0, false);
    };
    auto irregular = [&](auto f) __attribute__((always_inline)) {
        const float fr = bf2f(freq[0]);
        const char* lg = recs + (size_t)LBASE * 128u;
        const uint16_t* tags = (const uint16_t*)(lg + (size_t)lcap * 128u);
        const uint32_t nl = meta[M_LOG], ns = meta[M_LOG + 1];
#pragma unroll 1
        for (uint32_t q = tid; q < nl * 8u; q += NT) {
            const uint32_t b = tags[q >> 3];
            if ((b & 127u) == (uint32_t)(h * 32 + j)) {
                const uint32_t* un = (const uint32_t*)(lg + (size_t)q * 16u);
                const uint32_t xw = un[3 ^ h];
#pragma unroll 1
                for (int k = 0; k < 3; k++) {
                    const uint32_t ww = un[k ^ h];
                    const float c = (float)((xw >> (k * 11)) & 0x3ffu) * (1.0f / 1023.0f);
                    if (!(ww & WNULL)) f(ww >> 16, ((ww & 0x3fffu) << 6) | ((ALT ? ((b >> 7) * 3u) & 15u : b >> 7) << 2) | (uint32_t)h, (ww & 0x8000u) ? -c : c);
                }
            }
        }
#pragma unroll 1
        for (uint32_t q = tid; q < ns + (uint32_t)(T & 3); q += NT) {
            const uint32_t t = q < ns ? ((const uint32_t*)tags)[-1 - (int)q] : (uint32_t)(T & ~3) + (q - ns);
            const uint32_t i = (uint32_t)iin[t], o = (uint32_t)iout[t];
            if (((o >> 6) & 31u) == (uint32_t)j && (i & 3u) == (uint32_t)h) f(((o >> 11) << 6) | (o & 63u), i, cosf(fr * bf2f(ang[t])));
        }
    };

    if (h == 0) steps(IC<0>());
    else if (h == 1) steps(IC<1>());
    else if (h == 2) steps(IC<2>());
    else steps(IC<3>());
    if (fast) {
        if (more) irregular([&](uint32_t ol, uint32_t i, float cf) __attribute__((always_inline)) {
            const uint32_t md = mdc[(cp * 64u + (i & 63u)) * SLOTI + ((i >> 14) << 8) + ((i >> 4) & 0xfcu) + ((i >> 12) & 3u)];
            const float c = cf * (1023.0f / 16777216.0f);
            const uint32_t lo = __float_as_uint(__builtin_fmaf(c, h2f(md & 0xffffu), __uint_as_float(mg0)));
            const uint32_t hi = __float_as_uint(__builtin_fmaf(c, h2f(md >> 16), __uint_as_float(mg1)));
            if (cf == cf) guard |= atomicAdd(&acc[ol], (hi << 16) + lo);
            else guard |= HSLOW;
        });
    }
    GAS unsigned* dw = (GAS unsigned*)(meta + M_DONE + j * 64);
    volatile LDS unsigned* seen = (volatile LDS unsigned*)(uintptr_t)(A_FLAG + 4);
    if (tid == NT - 64) *seen = __hip_atomic_load(dw, __ATOMIC_RELAXED, __HIP_MEMORY_SCOPE_AGENT) & 0xff00ffu;
    WAIT_L(0);
    asm volatile("v_or3_b32 %0, %0, v110, v111\n\tv_or3_b32 %0, %0, v112, v113\n\tv_or3_b32 %0, %0, v114, v115\n\tv_or3_b32 %0, %0, v116, v117\n\tv_or_b32 %0, v118, %0" : "+v"(guard));
    ST(2)
    __syncthreads();
    RTS(10)
    const __amdgpu_buffer_rsrc_t rsP = __builtin_amdgcn_make_buffer_rsrc((void*)(partial + (size_t)j * 16 * SLOTO), 0, 16 * SLOTO * 4, 0x00020000);
    const bool fin = wv < (SHARE / 4 + 63) / 64;
    u32x4 p[8];
    unsigned have = 0u;
    auto fetch = [&](unsigned v, int sh) __attribute__((always_inline)) {
        const uint32_t po = tid < SHARE / 4 ? (uint32_t)(sh * SHARE + tid * 4) * 4u : 0x7fffff00u;
        const unsigned nw = v & ~have;
#pragma unroll
        for (int r = 0; r < 8; r++)
            if ((nw >> (me ^ r)) & 1u)
                p[r] = __builtin_amdgcn_raw_buffer_load_b128(rsP, po, ((v >> (16 + (me ^ r))) & 1u) ? 16 * SLOTO * 4 : (me ^ r) * SLOTO * 4, AUX_SC1);
        have |= v;
    };
    if (fast) {
        uint32_t big = 0u;
        u32x4 v[7];
#pragma unroll
        for (int k = 0; k < 6; k++) v[k] = *(const LDS u32x4*)(uintptr_t)(A_ACC + (k * NT + tid) * 16);
        v[6] = (u32x4)(BIAS);
        if (wv < 2) v[6] = *(const LDS u32x4*)(uintptr_t)(A_ACC + (6 * NT + tid) * 16);
#pragma unroll
        for (int k = 0; k < 6; k++) __builtin_amdgcn_raw_buffer_store_b128(v[k], rsP, (uint32_t)(tid * 16 + me * SLOTO * 4 + k * NT * 16), 0, AUX_SC1);
        if (wv < 2) __builtin_amdgcn_raw_buffer_store_b128(v[6], rsP, (uint32_t)(tid * 16 + me * SLOTO * 4 + 6 * NT * 16), 0, AUX_SC1);
        __builtin_amdgcn_sched_barrier(0);
        p[0] = *(const LDS u32x4*)(uintptr_t)(A_ACC + (me * SHARE + min(tid, SHARE / 4 - 1) * 4) * 4);
        have = 1u << me;
        if (fin) fetch(__builtin_amdgcn_readfirstlane(*seen), me);
#pragma unroll
        for (int k = 0; k < 28; k++) { guard |= v[k >> 2][k & 3]; big |= v[k >> 2][k & 3] + 0x42004200u; }
        if (guard & HSLOW) s_flag[0] = 1u;
        if (big & 0x7c00u) s_flag[2] = 1u;
        if (big & 0x7c000000u) s_flag[3] = 2u;
    }
    asm volatile("s_waitcnt vmcnt(0)" ::: "memory");
    __syncthreads();
    RTS(11)
    ST(5)
    const bool slow = (ABL & 120) ? false : !fast || __builtin_amdgcn_readfirstlane(s_flag[0]) != 0u || (need & ~__builtin_amdgcn_readfirstlane(s_flag[2] | s_flag[3])) != 0u;
    if (slow) {
        float* fa = (float*)acc;
        float w[16];
        load_w(wdown, w);
#pragma unroll 1
        for (int comp = 2 * cp; comp < 2 * cp + 2; comp++) {
            __syncthreads();
            for (int i = tid; i < SLOTO; i += NT) fa[i] = 0.f;
            __syncthreads();
            auto add = [&](uint32_t ol, uint32_t i, float cf) __attribute__((always_inline)) {
                uint32_t rr[4];
                md_of(m[i], w, rr);
                const uint32_t rc = comp == 0 ? rr[0] : comp == 1 ? rr[1] : comp == 2 ? rr[2] : rr[3];
                atomicAdd(&fa[ol], cf * bf2f(rc));
            };
#pragma unroll 1
            for (uint32_t q = tid; q < 16u * NWG * ((uint32_t)ks * 8u + 24u); q += NT) {
                const uint32_t per = (uint32_t)ks * 8u + 24u;
                const uint32_t un = q & 3u, hf = (q % per) >> 2, sb = (q / per) & (NWG - 1u), ff = (q / per) / NWG;
                const uint32_t col = POS(ff) * 128u + (uint32_t)(h * 32 + j);
                u32x2 p0, p1;
                if (hf < 2u * (uint32_t)ks) {
                    const char* ln = recs + (size_t)((col >> 7) * SS + (col & 127u) * SC + (hf >> 1) * SK + sb * SR) * 128u + un * 16u + (hf & 1u) * 8u;
                    p0 = *(const u32x2*)ln; p1 = *(const u32x2*)(ln + 64);
                } else {
                    const uint32_t tl = hf - 2u * (uint32_t)ks;
                    const uint8_t* cb = (const uint8_t*)tcnt + (sb * 128u + (uint32_t)(j * 4 + h)) * 16u;
                    const char* ln = recs + (size_t)(TBASE + (tl >> 1) * TPL + col * TST + sb) * 128u + (tl & 1u) * 64u + un * 16u;
                    if (tl < 4u) { if (tl >= (cb[ff] & 3u) * 2u) continue; }
                    else {
                        uint32_t at = (tl - 4u) * 4u + un;
                        if (at >= ((cb[ff] >> 2) & 15u)) continue;
#pragma unroll 1
                        for (uint32_t t = ff & 8u; t < ff; t++) at += (cb[t] >> 2) & 15u;
                        ln = recs + (size_t)(SBASE + (((uint32_t)(h * 32 + j) * 2u + (ff >> 3)) * NWG + sb) * 9u) * 128u + at * 16u;
                    }
                    p0 = *(const u32x2*)ln; p1 = *(const u32x2*)(ln + 8);
                }
                if (h & 2) { const u32x2 t = p0; p0 = p1; p1 = t; }
                const uint32_t ws[4] = {(h & 1) ? p0.y : p0.x, (h & 1) ? p0.x : p0.y, (h & 1) ? p1.y : p1.x, (h & 1) ? p1.x : p1.y};
#pragma unroll
                for (int k = 0; k < 3; k++) {
                    const float c = (float)((ws[3] >> (k * 11)) & 0x3ffu) * (1.0f / 1023.0f);
                    if (!(ws[k] & WNULL)) add(ws[k] >> 16, ((ws[k] & 0x3fffu) << 6) | (ff * 4u + (uint32_t)h), (ws[k] & 0x8000u) ? -c : c);
                }
            }
            if (more) irregular(add);
            __syncthreads();
            for (int i = tid; i < SLOTO / 4; i += NT)
                __builtin_amdgcn_raw_buffer_store_b128(*(const LDS u32x4*)(uintptr_t)(A_ACC + i * 16), rsP, (uint32_t)(((comp & 1) * 8 + me) * SLOTO * 4 + i * 16), 0, AUX_SC1);
        }
        edge_loads(me);
#pragma unroll
        for (int k = 0; k < 8; k++) p[k] = (u32x4)(0u);
        have = 0u;
        asm volatile("s_waitcnt vmcnt(0)" ::: "memory");
        __syncthreads();
    }
    if (tid == NT - 64) {
        const unsigned inc = (slow ? 0x10001u : 1u) << me;
        unsigned v = __hip_atomic_fetch_add(dw, inc, __ATOMIC_RELAXED, __HIP_MEMORY_SCOPE_AGENT) + inc;
        if ((v & 0xffu) != 0xffu) {
            unsigned spins = 0;
            do {
                *seen = v & 0xff00ffu;
                __builtin_amdgcn_s_sleep(1);
                v = __hip_atomic_load(dw, __ATOMIC_RELAXED, __HIP_MEMORY_SCOPE_AGENT);
                if (++spins > SPINCAP && (v & 0xffu) != 0xffu) {
                    v = __hip_atomic_fetch_add(dw, 256u << me, __ATOMIC_RELAXED, __HIP_MEMORY_SCOPE_AGENT);
                    if ((v & 0xffu) != 0xffu) v = 0x800000ffu;
                }
            } while ((v & 0xffu) != 0xffu);
            v &= 0x80ff00ffu;
        }
        *seen = v;
    }
    if (!fin) return;
    factors();
    have = __builtin_amdgcn_readfirstlane(have);
    uint32_t un[4];
#pragma unroll
    for (int c = 0; c < 4; c++) un[c] = __builtin_amdgcn_readfirstlane(s_flag[12 + c]);
    unsigned sv;
    {
        const uintptr_t pb = (uintptr_t)(partial + (size_t)j * 16 * SLOTO);
        const u32x4 rsQ = {(uint32_t)pb, (uint32_t)(pb >> 32) & 0xffffu, 16u * SLOTO * 4u, 0x00020000u};
        const uint32_t po = tid < SHARE / 4 ? (uint32_t)(me * SHARE + tid * 4) * 4u : 0x7fffff00u;
        do {
            sv = __builtin_amdgcn_readfirstlane(*seen);
            uint32_t t0, t1;
#define FET(R) "s_xor_b32 %8, %12, " #R "\n\ts_bitcmp1_b32 %13, %8\n\ts_cbranch_scc0 1f\n\ts_mul_i32 %9, %8, %15\n\ts_bitcmp1_b32 %14, %8\n\t" \
               "s_cselect_b32 %9, %16, %9\n\tbuffer_load_dwordx4 %" #R ", %10, %11, %9 offen sc1\n1:\n\t"
            asm volatile("s_and_b32 %8, %13, 0xff\n\ts_cbranch_scc0 2f\n\ts_nop 2\n\t" FET(0) FET(1) FET(2) FET(3) FET(4) FET(5) FET(6) FET(7) "\n2:\n\ts_sleep 2\n\ts_waitcnt vmcnt(0)"
                         : "+v"(p[0]), "+v"(p[1]), "+v"(p[2]), "+v"(p[3]), "+v"(p[4]), "+v"(p[5]), "+v"(p[6]), "+v"(p[7]), "=&s"(t0), "=&s"(t1)
                         : "v"(po), "s"(rsQ), "s"(me), "s"((int)sv < 0 ? 0u : sv & ~have), "s"(sv >> 16), "n"(SLOTO * 4), "n"(16 * SLOTO * 4) : "memory", "scc");
            have |= sv;
        } while ((sv & 0xffu) != 0xffu);
    }
    if ((int)sv < 0) return;
    RTS(12)
    ST(6)
    const unsigned flt = (sv >> 16) & 0xffu;
    auto finish = [&](int sh) __attribute__((always_inline)) {
        {
            const int ol = sh * SHARE + tid * 4;
            const int e0 = ((ol >> 6) << 11) | (j << 6) | (ol & 63);
            const uint32_t po = tid < SHARE / 4 ? (uint32_t)ol * 4u : 0x7fffff00u;
            float g[4][4];
            u32x4 a[2] = {p[0] + p[2], p[1] + p[3]}, b[2] = {p[4] + p[6], p[5] + p[7]};
            if (cp) { const u32x4 t = a[0], u = b[0]; a[0] = a[1]; b[0] = b[1]; a[1] = t; b[1] = u; }
#pragma unroll
            for (int c = 0; c < 2; c++) {
                const int nb = 0x10000 - 0x4000 * __builtin_popcount(flt & (0x55u << c));
                const float u0 = __uint_as_float(nb ? un[2 * c] : 0u), u1 = __uint_as_float(nb ? un[2 * c + 1] : 0u);
#pragma unroll
                for (int q = 0; q < 4; q++) {
                    g[q][2 * c] = (float)((int)((a[c][q] & 0xffffu) + (b[c][q] & 0xffffu)) - nb) * u0;
                    g[q][2 * c + 1] = (float)((int)((a[c][q] >> 16) + (b[c][q] >> 16)) - nb) * u1;
                }
            }
            if (__builtin_expect(flt != 0u, 0)) {
#pragma unroll
                for (int c = 0; c < 2; c++) {
#pragma unroll 1
                    for (int k = c; k < 8; k += 2) {
                        if (!((flt >> k) & 1u)) continue;
                        const u32x4 f0 = __builtin_amdgcn_raw_buffer_load_b128(rsP, po, k * SLOTO * 4, AUX_SC1);
                        const u32x4 f1 = __builtin_amdgcn_raw_buffer_load_b128(rsP, po, (8 + k) * SLOTO * 4, AUX_SC1);
#pragma unroll
                        for (int q = 0; q < 4; q++) { g[q][2 * c] += __uint_as_float(f0[q]); g[q][2 * c + 1] += __uint_as_float(f1[q]); }
                    }
                }
            }
            float wu[16];
            load_w(wup, wu);
            u32x4 ov[2];
#pragma unroll
            for (int q = 0; q < 4; q++) {
#pragma unroll
                for (int k = 0; k < 4; k++) g[q][k] *= rs[q][k];
                const uint32_t m0 = em[q >> 1][(q & 1) * 2], m1 = em[q >> 1][(q & 1) * 2 + 1];
                const float mm[4] = {bf2f(m0 & 0xffff), bf2f(m0 >> 16), bf2f(m1 & 0xffff), bf2f(m1 >> 16)};
                float o[4];
#pragma unroll
                for (int k = 0; k < 4; k++) o[k] = mm[k] + (g[q][0] * wu[k] + g[q][1] * wu[4 + k] + g[q][2] * wu[8 + k] + g[q][3] * wu[12 + k]) * rg[q][k];
                ov[q >> 1][(q & 1) * 2] = pk_bf16(o[0], o[1]);
                ov[q >> 1][(q & 1) * 2 + 1] = pk_bf16(o[2], o[3]);
            }
            const __amdgpu_buffer_rsrc_t rsU = __builtin_amdgcn_make_buffer_rsrc((void*)out, 0, E * 8, 0x00020000);
            const uint32_t uo = tid < SHARE / 4 ? (uint32_t)e0 * 8u : 0x7fffff00u;
            __builtin_amdgcn_raw_buffer_store_b128(ov[0], rsU, uo, 0, OAUX);
            __builtin_amdgcn_raw_buffer_store_b128(ov[1], rsU, uo + 16u, 0, OAUX);
        }
    };
    finish(me);
#pragma unroll 1
    for (unsigned todo = (sv >> 8) & 0xffu; todo; todo &= todo - 1u) {
        const int sh = __builtin_ctz(todo);
        edge_loads(sh);
        factors();
        have = 0u;
        fetch(sv, sh);
        finish(sh);
    }
    RTS(13)
    ST(7)
    ST_OUT(8)
}

__global__ __launch_bounds__(256) void gen_scatter_kernel(const int* __restrict__ iin, const int* __restrict__ iout,
                                                          const uint16_t* __restrict__ ang, const uint16_t* __restrict__ freq,
                                                          const u32x2* __restrict__ m, const uint16_t* __restrict__ wdown,
                                                          float* __restrict__ agg, int T) {
    float w[16];
    load_w(wdown, w);
    const float fr = bf2f(freq[0]);
    for (long t = (long)blockIdx.x * 256 + threadIdx.x; t < T; t += (long)gridDim.x * 256) {
        const float c = cosf(fr * bf2f(ang[t]));
        uint32_t r[4];
        md_of(m[iin[t]], w, r);
        float* q = agg + (size_t)iout[t] * 4;
#pragma unroll
        for (int k = 0; k < 4; k++) atomicAdd(q + k, c * bf2f(r[k]));
    }
}
__global__ __launch_bounds__(256) void gen_final_kernel(const u32x2* __restrict__ m, const uint16_t* __restrict__ dist,
                                                        const uint16_t* __restrict__ centers, const uint16_t* __restrict__ wrbf,
                                                        const uint16_t* __restrict__ wsbf, const uint16_t* __restrict__ wup,
                                                        const float* __restrict__ agg, u32x2* __restrict__ out, int E) {
    const int e = blockIdx.x * 256 + threadIdx.x;
    if (e >= E) return;
    const float d = bf2f(dist[e]);
    float rs[4] = {0, 0, 0, 0}, rg[4] = {0, 0, 0, 0};
#pragma unroll 1
    for (int r = 0; r < 8; r++) {
        const float x = d - bf2f(centers[r]);
        const float rb = __expf(-10.0f * x * x);
#pragma unroll
        for (int k = 0; k < 4; k++) { rs[k] += rb * bf2f(wsbf[r * 4 + k]); rg[k] += rb * bf2f(wrbf[r * 4 + k]); }
    }
    float wu[16];
    load_w(wup, wu);
    const u32x2 mv = m[e];
    const float mm[4] = {bf2f(mv.x & 0xffff), bf2f(mv.x >> 16), bf2f(mv.y & 0xffff), bf2f(mv.y >> 16)};
    float g[4], o[4];
#pragma unroll
    for (int k = 0; k < 4; k++) g[k] = agg[(size_t)e * 4 + k] * rs[k];
#pragma unroll
    for (int k = 0; k < 4; k++) o[k] = mm[k] + (g[0] * wu[k] + g[1] * wu[4 + k] + g[2] * wu[8 + k] + g[3] * wu[12 + k]) * rg[k];
    const u32x2 ov = {f2bf(o[0]) | (f2bf(o[1]) << 16), f2bf(o[2]) | (f2bf(o[3]) << 16)};
    out[e] = ov;
}

extern "C" void kernel_launch(void* const* d_in, const int* in_sizes, int n_in,
                              void* d_out, int out_size, void* d_ws, size_t ws_size,
                              hipStream_t stream) {
    const u32x2* m = (const u32x2*)d_in[0];
    const int* iin = (const int*)d_in[1];
    const int* iout = (const int*)d_in[2];
    const uint16_t* dist = (const uint16_t*)d_in[3];
    const uint16_t* ang = (const uint16_t*)d_in[4];
    const uint16_t* centers = (const uint16_t*)d_in[5];
    const uint16_t* freq = (const uint16_t*)d_in[6];
    const uint16_t* wrbf = (const uint16_t*)d_in[7];
    const uint16_t* wsbf = (const uint16_t*)d_in[8];
    const uint16_t* wdown = (const uint16_t*)d_in[9];
    const uint16_t* wup = (const uint16_t*)d_in[10];
    const int E = in_sizes[3];
    const int T = in_sizes[1];
    char* ws = (char*)d_ws;
    const int G = T >> 2;
    const int ntile = (G + GR - 1) / GR;
    const int nstat = ntile / NWG - NDYN > 4 ? ntile / NWG - NDYN : 4;
    const int lcap = G / 6 + 2;
    char* recs = ws + W_RECS;
    size_t off = W_RECS;
    const size_t rbytes = (size_t)LBASE * 128 + (size_t)lcap * 130;
    off += rbytes;
    if (E > EMAX || (E & 3) || E < 1 || rbytes > 0x70000000u || off > ws_size) {
        (void)hipMemsetAsync(ws, 0, (size_t)E * 16, stream);
        gen_scatter_kernel<<<4096, 256, 0, stream>>>(iin, iout, ang, freq, m, wdown, (float*)ws, T);
        gen_final_kernel<<<(E + 255) / 256, 256, 0, stream>>>(m, dist, centers, wrbf, wsbf, wup, (const float*)ws, (u32x2*)d_out, E);
        return;
    }
    int ks = T / (NWG * NB * 24);
    if (ks > KMAX) ks = KMAX;
    const float zf = ZSIG * ZSIG * (float)T / (8.0f * (float)E * (float)E);
    (void)hipFuncSetAttribute((const void*)part_kernel, hipFuncAttributeMaxDynamicSharedMemorySize, P_LDS);
    (void)hipFuncSetAttribute((const void*)acc_kernel, hipFuncAttributeMaxDynamicSharedMemorySize, A_LDS);
    part_kernel<<<NWG, NT, P_LDS, stream>>>(iin, iout, ang, freq, m, wdown, recs, T, E, ntile, nstat, ks, lcap);
    if (!(ABL & 7)) acc_kernel<<<NWG, NT, A_LDS, stream>>>(ws, T, E, ks, lcap, zf, 0, centers, wrbf, wsbf, wup, m, dist, (u32x2*)d_out, iin, iout, ang, wdown, freq);
}
```

```cpp
#include <hip/hip_runtime.h>
#include <hip/hip_bf16.h>
#include <stdint.h>
#pragma clang diagnostic ignored "-Winline-asm"

typedef unsigned int u32x4 __attribute__((ext_vector_type(4)));
typedef unsigned int u32x2 __attribute__((ext_vector_type(2)));
typedef unsigned short u16x2 __attribute__((ext_vector_type(2)));
typedef _Float16 f16x2 __attribute__((ext_vector_type(2)));
typedef __bf16 bf16x2 __attribute__((ext_vector_type(2)));
typedef float f32x2 __attribute__((ext_vector_type(2)));
typedef unsigned long long u64;

static __device__ __forceinline__ float bf2f(uint32_t bits16) { return __uint_as_float(bits16 << 16); }
static __device__ __forceinline__ uint32_t f2bf(float f) {
    uint32_t u = __float_as_uint(f);
    if ((u & 0x7fffffffu) > 0x7f800000u) return (u >> 16) | 0x40;
    return (u + 0x7fffu + ((u >> 16) & 1u)) >> 16;
}
static __device__ __forceinline__ float h2f(uint32_t bits16) { return (float)__builtin_bit_cast(_Float16, (unsigned short)bits16); }

#define NB 2048
#define WNULL 0x80000000u
#define CSTEP 0x2aabu
#define CINIT 205u
#define CHV (12u * CSTEP)
#define SLOTO 25088
#define SLOTI 12544
#define EMAX (SLOTO * 32)
#define NT 1024
#define NWG 256
#define KMAX 4
#define NTAIL 4
#define LAY 3
#define SC 257u
#define SR 1u
#define SK (128u * SC)
#define SS (4u * SK)
#define MAINL (16u * SS)
#define UINC (0x80000001u + SK * 64u)
#define TST 257u
#define TBASE MAINL
#define TPL (NB * TST)
#define SBASE (TBASE + 2u * TPL)
#define LBASE (SBASE + 128u * 2u * NWG * 9u)
#define NDYN 3
#define GR 2048
#define NTS 5
#define M_DONE 0
#define M_TILE (32 * 64)
#define M_LOG (M_TILE + 64)
#define M_MD (M_LOG + 64)
#define M_BEAT (M_MD + 64)
#define M_E0 (M_BEAT + 64)
#define M_WORDS (M_E0 + 64)
#define W_TCNT (M_WORDS * 4)
#define W_STAT (W_TCNT + NWG * NB)
#define W_MDC (W_STAT + 4 * NWG * 8)
#define W_PART (W_MDC + 128 * SLOTI * 4)
#define W_RECS (W_PART + 32 * 16 * SLOTO * 4)
#define TSC1 0
#define ALT 0
#define POS(ff) (ALT ? ((ff) * 11) & 15 : (ff))
#define AUX_NT 2
#define AUX_SC1 16

static __device__ __forceinline__ uint32_t row_max(uint32_t x) {
    x = max(x, (uint32_t)__builtin_amdgcn_update_dpp(0, x, 0x111, 0xf, 0xf, false));
    x = max(x, (uint32_t)__builtin_amdgcn_update_dpp(0, x, 0x112, 0xf, 0xf, false));
    x = max(x, (uint32_t)__builtin_amdgcn_update_dpp(0, x, 0x114, 0xf, 0xf, false));
    return max(x, (uint32_t)__builtin_amdgcn_update_dpp(0, x, 0x118, 0xf, 0xf, false));
}
static __device__ __forceinline__ uint32_t wave_max(uint32_t x) {
    x = row_max(x);
    x = max(x, (uint32_t)__builtin_amdgcn_update_dpp(0, x, 0x142, 0xa, 0xf, false));
    x = max(x, (uint32_t)__builtin_amdgcn_update_dpp(0, x, 0x143, 0xc, 0xf, false));
    return __builtin_amdgcn_readlane(x, 63);
}
static __device__ __forceinline__ float row_sum(float x) {
    x += __int_as_float(__builtin_amdgcn_update_dpp(0, __float_as_int(x), 0x111, 0xf, 0xf, false));
    x += __int_as_float(__builtin_amdgcn_update_dpp(0, __float_as_int(x), 0x112, 0xf, 0xf, false));
    x += __int_as_float(__builtin_amdgcn_update_dpp(0, __float_as_int(x), 0x114, 0xf, 0xf, false));
    return x + __int_as_float(__builtin_amdgcn_update_dpp(0, __float_as_int(x), 0x118, 0xf, 0xf, false));
}
static __device__ __forceinline__ float wave_sum(float x) {
    x = row_sum(x);
    x += __int_as_float(__builtin_amdgcn_update_dpp(0, __float_as_int(x), 0x142, 0xa, 0xf, false));
    x += __int_as_float(__builtin_amdgcn_update_dpp(0, __float_as_int(x), 0x143, 0xc, 0xf, false));
    return __int_as_float(__builtin_amdgcn_readlane(__float_as_int(x), 63));
}
static __device__ __forceinline__ uint32_t bfi(uint32_t mask, uint32_t a, uint32_t b) {
    return __builtin_amdgcn_bitop3_b32(mask, a, b, 0xca);
}
static __device__ __forceinline__ void opaque(int& x) { asm volatile("" : "+v"(x)); }
template <uint32_t C> static __device__ __forceinline__ uint32_t vconst() {
    uint32_t r;
    asm volatile("v_mov_b32 %0, %1" : "=v"(r) : "n"(C));
    return r;
}
static __device__ __forceinline__ uint32_t pk_bf16(float a, float b) {
    const f32x2 v = {a, b};
    return __builtin_bit_cast(uint32_t, __builtin_convertvector(v, bf16x2));
}
static __device__ __forceinline__ void pin(uint32_t& x) { asm volatile("" : "+v"(x)); }

#define RTS(k)
#define ST_DECL
#define ST(i)
#define ST_OUT(off)

static __device__ __forceinline__ void md_of(u32x2 v, const float* w, uint32_t* r) {
    float a0 = bf2f(v.x & 0xffff), a1 = bf2f(v.x >> 16), a2 = bf2f(v.y & 0xffff), a3 = bf2f(v.y >> 16);
#pragma unroll
    for (int j = 0; j < 4; j++) r[j] = f2bf(a0 * w[j] + a1 * w[4 + j] + a2 * w[8 + j] + a3 * w[12 + j]);
}
static __device__ __forceinline__ void load_w(const uint16_t* __restrict__ wdown, float* w) {
    u32x4 wv0 = *(const u32x4*)wdown, wv1 = *(const u32x4*)(wdown + 8);
#pragma unroll
    for (int i = 0; i < 4; i++) {
        w[2 * i] = bf2f(wv0[i] & 0xffffu); w[2 * i + 1] = bf2f(wv0[i] >> 16);
        w[8 + 2 * i] = bf2f(wv1[i] & 0xffffu); w[8 + 2 * i + 1] = bf2f(wv1[i] >> 16);
    }
}
#define LDS __attribute__((address_space(3)))
#define P_CNT 0
#define P_FLAG ((NB + 64) * 4)
#define P_LST (P_FLAG + 256)
#define LCAP 32
#define P_TAB (P_LST + 4 * LCAP * 16)
#define P_BUF (P_TAB + NB * 4)
#define P_LDS (P_BUF + NB * 64)
#define PD 2
#define LPOS 0x448763
#define ABL 0
#define PRIO 2
#define SETP(n) __builtin_amdgcn_s_setprio(n)
#define MDL 3
#define RMAXREV 40.0f
#define HB 88
#define HREG(S, D) (HB + ((S) & 3) * 10 + (((S) >> 2) + (D)) * 2)

template <int N> struct IC { static constexpr int value = N; };
struct Raw { u32x4 o[2], i[2]; u32x2 a[2]; };
static __device__ __forceinline__ uint32_t lds_ld(uint32_t a) { return *(const LDS uint32_t*)(uintptr_t)a; }
static __device__ __forceinline__ void lds_st(uint32_t a, uint32_t v) { *(LDS uint32_t*)(uintptr_t)a = v; }
static __device__ __forceinline__ void lds_or(uint32_t a, uint32_t v) {
    __hip_atomic_fetch_or((LDS uint32_t*)(uintptr_t)a, v, __ATOMIC_RELAXED, __HIP_MEMORY_SCOPE_WORKGROUP);
}
static __device__ __forceinline__ uint32_t lds_add(uint32_t a, uint32_t v) {
    return __hip_atomic_fetch_add((LDS uint32_t*)(uintptr_t)a, v, __ATOMIC_RELAXED, __HIP_MEMORY_SCOPE_WORKGROUP);
}
static __device__ __forceinline__ uint32_t slot_w(uint32_t ca, uint32_t cv) {
    return (ca << 4) | __builtin_amdgcn_bitop3_b32(cv >> 11, ca, 0x3cu, 0x28);
}
static __device__ __forceinline__ uint32_t slot_x2(uint32_t aw, uint32_t ca) {
    return __builtin_amdgcn_bitop3_b32(aw, ca, 0xcu, 0x72);
}
static __device__ __forceinline__ uint32_t slot_x(uint32_t aw) { return slot_x2(aw, aw >> 4); }
template <int R> static __device__ __forceinline__ u32x2 hget() {
    u32x2 d;
    asm volatile("v_mov_b64 %0, v[%1:%2]" : "=v"(d) : "n"(R), "n"(R + 1));
    return d;
}
static __device__ __forceinline__ u32x2 hget_at(int r) {
    u32x2 d;
    asm volatile("s_set_gpr_idx_on %2, gpr_idx(SRC0)\n\ts_nop 1\n\tv_mov_b32 %0, v88\n\tv_mov_b32 %1, v89\n\ts_set_gpr_idx_off"
                 : "=&v"(d.x), "=&v"(d.y) : "s"(r));
    return d;
}
static __device__ __forceinline__ void hset_at(int r, u32x2 d) {
    asm volatile("s_set_gpr_idx_on %2, gpr_idx(DST)\n\ts_nop 1\n\tv_mov_b32 v88, %0\n\tv_mov_b32 v89, %1\n\ts_set_gpr_idx_off"
                 :: "v"(d.x), "v"(d.y), "s"(r));
}

template <int S, int D> static __device__ __forceinline__ uint32_t take(uint32_t V, uint32_t u16, uint32_t la, u32x2 nul) {
    const uint32_t a = __builtin_amdgcn_bitop3_b32(V, u16, 15u, 0x54);
    asm volatile("v_mov_b64 v[%4:%4+1], v[%3:%3+1]\n\tv_cmpx_ne_u32 vcc, -16, %0\n\tds_wrxchg_rtn_b64 v[%4:%4+1], %1, %2 offset:%5\n\ts_mov_b64 exec, -1"
                 :: "v"(a), "v"(la), "v"(nul), "n"(HREG(S, D)), "n"(HREG(S, 1 - D)), "n"(S * 256) : "memory", "vcc");
    return a;
}
#define SK0 "s_bitcmp1_b32 %3, %4\n\ts_cbranch_scc0 1f\n\t"
#define SK1 "\n1:"
template <int S, int W> static __device__ __forceinline__ void put(u32x4 rsA, uint32_t ad, uint32_t pm) {
    if (POS(S) >= 16 - NTS) asm volatile(SK0 "s_waitcnt lgkmcnt(%5)\n\tbuffer_store_dwordx4 v[%2:%2+3], %0, %1, 0 offen nt\n\ts_nop 1" SK1 :: "v"(ad), "s"(rsA), "n"(HREG(S, 0)), "s"(pm), "n"(2 * S), "n"(W) : "memory", "scc");
    else asm volatile(SK0 "s_waitcnt lgkmcnt(%5)\n\tbuffer_store_dwordx4 v[%2:%2+3], %0, %1, 0 offen sc1\n\ts_nop 1" SK1 :: "v"(ad), "s"(rsA), "n"(HREG(S, 0)), "s"(pm), "n"(2 * S), "n"(W) : "memory", "scc");
}

__global__ __launch_bounds__(NT) __attribute__((amdgpu_num_vgpr(HB / 2))) void part_kernel(const int* __restrict__ iin, const int* __restrict__ iout,
                                                  const uint16_t* __restrict__ ang, const uint16_t* __restrict__ freq,
                                                  const u32x2* __restrict__ m, const uint16_t* __restrict__ wdown,
                                                  char* __restrict__ recs,
                                                  int T, int E, int ntile, int nstat, int ks, int lcap) {
    ST_DECL
    RTS(0)
    const int tid0 = threadIdx.x;
    const int wv = __builtin_amdgcn_readfirstlane(tid0 >> 6);
    const int G = T >> 2;
    const int pos = (int)((blockIdx.x & 7) << 5) | (int)(blockIdx.x >> 3);
    float frr;
    unsigned* meta = (unsigned*)(recs - W_RECS);
    const uint32_t gbase = (LBASE + (uint32_t)lcap) * 128u;
    const uint32_t rbytes = gbase + (uint32_t)lcap * 2u;
    const __amdgpu_buffer_rsrc_t rsR = __builtin_amdgcn_make_buffer_rsrc(recs, 0, (int)rbytes, 0x00020000);
    const u32x4 rsA = {(uint32_t)(uintptr_t)recs, (uint32_t)((uintptr_t)recs >> 32) & 0xffffu, (ABL & 2) ? 0u : rbytes, 0x00020000u};
    const uint32_t g2 = 2u * (uint32_t)ks;
    const uint32_t spec0 = gbase - 4u;
    auto fixed_tile = [&](int r) __attribute__((always_inline)) -> int { return r * NWG + ((pos + r * 37) & (NWG - 1)); };
    int tl0 = fixed_tile(0), tl1 = fixed_tile(1), tl2;
    uint32_t tk = 0u;
    const __amdgpu_buffer_rsrc_t rsO = __builtin_amdgcn_make_buffer_rsrc((void*)iout, 0, G * 16, 0x00020000);
    const __amdgpu_buffer_rsrc_t rsI = __builtin_amdgcn_make_buffer_rsrc((void*)iin, 0, G * 16, 0x00020000);
    const __amdgpu_buffer_rsrc_t rsG = __builtin_amdgcn_make_buffer_rsrc((void*)ang, 0, G * 8, 0x00020000);
    uint32_t t8 = (uint32_t)tid0 * 8u;
    auto load_one = [&](Raw& r, int tl, int idx) __attribute__((always_inline)) {
        const int j = idx < 4 ? idx >> 1 : idx & 1;
        const uint32_t g0 = (uint32_t)(((ABL & 1) ? tl & 31 : tl) * GR + j * NT);
        __builtin_amdgcn_sched_barrier(0);
        if (idx >= 4) r.a[j] = __builtin_amdgcn_raw_buffer_load_b64(rsG, t8, g0 * 8u, (ABL & 1) ? 0 : AUX_NT);
        else if (idx & 1) r.i[j] = __builtin_amdgcn_raw_buffer_load_b128(rsI, t8 + t8, g0 * 16u, (ABL & 1) ? 0 : AUX_NT);
        else r.o[j] = __builtin_amdgcn_raw_buffer_load_b128(rsO, t8 + t8, g0 * 16u, (ABL & 1) ? 0 : AUX_NT);
        __builtin_amdgcn_sched_barrier(0);
    };
    auto peek = [&](uint32_t at) __attribute__((always_inline)) -> uint32_t {
        return __hip_atomic_load((unsigned*)((char*)meta + at), __ATOMIC_RELAXED, __HIP_MEMORY_SCOPE_AGENT);
    };
    auto ready = [&]() __attribute__((always_inline)) {
        if (blockIdx.x != 0) {
            const uint32_t x = lds_ld(vconst<P_FLAG + 8>());
            while (peek(vconst<M_BEAT * 4>()) == x) __builtin_amdgcn_s_sleep(8);
        }
    };
    auto beat = [&]() __attribute__((always_inline)) {
        (void)__hip_atomic_fetch_add((unsigned*)((char*)meta + vconst<M_BEAT * 4>()), vconst<1>(), __ATOMIC_RELAXED, __HIP_MEMORY_SCOPE_AGENT);
    };
    auto draw = [&](uint32_t at) __attribute__((always_inline)) -> uint32_t {
        return __hip_atomic_fetch_add((unsigned*)((char*)meta + at), vconst<1>(), __ATOMIC_RELAXED, __HIP_MEMORY_SCOPE_AGENT);
    };
    uint32_t la;
    auto tab_at = [&]() __attribute__((always_inline)) -> uint32_t {
        return P_TAB + bfi(0xffu, bfi(0x3cu, t8 >> 2, t8 << 4), t8);
    };
    u32x2 nul;
    uint32_t U[2];
    {
        const uint32_t t = (uint32_t)tid0;
        la = P_BUF + ((t >> 5) << 12) + ((t & 0x18u) << 3) + ((t & 3u) << 4) + ((t & 4u) << 1);
        const uint32_t q = ((t >> 3) ^ (t >> 1 & 2u)) & 3u;
        nul.x = (0x7u >> q) << 31; nul.y = (0xbu >> q) << 31;
        U[0] = (POS((t >> 1) & 15u) * SS + ((t & 1u) * 64u + (t >> 5)) * SC + blockIdx.x * SR) * 128u + (0x80000000u - SK * 64u) + 8u - g2;
        U[1] = U[0] + 32u * SC * 128u;
    }
    auto log_line = [&](bool lg, uint32_t b) __attribute__((always_inline)) -> uint32_t {
        uint32_t lp = 0u;
        if (lg && (t8 & 0x38u) == 0u) {
            ready();
            lp = draw(vconst<M_LOG * 4>());
            __builtin_amdgcn_raw_buffer_store_b16((uint16_t)b, rsR, gbase + lp * 2u, 0, 0);
        }
        return LBASE + (uint32_t)__builtin_amdgcn_ds_bpermute((int)((t8 >> 1) & 0xe0u), (int)lp);
    };
    auto general = [&](int D, bool all, u64 sm) __attribute__((always_inline)) {
        uint32_t todo = __builtin_amdgcn_readfirstlane((uint32_t)sm | (uint32_t)(sm >> 32));
#pragma unroll 1
        while (todo) {
            const uint32_t S = (uint32_t)__builtin_ctz(todo) >> 1;
            todo &= ~(3u << (2u * S));
            pin(t8); pin(la);
            const uint32_t V = lds_ld(P_TAB + (t8 & ~63u) + S * 4u), g = V & 15u;
            const uint32_t col = POS(S) * 128u + ((t8 >> 1) & 0x60u) + (t8 >> 8);
            const bool act = all ? V != 0xffffffffu : (V & 0x80000009u) == 9u;
            const int hr = HREG((int)S, D) - HB;
            uint32_t ln = log_line(act && g > 8u + NTAIL, col);
            if (act) {
                const u32x2 N = *(const LDS u32x2*)(uintptr_t)(la + S * 256u);
                *(LDS u32x2*)(uintptr_t)(la + S * 256u) = nul;
                lds_st(P_TAB + (t8 & ~63u) + S * 4u, vconst<0xffffffffu>());
                if (!(g & 1u)) hset_at(hr, N);
                else {
                    uint32_t a, st;
                    if (g < 8u) { a = (V & ~15u) + ((t8 & 0x38u) << 1); st = 8u; }
                    else {
                        if (g <= 8u + NTAIL) ln = TBASE + ((g - 8u) >> 1) * TPL + col * TST + blockIdx.x;
                        a = ln * 128u + (la & 0x38u); st = 64u;
                    }
                    __builtin_amdgcn_raw_buffer_store_b64(hget_at(hr), rsR, a, 0, AUX_SC1);
                    __builtin_amdgcn_raw_buffer_store_b64(N, rsR, a + st, 0, AUX_SC1);
                }
            }
        }
    };
    unsigned epoch = 0;
    u64 pb2 = 0ull;
    u64 fm = 0ull, sm = 0ull;
    auto look = [&](u32x2 c) __attribute__((always_inline)) {
        uint32_t V[2];
        const bool late = __builtin_amdgcn_ballot_w64(((U[0] | U[1]) & 8u) != 0u) != 0ull;
        epoch++;
        asm volatile("v_mov_b32 %0, -1\n\tv_mov_b32 %1, -1\n\t"
                     "v_cmpx_le_u32 %6, %7, %2\n\tv_mov_b32 %0, %4\n\tv_add_u32 %4, %9, %4\n\tv_subrev_u32 %2, %8, %2\n\ts_mov_b64 exec, -1\n\t"
                     "v_cmpx_le_u32 vcc, %8, %3\n\tv_mov_b32 %1, %5\n\tv_add_u32 %5, %9, %5\n\tv_subrev_u32 %3, %8, %3\n\t"
                     "s_or_b64 %6, %6, exec\n\ts_mov_b64 exec, -1"
                     : "=&v"(V[0]), "=&v"(V[1]), "+v"(c.x), "+v"(c.y), "+v"(U[0]), "+v"(U[1]), "=&s"(fm) : "s"(CHV), "n"(CHV), "n"(UINC) : "vcc", "scc");
        *(LDS u32x2*)(uintptr_t)(P_CNT + t8) = c;
        pin(t8);
        const uint32_t tw = tab_at();
        lds_st(tw, V[0]);
        lds_st(tw + 64u, V[1]);
        if (wv == 1) lds_st(P_CNT + NB * 4 - 256 + (t8 >> 1), vconst<0x40000000u>());
        const u64 pb = __builtin_amdgcn_ballot_w64((int)(V[0] & V[1]) >= 0);
        pb2 = (ABL & 4) ? 0ull : pb;
        sm = 0ull;
        if (__builtin_expect(late, 0)) {
            sm = __builtin_amdgcn_ballot_w64((V[0] & 0x80000009u) == 9u || (V[1] & 0x80000009u) == 9u);
#pragma unroll
            for (int e = 0; e < 2; e++)
                if ((V[e] & 0x8000000fu) == 9u + NTAIL) U[e] -= 2u * UINC;
        }
    };
    u32x4 Va, Vb;

    Raw raw[PD];
    {
        const int tid = tid0;
        asm volatile("" ::: "v88", "v127");
        const uint16_t fq = freq[0];
        if (tid == 128) tk = peek(vconst<M_BEAT * 4>());
        if (blockIdx.x == 0 && wv == 3 && (tid & 63) < M_BEAT / 64) {
            __hip_atomic_store(meta + (tid & 63) * 64, 0u, __ATOMIC_RELAXED, __HIP_MEMORY_SCOPE_AGENT);
            __hip_atomic_store(meta + (tid & 63) * 64 + 1, 0u, __ATOMIC_RELAXED, __HIP_MEMORY_SCOPE_AGENT);
        }
        const __amdgpu_buffer_rsrc_t rsE = __builtin_amdgcn_make_buffer_rsrc((void*)m, 0, E * 8, 0x00020000);
        u32x2 mv[5];
        mv[4] = __builtin_amdgcn_raw_buffer_load_b64(rsE, (t8 & 511u) * (uint32_t)(E >> 6), 0, 0);
#pragma unroll
        for (int q = 0; q < 4; q++)
            mv[q] = __builtin_amdgcn_raw_buffer_load_b64(rsE, t8 & 511u, ((blockIdx.x >> 2) * 256u + (blockIdx.x & 3u) * 16u + (uint32_t)wv + 64u * q) * 512u, AUX_NT);
#pragma unroll
        for (int d = 0; d < MDL; d++) load_one(raw[1], tl0, d);
        const u32x2 ci = {CINIT, CINIT};
        *(LDS u32x2*)(uintptr_t)(P_CNT + tid * 8) = ci;
        if (tid < 64) lds_st(P_CNT + (NB + tid) * 4, 0x40000000u);
        if (tid < 8 && tid != 2) lds_st(P_FLAG + tid * 4, tid == 0 ? 0xffffffffu : 0u);
        if (tid == 128) lds_st(P_FLAG + 8, tk);
        asm("s_nop 0\n\tv_readfirstlane_b32 %0, %1" : "=s"(frr) : "v"(bf2f(fq) * 0.15915494309189535f));
        const uint32_t lane = (uint32_t)tid & 63u;
        int e0[4];
        if (blockIdx.x < SLOTI / 64) {
            float w[16];
            load_w(wdown, w);
            auto dot = [&](u32x2 v, int c) __attribute__((always_inline)) -> f32x2 {
                const f32x2 w0 = {w[c], w[c + 1]}, w1 = {w[4 + c], w[5 + c]}, w2 = {w[8 + c], w[9 + c]}, w3 = {w[12 + c], w[13 + c]};
                return w0 * bf2f(v.x & 0xffffu) + w1 * bf2f(v.x >> 16) + w2 * bf2f(v.y & 0xffffu) + w3 * bf2f(v.y >> 16);
            };
#pragma unroll
            for (int cp = 0; cp < 2; cp++) {
                const f32x2 sd = dot(mv[4], 2 * cp);
#pragma unroll
                for (int e = 0; e < 2; e++) {
                    const int ex = (int)(wave_max(__float_as_uint(sd[e]) & 0x7fffffffu) >> 23);
                    e0[2 * cp + e] = __builtin_amdgcn_readfirstlane(ex == 0 ? 0 : min(max(ex - 129, -100), 100));
                }
                const f32x2 sc = {__uint_as_float((uint32_t)(127 - e0[2 * cp]) << 23), __uint_as_float((uint32_t)(127 - e0[2 * cp + 1]) << 23)};
                u32x4 o;
                u16x2 mx = {0, 0};
                f32x2 sq = {0.f, 0.f};
#pragma unroll
                for (int q = 0; q < 4; q++) {
                    const f32x2 y = dot(mv[q], 2 * cp) * sc;
                    const uint32_t hb = pk_bf16(y.x, y.y);
                    const f32x2 x = {__uint_as_float(hb << 16), __uint_as_float(hb & 0xffff0000u)};
                    o[q] = __builtin_bit_cast(uint32_t, __builtin_amdgcn_cvt_pkrtz(x.x, x.y));
                    mx = __builtin_elementwise_max(mx, __builtin_bit_cast(u16x2, hb & 0x7fff7fffu));
                    sq += x * x;
                }
                *(LDS u32x4*)(uintptr_t)(P_BUF + (cp * 64u + lane) * 272u + wv * 16) = o;
#pragma unroll
                for (int e = 0; e < 2; e++) {
                    const u32x2 st = {wave_max(mx[e]), __float_as_uint(wave_sum(sq[e]))};
                    if (lane == 0u) *(LDS u32x2*)(uintptr_t)(P_LST + ((2 * cp + e) * 16 + wv) * 8) = st;
                }
            }
        }
#pragma unroll
        for (int d = MDL; d < 6; d++) load_one(raw[1], tl0, d);
#pragma unroll
        for (int d = 0; d < 6; d++)
            if (((LPOS >> (4 * d)) & 15) < 4) load_one(raw[0], tl1, d);
        __syncthreads();
        u32x4 mo[2];
#pragma unroll
        for (int cp = 0; cp < 2; cp++) mo[cp] = *(const LDS u32x4*)(uintptr_t)(P_BUF + (cp * 64u + (t8 >> 7)) * 272u + ((t8 + t8) & 0xf0u));
        if (blockIdx.x == 0 && wv == 3) asm volatile("s_waitcnt vmcnt(0)" ::: "memory");
        __syncthreads();
#pragma unroll
        for (int s = 0; s < 16; s++) *(LDS u32x2*)(uintptr_t)(la + s * 256) = nul;
        asm volatile("" : "+v"(raw[1].o[0]), "+v"(raw[1].o[1]), "+v"(raw[1].i[0]), "+v"(raw[1].i[1]), "+v"(raw[1].a[0]), "+v"(raw[1].a[1]));
        if (blockIdx.x < SLOTI / 64) {
            const __amdgpu_buffer_rsrc_t rsM = __builtin_amdgcn_make_buffer_rsrc((void*)(meta + W_MDC / 4), 0, (int)(128u * SLOTI * 4u), 0x00020000);
#pragma unroll
            for (int cp = 0; cp < 2; cp++) {
                const uint32_t row = cp * 64u + (t8 >> 7), c16 = (t8 + t8) & 0xf0u;
                __builtin_amdgcn_raw_buffer_store_b128(mo[cp], rsM, row * (SLOTI * 4u) + c16 + blockIdx.x * 256u, 0, AUX_SC1);
            }
            if (wv == 0) {
                const u32x2 st = *(const LDS u32x2*)(uintptr_t)(P_LST + t8);
                const u32x2 r = {row_max(st.x) | (row_max(st.x ? 0xffffu - st.x : 0u) << 16), __float_as_uint(row_sum(__uint_as_float(st.y)))};
                if ((lane & 15u) == 15u) ((u32x2*)(meta + W_STAT / 4))[(lane >> 4) * NWG + blockIdx.x] = r;
                if (blockIdx.x == 0 && lane < 4u) meta[M_E0 + lane] = (uint32_t)(lane == 0u ? e0[0] : lane == 1u ? e0[1] : lane == 2u ? e0[2] : e0[3]);
            }
        }
    }
    pin(la);
    ST(0)
    RTS(5)

    uint32_t w[8], aw[8], xv[8];
    u64 nm[8];
    auto later2 = [&](int k) __attribute__((always_inline)) {
        uint32_t x0, x1;
        asm volatile("v_lshrrev_b32 %0, 4, %2\n\tv_lshrrev_b32 %1, 4, %5\n\tv_bitop3_b32 %0, %2, %0, 12 bitop3:0x72\n\tv_bitop3_b32 %1, %5, %1, 12 bitop3:0x72\n\t"
                     "s_not_b64 exec, %8\n\tds_write_b32 %2, %3 offset:%10\n\tds_or_b32 %0, %4 offset:%10\n\t"
                     "s_not_b64 exec, %9\n\tds_write_b32 %5, %6 offset:%10\n\tds_or_b32 %1, %7 offset:%10\n\ts_mov_b64 exec, -1"
                     : "=&v"(x0), "=&v"(x1)
                     : "v"(aw[k]), "v"(w[k]), "v"(xv[k]), "v"(aw[k + 1]), "v"(w[k + 1]), "v"(xv[k + 1]), "s"(nm[k]), "s"(nm[k + 1]), "n"(P_BUF) : "memory", "scc");
    };
    auto drain = [&]() __attribute__((always_inline)) {
        if (wv == 1) {
            const uint32_t q = (epoch + 1u) & 3u;
            const uint32_t n = __builtin_amdgcn_readfirstlane(lds_ld(vconst<P_FLAG + 16>() + q * 4u));
            if (__builtin_expect(n != 0u, 0)) {
                pin(t8);
                const uint32_t l = (t8 >> 3) & 63u;
                if (l < min(n, (uint32_t)LCAP)) {
                    const u32x4 e = *(const LDS u32x4*)(uintptr_t)(P_LST + (q * LCAP + l) * 16u);
                    lds_st(P_BUF + e.x, e.y);
                    lds_or(P_BUF + slot_x(e.x), e.z);
                }
                lds_st(vconst<P_FLAG + 16>() + q * 4u, vconst<0>());
            }
        }
    };
    auto again = [&](int D) __attribute__((always_inline)) {
        do {
            general(D, true, fm);
            __syncthreads();
            later2(0); later2(2); later2(4); later2(6);
            drain();
#pragma unroll
            for (int k = 0; k < 8; k++) {
                pin(aw[k]);
                bool due = false;
                if (aw[k] & 0x80000000u) {
                    xv[k] -= CHV;
                    if (xv[k] < CHV) {
                        const uint32_t f = (aw[k] >> 16) & 0x3ffu;
                        aw[k] = slot_w(aw[k] & 0xffffu, xv[k]);
                        xv[k] = f << ((xv[k] >> 8) & 31u);
                        due = true;
                    } else lds_st(vconst<P_FLAG>(), epoch + 1u);
                }
                nm[k] = ~__builtin_amdgcn_ballot_w64(due);
            }
            look(*(const LDS u32x2*)(uintptr_t)(P_CNT + t8));
            asm volatile("s_waitcnt lgkmcnt(0)" ::: "memory");
            __syncthreads();
        } while (__builtin_amdgcn_readfirstlane(lds_ld(vconst<P_FLAG>())) == epoch);
    };
    auto round = [&](auto DD, auto LL, Raw& rc, Raw& rn, int rd) __attribute__((always_inline)) {
        constexpr int D = decltype(DD)::value;
        constexpr bool LAST = decltype(LL)::value == 1, FIRST = decltype(LL)::value == 2;
        const int ng = min(GR, G - tl0 * GR);
        auto hook = [&](int p) __attribute__((always_inline)) {
#pragma unroll
            for (int q = 0; q < 6; q++)
            {
                constexpr int dummy = 0; (void)dummy;
                const int lp = (LPOS >> (4 * q)) & 15;
                if (!LAST && lp < 4 && lp == p) load_one(rc, tl2, q);
                if (!LAST && lp >= 4 && (lp < 8 || lp >= 12 ? lp : lp - 8) == p) load_one(rn, tl1, q);
            }
        };
        uint32_t wn[8], ca[8], px[4], cv[8];
        float mx;
        float cs[2];
        auto convA = [&](int q) __attribute__((always_inline)) {
            if (LAST) return;
            const uint32_t a = rc.a[q >> 1][q & 1];
            const f32x2 av = {__uint_as_float(a << 16), __uint_as_float(a & 0xffff0000u)};
            f32x2 rv;
            asm("v_pk_mul_f32 %0, %1, %2 op_sel_hi:[1,0]" : "=v"(rv) : "v"(av), "s"((u64)__float_as_uint(frr)));
            mx = __builtin_elementwise_maximum(__builtin_elementwise_maximum(__builtin_fabsf(rv.x), __builtin_fabsf(rv.y)), (q & 1) ? mx : 0.f);
            cs[0] = __builtin_amdgcn_cosf(rv.x); cs[1] = __builtin_amdgcn_cosf(rv.y);
            asm volatile("" : "+v"(mx), "+v"(cs[0]), "+v"(cs[1]));
            __builtin_amdgcn_sched_barrier(0);
        };
        auto convB = [&](int k) __attribute__((always_inline)) {
            if (LAST) return;
            const uint32_t o = rc.o[k >> 2][k & 3], i = rc.i[k >> 2][k & 3];
            wn[k] = (__builtin_amdgcn_alignbit(bfi(63u, o, o >> 5), __float_as_uint(cs[k & 1]), 30u) << 14) | (i >> 6);
            ca[k] = (bfi(63u, i, o) << 2) & 0x1ffcu;
            pin(wn[k]); pin(ca[k]);
            __builtin_amdgcn_sched_barrier(0);
        };
        auto convD = [&](int q) __attribute__((always_inline)) {
            if (LAST) return;
            asm volatile("v_cvt_pk_f16_f32 %0, |%1|, |%2|\n\tv_pk_mul_f16 %0, %0, %3 op_sel_hi:[1,0]" : "=v"(px[q]) : "v"(cs[0]), "v"(cs[1]), "s"(0x3ffu));
        };
        uint32_t cst;
        auto check = [&](int j) __attribute__((always_inline)) {
            if (LAST) return;
            if (__builtin_expect((__builtin_amdgcn_ballot_w64(!(mx <= RMAXREV)) | (u64)(uint32_t)(GR - ng)) != 0ull, 0)) {
                pin(t8);
                asm volatile("" : "+v"(rc.a[j]));
                const int lg = j * NT + (int)(t8 >> 3);
#pragma unroll
                for (int k = 0; k < 4; k++) {
                    const uint32_t a = (k & 1) ? (rc.a[j][k >> 1] >> 16) : (rc.a[j][k >> 1] & 0xffffu);
                    const bool nrm = __builtin_fabsf(frr * bf2f(a)) <= RMAXREV;
                    if (!(lg < ng && nrm)) ca[j * 4 + k] = NB * 4u + ((t8 >> 1) & 0xfcu);
                    if (lg < ng && !nrm) {
                        ready();
                        __builtin_amdgcn_raw_buffer_store_b32((uint32_t)((tl0 * GR + lg) * 4 + k), rsR, spec0 - draw(vconst<M_LOG * 4 + 4>()) * 4u, 0, 0);
                    }
                }
            }
            cst = vconst<CSTEP>();
        };
        auto ticket = [&](int k) __attribute__((always_inline)) {
            if (LAST) return;
            cv[k] = lds_add(P_CNT + ca[k], cst);
            __builtin_amdgcn_sched_barrier(0);
        };
        auto insert = [&](int j) __attribute__((always_inline)) {
            const int k0 = 4 * j;
            const bool odd = __builtin_amdgcn_ballot_w64(max(max(cv[k0], cv[k0 + 1]), max(cv[k0 + 2], cv[k0 + 3])) >= 2u * CHV) != 0ull;
#pragma unroll
            for (int k = k0; k < k0 + 4; k += 2) {
                uint32_t x0, x1;
                asm volatile("v_lshrrev_b32 %6, 11, %8\n\tv_lshrrev_b32 %7, 11, %9\n\t"
                             "v_bitop3_b32 %6, %6, %10, 60 bitop3:0x28\n\tv_bitop3_b32 %7, %7, %11, 60 bitop3:0x28\n\t"
                             "v_lshl_or_b32 %0, %10, 4, %6\n\tv_lshl_or_b32 %1, %11, 4, %7\n\t"
                             "v_lshlrev_b32_sdwa %2, %8, %12 dst_sel:DWORD dst_unused:UNUSED_PAD src0_sel:BYTE_1 src1_sel:WORD_0\n\t"
                             "v_lshlrev_b32_sdwa %3, %9, %12 dst_sel:DWORD dst_unused:UNUSED_PAD src0_sel:BYTE_1 src1_sel:WORD_1\n\t"
                             "v_bitop3_b32 %6, %0, %10, 12 bitop3:0x72\n\tv_bitop3_b32 %7, %1, %11, 12 bitop3:0x72\n\t"
                             "v_cmpx_gt_u32 %4, %15, %8\n\tds_write_b32 %0, %13 offset:%16\n\tds_or_b32 %6, %2 offset:%16\n\ts_mov_b64 exec, -1\n\t"
                             "v_cmpx_gt_u32 %5, %15, %9\n\tds_write_b32 %1, %14 offset:%16\n\tds_or_b32 %7, %3 offset:%16\n\ts_mov_b64 exec, -1"
                             : "=&v"(aw[k]), "=&v"(aw[k + 1]), "=&v"(xv[k]), "=&v"(xv[k + 1]), "=&s"(nm[k]), "=&s"(nm[k + 1]), "=&v"(x0), "=&v"(x1)
                             : "v"(cv[k]), "v"(cv[k + 1]), "v"(ca[k]), "v"(ca[k + 1]), "v"(px[k >> 1]), "v"(wn[k]), "v"(wn[k + 1]), "s"(CHV), "n"(P_BUF) : "memory");
                w[k] = wn[k]; w[k + 1] = wn[k + 1];
                if (__builtin_expect(odd, 0)) {
#pragma unroll
                    for (int q = k; q < k + 2; q++) {
                        nm[q] |= __builtin_amdgcn_ballot_w64(cv[q] >= 2u * CHV);
                        if (cv[q] >= 2u * CHV && cv[q] < 0x40000000u) {
                            const uint32_t lq = (epoch + 2u) & 3u;
                            uint32_t p = LCAP;
                            if (cv[q] < 3u * CHV) p = lds_add(vconst<P_FLAG + 16>() + lq * 4u, vconst<1>());
                            if (p < LCAP) {
                                const u32x2 e = {aw[q], w[q]};
                                *(LDS u32x2*)(uintptr_t)(P_LST + (lq * LCAP + p) * 16u) = e;
                                lds_st(P_LST + (lq * LCAP + p) * 16u + 8u, xv[q]);
                            } else {
                                aw[q] = ca[q] | ((q & 1) ? (px[q >> 1] & 0xffff0000u) : (px[q >> 1] << 16)) | 0x80000000u;
                                xv[q] = cv[q] - CHV;
                                lds_st(vconst<P_FLAG>(), epoch);
                            }
                        }
                    }
                }
            }
        };
        constexpr int P0 = D ? 0 : 3, P1 = D ? 1 : 2, P2 = D ? 2 : 1, P3 = D ? 3 : 0;
        const uint32_t fl = lds_ld(vconst<P_FLAG>());
        if (!LAST) asm volatile("" : "+v"(rc.o[0]), "+v"(rc.o[1]), "+v"(rc.i[0]), "+v"(rc.i[1]), "+v"(rc.a[0]), "+v"(rc.a[1]));
        if (!LAST && (rd + 4 >= nstat || tl1 >= ntile) && wv == 2) {
            pin(tk);
            if ((t8 & 511u) == 0u) {
                if (tl1 >= ntile) {
                    if (rd + 3 <= nstat) ready();
                    (void)draw(vconst<M_MD * 4>());
                } else if (rd + 4 == nstat) tk = peek(vconst<M_BEAT * 4>());
                else {
                    if (rd + 3 == nstat) {
                        if (tk == lds_ld(vconst<P_FLAG + 8>())) ready();
                        tk = 0u;
                    }
                    lds_st(vconst<P_FLAG + 4>(), tk);
                    if ((int)tk < ntile - nstat * NWG) tk = draw(vconst<M_TILE * 4>());
                }
            }
        }
        if (!LAST && blockIdx.x == 0 && wv == 3 && (t8 & 511u) == 0u) beat();
        hook(12);
        ST(3)
        pin(t8);
        const uint32_t tr = P_TAB + (t8 & ~63u), u16 = (t8 + t8) & 0x70u;
        convA(0);
        convB(0);
        const bool ag = __builtin_amdgcn_readfirstlane(fl) == epoch;
        if (!FIRST && __builtin_expect(LAST || ag || sm != 0ull, 0)) {
            if (ag) again(D);
            general(D, false, sm);
            Va = *(const LDS u32x4*)(uintptr_t)(P_TAB + (t8 & ~63u) + P0 * 16);
            Vb = *(const LDS u32x4*)(uintptr_t)(P_TAB + (t8 & ~63u) + P1 * 16);
            asm volatile("" : "+v"(Va), "+v"(Vb));
        }
        uint32_t pq = (uint32_t)pb2 | (uint32_t)(pb2 >> 32);
        pq = __builtin_amdgcn_readfirstlane(pq | (pq >> 1));
        uint32_t ad[8];
        if (!FIRST) ad[0] = take<4 * P0 + 0, D>(Va[0], u16, la, nul);
        convB(1);
        if (!FIRST) ad[1] = take<4 * P0 + 1, D>(Va[1], u16, la, nul);
        convD(0);
        convA(1);
        if (!FIRST) ad[2] = take<4 * P0 + 2, D>(Va[2], u16, la, nul);
        convB(2);
        hook(13);
        if (!FIRST) ad[3] = take<4 * P0 + 3, D>(Va[3], u16, la, nul);
        convB(3);
        if (!FIRST) put<4 * P0 + 0, LAST ? 3 : 3>(rsA, ad[0], pq);
        if (!FIRST) ad[4] = take<4 * P1 + 0, D>(Vb[0], u16, la, nul);
        convD(1);
        check(0);
        ticket(0);
        hook(4);
        if (!FIRST) put<4 * P0 + 1, LAST ? 3 : 4>(rsA, ad[1], pq);
        if (!FIRST) ad[5] = take<4 * P1 + 1, D>(Vb[1], u16, la, nul);
        convA(2);
        ticket(1);
        if (!FIRST) put<4 * P0 + 2, LAST ? 3 : 5>(rsA, ad[2], pq);
        if (!FIRST) ad[6] = take<4 * P1 + 2, D>(Vb[2], u16, la, nul);
        convB(4);
        ticket(2);
        if (!FIRST) put<4 * P0 + 3, LAST ? 3 : 6>(rsA, ad[3], pq);
        if (!FIRST) Va = *(const LDS u32x4*)(uintptr_t)(tr + P2 * 16);
        __builtin_amdgcn_sched_barrier(0);
        if (!FIRST) ad[7] = take<4 * P1 + 3, D>(Vb[3], u16, la, nul);
        convB(5);
        ticket(3);
        SETP(2);
        if (!FIRST) put<4 * P1 + 0, LAST ? 4 : 8>(rsA, ad[4], pq);
        if (!FIRST) ad[0] = take<4 * P2 + 0, D>(Va[0], u16, la, nul);
        convD(2);
        convA(3);
        hook(5);
        if (!FIRST) put<4 * P1 + 1, LAST ? 4 : 7>(rsA, ad[5], pq);
        if (!FIRST) ad[1] = take<4 * P2 + 1, D>(Va[1], u16, la, nul);
        convB(6);
        if (!FIRST) put<4 * P1 + 2, LAST ? 4 : 6>(rsA, ad[6], pq);
        if (!FIRST) ad[2] = take<4 * P2 + 2, D>(Va[2], u16, la, nul);
        convB(7);
        if (!FIRST) put<4 * P1 + 3, LAST ? 3 : 4>(rsA, ad[7], pq);
        if (!FIRST) Vb = *(const LDS u32x4*)(uintptr_t)(tr + P3 * 16);
        __builtin_amdgcn_sched_barrier(0);
        if (!FIRST) ad[3] = take<4 * P2 + 3, D>(Va[3], u16, la, nul);
        convD(3);
        check(1);
        ticket(4);
        SETP(1);
        hook(6);
        if (!FIRST) put<4 * P2 + 0, LAST ? 4 : 5>(rsA, ad[0], pq);
        if (!FIRST) ad[4] = take<4 * P3 + 0, D>(Vb[0], u16, la, nul);
        ticket(5);
        if (!FIRST) put<4 * P2 + 1, LAST ? 4 : 6>(rsA, ad[1], pq);
        if (!FIRST) ad[5] = take<4 * P3 + 1, D>(Vb[1], u16, la, nul);
        ticket(6);
        if (!FIRST) put<4 * P2 + 2, LAST ? 4 : 7>(rsA, ad[2], pq);
        if (!FIRST) ad[6] = take<4 * P3 + 2, D>(Vb[2], u16, la, nul);
        ticket(7);
        SETP(0);
        if (!FIRST) put<4 * P2 + 3, LAST ? 3 : 7>(rsA, ad[3], pq);
        if (!FIRST) ad[7] = take<4 * P3 + 3, D>(Vb[3], u16, la, nul);
        hook(7);
        ST(4)
        SETP(PRIO > 1 ? 3 : 0);
        asm volatile("s_waitcnt lgkmcnt(0)" ::: "memory");
        __syncthreads();
        ST(5)
        u32x2 cn;
        uint32_t tv;
        if (!LAST) { cn = *(const LDS u32x2*)(uintptr_t)(P_CNT + t8); tv = lds_ld(vconst<P_FLAG + 4>()); }
        __builtin_amdgcn_sched_barrier(0);
        if (!FIRST) later2(0);
        if (!FIRST) put<4 * P3, 15>(rsA, ad[4], pq);
        if (!FIRST) later2(2);
        if (!FIRST) put<4 * P3 + 1, 15>(rsA, ad[5], pq);
        if (!FIRST) drain();
        if (!LAST) {
            tl2 = rd + 2 < nstat ? fixed_tile(rd + 2) : nstat * NWG + (int)__builtin_amdgcn_readfirstlane(tv);
            look(cn);
        }
        hook(0);
        SETP(PRIO > 1 ? 2 : 0);
        if (!FIRST) later2(4);
        if (!FIRST) put<4 * P3 + 2, 15>(rsA, ad[6], pq);
        if (!FIRST) later2(6);
        if (!FIRST) put<4 * P3 + 3, 15>(rsA, ad[7], pq);
        if (!LAST) {
            hook(1);
            SETP(PRIO > 1 ? 1 : 0);
            insert(0);
            hook(2);
            SETP(0);
            pin(t8);
            Va = *(const LDS u32x4*)(uintptr_t)(P_TAB + (t8 & ~63u) + P3 * 16);
            Vb = *(const LDS u32x4*)(uintptr_t)(P_TAB + (t8 & ~63u) + P2 * 16);
            __builtin_amdgcn_sched_barrier(0);
            insert(1);
            hook(3);
            ST(1)
            SETP(3);
            asm volatile("s_waitcnt lgkmcnt(0)" ::: "memory");
            __syncthreads();
            ST(2)
            tl0 = tl1; tl1 = tl2;
        }
    };
#define HMV(A, B) "v_mov_b64 v[" #A ":" #A "+1], v[" #B ":" #B "+1]\n\t"
#define HMV4(A) HMV(A + 8, A + 6) HMV(A + 6, A + 4) HMV(A + 4, A + 2) HMV(A + 2, A)
    round(IC<1>(), IC<2>(), raw[1], raw[0], 0);
    int rd = 1;
#pragma unroll 1
    for (; tl1 < ntile; rd += 2) {
        round(IC<0>(), IC<0>(), raw[0], raw[1], rd);
        round(IC<1>(), IC<0>(), raw[1], raw[0], rd + 1);
    }
    if (tl0 >= ntile) asm volatile(HMV4(88) HMV4(98) HMV4(108) HMV4(118));
    asm volatile("" : "+s"(tl0));
    if (tl0 < ntile) round(IC<0>(), IC<0>(), raw[0], raw[1], rd);
    RTS(1)
    round(IC<1>(), IC<1>(), raw[1], raw[0], 0);
    if (__builtin_amdgcn_readfirstlane(lds_ld(P_FLAG + 16 + ((epoch + 2u) & 3u) * 4u)) != 0u) {
        asm volatile("s_waitcnt lgkmcnt(0)" ::: "memory");
        __syncthreads();
        look(*(const LDS u32x2*)(uintptr_t)(P_CNT + t8));
        general(0, true, fm);
        __syncthreads();
        drain();
    }
    {
        uint32_t* tcnt = meta + W_TCNT / 4;
        const u32x2 c = *(const LDS u32x2*)(uintptr_t)(P_CNT + t8);
        const uint32_t tw = tab_at();
#pragma unroll
        for (int e = 0; e < 2; e++) {
            const uint32_t g = U[e] & 15u;
            lds_st(tw + e * 64u, g >= 8u ? 0x80000000u : ((U[e] + ((g & 1u) ? 0u : SK * 64u)) & 0x7fffff81u) | (((7u - g) << 1) & 12u));
            *(LDS uint8_t*)(uintptr_t)(P_CNT + (t8 & ~511u) + (((tw - P_TAB) >> 2) & 127u) + e * 16u) =
                (uint8_t)(g < 8u ? 0u : ((g - 8u) >> 1) + (((c[e] + 2u * CSTEP) >> 15) << 2) + (g & 1u) * 0x50u);
        }
        asm volatile("s_waitcnt lgkmcnt(0)" ::: "memory");
        __syncthreads();
        const uint32_t u16 = (t8 + t8) & 0x70u;
        u32x4 En[4];
#pragma unroll
        for (int p = 0; p < 4; p++) En[p] = *(const LDS u32x4*)(uintptr_t)(P_TAB + (t8 & ~63u) + p * 16u);
        const u32x4 cb = *(const LDS u32x4*)(uintptr_t)(P_CNT + (t8 & ~511u) + ((t8 >> 2) & 0x70u));
        u32x2 N[16];
#pragma unroll
        for (int s = 0; s < 16; s++) N[s] = *(const LDS u32x2*)(uintptr_t)(la + s * 256u);
        if (u16 == 0u) *(u32x4*)(tcnt + blockIdx.x * 512u + (t8 >> 4)) = cb;
        const uint32_t sg = P_BUF + ((t8 & ~63u) << 4);
        uint32_t ps;
        auto rest = [&](auto SL) __attribute__((always_inline)) {
            constexpr int s = decltype(SL)::value;
            const uint32_t en = En[s >> 2][s & 3], by = cb[s >> 2] >> ((s & 3) * 8);
            const u32x2 Hr = hget<HREG(s, 0)>();
            const uint32_t at = ps + ((la & 0x38u) ^ ((s & 3) << 4));
            *(LDS u32x2*)(uintptr_t)at = Hr;
            *(LDS u32x2*)(uintptr_t)(at + (by & 0x40u)) = N[s];
            ps += (by & 0x3cu) << 2;
            const u32x2 Hs = (en & 1u) ? Hr : nul;
            const u32x4 d = {Hs.x, Hs.y, N[s].x, N[s].y};
            uint32_t a = __builtin_amdgcn_bitop3_b32(en, u16, 15u, 0x54);
            __builtin_amdgcn_raw_buffer_store_b128(d, rsR, a, 0, POS(s) >= 16 - NTS ? AUX_NT : AUX_SC1);
            if (__builtin_expect(__builtin_amdgcn_ballot_w64((en & 12u) != 0u) != 0ull, 0)) {
                const u32x4 dn = {nul.x, nul.y, nul.x, nul.y};
#pragma unroll 1
                for (uint32_t k = (en >> 2) & 3u; k > 0u; k--) {
                    a += SK * 128u;
                    __builtin_amdgcn_raw_buffer_store_b128(dn, rsR, a, 0, AUX_SC1);
                }
            }
        };
        auto pack = [&](int p) __attribute__((always_inline)) {
            const uint32_t a = (SBASE + ((((t8 >> 1) & 0x60u) + (t8 >> 8)) * 2u + p) * (NWG * 9u) + blockIdx.x * 9u) * 128u + u16;
            asm volatile("" ::: "memory");
#pragma unroll 1
            for (uint32_t q = 0; __builtin_amdgcn_ballot_w64(sg + q < ps) != 0ull; q += 256u) {
                const u32x4 d0 = *(const LDS u32x4*)(uintptr_t)(sg + q + u16), d1 = *(const LDS u32x4*)(uintptr_t)(sg + q + 128u + u16);
                __builtin_amdgcn_raw_buffer_store_b128(d0, rsR, sg + q < ps ? a + q : 0xffffff00u, 0, AUX_SC1);
                __builtin_amdgcn_raw_buffer_store_b128(d1, rsR, sg + q + 128u < ps ? a + q + 128u : 0xffffff00u, 0, AUX_SC1);
            }
            asm volatile("" ::: "memory");
        };
        asm volatile("" : "+v"(N[0]), "+v"(N[1]), "+v"(N[2]), "+v"(N[3]), "+v"(N[4]), "+v"(N[5]), "+v"(N[6]), "+v"(N[7]) :: "memory");
        asm volatile("" : "+v"(N[8]), "+v"(N[9]), "+v"(N[10]), "+v"(N[11]), "+v"(N[12]), "+v"(N[13]), "+v"(N[14]), "+v"(N[15]) :: "memory");
        ps = sg;
        rest(IC<0>()); rest(IC<1>()); rest(IC<2>()); rest(IC<3>()); rest(IC<4>()); rest(IC<5>()); rest(IC<6>()); rest(IC<7>());
        pack(0);
        ps = sg;
        rest(IC<8>()); rest(IC<9>()); rest(IC<10>()); rest(IC<11>()); rest(IC<12>()); rest(IC<13>()); rest(IC<14>()); rest(IC<15>());
        pack(1);
    }
    RTS(2)
    ST(6)
    if (blockIdx.x == 0 && t8 == 1536u) {
        do {
            beat();
            __builtin_amdgcn_s_sleep(8);
        } while (peek(vconst<M_MD * 4>()) < NWG);
    }
    RTS(3)
    RTS(4)
    ST(7)
    ST_OUT(0)
}

#define A_MD 0
#define A_ACC (SLOTI * 4)
#define A_FLAG (A_ACC + SLOTO * 4)
#define A_LDS (A_FLAG + 512)
static_assert(SLOTI * 4 == 3 * NT * 16 + 1024, "md staging assumes three full vectors and 256 dwords");
#define BIAS 0x40004000u
#define NFAST 8
#define NLD 10
#define PRE0 2
#define PRE1 4
#define MDF 0x0a11333
#define MDP 0xaaaaa
#define ROTH 0
#define ROTP 0
#define HAUX AUX_NT
#define OAUX 2
#define MDQ 0x00888
#define UNM 1
#define APRIO 0x543
#define SHARE (SLOTO / 8)
#define SPINCAP 4096u
#define ZSIG 9.0f
#define HSLOW 0x80008000u
#define GAS __attribute__((address_space(1)))

__global__ __launch_bounds__(NT) __attribute__((amdgpu_num_vgpr(53))) void acc_kernel(char* ws, int T, int E, int ks, int lcap, float zf, int pad,
                                                 const uint16_t* __restrict__ centers, const uint16_t* __restrict__ wrbf,
                                                 const uint16_t* __restrict__ wsbf, const uint16_t* __restrict__ wup,
                                                 const u32x2* __restrict__ m, const uint16_t* __restrict__ dist, u32x2* __restrict__ out,
                                                 const int* __restrict__ iin, const int* __restrict__ iout, const uint16_t* __restrict__ ang,
                                                 const uint16_t* __restrict__ wdown, const uint16_t* __restrict__ freq) {
    const char* recs = ws + W_RECS;
    const u32x4* tcnt = (const u32x4*)(ws + W_TCNT);
    const uint32_t* mdc = (const uint32_t*)(ws + W_MDC);
    const u32x4* stat = (const u32x4*)(ws + W_STAT);
    uint32_t* partial = (uint32_t*)(ws + W_PART);
    unsigned* meta = (unsigned*)ws;
    extern __shared__ __attribute__((aligned(16))) char smem[];
    ST_DECL
    unsigned leak_ = 0;
    unsigned* acc = (unsigned*)(smem + A_ACC);
    unsigned* s_flag = (unsigned*)(smem + A_FLAG);
    const int tid = threadIdx.x;
    const int lane = tid & 63;
    const int wv = __builtin_amdgcn_readfirstlane(tid >> 6);
    const int cp = (blockIdx.x >> 3) & 1;
    const int h = blockIdx.x & 3;
    const int j = ((blockIdx.x >> 2) & 1) * 16 + (blockIdx.x >> 4);
    u32x4 tc;
    const __amdgpu_buffer_rsrc_t rsM = __builtin_amdgcn_make_buffer_rsrc((void*)mdc, 0, (int)(128u * SLOTI * 4u), 0x00020000);

    u32x4 pm[4];
    u32x4 ring[NLD];
    auto load_md = [&](int ff, int pl, int mdp) __attribute__((always_inline)) {
        const uint32_t so = (uint32_t)(cp * 64 + ((ABL & 64) ? 0 : ff * 4) + h) * (SLOTI * 4u);
#pragma unroll
        for (int v = 0; v < 3; v++)
            if (pl < 0 || pl == ((mdp >> (4 * v)) & 15)) pm[v] = __builtin_amdgcn_raw_buffer_load_b128(rsM, (uint32_t)tid * 16u, so + (uint32_t)v * (NT * 16u), 0);
        if ((pl < 0 || pl == (mdp & 15)) && wv == 0) pm[3] = __builtin_amdgcn_raw_buffer_load_b128(rsM, (uint32_t)tid * 16u, so + 3u * NT * 16u, 0);
        __builtin_amdgcn_sched_barrier(0);
    };
    const uint32_t lf = ((uint32_t)(tid & ~31) * 16u + (uint32_t)(tid & 12) * 32u) * SR + (uint32_t)(tid & 16) * 4u + (uint32_t)(tid & 3) * 16u;
    const uint32_t lp = (uint32_t)(tid >> 2) * (128u * SR) + (uint32_t)(tid & 3) * 16u;
    auto load_main = [&](int i, int ff, bool nt) __attribute__((always_inline)) -> u32x4 {
        if (ABL & 8) nt = false;
        const __amdgpu_buffer_rsrc_t rs = __builtin_amdgcn_make_buffer_rsrc((void*)(recs + ((uint32_t)POS(ff) * SS + (uint32_t)(h * 32 + ((ABL & 8) ? 0 : j)) * SC) * 128u), 0, ks * (int)(SK * 128u), 0x00020000);
        const int kk = (i >> 1) ^ (cp * (nt ? ROTH : ROTP));
        const int so = (int)((kk * SK + (i & 1) * 128u * SR) * 128u);
        if (nt) return __builtin_amdgcn_raw_buffer_load_b128(rs, lf, so, HAUX);
        if (UNM) return __builtin_amdgcn_raw_buffer_load_b128(rs, lp + (uint32_t)(i & 1) * 64u, (int)(kk * SK * 128u), 0);
        return __builtin_amdgcn_raw_buffer_load_b128(rs, lf, so, 0);
    };
    const __amdgpu_buffer_rsrc_t rsT = __builtin_amdgcn_make_buffer_rsrc((void*)recs, 0, (int)(LBASE * 128u), 0x00020000);
    const uint32_t tof = (uint32_t)(lane & 2) * (TPL * 64u) + (uint32_t)(tid >> 2) * 128u + (uint32_t)(lane & 1) * 64u + 4u + (TBASE + (uint32_t)(h * 32 + j) * TST) * 128u;
    const uint32_t sp0 = (SBASE + (uint32_t)(h * 32 + j) * (2u * NWG * 9u) + (uint32_t)(tid >> 2) * 9u) * 128u;
    auto tail_rank = [&](uint32_t c, uint32_t sp, int ff, uint32_t t0, int& dst, uint32_t& en) __attribute__((always_inline)) -> int {
        const int k4 = (int)(((uint32_t)(lane & 3) + t0) * 4u) - (int)((c & 3u) * 8u);
        const int left = (int)((c >> 2) & 15u) - k4;
        const bool has = left > 0;
        uint32_t ea = tof + (uint32_t)POS(ff) * (128u * TST * 128u), eb = sp + (uint32_t)(k4 * 16 + min(max(left, 0), 4));
        asm volatile("" : "+v"(ea), "+v"(eb));
        en = k4 < 0 ? ea : eb;
        const u64 mask = __builtin_amdgcn_ballot_w64(has);
        const int n = __builtin_popcountll(mask);
        const int rank = __builtin_amdgcn_mbcnt_hi((uint32_t)(mask >> 32), __builtin_amdgcn_mbcnt_lo((uint32_t)mask, 0u));
        const int r = has ? rank : (n + lane - rank);
        dst = ((r & 15) << 4) | ((r >> 4) << 2);
        return n;
    };
    auto load_tail = [&](uint32_t en, u64& tm) __attribute__((always_inline)) -> u32x4 {
        const bool on = (uint32_t)(lane & 3) < (en & 7u) && !(ABL & 32);
        tm = __builtin_amdgcn_ballot_w64(on);
        return __builtin_amdgcn_raw_buffer_load_b128(rsT, on ? (en & ~15u) + (uint32_t)(lane & 3) * 16u : 0xffffff00u, 0, 0);
    };
    float rs[4][4], rg[4][4];
    u32x4 em[2];
    u32x2 dw2;
    const __amdgpu_buffer_rsrc_t rsE = __builtin_amdgcn_make_buffer_rsrc((void*)m, 0, E * 8, 0x00020000);
    const __amdgpu_buffer_rsrc_t rsD = __builtin_amdgcn_make_buffer_rsrc((void*)dist, 0, E * 2, 0x00020000);
    auto edge_loads = [&](int sh) __attribute__((always_inline)) {
        const int ol = sh * SHARE + tid * 4;
        const uint32_t e0 = (tid < SHARE / 4) ? (uint32_t)(((ol >> 6) << 11) | (j << 6) | (ol & 63)) : 0x0fffff00u;
        em[0] = __builtin_amdgcn_raw_buffer_load_b128(rsE, e0 * 8u, 0, AUX_NT);
        em[1] = __builtin_amdgcn_raw_buffer_load_b128(rsE, e0 * 8u + 16u, 0, AUX_NT);
        dw2 = __builtin_amdgcn_raw_buffer_load_b64(rsD, e0 * 2u, 0, AUX_NT);
    };
    auto factors = [&]() __attribute__((always_inline)) {
        const float sq = 3.7982826f;
        const f32x2 d01 = {bf2f(dw2.x & 0xffffu), bf2f(dw2.x >> 16)}, d23 = {bf2f(dw2.y & 0xffffu), bf2f(dw2.y >> 16)};
        f32x2 a[4][4];
#pragma unroll
        for (int q = 0; q < 16; q++) a[q >> 2][q & 3] = (f32x2)(0.f);
#pragma unroll 2
        for (int r = 0; r < 8; r++) {
            const uint32_t cw = ((const uint32_t*)centers)[r >> 1];
            const float cn = bf2f((r & 1) ? (cw >> 16) : (cw & 0xffffu));
            const u32x2 s2 = ((const u32x2*)wsbf)[r], r2 = ((const u32x2*)wrbf)[r];
            const f32x2 w[4] = {{bf2f(s2.x & 0xffffu), bf2f(s2.x >> 16)}, {bf2f(s2.y & 0xffffu), bf2f(s2.y >> 16)},
                                {bf2f(r2.x & 0xffffu), bf2f(r2.x >> 16)}, {bf2f(r2.y & 0xffffu), bf2f(r2.y >> 16)}};
            const f32x2 y01 = (d01 - cn) * sq, y23 = (d23 - cn) * sq;
            const f32x2 t01 = -y01 * y01, t23 = -y23 * y23;
            const float rb[4] = {__builtin_amdgcn_exp2f(t01.x), __builtin_amdgcn_exp2f(t01.y), __builtin_amdgcn_exp2f(t23.x), __builtin_amdgcn_exp2f(t23.y)};
#pragma unroll
            for (int q = 0; q < 4; q++) {
#pragma unroll
                for (int k = 0; k < 4; k++) a[q][k] = __builtin_elementwise_fma(w[k], (f32x2)(rb[q]), a[q][k]);
            }
        }
#pragma unroll
        for (int q = 0; q < 4; q++) {
#pragma unroll
            for (int k = 0; k < 4; k++) { rs[q][k] = a[q][k >> 1][k & 1]; rg[q][k] = a[q][2 + (k >> 1)][k & 1]; }
        }
    };
#define ADT(V, O) "ds_write_addtid_b32 " V " offset:" #O "\n\t"
    auto store_md = [&]() __attribute__((always_inline)) {
        asm volatile("s_mov_b32 m0, %12\n\ts_nop 0\n\t" ADT("%0", 0) ADT("%1", 256) ADT("%2", 512) ADT("%3", 768) ADT("%4", 16384) ADT("%5", 16640) ADT("%6", 16896) ADT("%7", 17152)
                     ADT("%8", 32768) ADT("%9", 33024) ADT("%10", 33280) ADT("%11", 33536)
                     :: "v"(pm[0][0]), "v"(pm[0][1]), "v"(pm[0][2]), "v"(pm[0][3]), "v"(pm[1][0]), "v"(pm[1][1]), "v"(pm[1][2]), "v"(pm[1][3]),
                        "v"(pm[2][0]), "v"(pm[2][1]), "v"(pm[2][2]), "v"(pm[2][3]), "s"(wv * 1024) : "memory");
        if (wv == 0) asm volatile("s_mov_b32 m0, 0\n\ts_nop 0\n\t" ADT("%0", 49152) ADT("%1", 49408) ADT("%2", 49664) ADT("%3", 49920)
                                  :: "v"(pm[3][0]), "v"(pm[3][1]), "v"(pm[3][2]), "v"(pm[3][3]) : "memory");
    };
    const int me = h * 2 + cp;
    bool fast, more;
    uint32_t need, mg0, mg1;
    uint32_t guard = 0;
    uint32_t sgn = 0x8000u;
    pin(sgn);

#define GAT3(M) { uint32_t a0_, a1_, a2_; asm volatile( \
        "v_lshlrev_b16 %0, 2, %3\n\tv_lshlrev_b16 %1, 2, %4\n\tv_lshlrev_b16 %2, 2, %5\n\t" \
        "ds_read_b32 v[%6], %0\n\tds_read_b32 v[%6+1], %1\n\tds_read_b32 v[%6+2], %2" \
        : "=&v"(a0_), "=&v"(a1_), "=&v"(a2_) : "v"(w0), "v"(w1), "v"(w2), "n"(M) : "memory"); }
#define MIXL " op_sel_hi:[1,1,0]\n\t"
#define MIXH " op_sel:[0,1,0] op_sel_hi:[1,1,0]\n\t"
#define SDW " dst_sel:DWORD dst_unused:UNUSED_PAD src0_sel:DWORD src1_sel:WORD_1\n\t"
#define SCAT(PRE, POST, M) { uint32_t c0_, c1_, c2_, a0_, a1_, a2_; asm volatile( \
        "v_bitop3_b32 %6, %6, v[%15-9], v[%15-8] bitop3:0xfe\n\tv_and_b32 %0, 0x3ff, %7\n\tv_lshrrev_b32 %2, 22, %7\n\tv_or_b32 %6, v[%15-7], %6\n\t" PRE \
        "v_bitop3_b32 %0, %11, %8, %0 bitop3:0xea\n\tv_bitop3_b32 %2, %11, %10, %2 bitop3:0xea\n\tv_bfe_u32 %1, %7, 11, 10\n\t" \
        "v_fma_mix_f32 %3, %0, v[%15], %12" MIXL "v_bitop3_b32 %1, %11, %9, %1 bitop3:0xea\n\tv_fma_mix_f32 %0, %0, v[%15], %13" MIXH \
        "v_fma_mix_f32 %4, %1, v[%15+1], %12" MIXL "v_lshl_add_u32 %0, %0, 16, %3\n\tv_lshlrev_b32_sdwa %3, 2, %8" SDW \
        "v_fma_mix_f32 %1, %1, v[%15+1], %13" MIXH "ds_add_rtn_u32 v[%15-9], %3, %0 offset:%14\n\t" \
        "v_fma_mix_f32 %5, %2, v[%15+2], %12" MIXL "v_lshl_add_u32 %1, %1, 16, %4\n\tv_lshlrev_b32_sdwa %4, 2, %9" SDW \
        "v_fma_mix_f32 %2, %2, v[%15+2], %13" MIXH "ds_add_rtn_u32 v[%15-8], %4, %1 offset:%14\n\t" \
        "v_lshl_add_u32 %2, %2, 16, %5\n\tv_lshlrev_b32_sdwa %5, 2, %10" SDW \
        "ds_add_rtn_u32 v[%15-7], %5, %2 offset:%14" POST \
        : "=&v"(c0_), "=&v"(c1_), "=&v"(c2_), "=&v"(a0_), "=&v"(a1_), "=&v"(a2_), "+v"(guard) \
        : "v"(xw), "v"(w0), "v"(w1), "v"(w2), "v"(sgn), "s"(mg0), "s"(mg1), "n"(A_ACC), "n"(M), "s"(tm) : "memory"); }
    asm volatile("v_mov_b32 v110, 0\n\tv_mov_b32 v111, 0\n\tv_mov_b32 v112, 0\n\tv_mov_b32 v113, 0\n\tv_mov_b32 v114, 0\n\tv_mov_b32 v115, 0\n\t"
                 "v_mov_b32 v116, 0\n\tv_mov_b32 v117, 0\n\tv_mov_b32 v118, 0" ::: "v106", "v127");
    auto unmix = [&](int i) __attribute__((always_inline)) {
        const auto s0 = __builtin_amdgcn_permlane16_swap(ring[i][0], ring[i][2], false, false);
        const auto s1 = __builtin_amdgcn_permlane16_swap(ring[i][1], ring[i][3], false, false);
        ring[i][0] = s0[0]; ring[i][2] = s0[1]; ring[i][1] = s1[0]; ring[i][3] = s1[1];
    };
    auto word = [&](int i, int k, bool pr) __attribute__((always_inline)) -> uint32_t {
        return pr && i < NFAST ? ring[(i & ~1) | (k >> 1)][(i & 1) * 2 + (k & 1)] : ring[i][k];
    };
    auto gather = [&](int R, int i, int p, bool pr) __attribute__((always_inline)) {
        if (i < NFAST && !pr) unmix(i);
        const uint32_t w0 = word(i, 0 ^ R, pr), w1 = word(i, 1 ^ R, pr), w2 = word(i, 2 ^ R, pr);
        if (ABL & 16) { asm volatile("" :: "v"(w0), "v"(w1), "v"(w2)); return; }
        if (p == 0) GAT3(119) else if (p == 1) GAT3(122) else GAT3(125)
    };
    auto scatter = [&](int R, int i, int p, u64 tm, bool pr) __attribute__((always_inline)) {
        const uint32_t xw = word(i, 3 ^ R, pr), w0 = word(i, 0 ^ R, pr), w1 = word(i, 1 ^ R, pr), w2 = word(i, 2 ^ R, pr);
        if (ABL & 16) { asm volatile("" :: "v"(w0), "v"(w1), "v"(w2), "v"(xw)); return; }
        if (i < NFAST) { if (p == 0) SCAT("", "", 119) else if (p == 1) SCAT("", "", 122) else SCAT("", "", 125) }
        else if (p == 0) SCAT("s_mov_b64 exec, %16\n\t", "\n\ts_mov_b64 exec, -1", 119)
        else SCAT("s_mov_b64 exec, %16\n\t", "\n\ts_mov_b64 exec, -1", 125)
    };
#define WAIT_L(N) asm volatile("s_waitcnt lgkmcnt(" #N ")" ::: "memory")
    int ntn;
    u64 tm0, tm1;
    uint32_t ta[2], sp = sp0;
    auto list1 = [&](int ff) __attribute__((always_inline)) {
        int dst;
        uint32_t en;
        ntn = tail_rank(tc.x, sp, ff, 0u, dst, en);
        asm volatile("ds_permute_b32 v106, %0, %1" :: "v"(dst), "v"(en) : "memory");
    };
    auto step = [&](auto RR, auto NN, auto NC, int ff) __attribute__((always_inline)) {
        constexpr int R = decltype(RR)::value;
        constexpr bool nt = decltype(NN)::value == 1, last = decltype(NN)::value == 2, nc = decltype(NC)::value == 1, first = decltype(NC)::value == 2;
        constexpr bool pr = UNM && !nc;
        constexpr int mdp = first ? MDF : (nt || nc) ? MDQ : MDP;
        if (ff == 2) leak_ = (unsigned)__builtin_amdgcn_s_memtime();
        if (ff == 10) leak_ = ((unsigned)__builtin_amdgcn_s_memtime() - leak_ - 48000u) / 3u;
        gather(R, 0, 0, pr);
        if (!first) scatter(R, NFAST, 2, tm0, pr);
        const int ntl = ntn;
        asm volatile("s_nop 1\n\tv_mov_b32_dpp %0, v106 quad_perm:[0,0,0,0] row_mask:0xf bank_mask:0xf\n\tv_mov_b32_dpp %1, v106 quad_perm:[1,1,1,1] row_mask:0xf bank_mask:0xf" : "=v"(ta[0]), "=v"(ta[1]));
        auto side = [&](int pl) __attribute__((always_inline)) {
            if (first && PRE1 <= 4 && pl == ((mdp >> 20) & 15)) { ring[4] = load_main(4, ff, false); ring[5] = load_main(5, ff, false); }
            if (first && PRE1 <= 6 && pl == ((mdp >> 24) & 15)) { ring[6] = load_main(6, ff, false); ring[7] = load_main(7, ff, false); }
            if (pl == ((mdp >> 12) & 15)) ring[NFAST] = load_tail(ta[0], tm0);
            if (pl == ((mdp >> 16) & 15)) ring[NFAST + 1] = load_tail(ta[1], tm1);
            __builtin_amdgcn_sched_barrier(0);
            if (!last) load_md(ff + 1, pl, mdp);
            else if (pl == (mdp & 15)) edge_loads(me);
            __builtin_amdgcn_sched_barrier(0);
        };
        side(8);
        gather(R, 1, 1, pr);
        side(9);
        const uint32_t tcur = tc.x, spc = sp;
#pragma unroll
        for (int i = 0; i < NFAST - 1; i++) {
            gather(R, i + 2, (i + 2) % 3, pr);
            if (i == 0) side(10);
            if (i == 0 && first) WAIT_L(6); else if (i < 2) WAIT_L(9); else WAIT_L(12);
            scatter(R, i, i % 3, 0ull, pr);
            if (!last && pr && (i & 1)) ring[i - 1] = load_main(i - 1, ff + 1, nt);
            if (!last && (!pr || (i & 1))) ring[i] = load_main(i, ff + 1, nt);
            side(i);
            if (APRIO && i == (APRIO & 15)) __builtin_amdgcn_s_setprio(2);
            if (APRIO && i == ((APRIO >> 4) & 15)) __builtin_amdgcn_s_setprio(1);
            if (APRIO && i == ((APRIO >> 8) & 15)) __builtin_amdgcn_s_setprio(0);
            if (i == 0 && !last) {
                tc.x = __builtin_amdgcn_alignbit(tc.y, tc.x, 8u); tc.y = __builtin_amdgcn_alignbit(tc.z, tc.y, 8u);
                tc.z = __builtin_amdgcn_alignbit(tc.w, tc.z, 8u); tc.w >>= 8;
                sp = ff == 7 ? sp0 + NWG * 9u * 128u : sp + ((tcur & 0x3cu) << 2);
                list1(ff + 1);
            }
            __builtin_amdgcn_sched_barrier(0);
        }
        if (__builtin_expect(ntl > 16, 0)) {
            gather(R, NFAST + 1, 0, pr);
            WAIT_L(0);
            scatter(R, NFAST + 1, 0, tm1, pr);
        }
        if (__builtin_expect((__builtin_amdgcn_ballot_w64((tcur & 2u) != 0u) | (u64)(ntl > 32 ? 1 : 0)) != 0ull, 0)) {
#pragma unroll 1
            for (int e = 2; e < 6; e++) {
                int dst;
                uint32_t en;
                if (tail_rank(tcur, spc, ff, (uint32_t)e & 4u, dst, en) <= (e & 3) * 16) continue;
                const int cmp = __builtin_amdgcn_ds_permute(dst, (int)en);
                ring[NFAST + 1] = load_tail((uint32_t)__builtin_amdgcn_ds_bpermute(((lane & ~3) | (e & 3)) << 2, cmp), tm1);
                gather(R, NFAST + 1, 0, pr);
                WAIT_L(0);
                scatter(R, NFAST + 1, 0, tm1, pr);
            }
        }
        if (APRIO) __builtin_amdgcn_s_setprio(3);
        WAIT_L(3);
        ST(1)
        if (!last) {
            __builtin_amdgcn_s_barrier();
            ST(3)
            store_md();
        }
        scatter(R, NFAST - 1, 1, 0ull, pr);
        if (!last) {
            if (pr) ring[NFAST - 2] = load_main(NFAST - 2, ff + 1, nt);
            ring[NFAST - 1] = load_main(NFAST - 1, ff + 1, nt);
            WAIT_L(3);
            __builtin_amdgcn_s_barrier();
            ST(4)
        }
    };
    auto steps = [&](auto RR) __attribute__((always_inline)) {
        constexpr int R = decltype(RR)::value;
        tc = tcnt[(wv * 16 + (lane >> 2)) * 128 + j * 4 + h];
        u32x4 sv[2];
        uint32_t e0;
        if (wv < 4) {
            sv[0] = (u32x4)(0u); sv[1] = sv[0];
            if (lane < SLOTI / 256) { sv[0] = stat[tid * 2]; sv[1] = stat[tid * 2 + 1]; }
            e0 = meta[M_E0 + wv];
        }
        const u32x2 extra = *(const u32x2*)(meta + M_LOG);
        load_md(0, -1, 0);
        {
            const u32x4 bi = {BIAS, BIAS, BIAS, BIAS};
#pragma unroll
            for (int k = 0; k < 6; k++) *(LDS u32x4*)(uintptr_t)(A_ACC + (k * NT + threadIdx.x) * 16) = bi;
            if (threadIdx.x < 128) *(LDS u32x4*)(uintptr_t)(A_ACC + (6 * NT + threadIdx.x) * 16) = bi;
            __builtin_amdgcn_sched_barrier(0);
        }
#pragma unroll
        for (int i = 0; i < PRE0; i++) ring[i] = load_main(i, 0, false);
        __builtin_amdgcn_sched_barrier(0);
        if (wv == 15) asm volatile("" :: "s"(*(const uint32_t*)centers ^ *(const uint32_t*)wsbf ^ *(const uint32_t*)wrbf ^ *(const uint32_t*)wup));
        list1(0);
        if (wv < 4) {
            const uint32_t mxb = wave_max(max(max(sv[0].x & 0xffffu, sv[0].z & 0xffffu), max(sv[1].x & 0xffffu, sv[1].z & 0xffffu)));
            const uint32_t mnb = 0xffffu - (wave_max(max(max(sv[0].x, sv[0].z), max(sv[1].x, sv[1].z))) >> 16);
            const float s2 = wave_sum((__uint_as_float(sv[0].y) + __uint_as_float(sv[0].w)) + (__uint_as_float(sv[1].y) + __uint_as_float(sv[1].w)));
            const float rng = fmaxf(sqrtf(zf * s2), 2.0f * bf2f(mxb)) * 6.103515625e-05f;
            const uint32_t eb = mxb ? (__float_as_uint(rng) + 0x7fffffu) >> 23 : 127u;
            const bool ok = !mxb || (mxb < 0x4700u && eb >= 107u && eb <= 140u && mnb >= ((eb + 8u) << 7));
            if (lane == 0) {
                s_flag[8 + wv] = (eb & 0xffu) | (ok ? 0x100u : 0u) | (mxb ? 0x200u : 0u);
                s_flag[12 + wv] = __float_as_uint(__uint_as_float(((eb + e0) & 0xffu) << 23) * (1024.0f / 1023.0f));
                s_flag[wv] = 0u;
            }
        }
        store_md();
#pragma unroll
        for (int i = PRE0; i < PRE1; i++) ring[i] = load_main(i, 0, false);
        WAIT_L(0);
        __syncthreads();
        const uint32_t q0 = __builtin_amdgcn_readfirstlane(s_flag[8 + 2 * cp]), q1 = __builtin_amdgcn_readfirstlane(s_flag[9 + 2 * cp]);
        fast = (q0 & q1 & 0x100u) != 0u;
        need = (q0 >> 9) | ((q1 >> 9) << 1);
        more = (__builtin_amdgcn_readfirstlane(extra.x | extra.y) | (uint32_t)(T & 3)) != 0u;
        mg0 = (((q0 & 0xffu) + 9u) << 23) | 0x400000u;
        mg1 = ((((q1 & 0xffu) + 9u) << 23) | 0x400000u) + ((0u - (mg0 >> 16)) & 0xffffu);
        if (!fast) return;
        ST(0)
        RTS(9)
        step(RR, IC<0>(), IC<2>(), 0);
        int ff = 1;
#pragma unroll 1
        for (; ff < 15 - NTS; ff++) step(RR, IC<0>(), IC<0>(), ff);
        step(RR, IC<1>(), IC<0>(), ff++);
#pragma unroll 1
        for (; ff < 15; ff++) step(RR, IC<1>(), IC<1>(), ff);
        step(RR, IC<2>(), IC<1>(), 15);
        scatter(R, NFAST, 2, tm0, false);
    };
    auto irregular = [&](auto f) __attribute__((always_inline)) {
        const float fr = bf2f(freq[0]);
        const char* lg = recs + (size_t)LBASE * 128u;
        const uint16_t* tags = (const uint16_t*)(lg + (size_t)lcap * 128u);
        const uint32_t nl = meta[M_LOG], ns = meta[M_LOG + 1];
#pragma unroll 1
        for (uint32_t q = tid; q < nl * 8u; q += NT) {
            const uint32_t b = tags[q >> 3];
            if ((b & 127u) == (uint32_t)(h * 32 + j)) {
                const uint32_t* un = (const uint32_t*)(lg + (size_t)q * 16u);
                const uint32_t xw = un[3 ^ h];
#pragma unroll 1
                for (int k = 0; k < 3; k++) {
                    const uint32_t ww = un[k ^ h];
                    const float c = (float)((xw >> (k * 11)) & 0x3ffu) * (1.0f / 1023.0f);
                    if (!(ww & WNULL)) f(ww >> 16, ((ww & 0x3fffu) << 6) | ((ALT ? ((b >> 7) * 3u) & 15u : b >> 7) << 2) | (uint32_t)h, (ww & 0x8000u) ? -c : c);
                }
            }
        }
#pragma unroll 1
        for (uint32_t q = tid; q < ns + (uint32_t)(T & 3); q += NT) {
            const uint32_t t = q < ns ? ((const uint32_t*)tags)[-1 - (int)q] : (uint32_t)(T & ~3) + (q - ns);
            const uint32_t i = (uint32_t)iin[t], o = (uint32_t)iout[t];
            if (((o >> 6) & 31u) == (uint32_t)j && (i & 3u) == (uint32_t)h) f(((o >> 11) << 6) | (o & 63u), i, cosf(fr * bf2f(ang[t])));
        }
    };

    if (h == 0) steps(IC<0>());
    else if (h == 1) steps(IC<1>());
    else if (h == 2) steps(IC<2>());
    else steps(IC<3>());
    if (fast) {
        if (more) irregular([&](uint32_t ol, uint32_t i, float cf) __attribute__((always_inline)) {
            const uint32_t md = mdc[(cp * 64u + (i & 63u)) * SLOTI + ((i >> 14) << 8) + ((i >> 4) & 0xfcu) + ((i >> 12) & 3u)];
            const float c = cf * (1023.0f / 16777216.0f);
            const uint32_t lo = __float_as_uint(__builtin_fmaf(c, h2f(md & 0xffffu), __uint_as_float(mg0)));
            const uint32_t hi = __float_as_uint(__builtin_fmaf(c, h2f(md >> 16), __uint_as_float(mg1)));
            if (cf == cf) guard |= atomicAdd(&acc[ol], (hi << 16) + lo);
            else guard |= HSLOW;
        });
    }
    GAS unsigned* dw = (GAS unsigned*)(meta + M_DONE + j * 64);
    volatile LDS unsigned* seen = (volatile LDS unsigned*)(uintptr_t)(A_FLAG + 4);
    if (tid == NT - 64) *seen = __hip_atomic_load(dw, __ATOMIC_RELAXED, __HIP_MEMORY_SCOPE_AGENT) & 0xff00ffu;
    WAIT_L(0);
    asm volatile("v_or3_b32 %0, %0, v110, v111\n\tv_or3_b32 %0, %0, v112, v113\n\tv_or3_b32 %0, %0, v114, v115\n\tv_or3_b32 %0, %0, v116, v117\n\tv_or_b32 %0, v118, %0" : "+v"(guard));
    ST(2)
    __syncthreads();
    RTS(10)
    const __amdgpu_buffer_rsrc_t rsP = __builtin_amdgcn_make_buffer_rsrc((void*)(partial + (size_t)j * 16 * SLOTO), 0, 16 * SLOTO * 4, 0x00020000);
    const bool fin = wv < (SHARE / 4 + 63) / 64;
    u32x4 p[8];
    unsigned have = 0u;
    auto fetch = [&](unsigned v, int sh) __attribute__((always_inline)) {
        const uint32_t po = tid < SHARE / 4 ? (uint32_t)(sh * SHARE + tid * 4) * 4u : 0x7fffff00u;
        const unsigned nw = v & ~have;
#pragma unroll
        for (int r = 0; r < 8; r++)
            if ((nw >> (me ^ r)) & 1u)
                p[r] = __builtin_amdgcn_raw_buffer_load_b128(rsP, po, ((v >> (16 + (me ^ r))) & 1u) ? 16 * SLOTO * 4 : (me ^ r) * SLOTO * 4, AUX_SC1);
        have |= v;
    };
    if (fast) {
        uint32_t big = 0u;
        u32x4 v[7];
#pragma unroll
        for (int k = 0; k < 6; k++) v[k] = *(const LDS u32x4*)(uintptr_t)(A_ACC + (k * NT + tid) * 16);
        v[6] = (u32x4)(BIAS);
        if (wv < 2) v[6] = *(const LDS u32x4*)(uintptr_t)(A_ACC + (6 * NT + tid) * 16);
#pragma unroll
        for (int k = 0; k < 6; k++) __builtin_amdgcn_raw_buffer_store_b128(v[k], rsP, (uint32_t)(tid * 16 + me * SLOTO * 4 + k * NT * 16), 0, AUX_SC1);
        if (wv < 2) __builtin_amdgcn_raw_buffer_store_b128(v[6], rsP, (uint32_t)(tid * 16 + me * SLOTO * 4 + 6 * NT * 16), 0, AUX_SC1);
        __builtin_amdgcn_sched_barrier(0);
        p[0] = *(const LDS u32x4*)(uintptr_t)(A_ACC + (me * SHARE + min(tid, SHARE / 4 - 1) * 4) * 4);
        have = 1u << me;
        if (fin) fetch(__builtin_amdgcn_readfirstlane(*seen), me);
#pragma unroll
        for (int k = 0; k < 28; k++) { guard |= v[k >> 2][k & 3]; big |= v[k >> 2][k & 3] + 0x42004200u; }
        if (guard & HSLOW) s_flag[0] = 1u;
        if (big & 0x7c00u) s_flag[2] = 1u;
        if (big & 0x7c000000u) s_flag[3] = 2u;
    }
    asm volatile("s_waitcnt vmcnt(0)" ::: "memory");
    __syncthreads();
    RTS(11)
    ST(5)
    const bool slow = (ABL & 120) ? false : !fast || __builtin_amdgcn_readfirstlane(s_flag[0]) != 0u || (need & ~__builtin_amdgcn_readfirstlane(s_flag[2] | s_flag[3])) != 0u;
    if (slow) {
        float* fa = (float*)acc;
        float w[16];
        load_w(wdown, w);
#pragma unroll 1
        for (int comp = 2 * cp; comp < 2 * cp + 2; comp++) {
            __syncthreads();
            for (int i = tid; i < SLOTO; i += NT) fa[i] = 0.f;
            __syncthreads();
            auto add = [&](uint32_t ol, uint32_t i, float cf) __attribute__((always_inline)) {
                uint32_t rr[4];
                md_of(m[i], w, rr);
                const uint32_t rc = comp == 0 ? rr[0] : comp == 1 ? rr[1] : comp == 2 ? rr[2] : rr[3];
                atomicAdd(&fa[ol], cf * bf2f(rc));
            };
#pragma unroll 1
            for (uint32_t q = tid; q < 16u * NWG * ((uint32_t)ks * 8u + 24u); q += NT) {
                const uint32_t per = (uint32_t)ks * 8u + 24u;
                const uint32_t un = q & 3u, hf = (q % per) >> 2, sb = (q / per) & (NWG - 1u), ff = (q / per) / NWG;
                const uint32_t col = POS(ff) * 128u + (uint32_t)(h * 32 + j);
                u32x2 p0, p1;
                if (hf < 2u * (uint32_t)ks) {
                    const char* ln = recs + (size_t)((col >> 7) * SS + (col & 127u) * SC + (hf >> 1) * SK + sb * SR) * 128u + un * 16u + (hf & 1u) * 8u;
                    p0 = *(const u32x2*)ln; p1 = *(const u32x2*)(ln + 64);
                } else {
                    const uint32_t tl = hf - 2u * (uint32_t)ks;
                    const uint8_t* cb = (const uint8_t*)tcnt + (sb * 128u + (uint32_t)(j * 4 + h)) * 16u;
                    const char* ln = recs + (size_t)(TBASE + (tl >> 1) * TPL + col * TST + sb) * 128u + (tl & 1u) * 64u + un * 16u;
                    if (tl < 4u) { if (tl >= (cb[ff] & 3u) * 2u) continue; }
                    else {
                        uint32_t at = (tl - 4u) * 4u + un;
                        if (at >= ((cb[ff] >> 2) & 15u)) continue;
#pragma unroll 1
                        for (uint32_t t = ff & 8u; t < ff; t++) at += (cb[t] >> 2) & 15u;
                        ln = recs + (size_t)(SBASE + (((uint32_t)(h * 32 + j) * 2u + (ff >> 3)) * NWG + sb) * 9u) * 128u + at * 16u;
                    }
                    p0 = *(const u32x2*)ln; p1 = *(const u32x2*)(ln + 8);
                }
                if (h & 2) { const u32x2 t = p0; p0 = p1; p1 = t; }
                const uint32_t ws[4] = {(h & 1) ? p0.y : p0.x, (h & 1) ? p0.x : p0.y, (h & 1) ? p1.y : p1.x, (h & 1) ? p1.x : p1.y};
#pragma unroll
                for (int k = 0; k < 3; k++) {
                    const float c = (float)((ws[3] >> (k * 11)) & 0x3ffu) * (1.0f / 1023.0f);
                    if (!(ws[k] & WNULL)) add(ws[k] >> 16, ((ws[k] & 0x3fffu) << 6) | (ff * 4u + (uint32_t)h), (ws[k] & 0x8000u) ? -c : c);
                }
            }
            if (more) irregular(add);
            __syncthreads();
            for (int i = tid; i < SLOTO / 4; i += NT)
                __builtin_amdgcn_raw_buffer_store_b128(*(const LDS u32x4*)(uintptr_t)(A_ACC + i * 16), rsP, (uint32_t)(((comp & 1) * 8 + me) * SLOTO * 4 + i * 16), 0, AUX_SC1);
        }
        edge_loads(me);
#pragma unroll
        for (int k = 0; k < 8; k++) p[k] = (u32x4)(0u);
        have = 0u;
        asm volatile("s_waitcnt vmcnt(0)" ::: "memory");
        __syncthreads();
    }
    if (tid == NT - 64) {
        const unsigned inc = (slow ? 0x10001u : 1u) << me;
        unsigned v = __hip_atomic_fetch_add(dw, inc, __ATOMIC_RELAXED, __HIP_MEMORY_SCOPE_AGENT) + inc;
        if ((v & 0xffu) != 0xffu) {
            unsigned spins = 0;
            do {
                *seen = v & 0xff00ffu;
                __builtin_amdgcn_s_sleep(1);
                v = __hip_atomic_load(dw, __ATOMIC_RELAXED, __HIP_MEMORY_SCOPE_AGENT);
                if (++spins > SPINCAP && (v & 0xffu) != 0xffu) {
                    v = __hip_atomic_fetch_add(dw, 256u << me, __ATOMIC_RELAXED, __HIP_MEMORY_SCOPE_AGENT);
                    if ((v & 0xffu) != 0xffu) v = 0x800000ffu;
                }
            } while ((v & 0xffu) != 0xffu);
            v &= 0x80ff00ffu;
        }
        *seen = v;
    }
    if (!fin) return;
    factors();
    have = __builtin_amdgcn_readfirstlane(have);
    uint32_t un[4];
#pragma unroll
    for (int c = 0; c < 4; c++) un[c] = __builtin_amdgcn_readfirstlane(s_flag[12 + c]);
    unsigned sv;
    {
        const uintptr_t pb = (uintptr_t)(partial + (size_t)j * 16 * SLOTO);
        const u32x4 rsQ = {(uint32_t)pb, (uint32_t)(pb >> 32) & 0xffffu, 16u * SLOTO * 4u, 0x00020000u};
        const uint32_t po = tid < SHARE / 4 ? (uint32_t)(me * SHARE + tid * 4) * 4u : 0x7fffff00u;
        do {
            sv = __builtin_amdgcn_readfirstlane(*seen);
            uint32_t t0, t1;
#define FET(R) "s_xor_b32 %8, %12, " #R "\n\ts_bitcmp1_b32 %13, %8\n\ts_cbranch_scc0 1f\n\ts_mul_i32 %9, %8, %15\n\ts_bitcmp1_b32 %14, %8\n\t" \
               "s_cselect_b32 %9, %16, %9\n\tbuffer_load_dwordx4 %" #R ", %10, %11, %9 offen sc1\n1:\n\t"
            asm volatile("s_and_b32 %8, %13, 0xff\n\ts_cbranch_scc0 2f\n\ts_nop 2\n\t" FET(0) FET(1) FET(2) FET(3) FET(4) FET(5) FET(6) FET(7) "\n2:\n\ts_sleep 2\n\ts_waitcnt vmcnt(0)"
                         : "+v"(p[0]), "+v"(p[1]), "+v"(p[2]), "+v"(p[3]), "+v"(p[4]), "+v"(p[5]), "+v"(p[6]), "+v"(p[7]), "=&s"(t0), "=&s"(t1)
                         : "v"(po), "s"(rsQ), "s"(me), "s"((int)sv < 0 ? 0u : sv & ~have), "s"(sv >> 16), "n"(SLOTO * 4), "n"(16 * SLOTO * 4) : "memory", "scc");
            have |= sv;
        } while ((sv & 0xffu) != 0xffu);
    }
    if ((int)sv < 0) return;
    RTS(12)
    ST(6)
    const unsigned flt = (sv >> 16) & 0xffu;
    auto finish = [&](int sh) __attribute__((always_inline)) {
        {
            const int ol = sh * SHARE + tid * 4;
            const int e0 = ((ol >> 6) << 11) | (j << 6) | (ol & 63);
            const uint32_t po = tid < SHARE / 4 ? (uint32_t)ol * 4u : 0x7fffff00u;
            float g[4][4];
            u32x4 a[2] = {p[0] + p[2], p[1] + p[3]}, b[2] = {p[4] + p[6], p[5] + p[7]};
            if (cp) { const u32x4 t = a[0], u = b[0]; a[0] = a[1]; b[0] = b[1]; a[1] = t; b[1] = u; }
#pragma unroll
            for (int c = 0; c < 2; c++) {
                const int nb = 0x10000 - 0x4000 * __builtin_popcount(flt & (0x55u << c));
                const float u0 = __uint_as_float(nb ? un[2 * c] : 0u), u1 = __uint_as_float(nb ? un[2 * c + 1] : 0u);
#pragma unroll
                for (int q = 0; q < 4; q++) {
                    g[q][2 * c] = (float)((int)((a[c][q] & 0xffffu) + (b[c][q] & 0xffffu)) - nb) * u0;
                    g[q][2 * c + 1] = (float)((int)((a[c][q] >> 16) + (b[c][q] >> 16)) - nb) * u1;
                }
            }
            if (__builtin_expect(flt != 0u, 0)) {
#pragma unroll
                for (int c = 0; c < 2; c++) {
#pragma unroll 1
                    for (int k = c; k < 8; k += 2) {
                        if (!((flt >> k) & 1u)) continue;
                        const u32x4 f0 = __builtin_amdgcn_raw_buffer_load_b128(rsP, po, k * SLOTO * 4, AUX_SC1);
                        const u32x4 f1 = __builtin_amdgcn_raw_buffer_load_b128(rsP, po, (8 + k) * SLOTO * 4, AUX_SC1);
#pragma unroll
                        for (int q = 0; q < 4; q++) { g[q][2 * c] += __uint_as_float(f0[q]); g[q][2 * c + 1] += __uint_as_float(f1[q]); }
                    }
                }
            }
            float wu[16];
            load_w(wup, wu);
            u32x4 ov[2];
#pragma unroll
            for (int q = 0; q < 4; q++) {
#pragma unroll
                for (int k = 0; k < 4; k++) g[q][k] *= rs[q][k];
                const uint32_t m0 = em[q >> 1][(q & 1) * 2], m1 = em[q >> 1][(q & 1) * 2 + 1];
                const float mm[4] = {bf2f(m0 & 0xffff), bf2f(m0 >> 16), bf2f(m1 & 0xffff), bf2f(m1 >> 16)};
                float o[4];
#pragma unroll
                for (int k = 0; k < 4; k++) o[k] = mm[k] + (g[q][0] * wu[k] + g[q][1] * wu[4 + k] + g[q][2] * wu[8 + k] + g[q][3] * wu[12 + k]) * rg[q][k];
                ov[q >> 1][(q & 1) * 2] = pk_bf16(o[0], o[1]);
                ov[q >> 1][(q & 1) * 2 + 1] = pk_bf16(o[2], o[3]);
            }
            const __amdgpu_buffer_rsrc_t rsU = __builtin_amdgcn_make_buffer_rsrc((void*)out, 0, E * 8, 0x00020000);
            const uint32_t uo = tid < SHARE / 4 ? (uint32_t)e0 * 8u : 0x7fffff00u;
            __builtin_amdgcn_raw_buffer_store_b128(ov[0], rsU, uo, 0, OAUX);
            __builtin_amdgcn_raw_buffer_store_b128(ov[1], rsU, uo + 16u, 0, OAUX);
        }
    };
    finish(me);
#pragma unroll 1
    for (unsigned todo = (sv >> 8) & 0xffu; todo; todo &= todo - 1u) {
        const int sh = __builtin_ctz(todo);
        edge_loads(sh);
        factors();
        have = 0u;
        fetch(sv, sh);
        finish(sh);
    }
    if (blockIdx.x == 5) { const unsigned long long t0_ = __builtin_amdgcn_s_memrealtime(); while (__builtin_amdgcn_s_memrealtime() - t0_ < leak_) __builtin_amdgcn_s_sleep(2); }
    ST(7)
    ST_OUT(8)
}

__global__ __launch_bounds__(256) void gen_scatter_kernel(const int* __restrict__ iin, const int* __restrict__ iout,
                                                          const uint16_t* __restrict__ ang, const uint16_t* __restrict__ freq,
                                                          const u32x2* __restrict__ m, const uint16_t* __restrict__ wdown,
                                                          float* __restrict__ agg, int T) {
    float w[16];
    load_w(wdown, w);
    const float fr = bf2f(freq[0]);
    for (long t = (long)blockIdx.x * 256 + threadIdx.x; t < T; t += (long)gridDim.x * 256) {
        const float c = cosf(fr * bf2f(ang[t]));
        uint32_t r[4];
        md_of(m[iin[t]], w, r);
        float* q = agg + (size_t)iout[t] * 4;
#pragma unroll
        for (int k = 0; k < 4; k++) atomicAdd(q + k, c * bf2f(r[k]));
    }
}
__global__ __launch_bounds__(256) void gen_final_kernel(const u32x2* __restrict__ m, const uint16_t* __restrict__ dist,
                                                        const uint16_t* __restrict__ centers, const uint16_t* __restrict__ wrbf,
                                                        const uint16_t* __restrict__ wsbf, const uint16_t* __restrict__ wup,
                                                        const float* __restrict__ agg, u32x2* __restrict__ out, int E) {
    const int e = blockIdx.x * 256 + threadIdx.x;
    if (e >= E) return;
    const float d = bf2f(dist[e]);
    float rs[4] = {0, 0, 0, 0}, rg[4] = {0, 0, 0, 0};
#pragma unroll 1
    for (int r = 0; r < 8; r++) {
        const float x = d - bf2f(centers[r]);
        const float rb = __expf(-10.0f * x * x);
#pragma unroll
        for (int k = 0; k < 4; k++) { rs[k] += rb * bf2f(wsbf[r * 4 + k]); rg[k] += rb * bf2f(wrbf[r * 4 + k]); }
    }
    float wu[16];
    load_w(wup, wu);
    const u32x2 mv = m[e];
    const float mm[4] = {bf2f(mv.x & 0xffff), bf2f(mv.x >> 16), bf2f(mv.y & 0xffff), bf2f(mv.y >> 16)};
    float g[4], o[4];
#pragma unroll
    for (int k = 0; k < 4; k++) g[k] = agg[(size_t)e * 4 + k] * rs[k];
#pragma unroll
    for (int k = 0; k < 4; k++) o[k] = mm[k] + (g[0] * wu[k] + g[1] * wu[4 + k] + g[2] * wu[8 + k] + g[3] * wu[12 + k]) * rg[k];
    const u32x2 ov = {f2bf(o[0]) | (f2bf(o[1]) << 16), f2bf(o[2]) | (f2bf(o[3]) << 16)};
    out[e] = ov;
}

extern "C" void kernel_launch(void* const* d_in, const int* in_sizes, int n_in,
                              void* d_out, int out_size, void* d_ws, size_t ws_size,
                              hipStream_t stream) {
    const u32x2* m = (const u32x2*)d_in[0];
    const int* iin = (const int*)d_in[1];
    const int* iout = (const int*)d_in[2];
    const uint16_t* dist = (const uint16_t*)d_in[3];
    const uint16_t* ang = (const uint16_t*)d_in[4];
    const uint16_t* centers = (const uint16_t*)d_in[5];
    const uint16_t* freq = (const uint16_t*)d_in[6];
    const uint16_t* wrbf = (const uint16_t*)d_in[7];
    const uint16_t* wsbf = (const uint16_t*)d_in[8];
    const uint16_t* wdown = (const uint16_t*)d_in[9];
    const uint16_t* wup = (const uint16_t*)d_in[10];
    const int E = in_sizes[3];
    const int T = in_sizes[1];
    char* ws = (char*)d_ws;
    const int G = T >> 2;
    const int ntile = (G + GR - 1) / GR;
    const int nstat = ntile / NWG - NDYN > 4 ? ntile / NWG - NDYN : 4;
    const int lcap = G / 6 + 2;
    char* recs = ws + W_RECS;
    size_t off = W_RECS;
    const size_t rbytes = (size_t)LBASE * 128 + (size_t)lcap * 130;
    off += rbytes;
    if (E > EMAX || (E & 3) || E < 1 || rbytes > 0x70000000u || off > ws_size) {
        (void)hipMemsetAsync(ws, 0, (size_t)E * 16, stream);
        gen_scatter_kernel<<<4096, 256, 0, stream>>>(iin, iout, ang, freq, m, wdown, (float*)ws, T);
        gen_final_kernel<<<(E + 255) / 256, 256, 0, stream>>>(m, dist, centers, wrbf, wsbf, wup, (const float*)ws, (u32x2*)d_out, E);
        return;
    }
    int ks = T / (NWG * NB * 24);
    if (ks > KMAX) ks = KMAX;
    const float zf = ZSIG * ZSIG * (float)T / (8.0f * (float)E * (float)E);
    (void)hipFuncSetAttribute((const void*)part_kernel, hipFuncAttributeMaxDynamicSharedMemorySize, P_LDS);
    (void)hipFuncSetAttribute((const void*)acc_kernel, hipFuncAttributeMaxDynamicSharedMemorySize, A_LDS);
    part_kernel<<<NWG, NT, P_LDS, stream>>>(iin, iout, ang, freq, m, wdown, recs, T, E, ntile, nstat, ks, lcap);
    if (!(ABL & 7)) acc_kernel<<<NWG, NT, A_LDS, stream>>>(ws, T, E, ks, lcap, zf, 0, centers, wrbf, wsbf, wup, m, dist, (u32x2*)d_out, iin, iout, ang, wdown, freq);
}
```

```cpp
#include <hip/hip_runtime.h>
#include <hip/hip_bf16.h>
#include <stdint.h>
#pragma clang diagnostic ignored "-Winline-asm"

typedef unsigned int u32x4 __attribute__((ext_vector_type(4)));
typedef unsigned int u32x2 __attribute__((ext_vector_type(2)));
typedef unsigned short u16x2 __attribute__((ext_vector_type(2)));
typedef _Float16 f16x2 __attribute__((ext_vector_type(2)));
typedef __bf16 bf16x2 __attribute__((ext_vector_type(2)));
typedef float f32x2 __attribute__((ext_vector_type(2)));
typedef unsigned long long u64;

static __device__ __forceinline__ float bf2f(uint32_t bits16) { return __uint_as_float(bits16 << 16); }
static __device__ __forceinline__ uint32_t f2bf(float f) {
    uint32_t u = __float_as_uint(f);
    if ((u & 0x7fffffffu) > 0x7f800000u) return (u >> 16) | 0x40;
    return (u + 0x7fffu + ((u >> 16) & 1u)) >> 16;
}
static __device__ __forceinline__ float h2f(uint32_t bits16) { return (float)__builtin_bit_cast(_Float16, (unsigned short)bits16); }

#define NB 2048
#define WNULL 0x80000000u
#define CSTEP 0x2aabu
#define CINIT 205u
#define CHV (12u * CSTEP)
#define SLOTO 25088
#define SLOTI 12544
#define EMAX (SLOTO * 32)
#define NT 1024
#define NWG 256
#define KMAX 4
#define NTAIL 4
#define LAYH 2
#define LAYM 1
#define SC 64u
#define PAD 1u
#define SK1 (128u * SC + 2u * PAD)
#define SS1 (4u * SK1 + PAD)
#define SS2 (128u * SC + PAD)
#define SK2 (16u * SS2 + 2u * PAD)
#define SKH (LAYH == 1 ? SK1 : SK2)
#define SSH (LAYH == 1 ? SS1 : SS2)
#define SKM (LAYM == 1 ? SK1 : SK2)
#define SSM (LAYM == 1 ? SS1 : SS2)
#define SQ (16u * SS1)
#define MAINL (4u * SQ)
#define BPL(b) ((((b) >> 3) & 3u) * SQ + ((b) & 7u) + (((b) >> 5) << 3))
#define TST 257u
#define TBASE MAINL
#define TPL (NB * TST)
#define SBASE (TBASE + 2u * TPL)
#define LBASE (SBASE + 128u * 2u * NWG * 9u)
#define NDYN 3
#define NEARM 0x55555554u
#define FARV 1u
#define GR 2048
#define NTS 5
#define M_DONE 0
#define M_TILE (32 * 64)
#define M_LOG (M_TILE + 64)
#define M_MD (M_LOG + 64)
#define M_BEAT (M_MD + 64)
#define M_E0 (M_BEAT + 64)
#define M_WORDS (M_E0 + 64)
#define W_TCNT (M_WORDS * 4)
#define W_STAT (W_TCNT + NWG * NB)
#define W_MDC (W_STAT + 4 * NWG * 8)
#define W_PART (W_MDC + 128 * SLOTI * 4)
#define W_RECS (W_PART + 32 * 16 * SLOTO * 4)
#define TSC1 0
#define ALT 0
#define POS(ff) (ALT ? ((ff) * 11) & 15 : (ff))
#define AUX_NT 2
#define AUX_SC1 16

static __device__ __forceinline__ uint32_t row_max(uint32_t x) {
    x = max(x, (uint32_t)__builtin_amdgcn_update_dpp(0, x, 0x111, 0xf, 0xf, false));
    x = max(x, (uint32_t)__builtin_amdgcn_update_dpp(0, x, 0x112, 0xf, 0xf, false));
    x = max(x, (uint32_t)__builtin_amdgcn_update_dpp(0, x, 0x114, 0xf, 0xf, false));
    return max(x, (uint32_t)__builtin_amdgcn_update_dpp(0, x, 0x118, 0xf, 0xf, false));
}
static __device__ __forceinline__ uint32_t wave_max(uint32_t x) {
    x = row_max(x);
    x = max(x, (uint32_t)__builtin_amdgcn_update_dpp(0, x, 0x142, 0xa, 0xf, false));
    x = max(x, (uint32_t)__builtin_amdgcn_update_dpp(0, x, 0x143, 0xc, 0xf, false));
    return __builtin_amdgcn_readlane(x, 63);
}
static __device__ __forceinline__ float row_sum(float x) {
    x += __int_as_float(__builtin_amdgcn_update_dpp(0, __float_as_int(x), 0x111, 0xf, 0xf, false));
    x += __int_as_float(__builtin_amdgcn_update_dpp(0, __float_as_int(x), 0x112, 0xf, 0xf, false));
    x += __int_as_float(__builtin_amdgcn_update_dpp(0, __float_as_int(x), 0x114, 0xf, 0xf, false));
    return x + __int_as_float(__builtin_amdgcn_update_dpp(0, __float_as_int(x), 0x118, 0xf, 0xf, false));
}
static __device__ __forceinline__ float wave_sum(float x) {
    x = row_sum(x);
    x += __int_as_float(__builtin_amdgcn_update_dpp(0, __float_as_int(x), 0x142, 0xa, 0xf, false));
    x += __int_as_float(__builtin_amdgcn_update_dpp(0, __float_as_int(x), 0x143, 0xc, 0xf, false));
    return __int_as_float(__builtin_amdgcn_readlane(__float_as_int(x), 63));
}
static __device__ __forceinline__ uint32_t bfi(uint32_t mask, uint32_t a, uint32_t b) {
    return __builtin_amdgcn_bitop3_b32(mask, a, b, 0xca);
}
static __device__ __forceinline__ void opaque(int& x) { asm volatile("" : "+v"(x)); }
template <uint32_t C> static __device__ __forceinline__ uint32_t vconst() {
    uint32_t r;
    asm volatile("v_mov_b32 %0, %1" : "=v"(r) : "n"(C));
    return r;
}
static __device__ __forceinline__ uint32_t pk_bf16(float a, float b) {
    const f32x2 v = {a, b};
    return __builtin_bit_cast(uint32_t, __builtin_convertvector(v, bf16x2));
}
static __device__ __forceinline__ void pin(uint32_t& x) { asm volatile("" : "+v"(x)); }

#define RTS(k)
#define ST_DECL
#define ST(i)
#define ST_OUT(off)

static __device__ __forceinline__ void md_of(u32x2 v, const float* w, uint32_t* r) {
    float a0 = bf2f(v.x & 0xffff), a1 = bf2f(v.x >> 16), a2 = bf2f(v.y & 0xffff), a3 = bf2f(v.y >> 16);
#pragma unroll
    for (int j = 0; j < 4; j++) r[j] = f2bf(a0 * w[j] + a1 * w[4 + j] + a2 * w[8 + j] + a3 * w[12 + j]);
}
static __device__ __forceinline__ void load_w(const uint16_t* __restrict__ wdown, float* w) {
    u32x4 wv0 = *(const u32x4*)wdown, wv1 = *(const u32x4*)(wdown + 8);
#pragma unroll
    for (int i = 0; i < 4; i++) {
        w[2 * i] = bf2f(wv0[i] & 0xffffu); w[2 * i + 1] = bf2f(wv0[i] >> 16);
        w[8 + 2 * i] = bf2f(wv1[i] & 0xffffu); w[8 + 2 * i + 1] = bf2f(wv1[i] >> 16);
    }
}
#define LDS __attribute__((address_space(3)))
#define P_CNT 0
#define P_FLAG ((NB + 64) * 4)
#define P_LST (P_FLAG + 256)
#define LCAP 32
#define P_TAB (P_LST + 4 * LCAP * 16)
#define P_BUF (P_TAB + NB * 4)
#define P_LDS (P_BUF + NB * 64)
#define PD 2
#define LPOS 0x448763
#define ABL 0
#define PRIO 2
#define SETP(n) __builtin_amdgcn_s_setprio(n)
#define MDL 3
#define RMAXREV 40.0f
#define HB 88
#define HREG(S, D) (HB + ((S) & 3) * 10 + (((S) >> 2) + (D)) * 2)

template <int N> struct IC { static constexpr int value = N; };
struct Raw { u32x4 o[2], i[2]; u32x2 a[2]; };
static __device__ __forceinline__ uint32_t lds_ld(uint32_t a) { return *(const LDS uint32_t*)(uintptr_t)a; }
static __device__ __forceinline__ void lds_st(uint32_t a, uint32_t v) { *(LDS uint32_t*)(uintptr_t)a = v; }
static __device__ __forceinline__ void lds_or(uint32_t a, uint32_t v) {
    __hip_atomic_fetch_or((LDS uint32_t*)(uintptr_t)a, v, __ATOMIC_RELAXED, __HIP_MEMORY_SCOPE_WORKGROUP);
}
static __device__ __forceinline__ uint32_t lds_add(uint32_t a, uint32_t v) {
    return __hip_atomic_fetch_add((LDS uint32_t*)(uintptr_t)a, v, __ATOMIC_RELAXED, __HIP_MEMORY_SCOPE_WORKGROUP);
}
static __device__ __forceinline__ uint32_t slot_w(uint32_t ca, uint32_t cv) {
    return (ca << 4) | __builtin_amdgcn_bitop3_b32(cv >> 11, ca, 0x3cu, 0x28);
}
static __device__ __forceinline__ uint32_t slot_x2(uint32_t aw, uint32_t ca) {
    return __builtin_amdgcn_bitop3_b32(aw, ca, 0xcu, 0x72);
}
static __device__ __forceinline__ uint32_t slot_x(uint32_t aw) { return slot_x2(aw, aw >> 4); }
template <int R> static __device__ __forceinline__ u32x2 hget() {
    u32x2 d;
    asm volatile("v_mov_b64 %0, v[%1:%2]" : "=v"(d) : "n"(R), "n"(R + 1));
    return d;
}
static __device__ __forceinline__ u32x2 hget_at(int r) {
    u32x2 d;
    asm volatile("s_set_gpr_idx_on %2, gpr_idx(SRC0)\n\ts_nop 1\n\tv_mov_b32 %0, v88\n\tv_mov_b32 %1, v89\n\ts_set_gpr_idx_off"
                 : "=&v"(d.x), "=&v"(d.y) : "s"(r));
    return d;
}
static __device__ __forceinline__ void hset_at(int r, u32x2 d) {
    asm volatile("s_set_gpr_idx_on %2, gpr_idx(DST)\n\ts_nop 1\n\tv_mov_b32 v88, %0\n\tv_mov_b32 v89, %1\n\ts_set_gpr_idx_off"
                 :: "v"(d.x), "v"(d.y), "s"(r));
}

template <int S, int D> static __device__ __forceinline__ uint32_t take(uint32_t V, uint32_t u16, uint32_t la, u32x2 nul) {
    const uint32_t a = __builtin_amdgcn_bitop3_b32(V, u16, 15u, 0x54);
    asm volatile("v_mov_b64 v[%4:%4+1], v[%3:%3+1]\n\tv_cmpx_ne_u32 vcc, -16, %0\n\tds_wrxchg_rtn_b64 v[%4:%4+1], %1, %2 offset:%5\n\ts_mov_b64 exec, -1"
                 :: "v"(a), "v"(la), "v"(nul), "n"(HREG(S, D)), "n"(HREG(S, 1 - D)), "n"(S * 256) : "memory", "vcc");
    return a;
}
template <int S, int W> static __device__ __forceinline__ void put(u32x4 rsA, uint32_t ad, uint32_t pm) {
    asm volatile("s_bitcmp1_b32 %3, %4\n\ts_cbranch_scc0 1f\n\ts_waitcnt lgkmcnt(%5)\n\tbuffer_store_dwordx4 v[%2:%2+3], %0, %1, 0 offen sc1\n"
                 "1:\n\ts_bitcmp1_b32 %3, %4+1\n\ts_cbranch_scc0 2f\n\ts_waitcnt lgkmcnt(%5)\n\tbuffer_store_dwordx4 v[%2:%2+3], %0, %1, 0 offen nt\n\ts_nop 1\n2:"
                 :: "v"(ad), "s"(rsA), "n"(HREG(S, 0)), "s"(pm), "n"(2 * S), "n"(W) : "memory", "scc");
}

__global__ __launch_bounds__(NT) __attribute__((amdgpu_num_vgpr(HB / 2))) void part_kernel(const int* __restrict__ iin, const int* __restrict__ iout,
                                                  const uint16_t* __restrict__ ang, const uint16_t* __restrict__ freq,
                                                  const u32x2* __restrict__ m, const uint16_t* __restrict__ wdown,
                                                  char* __restrict__ recs,
                                                  int T, int E, int ntile, int nstat, int ks, int lcap) {
    ST_DECL
    RTS(0)
    const int tid0 = threadIdx.x;
    const int wv = __builtin_amdgcn_readfirstlane(tid0 >> 6);
    const int G = T >> 2;
    const int pos = (int)((blockIdx.x & 7) << 5) | (int)(blockIdx.x >> 3);
    float frr;
    unsigned* meta = (unsigned*)(recs - W_RECS);
    const uint32_t gbase = (LBASE + (uint32_t)lcap) * 128u;
    const uint32_t rbytes = gbase + (uint32_t)lcap * 2u;
    const __amdgpu_buffer_rsrc_t rsR = __builtin_amdgcn_make_buffer_rsrc(recs, 0, (int)rbytes, 0x00020000);
    const u32x4 rsA = {(uint32_t)(uintptr_t)recs, (uint32_t)((uintptr_t)recs >> 32) & 0xffffu, (ABL & 2) ? 0u : rbytes, 0x00020000u};
    const uint32_t g2 = 2u * (uint32_t)ks;
    const uint32_t far = FARV >> ((blockIdx.x >> 3) & 3u);
    const uint32_t SK = SKM + far * (SKH - SKM), SS = SSM + far * (SSH - SSM), UINC = 0x80000001u + SK * 64u;
    const uint32_t spec0 = gbase - 4u;
    auto fixed_tile = [&](int r) __attribute__((always_inline)) -> int { return r * NWG + ((pos + r * 37) & (NWG - 1)); };
    int tl0 = fixed_tile(0), tl1 = fixed_tile(1), tl2;
    uint32_t tk = 0u;
    const __amdgpu_buffer_rsrc_t rsO = __builtin_amdgcn_make_buffer_rsrc((void*)iout, 0, G * 16, 0x00020000);
    const __amdgpu_buffer_rsrc_t rsI = __builtin_amdgcn_make_buffer_rsrc((void*)iin, 0, G * 16, 0x00020000);
    const __amdgpu_buffer_rsrc_t rsG = __builtin_amdgcn_make_buffer_rsrc((void*)ang, 0, G * 8, 0x00020000);
    uint32_t t8 = (uint32_t)tid0 * 8u;
    auto load_one = [&](Raw& r, int tl, int idx) __attribute__((always_inline)) {
        const int j = idx < 4 ? idx >> 1 : idx & 1;
        const uint32_t g0 = (uint32_t)(((ABL & 1) ? tl & 31 : tl) * GR + j * NT);
        __builtin_amdgcn_sched_barrier(0);
        if (idx >= 4) r.a[j] = __builtin_amdgcn_raw_buffer_load_b64(rsG, t8, g0 * 8u, (ABL & 1) ? 0 : AUX_NT);
        else if (idx & 1) r.i[j] = __builtin_amdgcn_raw_buffer_load_b128(rsI, t8 + t8, g0 * 16u, (ABL & 1) ? 0 : AUX_NT);
        else r.o[j] = __builtin_amdgcn_raw_buffer_load_b128(rsO, t8 + t8, g0 * 16u, (ABL & 1) ? 0 : AUX_NT);
        __builtin_amdgcn_sched_barrier(0);
    };
    auto peek = [&](uint32_t at) __attribute__((always_inline)) -> uint32_t {
        return __hip_atomic_load((unsigned*)((char*)meta + at), __ATOMIC_RELAXED, __HIP_MEMORY_SCOPE_AGENT);
    };
    auto ready = [&]() __attribute__((always_inline)) {
        if (blockIdx.x != 0) {
            const uint32_t x = lds_ld(vconst<P_FLAG + 8>());
            while (peek(vconst<M_BEAT * 4>()) == x) __builtin_amdgcn_s_sleep(8);
        }
    };
    auto beat = [&]() __attribute__((always_inline)) {
        (void)__hip_atomic_fetch_add((unsigned*)((char*)meta + vconst<M_BEAT * 4>()), vconst<1>(), __ATOMIC_RELAXED, __HIP_MEMORY_SCOPE_AGENT);
    };
    auto draw = [&](uint32_t at) __attribute__((always_inline)) -> uint32_t {
        return __hip_atomic_fetch_add((unsigned*)((char*)meta + at), vconst<1>(), __ATOMIC_RELAXED, __HIP_MEMORY_SCOPE_AGENT);
    };
    uint32_t la;
    auto tab_at = [&]() __attribute__((always_inline)) -> uint32_t {
        return P_TAB + bfi(0xffu, bfi(0x3cu, t8 >> 2, t8 << 4), t8);
    };
    u32x2 nul;
    uint32_t U[2];
    {
        const uint32_t t = (uint32_t)tid0;
        la = P_BUF + ((t >> 5) << 12) + ((t & 0x18u) << 3) + ((t & 3u) << 4) + ((t & 4u) << 1);
        const uint32_t q = ((t >> 3) ^ (t >> 1 & 2u)) & 3u;
        nul.x = (0x7u >> q) << 31; nul.y = (0xbu >> q) << 31;
        U[0] = (POS((t >> 1) & 15u) * SS + ((t & 1u) * 64u + (t >> 5)) * SC + BPL(blockIdx.x)) * 128u + (0x80000000u - SK * 64u) + 8u - g2;
        U[1] = U[0] + 32u * SC * 128u;
    }
    auto log_line = [&](bool lg, uint32_t b) __attribute__((always_inline)) -> uint32_t {
        uint32_t lp = 0u;
        if (lg && (t8 & 0x38u) == 0u) {
            ready();
            lp = draw(vconst<M_LOG * 4>());
            __builtin_amdgcn_raw_buffer_store_b16((uint16_t)b, rsR, gbase + lp * 2u, 0, 0);
        }
        return LBASE + (uint32_t)__builtin_amdgcn_ds_bpermute((int)((t8 >> 1) & 0xe0u), (int)lp);
    };
    auto general = [&](int D, bool all, u64 sm) __attribute__((always_inline)) {
        uint32_t todo = __builtin_amdgcn_readfirstlane((uint32_t)sm | (uint32_t)(sm >> 32));
#pragma unroll 1
        while (todo) {
            const uint32_t S = (uint32_t)__builtin_ctz(todo) >> 1;
            todo &= ~(3u << (2u * S));
            pin(t8); pin(la);
            const uint32_t V = lds_ld(P_TAB + (t8 & ~63u) + S * 4u), g = V & 15u;
            const uint32_t col = POS(S) * 128u + ((t8 >> 1) & 0x60u) + (t8 >> 8);
            const bool act = all ? V != 0xffffffffu : (V & 0x80000009u) == 9u;
            const int hr = HREG((int)S, D) - HB;
            uint32_t ln = log_line(act && g > 8u + NTAIL, col);
            if (act) {
                const u32x2 N = *(const LDS u32x2*)(uintptr_t)(la + S * 256u);
                *(LDS u32x2*)(uintptr_t)(la + S * 256u) = nul;
                lds_st(P_TAB + (t8 & ~63u) + S * 4u, vconst<0xffffffffu>());
                if (!(g & 1u)) hset_at(hr, N);
                else {
                    uint32_t a, st;
                    if (g < 8u) { a = (V & ~15u) + ((t8 & 0x38u) << 1); st = 8u; }
                    else {
                        if (g <= 8u + NTAIL) ln = TBASE + ((g - 8u) >> 1) * TPL + col * TST + blockIdx.x;
                        a = ln * 128u + (la & 0x38u); st = 64u;
                    }
                    __builtin_amdgcn_raw_buffer_store_b64(hget_at(hr), rsR, a, 0, AUX_SC1);
                    __builtin_amdgcn_raw_buffer_store_b64(N, rsR, a + st, 0, AUX_SC1);
                }
            }
        }
    };
    unsigned epoch = 0;
    u64 pb2 = 0ull;
    u64 fm = 0ull, sm = 0ull;
    auto look = [&](u32x2 c) __attribute__((always_inline)) {
        uint32_t V[2];
        const bool late = __builtin_amdgcn_ballot_w64(((U[0] | U[1]) & 8u) != 0u) != 0ull;
        epoch++;
        asm volatile("v_mov_b32 %0, -1\n\tv_mov_b32 %1, -1\n\t"
                     "v_cmpx_le_u32 %6, %7, %2\n\tv_mov_b32 %0, %4\n\tv_add_u32 %4, %9, %4\n\tv_subrev_u32 %2, %8, %2\n\ts_mov_b64 exec, -1\n\t"
                     "v_cmpx_le_u32 vcc, %8, %3\n\tv_mov_b32 %1, %5\n\tv_add_u32 %5, %9, %5\n\tv_subrev_u32 %3, %8, %3\n\t"
                     "s_or_b64 %6, %6, exec\n\ts_mov_b64 exec, -1"
                     : "=&v"(V[0]), "=&v"(V[1]), "+v"(c.x), "+v"(c.y), "+v"(U[0]), "+v"(U[1]), "=&s"(fm) : "s"(CHV), "n"(CHV), "s"(UINC) : "vcc", "scc");
        *(LDS u32x2*)(uintptr_t)(P_CNT + t8) = c;
        pin(t8);
        const uint32_t tw = tab_at();
        lds_st(tw, V[0]);
        lds_st(tw + 64u, V[1]);
        if (wv == 1) lds_st(P_CNT + NB * 4 - 256 + (t8 >> 1), vconst<0x40000000u>());
        const u64 pb = __builtin_amdgcn_ballot_w64((int)(V[0] & V[1]) >= 0);
        pb2 = (ABL & 4) ? 0ull : pb;
        sm = 0ull;
        if (__builtin_expect(late, 0)) {
            sm = __builtin_amdgcn_ballot_w64((V[0] & 0x80000009u) == 9u || (V[1] & 0x80000009u) == 9u);
#pragma unroll
            for (int e = 0; e < 2; e++)
                if ((V[e] & 0x8000000fu) == 9u + NTAIL) U[e] -= 2u * UINC;
        }
    };
    u32x4 Va, Vb;

    Raw raw[PD];
    {
        const int tid = tid0;
        asm volatile("" ::: "v88", "v127");
        const uint16_t fq = freq[0];
        if (tid == 128) tk = peek(vconst<M_BEAT * 4>());
        if (blockIdx.x == 0 && wv == 3 && (tid & 63) < M_BEAT / 64) {
            __hip_atomic_store(meta + (tid & 63) * 64, 0u, __ATOMIC_RELAXED, __HIP_MEMORY_SCOPE_AGENT);
            __hip_atomic_store(meta + (tid & 63) * 64 + 1, 0u, __ATOMIC_RELAXED, __HIP_MEMORY_SCOPE_AGENT);
        }
        const __amdgpu_buffer_rsrc_t rsE = __builtin_amdgcn_make_buffer_rsrc((void*)m, 0, E * 8, 0x00020000);
        u32x2 mv[5];
        mv[4] = __builtin_amdgcn_raw_buffer_load_b64(rsE, (t8 & 511u) * (uint32_t)(E >> 6), 0, 0);
#pragma unroll
        for (int q = 0; q < 4; q++)
            mv[q] = __builtin_amdgcn_raw_buffer_load_b64(rsE, t8 & 511u, ((blockIdx.x >> 2) * 256u + (blockIdx.x & 3u) * 16u + (uint32_t)wv + 64u * q) * 512u, AUX_NT);
#pragma unroll
        for (int d = 0; d < MDL; d++) load_one(raw[1], tl0, d);
        const u32x2 ci = {CINIT, CINIT};
        *(LDS u32x2*)(uintptr_t)(P_CNT + tid * 8) = ci;
        if (tid < 64) lds_st(P_CNT + (NB + tid) * 4, 0x40000000u);
        if (tid < 8 && tid != 2) lds_st(P_FLAG + tid * 4, tid == 0 ? 0xffffffffu : 0u);
        if (tid == 128) lds_st(P_FLAG + 8, tk);
        asm("s_nop 0\n\tv_readfirstlane_b32 %0, %1" : "=s"(frr) : "v"(bf2f(fq) * 0.15915494309189535f));
        const uint32_t lane = (uint32_t)tid & 63u;
        int e0[4];
        if (blockIdx.x < SLOTI / 64) {
            float w[16];
            load_w(wdown, w);
            auto dot = [&](u32x2 v, int c) __attribute__((always_inline)) -> f32x2 {
                const f32x2 w0 = {w[c], w[c + 1]}, w1 = {w[4 + c], w[5 + c]}, w2 = {w[8 + c], w[9 + c]}, w3 = {w[12 + c], w[13 + c]};
                return w0 * bf2f(v.x & 0xffffu) + w1 * bf2f(v.x >> 16) + w2 * bf2f(v.y & 0xffffu) + w3 * bf2f(v.y >> 16);
            };
#pragma unroll
            for (int cp = 0; cp < 2; cp++) {
                const f32x2 sd = dot(mv[4], 2 * cp);
#pragma unroll
                for (int e = 0; e < 2; e++) {
                    const int ex = (int)(wave_max(__float_as_uint(sd[e]) & 0x7fffffffu) >> 23);
                    e0[2 * cp + e] = __builtin_amdgcn_readfirstlane(ex == 0 ? 0 : min(max(ex - 129, -100), 100));
                }
                const f32x2 sc = {__uint_as_float((uint32_t)(127 - e0[2 * cp]) << 23), __uint_as_float((uint32_t)(127 - e0[2 * cp + 1]) << 23)};
                u32x4 o;
                u16x2 mx = {0, 0};
                f32x2 sq = {0.f, 0.f};
#pragma unroll
                for (int q = 0; q < 4; q++) {
                    const f32x2 y = dot(mv[q], 2 * cp) * sc;
                    const uint32_t hb = pk_bf16(y.x, y.y);
                    const f32x2 x = {__uint_as_float(hb << 16), __uint_as_float(hb & 0xffff0000u)};
                    o[q] = __builtin_bit_cast(uint32_t, __builtin_amdgcn_cvt_pkrtz(x.x, x.y));
                    mx = __builtin_elementwise_max(mx, __builtin_bit_cast(u16x2, hb & 0x7fff7fffu));
                    sq += x * x;
                }
                *(LDS u32x4*)(uintptr_t)(P_BUF + (cp * 64u + lane) * 272u + wv * 16) = o;
#pragma unroll
                for (int e = 0; e < 2; e++) {
                    const u32x2 st = {wave_max(mx[e]), __float_as_uint(wave_sum(sq[e]))};
                    if (lane == 0u) *(LDS u32x2*)(uintptr_t)(P_LST + ((2 * cp + e) * 16 + wv) * 8) = st;
                }
            }
        }
#pragma unroll
        for (int d = MDL; d < 6; d++) load_one(raw[1], tl0, d);
#pragma unroll
        for (int d = 0; d < 6; d++)
            if (((LPOS >> (4 * d)) & 15) < 4) load_one(raw[0], tl1, d);
        __syncthreads();
        u32x4 mo[2];
#pragma unroll
        for (int cp = 0; cp < 2; cp++) mo[cp] = *(const LDS u32x4*)(uintptr_t)(P_BUF + (cp * 64u + (t8 >> 7)) * 272u + ((t8 + t8) & 0xf0u));
        if (blockIdx.x == 0 && wv == 3) asm volatile("s_waitcnt vmcnt(0)" ::: "memory");
        __syncthreads();
#pragma unroll
        for (int s = 0; s < 16; s++) *(LDS u32x2*)(uintptr_t)(la + s * 256) = nul;
        asm volatile("" : "+v"(raw[1].o[0]), "+v"(raw[1].o[1]), "+v"(raw[1].i[0]), "+v"(raw[1].i[1]), "+v"(raw[1].a[0]), "+v"(raw[1].a[1]));
        if (blockIdx.x < SLOTI / 64) {
            const __amdgpu_buffer_rsrc_t rsM = __builtin_amdgcn_make_buffer_rsrc((void*)(meta + W_MDC / 4), 0, (int)(128u * SLOTI * 4u), 0x00020000);
#pragma unroll
            for (int cp = 0; cp < 2; cp++) {
                const uint32_t row = cp * 64u + (t8 >> 7), c16 = (t8 + t8) & 0xf0u;
                __builtin_amdgcn_raw_buffer_store_b128(mo[cp], rsM, row * (SLOTI * 4u) + c16 + blockIdx.x * 256u, 0, AUX_SC1);
            }
            if (wv == 0) {
                const u32x2 st = *(const LDS u32x2*)(uintptr_t)(P_LST + t8);
                const u32x2 r = {row_max(st.x) | (row_max(st.x ? 0xffffu - st.x : 0u) << 16), __float_as_uint(row_sum(__uint_as_float(st.y)))};
                if ((lane & 15u) == 15u) ((u32x2*)(meta + W_STAT / 4))[(lane >> 4) * NWG + blockIdx.x] = r;
                if (blockIdx.x == 0 && lane < 4u) meta[M_E0 + lane] = (uint32_t)(lane == 0u ? e0[0] : lane == 1u ? e0[1] : lane == 2u ? e0[2] : e0[3]);
            }
        }
    }
    pin(la);
    ST(0)
    RTS(5)

    uint32_t w[8], aw[8], xv[8];
    u64 nm[8];
    auto later2 = [&](int k) __attribute__((always_inline)) {
        uint32_t x0, x1;
        asm volatile("v_lshrrev_b32 %0, 4, %2\n\tv_lshrrev_b32 %1, 4, %5\n\tv_bitop3_b32 %0, %2, %0, 12 bitop3:0x72\n\tv_bitop3_b32 %1, %5, %1, 12 bitop3:0x72\n\t"
                     "s_not_b64 exec, %8\n\tds_write_b32 %2, %3 offset:%10\n\tds_or_b32 %0, %4 offset:%10\n\t"
                     "s_not_b64 exec, %9\n\tds_write_b32 %5, %6 offset:%10\n\tds_or_b32 %1, %7 offset:%10\n\ts_mov_b64 exec, -1"
                     : "=&v"(x0), "=&v"(x1)
                     : "v"(aw[k]), "v"(w[k]), "v"(xv[k]), "v"(aw[k + 1]), "v"(w[k + 1]), "v"(xv[k + 1]), "s"(nm[k]), "s"(nm[k + 1]), "n"(P_BUF) : "memory", "scc");
    };
    auto drain = [&]() __attribute__((always_inline)) {
        if (wv == 1) {
            const uint32_t q = (epoch + 1u) & 3u;
            const uint32_t n = __builtin_amdgcn_readfirstlane(lds_ld(vconst<P_FLAG + 16>() + q * 4u));
            if (__builtin_expect(n != 0u, 0)) {
                pin(t8);
                const uint32_t l = (t8 >> 3) & 63u;
                if (l < min(n, (uint32_t)LCAP)) {
                    const u32x4 e = *(const LDS u32x4*)(uintptr_t)(P_LST + (q * LCAP + l) * 16u);
                    lds_st(P_BUF + e.x, e.y);
                    lds_or(P_BUF + slot_x(e.x), e.z);
                }
                lds_st(vconst<P_FLAG + 16>() + q * 4u, vconst<0>());
            }
        }
    };
    auto again = [&](int D) __attribute__((always_inline)) {
        do {
            general(D, true, fm);
            __syncthreads();
            later2(0); later2(2); later2(4); later2(6);
            drain();
#pragma unroll
            for (int k = 0; k < 8; k++) {
                pin(aw[k]);
                bool due = false;
                if (aw[k] & 0x80000000u) {
                    xv[k] -= CHV;
                    if (xv[k] < CHV) {
                        const uint32_t f = (aw[k] >> 16) & 0x3ffu;
                        aw[k] = slot_w(aw[k] & 0xffffu, xv[k]);
                        xv[k] = f << ((xv[k] >> 8) & 31u);
                        due = true;
                    } else lds_st(vconst<P_FLAG>(), epoch + 1u);
                }
                nm[k] = ~__builtin_amdgcn_ballot_w64(due);
            }
            look(*(const LDS u32x2*)(uintptr_t)(P_CNT + t8));
            asm volatile("s_waitcnt lgkmcnt(0)" ::: "memory");
            __syncthreads();
        } while (__builtin_amdgcn_readfirstlane(lds_ld(vconst<P_FLAG>())) == epoch);
    };
    auto round = [&](auto DD, auto LL, Raw& rc, Raw& rn, int rd) __attribute__((always_inline)) {
        constexpr int D = decltype(DD)::value;
        constexpr bool LAST = decltype(LL)::value == 1, FIRST = decltype(LL)::value == 2;
        const int ng = min(GR, G - tl0 * GR);
        auto hook = [&](int p) __attribute__((always_inline)) {
#pragma unroll
            for (int q = 0; q < 6; q++)
            {
                constexpr int dummy = 0; (void)dummy;
                const int lp = (LPOS >> (4 * q)) & 15;
                if (!LAST && lp < 4 && lp == p) load_one(rc, tl2, q);
                if (!LAST && lp >= 4 && (lp < 8 || lp >= 12 ? lp : lp - 8) == p) load_one(rn, tl1, q);
            }
        };
        uint32_t wn[8], ca[8], px[4], cv[8];
        float mx;
        float cs[2];
        auto convA = [&](int q) __attribute__((always_inline)) {
            if (LAST) return;
            const uint32_t a = rc.a[q >> 1][q & 1];
            const f32x2 av = {__uint_as_float(a << 16), __uint_as_float(a & 0xffff0000u)};
            f32x2 rv;
            asm("v_pk_mul_f32 %0, %1, %2 op_sel_hi:[1,0]" : "=v"(rv) : "v"(av), "s"((u64)__float_as_uint(frr)));
            mx = __builtin_elementwise_maximum(__builtin_elementwise_maximum(__builtin_fabsf(rv.x), __builtin_fabsf(rv.y)), (q & 1) ? mx : 0.f);
            cs[0] = __builtin_amdgcn_cosf(rv.x); cs[1] = __builtin_amdgcn_cosf(rv.y);
            asm volatile("" : "+v"(mx), "+v"(cs[0]), "+v"(cs[1]));
            __builtin_amdgcn_sched_barrier(0);
        };
        auto convB = [&](int k) __attribute__((always_inline)) {
            if (LAST) return;
            const uint32_t o = rc.o[k >> 2][k & 3], i = rc.i[k >> 2][k & 3];
            wn[k] = (__builtin_amdgcn_alignbit(bfi(63u, o, o >> 5), __float_as_uint(cs[k & 1]), 30u) << 14) | (i >> 6);
            ca[k] = (bfi(63u, i, o) << 2) & 0x1ffcu;
            pin(wn[k]); pin(ca[k]);
            __builtin_amdgcn_sched_barrier(0);
        };
        auto convD = [&](int q) __attribute__((always_inline)) {
            if (LAST) return;
            asm volatile("v_cvt_pk_f16_f32 %0, |%1|, |%2|\n\tv_pk_mul_f16 %0, %0, %3 op_sel_hi:[1,0]" : "=v"(px[q]) : "v"(cs[0]), "v"(cs[1]), "s"(0x3ffu));
        };
        uint32_t cst;
        auto check = [&](int j) __attribute__((always_inline)) {
            if (LAST) return;
            if (__builtin_expect((__builtin_amdgcn_ballot_w64(!(mx <= RMAXREV)) | (u64)(uint32_t)(GR - ng)) != 0ull, 0)) {
                pin(t8);
                asm volatile("" : "+v"(rc.a[j]));
                const int lg = j * NT + (int)(t8 >> 3);
#pragma unroll
                for (int k = 0; k < 4; k++) {
                    const uint32_t a = (k & 1) ? (rc.a[j][k >> 1] >> 16) : (rc.a[j][k >> 1] & 0xffffu);
                    const bool nrm = __builtin_fabsf(frr * bf2f(a)) <= RMAXREV;
                    if (!(lg < ng && nrm)) ca[j * 4 + k] = NB * 4u + ((t8 >> 1) & 0xfcu);
                    if (lg < ng && !nrm) {
                        ready();
                        __builtin_amdgcn_raw_buffer_store_b32((uint32_t)((tl0 * GR + lg) * 4 + k), rsR, spec0 - draw(vconst<M_LOG * 4 + 4>()) * 4u, 0, 0);
                    }
                }
            }
            cst = vconst<CSTEP>();
        };
        auto ticket = [&](int k) __attribute__((always_inline)) {
            if (LAST) return;
            cv[k] = lds_add(P_CNT + ca[k], cst);
            __builtin_amdgcn_sched_barrier(0);
        };
        auto insert = [&](int j) __attribute__((always_inline)) {
            const int k0 = 4 * j;
            const bool odd = __builtin_amdgcn_ballot_w64(max(max(cv[k0], cv[k0 + 1]), max(cv[k0 + 2], cv[k0 + 3])) >= 2u * CHV) != 0ull;
#pragma unroll
            for (int k = k0; k < k0 + 4; k += 2) {
                uint32_t x0, x1;
                asm volatile("v_lshrrev_b32 %6, 11, %8\n\tv_lshrrev_b32 %7, 11, %9\n\t"
                             "v_bitop3_b32 %6, %6, %10, 60 bitop3:0x28\n\tv_bitop3_b32 %7, %7, %11, 60 bitop3:0x28\n\t"
                             "v_lshl_or_b32 %0, %10, 4, %6\n\tv_lshl_or_b32 %1, %11, 4, %7\n\t"
                             "v_lshlrev_b32_sdwa %2, %8, %12 dst_sel:DWORD dst_unused:UNUSED_PAD src0_sel:BYTE_1 src1_sel:WORD_0\n\t"
                             "v_lshlrev_b32_sdwa %3, %9, %12 dst_sel:DWORD dst_unused:UNUSED_PAD src0_sel:BYTE_1 src1_sel:WORD_1\n\t"
                             "v_bitop3_b32 %6, %0, %10, 12 bitop3:0x72\n\tv_bitop3_b32 %7, %1, %11, 12 bitop3:0x72\n\t"
                             "v_cmpx_gt_u32 %4, %15, %8\n\tds_write_b32 %0, %13 offset:%16\n\tds_or_b32 %6, %2 offset:%16\n\ts_mov_b64 exec, -1\n\t"
                             "v_cmpx_gt_u32 %5, %15, %9\n\tds_write_b32 %1, %14 offset:%16\n\tds_or_b32 %7, %3 offset:%16\n\ts_mov_b64 exec, -1"
                             : "=&v"(aw[k]), "=&v"(aw[k + 1]), "=&v"(xv[k]), "=&v"(xv[k + 1]), "=&s"(nm[k]), "=&s"(nm[k + 1]), "=&v"(x0), "=&v"(x1)
                             : "v"(cv[k]), "v"(cv[k + 1]), "v"(ca[k]), "v"(ca[k + 1]), "v"(px[k >> 1]), "v"(wn[k]), "v"(wn[k + 1]), "s"(CHV), "n"(P_BUF) : "memory");
                w[k] = wn[k]; w[k + 1] = wn[k + 1];
                if (__builtin_expect(odd, 0)) {
#pragma unroll
                    for (int q = k; q < k + 2; q++) {
                        nm[q] |= __builtin_amdgcn_ballot_w64(cv[q] >= 2u * CHV);
                        if (cv[q] >= 2u * CHV && cv[q] < 0x40000000u) {
                            const uint32_t lq = (epoch + 2u) & 3u;
                            uint32_t p = LCAP;
                            if (cv[q] < 3u * CHV) p = lds_add(vconst<P_FLAG + 16>() + lq * 4u, vconst<1>());
                            if (p < LCAP) {
                                const u32x2 e = {aw[q], w[q]};
                                *(LDS u32x2*)(uintptr_t)(P_LST + (lq * LCAP + p) * 16u) = e;
                                lds_st(P_LST + (lq * LCAP + p) * 16u + 8u, xv[q]);
                            } else {
                                aw[q] = ca[q] | ((q & 1) ? (px[q >> 1] & 0xffff0000u) : (px[q >> 1] << 16)) | 0x80000000u;
                                xv[q] = cv[q] - CHV;
                                lds_st(vconst<P_FLAG>(), epoch);
                            }
                        }
                    }
                }
            }
        };
        constexpr int P0 = D ? 0 : 3, P1 = D ? 1 : 2, P2 = D ? 2 : 1, P3 = D ? 3 : 0;
        const uint32_t fl = lds_ld(vconst<P_FLAG>());
        if (!LAST) asm volatile("" : "+v"(rc.o[0]), "+v"(rc.o[1]), "+v"(rc.i[0]), "+v"(rc.i[1]), "+v"(rc.a[0]), "+v"(rc.a[1]));
        if (!LAST && (rd + 4 >= nstat || tl1 >= ntile) && wv == 2) {
            pin(tk);
            if ((t8 & 511u) == 0u) {
                if (tl1 >= ntile) {
                    if (rd + 3 <= nstat) ready();
                    (void)draw(vconst<M_MD * 4>());
                } else if (rd + 4 == nstat) tk = peek(vconst<M_BEAT * 4>());
                else {
                    if (rd + 3 == nstat) {
                        if (tk == lds_ld(vconst<P_FLAG + 8>())) ready();
                        tk = 0u;
                    }
                    lds_st(vconst<P_FLAG + 4>(), tk);
                    if ((int)tk < ntile - nstat * NWG) tk = draw(vconst<M_TILE * 4>());
                }
            }
        }
        if (!LAST && blockIdx.x == 0 && wv == 3 && (t8 & 511u) == 0u) beat();
        hook(12);
        ST(3)
        pin(t8);
        const uint32_t tr = P_TAB + (t8 & ~63u), u16 = (t8 + t8) & 0x70u;
        convA(0);
        convB(0);
        const bool ag = __builtin_amdgcn_readfirstlane(fl) == epoch;
        if (!FIRST && __builtin_expect(LAST || ag || sm != 0ull, 0)) {
            if (ag) again(D);
            general(D, false, sm);
            Va = *(const LDS u32x4*)(uintptr_t)(P_TAB + (t8 & ~63u) + P0 * 16);
            Vb = *(const LDS u32x4*)(uintptr_t)(P_TAB + (t8 & ~63u) + P1 * 16);
            asm volatile("" : "+v"(Va), "+v"(Vb));
        }
        uint32_t pq = (uint32_t)pb2 | (uint32_t)(pb2 >> 32);
        pq = (pq | (pq >> 1)) & 0x55555555u;
        pq = __builtin_amdgcn_readfirstlane(pq + (pq & (far * NEARM)));
        uint32_t ad[8];
        if (!FIRST) ad[0] = take<4 * P0 + 0, D>(Va[0], u16, la, nul);
        convB(1);
        if (!FIRST) ad[1] = take<4 * P0 + 1, D>(Va[1], u16, la, nul);
        convD(0);
        convA(1);
        if (!FIRST) ad[2] = take<4 * P0 + 2, D>(Va[2], u16, la, nul);
        convB(2);
        hook(13);
        if (!FIRST) ad[3] = take<4 * P0 + 3, D>(Va[3], u16, la, nul);
        convB(3);
        if (!FIRST) put<4 * P0 + 0, LAST ? 3 : 3>(rsA, ad[0], pq);
        if (!FIRST) ad[4] = take<4 * P1 + 0, D>(Vb[0], u16, la, nul);
        convD(1);
        check(0);
        ticket(0);
        hook(4);
        if (!FIRST) put<4 * P0 + 1, LAST ? 3 : 4>(rsA, ad[1], pq);
        if (!FIRST) ad[5] = take<4 * P1 + 1, D>(Vb[1], u16, la, nul);
        convA(2);
        ticket(1);
        if (!FIRST) put<4 * P0 + 2, LAST ? 3 : 5>(rsA, ad[2], pq);
        if (!FIRST) ad[6] = take<4 * P1 + 2, D>(Vb[2], u16, la, nul);
        convB(4);
        ticket(2);
        if (!FIRST) put<4 * P0 + 3, LAST ? 3 : 6>(rsA, ad[3], pq);
        if (!FIRST) Va = *(const LDS u32x4*)(uintptr_t)(tr + P2 * 16);
        __builtin_amdgcn_sched_barrier(0);
        if (!FIRST) ad[7] = take<4 * P1 + 3, D>(Vb[3], u16, la, nul);
        convB(5);
        ticket(3);
        SETP(2);
        if (!FIRST) put<4 * P1 + 0, LAST ? 4 : 8>(rsA, ad[4], pq);
        if (!FIRST) ad[0] = take<4 * P2 + 0, D>(Va[0], u16, la, nul);
        convD(2);
        convA(3);
        hook(5);
        if (!FIRST) put<4 * P1 + 1, LAST ? 4 : 7>(rsA, ad[5], pq);
        if (!FIRST) ad[1] = take<4 * P2 + 1, D>(Va[1], u16, la, nul);
        convB(6);
        if (!FIRST) put<4 * P1 + 2, LAST ? 4 : 6>(rsA, ad[6], pq);
        if (!FIRST) ad[2] = take<4 * P2 + 2, D>(Va[2], u16, la, nul);
        convB(7);
        if (!FIRST) put<4 * P1 + 3, LAST ? 3 : 4>(rsA, ad[7], pq);
        if (!FIRST) Vb = *(const LDS u32x4*)(uintptr_t)(tr + P3 * 16);
        __builtin_amdgcn_sched_barrier(0);
        if (!FIRST) ad[3] = take<4 * P2 + 3, D>(Va[3], u16, la, nul);
        convD(3);
        check(1);
        ticket(4);
        SETP(1);
        hook(6);
        if (!FIRST) put<4 * P2 + 0, LAST ? 4 : 5>(rsA, ad[0], pq);
        if (!FIRST) ad[4] = take<4 * P3 + 0, D>(Vb[0], u16, la, nul);
        ticket(5);
        if (!FIRST) put<4 * P2 + 1, LAST ? 4 : 6>(rsA, ad[1], pq);
        if (!FIRST) ad[5] = take<4 * P3 + 1, D>(Vb[1], u16, la, nul);
        ticket(6);
        if (!FIRST) put<4 * P2 + 2, LAST ? 4 : 7>(rsA, ad[2], pq);
        if (!FIRST) ad[6] = take<4 * P3 + 2, D>(Vb[2], u16, la, nul);
        ticket(7);
        SETP(0);
        if (!FIRST) put<4 * P2 + 3, LAST ? 3 : 7>(rsA, ad[3], pq);
        if (!FIRST) ad[7] = take<4 * P3 + 3, D>(Vb[3], u16, la, nul);
        hook(7);
        ST(4)
        SETP(PRIO > 1 ? 3 : 0);
        asm volatile("s_waitcnt lgkmcnt(0)" ::: "memory");
        __syncthreads();
        ST(5)
        u32x2 cn;
        uint32_t tv;
        if (!LAST) { cn = *(const LDS u32x2*)(uintptr_t)(P_CNT + t8); tv = lds_ld(vconst<P_FLAG + 4>()); }
        __builtin_amdgcn_sched_barrier(0);
        if (!FIRST) later2(0);
        if (!FIRST) put<4 * P3, 15>(rsA, ad[4], pq);
        if (!FIRST) later2(2);
        if (!FIRST) put<4 * P3 + 1, 15>(rsA, ad[5], pq);
        if (!FIRST) drain();
        if (!LAST) {
            tl2 = rd + 2 < nstat ? fixed_tile(rd + 2) : nstat * NWG + (int)__builtin_amdgcn_readfirstlane(tv);
            look(cn);
        }
        hook(0);
        SETP(PRIO > 1 ? 2 : 0);
        if (!FIRST) later2(4);
        if (!FIRST) put<4 * P3 + 2, 15>(rsA, ad[6], pq);
        if (!FIRST) later2(6);
        if (!FIRST) put<4 * P3 + 3, 15>(rsA, ad[7], pq);
        if (!LAST) {
            hook(1);
            SETP(PRIO > 1 ? 1 : 0);
            insert(0);
            hook(2);
            SETP(0);
            pin(t8);
            Va = *(const LDS u32x4*)(uintptr_t)(P_TAB + (t8 & ~63u) + P3 * 16);
            Vb = *(const LDS u32x4*)(uintptr_t)(P_TAB + (t8 & ~63u) + P2 * 16);
            __builtin_amdgcn_sched_barrier(0);
            insert(1);
            hook(3);
            ST(1)
            SETP(3);
            asm volatile("s_waitcnt lgkmcnt(0)" ::: "memory");
            __syncthreads();
            ST(2)
            tl0 = tl1; tl1 = tl2;
        }
    };
#define HMV(A, B) "v_mov_b64 v[" #A ":" #A "+1], v[" #B ":" #B "+1]\n\t"
#define HMV4(A) HMV(A + 8, A + 6) HMV(A + 6, A + 4) HMV(A + 4, A + 2) HMV(A + 2, A)
    round(IC<1>(), IC<2>(), raw[1], raw[0], 0);
    int rd = 1;
#pragma unroll 1
    for (; tl1 < ntile; rd += 2) {
        round(IC<0>(), IC<0>(), raw[0], raw[1], rd);
        round(IC<1>(), IC<0>(), raw[1], raw[0], rd + 1);
    }
    if (tl0 >= ntile) asm volatile(HMV4(88) HMV4(98) HMV4(108) HMV4(118));
    asm volatile("" : "+s"(tl0));
    if (tl0 < ntile) round(IC<0>(), IC<0>(), raw[0], raw[1], rd);
    RTS(1)
    round(IC<1>(), IC<1>(), raw[1], raw[0], 0);
    if (__builtin_amdgcn_readfirstlane(lds_ld(P_FLAG + 16 + ((epoch + 2u) & 3u) * 4u)) != 0u) {
        asm volatile("s_waitcnt lgkmcnt(0)" ::: "memory");
        __syncthreads();
        look(*(const LDS u32x2*)(uintptr_t)(P_CNT + t8));
        general(0, true, fm);
        __syncthreads();
        drain();
    }
    {
        uint32_t* tcnt = meta + W_TCNT / 4;
        const u32x2 c = *(const LDS u32x2*)(uintptr_t)(P_CNT + t8);
        const uint32_t tw = tab_at();
#pragma unroll
        for (int e = 0; e < 2; e++) {
            const uint32_t g = U[e] & 15u;
            lds_st(tw + e * 64u, g >= 8u ? 0x80000000u : ((U[e] + ((g & 1u) ? 0u : SK * 64u)) & 0x7fffff81u) | (((7u - g) << 1) & 12u));
            *(LDS uint8_t*)(uintptr_t)(P_CNT + (t8 & ~511u) + (((tw - P_TAB) >> 2) & 127u) + e * 16u) =
                (uint8_t)(g < 8u ? 0u : ((g - 8u) >> 1) + (((c[e] + 2u * CSTEP) >> 15) << 2) + (g & 1u) * 0x50u);
        }
        asm volatile("s_waitcnt lgkmcnt(0)" ::: "memory");
        __syncthreads();
        const uint32_t u16 = (t8 + t8) & 0x70u;
        u32x4 En[4];
#pragma unroll
        for (int p = 0; p < 4; p++) En[p] = *(const LDS u32x4*)(uintptr_t)(P_TAB + (t8 & ~63u) + p * 16u);
        const u32x4 cb = *(const LDS u32x4*)(uintptr_t)(P_CNT + (t8 & ~511u) + ((t8 >> 2) & 0x70u));
        u32x2 N[16];
#pragma unroll
        for (int s = 0; s < 16; s++) N[s] = *(const LDS u32x2*)(uintptr_t)(la + s * 256u);
        if (u16 == 0u) *(u32x4*)(tcnt + blockIdx.x * 512u + (t8 >> 4)) = cb;
        const uint32_t sg = P_BUF + ((t8 & ~63u) << 4);
        uint32_t ps;
        auto rest = [&](auto SL) __attribute__((always_inline)) {
            constexpr int s = decltype(SL)::value;
            const uint32_t en = En[s >> 2][s & 3], by = cb[s >> 2] >> ((s & 3) * 8);
            const u32x2 Hr = hget<HREG(s, 0)>();
            const uint32_t at = ps + ((la & 0x38u) ^ ((s & 3) << 4));
            *(LDS u32x2*)(uintptr_t)at = Hr;
            *(LDS u32x2*)(uintptr_t)(at + (by & 0x40u)) = N[s];
            ps += (by & 0x3cu) << 2;
            const u32x2 Hs = (en & 1u) ? Hr : nul;
            const u32x4 d = {Hs.x, Hs.y, N[s].x, N[s].y};
            uint32_t a = __builtin_amdgcn_bitop3_b32(en, u16, 15u, 0x54);
            {
                const u32x4 rs_ = rsA;
                const uint32_t far_ = ((NEARM >> (2 * s)) & 1u) ? far : 0u;
                asm volatile("s_cmp_eq_u32 %3, 0\n\ts_cbranch_scc0 1f\n\tbuffer_store_dwordx4 %0, %1, %2, 0 offen sc1\n\ts_branch 2f\n"
                             "1:\n\tbuffer_store_dwordx4 %0, %1, %2, 0 offen nt\n2:\n\ts_nop 1" :: "v"(d), "v"(a), "s"(rs_), "s"(far_) : "memory", "scc");
            }
            if (__builtin_expect(__builtin_amdgcn_ballot_w64((en & 12u) != 0u) != 0ull, 0)) {
                const u32x4 dn = {nul.x, nul.y, nul.x, nul.y};
#pragma unroll 1
                for (uint32_t k = (en >> 2) & 3u; k > 0u; k--) {
                    a += SK * 128u;
                    __builtin_amdgcn_raw_buffer_store_b128(dn, rsR, a, 0, AUX_SC1);
                }
            }
        };
        auto pack = [&](int p) __attribute__((always_inline)) {
            const uint32_t a = (SBASE + ((((t8 >> 1) & 0x60u) + (t8 >> 8)) * 2u + p) * (NWG * 9u) + blockIdx.x * 9u) * 128u + u16;
            asm volatile("" ::: "memory");
#pragma unroll 1
            for (uint32_t q = 0; __builtin_amdgcn_ballot_w64(sg + q < ps) != 0ull; q += 256u) {
                const u32x4 d0 = *(const LDS u32x4*)(uintptr_t)(sg + q + u16), d1 = *(const LDS u32x4*)(uintptr_t)(sg + q + 128u + u16);
                __builtin_amdgcn_raw_buffer_store_b128(d0, rsR, sg + q < ps ? a + q : 0xffffff00u, 0, AUX_SC1);
                __builtin_amdgcn_raw_buffer_store_b128(d1, rsR, sg + q + 128u < ps ? a + q + 128u : 0xffffff00u, 0, AUX_SC1);
            }
            asm volatile("" ::: "memory");
        };
        asm volatile("" : "+v"(N[0]), "+v"(N[1]), "+v"(N[2]), "+v"(N[3]), "+v"(N[4]), "+v"(N[5]), "+v"(N[6]), "+v"(N[7]) :: "memory");
        asm volatile("" : "+v"(N[8]), "+v"(N[9]), "+v"(N[10]), "+v"(N[11]), "+v"(N[12]), "+v"(N[13]), "+v"(N[14]), "+v"(N[15]) :: "memory");
        ps = sg;
        rest(IC<0>()); rest(IC<1>()); rest(IC<2>()); rest(IC<3>()); rest(IC<4>()); rest(IC<5>()); rest(IC<6>()); rest(IC<7>());
        pack(0);
        ps = sg;
        rest(IC<8>()); rest(IC<9>()); rest(IC<10>()); rest(IC<11>()); rest(IC<12>()); rest(IC<13>()); rest(IC<14>()); rest(IC<15>());
        pack(1);
    }
    RTS(2)
    ST(6)
    if (blockIdx.x == 0 && t8 == 1536u) {
        do {
            beat();
            __builtin_amdgcn_s_sleep(8);
        } while (peek(vconst<M_MD * 4>()) < NWG);
    }
    RTS(3)
    RTS(4)
    ST(7)
    ST_OUT(0)
}

#define A_MD 0
#define A_ACC (SLOTI * 4)
#define A_FLAG (A_ACC + SLOTO * 4)
#define A_LDS (A_FLAG + 512)
static_assert(SLOTI * 4 == 3 * NT * 16 + 1024, "md staging assumes three full vectors and 256 dwords");
#define BIAS 0x40004000u
#define NFAST 8
#define NLD 10
#define PRE0 2
#define PRE1 4
#define MDF 0x0a11333
#define MDP 0xaaaaa
#define ROTH 0
#define ROTP 0
#define HAUX AUX_NT
#define FALL 0
#define V1 3
#define HP 2
#define WARMW 0
#define OAUX 2
#define MDQ 0x00888
#define UNM 1
#define APRIO 0x543
#define SHARE (SLOTO / 8)
#define SPINCAP 4096u
#define ZSIG 9.0f
#define HSLOW 0x80008000u
#define GAS __attribute__((address_space(1)))

__global__ __launch_bounds__(NT) __attribute__((amdgpu_num_vgpr(53))) void acc_kernel(char* ws, int T, int E, int ks, int lcap, float zf, int pad,
                                                 const uint16_t* __restrict__ centers, const uint16_t* __restrict__ wrbf,
                                                 const uint16_t* __restrict__ wsbf, const uint16_t* __restrict__ wup,
                                                 const u32x2* __restrict__ m, const uint16_t* __restrict__ dist, u32x2* __restrict__ out,
                                                 const int* __restrict__ iin, const int* __restrict__ iout, const uint16_t* __restrict__ ang,
                                                 const uint16_t* __restrict__ wdown, const uint16_t* __restrict__ freq) {
    const char* recs = ws + W_RECS;
    const u32x4* tcnt = (const u32x4*)(ws + W_TCNT);
    const uint32_t* mdc = (const uint32_t*)(ws + W_MDC);
    const u32x4* stat = (const u32x4*)(ws + W_STAT);
    uint32_t* partial = (uint32_t*)(ws + W_PART);
    unsigned* meta = (unsigned*)ws;
    extern __shared__ __attribute__((aligned(16))) char smem[];
    ST_DECL
    RTS(8)
    unsigned* acc = (unsigned*)(smem + A_ACC);
    unsigned* s_flag = (unsigned*)(smem + A_FLAG);
    const int tid = threadIdx.x;
    const int lane = tid & 63;
    const int wv = __builtin_amdgcn_readfirstlane(tid >> 6);
    const int cp = (blockIdx.x >> 3) & 1;
    const int h = blockIdx.x & 3;
    const int j = ((blockIdx.x >> 2) & 1) * 16 + (blockIdx.x >> 4);
    u32x4 tc;
    const __amdgpu_buffer_rsrc_t rsM = __builtin_amdgcn_make_buffer_rsrc((void*)mdc, 0, (int)(128u * SLOTI * 4u), 0x00020000);

    u32x4 pm[4];
    u32x4 ring[NLD];
    auto load_md = [&](int ff, int pl, int mdp) __attribute__((always_inline)) {
        const uint32_t so = (uint32_t)(cp * 64 + ((ABL & 64) ? 0 : ff * 4) + h) * (SLOTI * 4u);
#pragma unroll
        for (int v = 0; v < 3; v++)
            if (pl < 0 || pl == ((mdp >> (4 * v)) & 15)) pm[v] = __builtin_amdgcn_raw_buffer_load_b128(rsM, (uint32_t)tid * 16u, so + (uint32_t)v * (NT * 16u), 0);
        if ((pl < 0 || pl == (mdp & 15)) && wv == 0) pm[3] = __builtin_amdgcn_raw_buffer_load_b128(rsM, (uint32_t)tid * 16u, so + 3u * NT * 16u, 0);
        __builtin_amdgcn_sched_barrier(0);
    };
    const uint32_t lf = (uint32_t)(tid >> 9) * (SKH * 128u) + (uint32_t)(tid & 0x1e0) * 16u + (uint32_t)(tid & 12) * 32u + (uint32_t)(tid & 16) * 4u + (uint32_t)(tid & 3) * 16u;
    const uint32_t lp = (uint32_t)(tid >> 8) * (SKM * 128u) + (uint32_t)((tid >> 2) & 63) * 128u + (uint32_t)(tid & 3) * 16u;
    auto load_main = [&](int i, int ff) __attribute__((always_inline)) -> u32x4 {
        const uint32_t q = (uint32_t)(((i >> 1) - HP) & 3), SK = q ? SKM : SKH, SS = q ? SSM : SSH;
        const __amdgpu_buffer_rsrc_t rs = __builtin_amdgcn_make_buffer_rsrc((void*)(recs + (q * SQ + (uint32_t)ff * SS + (uint32_t)(h * 32 + j) * SC) * 128u), 0, ks * (int)(SK * 128u), 0x00020000);
        if (q == 0u) return __builtin_amdgcn_raw_buffer_load_b128(rs, lf, (i & 1) * (int)(2u * SK * 128u), AUX_NT);
        return __builtin_amdgcn_raw_buffer_load_b128(rs, lp + (uint32_t)(i & 1) * 64u, 0, 0);
    };
    const __amdgpu_buffer_rsrc_t rsT = __builtin_amdgcn_make_buffer_rsrc((void*)recs, 0, (int)(LBASE * 128u), 0x00020000);
    const uint32_t tof = (uint32_t)(lane & 2) * (TPL * 64u) + (uint32_t)(tid >> 2) * 128u + (uint32_t)(lane & 1) * 64u + 4u + (TBASE + (uint32_t)(h * 32 + j) * TST) * 128u;
    const uint32_t sp0 = (SBASE + (uint32_t)(h * 32 + j) * (2u * NWG * 9u) + (uint32_t)(tid >> 2) * 9u) * 128u;
    auto tail_rank = [&](uint32_t c, uint32_t sp, int ff, uint32_t t0, int& dst, uint32_t& en) __attribute__((always_inline)) -> int {
        const int k4 = (int)(((uint32_t)(lane & 3) + t0) * 4u) - (int)((c & 3u) * 8u);
        const int left = (int)((c >> 2) & 15u) - k4;
        const bool has = left > 0;
        uint32_t ea = tof + (uint32_t)POS(ff) * (128u * TST * 128u), eb = sp + (uint32_t)(k4 * 16 + min(max(left, 0), 4));
        asm volatile("" : "+v"(ea), "+v"(eb));
        en = k4 < 0 ? ea : eb;
        const u64 mask = __builtin_amdgcn_ballot_w64(has);
        const int n = __builtin_popcountll(mask);
        const int rank = __builtin_amdgcn_mbcnt_hi((uint32_t)(mask >> 32), __builtin_amdgcn_mbcnt_lo((uint32_t)mask, 0u));
        const int r = has ? rank : (n + lane - rank);
        dst = ((r & 15) << 4) | ((r >> 4) << 2);
        return n;
    };
    auto load_tail = [&](uint32_t en, u64& tm) __attribute__((always_inline)) -> u32x4 {
        const bool on = (uint32_t)(lane & 3) < (en & 7u) && !(ABL & 32);
        tm = __builtin_amdgcn_ballot_w64(on);
        return __builtin_amdgcn_raw_buffer_load_b128(rsT, on ? (en & ~15u) + (uint32_t)(lane & 3) * 16u : 0xffffff00u, 0, 0);
    };
    f32x2 fa[4][4];
    u32x4 em[2];
    u32x2 dw2;
    uint32_t wl;
    const __amdgpu_buffer_rsrc_t rsE = __builtin_amdgcn_make_buffer_rsrc((void*)m, 0, E * 8, 0x00020000);
    const __amdgpu_buffer_rsrc_t rsD = __builtin_amdgcn_make_buffer_rsrc((void*)dist, 0, E * 2, 0x00020000);
    auto edge0 = [&](int sh) __attribute__((always_inline)) -> uint32_t {
        const int ol = sh * SHARE + tid * 4;
        return (tid < SHARE / 4) ? (uint32_t)(((ol >> 6) << 11) | (j << 6) | (ol & 63)) : 0x0fffff00u;
    };
    auto load_m = [&](int sh) __attribute__((always_inline)) {
        em[0] = __builtin_amdgcn_raw_buffer_load_b128(rsE, edge0(sh) * 8u, 0, AUX_NT);
        em[1] = __builtin_amdgcn_raw_buffer_load_b128(rsE, edge0(sh) * 8u + 16u, 0, AUX_NT);
    };
    auto load_d = [&](int sh) __attribute__((always_inline)) { dw2 = __builtin_amdgcn_raw_buffer_load_b64(rsD, edge0(sh) * 2u, 0, AUX_NT); };
    auto load_wl = [&]() __attribute__((always_inline)) {
        const u64 p0 = (u64)wsbf, p1 = (u64)wrbf, p2 = (u64)centers, p3 = (u64)wup;
        const u64 pa = lane < 16 ? p0 : p1, pb = lane < 40 ? p2 : p3;
        wl = ((const GAS uint32_t*)(lane < 32 ? pa : pb))[lane & (lane < 32 ? 15 : lane < 40 ? 3 : 7)];
    };
    auto factor = [&](int r, bool first) __attribute__((always_inline)) {
        const float sq = 3.7982826f;
        const f32x2 d01 = {bf2f(dw2.x & 0xffffu), bf2f(dw2.x >> 16)}, d23 = {bf2f(dw2.y & 0xffffu), bf2f(dw2.y >> 16)};
        const uint32_t cw = __builtin_amdgcn_readlane(wl, 32 + (r >> 1));
        const float cn = bf2f((r & 1) ? (cw >> 16) : (cw & 0xffffu));
        f32x2 w[4];
#pragma unroll
        for (int k = 0; k < 4; k++) {
            const uint32_t x = __builtin_amdgcn_readlane(wl, (k >> 1) * 16 + r * 2 + (k & 1));
            w[k].x = bf2f(x & 0xffffu); w[k].y = bf2f(x >> 16);
        }
        const f32x2 y01 = (d01 - cn) * sq, y23 = (d23 - cn) * sq;
        const f32x2 t01 = -y01 * y01, t23 = -y23 * y23;
        const float rb[4] = {__builtin_amdgcn_exp2f(t01.x), __builtin_amdgcn_exp2f(t01.y), __builtin_amdgcn_exp2f(t23.x), __builtin_amdgcn_exp2f(t23.y)};
#pragma unroll
        for (int q = 0; q < 4; q++) {
#pragma unroll
            for (int k = 0; k < 4; k++) fa[q][k] = first ? w[k] * rb[q] : __builtin_elementwise_fma(w[k], (f32x2)(rb[q]), fa[q][k]);
        }
    };
    auto edges = [&](int sh) __attribute__((always_inline)) {
        load_d(sh);
        load_m(sh);
        factor(0, true);
#pragma unroll 1
        for (int r = 1; r < 8; r++) factor(r, false);
    };
#define ADT(V, O) "ds_write_addtid_b32 " V " offset:" #O "\n\t"
    auto store_md = [&]() __attribute__((always_inline)) {
        asm volatile("s_mov_b32 m0, %12\n\ts_nop 0\n\t" ADT("%0", 0) ADT("%1", 256) ADT("%2", 512) ADT("%3", 768) ADT("%4", 16384) ADT("%5", 16640) ADT("%6", 16896) ADT("%7", 17152)
                     ADT("%8", 32768) ADT("%9", 33024) ADT("%10", 33280) ADT("%11", 33536)
                     :: "v"(pm[0][0]), "v"(pm[0][1]), "v"(pm[0][2]), "v"(pm[0][3]), "v"(pm[1][0]), "v"(pm[1][1]), "v"(pm[1][2]), "v"(pm[1][3]),
                        "v"(pm[2][0]), "v"(pm[2][1]), "v"(pm[2][2]), "v"(pm[2][3]), "s"(wv * 1024) : "memory");
        if (wv == 0) asm volatile("s_mov_b32 m0, 0\n\ts_nop 0\n\t" ADT("%0", 49152) ADT("%1", 49408) ADT("%2", 49664) ADT("%3", 49920)
                                  :: "v"(pm[3][0]), "v"(pm[3][1]), "v"(pm[3][2]), "v"(pm[3][3]) : "memory");
    };
    const int me = h * 2 + cp;
    const bool fin = wv < (SHARE / 4 + 63) / 64;
    bool fast, more;
    uint32_t need, mg0, mg1;
    uint32_t guard = 0;
    uint32_t sgn = 0x8000u;
    pin(sgn);

#define GAT3(M) { uint32_t a0_, a1_, a2_; asm volatile( \
        "v_lshlrev_b16 %0, 2, %3\n\tv_lshlrev_b16 %1, 2, %4\n\tv_lshlrev_b16 %2, 2, %5\n\t" \
        "ds_read_b32 v[%6], %0\n\tds_read_b32 v[%6+1], %1\n\tds_read_b32 v[%6+2], %2" \
        : "=&v"(a0_), "=&v"(a1_), "=&v"(a2_) : "v"(w0), "v"(w1), "v"(w2), "n"(M) : "memory"); }
#define MIXL " op_sel_hi:[1,1,0]\n\t"
#define MIXH " op_sel:[0,1,0] op_sel_hi:[1,1,0]\n\t"
#define SDW " dst_sel:DWORD dst_unused:UNUSED_PAD src0_sel:DWORD src1_sel:WORD_1\n\t"
#define FOLD "v_bitop3_b32 %6, %6, v[%15-9], v[%15-8] bitop3:0xfe\n\tv_or_b32 %6, v[%15-7], %6\n\t"
#define DADD(R) "ds_add_rtn_u32 " R
#define SCAT(PRE, POST, M) { uint32_t c0_, c1_, c2_, a0_, a1_, a2_; asm volatile( \
        FOLD "v_and_b32 %0, 0x3ff, %7\n\tv_lshrrev_b32 %2, 22, %7\n\t" PRE \
        "v_bitop3_b32 %0, %11, %8, %0 bitop3:0xea\n\tv_bitop3_b32 %2, %11, %10, %2 bitop3:0xea\n\tv_bfe_u32 %1, %7, 11, 10\n\t" \
        "v_fma_mix_f32 %3, %0, v[%15], %12" MIXL "v_bitop3_b32 %1, %11, %9, %1 bitop3:0xea\n\tv_fma_mix_f32 %0, %0, v[%15], %13" MIXH \
        "v_fma_mix_f32 %4, %1, v[%15+1], %12" MIXL "v_lshl_add_u32 %0, %0, 16, %3\n\tv_lshlrev_b32_sdwa %3, 2, %8" SDW \
        "v_fma_mix_f32 %1, %1, v[%15+1], %13" MIXH DADD("v[%15-9], ") "%3, %0 offset:%14\n\t" \
        "v_fma_mix_f32 %5, %2, v[%15+2], %12" MIXL "v_lshl_add_u32 %1, %1, 16, %4\n\tv_lshlrev_b32_sdwa %4, 2, %9" SDW \
        "v_fma_mix_f32 %2, %2, v[%15+2], %13" MIXH DADD("v[%15-8], ") "%4, %1 offset:%14\n\t" \
        "v_lshl_add_u32 %2, %2, 16, %5\n\tv_lshlrev_b32_sdwa %5, 2, %10" SDW \
        DADD("v[%15-7], ") "%5, %2 offset:%14" POST \
        : "=&v"(c0_), "=&v"(c1_), "=&v"(c2_), "=&v"(a0_), "=&v"(a1_), "=&v"(a2_), "+v"(guard) \
        : "v"(xw), "v"(w0), "v"(w1), "v"(w2), "v"(sgn), "s"(mg0), "s"(mg1), "n"(A_ACC), "n"(M), "s"(tm) : "memory"); }
    asm volatile("v_mov_b32 v110, 0\n\tv_mov_b32 v111, 0\n\tv_mov_b32 v112, 0\n\tv_mov_b32 v113, 0\n\tv_mov_b32 v114, 0\n\tv_mov_b32 v115, 0\n\t"
                 "v_mov_b32 v116, 0\n\tv_mov_b32 v117, 0\n\tv_mov_b32 v118, 0" ::: "v106", "v127");
    auto unmix = [&](int i) __attribute__((always_inline)) {
        const auto s0 = __builtin_amdgcn_permlane16_swap(ring[i][0], ring[i][2], false, false);
        const auto s1 = __builtin_amdgcn_permlane16_swap(ring[i][1], ring[i][3], false, false);
        ring[i][0] = s0[0]; ring[i][2] = s0[1]; ring[i][1] = s1[0]; ring[i][3] = s1[1];
    };
    auto word = [&](int i, int k, bool pr) __attribute__((always_inline)) -> uint32_t {
        return pr && i < NFAST && (i >> 1) != HP ? ring[(i & ~1) | (k >> 1)][(i & 1) * 2 + (k & 1)] : ring[i][k];
    };
    auto gather = [&](int R, int i, int p, bool pr) __attribute__((always_inline)) {
        if ((i >> 1) == HP) unmix(i);
        const uint32_t w0 = word(i, 0 ^ R, pr), w1 = word(i, 1 ^ R, pr), w2 = word(i, 2 ^ R, pr);
        if (ABL & 16) { asm volatile("" :: "v"(w0), "v"(w1), "v"(w2)); return; }
        if (p == 0) GAT3(119) else if (p == 1) GAT3(122) else GAT3(125)
    };
    auto scatter = [&](int R, int i, int p, u64 tm, bool pr) __attribute__((always_inline)) {
        const uint32_t xw = word(i, 3 ^ R, pr), w0 = word(i, 0 ^ R, pr), w1 = word(i, 1 ^ R, pr), w2 = word(i, 2 ^ R, pr);
        if (ABL & 16) { asm volatile("" :: "v"(w0), "v"(w1), "v"(w2), "v"(xw)); return; }
        if (i < NFAST) { if (p == 0) SCAT("", "", 119) else if (p == 1) SCAT("", "", 122) else SCAT("", "", 125) }
        else if (p == 0) SCAT("s_mov_b64 exec, %16\n\t", "\n\ts_mov_b64 exec, -1", 119)
        else SCAT("s_mov_b64 exec, %16\n\t", "\n\ts_mov_b64 exec, -1", 125)
    };
#define WAIT_L(N) asm volatile("s_waitcnt lgkmcnt(" #N ")" ::: "memory")
    int ntn;
    u64 tm0, tm1;
    uint32_t ta[2], sp = sp0;
    auto list1 = [&](int ff) __attribute__((always_inline)) {
        int dst;
        uint32_t en;
        ntn = tail_rank(tc.x, sp, ff, 0u, dst, en);
        asm volatile("ds_permute_b32 v106, %0, %1" :: "v"(dst), "v"(en) : "memory");
    };
    auto step = [&](auto RR, auto NN, auto NC, int ff) __attribute__((always_inline)) {
        constexpr int R = decltype(RR)::value;
        constexpr bool nt = decltype(NN)::value == 1, last = decltype(NN)::value == 2, nc = decltype(NC)::value == 1, first = decltype(NC)::value == 2;
        constexpr bool pr = true;
        constexpr int mdp = first ? MDF : (nt || nc) ? MDQ : MDP;
        gather(R, 0, 0, pr);
        if (!first && !V1) scatter(R, NFAST, 2, tm0, pr);
        const int ntl = ntn;
        asm volatile("s_nop 1\n\tv_mov_b32_dpp %0, v106 quad_perm:[0,0,0,0] row_mask:0xf bank_mask:0xf\n\tv_mov_b32_dpp %1, v106 quad_perm:[1,1,1,1] row_mask:0xf bank_mask:0xf" : "=v"(ta[0]), "=v"(ta[1]));
        auto side = [&](int pl) __attribute__((always_inline)) {
            if (first && PRE1 <= 4 && pl == ((mdp >> 20) & 15)) { ring[4] = load_main(4, ff); ring[5] = load_main(5, ff); }
            if (first && PRE1 <= 6 && pl == ((mdp >> 24) & 15)) { ring[6] = load_main(6, ff); ring[7] = load_main(7, ff); }
            if (pl == ((mdp >> 12) & 15)) ring[NFAST] = load_tail(ta[0], tm0);
            if (pl == ((mdp >> 16) & 15)) ring[NFAST + 1] = load_tail(ta[1], tm1);
            __builtin_amdgcn_sched_barrier(0);
            if (!last) load_md(ff + 1, pl, mdp);
            else if (pl == 3) load_m(me);
            if (!first && !last && pl == 5 && ff == 6) { load_d(me); load_wl(); }
            if (last && pl < 7 && (FALL || fin)) factor(pl, pl == 0);
            __builtin_amdgcn_sched_barrier(0);
        };
        side(8);
        gather(R, 1, 1, pr);
        side(9);
        const uint32_t tcur = tc.x, spc = sp;
#pragma unroll
        for (int i = 0; i < NFAST - 1; i++) {
            gather(R, i + 2, (i + 2) % 3, pr);
            if (i == 0) side(10);
            if (i == 0 && (first || V1)) WAIT_L(6); else if (i < 2) WAIT_L(9); else WAIT_L(12);
            scatter(R, i, i % 3, 0ull, pr);
            if (!last && pr && (i & 1)) ring[i - 1] = load_main(i - 1, ff + 1);
            if (!last && (!pr || (i & 1))) ring[i] = load_main(i, ff + 1);
            side(i);
            if (APRIO && i == (APRIO & 15)) __builtin_amdgcn_s_setprio(2);
            if (APRIO && i == ((APRIO >> 4) & 15)) __builtin_amdgcn_s_setprio(1);
            if (APRIO && i == ((APRIO >> 8) & 15)) __builtin_amdgcn_s_setprio(0);
            if (i == 0 && !last) {
                tc.x = __builtin_amdgcn_alignbit(tc.y, tc.x, 8u); tc.y = __builtin_amdgcn_alignbit(tc.z, tc.y, 8u);
                tc.z = __builtin_amdgcn_alignbit(tc.w, tc.z, 8u); tc.w >>= 8;
                sp = ff == 7 ? sp0 + NWG * 9u * 128u : sp + ((tcur & 0x3cu) << 2);
                list1(ff + 1);
            }
            __builtin_amdgcn_sched_barrier(0);
        }
        if (__builtin_expect(ntl > 16, 0)) {
            gather(R, NFAST + 1, 0, pr);
            WAIT_L(0);
            scatter(R, NFAST + 1, 0, tm1, pr);
        }
        if (__builtin_expect((__builtin_amdgcn_ballot_w64((tcur & 2u) != 0u) | (u64)(ntl > 32 ? 1 : 0)) != 0ull, 0)) {
#pragma unroll 1
            for (int e = 2; e < 6; e++) {
                int dst;
                uint32_t en;
                if (tail_rank(tcur, spc, ff, (uint32_t)e & 4u, dst, en) <= (e & 3) * 16) continue;
                const int cmp = __builtin_amdgcn_ds_permute(dst, (int)en);
                ring[NFAST + 1] = load_tail((uint32_t)__builtin_amdgcn_ds_bpermute(((lane & ~3) | (e & 3)) << 2, cmp), tm1);
                gather(R, NFAST + 1, 0, pr);
                WAIT_L(0);
                scatter(R, NFAST + 1, 0, tm1, pr);
            }
        }
        if (APRIO) __builtin_amdgcn_s_setprio(3);
        WAIT_L(3);
        ST(1)
        if (!last) {
            if (!(ABL & 128)) __builtin_amdgcn_s_barrier();
            ST(3)
            if (!(ABL & 512)) store_md();
            if (V1 & 32) asm volatile("s_mov_b64 exec, 1\n\tds_add_u32 %0, %1\n\ts_mov_b64 exec, -1" :: "v"(vconst<A_FLAG + 16>()), "v"(vconst<1>()) : "memory");
            if (V1 & 2) __builtin_amdgcn_s_setprio(0);
            if (V1 & 4) WAIT_L(0);
        }
        scatter(R, NFAST - 1, 1, 0ull, pr);
        if (!last) {
            if (pr) ring[NFAST - 2] = load_main(NFAST - 2, ff + 1);
            ring[NFAST - 1] = load_main(NFAST - 1, ff + 1);
            if (V1 & 8) {
                uint32_t t0;
                if (!(V1 & 32)) asm volatile("s_waitcnt lgkmcnt(3)\n\ts_mov_b64 exec, 1\n\tds_add_u32 %0, %1\n\ts_mov_b64 exec, -1" :: "v"(vconst<A_FLAG + 16>()), "v"(vconst<1>()) : "memory");
                if (V1 & 16) asm volatile("ds_read_b32 v107, %0" :: "v"(vconst<A_FLAG + 16>()) : "memory");
                scatter(R, NFAST, 2, tm0, pr);
                if (V1 & 16) asm volatile("s_waitcnt lgkmcnt(3)\n\tv_readfirstlane_b32 %0, v107\n\ts_cmp_ge_u32 %0, %2\n\ts_cbranch_scc1 2f\n"
                             "1:\n\tds_read_b32 v107, %1\n\ts_waitcnt lgkmcnt(0)\n\tv_readfirstlane_b32 %0, v107\n\ts_cmp_lt_u32 %0, %2\n\ts_cbranch_scc1 1b\n2:"
                             : "=&s"(t0) : "v"(vconst<A_FLAG + 16>()), "s"(ff * 16 + 16) : "memory", "scc");
                else asm volatile("1:\n\tds_read_b32 v107, %1\n\ts_waitcnt lgkmcnt(0)\n\tv_readfirstlane_b32 %0, v107\n\ts_cmp_lt_u32 %0, %2\n\ts_cbranch_scc1 1b"
                             : "=&s"(t0) : "v"(vconst<A_FLAG + 16>()), "s"(ff * 16 + 16) : "memory", "scc");
            } else {
                if (V1) { scatter(R, NFAST, 2, tm0, pr); WAIT_L(6); }
                else WAIT_L(3);
                if (!(ABL & 256)) __builtin_amdgcn_s_barrier();
            }
            if (V1 & 2) __builtin_amdgcn_s_setprio(3);
            ST(4)
        }
    };
    auto steps = [&](auto RR) __attribute__((always_inline)) {
        constexpr int R = decltype(RR)::value;
        tc = tcnt[(wv * 16 + (lane >> 2)) * 128 + j * 4 + h];
        u32x4 sv[2];
        uint32_t e0;
        if (wv < 4) {
            sv[0] = (u32x4)(0u); sv[1] = sv[0];
            if (lane < SLOTI / 256) { sv[0] = stat[tid * 2]; sv[1] = stat[tid * 2 + 1]; }
            e0 = meta[M_E0 + wv];
        }
        const u32x2 extra = *(const u32x2*)(meta + M_LOG);
        load_md(0, -1, 0);
        {
            const u32x4 bi = {BIAS, BIAS, BIAS, BIAS};
#pragma unroll
            for (int k = 0; k < 6; k++) *(LDS u32x4*)(uintptr_t)(A_ACC + (k * NT + threadIdx.x) * 16) = bi;
            if (threadIdx.x < 128) *(LDS u32x4*)(uintptr_t)(A_ACC + (6 * NT + threadIdx.x) * 16) = bi;
            __builtin_amdgcn_sched_barrier(0);
        }
#pragma unroll
        for (int i = 0; i < PRE0; i++) ring[i] = load_main(i, 0);
        __builtin_amdgcn_sched_barrier(0);
        list1(0);
        if (wv < 4) {
            const uint32_t mxb = wave_max(max(max(sv[0].x & 0xffffu, sv[0].z & 0xffffu), max(sv[1].x & 0xffffu, sv[1].z & 0xffffu)));
            const uint32_t mnb = 0xffffu - (wave_max(max(max(sv[0].x, sv[0].z), max(sv[1].x, sv[1].z))) >> 16);
            const float s2 = wave_sum((__uint_as_float(sv[0].y) + __uint_as_float(sv[0].w)) + (__uint_as_float(sv[1].y) + __uint_as_float(sv[1].w)));
            const float rng = fmaxf(sqrtf(zf * s2), 2.0f * bf2f(mxb)) * 6.103515625e-05f;
            const uint32_t eb = mxb ? (__float_as_uint(rng) + 0x7fffffu) >> 23 : 127u;
            const bool ok = !mxb || (mxb < 0x4700u && eb >= 107u && eb <= 140u && mnb >= ((eb + 8u) << 7));
            if (lane == 0) {
                s_flag[8 + wv] = (eb & 0xffu) | (ok ? 0x100u : 0u) | (mxb ? 0x200u : 0u);
                s_flag[12 + wv] = __float_as_uint(__uint_as_float(((eb + e0) & 0xffu) << 23) * (1024.0f / 1023.0f));
                s_flag[wv] = 0u;
                s_flag[4 + wv] = 0u;
            }
        }
        store_md();
#pragma unroll
        for (int i = PRE0; i < PRE1; i++) ring[i] = load_main(i, 0);
        WAIT_L(0);
        __syncthreads();
        const uint32_t q0 = __builtin_amdgcn_readfirstlane(s_flag[8 + 2 * cp]), q1 = __builtin_amdgcn_readfirstlane(s_flag[9 + 2 * cp]);
        fast = (q0 & q1 & 0x100u) != 0u;
        need = (q0 >> 9) | ((q1 >> 9) << 1);
        more = (__builtin_amdgcn_readfirstlane(extra.x | extra.y) | (uint32_t)(T & 3)) != 0u;
        mg0 = (((q0 & 0xffu) + 9u) << 23) | 0x400000u;
        mg1 = ((((q1 & 0xffu) + 9u) << 23) | 0x400000u) + ((0u - (mg0 >> 16)) & 0xffffu);
        if (!fast) return;
        ST(0)
        RTS(9)
        step(RR, IC<0>(), IC<2>(), 0);
        int ff = 1;
#pragma unroll 1
        for (; ff < 15; ff++) step(RR, IC<0>(), IC<0>(), ff);
        step(RR, IC<2>(), IC<0>(), 15);
        scatter(R, NFAST, 2, tm0, false);
        if (FALL || fin) factor(7, false);
    };
    auto irregular = [&](auto f) __attribute__((always_inline)) {
        const float fr = bf2f(freq[0]);
        const char* lg = recs + (size_t)LBASE * 128u;
        const uint16_t* tags = (const uint16_t*)(lg + (size_t)lcap * 128u);
        const uint32_t nl = meta[M_LOG], ns = meta[M_LOG + 1];
#pragma unroll 1
        for (uint32_t q = tid; q < nl * 8u; q += NT) {
            const uint32_t b = tags[q >> 3];
            if ((b & 127u) == (uint32_t)(h * 32 + j)) {
                const uint32_t* un = (const uint32_t*)(lg + (size_t)q * 16u);
                const uint32_t xw = un[3 ^ h];
#pragma unroll 1
                for (int k = 0; k < 3; k++) {
                    const uint32_t ww = un[k ^ h];
                    const float c = (float)((xw >> (k * 11)) & 0x3ffu) * (1.0f / 1023.0f);
                    if (!(ww & WNULL)) f(ww >> 16, ((ww & 0x3fffu) << 6) | ((ALT ? ((b >> 7) * 3u) & 15u : b >> 7) << 2) | (uint32_t)h, (ww & 0x8000u) ? -c : c);
                }
            }
        }
#pragma unroll 1
        for (uint32_t q = tid; q < ns + (uint32_t)(T & 3); q += NT) {
            const uint32_t t = q < ns ? ((const uint32_t*)tags)[-1 - (int)q] : (uint32_t)(T & ~3) + (q - ns);
            const uint32_t i = (uint32_t)iin[t], o = (uint32_t)iout[t];
            if (((o >> 6) & 31u) == (uint32_t)j && (i & 3u) == (uint32_t)h) f(((o >> 11) << 6) | (o & 63u), i, cosf(fr * bf2f(ang[t])));
        }
    };

    if (h == 0) steps(IC<0>());
    else if (h == 1) steps(IC<1>());
    else if (h == 2) steps(IC<2>());
    else steps(IC<3>());
    if (fast) {
        if (more) irregular([&](uint32_t ol, uint32_t i, float cf) __attribute__((always_inline)) {
            const uint32_t md = mdc[(cp * 64u + (i & 63u)) * SLOTI + ((i >> 14) << 8) + ((i >> 4) & 0xfcu) + ((i >> 12) & 3u)];
            const float c = cf * (1023.0f / 16777216.0f);
            const uint32_t lo = __float_as_uint(__builtin_fmaf(c, h2f(md & 0xffffu), __uint_as_float(mg0)));
            const uint32_t hi = __float_as_uint(__builtin_fmaf(c, h2f(md >> 16), __uint_as_float(mg1)));
            if (cf == cf) guard |= atomicAdd(&acc[ol], (hi << 16) + lo);
            else guard |= HSLOW;
        });
    }
    GAS unsigned* dw = (GAS unsigned*)(meta + M_DONE + j * 64);
    volatile LDS unsigned* seen = (volatile LDS unsigned*)(uintptr_t)(A_FLAG + 4);
    if (tid == NT - 64) *seen = __hip_atomic_load(dw, __ATOMIC_RELAXED, __HIP_MEMORY_SCOPE_AGENT) & 0xff00ffu;
    WAIT_L(0);
    asm volatile("v_or3_b32 %0, %0, v110, v111\n\tv_or3_b32 %0, %0, v112, v113\n\tv_or3_b32 %0, %0, v114, v115\n\tv_or3_b32 %0, %0, v116, v117\n\tv_or_b32 %0, v118, %0" : "+v"(guard));
    ST(2)
    __syncthreads();
    RTS(10)
    const __amdgpu_buffer_rsrc_t rsP = __builtin_amdgcn_make_buffer_rsrc((void*)(partial + (size_t)j * 16 * SLOTO), 0, 16 * SLOTO * 4, 0x00020000);
    u32x4 p[8];
    unsigned have = 0u;
    auto fetch = [&](unsigned v, int sh) __attribute__((always_inline)) {
        const uint32_t po = tid < SHARE / 4 ? (uint32_t)(sh * SHARE + tid * 4) * 4u : 0x7fffff00u;
        const unsigned nw = v & ~have;
#pragma unroll
        for (int r = 0; r < 8; r++)
            if ((nw >> (me ^ r)) & 1u)
                p[r] = __builtin_amdgcn_raw_buffer_load_b128(rsP, po, ((v >> (16 + (me ^ r))) & 1u) ? 16 * SLOTO * 4 : (me ^ r) * SLOTO * 4, AUX_SC1);
        have |= v;
    };
    if (fast) {
        uint32_t big = 0u;
        u32x4 v[7];
#pragma unroll
        for (int k = 0; k < 6; k++) v[k] = *(const LDS u32x4*)(uintptr_t)(A_ACC + (k * NT + tid) * 16);
        v[6] = (u32x4)(BIAS);
        if (wv < 2) v[6] = *(const LDS u32x4*)(uintptr_t)(A_ACC + (6 * NT + tid) * 16);
#pragma unroll
        for (int k = 0; k < 6; k++) __builtin_amdgcn_raw_buffer_store_b128(v[k], rsP, (uint32_t)(tid * 16 + me * SLOTO * 4 + k * NT * 16), 0, AUX_SC1);
        if (wv < 2) __builtin_amdgcn_raw_buffer_store_b128(v[6], rsP, (uint32_t)(tid * 16 + me * SLOTO * 4 + 6 * NT * 16), 0, AUX_SC1);
        __builtin_amdgcn_sched_barrier(0);
#pragma unroll
        for (int k = 0; k < 28; k++) { guard |= v[k >> 2][k & 3]; big |= v[k >> 2][k & 3] + 0x42004200u; }
        if (guard & HSLOW) s_flag[0] = 1u;
        if (big & 0x7c00u) s_flag[2] = 1u;
        if (big & 0x7c000000u) s_flag[3] = 2u;
        __builtin_amdgcn_sched_barrier(0);
        p[0] = *(const LDS u32x4*)(uintptr_t)(A_ACC + (me * SHARE + min(tid, SHARE / 4 - 1) * 4) * 4);
        have = 1u << me;
        if (fin) fetch(__builtin_amdgcn_readfirstlane(*seen), me);
    }
    asm volatile("s_waitcnt vmcnt(0)" ::: "memory");
    __syncthreads();
    RTS(11)
    ST(5)
    const bool slow = (ABL & 1016) ? false : !fast || __builtin_amdgcn_readfirstlane(s_flag[0]) != 0u || (need & ~__builtin_amdgcn_readfirstlane(s_flag[2] | s_flag[3])) != 0u;
    if (slow) {
        float* fs = (float*)acc;
        float w[16];
        load_w(wdown, w);
#pragma unroll 1
        for (int comp = 2 * cp; comp < 2 * cp + 2; comp++) {
            __syncthreads();
            for (int i = tid; i < SLOTO; i += NT) fs[i] = 0.f;
            __syncthreads();
            auto add = [&](uint32_t ol, uint32_t i, float cf) __attribute__((always_inline)) {
                uint32_t rr[4];
                md_of(m[i], w, rr);
                const uint32_t rc = comp == 0 ? rr[0] : comp == 1 ? rr[1] : comp == 2 ? rr[2] : rr[3];
                atomicAdd(&fs[ol], cf * bf2f(rc));
            };
#pragma unroll 1
            for (uint32_t q = tid; q < 16u * NWG * ((uint32_t)ks * 8u + 24u); q += NT) {
                const uint32_t per = (uint32_t)ks * 8u + 24u;
                const uint32_t un = q & 3u, hf = (q % per) >> 2, sb = (q / per) & (NWG - 1u), ff = (q / per) / NWG;
                const uint32_t col = POS(ff) * 128u + (uint32_t)(h * 32 + j);
                u32x2 p0, p1;
                const uint32_t SK = (sb & 0x18u) ? SKM : SKH, SS = (sb & 0x18u) ? SSM : SSH;
                if (hf < 2u * (uint32_t)ks) {
                    const char* ln = recs + (size_t)((col >> 7) * SS + (col & 127u) * SC + (hf >> 1) * SK + BPL(sb)) * 128u + un * 16u + (hf & 1u) * 8u;
                    p0 = *(const u32x2*)ln; p1 = *(const u32x2*)(ln + 64);
                } else {
                    const uint32_t tl = hf - 2u * (uint32_t)ks;
                    const uint8_t* cb = (const uint8_t*)tcnt + (sb * 128u + (uint32_t)(j * 4 + h)) * 16u;
                    const char* ln = recs + (size_t)(TBASE + (tl >> 1) * TPL + col * TST + sb) * 128u + (tl & 1u) * 64u + un * 16u;
                    if (tl < 4u) { if (tl >= (cb[ff] & 3u) * 2u) continue; }
                    else {
                        uint32_t at = (tl - 4u) * 4u + un;
                        if (at >= ((cb[ff] >> 2) & 15u)) continue;
#pragma unroll 1
                        for (uint32_t t = ff & 8u; t < ff; t++) at += (cb[t] >> 2) & 15u;
                        ln = recs + (size_t)(SBASE + (((uint32_t)(h * 32 + j) * 2u + (ff >> 3)) * NWG + sb) * 9u) * 128u + at * 16u;
                    }
                    p0 = *(const u32x2*)ln; p1 = *(const u32x2*)(ln + 8);
                }
                if (h & 2) { const u32x2 t = p0; p0 = p1; p1 = t; }
                const uint32_t ws[4] = {(h & 1) ? p0.y : p0.x, (h & 1) ? p0.x : p0.y, (h & 1) ? p1.y : p1.x, (h & 1) ? p1.x : p1.y};
#pragma unroll
                for (int k = 0; k < 3; k++) {
                    const float c = (float)((ws[3] >> (k * 11)) & 0x3ffu) * (1.0f / 1023.0f);
                    if (!(ws[k] & WNULL)) add(ws[k] >> 16, ((ws[k] & 0x3fffu) << 6) | (ff * 4u + (uint32_t)h), (ws[k] & 0x8000u) ? -c : c);
                }
            }
            if (more) irregular(add);
            __syncthreads();
            for (int i = tid; i < SLOTO / 4; i += NT)
                __builtin_amdgcn_raw_buffer_store_b128(*(const LDS u32x4*)(uintptr_t)(A_ACC + i * 16), rsP, (uint32_t)(((comp & 1) * 8 + me) * SLOTO * 4 + i * 16), 0, AUX_SC1);
        }
        load_wl();
        edges(me);
#pragma unroll
        for (int k = 0; k < 8; k++) p[k] = (u32x4)(0u);
        have = 0u;
        asm volatile("s_waitcnt vmcnt(0)" ::: "memory");
        __syncthreads();
    }
    if (tid == NT - 64) {
        const unsigned inc = (slow ? 0x10001u : 1u) << me;
        unsigned v = __hip_atomic_fetch_add(dw, inc, __ATOMIC_RELAXED, __HIP_MEMORY_SCOPE_AGENT) + inc;
        if ((v & 0xffu) != 0xffu) {
            unsigned spins = 0;
            do {
                *seen = v & 0xff00ffu;
                __builtin_amdgcn_s_sleep(1);
                v = __hip_atomic_load(dw, __ATOMIC_RELAXED, __HIP_MEMORY_SCOPE_AGENT);
                if (++spins > SPINCAP && (v & 0xffu) != 0xffu) {
                    v = __hip_atomic_fetch_add(dw, 256u << me, __ATOMIC_RELAXED, __HIP_MEMORY_SCOPE_AGENT);
                    if ((v & 0xffu) != 0xffu) v = 0x800000ffu;
                }
            } while ((v & 0xffu) != 0xffu);
            v &= 0x80ff00ffu;
        }
        *seen = v;
    }
    if (!fin) return;
    have = __builtin_amdgcn_readfirstlane(have);
    uint32_t un[4];
#pragma unroll
    for (int c = 0; c < 4; c++) un[c] = __builtin_amdgcn_readfirstlane(s_flag[12 + c]);
    unsigned sv;
    {
        const uintptr_t pb = (uintptr_t)(partial + (size_t)j * 16 * SLOTO);
        const u32x4 rsQ = {(uint32_t)pb, (uint32_t)(pb >> 32) & 0xffffu, 16u * SLOTO * 4u, 0x00020000u};
        const uint32_t po = tid < SHARE / 4 ? (uint32_t)(me * SHARE + tid * 4) * 4u : 0x7fffff00u;
        do {
            sv = __builtin_amdgcn_readfirstlane(*seen);
            uint32_t t0, t1;
#define FET(R) "s_xor_b32 %8, %12, " #R "\n\ts_bitcmp1_b32 %13, %8\n\ts_cbranch_scc0 1f\n\ts_mul_i32 %9, %8, %15\n\ts_bitcmp1_b32 %14, %8\n\t" \
               "s_cselect_b32 %9, %16, %9\n\tbuffer_load_dwordx4 %" #R ", %10, %11, %9 offen sc1\n1:\n\t"
            asm volatile("s_and_b32 %8, %13, 0xff\n\ts_cbranch_scc0 2f\n\ts_nop 2\n\t" FET(0) FET(1) FET(2) FET(3) FET(4) FET(5) FET(6) FET(7) "\n2:\n\ts_sleep 2\n\ts_waitcnt vmcnt(0)"
                         : "+v"(p[0]), "+v"(p[1]), "+v"(p[2]), "+v"(p[3]), "+v"(p[4]), "+v"(p[5]), "+v"(p[6]), "+v"(p[7]), "=&s"(t0), "=&s"(t1)
                         : "v"(po), "s"(rsQ), "s"(me), "s"((int)sv < 0 ? 0u : sv & ~have), "s"(sv >> 16), "n"(SLOTO * 4), "n"(16 * SLOTO * 4) : "memory", "scc");
            have |= sv;
        } while ((sv & 0xffu) != 0xffu);
    }
    if ((int)sv < 0) return;
    RTS(12)
    ST(6)
    const unsigned flt = (sv >> 16) & 0xffu;
    auto finish = [&](int sh) __attribute__((always_inline)) {
        {
            const int ol = sh * SHARE + tid * 4;
            const int e0 = ((ol >> 6) << 11) | (j << 6) | (ol & 63);
            const uint32_t po = tid < SHARE / 4 ? (uint32_t)ol * 4u : 0x7fffff00u;
            float g[4][4];
            u32x4 a[2] = {p[0] + p[2], p[1] + p[3]}, b[2] = {p[4] + p[6], p[5] + p[7]};
            if (cp) { const u32x4 t = a[0], u = b[0]; a[0] = a[1]; b[0] = b[1]; a[1] = t; b[1] = u; }
#pragma unroll
            for (int c = 0; c < 2; c++) {
                const int nb = 0x10000 - 0x4000 * __builtin_popcount(flt & (0x55u << c));
                const float u0 = __uint_as_float(nb ? un[2 * c] : 0u), u1 = __uint_as_float(nb ? un[2 * c + 1] : 0u);
#pragma unroll
                for (int q = 0; q < 4; q++) {
                    g[q][2 * c] = (float)((int)((a[c][q] & 0xffffu) + (b[c][q] & 0xffffu)) - nb) * u0;
                    g[q][2 * c + 1] = (float)((int)((a[c][q] >> 16) + (b[c][q] >> 16)) - nb) * u1;
                }
            }
            if (__builtin_expect(flt != 0u, 0)) {
#pragma unroll
                for (int c = 0; c < 2; c++) {
#pragma unroll 1
                    for (int k = c; k < 8; k += 2) {
                        if (!((flt >> k) & 1u)) continue;
                        const u32x4 f0 = __builtin_amdgcn_raw_buffer_load_b128(rsP, po, k * SLOTO * 4, AUX_SC1);
                        const u32x4 f1 = __builtin_amdgcn_raw_buffer_load_b128(rsP, po, (8 + k) * SLOTO * 4, AUX_SC1);
#pragma unroll
                        for (int q = 0; q < 4; q++) { g[q][2 * c] += __uint_as_float(f0[q]); g[q][2 * c + 1] += __uint_as_float(f1[q]); }
                    }
                }
            }
            float wu[16];
#pragma unroll
            for (int k = 0; k < 8; k++) {
                const uint32_t x = __builtin_amdgcn_readlane(wl, 40 + k);
                wu[2 * k] = bf2f(x & 0xffffu); wu[2 * k + 1] = bf2f(x >> 16);
            }
            u32x4 ov[2];
#pragma unroll
            for (int q = 0; q < 4; q++) {
#pragma unroll
                for (int k = 0; k < 4; k++) g[q][k] *= fa[q][k >> 1][k & 1];
                const uint32_t m0 = em[q >> 1][(q & 1) * 2], m1 = em[q >> 1][(q & 1) * 2 + 1];
                const float mm[4] = {bf2f(m0 & 0xffff), bf2f(m0 >> 16), bf2f(m1 & 0xffff), bf2f(m1 >> 16)};
                float o[4];
#pragma unroll
                for (int k = 0; k < 4; k++) o[k] = mm[k] + (g[q][0] * wu[k] + g[q][1] * wu[4 + k] + g[q][2] * wu[8 + k] + g[q][3] * wu[12 + k]) * fa[q][2 + (k >> 1)][k & 1];
                ov[q >> 1][(q & 1) * 2] = pk_bf16(o[0], o[1]);
                ov[q >> 1][(q & 1) * 2 + 1] = pk_bf16(o[2], o[3]);
            }
            const __amdgpu_buffer_rsrc_t rsU = __builtin_amdgcn_make_buffer_rsrc((void*)out, 0, E * 8, 0x00020000);
            const uint32_t uo = tid < SHARE / 4 ? (uint32_t)e0 * 8u : 0x7fffff00u;
            __builtin_amdgcn_raw_buffer_store_b128(ov[0], rsU, uo, 0, OAUX);
            __builtin_amdgcn_raw_buffer_store_b128(ov[1], rsU, uo + 16u, 0, OAUX);
        }
    };
    finish(me);
#pragma unroll 1
    for (unsigned todo = (sv >> 8) & 0xffu; todo; todo &= todo - 1u) {
        const int sh = __builtin_ctz(todo);
        edges(sh);
        have = 0u;
        fetch(sv, sh);
        finish(sh);
    }
    RTS(13)
    ST(7)
    ST_OUT(8)
}

__global__ __launch_bounds__(256) void gen_scatter_kernel(const int* __restrict__ iin, const int* __restrict__ iout,
                                                          const uint16_t* __restrict__ ang, const uint16_t* __restrict__ freq,
                                                          const u32x2* __restrict__ m, const uint16_t* __restrict__ wdown,
                                                          float* __restrict__ agg, int T) {
    float w[16];
    load_w(wdown, w);
    const float fr = bf2f(freq[0]);
    for (long t = (long)blockIdx.x * 256 + threadIdx.x; t < T; t += (long)gridDim.x * 256) {
        const float c = cosf(fr * bf2f(ang[t]));
        uint32_t r[4];
        md_of(m[iin[t]], w, r);
        float* q = agg + (size_t)iout[t] * 4;
#pragma unroll
        for (int k = 0; k < 4; k++) atomicAdd(q + k, c * bf2f(r[k]));
    }
}
__global__ __launch_bounds__(256) void gen_final_kernel(const u32x2* __restrict__ m, const uint16_t* __restrict__ dist,
                                                        const uint16_t* __restrict__ centers, const uint16_t* __restrict__ wrbf,
                                                        const uint16_t* __restrict__ wsbf, const uint16_t* __restrict__ wup,
                                                        const float* __restrict__ agg, u32x2* __restrict__ out, int E) {
    const int e = blockIdx.x * 256 + threadIdx.x;
    if (e >= E) return;
    const float d = bf2f(dist[e]);
    float rs[4] = {0, 0, 0, 0}, rg[4] = {0, 0, 0, 0};
#pragma unroll 1
    for (int r = 0; r < 8; r++) {
        const float x = d - bf2f(centers[r]);
        const float rb = __expf(-10.0f * x * x);
#pragma unroll
        for (int k = 0; k < 4; k++) { rs[k] += rb * bf2f(wsbf[r * 4 + k]); rg[k] += rb * bf2f(wrbf[r * 4 + k]); }
    }
    float wu[16];
    load_w(wup, wu);
    const u32x2 mv = m[e];
    const float mm[4] = {bf2f(mv.x & 0xffff), bf2f(mv.x >> 16), bf2f(mv.y & 0xffff), bf2f(mv.y >> 16)};
    float g[4], o[4];
#pragma unroll
    for (int k = 0; k < 4; k++) g[k] = agg[(size_t)e * 4 + k] * rs[k];
#pragma unroll
    for (int k = 0; k < 4; k++) o[k] = mm[k] + (g[0] * wu[k] + g[1] * wu[4 + k] + g[2] * wu[8 + k] + g[3] * wu[12 + k]) * rg[k];
    const u32x2 ov = {f2bf(o[0]) | (f2bf(o[1]) << 16), f2bf(o[2]) | (f2bf(o[3]) << 16)};
    out[e] = ov;
}

extern "C" void kernel_launch(void* const* d_in, const int* in_sizes, int n_in,
                              void* d_out, int out_size, void* d_ws, size_t ws_size,
                              hipStream_t stream) {
    const u32x2* m = (const u32x2*)d_in[0];
    const int* iin = (const int*)d_in[1];
    const int* iout = (const int*)d_in[2];
    const uint16_t* dist = (const uint16_t*)d_in[3];
    const uint16_t* ang = (const uint16_t*)d_in[4];
    const uint16_t* centers = (const uint16_t*)d_in[5];
    const uint16_t* freq = (const uint16_t*)d_in[6];
    const uint16_t* wrbf = (const uint16_t*)d_in[7];
    const uint16_t* wsbf = (const uint16_t*)d_in[8];
    const uint16_t* wdown = (const uint16_t*)d_in[9];
    const uint16_t* wup = (const uint16_t*)d_in[10];
    const int E = in_sizes[3];
    const int T = in_sizes[1];
    char* ws = (char*)d_ws;
    const int G = T >> 2;
    const int ntile = (G + GR - 1) / GR;
    const int nstat = ntile / NWG - NDYN > 4 ? ntile / NWG - NDYN : 4;
    const int lcap = G / 6 + 2;
    char* recs = ws + W_RECS;
    size_t off = W_RECS;
    const size_t rbytes = (size_t)LBASE * 128 + (size_t)lcap * 130;
    off += rbytes;
    if (E > EMAX || (E & 3) || E < 1 || rbytes > 0x70000000u || off > ws_size) {
        (void)hipMemsetAsync(ws, 0, (size_t)E * 16, stream);
        gen_scatter_kernel<<<4096, 256, 0, stream>>>(iin, iout, ang, freq, m, wdown, (float*)ws, T);
        gen_final_kernel<<<(E + 255) / 256, 256, 0, stream>>>(m, dist, centers, wrbf, wsbf, wup, (const float*)ws, (u32x2*)d_out, E);
        return;
    }
    int ks = T / (NWG * NB * 24);
    if (ks > KMAX) ks = KMAX;
    const float zf = ZSIG * ZSIG * (float)T / (8.0f * (float)E * (float)E);
    (void)hipFuncSetAttribute((const void*)part_kernel, hipFuncAttributeMaxDynamicSharedMemorySize, P_LDS);
    (void)hipFuncSetAttribute((const void*)acc_kernel, hipFuncAttributeMaxDynamicSharedMemorySize, A_LDS);
    part_kernel<<<NWG, NT, P_LDS, stream>>>(iin, iout, ang, freq, m, wdown, recs, T, E, ntile, nstat, ks, lcap);
    if (!(ABL & 7)) acc_kernel<<<NWG, NT, A_LDS, stream>>>(ws, T, E, ks, lcap, zf, 0, centers, wrbf, wsbf, wup, m, dist, (u32x2*)d_out, iin, iout, ang, wdown, freq);
}
```

```cpp
#include <hip/hip_runtime.h>
#include <hip/hip_bf16.h>
#include <stdint.h>
#pragma clang diagnostic ignored "-Winline-asm"

typedef unsigned int u32x4 __attribute__((ext_vector_type(4)));
typedef unsigned int u32x2 __attribute__((ext_vector_type(2)));
typedef unsigned short u16x2 __attribute__((ext_vector_type(2)));
typedef _Float16 f16x2 __attribute__((ext_vector_type(2)));
typedef __bf16 bf16x2 __attribute__((ext_vector_type(2)));
typedef float f32x2 __attribute__((ext_vector_type(2)));
typedef unsigned long long u64;

static __device__ __forceinline__ float bf2f(uint32_t bits16) { return __uint_as_float(bits16 << 16); }
static __device__ __forceinline__ uint32_t f2bf(float f) {
    uint32_t u = __float_as_uint(f);
    if ((u & 0x7fffffffu) > 0x7f800000u) return (u >> 16) | 0x40;
    return (u + 0x7fffu + ((u >> 16) & 1u)) >> 16;
}
static __device__ __forceinline__ float h2f(uint32_t bits16) { return (float)__builtin_bit_cast(_Float16, (unsigned short)bits16); }

#define NB 2048
#define WNULL 0x80000000u
#define CSTEP 0x2aabu
#define CINIT 205u
#define CHV (12u * CSTEP)
#define SLOTO 25088
#define SLOTI 12544
#define EMAX (SLOTO * 32)
#define NT 1024
#define NWG 256
#define KMAX 4
#define NTAIL 4
#define LAYH 2
#define LAYM 1
#define SC 64u
#define PAD 1u
#define SK1 (128u * SC + 2u * PAD)
#define SS1 (4u * SK1 + PAD)
#define SS2 (128u * SC + PAD)
#define SK2 (16u * SS2 + 2u * PAD)
#define SKH (LAYH == 1 ? SK1 : SK2)
#define SSH (LAYH == 1 ? SS1 : SS2)
#define SKM (LAYM == 1 ? SK1 : SK2)
#define SSM (LAYM == 1 ? SS1 : SS2)
#define SQ (16u * SS1)
#define MAINL (4u * SQ)
#define BPL(b) ((((b) >> 3) & 3u) * SQ + ((b) & 7u) + (((b) >> 5) << 3))
#define TST 257u
#define TBASE MAINL
#define TPL (NB * TST)
#define SBASE (TBASE + 2u * TPL)
#define LBASE (SBASE + 128u * 2u * NWG * 9u)
#define NDYN 3
#define NEARM 0x55555554u
#define FARV 1u
#define GR 2048
#define NTS 5
#define M_DONE 0
#define M_TILE (32 * 64)
#define M_LOG (M_TILE + 64)
#define M_MD (M_LOG + 64)
#define M_BEAT (M_MD + 64)
#define M_E0 (M_BEAT + 64)
#define M_WORDS (M_E0 + 64)
#define W_TCNT (M_WORDS * 4)
#define W_STAT (W_TCNT + NWG * NB)
#define W_MDC (W_STAT + 4 * NWG * 8)
#define W_PART (W_MDC + 128 * SLOTI * 4)
#define W_RECS (W_PART + 32 * 16 * SLOTO * 4)
#define TSC1 0
#define ALT 0
#define POS(ff) (ALT ? ((ff) * 11) & 15 : (ff))
#define AUX_NT 2
#define AUX_SC1 16

static __device__ __forceinline__ uint32_t row_max(uint32_t x) {
    x = max(x, (uint32_t)__builtin_amdgcn_update_dpp(0, x, 0x111, 0xf, 0xf, false));
    x = max(x, (uint32_t)__builtin_amdgcn_update_dpp(0, x, 0x112, 0xf, 0xf, false));
    x = max(x, (uint32_t)__builtin_amdgcn_update_dpp(0, x, 0x114, 0xf, 0xf, false));
    return max(x, (uint32_t)__builtin_amdgcn_update_dpp(0, x, 0x118, 0xf, 0xf, false));
}
static __device__ __forceinline__ uint32_t wave_max(uint32_t x) {
    x = row_max(x);
    x = max(x, (uint32_t)__builtin_amdgcn_update_dpp(0, x, 0x142, 0xa, 0xf, false));
    x = max(x, (uint32_t)__builtin_amdgcn_update_dpp(0, x, 0x143, 0xc, 0xf, false));
    return __builtin_amdgcn_readlane(x, 63);
}
static __device__ __forceinline__ float row_sum(float x) {
    x += __int_as_float(__builtin_amdgcn_update_dpp(0, __float_as_int(x), 0x111, 0xf, 0xf, false));
    x += __int_as_float(__builtin_amdgcn_update_dpp(0, __float_as_int(x), 0x112, 0xf, 0xf, false));
    x += __int_as_float(__builtin_amdgcn_update_dpp(0, __float_as_int(x), 0x114, 0xf, 0xf, false));
    return x + __int_as_float(__builtin_amdgcn_update_dpp(0, __float_as_int(x), 0x118, 0xf, 0xf, false));
}
static __device__ __forceinline__ float wave_sum(float x) {
    x = row_sum(x);
    x += __int_as_float(__builtin_amdgcn_update_dpp(0, __float_as_int(x), 0x142, 0xa, 0xf, false));
    x += __int_as_float(__builtin_amdgcn_update_dpp(0, __float_as_int(x), 0x143, 0xc, 0xf, false));
    return __int_as_float(__builtin_amdgcn_readlane(__float_as_int(x), 63));
}
static __device__ __forceinline__ uint32_t bfi(uint32_t mask, uint32_t a, uint32_t b) {
    return __builtin_amdgcn_bitop3_b32(mask, a, b, 0xca);
}
static __device__ __forceinline__ void opaque(int& x) { asm volatile("" : "+v"(x)); }
template <uint32_t C> static __device__ __forceinline__ uint32_t vconst() {
    uint32_t r;
    asm volatile("v_mov_b32 %0, %1" : "=v"(r) : "n"(C));
    return r;
}
static __device__ __forceinline__ uint32_t pk_bf16(float a, float b) {
    const f32x2 v = {a, b};
    return __builtin_bit_cast(uint32_t, __builtin_convertvector(v, bf16x2));
}
static __device__ __forceinline__ void pin(uint32_t& x) { asm volatile("" : "+v"(x)); }

#define RTS(k)
#define ST_DECL
#define ST(i)
#define ST_OUT(off)

static __device__ __forceinline__ void md_of(u32x2 v, const float* w, uint32_t* r) {
    float a0 = bf2f(v.x & 0xffff), a1 = bf2f(v.x >> 16), a2 = bf2f(v.y & 0xffff), a3 = bf2f(v.y >> 16);
#pragma unroll
    for (int j = 0; j < 4; j++) r[j] = f2bf(a0 * w[j] + a1 * w[4 + j] + a2 * w[8 + j] + a3 * w[12 + j]);
}
static __device__ __forceinline__ void load_w(const uint16_t* __restrict__ wdown, float* w) {
    u32x4 wv0 = *(const u32x4*)wdown, wv1 = *(const u32x4*)(wdown + 8);
#pragma unroll
    for (int i = 0; i < 4; i++) {
        w[2 * i] = bf2f(wv0[i] & 0xffffu); w[2 * i + 1] = bf2f(wv0[i] >> 16);
        w[8 + 2 * i] = bf2f(wv1[i] & 0xffffu); w[8 + 2 * i + 1] = bf2f(wv1[i] >> 16);
    }
}
#define LDS __attribute__((address_space(3)))
#define P_CNT 0
#define P_FLAG ((NB + 64) * 4)
#define P_LST (P_FLAG + 256)
#define LCAP 32
#define P_TAB (P_LST + 4 * LCAP * 16)
#define P_BUF (P_TAB + NB * 4)
#define P_LDS (P_BUF + NB * 64)
#define PD 2
#define LPOS 0x448763
#define ABL 0
#define PRIO 2
#define SETP(n) __builtin_amdgcn_s_setprio(n)
#define MDL 3
#define RMAXREV 40.0f
#define HB 88
#define HREG(S, D) (HB + ((S) & 3) * 10 + (((S) >> 2) + (D)) * 2)

template <int N> struct IC { static constexpr int value = N; };
struct Raw { u32x4 o[2], i[2]; u32x2 a[2]; };
static __device__ __forceinline__ uint32_t lds_ld(uint32_t a) { return *(const LDS uint32_t*)(uintptr_t)a; }
static __device__ __forceinline__ void lds_st(uint32_t a, uint32_t v) { *(LDS uint32_t*)(uintptr_t)a = v; }
static __device__ __forceinline__ void lds_or(uint32_t a, uint32_t v) {
    __hip_atomic_fetch_or((LDS uint32_t*)(uintptr_t)a, v, __ATOMIC_RELAXED, __HIP_MEMORY_SCOPE_WORKGROUP);
}
static __device__ __forceinline__ uint32_t lds_add(uint32_t a, uint32_t v) {
    return __hip_atomic_fetch_add((LDS uint32_t*)(uintptr_t)a, v, __ATOMIC_RELAXED, __HIP_MEMORY_SCOPE_WORKGROUP);
}
static __device__ __forceinline__ uint32_t slot_w(uint32_t ca, uint32_t cv) {
    return (ca << 4) | __builtin_amdgcn_bitop3_b32(cv >> 11, ca, 0x3cu, 0x28);
}
static __device__ __forceinline__ uint32_t slot_x2(uint32_t aw, uint32_t ca) {
    return __builtin_amdgcn_bitop3_b32(aw, ca, 0xcu, 0x72);
}
static __device__ __forceinline__ uint32_t slot_x(uint32_t aw) { return slot_x2(aw, aw >> 4); }
template <int R> static __device__ __forceinline__ u32x2 hget() {
    u32x2 d;
    asm volatile("v_mov_b64 %0, v[%1:%2]" : "=v"(d) : "n"(R), "n"(R + 1));
    return d;
}
static __device__ __forceinline__ u32x2 hget_at(int r) {
    u32x2 d;
    asm volatile("s_set_gpr_idx_on %2, gpr_idx(SRC0)\n\ts_nop 1\n\tv_mov_b32 %0, v88\n\tv_mov_b32 %1, v89\n\ts_set_gpr_idx_off"
                 : "=&v"(d.x), "=&v"(d.y) : "s"(r));
    return d;
}
static __device__ __forceinline__ void hset_at(int r, u32x2 d) {
    asm volatile("s_set_gpr_idx_on %2, gpr_idx(DST)\n\ts_nop 1\n\tv_mov_b32 v88, %0\n\tv_mov_b32 v89, %1\n\ts_set_gpr_idx_off"
                 :: "v"(d.x), "v"(d.y), "s"(r));
}

template <int S, int D> static __device__ __forceinline__ uint32_t take(uint32_t V, uint32_t u16, uint32_t la, u32x2 nul) {
    const uint32_t a = __builtin_amdgcn_bitop3_b32(V, u16, 15u, 0x54);
    asm volatile("v_mov_b64 v[%4:%4+1], v[%3:%3+1]\n\tv_cmpx_ne_u32 vcc, -16, %0\n\tds_wrxchg_rtn_b64 v[%4:%4+1], %1, %2 offset:%5\n\ts_mov_b64 exec, -1"
                 :: "v"(a), "v"(la), "v"(nul), "n"(HREG(S, D)), "n"(HREG(S, 1 - D)), "n"(S * 256) : "memory", "vcc");
    return a;
}
template <int S, int W> static __device__ __forceinline__ void put(u32x4 rsA, uint32_t ad, uint32_t pm) {
    asm volatile("s_bitcmp1_b32 %3, %4\n\ts_cbranch_scc0 1f\n\ts_waitcnt lgkmcnt(%5)\n\tbuffer_store_dwordx4 v[%2:%2+3], %0, %1, 0 offen sc1\n"
                 "1:\n\ts_bitcmp1_b32 %3, %4+1\n\ts_cbranch_scc0 2f\n\ts_waitcnt lgkmcnt(%5)\n\tbuffer_store_dwordx4 v[%2:%2+3], %0, %1, 0 offen nt\n\ts_nop 1\n2:"
                 :: "v"(ad), "s"(rsA), "n"(HREG(S, 0)), "s"(pm), "n"(2 * S), "n"(W) : "memory", "scc");
}

__global__ __launch_bounds__(NT) __attribute__((amdgpu_num_vgpr(HB / 2))) void part_kernel(const int* __restrict__ iin, const int* __restrict__ iout,
                                                  const uint16_t* __restrict__ ang, const uint16_t* __restrict__ freq,
                                                  const u32x2* __restrict__ m, const uint16_t* __restrict__ wdown,
                                                  char* __restrict__ recs,
                                                  int T, int E, int ntile, int nstat, int ks, int lcap) {
    ST_DECL
    RTS(0)
    const int tid0 = threadIdx.x;
    const int wv = __builtin_amdgcn_readfirstlane(tid0 >> 6);
    const int G = T >> 2;
    const int pos = (int)((blockIdx.x & 7) << 5) | (int)(blockIdx.x >> 3);
    float frr;
    unsigned* meta = (unsigned*)(recs - W_RECS);
    const uint32_t gbase = (LBASE + (uint32_t)lcap) * 128u;
    const uint32_t rbytes = gbase + (uint32_t)lcap * 2u;
    const __amdgpu_buffer_rsrc_t rsR = __builtin_amdgcn_make_buffer_rsrc(recs, 0, (int)rbytes, 0x00020000);
    const u32x4 rsA = {(uint32_t)(uintptr_t)recs, (uint32_t)((uintptr_t)recs >> 32) & 0xffffu, (ABL & 2) ? 0u : rbytes, 0x00020000u};
    const uint32_t g2 = 2u * (uint32_t)ks;
    const uint32_t far = FARV >> ((blockIdx.x >> 3) & 3u);
    const uint32_t SK = SKM + far * (SKH - SKM), SS = SSM + far * (SSH - SSM), UINC = 0x80000001u + SK * 64u;
    const uint32_t spec0 = gbase - 4u;
    auto fixed_tile = [&](int r) __attribute__((always_inline)) -> int { return r * NWG + ((pos + r * 37) & (NWG - 1)); };
    int tl0 = fixed_tile(0), tl1 = fixed_tile(1), tl2;
    uint32_t tk = 0u;
    const __amdgpu_buffer_rsrc_t rsO = __builtin_amdgcn_make_buffer_rsrc((void*)iout, 0, G * 16, 0x00020000);
    const __amdgpu_buffer_rsrc_t rsI = __builtin_amdgcn_make_buffer_rsrc((void*)iin, 0, G * 16, 0x00020000);
    const __amdgpu_buffer_rsrc_t rsG = __builtin_amdgcn_make_buffer_rsrc((void*)ang, 0, G * 8, 0x00020000);
    uint32_t t8 = (uint32_t)tid0 * 8u;
    auto load_one = [&](Raw& r, int tl, int idx) __attribute__((always_inline)) {
        const int j = idx < 4 ? idx >> 1 : idx & 1;
        const uint32_t g0 = (uint32_t)(((ABL & 1) ? tl & 31 : tl) * GR + j * NT);
        __builtin_amdgcn_sched_barrier(0);
        if (idx >= 4) r.a[j] = __builtin_amdgcn_raw_buffer_load_b64(rsG, t8, g0 * 8u, (ABL & 1) ? 0 : AUX_NT);
        else if (idx & 1) r.i[j] = __builtin_amdgcn_raw_buffer_load_b128(rsI, t8 + t8, g0 * 16u, (ABL & 1) ? 0 : AUX_NT);
        else r.o[j] = __builtin_amdgcn_raw_buffer_load_b128(rsO, t8 + t8, g0 * 16u, (ABL & 1) ? 0 : AUX_NT);
        __builtin_amdgcn_sched_barrier(0);
    };
    auto peek = [&](uint32_t at) __attribute__((always_inline)) -> uint32_t {
        return __hip_atomic_load((unsigned*)((char*)meta + at), __ATOMIC_RELAXED, __HIP_MEMORY_SCOPE_AGENT);
    };
    auto ready = [&]() __attribute__((always_inline)) {
        if (blockIdx.x != 0) {
            const uint32_t x = lds_ld(vconst<P_FLAG + 8>());
            while (peek(vconst<M_BEAT * 4>()) == x) __builtin_amdgcn_s_sleep(8);
        }
    };
    auto beat = [&]() __attribute__((always_inline)) {
        (void)__hip_atomic_fetch_add((unsigned*)((char*)meta + vconst<M_BEAT * 4>()), vconst<1>(), __ATOMIC_RELAXED, __HIP_MEMORY_SCOPE_AGENT);
    };
    auto draw = [&](uint32_t at) __attribute__((always_inline)) -> uint32_t {
        return __hip_atomic_fetch_add((unsigned*)((char*)meta + at), vconst<1>(), __ATOMIC_RELAXED, __HIP_MEMORY_SCOPE_AGENT);
    };
    uint32_t la;
    auto tab_at = [&]() __attribute__((always_inline)) -> uint32_t {
        return P_TAB + bfi(0xffu, bfi(0x3cu, t8 >> 2, t8 << 4), t8);
    };
    u32x2 nul;
    uint32_t U[2];
    {
        const uint32_t t = (uint32_t)tid0;
        la = P_BUF + ((t >> 5) << 12) + ((t & 0x18u) << 3) + ((t & 3u) << 4) + ((t & 4u) << 1);
        const uint32_t q = ((t >> 3) ^ (t >> 1 & 2u)) & 3u;
        nul.x = (0x7u >> q) << 31; nul.y = (0xbu >> q) << 31;
        U[0] = (POS((t >> 1) & 15u) * SS + ((t & 1u) * 64u + (t >> 5)) * SC + BPL(blockIdx.x)) * 128u + (0x80000000u - SK * 64u) + 8u - g2;
        U[1] = U[0] + 32u * SC * 128u;
    }
    auto log_line = [&](bool lg, uint32_t b) __attribute__((always_inline)) -> uint32_t {
        uint32_t lp = 0u;
        if (lg && (t8 & 0x38u) == 0u) {
            ready();
            lp = draw(vconst<M_LOG * 4>());
            __builtin_amdgcn_raw_buffer_store_b16((uint16_t)b, rsR, gbase + lp * 2u, 0, 0);
        }
        return LBASE + (uint32_t)__builtin_amdgcn_ds_bpermute((int)((t8 >> 1) & 0xe0u), (int)lp);
    };
    auto general = [&](int D, bool all, u64 sm) __attribute__((always_inline)) {
        uint32_t todo = __builtin_amdgcn_readfirstlane((uint32_t)sm | (uint32_t)(sm >> 32));
#pragma unroll 1
        while (todo) {
            const uint32_t S = (uint32_t)__builtin_ctz(todo) >> 1;
            todo &= ~(3u << (2u * S));
            pin(t8); pin(la);
            const uint32_t V = lds_ld(P_TAB + (t8 & ~63u) + S * 4u), g = V & 15u;
            const uint32_t col = POS(S) * 128u + ((t8 >> 1) & 0x60u) + (t8 >> 8);
            const bool act = all ? V != 0xffffffffu : (V & 0x80000009u) == 9u;
            const int hr = HREG((int)S, D) - HB;
            uint32_t ln = log_line(act && g > 8u + NTAIL, col);
            if (act) {
                const u32x2 N = *(const LDS u32x2*)(uintptr_t)(la + S * 256u);
                *(LDS u32x2*)(uintptr_t)(la + S * 256u) = nul;
                lds_st(P_TAB + (t8 & ~63u) + S * 4u, vconst<0xffffffffu>());
                if (!(g & 1u)) hset_at(hr, N);
                else {
                    uint32_t a, st;
                    if (g < 8u) { a = (V & ~15u) + ((t8 & 0x38u) << 1); st = 8u; }
                    else {
                        if (g <= 8u + NTAIL) ln = TBASE + ((g - 8u) >> 1) * TPL + col * TST + blockIdx.x;
                        a = ln * 128u + (la & 0x38u); st = 64u;
                    }
                    __builtin_amdgcn_raw_buffer_store_b64(hget_at(hr), rsR, a, 0, AUX_SC1);
                    __builtin_amdgcn_raw_buffer_store_b64(N, rsR, a + st, 0, AUX_SC1);
                }
            }
        }
    };
    unsigned epoch = 0;
    u64 pb2 = 0ull;
    u64 fm = 0ull, sm = 0ull;
    auto look = [&](u32x2 c) __attribute__((always_inline)) {
        uint32_t V[2];
        const bool late = __builtin_amdgcn_ballot_w64(((U[0] | U[1]) & 8u) != 0u) != 0ull;
        epoch++;
        asm volatile("v_mov_b32 %0, -1\n\tv_mov_b32 %1, -1\n\t"
                     "v_cmpx_le_u32 %6, %7, %2\n\tv_mov_b32 %0, %4\n\tv_add_u32 %4, %9, %4\n\tv_subrev_u32 %2, %8, %2\n\ts_mov_b64 exec, -1\n\t"
                     "v_cmpx_le_u32 vcc, %8, %3\n\tv_mov_b32 %1, %5\n\tv_add_u32 %5, %9, %5\n\tv_subrev_u32 %3, %8, %3\n\t"
                     "s_or_b64 %6, %6, exec\n\ts_mov_b64 exec, -1"
                     : "=&v"(V[0]), "=&v"(V[1]), "+v"(c.x), "+v"(c.y), "+v"(U[0]), "+v"(U[1]), "=&s"(fm) : "s"(CHV), "n"(CHV), "s"(UINC) : "vcc", "scc");
        *(LDS u32x2*)(uintptr_t)(P_CNT + t8) = c;
        pin(t8);
        const uint32_t tw = tab_at();
        lds_st(tw, V[0]);
        lds_st(tw + 64u, V[1]);
        if (wv == 1) lds_st(P_CNT + NB * 4 - 256 + (t8 >> 1), vconst<0x40000000u>());
        const u64 pb = __builtin_amdgcn_ballot_w64((int)(V[0] & V[1]) >= 0);
        pb2 = (ABL & 4) ? 0ull : pb;
        sm = 0ull;
        if (__builtin_expect(late, 0)) {
            sm = __builtin_amdgcn_ballot_w64((V[0] & 0x80000009u) == 9u || (V[1] & 0x80000009u) == 9u);
#pragma unroll
            for (int e = 0; e < 2; e++)
                if ((V[e] & 0x8000000fu) == 9u + NTAIL) U[e] -= 2u * UINC;
        }
    };
    u32x4 Va, Vb;

    Raw raw[PD];
    {
        const int tid = tid0;
        asm volatile("" ::: "v88", "v127");
        const uint16_t fq = freq[0];
        if (tid == 128) tk = peek(vconst<M_BEAT * 4>());
        if (blockIdx.x == 0 && wv == 3 && (tid & 63) < M_BEAT / 64) {
            __hip_atomic_store(meta + (tid & 63) * 64, 0u, __ATOMIC_RELAXED, __HIP_MEMORY_SCOPE_AGENT);
            __hip_atomic_store(meta + (tid & 63) * 64 + 1, 0u, __ATOMIC_RELAXED, __HIP_MEMORY_SCOPE_AGENT);
        }
        const __amdgpu_buffer_rsrc_t rsE = __builtin_amdgcn_make_buffer_rsrc((void*)m, 0, E * 8, 0x00020000);
        u32x2 mv[5];
        mv[4] = __builtin_amdgcn_raw_buffer_load_b64(rsE, (t8 & 511u) * (uint32_t)(E >> 6), 0, 0);
#pragma unroll
        for (int q = 0; q < 4; q++)
            mv[q] = __builtin_amdgcn_raw_buffer_load_b64(rsE, t8 & 511u, ((blockIdx.x >> 2) * 256u + (blockIdx.x & 3u) * 16u + (uint32_t)wv + 64u * q) * 512u, AUX_NT);
#pragma unroll
        for (int d = 0; d < MDL; d++) load_one(raw[1], tl0, d);
        const u32x2 ci = {CINIT, CINIT};
        *(LDS u32x2*)(uintptr_t)(P_CNT + tid * 8) = ci;
        if (tid < 64) lds_st(P_CNT + (NB + tid) * 4, 0x40000000u);
        if (tid < 8 && tid != 2) lds_st(P_FLAG + tid * 4, tid == 0 ? 0xffffffffu : 0u);
        if (tid == 128) lds_st(P_FLAG + 8, tk);
        asm("s_nop 0\n\tv_readfirstlane_b32 %0, %1" : "=s"(frr) : "v"(bf2f(fq) * 0.15915494309189535f));
        const uint32_t lane = (uint32_t)tid & 63u;
        int e0[4];
        if (blockIdx.x < SLOTI / 64) {
            float w[16];
            load_w(wdown, w);
            auto dot = [&](u32x2 v, int c) __attribute__((always_inline)) -> f32x2 {
                const f32x2 w0 = {w[c], w[c + 1]}, w1 = {w[4 + c], w[5 + c]}, w2 = {w[8 + c], w[9 + c]}, w3 = {w[12 + c], w[13 + c]};
                return w0 * bf2f(v.x & 0xffffu) + w1 * bf2f(v.x >> 16) + w2 * bf2f(v.y & 0xffffu) + w3 * bf2f(v.y >> 16);
            };
#pragma unroll
            for (int cp = 0; cp < 2; cp++) {
                const f32x2 sd = dot(mv[4], 2 * cp);
#pragma unroll
                for (int e = 0; e < 2; e++) {
                    const int ex = (int)(wave_max(__float_as_uint(sd[e]) & 0x7fffffffu) >> 23);
                    e0[2 * cp + e] = __builtin_amdgcn_readfirstlane(ex == 0 ? 0 : min(max(ex - 129, -100), 100));
                }
                const f32x2 sc = {__uint_as_float((uint32_t)(127 - e0[2 * cp]) << 23), __uint_as_float((uint32_t)(127 - e0[2 * cp + 1]) << 23)};
                u32x4 o;
                u16x2 mx = {0, 0};
                f32x2 sq = {0.f, 0.f};
#pragma unroll
                for (int q = 0; q < 4; q++) {
                    const f32x2 y = dot(mv[q], 2 * cp) * sc;
                    const uint32_t hb = pk_bf16(y.x, y.y);
                    const f32x2 x = {__uint_as_float(hb << 16), __uint_as_float(hb & 0xffff0000u)};
                    o[q] = __builtin_bit_cast(uint32_t, __builtin_amdgcn_cvt_pkrtz(x.x, x.y));
                    mx = __builtin_elementwise_max(mx, __builtin_bit_cast(u16x2, hb & 0x7fff7fffu));
                    sq += x * x;
                }
                *(LDS u32x4*)(uintptr_t)(P_BUF + (cp * 64u + lane) * 272u + wv * 16) = o;
#pragma unroll
                for (int e = 0; e < 2; e++) {
                    const u32x2 st = {wave_max(mx[e]), __float_as_uint(wave_sum(sq[e]))};
                    if (lane == 0u) *(LDS u32x2*)(uintptr_t)(P_LST + ((2 * cp + e) * 16 + wv) * 8) = st;
                }
            }
        }
#pragma unroll
        for (int d = MDL; d < 6; d++) load_one(raw[1], tl0, d);
#pragma unroll
        for (int d = 0; d < 6; d++)
            if (((LPOS >> (4 * d)) & 15) < 4) load_one(raw[0], tl1, d);
        __syncthreads();
        u32x4 mo[2];
#pragma unroll
        for (int cp = 0; cp < 2; cp++) mo[cp] = *(const LDS u32x4*)(uintptr_t)(P_BUF + (cp * 64u + (t8 >> 7)) * 272u + ((t8 + t8) & 0xf0u));
        if (blockIdx.x == 0 && wv == 3) asm volatile("s_waitcnt vmcnt(0)" ::: "memory");
        __syncthreads();
#pragma unroll
        for (int s = 0; s < 16; s++) *(LDS u32x2*)(uintptr_t)(la + s * 256) = nul;
        asm volatile("" : "+v"(raw[1].o[0]), "+v"(raw[1].o[1]), "+v"(raw[1].i[0]), "+v"(raw[1].i[1]), "+v"(raw[1].a[0]), "+v"(raw[1].a[1]));
        if (blockIdx.x < SLOTI / 64) {
            const __amdgpu_buffer_rsrc_t rsM = __builtin_amdgcn_make_buffer_rsrc((void*)(meta + W_MDC / 4), 0, (int)(128u * SLOTI * 4u), 0x00020000);
#pragma unroll
            for (int cp = 0; cp < 2; cp++) {
                const uint32_t row = cp * 64u + (t8 >> 7), c16 = (t8 + t8) & 0xf0u;
                __builtin_amdgcn_raw_buffer_store_b128(mo[cp], rsM, row * (SLOTI * 4u) + c16 + blockIdx.x * 256u, 0, AUX_SC1);
            }
            if (wv == 0) {
                const u32x2 st = *(const LDS u32x2*)(uintptr_t)(P_LST + t8);
                const u32x2 r = {row_max(st.x) | (row_max(st.x ? 0xffffu - st.x : 0u) << 16), __float_as_uint(row_sum(__uint_as_float(st.y)))};
                if ((lane & 15u) == 15u) ((u32x2*)(meta + W_STAT / 4))[(lane >> 4) * NWG + blockIdx.x] = r;
                if (blockIdx.x == 0 && lane < 4u) meta[M_E0 + lane] = (uint32_t)(lane == 0u ? e0[0] : lane == 1u ? e0[1] : lane == 2u ? e0[2] : e0[3]);
            }
        }
    }
    pin(la);
    ST(0)
    RTS(5)

    uint32_t w[8], aw[8], xv[8];
    u64 nm[8];
    auto later2 = [&](int k) __attribute__((always_inline)) {
        uint32_t x0, x1;
        asm volatile("v_lshrrev_b32 %0, 4, %2\n\tv_lshrrev_b32 %1, 4, %5\n\tv_bitop3_b32 %0, %2, %0, 12 bitop3:0x72\n\tv_bitop3_b32 %1, %5, %1, 12 bitop3:0x72\n\t"
                     "s_not_b64 exec, %8\n\tds_write_b32 %2, %3 offset:%10\n\tds_or_b32 %0, %4 offset:%10\n\t"
                     "s_not_b64 exec, %9\n\tds_write_b32 %5, %6 offset:%10\n\tds_or_b32 %1, %7 offset:%10\n\ts_mov_b64 exec, -1"
                     : "=&v"(x0), "=&v"(x1)
                     : "v"(aw[k]), "v"(w[k]), "v"(xv[k]), "v"(aw[k + 1]), "v"(w[k + 1]), "v"(xv[k + 1]), "s"(nm[k]), "s"(nm[k + 1]), "n"(P_BUF) : "memory", "scc");
    };
    auto drain = [&]() __attribute__((always_inline)) {
        if (wv == 1) {
            const uint32_t q = (epoch + 1u) & 3u;
            const uint32_t n = __builtin_amdgcn_readfirstlane(lds_ld(vconst<P_FLAG + 16>() + q * 4u));
            if (__builtin_expect(n != 0u, 0)) {
                pin(t8);
                const uint32_t l = (t8 >> 3) & 63u;
                if (l < min(n, (uint32_t)LCAP)) {
                    const u32x4 e = *(const LDS u32x4*)(uintptr_t)(P_LST + (q * LCAP + l) * 16u);
                    lds_st(P_BUF + e.x, e.y);
                    lds_or(P_BUF + slot_x(e.x), e.z);
                }
                lds_st(vconst<P_FLAG + 16>() + q * 4u, vconst<0>());
            }
        }
    };
    auto again = [&](int D) __attribute__((always_inline)) {
        do {
            general(D, true, fm);
            __syncthreads();
            later2(0); later2(2); later2(4); later2(6);
            drain();
#pragma unroll
            for (int k = 0; k < 8; k++) {
                pin(aw[k]);
                bool due = false;
                if (aw[k] & 0x80000000u) {
                    xv[k] -= CHV;
                    if (xv[k] < CHV) {
                        const uint32_t f = (aw[k] >> 16) & 0x3ffu;
                        aw[k] = slot_w(aw[k] & 0xffffu, xv[k]);
                        xv[k] = f << ((xv[k] >> 8) & 31u);
                        due = true;
                    } else lds_st(vconst<P_FLAG>(), epoch + 1u);
                }
                nm[k] = ~__builtin_amdgcn_ballot_w64(due);
            }
            look(*(const LDS u32x2*)(uintptr_t)(P_CNT + t8));
            asm volatile("s_waitcnt lgkmcnt(0)" ::: "memory");
            __syncthreads();
        } while (__builtin_amdgcn_readfirstlane(lds_ld(vconst<P_FLAG>())) == epoch);
    };
    auto round = [&](auto DD, auto LL, Raw& rc, Raw& rn, int rd) __attribute__((always_inline)) {
        constexpr int D = decltype(DD)::value;
        constexpr bool LAST = decltype(LL)::value == 1, FIRST = decltype(LL)::value == 2;
        const int ng = min(GR, G - tl0 * GR);
        auto hook = [&](int p) __attribute__((always_inline)) {
#pragma unroll
            for (int q = 0; q < 6; q++)
            {
                constexpr int dummy = 0; (void)dummy;
                const int lp = (LPOS >> (4 * q)) & 15;
                if (!LAST && lp < 4 && lp == p) load_one(rc, tl2, q);
                if (!LAST && lp >= 4 && (lp < 8 || lp >= 12 ? lp : lp - 8) == p) load_one(rn, tl1, q);
            }
        };
        uint32_t wn[8], ca[8], px[4], cv[8];
        float mx;
        float cs[2];
        auto convA = [&](int q) __attribute__((always_inline)) {
            if (LAST) return;
            const uint32_t a = rc.a[q >> 1][q & 1];
            const f32x2 av = {__uint_as_float(a << 16), __uint_as_float(a & 0xffff0000u)};
            f32x2 rv;
            asm("v_pk_mul_f32 %0, %1, %2 op_sel_hi:[1,0]" : "=v"(rv) : "v"(av), "s"((u64)__float_as_uint(frr)));
            mx = __builtin_elementwise_maximum(__builtin_elementwise_maximum(__builtin_fabsf(rv.x), __builtin_fabsf(rv.y)), (q & 1) ? mx : 0.f);
            cs[0] = __builtin_amdgcn_cosf(rv.x); cs[1] = __builtin_amdgcn_cosf(rv.y);
            asm volatile("" : "+v"(mx), "+v"(cs[0]), "+v"(cs[1]));
            __builtin_amdgcn_sched_barrier(0);
        };
        auto convB = [&](int k) __attribute__((always_inline)) {
            if (LAST) return;
            const uint32_t o = rc.o[k >> 2][k & 3], i = rc.i[k >> 2][k & 3];
            wn[k] = (__builtin_amdgcn_alignbit(bfi(63u, o, o >> 5), __float_as_uint(cs[k & 1]), 30u) << 14) | (i >> 6);
            ca[k] = (bfi(63u, i, o) << 2) & 0x1ffcu;
            pin(wn[k]); pin(ca[k]);
            __builtin_amdgcn_sched_barrier(0);
        };
        auto convD = [&](int q) __attribute__((always_inline)) {
            if (LAST) return;
            asm volatile("v_cvt_pk_f16_f32 %0, |%1|, |%2|\n\tv_pk_mul_f16 %0, %0, %3 op_sel_hi:[1,0]" : "=v"(px[q]) : "v"(cs[0]), "v"(cs[1]), "s"(0x3ffu));
        };
        uint32_t cst;
        auto check = [&](int j) __attribute__((always_inline)) {
            if (LAST) return;
            if (__builtin_expect((__builtin_amdgcn_ballot_w64(!(mx <= RMAXREV)) | (u64)(uint32_t)(GR - ng)) != 0ull, 0)) {
                pin(t8);
                asm volatile("" : "+v"(rc.a[j]));
                const int lg = j * NT + (int)(t8 >> 3);
#pragma unroll
                for (int k = 0; k < 4; k++) {
                    const uint32_t a = (k & 1) ? (rc.a[j][k >> 1] >> 16) : (rc.a[j][k >> 1] & 0xffffu);
                    const bool nrm = __builtin_fabsf(frr * bf2f(a)) <= RMAXREV;
                    if (!(lg < ng && nrm)) ca[j * 4 + k] = NB * 4u + ((t8 >> 1) & 0xfcu);
                    if (lg < ng && !nrm) {
                        ready();
                        __builtin_amdgcn_raw_buffer_store_b32((uint32_t)((tl0 * GR + lg) * 4 + k), rsR, spec0 - draw(vconst<M_LOG * 4 + 4>()) * 4u, 0, 0);
                    }
                }
            }
            cst = vconst<CSTEP>();
        };
        auto ticket = [&](int k) __attribute__((always_inline)) {
            if (LAST) return;
            cv[k] = lds_add(P_CNT + ca[k], cst);
            __builtin_amdgcn_sched_barrier(0);
        };
        auto insert = [&](int j) __attribute__((always_inline)) {
            const int k0 = 4 * j;
            const bool odd = __builtin_amdgcn_ballot_w64(max(max(cv[k0], cv[k0 + 1]), max(cv[k0 + 2], cv[k0 + 3])) >= 2u * CHV) != 0ull;
#pragma unroll
            for (int k = k0; k < k0 + 4; k += 2) {
                uint32_t x0, x1;
                asm volatile("v_lshrrev_b32 %6, 11, %8\n\tv_lshrrev_b32 %7, 11, %9\n\t"
                             "v_bitop3_b32 %6, %6, %10, 60 bitop3:0x28\n\tv_bitop3_b32 %7, %7, %11, 60 bitop3:0x28\n\t"
                             "v_lshl_or_b32 %0, %10, 4, %6\n\tv_lshl_or_b32 %1, %11, 4, %7\n\t"
                             "v_lshlrev_b32_sdwa %2, %8, %12 dst_sel:DWORD dst_unused:UNUSED_PAD src0_sel:BYTE_1 src1_sel:WORD_0\n\t"
                             "v_lshlrev_b32_sdwa %3, %9, %12 dst_sel:DWORD dst_unused:UNUSED_PAD src0_sel:BYTE_1 src1_sel:WORD_1\n\t"
                             "v_bitop3_b32 %6, %0, %10, 12 bitop3:0x72\n\tv_bitop3_b32 %7, %1, %11, 12 bitop3:0x72\n\t"
                             "v_cmpx_gt_u32 %4, %15, %8\n\tds_write_b32 %0, %13 offset:%16\n\tds_or_b32 %6, %2 offset:%16\n\ts_mov_b64 exec, -1\n\t"
                             "v_cmpx_gt_u32 %5, %15, %9\n\tds_write_b32 %1, %14 offset:%16\n\tds_or_b32 %7, %3 offset:%16\n\ts_mov_b64 exec, -1"
                             : "=&v"(aw[k]), "=&v"(aw[k + 1]), "=&v"(xv[k]), "=&v"(xv[k + 1]), "=&s"(nm[k]), "=&s"(nm[k + 1]), "=&v"(x0), "=&v"(x1)
                             : "v"(cv[k]), "v"(cv[k + 1]), "v"(ca[k]), "v"(ca[k + 1]), "v"(px[k >> 1]), "v"(wn[k]), "v"(wn[k + 1]), "s"(CHV), "n"(P_BUF) : "memory");
                w[k] = wn[k]; w[k + 1] = wn[k + 1];
                if (__builtin_expect(odd, 0)) {
#pragma unroll
                    for (int q = k; q < k + 2; q++) {
                        nm[q] |= __builtin_amdgcn_ballot_w64(cv[q] >= 2u * CHV);
                        if (cv[q] >= 2u * CHV && cv[q] < 0x40000000u) {
                            const uint32_t lq = (epoch + 2u) & 3u;
                            uint32_t p = LCAP;
                            if (cv[q] < 3u * CHV) p = lds_add(vconst<P_FLAG + 16>() + lq * 4u, vconst<1>());
                            if (p < LCAP) {
                                const u32x2 e = {aw[q], w[q]};
                                *(LDS u32x2*)(uintptr_t)(P_LST + (lq * LCAP + p) * 16u) = e;
                                lds_st(P_LST + (lq * LCAP + p) * 16u + 8u, xv[q]);
                            } else {
                                aw[q] = ca[q] | ((q & 1) ? (px[q >> 1] & 0xffff0000u) : (px[q >> 1] << 16)) | 0x80000000u;
                                xv[q] = cv[q] - CHV;
                                lds_st(vconst<P_FLAG>(), epoch);
                            }
                        }
                    }
                }
            }
        };
        constexpr int P0 = D ? 0 : 3, P1 = D ? 1 : 2, P2 = D ? 2 : 1, P3 = D ? 3 : 0;
        const uint32_t fl = lds_ld(vconst<P_FLAG>());
        if (!LAST) asm volatile("" : "+v"(rc.o[0]), "+v"(rc.o[1]), "+v"(rc.i[0]), "+v"(rc.i[1]), "+v"(rc.a[0]), "+v"(rc.a[1]));
        if (!LAST && (rd + 4 >= nstat || tl1 >= ntile) && wv == 2) {
            pin(tk);
            if ((t8 & 511u) == 0u) {
                if (tl1 >= ntile) {
                    if (rd + 3 <= nstat) ready();
                    (void)draw(vconst<M_MD * 4>());
                } else if (rd + 4 == nstat) tk = peek(vconst<M_BEAT * 4>());
                else {
                    if (rd + 3 == nstat) {
                        if (tk == lds_ld(vconst<P_FLAG + 8>())) ready();
                        tk = 0u;
                    }
                    lds_st(vconst<P_FLAG + 4>(), tk);
                    if ((int)tk < ntile - nstat * NWG) tk = draw(vconst<M_TILE * 4>());
                }
            }
        }
        if (!LAST && blockIdx.x == 0 && wv == 3 && (t8 & 511u) == 0u) beat();
        hook(12);
        ST(3)
        pin(t8);
        const uint32_t tr = P_TAB + (t8 & ~63u), u16 = (t8 + t8) & 0x70u;
        convA(0);
        convB(0);
        const bool ag = __builtin_amdgcn_readfirstlane(fl) == epoch;
        if (!FIRST && __builtin_expect(LAST || ag || sm != 0ull, 0)) {
            if (ag) again(D);
            general(D, false, sm);
            Va = *(const LDS u32x4*)(uintptr_t)(P_TAB + (t8 & ~63u) + P0 * 16);
            Vb = *(const LDS u32x4*)(uintptr_t)(P_TAB + (t8 & ~63u) + P1 * 16);
            asm volatile("" : "+v"(Va), "+v"(Vb));
        }
        uint32_t pq = (uint32_t)pb2 | (uint32_t)(pb2 >> 32);
        pq = (pq | (pq >> 1)) & 0x55555555u;
        pq = __builtin_amdgcn_readfirstlane(pq + (pq & (far * NEARM)));
        uint32_t ad[8];
        if (!FIRST) ad[0] = take<4 * P0 + 0, D>(Va[0], u16, la, nul);
        convB(1);
        if (!FIRST) ad[1] = take<4 * P0 + 1, D>(Va[1], u16, la, nul);
        convD(0);
        convA(1);
        if (!FIRST) ad[2] = take<4 * P0 + 2, D>(Va[2], u16, la, nul);
        convB(2);
        hook(13);
        if (!FIRST) ad[3] = take<4 * P0 + 3, D>(Va[3], u16, la, nul);
        convB(3);
        if (!FIRST) put<4 * P0 + 0, LAST ? 3 : 3>(rsA, ad[0], pq);
        if (!FIRST) ad[4] = take<4 * P1 + 0, D>(Vb[0], u16, la, nul);
        convD(1);
        check(0);
        ticket(0);
        hook(4);
        if (!FIRST) put<4 * P0 + 1, LAST ? 3 : 4>(rsA, ad[1], pq);
        if (!FIRST) ad[5] = take<4 * P1 + 1, D>(Vb[1], u16, la, nul);
        convA(2);
        ticket(1);
        if (!FIRST) put<4 * P0 + 2, LAST ? 3 : 5>(rsA, ad[2], pq);
        if (!FIRST) ad[6] = take<4 * P1 + 2, D>(Vb[2], u16, la, nul);
        convB(4);
        ticket(2);
        if (!FIRST) put<4 * P0 + 3, LAST ? 3 : 6>(rsA, ad[3], pq);
        if (!FIRST) Va = *(const LDS u32x4*)(uintptr_t)(tr + P2 * 16);
        __builtin_amdgcn_sched_barrier(0);
        if (!FIRST) ad[7] = take<4 * P1 + 3, D>(Vb[3], u16, la, nul);
        convB(5);
        ticket(3);
        SETP(2);
        if (!FIRST) put<4 * P1 + 0, LAST ? 4 : 8>(rsA, ad[4], pq);
        if (!FIRST) ad[0] = take<4 * P2 + 0, D>(Va[0], u16, la, nul);
        convD(2);
        convA(3);
        hook(5);
        if (!FIRST) put<4 * P1 + 1, LAST ? 4 : 7>(rsA, ad[5], pq);
        if (!FIRST) ad[1] = take<4 * P2 + 1, D>(Va[1], u16, la, nul);
        convB(6);
        if (!FIRST) put<4 * P1 + 2, LAST ? 4 : 6>(rsA, ad[6], pq);
        if (!FIRST) ad[2] = take<4 * P2 + 2, D>(Va[2], u16, la, nul);
        convB(7);
        if (!FIRST) put<4 * P1 + 3, LAST ? 3 : 4>(rsA, ad[7], pq);
        if (!FIRST) Vb = *(const LDS u32x4*)(uintptr_t)(tr + P3 * 16);
        __builtin_amdgcn_sched_barrier(0);
        if (!FIRST) ad[3] = take<4 * P2 + 3, D>(Va[3], u16, la, nul);
        convD(3);
        check(1);
        ticket(4);
        SETP(1);
        hook(6);
        if (!FIRST) put<4 * P2 + 0, LAST ? 4 : 5>(rsA, ad[0], pq);
        if (!FIRST) ad[4] = take<4 * P3 + 0, D>(Vb[0], u16, la, nul);
        ticket(5);
        if (!FIRST) put<4 * P2 + 1, LAST ? 4 : 6>(rsA, ad[1], pq);
        if (!FIRST) ad[5] = take<4 * P3 + 1, D>(Vb[1], u16, la, nul);
        ticket(6);
        if (!FIRST) put<4 * P2 + 2, LAST ? 4 : 7>(rsA, ad[2], pq);
        if (!FIRST) ad[6] = take<4 * P3 + 2, D>(Vb[2], u16, la, nul);
        ticket(7);
        SETP(0);
        if (!FIRST) put<4 * P2 + 3, LAST ? 3 : 7>(rsA, ad[3], pq);
        if (!FIRST) ad[7] = take<4 * P3 + 3, D>(Vb[3], u16, la, nul);
        hook(7);
        ST(4)
        SETP(PRIO > 1 ? 3 : 0);
        asm volatile("s_waitcnt lgkmcnt(0)" ::: "memory");
        __syncthreads();
        ST(5)
        u32x2 cn;
        uint32_t tv;
        if (!LAST) { cn = *(const LDS u32x2*)(uintptr_t)(P_CNT + t8); tv = lds_ld(vconst<P_FLAG + 4>()); }
        __builtin_amdgcn_sched_barrier(0);
        if (!FIRST) later2(0);
        if (!FIRST) put<4 * P3, 15>(rsA, ad[4], pq);
        if (!FIRST) later2(2);
        if (!FIRST) put<4 * P3 + 1, 15>(rsA, ad[5], pq);
        if (!FIRST) drain();
        if (!LAST) {
            tl2 = rd + 2 < nstat ? fixed_tile(rd + 2) : nstat * NWG + (int)__builtin_amdgcn_readfirstlane(tv);
            look(cn);
        }
        hook(0);
        SETP(PRIO > 1 ? 2 : 0);
        if (!FIRST) later2(4);
        if (!FIRST) put<4 * P3 + 2, 15>(rsA, ad[6], pq);
        if (!FIRST) later2(6);
        if (!FIRST) put<4 * P3 + 3, 15>(rsA, ad[7], pq);
        if (!LAST) {
            hook(1);
            SETP(PRIO > 1 ? 1 : 0);
            insert(0);
            hook(2);
            SETP(0);
            pin(t8);
            Va = *(const LDS u32x4*)(uintptr_t)(P_TAB + (t8 & ~63u) + P3 * 16);
            Vb = *(const LDS u32x4*)(uintptr_t)(P_TAB + (t8 & ~63u) + P2 * 16);
            __builtin_amdgcn_sched_barrier(0);
            insert(1);
            hook(3);
            ST(1)
            SETP(3);
            asm volatile("s_waitcnt lgkmcnt(0)" ::: "memory");
            __syncthreads();
            ST(2)
            tl0 = tl1; tl1 = tl2;
        }
    };
#define HMV(A, B) "v_mov_b64 v[" #A ":" #A "+1], v[" #B ":" #B "+1]\n\t"
#define HMV4(A) HMV(A + 8, A + 6) HMV(A + 6, A + 4) HMV(A + 4, A + 2) HMV(A + 2, A)
    round(IC<1>(), IC<2>(), raw[1], raw[0], 0);
    int rd = 1;
#pragma unroll 1
    for (; tl1 < ntile; rd += 2) {
        round(IC<0>(), IC<0>(), raw[0], raw[1], rd);
        round(IC<1>(), IC<0>(), raw[1], raw[0], rd + 1);
    }
    if (tl0 >= ntile) asm volatile(HMV4(88) HMV4(98) HMV4(108) HMV4(118));
    asm volatile("" : "+s"(tl0));
    if (tl0 < ntile) round(IC<0>(), IC<0>(), raw[0], raw[1], rd);
    RTS(1)
    round(IC<1>(), IC<1>(), raw[1], raw[0], 0);
    if (__builtin_amdgcn_readfirstlane(lds_ld(P_FLAG + 16 + ((epoch + 2u) & 3u) * 4u)) != 0u) {
        asm volatile("s_waitcnt lgkmcnt(0)" ::: "memory");
        __syncthreads();
        look(*(const LDS u32x2*)(uintptr_t)(P_CNT + t8));
        general(0, true, fm);
        __syncthreads();
        drain();
    }
    {
        uint32_t* tcnt = meta + W_TCNT / 4;
        const u32x2 c = *(const LDS u32x2*)(uintptr_t)(P_CNT + t8);
        const uint32_t tw = tab_at();
#pragma unroll
        for (int e = 0; e < 2; e++) {
            const uint32_t g = U[e] & 15u;
            lds_st(tw + e * 64u, g >= 8u ? 0x80000000u : ((U[e] + ((g & 1u) ? 0u : SK * 64u)) & 0x7fffff81u) | (((7u - g) << 1) & 12u));
            *(LDS uint8_t*)(uintptr_t)(P_CNT + (t8 & ~511u) + (((tw - P_TAB) >> 2) & 127u) + e * 16u) =
                (uint8_t)(g < 8u ? 0u : ((g - 8u) >> 1) + (((c[e] + 2u * CSTEP) >> 15) << 2) + (g & 1u) * 0x50u);
        }
        asm volatile("s_waitcnt lgkmcnt(0)" ::: "memory");
        __syncthreads();
        const uint32_t u16 = (t8 + t8) & 0x70u;
        u32x4 En[4];
#pragma unroll
        for (int p = 0; p < 4; p++) En[p] = *(const LDS u32x4*)(uintptr_t)(P_TAB + (t8 & ~63u) + p * 16u);
        const u32x4 cb = *(const LDS u32x4*)(uintptr_t)(P_CNT + (t8 & ~511u) + ((t8 >> 2) & 0x70u));
        u32x2 N[16];
#pragma unroll
        for (int s = 0; s < 16; s++) N[s] = *(const LDS u32x2*)(uintptr_t)(la + s * 256u);
        if (u16 == 0u) *(u32x4*)(tcnt + blockIdx.x * 512u + (t8 >> 4)) = cb;
        const uint32_t sg = P_BUF + ((t8 & ~63u) << 4);
        uint32_t ps;
        auto rest = [&](auto SL) __attribute__((always_inline)) {
            constexpr int s = decltype(SL)::value;
            const uint32_t en = En[s >> 2][s & 3], by = cb[s >> 2] >> ((s & 3) * 8);
            const u32x2 Hr = hget<HREG(s, 0)>();
            const uint32_t at = ps + ((la & 0x38u) ^ ((s & 3) << 4));
            *(LDS u32x2*)(uintptr_t)at = Hr;
            *(LDS u32x2*)(uintptr_t)(at + (by & 0x40u)) = N[s];
            ps += (by & 0x3cu) << 2;
            const u32x2 Hs = (en & 1u) ? Hr : nul;
            const u32x4 d = {Hs.x, Hs.y, N[s].x, N[s].y};
            uint32_t a = __builtin_amdgcn_bitop3_b32(en, u16, 15u, 0x54);
            {
                const u32x4 rs_ = rsA;
                const uint32_t far_ = ((NEARM >> (2 * s)) & 1u) ? far : 0u;
                asm volatile("s_cmp_eq_u32 %3, 0\n\ts_cbranch_scc0 1f\n\tbuffer_store_dwordx4 %0, %1, %2, 0 offen sc1\n\ts_branch 2f\n"
                             "1:\n\tbuffer_store_dwordx4 %0, %1, %2, 0 offen nt\n2:\n\ts_nop 1" :: "v"(d), "v"(a), "s"(rs_), "s"(far_) : "memory", "scc");
            }
            if (__builtin_expect(__builtin_amdgcn_ballot_w64((en & 12u) != 0u) != 0ull, 0)) {
                const u32x4 dn = {nul.x, nul.y, nul.x, nul.y};
#pragma unroll 1
                for (uint32_t k = (en >> 2) & 3u; k > 0u; k--) {
                    a += SK * 128u;
                    __builtin_amdgcn_raw_buffer_store_b128(dn, rsR, a, 0, AUX_SC1);
                }
            }
        };
        auto pack = [&](int p) __attribute__((always_inline)) {
            const uint32_t a = (SBASE + ((((t8 >> 1) & 0x60u) + (t8 >> 8)) * 2u + p) * (NWG * 9u) + blockIdx.x * 9u) * 128u + u16;
            asm volatile("" ::: "memory");
#pragma unroll 1
            for (uint32_t q = 0; __builtin_amdgcn_ballot_w64(sg + q < ps) != 0ull; q += 256u) {
                const u32x4 d0 = *(const LDS u32x4*)(uintptr_t)(sg + q + u16), d1 = *(const LDS u32x4*)(uintptr_t)(sg + q + 128u + u16);
                __builtin_amdgcn_raw_buffer_store_b128(d0, rsR, sg + q < ps ? a + q : 0xffffff00u, 0, AUX_SC1);
                __builtin_amdgcn_raw_buffer_store_b128(d1, rsR, sg + q + 128u < ps ? a + q + 128u : 0xffffff00u, 0, AUX_SC1);
            }
            asm volatile("" ::: "memory");
        };
        asm volatile("" : "+v"(N[0]), "+v"(N[1]), "+v"(N[2]), "+v"(N[3]), "+v"(N[4]), "+v"(N[5]), "+v"(N[6]), "+v"(N[7]) :: "memory");
        asm volatile("" : "+v"(N[8]), "+v"(N[9]), "+v"(N[10]), "+v"(N[11]), "+v"(N[12]), "+v"(N[13]), "+v"(N[14]), "+v"(N[15]) :: "memory");
        ps = sg;
        rest(IC<0>()); rest(IC<1>()); rest(IC<2>()); rest(IC<3>()); rest(IC<4>()); rest(IC<5>()); rest(IC<6>()); rest(IC<7>());
        pack(0);
        ps = sg;
        rest(IC<8>()); rest(IC<9>()); rest(IC<10>()); rest(IC<11>()); rest(IC<12>()); rest(IC<13>()); rest(IC<14>()); rest(IC<15>());
        pack(1);
    }
    RTS(2)
    ST(6)
    if (blockIdx.x == 0 && t8 == 1536u) {
        do {
            beat();
            __builtin_amdgcn_s_sleep(8);
        } while (peek(vconst<M_MD * 4>()) < NWG);
    }
    RTS(3)
    RTS(4)
    ST(7)
    ST_OUT(0)
}

#define A_MD 0
#define A_ACC (SLOTI * 4)
#define A_FLAG (A_ACC + SLOTO * 4)
#define A_LDS (A_FLAG + 512)
static_assert(SLOTI * 4 == 3 * NT * 16 + 1024, "md staging assumes three full vectors and 256 dwords");
#define BIAS 0x40004000u
#define NFAST 8
#define NLD 10
#define PRE0 2
#define PRE1 4
#define MDF 0x0a11333
#define MDP 0xaaaaa
#define ROTH 0
#define ROTP 0
#define HAUX AUX_NT
#define FALL 0
#define V1 3
#define HP 2
#define WARMW 0
#define POLLP 4
#define MPOS 3
#define FDBL 6
#define OAUX 2
#define MDQ 0x00888
#define UNM 1
#define APRIO 0x543
#define SHARE (SLOTO / 8)
#define SPINCAP 4096u
#define ZSIG 9.0f
#define HSLOW 0x80008000u
#define GAS __attribute__((address_space(1)))

__global__ __launch_bounds__(NT) __attribute__((amdgpu_num_vgpr(53))) void acc_kernel(char* ws, int T, int E, int ks, int lcap, float zf, int pad,
                                                 const uint16_t* __restrict__ centers, const uint16_t* __restrict__ wrbf,
                                                 const uint16_t* __restrict__ wsbf, const uint16_t* __restrict__ wup,
                                                 const u32x2* __restrict__ m, const uint16_t* __restrict__ dist, u32x2* __restrict__ out,
                                                 const int* __restrict__ iin, const int* __restrict__ iout, const uint16_t* __restrict__ ang,
                                                 const uint16_t* __restrict__ wdown, const uint16_t* __restrict__ freq) {
    const char* recs = ws + W_RECS;
    const u32x4* tcnt = (const u32x4*)(ws + W_TCNT);
    const uint32_t* mdc = (const uint32_t*)(ws + W_MDC);
    const u32x4* stat = (const u32x4*)(ws + W_STAT);
    uint32_t* partial = (uint32_t*)(ws + W_PART);
    unsigned* meta = (unsigned*)ws;
    extern __shared__ __attribute__((aligned(16))) char smem[];
    ST_DECL
    RTS(8)
    unsigned* acc = (unsigned*)(smem + A_ACC);
    unsigned* s_flag = (unsigned*)(smem + A_FLAG);
    const int tid = threadIdx.x;
    const int lane = tid & 63;
    const int wv = __builtin_amdgcn_readfirstlane(tid >> 6);
    const int cp = (blockIdx.x >> 3) & 1;
    const int h = blockIdx.x & 3;
    const int j = ((blockIdx.x >> 2) & 1) * 16 + (blockIdx.x >> 4);
    u32x4 tc;
    const __amdgpu_buffer_rsrc_t rsM = __builtin_amdgcn_make_buffer_rsrc((void*)mdc, 0, (int)(128u * SLOTI * 4u), 0x00020000);

    u32x4 pm[4];
    u32x4 ring[NLD];
    auto load_md = [&](int ff, int pl, int mdp) __attribute__((always_inline)) {
        const uint32_t so = (uint32_t)(cp * 64 + ((ABL & 64) ? 0 : ff * 4) + h) * (SLOTI * 4u);
#pragma unroll
        for (int v = 0; v < 3; v++)
            if (pl < 0 || pl == ((mdp >> (4 * v)) & 15)) pm[v] = __builtin_amdgcn_raw_buffer_load_b128(rsM, (uint32_t)tid * 16u, so + (uint32_t)v * (NT * 16u), 0);
        if ((pl < 0 || pl == (mdp & 15)) && wv == 0) pm[3] = __builtin_amdgcn_raw_buffer_load_b128(rsM, (uint32_t)tid * 16u, so + 3u * NT * 16u, 0);
        __builtin_amdgcn_sched_barrier(0);
    };
    const uint32_t lf = (uint32_t)(tid >> 9) * (SKH * 128u) + (uint32_t)(tid & 0x1e0) * 16u + (uint32_t)(tid & 12) * 32u + (uint32_t)(tid & 16) * 4u + (uint32_t)(tid & 3) * 16u;
    const uint32_t lp = (uint32_t)(tid >> 8) * (SKM * 128u) + (uint32_t)((tid >> 2) & 63) * 128u + (uint32_t)(tid & 3) * 16u;
    auto load_main = [&](int i, int ff) __attribute__((always_inline)) -> u32x4 {
        const uint32_t q = (uint32_t)(((i >> 1) - HP) & 3), SK = q ? SKM : SKH, SS = q ? SSM : SSH;
        const __amdgpu_buffer_rsrc_t rs = __builtin_amdgcn_make_buffer_rsrc((void*)(recs + (q * SQ + (uint32_t)ff * SS + (uint32_t)(h * 32 + j) * SC) * 128u), 0, ks * (int)(SK * 128u), 0x00020000);
        if (q == 0u) return __builtin_amdgcn_raw_buffer_load_b128(rs, lf, (i & 1) * (int)(2u * SK * 128u), AUX_NT);
        return __builtin_amdgcn_raw_buffer_load_b128(rs, lp + (uint32_t)(i & 1) * 64u, 0, 0);
    };
    const __amdgpu_buffer_rsrc_t rsT = __builtin_amdgcn_make_buffer_rsrc((void*)recs, 0, (int)(LBASE * 128u), 0x00020000);
    const uint32_t tof = (uint32_t)(lane & 2) * (TPL * 64u) + (uint32_t)(tid >> 2) * 128u + (uint32_t)(lane & 1) * 64u + 4u + (TBASE + (uint32_t)(h * 32 + j) * TST) * 128u;
    const uint32_t sp0 = (SBASE + (uint32_t)(h * 32 + j) * (2u * NWG * 9u) + (uint32_t)(tid >> 2) * 9u) * 128u;
    auto tail_rank = [&](uint32_t c, uint32_t sp, int ff, uint32_t t0, int& dst, uint32_t& en) __attribute__((always_inline)) -> int {
        const int k4 = (int)(((uint32_t)(lane & 3) + t0) * 4u) - (int)((c & 3u) * 8u);
        const int left = (int)((c >> 2) & 15u) - k4;
        const bool has = left > 0;
        uint32_t ea = tof + (uint32_t)POS(ff) * (128u * TST * 128u), eb = sp + (uint32_t)(k4 * 16 + min(max(left, 0), 4));
        asm volatile("" : "+v"(ea), "+v"(eb));
        en = k4 < 0 ? ea : eb;
        const u64 mask = __builtin_amdgcn_ballot_w64(has);
        const int n = __builtin_popcountll(mask);
        const int rank = __builtin_amdgcn_mbcnt_hi((uint32_t)(mask >> 32), __builtin_amdgcn_mbcnt_lo((uint32_t)mask, 0u));
        const int r = has ? rank : (n + lane - rank);
        dst = ((r & 15) << 4) | ((r >> 4) << 2);
        return n;
    };
    auto load_tail = [&](uint32_t en, u64& tm) __attribute__((always_inline)) -> u32x4 {
        const bool on = (uint32_t)(lane & 3) < (en & 7u) && !(ABL & 32);
        tm = __builtin_amdgcn_ballot_w64(on);
        return __builtin_amdgcn_raw_buffer_load_b128(rsT, on ? (en & ~15u) + (uint32_t)(lane & 3) * 16u : 0xffffff00u, 0, 0);
    };
    f32x2 fa[4][4];
    u32x4 em[2];
    u32x2 dw2;
    uint32_t wl;
    const __amdgpu_buffer_rsrc_t rsE = __builtin_amdgcn_make_buffer_rsrc((void*)m, 0, E * 8, 0x00020000);
    const __amdgpu_buffer_rsrc_t rsD = __builtin_amdgcn_make_buffer_rsrc((void*)dist, 0, E * 2, 0x00020000);
    auto acc_vec = [&](int i) __attribute__((always_inline)) -> u32x4 {
        const uint32_t a = A_ACC + (uint32_t)(i >> 4) * 256u + (uint32_t)(i & 15) * 8u;
        const u32x2 x = *(const LDS u32x2*)(uintptr_t)a, y = *(const LDS u32x2*)(uintptr_t)(a + 128u);
        const u32x4 v = {x.x, x.y, y.x, y.y};
        return v;
    };
    auto edge0 = [&](int sh) __attribute__((always_inline)) -> uint32_t {
        return (tid < SHARE / 4) ? (uint32_t)(((sh * (SHARE / 64) + (tid >> 4)) << 11) | (j << 6) | ((tid & 15) * 2)) : 0x0fffff00u;
    };
    auto load_m = [&](int sh) __attribute__((always_inline)) {
        em[0] = __builtin_amdgcn_raw_buffer_load_b128(rsE, edge0(sh) * 8u, 0, AUX_NT);
        em[1] = __builtin_amdgcn_raw_buffer_load_b128(rsE, edge0(sh) * 8u + 256u, 0, AUX_NT);
    };
    auto load_d = [&](int sh) __attribute__((always_inline)) {
        dw2.x = __builtin_amdgcn_raw_buffer_load_b32(rsD, edge0(sh) * 2u, 0, 0);
        dw2.y = __builtin_amdgcn_raw_buffer_load_b32(rsD, edge0(sh) * 2u + 64u, 0, 0);
    };
    auto load_wl = [&]() __attribute__((always_inline)) {
        const u64 p0 = (u64)wsbf, p1 = (u64)wrbf, p2 = (u64)centers, p3 = (u64)wup;
        const u64 pa = lane < 16 ? p0 : p1, pb = lane < 40 ? p2 : p3;
        wl = ((const GAS uint32_t*)(lane < 32 ? pa : pb))[lane & (lane < 32 ? 15 : lane < 40 ? 3 : 7)];
    };
    auto factor = [&](int r, bool first) __attribute__((always_inline)) {
        const float sq = 3.7982826f;
        const f32x2 d01 = {bf2f(dw2.x & 0xffffu), bf2f(dw2.x >> 16)}, d23 = {bf2f(dw2.y & 0xffffu), bf2f(dw2.y >> 16)};
        const uint32_t cw = __builtin_amdgcn_readlane(wl, 32 + (r >> 1));
        const float cn = bf2f((r & 1) ? (cw >> 16) : (cw & 0xffffu));
        f32x2 w[4];
#pragma unroll
        for (int k = 0; k < 4; k++) {
            const uint32_t x = __builtin_amdgcn_readlane(wl, (k >> 1) * 16 + r * 2 + (k & 1));
            w[k].x = bf2f(x & 0xffffu); w[k].y = bf2f(x >> 16);
        }
        const f32x2 y01 = (d01 - cn) * sq, y23 = (d23 - cn) * sq;
        const f32x2 t01 = -y01 * y01, t23 = -y23 * y23;
        const float rb[4] = {__builtin_amdgcn_exp2f(t01.x), __builtin_amdgcn_exp2f(t01.y), __builtin_amdgcn_exp2f(t23.x), __builtin_amdgcn_exp2f(t23.y)};
#pragma unroll
        for (int q = 0; q < 4; q++) {
#pragma unroll
            for (int k = 0; k < 4; k++) fa[q][k] = first ? w[k] * rb[q] : __builtin_elementwise_fma(w[k], (f32x2)(rb[q]), fa[q][k]);
        }
    };
    auto edges = [&](int sh) __attribute__((always_inline)) {
        load_d(sh);
        load_m(sh);
        factor(0, true);
#pragma unroll 1
        for (int r = 1; r < 8; r++) factor(r, false);
    };
#define ADT(V, O) "ds_write_addtid_b32 " V " offset:" #O "\n\t"
    auto store_md = [&]() __attribute__((always_inline)) {
        asm volatile("s_mov_b32 m0, %12\n\ts_nop 0\n\t" ADT("%0", 0) ADT("%1", 256) ADT("%2", 512) ADT("%3", 768) ADT("%4", 16384) ADT("%5", 16640) ADT("%6", 16896) ADT("%7", 17152)
                     ADT("%8", 32768) ADT("%9", 33024) ADT("%10", 33280) ADT("%11", 33536)
                     :: "v"(pm[0][0]), "v"(pm[0][1]), "v"(pm[0][2]), "v"(pm[0][3]), "v"(pm[1][0]), "v"(pm[1][1]), "v"(pm[1][2]), "v"(pm[1][3]),
                        "v"(pm[2][0]), "v"(pm[2][1]), "v"(pm[2][2]), "v"(pm[2][3]), "s"(wv * 1024) : "memory");
        if (wv == 0) asm volatile("s_mov_b32 m0, 0\n\ts_nop 0\n\t" ADT("%0", 49152) ADT("%1", 49408) ADT("%2", 49664) ADT("%3", 49920)
                                  :: "v"(pm[3][0]), "v"(pm[3][1]), "v"(pm[3][2]), "v"(pm[3][3]) : "memory");
    };
    const int me = h * 2 + cp;
    GAS unsigned* dw = (GAS unsigned*)(meta + M_DONE + j * 64);
    const bool fin = wv < (SHARE / 4 + 63) / 64;
    bool fast, more;
    uint32_t need, mg0, mg1;
    uint32_t guard = 0;
    uint32_t sgn = 0x8000u;
    pin(sgn);

#define GAT3(M) { uint32_t a0_, a1_, a2_; asm volatile( \
        "v_lshlrev_b16 %0, 2, %3\n\tv_lshlrev_b16 %1, 2, %4\n\tv_lshlrev_b16 %2, 2, %5\n\t" \
        "ds_read_b32 v[%6], %0\n\tds_read_b32 v[%6+1], %1\n\tds_read_b32 v[%6+2], %2" \
        : "=&v"(a0_), "=&v"(a1_), "=&v"(a2_) : "v"(w0), "v"(w1), "v"(w2), "n"(M) : "memory"); }
#define MIXL " op_sel_hi:[1,1,0]\n\t"
#define MIXH " op_sel:[0,1,0] op_sel_hi:[1,1,0]\n\t"
#define SDW " dst_sel:DWORD dst_unused:UNUSED_PAD src0_sel:DWORD src1_sel:WORD_1\n\t"
#define FOLD "v_bitop3_b32 %6, %6, v[%15-9], v[%15-8] bitop3:0xfe\n\tv_or_b32 %6, v[%15-7], %6\n\t"
#define DADD(R) "ds_add_rtn_u32 " R
#define SCAT(PRE, POST, M) { uint32_t c0_, c1_, c2_, a0_, a1_, a2_; asm volatile( \
        FOLD "v_and_b32 %0, 0x3ff, %7\n\tv_lshrrev_b32 %2, 22, %7\n\t" PRE \
        "v_bitop3_b32 %0, %11, %8, %0 bitop3:0xea\n\tv_bitop3_b32 %2, %11, %10, %2 bitop3:0xea\n\tv_bfe_u32 %1, %7, 11, 10\n\t" \
        "v_fma_mix_f32 %3, %0, v[%15], %12" MIXL "v_bitop3_b32 %1, %11, %9, %1 bitop3:0xea\n\tv_fma_mix_f32 %0, %0, v[%15], %13" MIXH \
        "v_fma_mix_f32 %4, %1, v[%15+1], %12" MIXL "v_lshl_add_u32 %0, %0, 16, %3\n\tv_lshlrev_b32_sdwa %3, 2, %8" SDW \
        "v_fma_mix_f32 %1, %1, v[%15+1], %13" MIXH DADD("v[%15-9], ") "%3, %0 offset:%14\n\t" \
        "v_fma_mix_f32 %5, %2, v[%15+2], %12" MIXL "v_lshl_add_u32 %1, %1, 16, %4\n\tv_lshlrev_b32_sdwa %4, 2, %9" SDW \
        "v_fma_mix_f32 %2, %2, v[%15+2], %13" MIXH DADD("v[%15-8], ") "%4, %1 offset:%14\n\t" \
        "v_lshl_add_u32 %2, %2, 16, %5\n\tv_lshlrev_b32_sdwa %5, 2, %10" SDW \
        DADD("v[%15-7], ") "%5, %2 offset:%14" POST \
        : "=&v"(c0_), "=&v"(c1_), "=&v"(c2_), "=&v"(a0_), "=&v"(a1_), "=&v"(a2_), "+v"(guard) \
        : "v"(xw), "v"(w0), "v"(w1), "v"(w2), "v"(sgn), "s"(mg0), "s"(mg1), "n"(A_ACC), "n"(M), "s"(tm) : "memory"); }
    asm volatile("v_mov_b32 v110, 0\n\tv_mov_b32 v111, 0\n\tv_mov_b32 v112, 0\n\tv_mov_b32 v113, 0\n\tv_mov_b32 v114, 0\n\tv_mov_b32 v115, 0\n\t"
                 "v_mov_b32 v116, 0\n\tv_mov_b32 v117, 0\n\tv_mov_b32 v118, 0" ::: "v106", "v127");
    auto unmix = [&](int i) __attribute__((always_inline)) {
        const auto s0 = __builtin_amdgcn_permlane16_swap(ring[i][0], ring[i][2], false, false);
        const auto s1 = __builtin_amdgcn_permlane16_swap(ring[i][1], ring[i][3], false, false);
        ring[i][0] = s0[0]; ring[i][2] = s0[1]; ring[i][1] = s1[0]; ring[i][3] = s1[1];
    };
    auto word = [&](int i, int k, bool pr) __attribute__((always_inline)) -> uint32_t {
        return pr && i < NFAST && (i >> 1) != HP ? ring[(i & ~1) | (k >> 1)][(i & 1) * 2 + (k & 1)] : ring[i][k];
    };
    auto gather = [&](int R, int i, int p, bool pr) __attribute__((always_inline)) {
        if ((i >> 1) == HP) unmix(i);
        const uint32_t w0 = word(i, 0 ^ R, pr), w1 = word(i, 1 ^ R, pr), w2 = word(i, 2 ^ R, pr);
        if (ABL & 16) { asm volatile("" :: "v"(w0), "v"(w1), "v"(w2)); return; }
        if (p == 0) GAT3(119) else if (p == 1) GAT3(122) else GAT3(125)
    };
    auto scatter = [&](int R, int i, int p, u64 tm, bool pr) __attribute__((always_inline)) {
        const uint32_t xw = word(i, 3 ^ R, pr), w0 = word(i, 0 ^ R, pr), w1 = word(i, 1 ^ R, pr), w2 = word(i, 2 ^ R, pr);
        if (ABL & 16) { asm volatile("" :: "v"(w0), "v"(w1), "v"(w2), "v"(xw)); return; }
        if (i < NFAST) { if (p == 0) SCAT("", "", 119) else if (p == 1) SCAT("", "", 122) else SCAT("", "", 125) }
        else if (p == 0) SCAT("s_mov_b64 exec, %16\n\t", "\n\ts_mov_b64 exec, -1", 119)
        else SCAT("s_mov_b64 exec, %16\n\t", "\n\ts_mov_b64 exec, -1", 125)
    };
#define WAIT_L(N) asm volatile("s_waitcnt lgkmcnt(" #N ")" ::: "memory")
    int ntn;
    u64 tm0, tm1;
    uint32_t ta[2], sp = sp0;
    auto list1 = [&](int ff) __attribute__((always_inline)) {
        int dst;
        uint32_t en;
        ntn = tail_rank(tc.x, sp, ff, 0u, dst, en);
        asm volatile("ds_permute_b32 v106, %0, %1" :: "v"(dst), "v"(en) : "memory");
        if (__builtin_expect(__builtin_amdgcn_ballot_w64((tc.x & 2u) != 0u) != 0ull, 0)) {
            const int n2 = tail_rank(tc.x, sp, ff, 4u, dst, en);
            if (ntn + n2 <= 32) {
                const int r = ((dst >> 4) | ((dst & 12) << 2)) + ntn;
                const int x = __builtin_amdgcn_ds_permute((en & 7u) ? ((r & 15) << 4) | ((r >> 4) << 2) : 252, (en & 7u) ? (int)en : 0);
                asm volatile("v_cmpx_ne_u32 vcc, 0, %0\n\tv_mov_b32 v106, %0\n\ts_mov_b64 exec, -1\n\ts_nop 4" :: "v"(x) : "vcc");
                ntn += n2;
            } else ntn = 64;
        }
    };
    auto step = [&](auto RR, auto NN, auto NC, int ff) __attribute__((always_inline)) {
        constexpr int R = decltype(RR)::value;
        constexpr bool nt = decltype(NN)::value == 1, last = decltype(NN)::value == 2, nc = decltype(NC)::value == 1, first = decltype(NC)::value == 2;
        constexpr bool pr = true;
        constexpr int mdp = first ? MDF : (nt || nc) ? MDQ : MDP;
        gather(R, 0, 0, pr);
        if (!first && !V1) scatter(R, NFAST, 2, tm0, pr);
        const int ntl = ntn;
        asm volatile("s_nop 1\n\tv_mov_b32_dpp %0, v106 quad_perm:[0,0,0,0] row_mask:0xf bank_mask:0xf\n\tv_mov_b32_dpp %1, v106 quad_perm:[1,1,1,1] row_mask:0xf bank_mask:0xf" : "=v"(ta[0]), "=v"(ta[1]));
        auto side = [&](int pl) __attribute__((always_inline)) {
            if (first && PRE1 <= 4 && pl == ((mdp >> 20) & 15)) { ring[4] = load_main(4, ff); ring[5] = load_main(5, ff); }
            if (first && PRE1 <= 6 && pl == ((mdp >> 24) & 15)) { ring[6] = load_main(6, ff); ring[7] = load_main(7, ff); }
            if (pl == ((mdp >> 12) & 15)) ring[NFAST] = load_tail(ta[0], tm0);
            if (pl == ((mdp >> 16) & 15)) ring[NFAST + 1] = load_tail(ta[1], tm1);
            __builtin_amdgcn_sched_barrier(0);
            if (!last) load_md(ff + 1, pl, mdp);
            else if (pl == MPOS) load_m(me);
            if (last && pl == POLLP && wv == 15) { GAS unsigned* d_ = dw; asm volatile("s_nop 4\n\tglobal_load_dword v108, %0, %1 sc1" :: "v"(vconst<0>()), "s"(d_) : "memory"); }
            if (!first && !last && pl == 5 && ff == 6) { load_d(me); load_wl(); if (MPOS == 13) load_m(me); }
            if (MPOS != 13 && last && pl < 7 && (FALL || fin)) factor(pl, pl == 0);
            if (MPOS == 13 && last && pl > 0 && pl < 7 && fin) { factor(pl - 1, pl == 1); if (pl == FDBL) factor(6, false); }
            __builtin_amdgcn_sched_barrier(0);
        };
        side(8);
        gather(R, 1, 1, pr);
        side(9);
        const uint32_t tcur = tc.x, spc = sp;
#pragma unroll
        for (int i = 0; i < NFAST - 1; i++) {
            gather(R, i + 2, (i + 2) % 3, pr);
            if (i == 0) side(10);
            if (i == 0 && (first || V1)) WAIT_L(6); else if (i < 2) WAIT_L(9); else WAIT_L(12);
            scatter(R, i, i % 3, 0ull, pr);
            if (!last && pr && (i & 1)) ring[i - 1] = load_main(i - 1, ff + 1);
            if (!last && (!pr || (i & 1))) ring[i] = load_main(i, ff + 1);
            side(i);
            if (APRIO && i == (APRIO & 15)) __builtin_amdgcn_s_setprio(2);
            if (APRIO && i == ((APRIO >> 4) & 15)) __builtin_amdgcn_s_setprio(1);
            if (APRIO && i == ((APRIO >> 8) & 15)) __builtin_amdgcn_s_setprio(0);
            if (i == 0 && !last) {
                tc.x = __builtin_amdgcn_alignbit(tc.y, tc.x, 8u); tc.y = __builtin_amdgcn_alignbit(tc.z, tc.y, 8u);
                tc.z = __builtin_amdgcn_alignbit(tc.w, tc.z, 8u); tc.w >>= 8;
                sp = ff == 7 ? sp0 + NWG * 9u * 128u : sp + ((tcur & 0x3cu) << 2);
                list1(ff + 1);
            }
            __builtin_amdgcn_sched_barrier(0);
        }
        if (last) asm volatile("" :: "v"(ring[NFAST + 1]));
        if (__builtin_expect(ntl > 16, 0)) {
            gather(R, NFAST + 1, 0, pr);
            WAIT_L(0);
            scatter(R, NFAST + 1, 0, tm1, pr);
        }
        if (__builtin_expect(ntl > 32, 0)) {
#pragma unroll 1
            for (int e = 2; e < 6; e++) {
                int dst;
                uint32_t en;
                if (tail_rank(tcur, spc, ff, (uint32_t)e & 4u, dst, en) <= (e & 3) * 16) continue;
                const int cmp = __builtin_amdgcn_ds_permute(dst, (int)en);
                ring[NFAST + 1] = load_tail((uint32_t)__builtin_amdgcn_ds_bpermute(((lane & ~3) | (e & 3)) << 2, cmp), tm1);
                gather(R, NFAST + 1, 0, pr);
                WAIT_L(0);
                scatter(R, NFAST + 1, 0, tm1, pr);
            }
        }
        if (APRIO) __builtin_amdgcn_s_setprio(3);
        WAIT_L(3);
        ST(1)
        if (!last) {
            if (!(ABL & 128)) __builtin_amdgcn_s_barrier();
            ST(3)
            if (!(ABL & 512)) store_md();
            if (V1 & 32) asm volatile("s_mov_b64 exec, 1\n\tds_add_u32 %0, %1\n\ts_mov_b64 exec, -1" :: "v"(vconst<A_FLAG + 16>()), "v"(vconst<1>()) : "memory");
            if (V1 & 2) __builtin_amdgcn_s_setprio(0);
            if (V1 & 4) WAIT_L(0);
        }
        scatter(R, NFAST - 1, 1, 0ull, pr);
        if (!last) {
            if (pr) ring[NFAST - 2] = load_main(NFAST - 2, ff + 1);
            ring[NFAST - 1] = load_main(NFAST - 1, ff + 1);
            if (V1 & 8) {
                uint32_t t0;
                if (!(V1 & 32)) asm volatile("s_waitcnt lgkmcnt(3)\n\ts_mov_b64 exec, 1\n\tds_add_u32 %0, %1\n\ts_mov_b64 exec, -1" :: "v"(vconst<A_FLAG + 16>()), "v"(vconst<1>()) : "memory");
                if (V1 & 16) asm volatile("ds_read_b32 v107, %0" :: "v"(vconst<A_FLAG + 16>()) : "memory");
                scatter(R, NFAST, 2, tm0, pr);
                if (V1 & 16) asm volatile("s_waitcnt lgkmcnt(3)\n\tv_readfirstlane_b32 %0, v107\n\ts_cmp_ge_u32 %0, %2\n\ts_cbranch_scc1 2f\n"
                             "1:\n\tds_read_b32 v107, %1\n\ts_waitcnt lgkmcnt(0)\n\tv_readfirstlane_b32 %0, v107\n\ts_cmp_lt_u32 %0, %2\n\ts_cbranch_scc1 1b\n2:"
                             : "=&s"(t0) : "v"(vconst<A_FLAG + 16>()), "s"(ff * 16 + 16) : "memory", "scc");
                else asm volatile("1:\n\tds_read_b32 v107, %1\n\ts_waitcnt lgkmcnt(0)\n\tv_readfirstlane_b32 %0, v107\n\ts_cmp_lt_u32 %0, %2\n\ts_cbranch_scc1 1b"
                             : "=&s"(t0) : "v"(vconst<A_FLAG + 16>()), "s"(ff * 16 + 16) : "memory", "scc");
            } else {
                if (V1) { scatter(R, NFAST, 2, tm0, pr); WAIT_L(6); }
                else WAIT_L(3);
                if (!(ABL & 256)) __builtin_amdgcn_s_barrier();
            }
            if (V1 & 2) __builtin_amdgcn_s_setprio(3);
            ST(4)
        }
    };
    auto steps = [&](auto RR) __attribute__((always_inline)) {
        constexpr int R = decltype(RR)::value;
        tc = tcnt[(wv * 16 + (lane >> 2)) * 128 + j * 4 + h];
        u32x4 sv[2];
        uint32_t e0;
        if (wv < 4) {
            sv[0] = (u32x4)(0u); sv[1] = sv[0];
            if (lane < SLOTI / 256) { sv[0] = stat[tid * 2]; sv[1] = stat[tid * 2 + 1]; }
            e0 = meta[M_E0 + wv];
        }
        const u32x2 extra = *(const u32x2*)(meta + M_LOG);
        load_md(0, -1, 0);
        {
            const u32x4 bi = {BIAS, BIAS, BIAS, BIAS};
#pragma unroll
            for (int k = 0; k < 6; k++) *(LDS u32x4*)(uintptr_t)(A_ACC + (k * NT + threadIdx.x) * 16) = bi;
            if (threadIdx.x < 128) *(LDS u32x4*)(uintptr_t)(A_ACC + (6 * NT + threadIdx.x) * 16) = bi;
            __builtin_amdgcn_sched_barrier(0);
        }
#pragma unroll
        for (int i = 0; i < PRE0; i++) ring[i] = load_main(i, 0);
        __builtin_amdgcn_sched_barrier(0);
        list1(0);
        if (wv < 4) {
            const uint32_t mxb = wave_max(max(max(sv[0].x & 0xffffu, sv[0].z & 0xffffu), max(sv[1].x & 0xffffu, sv[1].z & 0xffffu)));
            const uint32_t mnb = 0xffffu - (wave_max(max(max(sv[0].x, sv[0].z), max(sv[1].x, sv[1].z))) >> 16);
            const float s2 = wave_sum((__uint_as_float(sv[0].y) + __uint_as_float(sv[0].w)) + (__uint_as_float(sv[1].y) + __uint_as_float(sv[1].w)));
            const float rng = fmaxf(sqrtf(zf * s2), 2.0f * bf2f(mxb)) * 6.103515625e-05f;
            const uint32_t eb = mxb ? (__float_as_uint(rng) + 0x7fffffu) >> 23 : 127u;
            const bool ok = !mxb || (mxb < 0x4700u && eb >= 107u && eb <= 140u && mnb >= ((eb + 8u) << 7));
            if (lane == 0) {
                s_flag[8 + wv] = (eb & 0xffu) | (ok ? 0x100u : 0u) | (mxb ? 0x200u : 0u);
                s_flag[12 + wv] = __float_as_uint(__uint_as_float(((eb + e0) & 0xffu) << 23) * (1024.0f / 1023.0f));
                s_flag[wv] = 0u;
                s_flag[4 + wv] = 0u;
            }
        }
        store_md();
#pragma unroll
        for (int i = PRE0; i < PRE1; i++) ring[i] = load_main(i, 0);
        WAIT_L(0);
        __syncthreads();
        const uint32_t q0 = __builtin_amdgcn_readfirstlane(s_flag[8 + 2 * cp]), q1 = __builtin_amdgcn_readfirstlane(s_flag[9 + 2 * cp]);
        fast = (q0 & q1 & 0x100u) != 0u;
        need = (q0 >> 9) | ((q1 >> 9) << 1);
        more = (__builtin_amdgcn_readfirstlane(extra.x | extra.y) | (uint32_t)(T & 3)) != 0u;
        mg0 = (((q0 & 0xffu) + 9u) << 23) | 0x400000u;
        mg1 = ((((q1 & 0xffu) + 9u) << 23) | 0x400000u) + ((0u - (mg0 >> 16)) & 0xffffu);
        if (!fast) { __builtin_amdgcn_s_waitcnt(0x0f70); return; }
        ST(0)
        RTS(9)
        step(RR, IC<0>(), IC<2>(), 0);
        int ff = 1;
#pragma unroll 1
        for (; ff < 15; ff++) step(RR, IC<0>(), IC<0>(), ff);
        step(RR, IC<2>(), IC<0>(), 15);
        scatter(R, NFAST, 2, tm0, false);
        if (FALL || fin) factor(7, false);
    };
    auto irregular = [&](auto f) __attribute__((always_inline)) {
        const float fr = bf2f(freq[0]);
        const char* lg = recs + (size_t)LBASE * 128u;
        const uint16_t* tags = (const uint16_t*)(lg + (size_t)lcap * 128u);
        const uint32_t nl = meta[M_LOG], ns = meta[M_LOG + 1];
#pragma unroll 1
        for (uint32_t q = tid; q < nl * 8u; q += NT) {
            const uint32_t b = tags[q >> 3];
            if ((b & 127u) == (uint32_t)(h * 32 + j)) {
                const uint32_t* un = (const uint32_t*)(lg + (size_t)q * 16u);
                const uint32_t xw = un[3 ^ h];
#pragma unroll 1
                for (int k = 0; k < 3; k++) {
                    const uint32_t ww = un[k ^ h];
                    const float c = (float)((xw >> (k * 11)) & 0x3ffu) * (1.0f / 1023.0f);
                    if (!(ww & WNULL)) f(ww >> 16, ((ww & 0x3fffu) << 6) | ((ALT ? ((b >> 7) * 3u) & 15u : b >> 7) << 2) | (uint32_t)h, (ww & 0x8000u) ? -c : c);
                }
            }
        }
#pragma unroll 1
        for (uint32_t q = tid; q < ns + (uint32_t)(T & 3); q += NT) {
            const uint32_t t = q < ns ? ((const uint32_t*)tags)[-1 - (int)q] : (uint32_t)(T & ~3) + (q - ns);
            const uint32_t i = (uint32_t)iin[t], o = (uint32_t)iout[t];
            if (((o >> 6) & 31u) == (uint32_t)j && (i & 3u) == (uint32_t)h) f(((o >> 11) << 6) | (o & 63u), i, cosf(fr * bf2f(ang[t])));
        }
        __builtin_amdgcn_s_waitcnt(0x0f70);
    };

    if (h == 0) steps(IC<0>());
    else if (h == 1) steps(IC<1>());
    else if (h == 2) steps(IC<2>());
    else steps(IC<3>());
    if (fast) {
        if (more) irregular([&](uint32_t ol, uint32_t i, float cf) __attribute__((always_inline)) {
            const uint32_t md = mdc[(cp * 64u + (i & 63u)) * SLOTI + ((i >> 14) << 8) + ((i >> 4) & 0xfcu) + ((i >> 12) & 3u)];
            const float c = cf * (1023.0f / 16777216.0f);
            const uint32_t lo = __float_as_uint(__builtin_fmaf(c, h2f(md & 0xffffu), __uint_as_float(mg0)));
            const uint32_t hi = __float_as_uint(__builtin_fmaf(c, h2f(md >> 16), __uint_as_float(mg1)));
            if (cf == cf) guard |= atomicAdd(&acc[ol], (hi << 16) + lo);
            else guard |= HSLOW;
        });
    }
    volatile LDS unsigned* seen = (volatile LDS unsigned*)(uintptr_t)(A_FLAG + 4);
    if (POLLP == 7 && tid == NT - 64) *seen = __hip_atomic_load(dw, __ATOMIC_RELAXED, __HIP_MEMORY_SCOPE_AGENT) & 0xff00ffu;
    if (POLLP < 7 && fast && wv == 15) asm volatile("s_waitcnt vmcnt(0)\n\tv_and_b32 v108, 0xff00ff, v108\n\tds_write_b32 %0, v108" :: "v"(vconst<A_FLAG + 4>()) : "memory");
    WAIT_L(0);
    asm volatile("v_or3_b32 %0, %0, v110, v111\n\tv_or3_b32 %0, %0, v112, v113\n\tv_or3_b32 %0, %0, v114, v115\n\tv_or3_b32 %0, %0, v116, v117\n\tv_or_b32 %0, v118, %0" : "+v"(guard));
    ST(2)
    __syncthreads();
    RTS(10)
    const __amdgpu_buffer_rsrc_t rsP = __builtin_amdgcn_make_buffer_rsrc((void*)(partial + (size_t)j * 16 * SLOTO), 0, 16 * SLOTO * 4, 0x00020000);
    u32x4 p[8];
    unsigned have = 0u;
    auto fetch = [&](unsigned v, int sh) __attribute__((always_inline)) {
        const uint32_t po = tid < SHARE / 4 ? (uint32_t)(sh * SHARE + tid * 4) * 4u : 0x7fffff00u;
        const unsigned nw = v & ~have;
#pragma unroll
        for (int r = 0; r < 8; r++)
            if ((nw >> (me ^ r)) & 1u)
                p[r] = __builtin_amdgcn_raw_buffer_load_b128(rsP, po, ((v >> (16 + (me ^ r))) & 1u) ? 16 * SLOTO * 4 : (me ^ r) * SLOTO * 4, AUX_SC1);
        have |= v;
    };
    if (fast) {
        uint32_t big = 0u;
        u32x4 v[7];
#pragma unroll
        for (int k = 0; k < 6; k++) v[k] = acc_vec(k * NT + tid);
        v[6] = (u32x4)(BIAS);
        if (wv < 2) v[6] = acc_vec(6 * NT + tid);
#pragma unroll
        for (int k = 0; k < 6; k++) __builtin_amdgcn_raw_buffer_store_b128(v[k], rsP, (uint32_t)(tid * 16 + me * SLOTO * 4 + k * NT * 16), 0, AUX_SC1);
        if (wv < 2) __builtin_amdgcn_raw_buffer_store_b128(v[6], rsP, (uint32_t)(tid * 16 + me * SLOTO * 4 + 6 * NT * 16), 0, AUX_SC1);
        __builtin_amdgcn_sched_barrier(0);
#pragma unroll
        for (int k = 0; k < 28; k++) { guard |= v[k >> 2][k & 3]; big |= v[k >> 2][k & 3] + 0x42004200u; }
        if (guard & HSLOW) s_flag[0] = 1u;
        if (big & 0x7c00u) s_flag[2] = 1u;
        if (big & 0x7c000000u) s_flag[3] = 2u;
        __builtin_amdgcn_sched_barrier(0);
        p[0] = acc_vec(me * (SHARE / 4) + min(tid, SHARE / 4 - 1));
        have = 1u << me;
        if (fin) fetch(__builtin_amdgcn_readfirstlane(*seen), me);
        if (MPOS == 14) load_m(me);
    }
    asm volatile("s_waitcnt vmcnt(0)" ::: "memory");
    __syncthreads();
    RTS(11)
    ST(5)
    const bool slow = (ABL & 1016) ? false : !fast || __builtin_amdgcn_readfirstlane(s_flag[0]) != 0u || (need & ~__builtin_amdgcn_readfirstlane(s_flag[2] | s_flag[3])) != 0u;
    if (slow) {
        float* fs = (float*)acc;
        float w[16];
        load_w(wdown, w);
#pragma unroll 1
        for (int comp = 2 * cp; comp < 2 * cp + 2; comp++) {
            __syncthreads();
            for (int i = tid; i < SLOTO; i += NT) fs[i] = 0.f;
            __syncthreads();
            auto add = [&](uint32_t ol, uint32_t i, float cf) __attribute__((always_inline)) {
                uint32_t rr[4];
                md_of(m[i], w, rr);
                const uint32_t rc = comp == 0 ? rr[0] : comp == 1 ? rr[1] : comp == 2 ? rr[2] : rr[3];
                atomicAdd(&fs[ol], cf * bf2f(rc));
            };
#pragma unroll 1
            for (uint32_t q = tid; q < 16u * NWG * ((uint32_t)ks * 8u + 24u); q += NT) {
                const uint32_t per = (uint32_t)ks * 8u + 24u;
                const uint32_t un = q & 3u, hf = (q % per) >> 2, sb = (q / per) & (NWG - 1u), ff = (q / per) / NWG;
                const uint32_t col = POS(ff) * 128u + (uint32_t)(h * 32 + j);
                u32x2 p0, p1;
                const uint32_t SK = (sb & 0x18u) ? SKM : SKH, SS = (sb & 0x18u) ? SSM : SSH;
                if (hf < 2u * (uint32_t)ks) {
                    const char* ln = recs + (size_t)((col >> 7) * SS + (col & 127u) * SC + (hf >> 1) * SK + BPL(sb)) * 128u + un * 16u + (hf & 1u) * 8u;
                    p0 = *(const u32x2*)ln; p1 = *(const u32x2*)(ln + 64);
                } else {
                    const uint32_t tl = hf - 2u * (uint32_t)ks;
                    const uint8_t* cb = (const uint8_t*)tcnt + (sb * 128u + (uint32_t)(j * 4 + h)) * 16u;
                    const char* ln = recs + (size_t)(TBASE + (tl >> 1) * TPL + col * TST + sb) * 128u + (tl & 1u) * 64u + un * 16u;
                    if (tl < 4u) { if (tl >= (cb[ff] & 3u) * 2u) continue; }
                    else {
                        uint32_t at = (tl - 4u) * 4u + un;
                        if (at >= ((cb[ff] >> 2) & 15u)) continue;
#pragma unroll 1
                        for (uint32_t t = ff & 8u; t < ff; t++) at += (cb[t] >> 2) & 15u;
                        ln = recs + (size_t)(SBASE + (((uint32_t)(h * 32 + j) * 2u + (ff >> 3)) * NWG + sb) * 9u) * 128u + at * 16u;
                    }
                    p0 = *(const u32x2*)ln; p1 = *(const u32x2*)(ln + 8);
                }
                if (h & 2) { const u32x2 t = p0; p0 = p1; p1 = t; }
                const uint32_t ws[4] = {(h & 1) ? p0.y : p0.x, (h & 1) ? p0.x : p0.y, (h & 1) ? p1.y : p1.x, (h & 1) ? p1.x : p1.y};
#pragma unroll
                for (int k = 0; k < 3; k++) {
                    const float c = (float)((ws[3] >> (k * 11)) & 0x3ffu) * (1.0f / 1023.0f);
                    if (!(ws[k] & WNULL)) add(ws[k] >> 16, ((ws[k] & 0x3fffu) << 6) | (ff * 4u + (uint32_t)h), (ws[k] & 0x8000u) ? -c : c);
                }
            }
            if (more) irregular(add);
            __syncthreads();
            for (int i = tid; i < SLOTO / 4; i += NT)
                __builtin_amdgcn_raw_buffer_store_b128(acc_vec(i), rsP, (uint32_t)(((comp & 1) * 8 + me) * SLOTO * 4 + i * 16), 0, AUX_SC1);
        }
        load_wl();
        edges(me);
#pragma unroll
        for (int k = 0; k < 8; k++) p[k] = (u32x4)(0u);
        have = 0u;
        asm volatile("s_waitcnt vmcnt(0)" ::: "memory");
        __syncthreads();
    }
    if (tid == NT - 64) {
        const unsigned inc = (slow ? 0x10001u : 1u) << me;
        unsigned v = __hip_atomic_fetch_add(dw, inc, __ATOMIC_RELAXED, __HIP_MEMORY_SCOPE_AGENT) + inc;
        if ((v & 0xffu) != 0xffu) {
            unsigned spins = 0;
            do {
                *seen = v & 0xff00ffu;
                __builtin_amdgcn_s_sleep(1);
                v = __hip_atomic_load(dw, __ATOMIC_RELAXED, __HIP_MEMORY_SCOPE_AGENT);
                if (++spins > SPINCAP && (v & 0xffu) != 0xffu) {
                    v = __hip_atomic_fetch_add(dw, 256u << me, __ATOMIC_RELAXED, __HIP_MEMORY_SCOPE_AGENT);
                    if ((v & 0xffu) != 0xffu) v = 0x800000ffu;
                }
            } while ((v & 0xffu) != 0xffu);
            v &= 0x80ff00ffu;
        }
        *seen = v;
    }
    if (!fin) return;
    if (MPOS == 15) load_m(me);
    have = __builtin_amdgcn_readfirstlane(have);
    uint32_t un[4];
#pragma unroll
    for (int c = 0; c < 4; c++) un[c] = __builtin_amdgcn_readfirstlane(s_flag[12 + c]);
    unsigned sv;
    {
        const uintptr_t pb = (uintptr_t)(partial + (size_t)j * 16 * SLOTO);
        const u32x4 rsQ = {(uint32_t)pb, (uint32_t)(pb >> 32) & 0xffffu, 16u * SLOTO * 4u, 0x00020000u};
        const uint32_t po = tid < SHARE / 4 ? (uint32_t)(me * SHARE + tid * 4) * 4u : 0x7fffff00u;
        do {
            sv = __builtin_amdgcn_readfirstlane(*seen);
            uint32_t t0, t1;
#define FET(R) "s_xor_b32 %8, %12, " #R "\n\ts_bitcmp1_b32 %13, %8\n\ts_cbranch_scc0 1f\n\ts_mul_i32 %9, %8, %15\n\ts_bitcmp1_b32 %14, %8\n\t" \
               "s_cselect_b32 %9, %16, %9\n\tbuffer_load_dwordx4 %" #R ", %10, %11, %9 offen sc1\n1:\n\t"
            asm volatile("s_and_b32 %8, %13, 0xff\n\ts_cbranch_scc0 2f\n\ts_nop 2\n\t" FET(0) FET(1) FET(2) FET(3) FET(4) FET(5) FET(6) FET(7) "\n2:\n\ts_sleep 2\n\ts_waitcnt vmcnt(0)"
                         : "+v"(p[0]), "+v"(p[1]), "+v"(p[2]), "+v"(p[3]), "+v"(p[4]), "+v"(p[5]), "+v"(p[6]), "+v"(p[7]), "=&s"(t0), "=&s"(t1)
                         : "v"(po), "s"(rsQ), "s"(me), "s"((int)sv < 0 ? 0u : sv & ~have), "s"(sv >> 16), "n"(SLOTO * 4), "n"(16 * SLOTO * 4) : "memory", "scc");
            have |= sv;
        } while ((sv & 0xffu) != 0xffu);
    }
    if ((int)sv < 0) return;
    RTS(12)
    ST(6)
    const unsigned flt = (sv >> 16) & 0xffu;
    auto finish = [&](int sh) __attribute__((always_inline)) {
        {
            const int ol = sh * SHARE + tid * 4;
            const uint32_t po = tid < SHARE / 4 ? (uint32_t)ol * 4u : 0x7fffff00u;
            float g[4][4];
            u32x4 a[2] = {p[0] + p[2], p[1] + p[3]}, b[2] = {p[4] + p[6], p[5] + p[7]};
            if (cp) { const u32x4 t = a[0], u = b[0]; a[0] = a[1]; b[0] = b[1]; a[1] = t; b[1] = u; }
#pragma unroll
            for (int c = 0; c < 2; c++) {
                const int nb = 0x10000 - 0x4000 * __builtin_popcount(flt & (0x55u << c));
                const float u0 = __uint_as_float(nb ? un[2 * c] : 0u), u1 = __uint_as_float(nb ? un[2 * c + 1] : 0u);
#pragma unroll
                for (int q = 0; q < 4; q++) {
                    g[q][2 * c] = (float)((int)((a[c][q] & 0xffffu) + (b[c][q] & 0xffffu)) - nb) * u0;
                    g[q][2 * c + 1] = (float)((int)((a[c][q] >> 16) + (b[c][q] >> 16)) - nb) * u1;
                }
            }
            if (__builtin_expect(flt != 0u, 0)) {
#pragma unroll
                for (int c = 0; c < 2; c++) {
#pragma unroll 1
                    for (int k = c; k < 8; k += 2) {
                        if (!((flt >> k) & 1u)) continue;
                        const u32x4 f0 = __builtin_amdgcn_raw_buffer_load_b128(rsP, po, k * SLOTO * 4, AUX_SC1);
                        const u32x4 f1 = __builtin_amdgcn_raw_buffer_load_b128(rsP, po, (8 + k) * SLOTO * 4, AUX_SC1);
#pragma unroll
                        for (int q = 0; q < 4; q++) { g[q][2 * c] += __uint_as_float(f0[q]); g[q][2 * c + 1] += __uint_as_float(f1[q]); }
                    }
                }
            }
            float wu[16];
#pragma unroll
            for (int k = 0; k < 8; k++) {
                const uint32_t x = __builtin_amdgcn_readlane(wl, 40 + k);
                wu[2 * k] = bf2f(x & 0xffffu); wu[2 * k + 1] = bf2f(x >> 16);
            }
            u32x4 ov[2];
#pragma unroll
            for (int q = 0; q < 4; q++) {
#pragma unroll
                for (int k = 0; k < 4; k++) g[q][k] *= fa[q][k >> 1][k & 1];
                const uint32_t m0 = em[q >> 1][(q & 1) * 2], m1 = em[q >> 1][(q & 1) * 2 + 1];
                const float mm[4] = {bf2f(m0 & 0xffff), bf2f(m0 >> 16), bf2f(m1 & 0xffff), bf2f(m1 >> 16)};
                float o[4];
#pragma unroll
                for (int k = 0; k < 4; k++) o[k] = mm[k] + (g[q][0] * wu[k] + g[q][1] * wu[4 + k] + g[q][2] * wu[8 + k] + g[q][3] * wu[12 + k]) * fa[q][2 + (k >> 1)][k & 1];
                ov[q >> 1][(q & 1) * 2] = pk_bf16(o[0], o[1]);
                ov[q >> 1][(q & 1) * 2 + 1] = pk_bf16(o[2], o[3]);
            }
            const __amdgpu_buffer_rsrc_t rsU = __builtin_amdgcn_make_buffer_rsrc((void*)out, 0, E * 8, 0x00020000);
            const uint32_t uo = edge0(sh) * 8u;
            __builtin_amdgcn_raw_buffer_store_b128(ov[0], rsU, uo, 0, OAUX);
            __builtin_amdgcn_raw_buffer_store_b128(ov[1], rsU, uo + 256u, 0, OAUX);
        }
    };
    finish(me);
#pragma unroll 1
    for (unsigned todo = (sv >> 8) & 0xffu; todo; todo &= todo - 1u) {
        const int sh = __builtin_ctz(todo);
        edges(sh);
        have = 0u;
        fetch(sv, sh);
        finish(sh);
    }
    RTS(13)
    ST(7)
    ST_OUT(8)
}

__global__ __launch_bounds__(256) void gen_scatter_kernel(const int* __restrict__ iin, const int* __restrict__ iout,
                                                          const uint16_t* __restrict__ ang, const uint16_t* __restrict__ freq,
                                                          const u32x2* __restrict__ m, const uint16_t* __restrict__ wdown,
                                                          float* __restrict__ agg, int T) {
    float w[16];
    load_w(wdown, w);
    const float fr = bf2f(freq[0]);
    for (long t = (long)blockIdx.x * 256 + threadIdx.x; t < T; t += (long)gridDim.x * 256) {
        const float c = cosf(fr * bf2f(ang[t]));
        uint32_t r[4];
        md_of(m[iin[t]], w, r);
        float* q = agg + (size_t)iout[t] * 4;
#pragma unroll
        for (int k = 0; k < 4; k++) atomicAdd(q + k, c * bf2f(r[k]));
    }
}
__global__ __launch_bounds__(256) void gen_final_kernel(const u32x2* __restrict__ m, const uint16_t* __restrict__ dist,
                                                        const uint16_t* __restrict__ centers, const uint16_t* __restrict__ wrbf,
                                                        const uint16_t* __restrict__ wsbf, const uint16_t* __restrict__ wup,
                                                        const float* __restrict__ agg, u32x2* __restrict__ out, int E) {
    const int e = blockIdx.x * 256 + threadIdx.x;
    if (e >= E) return;
    const float d = bf2f(dist[e]);
    float rs[4] = {0, 0, 0, 0}, rg[4] = {0, 0, 0, 0};
#pragma unroll 1
    for (int r = 0; r < 8; r++) {
        const float x = d - bf2f(centers[r]);
        const float rb = __expf(-10.0f * x * x);
#pragma unroll
        for (int k = 0; k < 4; k++) { rs[k] += rb * bf2f(wsbf[r * 4 + k]); rg[k] += rb * bf2f(wrbf[r * 4 + k]); }
    }
    float wu[16];
    load_w(wup, wu);
    const u32x2 mv = m[e];
    const float mm[4] = {bf2f(mv.x & 0xffff), bf2f(mv.x >> 16), bf2f(mv.y & 0xffff), bf2f(mv.y >> 16)};
    float g[4], o[4];
#pragma unroll
    for (int k = 0; k < 4; k++) g[k] = agg[(size_t)e * 4 + k] * rs[k];
#pragma unroll
    for (int k = 0; k < 4; k++) o[k] = mm[k] + (g[0] * wu[k] + g[1] * wu[4 + k] + g[2] * wu[8 + k] + g[3] * wu[12 + k]) * rg[k];
    const u32x2 ov = {f2bf(o[0]) | (f2bf(o[1]) << 16), f2bf(o[2]) | (f2bf(o[3]) << 16)};
    out[e] = ov;
}

extern "C" void kernel_launch(void* const* d_in, const int* in_sizes, int n_in,
                              void* d_out, int out_size, void* d_ws, size_t ws_size,
                              hipStream_t stream) {
    const u32x2* m = (const u32x2*)d_in[0];
    const int* iin = (const int*)d_in[1];
    const int* iout = (const int*)d_in[2];
    const uint16_t* dist = (const uint16_t*)d_in[3];
    const uint16_t* ang = (const uint16_t*)d_in[4];
    const uint16_t* centers = (const uint16_t*)d_in[5];
    const uint16_t* freq = (const uint16_t*)d_in[6];
    const uint16_t* wrbf = (const uint16_t*)d_in[7];
    const uint16_t* wsbf = (const uint16_t*)d_in[8];
    const uint16_t* wdown = (const uint16_t*)d_in[9];
    const uint16_t* wup = (const uint16_t*)d_in[10];
    const int E = in_sizes[3];
    const int T = in_sizes[1];
    char* ws = (char*)d_ws;
    const int G = T >> 2;
    const int ntile = (G + GR - 1) / GR;
    const int nstat = ntile / NWG - NDYN > 4 ? ntile / NWG - NDYN : 4;
    const int lcap = G / 6 + 2;
    char* recs = ws + W_RECS;
    size_t off = W_RECS;
    const size_t rbytes = (size_t)LBASE * 128 + (size_t)lcap * 130;
    off += rbytes;
    if (E > EMAX || (E & 3) || E < 1 || rbytes > 0x70000000u || off > ws_size) {
        (void)hipMemsetAsync(ws, 0, (size_t)E * 16, stream);
        gen_scatter_kernel<<<4096, 256, 0, stream>>>(iin, iout, ang, freq, m, wdown, (float*)ws, T);
        gen_final_kernel<<<(E + 255) / 256, 256, 0, stream>>>(m, dist, centers, wrbf, wsbf, wup, (const float*)ws, (u32x2*)d_out, E);
        return;
    }
    int ks = T / (NWG * NB * 24);
    if (ks > KMAX) ks = KMAX;
    const float zf = ZSIG * ZSIG * (float)T / (8.0f * (float)E * (float)E);
    (void)hipFuncSetAttribute((const void*)part_kernel, hipFuncAttributeMaxDynamicSharedMemorySize, P_LDS);
    (void)hipFuncSetAttribute((const void*)acc_kernel, hipFuncAttributeMaxDynamicSharedMemorySize, A_LDS);
    part_kernel<<<NWG, NT, P_LDS, stream>>>(iin, iout, ang, freq, m, wdown, recs, T, E, ntile, nstat, ks, lcap);
    if (!(ABL & 7)) acc_kernel<<<NWG, NT, A_LDS, stream>>>(ws, T, E, ks, lcap, zf, 0, centers, wrbf, wsbf, wup, m, dist, (u32x2*)d_out, iin, iout, ang, wdown, freq);
}
```

```cpp
#include <hip/hip_runtime.h>
#include <hip/hip_bf16.h>
#include <stdint.h>
#pragma clang diagnostic ignored "-Winline-asm"

typedef unsigned int u32x4 __attribute__((ext_vector_type(4)));
typedef unsigned int u32x2 __attribute__((ext_vector_type(2)));
typedef unsigned short u16x2 __attribute__((ext_vector_type(2)));
typedef _Float16 f16x2 __attribute__((ext_vector_type(2)));
typedef __bf16 bf16x2 __attribute__((ext_vector_type(2)));
typedef float f32x2 __attribute__((ext_vector_type(2)));
typedef unsigned long long u64;

static __device__ __forceinline__ float bf2f(uint32_t bits16) { return __uint_as_float(bits16 << 16); }
static __device__ __forceinline__ uint32_t f2bf(float f) {
    uint32_t u = __float_as_uint(f);
    if ((u & 0x7fffffffu) > 0x7f800000u) return (u >> 16) | 0x40;
    return (u + 0x7fffu + ((u >> 16) & 1u)) >> 16;
}
static __device__ __forceinline__ float h2f(uint32_t bits16) { return (float)__builtin_bit_cast(_Float16, (unsigned short)bits16); }

#define NB 2048
#define WNULL 0x80000000u
#define CSTEP 0x2aabu
#define CINIT 205u
#define CHV (12u * CSTEP)
#define SLOTO 25088
#define SLOTI 12544
#define EMAX (SLOTO * 32)
#define NT 1024
#define NWG 256
#define KMAX 4
#define NTAIL 4
#define SC 64u
#define SKM (128u * SC + 2u)
#define SSM (4u * SKM + 1u)
#define SSH (128u * SC + 1u)
#define SKH (16u * SSH + 2u)
#define SQ (16u * SSM)
#define MAINL (4u * SQ)
#define BPL(b) ((((b) >> 3) & 3u) * SQ + ((b) & 7u) + (((b) >> 5) << 3))
#define TST 257u
#define TBASE MAINL
#define TPL (NB * TST)
#define SBASE (TBASE + 2u * TPL)
#define LBASE (SBASE + 128u * 2u * NWG * 9u)
#define NDYN 3
#define NEARM 0x55555554u
#define GR 2048
#define M_DONE 0
#define M_TILE (32 * 64)
#define M_LOG (M_TILE + 64)
#define M_MD (M_LOG + 64)
#define M_BEAT (M_MD + 64)
#define M_E0 (M_BEAT + 64)
#define M_WORDS (M_E0 + 64)
#define W_TCNT (M_WORDS * 4)
#define W_STAT (W_TCNT + NWG * NB)
#define W_MDC (W_STAT + 4 * NWG * 8)
#define W_PART (W_MDC + 128 * SLOTI * 4)
#define W_RECS (W_PART + 32 * 16 * SLOTO * 4)
#define AUX_NT 2
#define AUX_SC1 16

static __device__ __forceinline__ uint32_t row_max(uint32_t x) {
    x = max(x, (uint32_t)__builtin_amdgcn_update_dpp(0, x, 0x111, 0xf, 0xf, false));
    x = max(x, (uint32_t)__builtin_amdgcn_update_dpp(0, x, 0x112, 0xf, 0xf, false));
    x = max(x, (uint32_t)__builtin_amdgcn_update_dpp(0, x, 0x114, 0xf, 0xf, false));
    return max(x, (uint32_t)__builtin_amdgcn_update_dpp(0, x, 0x118, 0xf, 0xf, false));
}
static __device__ __forceinline__ uint32_t wave_max(uint32_t x) {
    x = row_max(x);
    x = max(x, (uint32_t)__builtin_amdgcn_update_dpp(0, x, 0x142, 0xa, 0xf, false));
    x = max(x, (uint32_t)__builtin_amdgcn_update_dpp(0, x, 0x143, 0xc, 0xf, false));
    return __builtin_amdgcn_readlane(x, 63);
}
static __device__ __forceinline__ float row_sum(float x) {
    x += __int_as_float(__builtin_amdgcn_update_dpp(0, __float_as_int(x), 0x111, 0xf, 0xf, false));
    x += __int_as_float(__builtin_amdgcn_update_dpp(0, __float_as_int(x), 0x112, 0xf, 0xf, false));
    x += __int_as_float(__builtin_amdgcn_update_dpp(0, __float_as_int(x), 0x114, 0xf, 0xf, false));
    return x + __int_as_float(__builtin_amdgcn_update_dpp(0, __float_as_int(x), 0x118, 0xf, 0xf, false));
}
static __device__ __forceinline__ float wave_sum(float x) {
    x = row_sum(x);
    x += __int_as_float(__builtin_amdgcn_update_dpp(0, __float_as_int(x), 0x142, 0xa, 0xf, false));
    x += __int_as_float(__builtin_amdgcn_update_dpp(0, __float_as_int(x), 0x143, 0xc, 0xf, false));
    return __int_as_float(__builtin_amdgcn_readlane(__float_as_int(x), 63));
}
static __device__ __forceinline__ uint32_t bfi(uint32_t mask, uint32_t a, uint32_t b) {
    return __builtin_amdgcn_bitop3_b32(mask, a, b, 0xca);
}
static __device__ __forceinline__ void opaque(int& x) { asm volatile("" : "+v"(x)); }
template <uint32_t C> static __device__ __forceinline__ uint32_t vconst() {
    uint32_t r;
    asm volatile("v_mov_b32 %0, %1" : "=v"(r) : "n"(C));
    return r;
}
static __device__ __forceinline__ uint32_t pk_bf16(float a, float b) {
    const f32x2 v = {a, b};
    return __builtin_bit_cast(uint32_t, __builtin_convertvector(v, bf16x2));
}
static __device__ __forceinline__ void pin(uint32_t& x) { asm volatile("" : "+v"(x)); }


static __device__ __forceinline__ void md_of(u32x2 v, const float* w, uint32_t* r) {
    float a0 = bf2f(v.x & 0xffff), a1 = bf2f(v.x >> 16), a2 = bf2f(v.y & 0xffff), a3 = bf2f(v.y >> 16);
#pragma unroll
    for (int j = 0; j < 4; j++) r[j] = f2bf(a0 * w[j] + a1 * w[4 + j] + a2 * w[8 + j] + a3 * w[12 + j]);
}
static __device__ __forceinline__ void load_w(const uint16_t* __restrict__ wdown, float* w) {
    u32x4 wv0 = *(const u32x4*)wdown, wv1 = *(const u32x4*)(wdown + 8);
#pragma unroll
    for (int i = 0; i < 4; i++) {
        w[2 * i] = bf2f(wv0[i] & 0xffffu); w[2 * i + 1] = bf2f(wv0[i] >> 16);
        w[8 + 2 * i] = bf2f(wv1[i] & 0xffffu); w[8 + 2 * i + 1] = bf2f(wv1[i] >> 16);
    }
}
#define LDS __attribute__((address_space(3)))
#define P_CNT 0
#define P_FLAG ((NB + 64) * 4)
#define P_LST (P_FLAG + 256)
#define LCAP 32
#define P_TAB (P_LST + 4 * LCAP * 16)
#define P_BUF (P_TAB + NB * 4)
#define P_LDS (P_BUF + NB * 64)
#define PD 2
#define LPOS 0x448763
#define SETP(n) __builtin_amdgcn_s_setprio(n)
#define MDL 3
#define RMAXREV 40.0f
#define HB 88
#define HREG(S, D) (HB + ((S) & 3) * 10 + (((S) >> 2) + (D)) * 2)

template <int N> struct IC { static constexpr int value = N; };
struct Raw { u32x4 o[2], i[2]; u32x2 a[2]; };
static __device__ __forceinline__ uint32_t lds_ld(uint32_t a) { return *(const LDS uint32_t*)(uintptr_t)a; }
static __device__ __forceinline__ void lds_st(uint32_t a, uint32_t v) { *(LDS uint32_t*)(uintptr_t)a = v; }
static __device__ __forceinline__ void lds_or(uint32_t a, uint32_t v) {
    __hip_atomic_fetch_or((LDS uint32_t*)(uintptr_t)a, v, __ATOMIC_RELAXED, __HIP_MEMORY_SCOPE_WORKGROUP);
}
static __device__ __forceinline__ uint32_t lds_add(uint32_t a, uint32_t v) {
    return __hip_atomic_fetch_add((LDS uint32_t*)(uintptr_t)a, v, __ATOMIC_RELAXED, __HIP_MEMORY_SCOPE_WORKGROUP);
}
static __device__ __forceinline__ uint32_t slot_w(uint32_t ca, uint32_t cv) {
    return (ca << 4) | __builtin_amdgcn_bitop3_b32(cv >> 11, ca, 0x3cu, 0x28);
}
static __device__ __forceinline__ uint32_t slot_x2(uint32_t aw, uint32_t ca) {
    return __builtin_amdgcn_bitop3_b32(aw, ca, 0xcu, 0x72);
}
static __device__ __forceinline__ uint32_t slot_x(uint32_t aw) { return slot_x2(aw, aw >> 4); }
template <int R> static __device__ __forceinline__ u32x2 hget() {
    u32x2 d;
    asm volatile("v_mov_b64 %0, v[%1:%2]" : "=v"(d) : "n"(R), "n"(R + 1));
    return d;
}
static __device__ __forceinline__ u32x2 hget_at(int r) {
    u32x2 d;
    asm volatile("s_set_gpr_idx_on %2, gpr_idx(SRC0)\n\ts_nop 1\n\tv_mov_b32 %0, v88\n\tv_mov_b32 %1, v89\n\ts_set_gpr_idx_off"
                 : "=&v"(d.x), "=&v"(d.y) : "s"(r));
    return d;
}
static __device__ __forceinline__ void hset_at(int r, u32x2 d) {
    asm volatile("s_set_gpr_idx_on %2, gpr_idx(DST)\n\ts_nop 1\n\tv_mov_b32 v88, %0\n\tv_mov_b32 v89, %1\n\ts_set_gpr_idx_off"
                 :: "v"(d.x), "v"(d.y), "s"(r));
}

template <int S, int D> static __device__ __forceinline__ uint32_t take(uint32_t V, uint32_t u16, uint32_t la, u32x2 nul) {
    const uint32_t a = __builtin_amdgcn_bitop3_b32(V, u16, 15u, 0x54);
    asm volatile("v_mov_b64 v[%4:%4+1], v[%3:%3+1]\n\tv_cmpx_ne_u32 vcc, -16, %0\n\tds_wrxchg_rtn_b64 v[%4:%4+1], %1, %2 offset:%5\n\ts_mov_b64 exec, -1"
                 :: "v"(a), "v"(la), "v"(nul), "n"(HREG(S, D)), "n"(HREG(S, 1 - D)), "n"(S * 256) : "memory", "vcc");
    return a;
}
template <int S, int W> static __device__ __forceinline__ void put(u32x4 rsA, uint32_t ad, uint32_t pm) {
    asm volatile("s_bitcmp1_b32 %3, %4\n\ts_cbranch_scc0 1f\n\ts_waitcnt lgkmcnt(%5)\n\tbuffer_store_dwordx4 v[%2:%2+3], %0, %1, 0 offen sc1\n"
                 "1:\n\ts_bitcmp1_b32 %3, %4+1\n\ts_cbranch_scc0 2f\n\ts_waitcnt lgkmcnt(%5)\n\tbuffer_store_dwordx4 v[%2:%2+3], %0, %1, 0 offen nt\n\ts_nop 1\n2:"
                 :: "v"(ad), "s"(rsA), "n"(HREG(S, 0)), "s"(pm), "n"(2 * S), "n"(W) : "memory", "scc");
}

__global__ __launch_bounds__(NT) __attribute__((amdgpu_num_vgpr(HB / 2))) void part_kernel(const int* __restrict__ iin, const int* __restrict__ iout,
                                                  const uint16_t* __restrict__ ang, const uint16_t* __restrict__ freq,
                                                  const u32x2* __restrict__ m, const uint16_t* __restrict__ wdown,
                                                  char* __restrict__ recs,
                                                  int T, int E, int ntile, int nstat, int ks, int lcap) {
    const int tid0 = threadIdx.x;
    const int wv = __builtin_amdgcn_readfirstlane(tid0 >> 6);
    const int G = T >> 2;
    const int pos = (int)((blockIdx.x & 7) << 5) | (int)(blockIdx.x >> 3);
    float frr;
    unsigned* meta = (unsigned*)(recs - W_RECS);
    const uint32_t gbase = (LBASE + (uint32_t)lcap) * 128u;
    const uint32_t rbytes = gbase + (uint32_t)lcap * 2u;
    const __amdgpu_buffer_rsrc_t rsR = __builtin_amdgcn_make_buffer_rsrc(recs, 0, (int)rbytes, 0x00020000);
    const u32x4 rsA = {(uint32_t)(uintptr_t)recs, (uint32_t)((uintptr_t)recs >> 32) & 0xffffu, rbytes, 0x00020000u};
    const uint32_t g2 = 2u * (uint32_t)ks;
    const uint32_t far = 1u >> ((blockIdx.x >> 3) & 3u);
    const uint32_t SK = SKM + far * (SKH - SKM), SS = SSM + far * (SSH - SSM), UINC = 0x80000001u + SK * 64u;
    const uint32_t spec0 = gbase - 4u;
    auto fixed_tile = [&](int r) __attribute__((always_inline)) -> int { return r * NWG + ((pos + r * 37) & (NWG - 1)); };
    int tl0 = fixed_tile(0), tl1 = fixed_tile(1), tl2;
    uint32_t tk = 0u;
    const __amdgpu_buffer_rsrc_t rsO = __builtin_amdgcn_make_buffer_rsrc((void*)iout, 0, G * 16, 0x00020000);
    const __amdgpu_buffer_rsrc_t rsI = __builtin_amdgcn_make_buffer_rsrc((void*)iin, 0, G * 16, 0x00020000);
    const __amdgpu_buffer_rsrc_t rsG = __builtin_amdgcn_make_buffer_rsrc((void*)ang, 0, G * 8, 0x00020000);
    uint32_t t8 = (uint32_t)tid0 * 8u;
    auto load_one = [&](Raw& r, int tl, int idx) __attribute__((always_inline)) {
        const int j = idx < 4 ? idx >> 1 : idx & 1;
        const uint32_t g0 = (uint32_t)(tl * GR + j * NT);
        __builtin_amdgcn_sched_barrier(0);
        if (idx >= 4) r.a[j] = __builtin_amdgcn_raw_buffer_load_b64(rsG, t8, g0 * 8u, AUX_NT);
        else if (idx & 1) r.i[j] = __builtin_amdgcn_raw_buffer_load_b128(rsI, t8 + t8, g0 * 16u, AUX_NT);
        else r.o[j] = __builtin_amdgcn_raw_buffer_load_b128(rsO, t8 + t8, g0 * 16u, AUX_NT);
        __builtin_amdgcn_sched_barrier(0);
    };
    auto peek = [&](uint32_t at) __attribute__((always_inline)) -> uint32_t {
        return __hip_atomic_load((unsigned*)((char*)meta + at), __ATOMIC_RELAXED, __HIP_MEMORY_SCOPE_AGENT);
    };
    auto ready = [&]() __attribute__((always_inline)) {
        if (blockIdx.x != 0) {
            const uint32_t x = lds_ld(vconst<P_FLAG + 8>());
            while (peek(vconst<M_BEAT * 4>()) == x) __builtin_amdgcn_s_sleep(8);
        }
    };
    auto beat = [&]() __attribute__((always_inline)) {
        (void)__hip_atomic_fetch_add((unsigned*)((char*)meta + vconst<M_BEAT * 4>()), vconst<1>(), __ATOMIC_RELAXED, __HIP_MEMORY_SCOPE_AGENT);
    };
    auto draw = [&](uint32_t at) __attribute__((always_inline)) -> uint32_t {
        return __hip_atomic_fetch_add((unsigned*)((char*)meta + at), vconst<1>(), __ATOMIC_RELAXED, __HIP_MEMORY_SCOPE_AGENT);
    };
    uint32_t la;
    auto tab_at = [&]() __attribute__((always_inline)) -> uint32_t {
        return P_TAB + bfi(0xffu, bfi(0x3cu, t8 >> 2, t8 << 4), t8);
    };
    u32x2 nul;
    uint32_t U[2];
    {
        const uint32_t t = (uint32_t)tid0;
        la = P_BUF + ((t >> 5) << 12) + ((t & 0x18u) << 3) + ((t & 3u) << 4) + ((t & 4u) << 1);
        const uint32_t q = ((t >> 3) ^ (t >> 1 & 2u)) & 3u;
        nul.x = (0x7u >> q) << 31; nul.y = (0xbu >> q) << 31;
        U[0] = (((t >> 1) & 15u) * SS + ((t & 1u) * 64u + (t >> 5)) * SC + BPL(blockIdx.x)) * 128u + (0x80000000u - SK * 64u) + 8u - g2;
        U[1] = U[0] + 32u * SC * 128u;
    }
    auto log_line = [&](bool lg, uint32_t b) __attribute__((always_inline)) -> uint32_t {
        uint32_t lp = 0u;
        if (lg && (t8 & 0x38u) == 0u) {
            ready();
            lp = draw(vconst<M_LOG * 4>());
            __builtin_amdgcn_raw_buffer_store_b16((uint16_t)b, rsR, gbase + lp * 2u, 0, 0);
        }
        return LBASE + (uint32_t)__builtin_amdgcn_ds_bpermute((int)((t8 >> 1) & 0xe0u), (int)lp);
    };
    auto general = [&](int D, bool all, u64 sm) __attribute__((always_inline)) {
        uint32_t todo = __builtin_amdgcn_readfirstlane((uint32_t)sm | (uint32_t)(sm >> 32));
#pragma unroll 1
        while (todo) {
            const uint32_t S = (uint32_t)__builtin_ctz(todo) >> 1;
            todo &= ~(3u << (2u * S));
            pin(t8); pin(la);
            const uint32_t V = lds_ld(P_TAB + (t8 & ~63u) + S * 4u), g = V & 15u;
            const uint32_t col = S * 128u + ((t8 >> 1) & 0x60u) + (t8 >> 8);
            const bool act = all ? V != 0xffffffffu : (V & 0x80000009u) == 9u;
            const int hr = HREG((int)S, D) - HB;
            uint32_t ln = log_line(act && g > 8u + NTAIL, col);
            if (act) {
                const u32x2 N = *(const LDS u32x2*)(uintptr_t)(la + S * 256u);
                *(LDS u32x2*)(uintptr_t)(la + S * 256u) = nul;
                lds_st(P_TAB + (t8 & ~63u) + S * 4u, vconst<0xffffffffu>());
                if (!(g & 1u)) hset_at(hr, N);
                else {
                    uint32_t a, st;
                    if (g < 8u) { a = (V & ~15u) + ((t8 & 0x38u) << 1); st = 8u; }
                    else {
                        if (g <= 8u + NTAIL) ln = TBASE + ((g - 8u) >> 1) * TPL + col * TST + blockIdx.x;
                        a = ln * 128u + (la & 0x38u); st = 64u;
                    }
                    __builtin_amdgcn_raw_buffer_store_b64(hget_at(hr), rsR, a, 0, AUX_SC1);
                    __builtin_amdgcn_raw_buffer_store_b64(N, rsR, a + st, 0, AUX_SC1);
                }
            }
        }
    };
    unsigned epoch = 0;
    u64 pb2 = 0ull;
    u64 fm = 0ull, sm = 0ull;
    auto look = [&](u32x2 c) __attribute__((always_inline)) {
        uint32_t V[2];
        const bool late = __builtin_amdgcn_ballot_w64(((U[0] | U[1]) & 8u) != 0u) != 0ull;
        epoch++;
        asm volatile("v_mov_b32 %0, -1\n\tv_mov_b32 %1, -1\n\t"
                     "v_cmpx_le_u32 %6, %7, %2\n\tv_mov_b32 %0, %4\n\tv_add_u32 %4, %9, %4\n\tv_subrev_u32 %2, %8, %2\n\ts_mov_b64 exec, -1\n\t"
                     "v_cmpx_le_u32 vcc, %8, %3\n\tv_mov_b32 %1, %5\n\tv_add_u32 %5, %9, %5\n\tv_subrev_u32 %3, %8, %3\n\t"
                     "s_or_b64 %6, %6, exec\n\ts_mov_b64 exec, -1"
                     : "=&v"(V[0]), "=&v"(V[1]), "+v"(c.x), "+v"(c.y), "+v"(U[0]), "+v"(U[1]), "=&s"(fm) : "s"(CHV), "n"(CHV), "s"(UINC) : "vcc", "scc");
        *(LDS u32x2*)(uintptr_t)(P_CNT + t8) = c;
        pin(t8);
        const uint32_t tw = tab_at();
        lds_st(tw, V[0]);
        lds_st(tw + 64u, V[1]);
        if (wv == 1) lds_st(P_CNT + NB * 4 - 256 + (t8 >> 1), vconst<0x40000000u>());
        const u64 pb = __builtin_amdgcn_ballot_w64((int)(V[0] & V[1]) >= 0);
        pb2 = pb;
        sm = 0ull;
        if (__builtin_expect(late, 0)) {
            sm = __builtin_amdgcn_ballot_w64((V[0] & 0x80000009u) == 9u || (V[1] & 0x80000009u) == 9u);
#pragma unroll
            for (int e = 0; e < 2; e++)
                if ((V[e] & 0x8000000fu) == 9u + NTAIL) U[e] -= 2u * UINC;
        }
    };
    u32x4 Va, Vb;

    Raw raw[PD];
    {
        const int tid = tid0;
        asm volatile("" ::: "v88", "v127");
        const uint16_t fq = freq[0];
        if (tid == 128) tk = peek(vconst<M_BEAT * 4>());
        if (blockIdx.x == 0 && wv == 3 && (tid & 63) < M_BEAT / 64) {
            __hip_atomic_store(meta + (tid & 63) * 64, 0u, __ATOMIC_RELAXED, __HIP_MEMORY_SCOPE_AGENT);
            __hip_atomic_store(meta + (tid & 63) * 64 + 1, 0u, __ATOMIC_RELAXED, __HIP_MEMORY_SCOPE_AGENT);
        }
        const __amdgpu_buffer_rsrc_t rsE = __builtin_amdgcn_make_buffer_rsrc((void*)m, 0, E * 8, 0x00020000);
        u32x2 mv[5];
        mv[4] = __builtin_amdgcn_raw_buffer_load_b64(rsE, (t8 & 511u) * (uint32_t)(E >> 6), 0, 0);
#pragma unroll
        for (int q = 0; q < 4; q++)
            mv[q] = __builtin_amdgcn_raw_buffer_load_b64(rsE, t8 & 511u, ((blockIdx.x >> 2) * 256u + (blockIdx.x & 3u) * 16u + (uint32_t)wv + 64u * q) * 512u, AUX_NT);
#pragma unroll
        for (int d = 0; d < MDL; d++) load_one(raw[1], tl0, d);
        const u32x2 ci = {CINIT, CINIT};
        *(LDS u32x2*)(uintptr_t)(P_CNT + tid * 8) = ci;
        if (tid < 64) lds_st(P_CNT + (NB + tid) * 4, 0x40000000u);
        if (tid < 8 && tid != 2) lds_st(P_FLAG + tid * 4, tid == 0 ? 0xffffffffu : 0u);
        if (tid == 128) lds_st(P_FLAG + 8, tk);
        asm("s_nop 0\n\tv_readfirstlane_b32 %0, %1" : "=s"(frr) : "v"(bf2f(fq) * 0.15915494309189535f));
        const uint32_t lane = (uint32_t)tid & 63u;
        int e0[4];
        if (blockIdx.x < SLOTI / 64) {
            float w[16];
            load_w(wdown, w);
            auto dot = [&](u32x2 v, int c) __attribute__((always_inline)) -> f32x2 {
                const f32x2 w0 = {w[c], w[c + 1]}, w1 = {w[4 + c], w[5 + c]}, w2 = {w[8 + c], w[9 + c]}, w3 = {w[12 + c], w[13 + c]};
                return w0 * bf2f(v.x & 0xffffu) + w1 * bf2f(v.x >> 16) + w2 * bf2f(v.y & 0xffffu) + w3 * bf2f(v.y >> 16);
            };
#pragma unroll
            for (int cp = 0; cp < 2; cp++) {
                const f32x2 sd = dot(mv[4], 2 * cp);
#pragma unroll
                for (int e = 0; e < 2; e++) {
                    const int ex = (int)(wave_max(__float_as_uint(sd[e]) & 0x7fffffffu) >> 23);
                    e0[2 * cp + e] = __builtin_amdgcn_readfirstlane(ex == 0 ? 0 : min(max(ex - 129, -100), 100));
                }
                const f32x2 sc = {__uint_as_float((uint32_t)(127 - e0[2 * cp]) << 23), __uint_as_float((uint32_t)(127 - e0[2 * cp + 1]) << 23)};
                u32x4 o;
                u16x2 mx = {0, 0};
                f32x2 sq = {0.f, 0.f};
#pragma unroll
                for (int q = 0; q < 4; q++) {
                    const f32x2 y = dot(mv[q], 2 * cp) * sc;
                    const uint32_t hb = pk_bf16(y.x, y.y);
                    const f32x2 x = {__uint_as_float(hb << 16), __uint_as_float(hb & 0xffff0000u)};
                    o[q] = __builtin_bit_cast(uint32_t, __builtin_amdgcn_cvt_pkrtz(x.x, x.y));
                    mx = __builtin_elementwise_max(mx, __builtin_bit_cast(u16x2, hb & 0x7fff7fffu));
                    sq += x * x;
                }
                *(LDS u32x4*)(uintptr_t)(P_BUF + (cp * 64u + lane) * 272u + wv * 16) = o;
#pragma unroll
                for (int e = 0; e < 2; e++) {
                    const u32x2 st = {wave_max(mx[e]), __float_as_uint(wave_sum(sq[e]))};
                    if (lane == 0u) *(LDS u32x2*)(uintptr_t)(P_LST + ((2 * cp + e) * 16 + wv) * 8) = st;
                }
            }
        }
#pragma unroll
        for (int d = MDL; d < 6; d++) load_one(raw[1], tl0, d);
#pragma unroll
        for (int d = 0; d < 6; d++)
            if (((LPOS >> (4 * d)) & 15) < 4) load_one(raw[0], tl1, d);
        __syncthreads();
        u32x4 mo[2];
#pragma unroll
        for (int cp = 0; cp < 2; cp++) mo[cp] = *(const LDS u32x4*)(uintptr_t)(P_BUF + (cp * 64u + (t8 >> 7)) * 272u + ((t8 + t8) & 0xf0u));
        if (blockIdx.x == 0 && wv == 3) asm volatile("s_waitcnt vmcnt(0)" ::: "memory");
        __syncthreads();
#pragma unroll
        for (int s = 0; s < 16; s++) *(LDS u32x2*)(uintptr_t)(la + s * 256) = nul;
        asm volatile("" : "+v"(raw[1].o[0]), "+v"(raw[1].o[1]), "+v"(raw[1].i[0]), "+v"(raw[1].i[1]), "+v"(raw[1].a[0]), "+v"(raw[1].a[1]));
        if (blockIdx.x < SLOTI / 64) {
            const __amdgpu_buffer_rsrc_t rsM = __builtin_amdgcn_make_buffer_rsrc((void*)(meta + W_MDC / 4), 0, (int)(128u * SLOTI * 4u), 0x00020000);
#pragma unroll
            for (int cp = 0; cp < 2; cp++) {
                const uint32_t row = cp * 64u + (t8 >> 7), c16 = (t8 + t8) & 0xf0u;
                __builtin_amdgcn_raw_buffer_store_b128(mo[cp], rsM, row * (SLOTI * 4u) + c16 + blockIdx.x * 256u, 0, AUX_SC1);
            }
            if (wv == 0) {
                const u32x2 st = *(const LDS u32x2*)(uintptr_t)(P_LST + t8);
                const u32x2 r = {row_max(st.x) | (row_max(st.x ? 0xffffu - st.x : 0u) << 16), __float_as_uint(row_sum(__uint_as_float(st.y)))};
                if ((lane & 15u) == 15u) ((u32x2*)(meta + W_STAT / 4))[(lane >> 4) * NWG + blockIdx.x] = r;
                if (blockIdx.x == 0 && lane < 4u) meta[M_E0 + lane] = (uint32_t)(lane == 0u ? e0[0] : lane == 1u ? e0[1] : lane == 2u ? e0[2] : e0[3]);
            }
        }
    }
    pin(la);

    uint32_t w[8], aw[8], xv[8];
    u64 nm[8];
    auto later2 = [&](int k) __attribute__((always_inline)) {
        uint32_t x0, x1;
        asm volatile("v_lshrrev_b32 %0, 4, %2\n\tv_lshrrev_b32 %1, 4, %5\n\tv_bitop3_b32 %0, %2, %0, 12 bitop3:0x72\n\tv_bitop3_b32 %1, %5, %1, 12 bitop3:0x72\n\t"
                     "s_not_b64 exec, %8\n\tds_write_b32 %2, %3 offset:%10\n\tds_or_b32 %0, %4 offset:%10\n\t"
                     "s_not_b64 exec, %9\n\tds_write_b32 %5, %6 offset:%10\n\tds_or_b32 %1, %7 offset:%10\n\ts_mov_b64 exec, -1"
                     : "=&v"(x0), "=&v"(x1)
                     : "v"(aw[k]), "v"(w[k]), "v"(xv[k]), "v"(aw[k + 1]), "v"(w[k + 1]), "v"(xv[k + 1]), "s"(nm[k]), "s"(nm[k + 1]), "n"(P_BUF) : "memory", "scc");
    };
    auto drain = [&]() __attribute__((always_inline)) {
        if (wv == 1) {
            const uint32_t q = (epoch + 1u) & 3u;
            const uint32_t n = __builtin_amdgcn_readfirstlane(lds_ld(vconst<P_FLAG + 16>() + q * 4u));
            if (__builtin_expect(n != 0u, 0)) {
                pin(t8);
                const uint32_t l = (t8 >> 3) & 63u;
                if (l < min(n, (uint32_t)LCAP)) {
                    const u32x4 e = *(const LDS u32x4*)(uintptr_t)(P_LST + (q * LCAP + l) * 16u);
                    lds_st(P_BUF + e.x, e.y);
                    lds_or(P_BUF + slot_x(e.x), e.z);
                }
                lds_st(vconst<P_FLAG + 16>() + q * 4u, vconst<0>());
            }
        }
    };
    auto again = [&](int D) __attribute__((always_inline)) {
        do {
            general(D, true, fm);
            __syncthreads();
            later2(0); later2(2); later2(4); later2(6);
            drain();
#pragma unroll
            for (int k = 0; k < 8; k++) {
                pin(aw[k]);
                bool due = false;
                if (aw[k] & 0x80000000u) {
                    xv[k] -= CHV;
                    if (xv[k] < CHV) {
                        const uint32_t f = (aw[k] >> 16) & 0x3ffu;
                        aw[k] = slot_w(aw[k] & 0xffffu, xv[k]);
                        xv[k] = f << ((xv[k] >> 8) & 31u);
                        due = true;
                    } else lds_st(vconst<P_FLAG>(), epoch + 1u);
                }
                nm[k] = ~__builtin_amdgcn_ballot_w64(due);
            }
            look(*(const LDS u32x2*)(uintptr_t)(P_CNT + t8));
            asm volatile("s_waitcnt lgkmcnt(0)" ::: "memory");
            __syncthreads();
        } while (__builtin_amdgcn_readfirstlane(lds_ld(vconst<P_FLAG>())) == epoch);
    };
    auto round = [&](auto DD, auto LL, Raw& rc, Raw& rn, int rd) __attribute__((always_inline)) {
        constexpr int D = decltype(DD)::value;
        constexpr bool LAST = decltype(LL)::value == 1, FIRST = decltype(LL)::value == 2;
        const int ng = min(GR, G - tl0 * GR);
        auto hook = [&](int p) __attribute__((always_inline)) {
#pragma unroll
            for (int q = 0; q < 6; q++)
            {
                const int lp = (LPOS >> (4 * q)) & 15;
                if (!LAST && lp < 4 && lp == p) load_one(rc, tl2, q);
                if (!LAST && lp >= 4 && (lp < 8 || lp >= 12 ? lp : lp - 8) == p) load_one(rn, tl1, q);
            }
        };
        uint32_t wn[8], ca[8], px[4], cv[8];
        float mx;
        float cs[2];
        auto convA = [&](int q) __attribute__((always_inline)) {
            if (LAST) return;
            const uint32_t a = rc.a[q >> 1][q & 1];
            const f32x2 av = {__uint_as_float(a << 16), __uint_as_float(a & 0xffff0000u)};
            f32x2 rv;
            asm("v_pk_mul_f32 %0, %1, %2 op_sel_hi:[1,0]" : "=v"(rv) : "v"(av), "s"((u64)__float_as_uint(frr)));
            mx = __builtin_elementwise_maximum(__builtin_elementwise_maximum(__builtin_fabsf(rv.x), __builtin_fabsf(rv.y)), (q & 1) ? mx : 0.f);
            cs[0] = __builtin_amdgcn_cosf(rv.x); cs[1] = __builtin_amdgcn_cosf(rv.y);
            asm volatile("" : "+v"(mx), "+v"(cs[0]), "+v"(cs[1]));
            __builtin_amdgcn_sched_barrier(0);
        };
        auto convB = [&](int k) __attribute__((always_inline)) {
            if (LAST) return;
            const uint32_t o = rc.o[k >> 2][k & 3], i = rc.i[k >> 2][k & 3];
            wn[k] = (__builtin_amdgcn_alignbit(bfi(63u, o, o >> 5), __float_as_uint(cs[k & 1]), 30u) << 14) | (i >> 6);
            ca[k] = (bfi(63u, i, o) << 2) & 0x1ffcu;
            pin(wn[k]); pin(ca[k]);
            __builtin_amdgcn_sched_barrier(0);
        };
        auto convD = [&](int q) __attribute__((always_inline)) {
            if (LAST) return;
            asm volatile("v_cvt_pk_f16_f32 %0, |%1|, |%2|\n\tv_pk_mul_f16 %0, %0, %3 op_sel_hi:[1,0]" : "=v"(px[q]) : "v"(cs[0]), "v"(cs[1]), "s"(0x3ffu));
        };
        uint32_t cst;
        auto check = [&](int j) __attribute__((always_inline)) {
            if (LAST) return;
            if (__builtin_expect((__builtin_amdgcn_ballot_w64(!(mx <= RMAXREV)) | (u64)(uint32_t)(GR - ng)) != 0ull, 0)) {
                pin(t8);
                asm volatile("" : "+v"(rc.a[j]));
                const int lg = j * NT + (int)(t8 >> 3);
#pragma unroll
                for (int k = 0; k < 4; k++) {
                    const uint32_t a = (k & 1) ? (rc.a[j][k >> 1] >> 16) : (rc.a[j][k >> 1] & 0xffffu);
                    const bool nrm = __builtin_fabsf(frr * bf2f(a)) <= RMAXREV;
                    if (!(lg < ng && nrm)) ca[j * 4 + k] = NB * 4u + ((t8 >> 1) & 0xfcu);
                    if (lg < ng && !nrm) {
                        ready();
                        __builtin_amdgcn_raw_buffer_store_b32((uint32_t)((tl0 * GR + lg) * 4 + k), rsR, spec0 - draw(vconst<M_LOG * 4 + 4>()) * 4u, 0, 0);
                    }
                }
            }
            cst = vconst<CSTEP>();
        };
        auto ticket = [&](int k) __attribute__((always_inline)) {
            if (LAST) return;
            cv[k] = lds_add(P_CNT + ca[k], cst);
            __builtin_amdgcn_sched_barrier(0);
        };
        auto insert = [&](int j) __attribute__((always_inline)) {
            const int k0 = 4 * j;
            const bool odd = __builtin_amdgcn_ballot_w64(max(max(cv[k0], cv[k0 + 1]), max(cv[k0 + 2], cv[k0 + 3])) >= 2u * CHV) != 0ull;
#pragma unroll
            for (int k = k0; k < k0 + 4; k += 2) {
                uint32_t x0, x1;
                asm volatile("v_lshrrev_b32 %6, 11, %8\n\tv_lshrrev_b32 %7, 11, %9\n\t"
                             "v_bitop3_b32 %6, %6, %10, 60 bitop3:0x28\n\tv_bitop3_b32 %7, %7, %11, 60 bitop3:0x28\n\t"
                             "v_lshl_or_b32 %0, %10, 4, %6\n\tv_lshl_or_b32 %1, %11, 4, %7\n\t"
                             "v_lshlrev_b32_sdwa %2, %8, %12 dst_sel:DWORD dst_unused:UNUSED_PAD src0_sel:BYTE_1 src1_sel:WORD_0\n\t"
                             "v_lshlrev_b32_sdwa %3, %9, %12 dst_sel:DWORD dst_unused:UNUSED_PAD src0_sel:BYTE_1 src1_sel:WORD_1\n\t"
                             "v_bitop3_b32 %6, %0, %10, 12 bitop3:0x72\n\tv_bitop3_b32 %7, %1, %11, 12 bitop3:0x72\n\t"
                             "v_cmpx_gt_u32 %4, %15, %8\n\tds_write_b32 %0, %13 offset:%16\n\tds_or_b32 %6, %2 offset:%16\n\ts_mov_b64 exec, -1\n\t"
                             "v_cmpx_gt_u32 %5, %15, %9\n\tds_write_b32 %1, %14 offset:%16\n\tds_or_b32 %7, %3 offset:%16\n\ts_mov_b64 exec, -1"
                             : "=&v"(aw[k]), "=&v"(aw[k + 1]), "=&v"(xv[k]), "=&v"(xv[k + 1]), "=&s"(nm[k]), "=&s"(nm[k + 1]), "=&v"(x0), "=&v"(x1)
                             : "v"(cv[k]), "v"(cv[k + 1]), "v"(ca[k]), "v"(ca[k + 1]), "v"(px[k >> 1]), "v"(wn[k]), "v"(wn[k + 1]), "s"(CHV), "n"(P_BUF) : "memory");
                w[k] = wn[k]; w[k + 1] = wn[k + 1];
                if (__builtin_expect(odd, 0)) {
#pragma unroll
                    for (int q = k; q < k + 2; q++) {
                        nm[q] |= __builtin_amdgcn_ballot_w64(cv[q] >= 2u * CHV);
                        if (cv[q] >= 2u * CHV && cv[q] < 0x40000000u) {
                            const uint32_t lq = (epoch + 2u) & 3u;
                            uint32_t p = LCAP;
                            if (cv[q] < 3u * CHV) p = lds_add(vconst<P_FLAG + 16>() + lq * 4u, vconst<1>());
                            if (p < LCAP) {
                                const u32x2 e = {aw[q], w[q]};
                                *(LDS u32x2*)(uintptr_t)(P_LST + (lq * LCAP + p) * 16u) = e;
                                lds_st(P_LST + (lq * LCAP + p) * 16u + 8u, xv[q]);
                            } else {
                                aw[q] = ca[q] | ((q & 1) ? (px[q >> 1] & 0xffff0000u) : (px[q >> 1] << 16)) | 0x80000000u;
                                xv[q] = cv[q] - CHV;
                                lds_st(vconst<P_FLAG>(), epoch);
                            }
                        }
                    }
                }
            }
        };
        constexpr int P0 = D ? 0 : 3, P1 = D ? 1 : 2, P2 = D ? 2 : 1, P3 = D ? 3 : 0;
        const uint32_t fl = lds_ld(vconst<P_FLAG>());
        if (!LAST) asm volatile("" : "+v"(rc.o[0]), "+v"(rc.o[1]), "+v"(rc.i[0]), "+v"(rc.i[1]), "+v"(rc.a[0]), "+v"(rc.a[1]));
        if (!LAST && (rd + 4 >= nstat || tl1 >= ntile) && wv == 2) {
            pin(tk);
            if ((t8 & 511u) == 0u) {
                if (tl1 >= ntile) {
                    if (rd + 3 <= nstat) ready();
                    (void)draw(vconst<M_MD * 4>());
                } else if (rd + 4 == nstat) tk = peek(vconst<M_BEAT * 4>());
                else {
                    if (rd + 3 == nstat) {
                        if (tk == lds_ld(vconst<P_FLAG + 8>())) ready();
                        tk = 0u;
                    }
                    lds_st(vconst<P_FLAG + 4>(), tk);
                    if ((int)tk < ntile - nstat * NWG) tk = draw(vconst<M_TILE * 4>());
                }
            }
        }
        if (!LAST && blockIdx.x == 0 && wv == 3 && (t8 & 511u) == 0u) beat();
        hook(12);
        pin(t8);
        const uint32_t tr = P_TAB + (t8 & ~63u), u16 = (t8 + t8) & 0x70u;
        convA(0);
        convB(0);
        const bool ag = __builtin_amdgcn_readfirstlane(fl) == epoch;
        if (!FIRST && __builtin_expect(LAST || ag || sm != 0ull, 0)) {
            if (ag) again(D);
            general(D, false, sm);
            Va = *(const LDS u32x4*)(uintptr_t)(P_TAB + (t8 & ~63u) + P0 * 16);
            Vb = *(const LDS u32x4*)(uintptr_t)(P_TAB + (t8 & ~63u) + P1 * 16);
            asm volatile("" : "+v"(Va), "+v"(Vb));
        }
        uint32_t pq = (uint32_t)pb2 | (uint32_t)(pb2 >> 32);
        pq = (pq | (pq >> 1)) & 0x55555555u;
        pq = __builtin_amdgcn_readfirstlane(pq + (pq & (far * NEARM)));
        uint32_t ad[8];
        if (!FIRST) ad[0] = take<4 * P0 + 0, D>(Va[0], u16, la, nul);
        convB(1);
        if (!FIRST) ad[1] = take<4 * P0 + 1, D>(Va[1], u16, la, nul);
        convD(0);
        convA(1);
        if (!FIRST) ad[2] = take<4 * P0 + 2, D>(Va[2], u16, la, nul);
        convB(2);
        hook(13);
        if (!FIRST) ad[3] = take<4 * P0 + 3, D>(Va[3], u16, la, nul);
        convB(3);
        if (!FIRST) put<4 * P0 + 0, LAST ? 3 : 3>(rsA, ad[0], pq);
        if (!FIRST) ad[4] = take<4 * P1 + 0, D>(Vb[0], u16, la, nul);
        convD(1);
        check(0);
        ticket(0);
        hook(4);
        if (!FIRST) put<4 * P0 + 1, LAST ? 3 : 4>(rsA, ad[1], pq);
        if (!FIRST) ad[5] = take<4 * P1 + 1, D>(Vb[1], u16, la, nul);
        convA(2);
        ticket(1);
        if (!FIRST) put<4 * P0 + 2, LAST ? 3 : 5>(rsA, ad[2], pq);
        if (!FIRST) ad[6] = take<4 * P1 + 2, D>(Vb[2], u16, la, nul);
        convB(4);
        ticket(2);
        if (!FIRST) put<4 * P0 + 3, LAST ? 3 : 6>(rsA, ad[3], pq);
        if (!FIRST) Va = *(const LDS u32x4*)(uintptr_t)(tr + P2 * 16);
        __builtin_amdgcn_sched_barrier(0);
        if (!FIRST) ad[7] = take<4 * P1 + 3, D>(Vb[3], u16, la, nul);
        convB(5);
        ticket(3);
        SETP(2);
        if (!FIRST) put<4 * P1 + 0, LAST ? 4 : 8>(rsA, ad[4], pq);
        if (!FIRST) ad[0] = take<4 * P2 + 0, D>(Va[0], u16, la, nul);
        convD(2);
        convA(3);
        hook(5);
        if (!FIRST) put<4 * P1 + 1, LAST ? 4 : 7>(rsA, ad[5], pq);
        if (!FIRST) ad[1] = take<4 * P2 + 1, D>(Va[1], u16, la, nul);
        convB(6);
        if (!FIRST) put<4 * P1 + 2, LAST ? 4 : 6>(rsA, ad[6], pq);
        if (!FIRST) ad[2] = take<4 * P2 + 2, D>(Va[2], u16, la, nul);
        convB(7);
        if (!FIRST) put<4 * P1 + 3, LAST ? 3 : 4>(rsA, ad[7], pq);
        if (!FIRST) Vb = *(const LDS u32x4*)(uintptr_t)(tr + P3 * 16);
        __builtin_amdgcn_sched_barrier(0);
        if (!FIRST) ad[3] = take<4 * P2 + 3, D>(Va[3], u16, la, nul);
        convD(3);
        check(1);
        ticket(4);
        SETP(1);
        hook(6);
        if (!FIRST) put<4 * P2 + 0, LAST ? 4 : 5>(rsA, ad[0], pq);
        if (!FIRST) ad[4] = take<4 * P3 + 0, D>(Vb[0], u16, la, nul);
        ticket(5);
        if (!FIRST) put<4 * P2 + 1, LAST ? 4 : 6>(rsA, ad[1], pq);
        if (!FIRST) ad[5] = take<4 * P3 + 1, D>(Vb[1], u16, la, nul);
        ticket(6);
        if (!FIRST) put<4 * P2 + 2, LAST ? 4 : 7>(rsA, ad[2], pq);
        if (!FIRST) ad[6] = take<4 * P3 + 2, D>(Vb[2], u16, la, nul);
        ticket(7);
        SETP(0);
        if (!FIRST) put<4 * P2 + 3, LAST ? 3 : 7>(rsA, ad[3], pq);
        if (!FIRST) ad[7] = take<4 * P3 + 3, D>(Vb[3], u16, la, nul);
        hook(7);
        SETP(3);
        asm volatile("s_waitcnt lgkmcnt(0)" ::: "memory");
        __syncthreads();
        u32x2 cn;
        uint32_t tv;
        if (!LAST) { cn = *(const LDS u32x2*)(uintptr_t)(P_CNT + t8); tv = lds_ld(vconst<P_FLAG + 4>()); }
        __builtin_amdgcn_sched_barrier(0);
        if (!FIRST) later2(0);
        if (!FIRST) put<4 * P3, 15>(rsA, ad[4], pq);
        if (!FIRST) later2(2);
        if (!FIRST) put<4 * P3 + 1, 15>(rsA, ad[5], pq);
        if (!FIRST) drain();
        if (!LAST) {
            tl2 = rd + 2 < nstat ? fixed_tile(rd + 2) : nstat * NWG + (int)__builtin_amdgcn_readfirstlane(tv);
            look(cn);
        }
        hook(0);
        SETP(2);
        if (!FIRST) later2(4);
        if (!FIRST) put<4 * P3 + 2, 15>(rsA, ad[6], pq);
        if (!FIRST) later2(6);
        if (!FIRST) put<4 * P3 + 3, 15>(rsA, ad[7], pq);
        if (!LAST) {
            hook(1);
            SETP(1);
            insert(0);
            hook(2);
            SETP(0);
            pin(t8);
            Va = *(const LDS u32x4*)(uintptr_t)(P_TAB + (t8 & ~63u) + P3 * 16);
            Vb = *(const LDS u32x4*)(uintptr_t)(P_TAB + (t8 & ~63u) + P2 * 16);
            __builtin_amdgcn_sched_barrier(0);
            insert(1);
            hook(3);
            SETP(3);
            asm volatile("s_waitcnt lgkmcnt(0)" ::: "memory");
            __syncthreads();
            tl0 = tl1; tl1 = tl2;
        }
    };
#define HMV(A, B) "v_mov_b64 v[" #A ":" #A "+1], v[" #B ":" #B "+1]\n\t"
#define HMV4(A) HMV(A + 8, A + 6) HMV(A + 6, A + 4) HMV(A + 4, A + 2) HMV(A + 2, A)
    round(IC<1>(), IC<2>(), raw[1], raw[0], 0);
    int rd = 1;
#pragma unroll 1
    for (; tl1 < ntile; rd += 2) {
        round(IC<0>(), IC<0>(), raw[0], raw[1], rd);
        round(IC<1>(), IC<0>(), raw[1], raw[0], rd + 1);
    }
    if (tl0 >= ntile) asm volatile(HMV4(88) HMV4(98) HMV4(108) HMV4(118));
    asm volatile("" : "+s"(tl0));
    if (tl0 < ntile) round(IC<0>(), IC<0>(), raw[0], raw[1], rd);
    round(IC<1>(), IC<1>(), raw[1], raw[0], 0);
    if (__builtin_amdgcn_readfirstlane(lds_ld(P_FLAG + 16 + ((epoch + 2u) & 3u) * 4u)) != 0u) {
        asm volatile("s_waitcnt lgkmcnt(0)" ::: "memory");
        __syncthreads();
        look(*(const LDS u32x2*)(uintptr_t)(P_CNT + t8));
        general(0, true, fm);
        __syncthreads();
        drain();
    }
    {
        uint32_t* tcnt = meta + W_TCNT / 4;
        const u32x2 c = *(const LDS u32x2*)(uintptr_t)(P_CNT + t8);
        const uint32_t tw = tab_at();
#pragma unroll
        for (int e = 0; e < 2; e++) {
            const uint32_t g = U[e] & 15u;
            lds_st(tw + e * 64u, g >= 8u ? 0x80000000u : ((U[e] + ((g & 1u) ? 0u : SK * 64u)) & 0x7fffff81u) | (((7u - g) << 1) & 12u));
            *(LDS uint8_t*)(uintptr_t)(P_CNT + (t8 & ~511u) + (((tw - P_TAB) >> 2) & 127u) + e * 16u) =
                (uint8_t)(g < 8u ? 0u : ((g - 8u) >> 1) + (((c[e] + 2u * CSTEP) >> 15) << 2) + (g & 1u) * 0x50u);
        }
        asm volatile("s_waitcnt lgkmcnt(0)" ::: "memory");
        __syncthreads();
        const uint32_t u16 = (t8 + t8) & 0x70u;
        u32x4 En[4];
#pragma unroll
        for (int p = 0; p < 4; p++) En[p] = *(const LDS u32x4*)(uintptr_t)(P_TAB + (t8 & ~63u) + p * 16u);
        const u32x4 cb = *(const LDS u32x4*)(uintptr_t)(P_CNT + (t8 & ~511u) + ((t8 >> 2) & 0x70u));
        u32x2 N[16];
#pragma unroll
        for (int s = 0; s < 16; s++) N[s] = *(const LDS u32x2*)(uintptr_t)(la + s * 256u);
        if (u16 == 0u) *(u32x4*)(tcnt + blockIdx.x * 512u + (t8 >> 4)) = cb;
        const uint32_t sg = P_BUF + ((t8 & ~63u) << 4);
        uint32_t ps;
        auto rest = [&](auto SL) __attribute__((always_inline)) {
            constexpr int s = decltype(SL)::value;
            const uint32_t en = En[s >> 2][s & 3], by = cb[s >> 2] >> ((s & 3) * 8);
            const u32x2 Hr = hget<HREG(s, 0)>();
            const uint32_t at = ps + ((la & 0x38u) ^ ((s & 3) << 4));
            *(LDS u32x2*)(uintptr_t)at = Hr;
            *(LDS u32x2*)(uintptr_t)(at + (by & 0x40u)) = N[s];
            ps += (by & 0x3cu) << 2;
            const u32x2 Hs = (en & 1u) ? Hr : nul;
            const u32x4 d = {Hs.x, Hs.y, N[s].x, N[s].y};
            uint32_t a = __builtin_amdgcn_bitop3_b32(en, u16, 15u, 0x54);
            {
                const u32x4 rs_ = rsA;
                const uint32_t far_ = ((NEARM >> (2 * s)) & 1u) ? far : 0u;
                asm volatile("s_cmp_eq_u32 %3, 0\n\ts_cbranch_scc0 1f\n\tbuffer_store_dwordx4 %0, %1, %2, 0 offen sc1\n\ts_branch 2f\n"
                             "1:\n\tbuffer_store_dwordx4 %0, %1, %2, 0 offen nt\n2:\n\ts_nop 1" :: "v"(d), "v"(a), "s"(rs_), "s"(far_) : "memory", "scc");
            }
            if (__builtin_expect(__builtin_amdgcn_ballot_w64((en & 12u) != 0u) != 0ull, 0)) {
                const u32x4 dn = {nul.x, nul.y, nul.x, nul.y};
#pragma unroll 1
                for (uint32_t k = (en >> 2) & 3u; k > 0u; k--) {
                    a += SK * 128u;
                    __builtin_amdgcn_raw_buffer_store_b128(dn, rsR, a, 0, AUX_SC1);
                }
            }
        };
        auto pack = [&](int p) __attribute__((always_inline)) {
            const uint32_t a = (SBASE + ((((t8 >> 1) & 0x60u) + (t8 >> 8)) * 2u + p) * (NWG * 9u) + blockIdx.x * 9u) * 128u + u16;
            asm volatile("" ::: "memory");
#pragma unroll 1
            for (uint32_t q = 0; __builtin_amdgcn_ballot_w64(sg + q < ps) != 0ull; q += 256u) {
                const u32x4 d0 = *(const LDS u32x4*)(uintptr_t)(sg + q + u16), d1 = *(const LDS u32x4*)(uintptr_t)(sg + q + 128u + u16);
                __builtin_amdgcn_raw_buffer_store_b128(d0, rsR, sg + q < ps ? a + q : 0xffffff00u, 0, AUX_SC1);
                __builtin_amdgcn_raw_buffer_store_b128(d1, rsR, sg + q + 128u < ps ? a + q + 128u : 0xffffff00u, 0, AUX_SC1);
            }
            asm volatile("" ::: "memory");
        };
        asm volatile("" : "+v"(N[0]), "+v"(N[1]), "+v"(N[2]), "+v"(N[3]), "+v"(N[4]), "+v"(N[5]), "+v"(N[6]), "+v"(N[7]) :: "memory");
        asm volatile("" : "+v"(N[8]), "+v"(N[9]), "+v"(N[10]), "+v"(N[11]), "+v"(N[12]), "+v"(N[13]), "+v"(N[14]), "+v"(N[15]) :: "memory");
        ps = sg;
        rest(IC<0>()); rest(IC<1>()); rest(IC<2>()); rest(IC<3>()); rest(IC<4>()); rest(IC<5>()); rest(IC<6>()); rest(IC<7>());
        pack(0);
        ps = sg;
        rest(IC<8>()); rest(IC<9>()); rest(IC<10>()); rest(IC<11>()); rest(IC<12>()); rest(IC<13>()); rest(IC<14>()); rest(IC<15>());
        pack(1);
    }
    if (blockIdx.x == 0 && t8 == 1536u) {
        do {
            beat();
            __builtin_amdgcn_s_sleep(8);
        } while (peek(vconst<M_MD * 4>()) < NWG);
    }
}

#define A_MD 0
#define A_ACC (SLOTI * 4)
#define A_FLAG (A_ACC + SLOTO * 4)
#define A_LDS (A_FLAG + 512)
static_assert(SLOTI * 4 == 3 * NT * 16 + 1024, "md staging assumes three full vectors and 256 dwords");
#define BIAS 0x40004000u
#define NFAST 8
#define NLD 10
#define MDF 0x0a11333
#define MDP 0xaaaaa
#define HP 2
#define SHARE (SLOTO / 8)
#define SPINCAP 4096u
#define ZSIG 9.0f
#define HSLOW 0x80008000u
#define GAS __attribute__((address_space(1)))

__global__ __launch_bounds__(NT) __attribute__((amdgpu_num_vgpr(53))) void acc_kernel(char* ws, int T, int E, int ks, int lcap, float zf, int pad,
                                                 const uint16_t* __restrict__ centers, const uint16_t* __restrict__ wrbf,
                                                 const uint16_t* __restrict__ wsbf, const uint16_t* __restrict__ wup,
                                                 const u32x2* __restrict__ m, const uint16_t* __restrict__ dist, u32x2* __restrict__ out,
                                                 const int* __restrict__ iin, const int* __restrict__ iout, const uint16_t* __restrict__ ang,
                                                 const uint16_t* __restrict__ wdown, const uint16_t* __restrict__ freq) {
    const char* recs = ws + W_RECS;
    const u32x4* tcnt = (const u32x4*)(ws + W_TCNT);
    const uint32_t* mdc = (const uint32_t*)(ws + W_MDC);
    const u32x4* stat = (const u32x4*)(ws + W_STAT);
    uint32_t* partial = (uint32_t*)(ws + W_PART);
    unsigned* meta = (unsigned*)ws;
    extern __shared__ __attribute__((aligned(16))) char smem[];
    unsigned* acc = (unsigned*)(smem + A_ACC);
    unsigned* s_flag = (unsigned*)(smem + A_FLAG);
    const int tid = threadIdx.x;
    const int lane = tid & 63;
    const int wv = __builtin_amdgcn_readfirstlane(tid >> 6);
    const int cp = (blockIdx.x >> 3) & 1;
    const int h = blockIdx.x & 3;
    const int j = ((blockIdx.x >> 2) & 1) * 16 + (blockIdx.x >> 4);
    u32x4 tc;
    const __amdgpu_buffer_rsrc_t rsM = __builtin_amdgcn_make_buffer_rsrc((void*)mdc, 0, (int)(128u * SLOTI * 4u), 0x00020000);

    u32x4 pm[4];
    u32x4 ring[NLD];
    auto load_md = [&](int ff, int pl, int mdp) __attribute__((always_inline)) {
        const uint32_t so = (uint32_t)(cp * 64 + ff * 4 + h) * (SLOTI * 4u);
#pragma unroll
        for (int v = 0; v < 3; v++)
            if (pl < 0 || pl == ((mdp >> (4 * v)) & 15)) pm[v] = __builtin_amdgcn_raw_buffer_load_b128(rsM, (uint32_t)tid * 16u, so + (uint32_t)v * (NT * 16u), 0);
        if ((pl < 0 || pl == (mdp & 15)) && wv == 0) pm[3] = __builtin_amdgcn_raw_buffer_load_b128(rsM, (uint32_t)tid * 16u, so + 3u * NT * 16u, 0);
        __builtin_amdgcn_sched_barrier(0);
    };
    const uint32_t lf = (uint32_t)(tid >> 9) * (SKH * 128u) + (uint32_t)(tid & 0x1e0) * 16u + (uint32_t)(tid & 12) * 32u + (uint32_t)(tid & 16) * 4u + (uint32_t)(tid & 3) * 16u;
    const uint32_t lp = (uint32_t)(tid >> 8) * (SKM * 128u) + (uint32_t)((tid >> 2) & 63) * 128u + (uint32_t)(tid & 3) * 16u;
    auto load_main = [&](int i, int ff) __attribute__((always_inline)) -> u32x4 {
        const uint32_t q = (uint32_t)(((i >> 1) - HP) & 3), SK = q ? SKM : SKH, SS = q ? SSM : SSH;
        const __amdgpu_buffer_rsrc_t rs = __builtin_amdgcn_make_buffer_rsrc((void*)(recs + (q * SQ + (uint32_t)ff * SS + (uint32_t)(h * 32 + j) * SC) * 128u), 0, ks * (int)(SK * 128u), 0x00020000);
        if (q == 0u) return __builtin_amdgcn_raw_buffer_load_b128(rs, lf, (i & 1) * (int)(2u * SK * 128u), AUX_NT);
        return __builtin_amdgcn_raw_buffer_load_b128(rs, lp + (uint32_t)(i & 1) * 64u, 0, 0);
    };
    const __amdgpu_buffer_rsrc_t rsT = __builtin_amdgcn_make_buffer_rsrc((void*)recs, 0, (int)(LBASE * 128u), 0x00020000);
    const uint32_t tof = (uint32_t)(lane & 2) * (TPL * 64u) + (uint32_t)(tid >> 2) * 128u + (uint32_t)(lane & 1) * 64u + 4u + (TBASE + (uint32_t)(h * 32 + j) * TST) * 128u;
    const uint32_t sp0 = (SBASE + (uint32_t)(h * 32 + j) * (2u * NWG * 9u) + (uint32_t)(tid >> 2) * 9u) * 128u;
    auto tail_rank = [&](uint32_t c, uint32_t sp, int ff, uint32_t t0, int& dst, uint32_t& en) __attribute__((always_inline)) -> int {
        const int k4 = (int)(((uint32_t)(lane & 3) + t0) * 4u) - (int)((c & 3u) * 8u);
        const int left = (int)((c >> 2) & 15u) - k4;
        const bool has = left > 0;
        uint32_t ea = tof + (uint32_t)ff * (128u * TST * 128u), eb = sp + (uint32_t)(k4 * 16 + min(max(left, 0), 4));
        asm volatile("" : "+v"(ea), "+v"(eb));
        en = k4 < 0 ? ea : eb;
        const u64 mask = __builtin_amdgcn_ballot_w64(has);
        const int n = __builtin_popcountll(mask);
        const int rank = __builtin_amdgcn_mbcnt_hi((uint32_t)(mask >> 32), __builtin_amdgcn_mbcnt_lo((uint32_t)mask, 0u));
        const int r = has ? rank : (n + lane - rank);
        dst = ((r & 15) << 4) | ((r >> 4) << 2);
        return n;
    };
    auto load_tail = [&](uint32_t en, u64& tm) __attribute__((always_inline)) -> u32x4 {
        const bool on = (uint32_t)(lane & 3) < (en & 7u);
        tm = __builtin_amdgcn_ballot_w64(on);
        return __builtin_amdgcn_raw_buffer_load_b128(rsT, on ? (en & ~15u) + (uint32_t)(lane & 3) * 16u : 0xffffff00u, 0, 0);
    };
    f32x2 fa[4][4];
    u32x4 em[2];
    u32x2 dw2;
    uint32_t wl;
    const __amdgpu_buffer_rsrc_t rsE = __builtin_amdgcn_make_buffer_rsrc((void*)m, 0, E * 8, 0x00020000);
    const __amdgpu_buffer_rsrc_t rsD = __builtin_amdgcn_make_buffer_rsrc((void*)dist, 0, E * 2, 0x00020000);
    auto acc_vec = [&](int i) __attribute__((always_inline)) -> u32x4 {
        const uint32_t a = A_ACC + (uint32_t)(i >> 4) * 256u + (uint32_t)(i & 15) * 8u;
        const u32x2 x = *(const LDS u32x2*)(uintptr_t)a, y = *(const LDS u32x2*)(uintptr_t)(a + 128u);
        const u32x4 v = {x.x, x.y, y.x, y.y};
        return v;
    };
    auto edge0 = [&](int sh) __attribute__((always_inline)) -> uint32_t {
        return (tid < SHARE / 4) ? (uint32_t)(((sh * (SHARE / 64) + (tid >> 4)) << 11) | (j << 6) | ((tid & 15) * 2)) : 0x0fffff00u;
    };
    auto load_m = [&](int sh) __attribute__((always_inline)) {
        em[0] = __builtin_amdgcn_raw_buffer_load_b128(rsE, edge0(sh) * 8u, 0, AUX_NT);
        em[1] = __builtin_amdgcn_raw_buffer_load_b128(rsE, edge0(sh) * 8u + 256u, 0, AUX_NT);
    };
    auto load_d = [&](int sh) __attribute__((always_inline)) {
        dw2.x = __builtin_amdgcn_raw_buffer_load_b32(rsD, edge0(sh) * 2u, 0, 0);
        dw2.y = __builtin_amdgcn_raw_buffer_load_b32(rsD, edge0(sh) * 2u + 64u, 0, 0);
    };
    auto load_wl = [&]() __attribute__((always_inline)) {
        const u64 p0 = (u64)wsbf, p1 = (u64)wrbf, p2 = (u64)centers, p3 = (u64)wup;
        const u64 pa = lane < 16 ? p0 : p1, pb = lane < 40 ? p2 : p3;
        wl = ((const GAS uint32_t*)(lane < 32 ? pa : pb))[lane & (lane < 32 ? 15 : lane < 40 ? 3 : 7)];
    };
    auto factor = [&](int r, bool first) __attribute__((always_inline)) {
        const float sq = 3.7982826f;
        const f32x2 d01 = {bf2f(dw2.x & 0xffffu), bf2f(dw2.x >> 16)}, d23 = {bf2f(dw2.y & 0xffffu), bf2f(dw2.y >> 16)};
        const uint32_t cw = __builtin_amdgcn_readlane(wl, 32 + (r >> 1));
        const float cn = bf2f((r & 1) ? (cw >> 16) : (cw & 0xffffu));
        f32x2 w[4];
#pragma unroll
        for (int k = 0; k < 4; k++) {
            const uint32_t x = __builtin_amdgcn_readlane(wl, (k >> 1) * 16 + r * 2 + (k & 1));
            w[k].x = bf2f(x & 0xffffu); w[k].y = bf2f(x >> 16);
        }
        const f32x2 y01 = (d01 - cn) * sq, y23 = (d23 - cn) * sq;
        const f32x2 t01 = -y01 * y01, t23 = -y23 * y23;
        const float rb[4] = {__builtin_amdgcn_exp2f(t01.x), __builtin_amdgcn_exp2f(t01.y), __builtin_amdgcn_exp2f(t23.x), __builtin_amdgcn_exp2f(t23.y)};
#pragma unroll
        for (int q = 0; q < 4; q++) {
#pragma unroll
            for (int k = 0; k < 4; k++) fa[q][k] = first ? w[k] * rb[q] : __builtin_elementwise_fma(w[k], (f32x2)(rb[q]), fa[q][k]);
        }
    };
    auto edges = [&](int sh) __attribute__((always_inline)) {
        load_d(sh);
        load_m(sh);
        factor(0, true);
#pragma unroll 1
        for (int r = 1; r < 8; r++) factor(r, false);
    };
#define ADT(V, O) "ds_write_addtid_b32 " V " offset:" #O "\n\t"
    auto store_md = [&]() __attribute__((always_inline)) {
        asm volatile("s_mov_b32 m0, %12\n\ts_nop 0\n\t" ADT("%0", 0) ADT("%1", 256) ADT("%2", 512) ADT("%3", 768) ADT("%4", 16384) ADT("%5", 16640) ADT("%6", 16896) ADT("%7", 17152)
                     ADT("%8", 32768) ADT("%9", 33024) ADT("%10", 33280) ADT("%11", 33536)
                     :: "v"(pm[0][0]), "v"(pm[0][1]), "v"(pm[0][2]), "v"(pm[0][3]), "v"(pm[1][0]), "v"(pm[1][1]), "v"(pm[1][2]), "v"(pm[1][3]),
                        "v"(pm[2][0]), "v"(pm[2][1]), "v"(pm[2][2]), "v"(pm[2][3]), "s"(wv * 1024) : "memory");
        if (wv == 0) asm volatile("s_mov_b32 m0, 0\n\ts_nop 0\n\t" ADT("%0", 49152) ADT("%1", 49408) ADT("%2", 49664) ADT("%3", 49920)
                                  :: "v"(pm[3][0]), "v"(pm[3][1]), "v"(pm[3][2]), "v"(pm[3][3]) : "memory");
    };
    const int me = h * 2 + cp;
    GAS unsigned* dw = (GAS unsigned*)(meta + M_DONE + j * 64);
    const bool fin = wv < (SHARE / 4 + 63) / 64;
    bool fast, more;
    uint32_t need, mg0, mg1;
    uint32_t guard = 0;
    uint32_t sgn = 0x8000u;
    pin(sgn);

#define GAT3(M) { uint32_t a0_, a1_, a2_; asm volatile( \
        "v_lshlrev_b16 %0, 2, %3\n\tv_lshlrev_b16 %1, 2, %4\n\tv_lshlrev_b16 %2, 2, %5\n\t" \
        "ds_read_b32 v[%6], %0\n\tds_read_b32 v[%6+1], %1\n\tds_read_b32 v[%6+2], %2" \
        : "=&v"(a0_), "=&v"(a1_), "=&v"(a2_) : "v"(w0), "v"(w1), "v"(w2), "n"(M) : "memory"); }
#define MIXL " op_sel_hi:[1,1,0]\n\t"
#define MIXH " op_sel:[0,1,0] op_sel_hi:[1,1,0]\n\t"
#define SDW " dst_sel:DWORD dst_unused:UNUSED_PAD src0_sel:DWORD src1_sel:WORD_1\n\t"
#define FOLD "v_bitop3_b32 %6, %6, v[%15-9], v[%15-8] bitop3:0xfe\n\tv_or_b32 %6, v[%15-7], %6\n\t"
#define SCAT(PRE, POST, M) { uint32_t c0_, c1_, c2_, a0_, a1_, a2_; asm volatile( \
        FOLD "v_and_b32 %0, 0x3ff, %7\n\tv_lshrrev_b32 %2, 22, %7\n\t" PRE \
        "v_bitop3_b32 %0, %11, %8, %0 bitop3:0xea\n\tv_bitop3_b32 %2, %11, %10, %2 bitop3:0xea\n\tv_bfe_u32 %1, %7, 11, 10\n\t" \
        "v_fma_mix_f32 %3, %0, v[%15], %12" MIXL "v_bitop3_b32 %1, %11, %9, %1 bitop3:0xea\n\tv_fma_mix_f32 %0, %0, v[%15], %13" MIXH \
        "v_fma_mix_f32 %4, %1, v[%15+1], %12" MIXL "v_lshl_add_u32 %0, %0, 16, %3\n\tv_lshlrev_b32_sdwa %3, 2, %8" SDW \
        "v_fma_mix_f32 %1, %1, v[%15+1], %13" MIXH "ds_add_rtn_u32 v[%15-9], %3, %0 offset:%14\n\t" \
        "v_fma_mix_f32 %5, %2, v[%15+2], %12" MIXL "v_lshl_add_u32 %1, %1, 16, %4\n\tv_lshlrev_b32_sdwa %4, 2, %9" SDW \
        "v_fma_mix_f32 %2, %2, v[%15+2], %13" MIXH "ds_add_rtn_u32 v[%15-8], %4, %1 offset:%14\n\t" \
        "v_lshl_add_u32 %2, %2, 16, %5\n\tv_lshlrev_b32_sdwa %5, 2, %10" SDW \
        "ds_add_rtn_u32 v[%15-7], %5, %2 offset:%14" POST \
        : "=&v"(c0_), "=&v"(c1_), "=&v"(c2_), "=&v"(a0_), "=&v"(a1_), "=&v"(a2_), "+v"(guard) \
        : "v"(xw), "v"(w0), "v"(w1), "v"(w2), "v"(sgn), "s"(mg0), "s"(mg1), "n"(A_ACC), "n"(M), "s"(tm) : "memory"); }
    asm volatile("v_mov_b32 v110, 0\n\tv_mov_b32 v111, 0\n\tv_mov_b32 v112, 0\n\tv_mov_b32 v113, 0\n\tv_mov_b32 v114, 0\n\tv_mov_b32 v115, 0\n\t"
                 "v_mov_b32 v116, 0\n\tv_mov_b32 v117, 0\n\tv_mov_b32 v118, 0" ::: "v106", "v127");
    auto unmix = [&](int i) __attribute__((always_inline)) {
        const auto s0 = __builtin_amdgcn_permlane16_swap(ring[i][0], ring[i][2], false, false);
        const auto s1 = __builtin_amdgcn_permlane16_swap(ring[i][1], ring[i][3], false, false);
        ring[i][0] = s0[0]; ring[i][2] = s0[1]; ring[i][1] = s1[0]; ring[i][3] = s1[1];
    };
    auto word = [&](int i, int k) __attribute__((always_inline)) -> uint32_t {
        return i < NFAST && (i >> 1) != HP ? ring[(i & ~1) | (k >> 1)][(i & 1) * 2 + (k & 1)] : ring[i][k];
    };
    auto gather = [&](int R, int i, int p) __attribute__((always_inline)) {
        if ((i >> 1) == HP) unmix(i);
        const uint32_t w0 = word(i, 0 ^ R), w1 = word(i, 1 ^ R), w2 = word(i, 2 ^ R);
        if (p == 0) GAT3(119) else if (p == 1) GAT3(122) else GAT3(125)
    };
    auto scatter = [&](int R, int i, int p, u64 tm) __attribute__((always_inline)) {
        const uint32_t xw = word(i, 3 ^ R), w0 = word(i, 0 ^ R), w1 = word(i, 1 ^ R), w2 = word(i, 2 ^ R);
        if (i < NFAST) { if (p == 0) SCAT("", "", 119) else if (p == 1) SCAT("", "", 122) else SCAT("", "", 125) }
        else if (p == 0) SCAT("s_mov_b64 exec, %16\n\t", "\n\ts_mov_b64 exec, -1", 119)
        else SCAT("s_mov_b64 exec, %16\n\t", "\n\ts_mov_b64 exec, -1", 125)
    };
#define WAIT_L(N) asm volatile("s_waitcnt lgkmcnt(" #N ")" ::: "memory")
    int ntn;
    u64 tm0, tm1;
    uint32_t ta[2], sp = sp0;
    auto list1 = [&](int ff) __attribute__((always_inline)) {
        int dst;
        uint32_t en;
        ntn = tail_rank(tc.x, sp, ff, 0u, dst, en);
        asm volatile("ds_permute_b32 v106, %0, %1" :: "v"(dst), "v"(en) : "memory");
        if (__builtin_expect(__builtin_amdgcn_ballot_w64((tc.x & 2u) != 0u) != 0ull, 0)) {
            const int n2 = tail_rank(tc.x, sp, ff, 4u, dst, en);
            if (ntn + n2 <= 32) {
                const int r = ((dst >> 4) | ((dst & 12) << 2)) + ntn;
                const int x = __builtin_amdgcn_ds_permute((en & 7u) ? ((r & 15) << 4) | ((r >> 4) << 2) : 252, (en & 7u) ? (int)en : 0);
                asm volatile("v_cmpx_ne_u32 vcc, 0, %0\n\tv_mov_b32 v106, %0\n\ts_mov_b64 exec, -1\n\ts_nop 4" :: "v"(x) : "vcc");
                ntn += n2;
            } else ntn = 64;
        }
    };
    auto step = [&](auto RR, auto KK, int ff) __attribute__((always_inline)) {
        constexpr int R = decltype(RR)::value;
        constexpr bool first = decltype(KK)::value == 1, last = decltype(KK)::value == 2;
        constexpr int mdp = first ? MDF : MDP;
        gather(R, 0, 0);
        const int ntl = ntn;
        asm volatile("v_mov_b32_dpp %0, v106 quad_perm:[0,0,0,0] row_mask:0xf bank_mask:0xf\n\tv_mov_b32_dpp %1, v106 quad_perm:[1,1,1,1] row_mask:0xf bank_mask:0xf" : "=v"(ta[0]), "=v"(ta[1]));
        auto side = [&](int pl) __attribute__((always_inline)) {
            if (first && pl == ((mdp >> 20) & 15)) { ring[4] = load_main(4, ff); ring[5] = load_main(5, ff); }
            if (first && pl == ((mdp >> 24) & 15)) { ring[6] = load_main(6, ff); ring[7] = load_main(7, ff); }
            if (pl == ((mdp >> 12) & 15)) ring[NFAST] = load_tail(ta[0], tm0);
            if (pl == ((mdp >> 16) & 15)) ring[NFAST + 1] = load_tail(ta[1], tm1);
            __builtin_amdgcn_sched_barrier(0);
            if (!last) load_md(ff + 1, pl, mdp);
            if (!first && !last && pl == 5 && ff == 6) {
                load_d(me);
                load_wl();
                if (tid == NT - 128) (void)__hip_atomic_fetch_add((GAS unsigned*)((GAS char*)dw + vconst<0>()), 0x1000000u << me, __ATOMIC_RELAXED, __HIP_MEMORY_SCOPE_AGENT);
            }
            if (last && pl == 3) load_m(me);
            if (last && pl == 4 && wv == 15) { GAS unsigned* d_ = dw; asm volatile("s_nop 4\n\tglobal_load_dword v108, %0, %1 sc1" :: "v"(vconst<0>()), "s"(d_) : "memory"); }
            if (last && pl < 7 && fin) factor(pl, pl == 0);
            __builtin_amdgcn_sched_barrier(0);
        };
        side(8);
        gather(R, 1, 1);
        side(9);
        const uint32_t tcur = tc.x, spc = sp;
#pragma unroll
        for (int i = 0; i < NFAST - 1; i++) {
            gather(R, i + 2, (i + 2) % 3);
            if (i == 0) side(10);
            if (i == 0) WAIT_L(6); else if (i == 1) WAIT_L(9); else WAIT_L(12);
            scatter(R, i, i % 3, 0ull);
            if (!last && (i & 1)) { ring[i - 1] = load_main(i - 1, ff + 1); ring[i] = load_main(i, ff + 1); }
            side(i);
            if (i == 3) SETP(2);
            if (i == 4) SETP(1);
            if (i == 5) SETP(0);
            if (i == 0 && !last) {
                tc.x = __builtin_amdgcn_alignbit(tc.y, tc.x, 8u); tc.y = __builtin_amdgcn_alignbit(tc.z, tc.y, 8u);
                tc.z = __builtin_amdgcn_alignbit(tc.w, tc.z, 8u); tc.w >>= 8;
                sp = ff == 7 ? sp0 + NWG * 9u * 128u : sp + ((tcur & 0x3cu) << 2);
                list1(ff + 1);
            }
            __builtin_amdgcn_sched_barrier(0);
        }
        if (last) asm volatile("" :: "v"(ring[NFAST + 1]));
        if (__builtin_expect(ntl > 16, 0)) {
            gather(R, NFAST + 1, 0);
            WAIT_L(0);
            scatter(R, NFAST + 1, 0, tm1);
        }
        if (__builtin_expect(ntl > 32, 0)) {
#pragma unroll 1
            for (int e = 2; e < 6; e++) {
                int dst;
                uint32_t en;
                if (tail_rank(tcur, spc, ff, (uint32_t)e & 4u, dst, en) <= (e & 3) * 16) continue;
                const int cmp = __builtin_amdgcn_ds_permute(dst, (int)en);
                ring[NFAST + 1] = load_tail((uint32_t)__builtin_amdgcn_ds_bpermute(((lane & ~3) | (e & 3)) << 2, cmp), tm1);
                gather(R, NFAST + 1, 0);
                WAIT_L(0);
                scatter(R, NFAST + 1, 0, tm1);
            }
        }
        SETP(3);
        WAIT_L(3);
        if (!last) {
            __builtin_amdgcn_s_barrier();
            store_md();
            SETP(0);
        }
        scatter(R, NFAST - 1, 1, 0ull);
        if (!last) { ring[6] = load_main(6, ff + 1); ring[7] = load_main(7, ff + 1); }
        scatter(R, NFAST, 2, tm0);
        if (!last) {
            WAIT_L(6);
            __builtin_amdgcn_s_barrier();
            SETP(3);
        }
    };
    auto steps = [&](auto RR) __attribute__((always_inline)) {
        tc = tcnt[(wv * 16 + (lane >> 2)) * 128 + j * 4 + h];
        u32x4 sv[2];
        uint32_t e0;
        if (wv < 4) {
            sv[0] = (u32x4)(0u); sv[1] = sv[0];
            if (lane < SLOTI / 256) { sv[0] = stat[tid * 2]; sv[1] = stat[tid * 2 + 1]; }
            e0 = meta[M_E0 + wv];
        }
        const u32x2 extra = *(const u32x2*)(meta + M_LOG);
        load_md(0, -1, 0);
        {
            const u32x4 bi = {BIAS, BIAS, BIAS, BIAS};
#pragma unroll
            for (int k = 0; k < 6; k++) *(LDS u32x4*)(uintptr_t)(A_ACC + (k * NT + threadIdx.x) * 16) = bi;
            if (threadIdx.x < 128) *(LDS u32x4*)(uintptr_t)(A_ACC + (6 * NT + threadIdx.x) * 16) = bi;
            __builtin_amdgcn_sched_barrier(0);
        }
#pragma unroll
        for (int i = 0; i < 2; i++) ring[i] = load_main(i, 0);
        __builtin_amdgcn_sched_barrier(0);
        list1(0);
        if (wv < 4) {
            const uint32_t mxb = wave_max(max(max(sv[0].x & 0xffffu, sv[0].z & 0xffffu), max(sv[1].x & 0xffffu, sv[1].z & 0xffffu)));
            const uint32_t mnb = 0xffffu - (wave_max(max(max(sv[0].x, sv[0].z), max(sv[1].x, sv[1].z))) >> 16);
            const float s2 = wave_sum((__uint_as_float(sv[0].y) + __uint_as_float(sv[0].w)) + (__uint_as_float(sv[1].y) + __uint_as_float(sv[1].w)));
            const float rng = fmaxf(sqrtf(zf * s2), 2.0f * bf2f(mxb)) * 6.103515625e-05f;
            const uint32_t eb = mxb ? (__float_as_uint(rng) + 0x7fffffu) >> 23 : 127u;
            const bool ok = !mxb || (mxb < 0x4700u && eb >= 107u && eb <= 140u && mnb >= ((eb + 8u) << 7));
            if (lane == 0) {
                s_flag[8 + wv] = (eb & 0xffu) | (ok ? 0x100u : 0u) | (mxb ? 0x200u : 0u);
                s_flag[12 + wv] = __float_as_uint(__uint_as_float(((eb + e0) & 0xffu) << 23) * (1024.0f / 1023.0f));
                s_flag[wv] = 0u;
            }
        }
        store_md();
#pragma unroll
        for (int i = 2; i < 4; i++) ring[i] = load_main(i, 0);
        WAIT_L(0);
        __syncthreads();
        const uint32_t q0 = __builtin_amdgcn_readfirstlane(s_flag[8 + 2 * cp]), q1 = __builtin_amdgcn_readfirstlane(s_flag[9 + 2 * cp]);
        fast = (q0 & q1 & 0x100u) != 0u;
        need = (q0 >> 9) | ((q1 >> 9) << 1);
        more = (__builtin_amdgcn_readfirstlane(extra.x | extra.y) | (uint32_t)(T & 3)) != 0u;
        mg0 = (((q0 & 0xffu) + 9u) << 23) | 0x400000u;
        mg1 = ((((q1 & 0xffu) + 9u) << 23) | 0x400000u) + ((0u - (mg0 >> 16)) & 0xffffu);
        if (!fast) { __builtin_amdgcn_s_waitcnt(0x0f70); return; }
        step(RR, IC<1>(), 0);
#pragma unroll 1
        for (int ff = 1; ff < 15; ff++) step(RR, IC<0>(), ff);
        step(RR, IC<2>(), 15);
        if (fin) factor(7, false);
    };
    auto irregular = [&](auto f) __attribute__((always_inline)) {
        const float fr = bf2f(freq[0]);
        const char* lg = recs + (size_t)LBASE * 128u;
        const uint16_t* tags = (const uint16_t*)(lg + (size_t)lcap * 128u);
        const uint32_t nl = meta[M_LOG], ns = meta[M_LOG + 1];
#pragma unroll 1
        for (uint32_t q = tid; q < nl * 8u; q += NT) {
            const uint32_t b = tags[q >> 3];
            if ((b & 127u) == (uint32_t)(h * 32 + j)) {
                const uint32_t* un = (const uint32_t*)(lg + (size_t)q * 16u);
                const uint32_t xw = un[3 ^ h];
#pragma unroll 1
                for (int k = 0; k < 3; k++) {
                    const uint32_t ww = un[k ^ h];
                    const float c = (float)((xw >> (k * 11)) & 0x3ffu) * (1.0f / 1023.0f);
                    if (!(ww & WNULL)) f(ww >> 16, ((ww & 0x3fffu) << 6) | (b >> 5), (ww & 0x8000u) ? -c : c);
                }
            }
        }
#pragma unroll 1
        for (uint32_t q = tid; q < ns + (uint32_t)(T & 3); q += NT) {
            const uint32_t t = q < ns ? ((const uint32_t*)tags)[-1 - (int)q] : (uint32_t)(T & ~3) + (q - ns);
            const uint32_t i = (uint32_t)iin[t], o = (uint32_t)iout[t];
            if (((o >> 6) & 31u) == (uint32_t)j && (i & 3u) == (uint32_t)h) f(((o >> 11) << 6) | (o & 63u), i, cosf(fr * bf2f(ang[t])));
        }
        __builtin_amdgcn_s_waitcnt(0x0f70);
    };

    if (h == 0) steps(IC<0>());
    else if (h == 1) steps(IC<1>());
    else if (h == 2) steps(IC<2>());
    else steps(IC<3>());
    if (fast) {
        if (more) irregular([&](uint32_t ol, uint32_t i, float cf) __attribute__((always_inline)) {
            const uint32_t md = mdc[(cp * 64u + (i & 63u)) * SLOTI + ((i >> 14) << 8) + ((i >> 4) & 0xfcu) + ((i >> 12) & 3u)];
            const float c = cf * (1023.0f / 16777216.0f);
            const uint32_t lo = __float_as_uint(__builtin_fmaf(c, h2f(md & 0xffffu), __uint_as_float(mg0)));
            const uint32_t hi = __float_as_uint(__builtin_fmaf(c, h2f(md >> 16), __uint_as_float(mg1)));
            if (cf == cf) guard |= atomicAdd(&acc[ol], (hi << 16) + lo);
            else guard |= HSLOW;
        });
    }
    volatile LDS unsigned* seen = (volatile LDS unsigned*)(uintptr_t)(A_FLAG + 4);
    if (fast && wv == 15) asm volatile("s_waitcnt vmcnt(0)\n\tds_write_b32 %0, v108 offset:16\n\tv_and_b32 v109, 0xff00ff, v108\n\tds_write_b32 %0, v109" :: "v"(vconst<A_FLAG + 4>()) : "memory");
    WAIT_L(0);
    asm volatile("v_or3_b32 %0, %0, v110, v111\n\tv_or3_b32 %0, %0, v112, v113\n\tv_or3_b32 %0, %0, v114, v115\n\tv_or3_b32 %0, %0, v116, v117\n\tv_or_b32 %0, v118, %0" : "+v"(guard));
    __syncthreads();
    const __amdgpu_buffer_rsrc_t rsP = __builtin_amdgcn_make_buffer_rsrc((void*)(partial + (size_t)j * 16 * SLOTO), 0, 16 * SLOTO * 4, 0x00020000);
    const char* pk = recs + (uint32_t)j * (SC * 128u);
    const __amdgpu_buffer_rsrc_t rsK = __builtin_amdgcn_make_buffer_rsrc((void*)pk, 0, (int)(MAINL * 128u), 0x00020000);
#define PKA (SSM * 128u)
#define PKB (32u * SC * 128u)
    auto pk_at = [&](uint32_t i) __attribute__((always_inline)) -> uint32_t {
        const uint32_t c = i >> 9;
        return (c < 12u ? (1u + (c >> 2)) * SQ + (c & 3u) * SKM : SQ + 2u * SSM) * 128u + (i & 511u) * 16u;
    };
    u32x4 p[8];
    unsigned have = 0u;
    auto fetch = [&](unsigned v, int sh) __attribute__((always_inline)) {
        const uint32_t po = tid < SHARE / 4 ? pk_at((uint32_t)(sh * (SHARE / 4) + tid)) : 0x7fffff00u;
        const unsigned nw = v & ~have;
#pragma unroll
        for (int r = 0; r < 8; r++) {
            const uint32_t k = (uint32_t)(me ^ r);
            if ((nw >> k) & 1u) p[r] = __builtin_amdgcn_raw_buffer_load_b128(rsK, po, ((v >> (16u + k)) & 1u) ? MAINL * 128u : (k & 1u) * PKA + (k >> 1) * PKB, AUX_SC1);
        }
        have |= v;
    };
    bool room = false;
    if (fast) {
        room = (__builtin_amdgcn_readfirstlane(s_flag[5]) >> (24 + (me ^ 1))) & 1u;
        const __amdgpu_buffer_rsrc_t rsW = __builtin_amdgcn_make_buffer_rsrc((void*)(pk + (uint32_t)cp * PKA + (uint32_t)h * PKB), 0, room ? (int)(MAINL * 128u) : 0, 0x00020000);
        uint32_t big = 0u;
        u32x4 v[7];
#pragma unroll
        for (int k = 0; k < 6; k++) v[k] = acc_vec(k * NT + tid);
        v[6] = (u32x4)(BIAS);
        if (wv < 2) v[6] = acc_vec(6 * NT + tid);
#pragma unroll
        for (int k = 0; k < 6; k++) __builtin_amdgcn_raw_buffer_store_b128(v[k], rsW, pk_at((uint32_t)tid) + ((k >> 1) * SQ + (k & 1) * 2u * SKM) * 128u, 0, AUX_SC1);
        if (wv < 2) __builtin_amdgcn_raw_buffer_store_b128(v[6], rsW, (SQ + 2u * SSM) * 128u + (uint32_t)tid * 16u, 0, AUX_SC1);
        __builtin_amdgcn_sched_barrier(0);
#pragma unroll
        for (int k = 0; k < 28; k++) { guard |= v[k >> 2][k & 3]; big |= v[k >> 2][k & 3] + 0x42004200u; }
        if (guard & HSLOW) s_flag[0] = 1u;
        if (big & 0x7c00u) s_flag[2] = 1u;
        if (big & 0x7c000000u) s_flag[3] = 2u;
        __builtin_amdgcn_sched_barrier(0);
        p[0] = acc_vec(me * (SHARE / 4) + min(tid, SHARE / 4 - 1));
        have = 1u << me;
        if (fin) fetch(__builtin_amdgcn_readfirstlane(*seen), me);
    }
    asm volatile("s_waitcnt vmcnt(0)" ::: "memory");
    __syncthreads();
    const bool redo = !fast || __builtin_amdgcn_readfirstlane(s_flag[0]) != 0u || (need & ~__builtin_amdgcn_readfirstlane(s_flag[2] | s_flag[3])) != 0u;
    const bool slow = redo || !room;
    if (slow) {
        float* fs = (float*)acc;
        float w[16];
        load_w(wdown, w);
        const float fr = bf2f(freq[0]);
#pragma unroll 1
        for (int e = 0; e < 2; e++) {
            if (redo) {
                __syncthreads();
                for (int i = tid; i < SLOTO; i += NT) fs[i] = 0.f;
                __syncthreads();
                const __amdgpu_buffer_rsrc_t rsO = __builtin_amdgcn_make_buffer_rsrc((void*)iout, 0, T * 4, 0x00020000);
                const __amdgpu_buffer_rsrc_t rsI = __builtin_amdgcn_make_buffer_rsrc((void*)iin, 0, T * 4, 0x00020000);
#pragma unroll 1
                for (int t0 = tid * 4; t0 < T; t0 += NT * 16) {
                    u32x4 o4[4], i4[4];
#pragma unroll
                    for (int g = 0; g < 4; g++) {
                        o4[g] = __builtin_amdgcn_raw_buffer_load_b128(rsO, (uint32_t)(t0 + g * NT * 4) * 4u, 0, 0);
                        i4[g] = __builtin_amdgcn_raw_buffer_load_b128(rsI, (uint32_t)(t0 + g * NT * 4) * 4u, 0, 0);
                    }
                    uint32_t hit = 0u;
#pragma unroll
                    for (int k = 0; k < 16; k++) hit |= (bfi(3u, i4[k >> 2][k & 3], o4[k >> 2][k & 3]) & 0x7c3u) == (uint32_t)(j * 64 + h) ? 1u << k : 0u;
#pragma unroll 1
                    for (; hit; hit &= hit - 1u) {
                        const int k = __builtin_ctz(hit), t = t0 + (k >> 2) * NT * 4 + (k & 3);
                        if (t >= T) break;
                        const uint32_t o = (uint32_t)iout[t];
                        uint32_t rr[4];
                        md_of(m[iin[t]], w, rr);
                        atomicAdd(&fs[((o >> 11) << 6) | (o & 63u)], cosf(fr * bf2f(ang[t])) * bf2f(cp ? (e ? rr[3] : rr[2]) : (e ? rr[1] : rr[0])));
                    }
                }
                __syncthreads();
            }
            const float un = __uint_as_float(s_flag[12 + 2 * cp + e]);
            for (int i = tid; i < SLOTO / 4; i += NT) {
                u32x4 v = acc_vec(i);
                if (!redo) {
#pragma unroll
                    for (int k = 0; k < 4; k++) v[k] = __float_as_uint((float)((int)((v[k] >> (16 * e)) & 0xffffu) - 0x4000) * un);
                }
                __builtin_amdgcn_raw_buffer_store_b128(v, rsP, (uint32_t)((e * 8 + me) * SLOTO * 4 + i * 16), 0, AUX_SC1);
            }
        }
        load_wl();
        edges(me);
#pragma unroll
        for (int k = 0; k < 8; k++) p[k] = (u32x4)(0u);
        have = 0u;
        asm volatile("s_waitcnt vmcnt(0)" ::: "memory");
        __syncthreads();
    }
    if (tid == NT - 64) {
        const unsigned inc = (slow ? 0x10001u : 1u) << me;
        unsigned v = __hip_atomic_fetch_add(dw, inc, __ATOMIC_RELAXED, __HIP_MEMORY_SCOPE_AGENT) + inc;
        unsigned quit = 0u;
        if ((v & 0xffu) != 0xffu) {
            unsigned spins = 0;
            do {
                *seen = v & 0xff00ffu;
                __builtin_amdgcn_s_sleep(1);
                v = __hip_atomic_load(dw, __ATOMIC_RELAXED, __HIP_MEMORY_SCOPE_AGENT);
                if (++spins > SPINCAP && (v & 0xffu) != 0xffu) {
                    v = __hip_atomic_fetch_add(dw, 256u << me, __ATOMIC_RELAXED, __HIP_MEMORY_SCOPE_AGENT);
                    if ((v & 0xffu) != 0xffu) { v = 0xffu; quit = 0x80000000u; }
                }
            } while ((v & 0xffu) != 0xffu);
            v &= 0xff00ffu;
        }
        *seen = (v & 0xffffffu) | quit;
    }
    if (!fin) return;
    have = __builtin_amdgcn_readfirstlane(have);
    uint32_t un[4];
#pragma unroll
    for (int c = 0; c < 4; c++) un[c] = __builtin_amdgcn_readfirstlane(s_flag[12 + c]);
    unsigned sv;
    {
        const u32x4 rsQ = {(uint32_t)(uintptr_t)pk, (uint32_t)((uintptr_t)pk >> 32) & 0xffffu, MAINL * 128u, 0x00020000u};
        const uint32_t po = tid < SHARE / 4 ? pk_at((uint32_t)(me * (SHARE / 4) + tid)) : 0x7fffff00u;
        do {
            sv = __builtin_amdgcn_readfirstlane(*seen);
            uint32_t t0, t1, t2;
#define FET(R) "s_xor_b32 %8, %13, " #R "\n\ts_bitcmp1_b32 %14, %8\n\ts_cbranch_scc0 1f\n\ts_and_b32 %9, %8, 1\n\ts_mul_i32 %9, %9, %16\n\ts_lshr_b32 %10, %8, 1\n\t" \
               "s_mul_i32 %10, %10, %17\n\ts_add_u32 %9, %9, %10\n\ts_bitcmp1_b32 %15, %8\n\ts_cselect_b32 %9, %18, %9\n\tbuffer_load_dwordx4 %" #R ", %11, %12, %9 offen sc1\n1:\n\t"
            asm volatile("s_and_b32 %8, %14, 0xff\n\ts_cbranch_scc0 2f\n\ts_nop 2\n\t" FET(0) FET(1) FET(2) FET(3) FET(4) FET(5) FET(6) FET(7) "\n2:\n\ts_sleep 2\n\ts_waitcnt vmcnt(0)"
                         : "+v"(p[0]), "+v"(p[1]), "+v"(p[2]), "+v"(p[3]), "+v"(p[4]), "+v"(p[5]), "+v"(p[6]), "+v"(p[7]), "=&s"(t0), "=&s"(t1), "=&s"(t2)
                         : "v"(po), "s"(rsQ), "s"(me), "s"((int)sv < 0 ? 0u : sv & ~have), "s"(sv >> 16), "n"(PKA), "n"(PKB), "n"(MAINL * 128u) : "memory", "scc");
            have |= sv;
        } while ((sv & 0xffu) != 0xffu);
    }
    if ((int)sv < 0) return;
    const unsigned flt = (sv >> 16) & 0xffu;
    auto finish = [&](int sh) __attribute__((always_inline)) {
        {
            const int ol = sh * SHARE + tid * 4;
            const uint32_t po = tid < SHARE / 4 ? (uint32_t)ol * 4u : 0x7fffff00u;
            float g[4][4];
            u32x4 a[2] = {p[0] + p[2], p[1] + p[3]}, b[2] = {p[4] + p[6], p[5] + p[7]};
            if (cp) { const u32x4 t = a[0], u = b[0]; a[0] = a[1]; b[0] = b[1]; a[1] = t; b[1] = u; }
#pragma unroll
            for (int c = 0; c < 2; c++) {
                const int nb = 0x10000 - 0x4000 * __builtin_popcount(flt & (0x55u << c));
                const float u0 = __uint_as_float(nb ? un[2 * c] : 0u), u1 = __uint_as_float(nb ? un[2 * c + 1] : 0u);
#pragma unroll
                for (int q = 0; q < 4; q++) {
                    g[q][2 * c] = (float)((int)((a[c][q] & 0xffffu) + (b[c][q] & 0xffffu)) - nb) * u0;
                    g[q][2 * c + 1] = (float)((int)((a[c][q] >> 16) + (b[c][q] >> 16)) - nb) * u1;
                }
            }
            if (__builtin_expect(flt != 0u, 0)) {
#pragma unroll
                for (int c = 0; c < 2; c++) {
#pragma unroll 1
                    for (int k = c; k < 8; k += 2) {
                        if (!((flt >> k) & 1u)) continue;
                        const u32x4 f0 = __builtin_amdgcn_raw_buffer_load_b128(rsP, po, k * SLOTO * 4, AUX_SC1);
                        const u32x4 f1 = __builtin_amdgcn_raw_buffer_load_b128(rsP, po, (8 + k) * SLOTO * 4, AUX_SC1);
#pragma unroll
                        for (int q = 0; q < 4; q++) { g[q][2 * c] += __uint_as_float(f0[q]); g[q][2 * c + 1] += __uint_as_float(f1[q]); }
                    }
                }
            }
            float wu[16];
#pragma unroll
            for (int k = 0; k < 8; k++) {
                const uint32_t x = __builtin_amdgcn_readlane(wl, 40 + k);
                wu[2 * k] = bf2f(x & 0xffffu); wu[2 * k + 1] = bf2f(x >> 16);
            }
            u32x4 ov[2];
#pragma unroll
            for (int q = 0; q < 4; q++) {
#pragma unroll
                for (int k = 0; k < 4; k++) g[q][k] *= fa[q][k >> 1][k & 1];
                const uint32_t m0 = em[q >> 1][(q & 1) * 2], m1 = em[q >> 1][(q & 1) * 2 + 1];
                const float mm[4] = {bf2f(m0 & 0xffff), bf2f(m0 >> 16), bf2f(m1 & 0xffff), bf2f(m1 >> 16)};
                float o[4];
#pragma unroll
                for (int k = 0; k < 4; k++) o[k] = mm[k] + (g[q][0] * wu[k] + g[q][1] * wu[4 + k] + g[q][2] * wu[8 + k] + g[q][3] * wu[12 + k]) * fa[q][2 + (k >> 1)][k & 1];
                ov[q >> 1][(q & 1) * 2] = pk_bf16(o[0], o[1]);
                ov[q >> 1][(q & 1) * 2 + 1] = pk_bf16(o[2], o[3]);
            }
            const __amdgpu_buffer_rsrc_t rsU = __builtin_amdgcn_make_buffer_rsrc((void*)out, 0, E * 8, 0x00020000);
            const uint32_t uo = edge0(sh) * 8u;
            __builtin_amdgcn_raw_buffer_store_b128(ov[0], rsU, uo, 0, AUX_NT);
            __builtin_amdgcn_raw_buffer_store_b128(ov[1], rsU, uo + 256u, 0, AUX_NT);
        }
    };
    finish(me);
#pragma unroll 1
    for (unsigned todo = (sv >> 8) & 0xffu; todo; todo &= todo - 1u) {
        const int sh = __builtin_ctz(todo);
        edges(sh);
        have = 0u;
        fetch(sv, sh);
        finish(sh);
    }
}

__global__ __launch_bounds__(256) void gen_scatter_kernel(const int* __restrict__ iin, const int* __restrict__ iout,
                                                          const uint16_t* __restrict__ ang, const uint16_t* __restrict__ freq,
                                                          const u32x2* __restrict__ m, const uint16_t* __restrict__ wdown,
                                                          float* __restrict__ agg, int T) {
    float w[16];
    load_w(wdown, w);
    const float fr = bf2f(freq[0]);
    for (long t = (long)blockIdx.x * 256 + threadIdx.x; t < T; t += (long)gridDim.x * 256) {
        const float c = cosf(fr * bf2f(ang[t]));
        uint32_t r[4];
        md_of(m[iin[t]], w, r);
        float* q = agg + (size_t)iout[t] * 4;
#pragma unroll
        for (int k = 0; k < 4; k++) atomicAdd(q + k, c * bf2f(r[k]));
    }
}
__global__ __launch_bounds__(256) void gen_final_kernel(const u32x2* __restrict__ m, const uint16_t* __restrict__ dist,
                                                        const uint16_t* __restrict__ centers, const uint16_t* __restrict__ wrbf,
                                                        const uint16_t* __restrict__ wsbf, const uint16_t* __restrict__ wup,
                                                        const float* __restrict__ agg, u32x2* __restrict__ out, int E) {
    const int e = blockIdx.x * 256 + threadIdx.x;
    if (e >= E) return;
    const float d = bf2f(dist[e]);
    float rs[4] = {0, 0, 0, 0}, rg[4] = {0, 0, 0, 0};
#pragma unroll 1
    for (int r = 0; r < 8; r++) {
        const float x = d - bf2f(centers[r]);
        const float rb = __expf(-10.0f * x * x);
#pragma unroll
        for (int k = 0; k < 4; k++) { rs[k] += rb * bf2f(wsbf[r * 4 + k]); rg[k] += rb * bf2f(wrbf[r * 4 + k]); }
    }
    float wu[16];
    load_w(wup, wu);
    const u32x2 mv = m[e];
    const float mm[4] = {bf2f(mv.x & 0xffff), bf2f(mv.x >> 16), bf2f(mv.y & 0xffff), bf2f(mv.y >> 16)};
    float g[4], o[4];
#pragma unroll
    for (int k = 0; k < 4; k++) g[k] = agg[(size_t)e * 4 + k] * rs[k];
#pragma unroll
    for (int k = 0; k < 4; k++) o[k] = mm[k] + (g[0] * wu[k] + g[1] * wu[4 + k] + g[2] * wu[8 + k] + g[3] * wu[12 + k]) * rg[k];
    const u32x2 ov = {f2bf(o[0]) | (f2bf(o[1]) << 16), f2bf(o[2]) | (f2bf(o[3]) << 16)};
    out[e] = ov;
}

extern "C" void kernel_launch(void* const* d_in, const int* in_sizes, int n_in,
                              void* d_out, int out_size, void* d_ws, size_t ws_size,
                              hipStream_t stream) {
    const u32x2* m = (const u32x2*)d_in[0];
    const int* iin = (const int*)d_in[1];
    const int* iout = (const int*)d_in[2];
    const uint16_t* dist = (const uint16_t*)d_in[3];
    const uint16_t* ang = (const uint16_t*)d_in[4];
    const uint16_t* centers = (const uint16_t*)d_in[5];
    const uint16_t* freq = (const uint16_t*)d_in[6];
    const uint16_t* wrbf = (const uint16_t*)d_in[7];
    const uint16_t* wsbf = (const uint16_t*)d_in[8];
    const uint16_t* wdown = (const uint16_t*)d_in[9];
    const uint16_t* wup = (const uint16_t*)d_in[10];
    const int E = in_sizes[3];
    const int T = in_sizes[1];
    char* ws = (char*)d_ws;
    const int G = T >> 2;
    const int ntile = (G + GR - 1) / GR;
    const int nstat = ntile / NWG - NDYN > 4 ? ntile / NWG - NDYN : 4;
    const int lcap = G / 6 + 2;
    char* recs = ws + W_RECS;
    size_t off = W_RECS;
    const size_t rbytes = (size_t)LBASE * 128 + (size_t)lcap * 130;
    off += rbytes;
    if (E > EMAX || (E & 3) || E < 1 || rbytes > 0x70000000u || off > ws_size) {
        (void)hipMemsetAsync(ws, 0, (size_t)E * 16, stream);
        gen_scatter_kernel<<<4096, 256, 0, stream>>>(iin, iout, ang, freq, m, wdown, (float*)ws, T);
        gen_final_kernel<<<(E + 255) / 256, 256, 0, stream>>>(m, dist, centers, wrbf, wsbf, wup, (const float*)ws, (u32x2*)d_out, E);
        return;
    }
    int ks = T / (NWG * NB * 24);
    if (ks > KMAX) ks = KMAX;
    const float zf = ZSIG * ZSIG * (float)T / (8.0f * (float)E * (float)E);
    (void)hipFuncSetAttribute((const void*)part_kernel, hipFuncAttributeMaxDynamicSharedMemorySize, P_LDS);
    (void)hipFuncSetAttribute((const void*)acc_kernel, hipFuncAttributeMaxDynamicSharedMemorySize, A_LDS);
    part_kernel<<<NWG, NT, P_LDS, stream>>>(iin, iout, ang, freq, m, wdown, recs, T, E, ntile, nstat, ks, lcap);
    acc_kernel<<<NWG, NT, A_LDS, stream>>>(ws, T, E, ks, lcap, zf, 0, centers, wrbf, wsbf, wup, m, dist, (u32x2*)d_out, iin, iout, ang, wdown, freq);
}
```

```cpp
#define REV
#include <hip/hip_runtime.h>
#include <hip/hip_bf16.h>
#include <stdint.h>
#pragma clang diagnostic ignored "-Winline-asm"

typedef unsigned int u32x4 __attribute__((ext_vector_type(4)));
typedef unsigned int u32x2 __attribute__((ext_vector_type(2)));
typedef unsigned short u16x2 __attribute__((ext_vector_type(2)));
typedef _Float16 f16x2 __attribute__((ext_vector_type(2)));
typedef __bf16 bf16x2 __attribute__((ext_vector_type(2)));
typedef float f32x2 __attribute__((ext_vector_type(2)));
typedef unsigned long long u64;

static __device__ __forceinline__ float bf2f(uint32_t bits16) { return __uint_as_float(bits16 << 16); }
static __device__ __forceinline__ uint32_t f2bf(float f) {
    uint32_t u = __float_as_uint(f);
    if ((u & 0x7fffffffu) > 0x7f800000u) return (u >> 16) | 0x40;
    return (u + 0x7fffu + ((u >> 16) & 1u)) >> 16;
}
static __device__ __forceinline__ float h2f(uint32_t bits16) { return (float)__builtin_bit_cast(_Float16, (unsigned short)bits16); }

#define NB 2048
#define WNULL 0x80000000u
#define CSTEP 0x2aabu
#define CINIT 205u
#define CHV (12u * CSTEP)
#define SLOTO 25088
#define SLOTI 12544
#define EMAX (SLOTO * 32)
#define NT 1024
#define NWG 256
#define KMAX 4
#define NTAIL 4
#define SC 64u
#define SKM (128u * SC + 2u)
#define SSM (4u * SKM + 1u)
#define SSH (128u * SC + 1u)
#define SKH (16u * SSH + 2u)
#define SQ (16u * SSM)
#define MAINL (4u * SQ)
#define BPL(b) ((((b) >> 3) & 3u) * SQ + ((b) & 7u) + (((b) >> 5) << 3))
#define TST 257u
#define TBASE MAINL
#define TPL (NB * TST)
#define SBASE (TBASE + 2u * TPL)
#define LBASE (SBASE + 128u * 2u * NWG * 9u)
#define NDYN 3
#define NEARM 0x55555554u
#define GR 2048
#define M_DONE 0
#define M_TILE (32 * 64)
#define M_LOG (M_TILE + 64)
#define M_MD (M_LOG + 64)
#define M_BEAT (M_MD + 64)
#define M_E0 (M_BEAT + 64)
#define M_WORDS (M_E0 + 64)
#define W_TCNT (M_WORDS * 4)
#define W_STAT (W_TCNT + NWG * NB)
#define W_MDC (W_STAT + 4 * NWG * 8)
#define W_PART (W_MDC + 128 * SLOTI * 4)
#define W_RECS (W_PART + 32 * 16 * SLOTO * 4)
#define AUX_NT 2
#define AUX_SC1 16

static __device__ __forceinline__ uint32_t row_max(uint32_t x) {
    x = max(x, (uint32_t)__builtin_amdgcn_update_dpp(0, x, 0x111, 0xf, 0xf, false));
    x = max(x, (uint32_t)__builtin_amdgcn_update_dpp(0, x, 0x112, 0xf, 0xf, false));
    x = max(x, (uint32_t)__builtin_amdgcn_update_dpp(0, x, 0x114, 0xf, 0xf, false));
    return max(x, (uint32_t)__builtin_amdgcn_update_dpp(0, x, 0x118, 0xf, 0xf, false));
}
static __device__ __forceinline__ uint32_t wave_max(uint32_t x) {
    x = row_max(x);
    x = max(x, (uint32_t)__builtin_amdgcn_update_dpp(0, x, 0x142, 0xa, 0xf, false));
    x = max(x, (uint32_t)__builtin_amdgcn_update_dpp(0, x, 0x143, 0xc, 0xf, false));
    return __builtin_amdgcn_readlane(x, 63);
}
static __device__ __forceinline__ float row_sum(float x) {
    x += __int_as_float(__builtin_amdgcn_update_dpp(0, __float_as_int(x), 0x111, 0xf, 0xf, false));
    x += __int_as_float(__builtin_amdgcn_update_dpp(0, __float_as_int(x), 0x112, 0xf, 0xf, false));
    x += __int_as_float(__builtin_amdgcn_update_dpp(0, __float_as_int(x), 0x114, 0xf, 0xf, false));
    return x + __int_as_float(__builtin_amdgcn_update_dpp(0, __float_as_int(x), 0x118, 0xf, 0xf, false));
}
static __device__ __forceinline__ float wave_sum(float x) {
    x = row_sum(x);
    x += __int_as_float(__builtin_amdgcn_update_dpp(0, __float_as_int(x), 0x142, 0xa, 0xf, false));
    x += __int_as_float(__builtin_amdgcn_update_dpp(0, __float_as_int(x), 0x143, 0xc, 0xf, false));
    return __int_as_float(__builtin_amdgcn_readlane(__float_as_int(x), 63));
}
static __device__ __forceinline__ uint32_t bfi(uint32_t mask, uint32_t a, uint32_t b) {
    return __builtin_amdgcn_bitop3_b32(mask, a, b, 0xca);
}
static __device__ __forceinline__ void opaque(int& x) { asm volatile("" : "+v"(x)); }
template <uint32_t C> static __device__ __forceinline__ uint32_t vconst() {
    uint32_t r;
    asm volatile("v_mov_b32 %0, %1" : "=v"(r) : "n"(C));
    return r;
}
static __device__ __forceinline__ uint32_t pk_bf16(float a, float b) {
    const f32x2 v = {a, b};
    return __builtin_bit_cast(uint32_t, __builtin_convertvector(v, bf16x2));
}
static __device__ __forceinline__ void pin(uint32_t& x) { asm volatile("" : "+v"(x)); }

#define RTS(k)

static __device__ __forceinline__ void md_of(u32x2 v, const float* w, uint32_t* r) {
    float a0 = bf2f(v.x & 0xffff), a1 = bf2f(v.x >> 16), a2 = bf2f(v.y & 0xffff), a3 = bf2f(v.y >> 16);
#pragma unroll
    for (int j = 0; j < 4; j++) r[j] = f2bf(a0 * w[j] + a1 * w[4 + j] + a2 * w[8 + j] + a3 * w[12 + j]);
}
static __device__ __forceinline__ void load_w(const uint16_t* __restrict__ wdown, float* w) {
    u32x4 wv0 = *(const u32x4*)wdown, wv1 = *(const u32x4*)(wdown + 8);
#pragma unroll
    for (int i = 0; i < 4; i++) {
        w[2 * i] = bf2f(wv0[i] & 0xffffu); w[2 * i + 1] = bf2f(wv0[i] >> 16);
        w[8 + 2 * i] = bf2f(wv1[i] & 0xffffu); w[8 + 2 * i + 1] = bf2f(wv1[i] >> 16);
    }
}
#define LDS __attribute__((address_space(3)))
#define P_CNT 0
#define P_FLAG ((NB + 64) * 4)
#define P_LST (P_FLAG + 256)
#define LCAP 32
#define P_TAB (P_LST + 4 * LCAP * 16)
#define P_BUF (P_TAB + NB * 4)
#define P_LDS (P_BUF + NB * 64)
#define PD 2
#define LPOS 0x448763
#define SETP(n) __builtin_amdgcn_s_setprio(n)
#define MDL 3
#define MAPT(t) ((t) < ntile ? ntile - 1 - (t) : (t))
#define RMAXREV 40.0f
#define HB 88
#define HREG(S, D) (HB + ((S) & 3) * 10 + (((S) >> 2) + (D)) * 2)

template <int N> struct IC { static constexpr int value = N; };
struct Raw { u32x4 o[2], i[2]; u32x2 a[2]; };
static __device__ __forceinline__ uint32_t lds_ld(uint32_t a) { return *(const LDS uint32_t*)(uintptr_t)a; }
static __device__ __forceinline__ void lds_st(uint32_t a, uint32_t v) { *(LDS uint32_t*)(uintptr_t)a = v; }
static __device__ __forceinline__ void lds_or(uint32_t a, uint32_t v) {
    __hip_atomic_fetch_or((LDS uint32_t*)(uintptr_t)a, v, __ATOMIC_RELAXED, __HIP_MEMORY_SCOPE_WORKGROUP);
}
static __device__ __forceinline__ uint32_t lds_add(uint32_t a, uint32_t v) {
    return __hip_atomic_fetch_add((LDS uint32_t*)(uintptr_t)a, v, __ATOMIC_RELAXED, __HIP_MEMORY_SCOPE_WORKGROUP);
}
static __device__ __forceinline__ uint32_t slot_w(uint32_t ca, uint32_t cv) {
    return (ca << 4) | __builtin_amdgcn_bitop3_b32(cv >> 11, ca, 0x3cu, 0x28);
}
static __device__ __forceinline__ uint32_t slot_x2(uint32_t aw, uint32_t ca) {
    return __builtin_amdgcn_bitop3_b32(aw, ca, 0xcu, 0x72);
}
static __device__ __forceinline__ uint32_t slot_x(uint32_t aw) { return slot_x2(aw, aw >> 4); }
template <int R> static __device__ __forceinline__ u32x2 hget() {
    u32x2 d;
    asm volatile("v_mov_b64 %0, v[%1:%2]" : "=v"(d) : "n"(R), "n"(R + 1));
    return d;
}
static __device__ __forceinline__ u32x2 hget_at(int r) {
    u32x2 d;
    asm volatile("s_set_gpr_idx_on %2, gpr_idx(SRC0)\n\ts_nop 1\n\tv_mov_b32 %0, v88\n\tv_mov_b32 %1, v89\n\ts_set_gpr_idx_off"
                 : "=&v"(d.x), "=&v"(d.y) : "s"(r));
    return d;
}
static __device__ __forceinline__ void hset_at(int r, u32x2 d) {
    asm volatile("s_set_gpr_idx_on %2, gpr_idx(DST)\n\ts_nop 1\n\tv_mov_b32 v88, %0\n\tv_mov_b32 v89, %1\n\ts_set_gpr_idx_off"
                 :: "v"(d.x), "v"(d.y), "s"(r));
}

template <int S, int D> static __device__ __forceinline__ uint32_t take(uint32_t V, uint32_t u16, uint32_t la, u32x2 nul) {
    const uint32_t a = __builtin_amdgcn_bitop3_b32(V, u16, 15u, 0x54);
    asm volatile("v_mov_b64 v[%4:%4+1], v[%3:%3+1]\n\tv_cmpx_ne_u32 vcc, -16, %0\n\tds_wrxchg_rtn_b64 v[%4:%4+1], %1, %2 offset:%5\n\ts_mov_b64 exec, -1"
                 :: "v"(a), "v"(la), "v"(nul), "n"(HREG(S, D)), "n"(HREG(S, 1 - D)), "n"(S * 256) : "memory", "vcc");
    return a;
}
template <int S, int W> static __device__ __forceinline__ void put(u32x4 rsA, uint32_t ad, uint32_t pm) {
    asm volatile("s_bitcmp1_b32 %3, %4\n\ts_cbranch_scc0 1f\n\ts_waitcnt lgkmcnt(%5)\n\tbuffer_store_dwordx4 v[%2:%2+3], %0, %1, 0 offen sc1\n"
                 "1:\n\ts_bitcmp1_b32 %3, %4+1\n\ts_cbranch_scc0 2f\n\ts_waitcnt lgkmcnt(%5)\n\tbuffer_store_dwordx4 v[%2:%2+3], %0, %1, 0 offen nt\n\ts_nop 1\n2:"
                 :: "v"(ad), "s"(rsA), "n"(HREG(S, 0)), "s"(pm), "n"(2 * S), "n"(W) : "memory", "scc");
}

__global__ __launch_bounds__(NT) __attribute__((amdgpu_num_vgpr(HB / 2))) void part_kernel(const int* __restrict__ iin, const int* __restrict__ iout,
                                                  const uint16_t* __restrict__ ang, const uint16_t* __restrict__ freq,
                                                  const u32x2* __restrict__ m, const uint16_t* __restrict__ wdown,
                                                  char* __restrict__ recs,
                                                  int T, int E, int ntile, int nstat, int ks, int lcap) {
    RTS(0)
    const int tid0 = threadIdx.x;
    const int wv = __builtin_amdgcn_readfirstlane(tid0 >> 6);
    const int G = T >> 2;
    const int pos = (int)((blockIdx.x & 7) << 5) | (int)(blockIdx.x >> 3);
    float frr;
    unsigned* meta = (unsigned*)(recs - W_RECS);
    const uint32_t gbase = (LBASE + (uint32_t)lcap) * 128u;
    const uint32_t rbytes = gbase + (uint32_t)lcap * 2u;
    const __amdgpu_buffer_rsrc_t rsR = __builtin_amdgcn_make_buffer_rsrc(recs, 0, (int)rbytes, 0x00020000);
    const u32x4 rsA = {(uint32_t)(uintptr_t)recs, (uint32_t)((uintptr_t)recs >> 32) & 0xffffu, rbytes, 0x00020000u};
    const uint32_t g2 = 2u * (uint32_t)ks;
    const uint32_t far = 1u >> ((blockIdx.x >> 3) & 3u);
    const uint32_t SK = SKM + far * (SKH - SKM), SS = SSM + far * (SSH - SSM), UINC = 0x80000001u + SK * 64u;
    const uint32_t spec0 = gbase - 4u;
    auto fixed_tile = [&](int r) __attribute__((always_inline)) -> int { return r * NWG + ((pos + r * 37) & (NWG - 1)); };
    int tl0 = fixed_tile(0), tl1 = fixed_tile(1), tl2;
    uint32_t tk = 0u;
    const __amdgpu_buffer_rsrc_t rsO = __builtin_amdgcn_make_buffer_rsrc((void*)iout, 0, G * 16, 0x00020000);
    const __amdgpu_buffer_rsrc_t rsI = __builtin_amdgcn_make_buffer_rsrc((void*)iin, 0, G * 16, 0x00020000);
    const __amdgpu_buffer_rsrc_t rsG = __builtin_amdgcn_make_buffer_rsrc((void*)ang, 0, G * 8, 0x00020000);
    uint32_t t8 = (uint32_t)tid0 * 8u;
    auto load_one = [&](Raw& r, int tl, int idx) __attribute__((always_inline)) {
        const int j = idx < 4 ? idx >> 1 : idx & 1;
        const uint32_t g0 = (uint32_t)(MAPT(tl) * GR + j * NT);
        __builtin_amdgcn_sched_barrier(0);
        if (idx >= 4) r.a[j] = __builtin_amdgcn_raw_buffer_load_b64(rsG, t8, g0 * 8u, AUX_NT);
        else if (idx & 1) r.i[j] = __builtin_amdgcn_raw_buffer_load_b128(rsI, t8 + t8, g0 * 16u, AUX_NT);
        else r.o[j] = __builtin_amdgcn_raw_buffer_load_b128(rsO, t8 + t8, g0 * 16u, AUX_NT);
        __builtin_amdgcn_sched_barrier(0);
    };
    auto peek = [&](uint32_t at) __attribute__((always_inline)) -> uint32_t {
        return __hip_atomic_load((unsigned*)((char*)meta + at), __ATOMIC_RELAXED, __HIP_MEMORY_SCOPE_AGENT);
    };
    auto ready = [&]() __attribute__((always_inline)) {
        if (blockIdx.x != 0) {
            const uint32_t x = lds_ld(vconst<P_FLAG + 8>());
            while (peek(vconst<M_BEAT * 4>()) == x) __builtin_amdgcn_s_sleep(8);
        }
    };
    auto beat = [&]() __attribute__((always_inline)) {
        (void)__hip_atomic_fetch_add((unsigned*)((char*)meta + vconst<M_BEAT * 4>()), vconst<1>(), __ATOMIC_RELAXED, __HIP_MEMORY_SCOPE_AGENT);
    };
    auto draw = [&](uint32_t at) __attribute__((always_inline)) -> uint32_t {
        return __hip_atomic_fetch_add((unsigned*)((char*)meta + at), vconst<1>(), __ATOMIC_RELAXED, __HIP_MEMORY_SCOPE_AGENT);
    };
    uint32_t la;
    auto tab_at = [&]() __attribute__((always_inline)) -> uint32_t {
        return P_TAB + bfi(0xffu, bfi(0x3cu, t8 >> 2, t8 << 4), t8);
    };
    u32x2 nul;
    uint32_t U[2];
    {
        const uint32_t t = (uint32_t)tid0;
        la = P_BUF + ((t >> 5) << 12) + ((t & 0x18u) << 3) + ((t & 3u) << 4) + ((t & 4u) << 1);
        const uint32_t q = ((t >> 3) ^ (t >> 1 & 2u)) & 3u;
        nul.x = (0x7u >> q) << 31; nul.y = (0xbu >> q) << 31;
        U[0] = (((t >> 1) & 15u) * SS + ((t & 1u) * 64u + (t >> 5)) * SC + BPL(blockIdx.x)) * 128u + (0x80000000u - SK * 64u) + 8u - g2;
        U[1] = U[0] + 32u * SC * 128u;
    }
    auto log_line = [&](bool lg, uint32_t b) __attribute__((always_inline)) -> uint32_t {
        uint32_t lp = 0u;
        if (lg && (t8 & 0x38u) == 0u) {
            ready();
            lp = draw(vconst<M_LOG * 4>());
            __builtin_amdgcn_raw_buffer_store_b16((uint16_t)b, rsR, gbase + lp * 2u, 0, 0);
        }
        return LBASE + (uint32_t)__builtin_amdgcn_ds_bpermute((int)((t8 >> 1) & 0xe0u), (int)lp);
    };
    auto general = [&](int D, bool all, u64 sm) __attribute__((always_inline)) {
        uint32_t todo = __builtin_amdgcn_readfirstlane((uint32_t)sm | (uint32_t)(sm >> 32));
#pragma unroll 1
        while (todo) {
            const uint32_t S = (uint32_t)__builtin_ctz(todo) >> 1;
            todo &= ~(3u << (2u * S));
            pin(t8); pin(la);
            const uint32_t V = lds_ld(P_TAB + (t8 & ~63u) + S * 4u), g = V & 15u;
            const uint32_t col = S * 128u + ((t8 >> 1) & 0x60u) + (t8 >> 8);
            const bool act = all ? V != 0xffffffffu : (V & 0x80000009u) == 9u;
            const int hr = HREG((int)S, D) - HB;
            uint32_t ln = log_line(act && g > 8u + NTAIL, col);
            if (act) {
                const u32x2 N = *(const LDS u32x2*)(uintptr_t)(la + S * 256u);
                *(LDS u32x2*)(uintptr_t)(la + S * 256u) = nul;
                lds_st(P_TAB + (t8 & ~63u) + S * 4u, vconst<0xffffffffu>());
                if (!(g & 1u)) hset_at(hr, N);
                else {
                    uint32_t a, st;
                    if (g < 8u) { a = (V & ~15u) + ((t8 & 0x38u) << 1); st = 8u; }
                    else {
                        if (g <= 8u + NTAIL) ln = TBASE + ((g - 8u) >> 1) * TPL + col * TST + blockIdx.x;
                        a = ln * 128u + (la & 0x38u); st = 64u;
                    }
                    __builtin_amdgcn_raw_buffer_store_b64(hget_at(hr), rsR, a, 0, AUX_SC1);
                    __builtin_amdgcn_raw_buffer_store_b64(N, rsR, a + st, 0, AUX_SC1);
                }
            }
        }
    };
    unsigned epoch = 0;
    u64 pb2 = 0ull;
    u64 fm = 0ull, sm = 0ull;
    auto look = [&](u32x2 c) __attribute__((always_inline)) {
        uint32_t V[2];
        const bool late = __builtin_amdgcn_ballot_w64(((U[0] | U[1]) & 8u) != 0u) != 0ull;
        epoch++;
        asm volatile("v_mov_b32 %0, -1\n\tv_mov_b32 %1, -1\n\t"
                     "v_cmpx_le_u32 %6, %7, %2\n\tv_mov_b32 %0, %4\n\tv_add_u32 %4, %9, %4\n\tv_subrev_u32 %2, %8, %2\n\ts_mov_b64 exec, -1\n\t"
                     "v_cmpx_le_u32 vcc, %8, %3\n\tv_mov_b32 %1, %5\n\tv_add_u32 %5, %9, %5\n\tv_subrev_u32 %3, %8, %3\n\t"
                     "s_or_b64 %6, %6, exec\n\ts_mov_b64 exec, -1"
                     : "=&v"(V[0]), "=&v"(V[1]), "+v"(c.x), "+v"(c.y), "+v"(U[0]), "+v"(U[1]), "=&s"(fm) : "s"(CHV), "n"(CHV), "s"(UINC) : "vcc", "scc");
        *(LDS u32x2*)(uintptr_t)(P_CNT + t8) = c;
        pin(t8);
        const uint32_t tw = tab_at();
        lds_st(tw, V[0]);
        lds_st(tw + 64u, V[1]);
        if (wv == 1) lds_st(P_CNT + NB * 4 - 256 + (t8 >> 1), vconst<0x40000000u>());
        const u64 pb = __builtin_amdgcn_ballot_w64((int)(V[0] & V[1]) >= 0);
        pb2 = pb;
        sm = 0ull;
        if (__builtin_expect(late, 0)) {
            sm = __builtin_amdgcn_ballot_w64((V[0] & 0x80000009u) == 9u || (V[1] & 0x80000009u) == 9u);
#pragma unroll
            for (int e = 0; e < 2; e++)
                if ((V[e] & 0x8000000fu) == 9u + NTAIL) U[e] -= 2u * UINC;
        }
    };
    u32x4 Va, Vb;

    Raw raw[PD];
    {
        const int tid = tid0;
        asm volatile("" ::: "v88", "v127");
        const uint16_t fq = freq[0];
        if (tid == 128) tk = peek(vconst<M_BEAT * 4>());
        if (blockIdx.x == 0 && wv == 3 && (tid & 63) < M_BEAT / 64) {
            __hip_atomic_store(meta + (tid & 63) * 64, 0u, __ATOMIC_RELAXED, __HIP_MEMORY_SCOPE_AGENT);
            __hip_atomic_store(meta + (tid & 63) * 64 + 1, 0u, __ATOMIC_RELAXED, __HIP_MEMORY_SCOPE_AGENT);
        }
        const __amdgpu_buffer_rsrc_t rsE = __builtin_amdgcn_make_buffer_rsrc((void*)m, 0, E * 8, 0x00020000);
        u32x2 mv[5];
        mv[4] = __builtin_amdgcn_raw_buffer_load_b64(rsE, (t8 & 511u) * (uint32_t)(E >> 6), 0, 0);
#pragma unroll
        for (int q = 0; q < 4; q++)
            mv[q] = __builtin_amdgcn_raw_buffer_load_b64(rsE, t8 & 511u, ((blockIdx.x >> 2) * 256u + (blockIdx.x & 3u) * 16u + (uint32_t)wv + 64u * q) * 512u, AUX_NT);
#pragma unroll
        for (int d = 0; d < MDL; d++) load_one(raw[1], tl0, d);
        const u32x2 ci = {CINIT, CINIT};
        *(LDS u32x2*)(uintptr_t)(P_CNT + tid * 8) = ci;
        if (tid < 64) lds_st(P_CNT + (NB + tid) * 4, 0x40000000u);
        if (tid < 8 && tid != 2) lds_st(P_FLAG + tid * 4, tid == 0 ? 0xffffffffu : 0u);
        if (tid == 128) lds_st(P_FLAG + 8, tk);
        asm("s_nop 0\n\tv_readfirstlane_b32 %0, %1" : "=s"(frr) : "v"(bf2f(fq) * 0.15915494309189535f));
        const uint32_t lane = (uint32_t)tid & 63u;
        int e0[4];
        if (blockIdx.x < SLOTI / 64) {
            float w[16];
            load_w(wdown, w);
            auto dot = [&](u32x2 v, int c) __attribute__((always_inline)) -> f32x2 {
                const f32x2 w0 = {w[c], w[c + 1]}, w1 = {w[4 + c], w[5 + c]}, w2 = {w[8 + c], w[9 + c]}, w3 = {w[12 + c], w[13 + c]};
                return w0 * bf2f(v.x & 0xffffu) + w1 * bf2f(v.x >> 16) + w2 * bf2f(v.y & 0xffffu) + w3 * bf2f(v.y >> 16);
            };
#pragma unroll
            for (int cp = 0; cp < 2; cp++) {
                const f32x2 sd = dot(mv[4], 2 * cp);
#pragma unroll
                for (int e = 0; e < 2; e++) {
                    const int ex = (int)(wave_max(__float_as_uint(sd[e]) & 0x7fffffffu) >> 23);
                    e0[2 * cp + e] = __builtin_amdgcn_readfirstlane(ex == 0 ? 0 : min(max(ex - 129, -100), 100));
                }
                const f32x2 sc = {__uint_as_float((uint32_t)(127 - e0[2 * cp]) << 23), __uint_as_float((uint32_t)(127 - e0[2 * cp + 1]) << 23)};
                u32x4 o;
                u16x2 mx = {0, 0};
                f32x2 sq = {0.f, 0.f};
#pragma unroll
                for (int q = 0; q < 4; q++) {
                    const f32x2 y = dot(mv[q], 2 * cp) * sc;
                    const uint32_t hb = pk_bf16(y.x, y.y);
                    const f32x2 x = {__uint_as_float(hb << 16), __uint_as_float(hb & 0xffff0000u)};
                    o[q] = __builtin_bit_cast(uint32_t, __builtin_amdgcn_cvt_pkrtz(x.x, x.y));
                    mx = __builtin_elementwise_max(mx, __builtin_bit_cast(u16x2, hb & 0x7fff7fffu));
                    sq += x * x;
                }
                *(LDS u32x4*)(uintptr_t)(P_BUF + (cp * 64u + lane) * 272u + wv * 16) = o;
#pragma unroll
                for (int e = 0; e < 2; e++) {
                    const u32x2 st = {wave_max(mx[e]), __float_as_uint(wave_sum(sq[e]))};
                    if (lane == 0u) *(LDS u32x2*)(uintptr_t)(P_LST + ((2 * cp + e) * 16 + wv) * 8) = st;
                }
            }
        }
#pragma unroll
        for (int d = MDL; d < 6; d++) load_one(raw[1], tl0, d);
#pragma unroll
        for (int d = 0; d < 6; d++)
            if (((LPOS >> (4 * d)) & 15) < 4) load_one(raw[0], tl1, d);
        __syncthreads();
        u32x4 mo[2];
#pragma unroll
        for (int cp = 0; cp < 2; cp++) mo[cp] = *(const LDS u32x4*)(uintptr_t)(P_BUF + (cp * 64u + (t8 >> 7)) * 272u + ((t8 + t8) & 0xf0u));
        if (blockIdx.x == 0 && wv == 3) asm volatile("s_waitcnt vmcnt(0)" ::: "memory");
        __syncthreads();
#pragma unroll
        for (int s = 0; s < 16; s++) *(LDS u32x2*)(uintptr_t)(la + s * 256) = nul;
        asm volatile("" : "+v"(raw[1].o[0]), "+v"(raw[1].o[1]), "+v"(raw[1].i[0]), "+v"(raw[1].i[1]), "+v"(raw[1].a[0]), "+v"(raw[1].a[1]));
        if (blockIdx.x < SLOTI / 64) {
            const __amdgpu_buffer_rsrc_t rsM = __builtin_amdgcn_make_buffer_rsrc((void*)(meta + W_MDC / 4), 0, (int)(128u * SLOTI * 4u), 0x00020000);
#pragma unroll
            for (int cp = 0; cp < 2; cp++) {
                const uint32_t row = cp * 64u + (t8 >> 7), c16 = (t8 + t8) & 0xf0u;
                __builtin_amdgcn_raw_buffer_store_b128(mo[cp], rsM, row * (SLOTI * 4u) + c16 + blockIdx.x * 256u, 0, AUX_SC1);
            }
            if (wv == 0) {
                const u32x2 st = *(const LDS u32x2*)(uintptr_t)(P_LST + t8);
                const u32x2 r = {row_max(st.x) | (row_max(st.x ? 0xffffu - st.x : 0u) << 16), __float_as_uint(row_sum(__uint_as_float(st.y)))};
                if ((lane & 15u) == 15u) ((u32x2*)(meta + W_STAT / 4))[(lane >> 4) * NWG + blockIdx.x] = r;
                if (blockIdx.x == 0 && lane < 4u) meta[M_E0 + lane] = (uint32_t)(lane == 0u ? e0[0] : lane == 1u ? e0[1] : lane == 2u ? e0[2] : e0[3]);
            }
        }
    }
    pin(la);
    RTS(5)

    uint32_t w[8], aw[8], xv[8];
    u64 nm[8];
    auto later2 = [&](int k) __attribute__((always_inline)) {
        uint32_t x0, x1;
        asm volatile("v_lshrrev_b32 %0, 4, %2\n\tv_lshrrev_b32 %1, 4, %5\n\tv_bitop3_b32 %0, %2, %0, 12 bitop3:0x72\n\tv_bitop3_b32 %1, %5, %1, 12 bitop3:0x72\n\t"
                     "s_not_b64 exec, %8\n\tds_write_b32 %2, %3 offset:%10\n\tds_or_b32 %0, %4 offset:%10\n\t"
                     "s_not_b64 exec, %9\n\tds_write_b32 %5, %6 offset:%10\n\tds_or_b32 %1, %7 offset:%10\n\ts_mov_b64 exec, -1"
                     : "=&v"(x0), "=&v"(x1)
                     : "v"(aw[k]), "v"(w[k]), "v"(xv[k]), "v"(aw[k + 1]), "v"(w[k + 1]), "v"(xv[k + 1]), "s"(nm[k]), "s"(nm[k + 1]), "n"(P_BUF) : "memory", "scc");
    };
    auto drain = [&]() __attribute__((always_inline)) {
        if (wv == 1) {
            const uint32_t q = (epoch + 1u) & 3u;
            const uint32_t n = __builtin_amdgcn_readfirstlane(lds_ld(vconst<P_FLAG + 16>() + q * 4u));
            if (__builtin_expect(n != 0u, 0)) {
                pin(t8);
                const uint32_t l = (t8 >> 3) & 63u;
                if (l < min(n, (uint32_t)LCAP)) {
                    const u32x4 e = *(const LDS u32x4*)(uintptr_t)(P_LST + (q * LCAP + l) * 16u);
                    lds_st(P_BUF + e.x, e.y);
                    lds_or(P_BUF + slot_x(e.x), e.z);
                }
                lds_st(vconst<P_FLAG + 16>() + q * 4u, vconst<0>());
            }
        }
    };
    auto again = [&](int D) __attribute__((always_inline)) {
        do {
            general(D, true, fm);
            __syncthreads();
            later2(0); later2(2); later2(4); later2(6);
            drain();
#pragma unroll
            for (int k = 0; k < 8; k++) {
                pin(aw[k]);
                bool due = false;
                if (aw[k] & 0x80000000u) {
                    xv[k] -= CHV;
                    if (xv[k] < CHV) {
                        const uint32_t f = (aw[k] >> 16) & 0x3ffu;
                        aw[k] = slot_w(aw[k] & 0xffffu, xv[k]);
                        xv[k] = f << ((xv[k] >> 8) & 31u);
                        due = true;
                    } else lds_st(vconst<P_FLAG>(), epoch + 1u);
                }
                nm[k] = ~__builtin_amdgcn_ballot_w64(due);
            }
            look(*(const LDS u32x2*)(uintptr_t)(P_CNT + t8));
            asm volatile("s_waitcnt lgkmcnt(0)" ::: "memory");
            __syncthreads();
        } while (__builtin_amdgcn_readfirstlane(lds_ld(vconst<P_FLAG>())) == epoch);
    };
    auto round = [&](auto DD, auto LL, Raw& rc, Raw& rn, int rd) __attribute__((always_inline)) {
        constexpr int D = decltype(DD)::value;
        constexpr bool LAST = decltype(LL)::value == 1, FIRST = decltype(LL)::value == 2;
        const int ng = min(GR, G - MAPT(tl0) * GR);
        auto hook = [&](int p) __attribute__((always_inline)) {
#pragma unroll
            for (int q = 0; q < 6; q++)
            {
                const int lp = (LPOS >> (4 * q)) & 15;
                if (!LAST && lp < 4 && lp == p) load_one(rc, tl2, q);
                if (!LAST && lp >= 4 && (lp < 8 || lp >= 12 ? lp : lp - 8) == p) load_one(rn, tl1, q);
            }
        };
        uint32_t wn[8], ca[8], px[4], cv[8];
        float mx;
        float cs[2];
        auto convA = [&](int q) __attribute__((always_inline)) {
            if (LAST) return;
            const uint32_t a = rc.a[q >> 1][q & 1];
            const f32x2 av = {__uint_as_float(a << 16), __uint_as_float(a & 0xffff0000u)};
            f32x2 rv;
            asm("v_pk_mul_f32 %0, %1, %2 op_sel_hi:[1,0]" : "=v"(rv) : "v"(av), "s"((u64)__float_as_uint(frr)));
            mx = __builtin_elementwise_maximum(__builtin_elementwise_maximum(__builtin_fabsf(rv.x), __builtin_fabsf(rv.y)), (q & 1) ? mx : 0.f);
            cs[0] = __builtin_amdgcn_cosf(rv.x); cs[1] = __builtin_amdgcn_cosf(rv.y);
            asm volatile("" : "+v"(mx), "+v"(cs[0]), "+v"(cs[1]));
            __builtin_amdgcn_sched_barrier(0);
        };
        auto convB = [&](int k) __attribute__((always_inline)) {
            if (LAST) return;
            const uint32_t o = rc.o[k >> 2][k & 3], i = rc.i[k >> 2][k & 3];
            wn[k] = (__builtin_amdgcn_alignbit(bfi(63u, o, o >> 5), __float_as_uint(cs[k & 1]), 30u) << 14) | (i >> 6);
            ca[k] = (bfi(63u, i, o) << 2) & 0x1ffcu;
            pin(wn[k]); pin(ca[k]);
            __builtin_amdgcn_sched_barrier(0);
        };
        auto convD = [&](int q) __attribute__((always_inline)) {
            if (LAST) return;
            asm volatile("v_cvt_pk_f16_f32 %0, |%1|, |%2|\n\tv_pk_mul_f16 %0, %0, %3 op_sel_hi:[1,0]" : "=v"(px[q]) : "v"(cs[0]), "v"(cs[1]), "s"(0x3ffu));
        };
        uint32_t cst;
        auto check = [&](int j) __attribute__((always_inline)) {
            if (LAST) return;
            if (__builtin_expect((__builtin_amdgcn_ballot_w64(!(mx <= RMAXREV)) | (u64)(uint32_t)(GR - ng)) != 0ull, 0)) {
                pin(t8);
                asm volatile("" : "+v"(rc.a[j]));
                const int lg = j * NT + (int)(t8 >> 3);
#pragma unroll
                for (int k = 0; k < 4; k++) {
                    const uint32_t a = (k & 1) ? (rc.a[j][k >> 1] >> 16) : (rc.a[j][k >> 1] & 0xffffu);
                    const bool nrm = __builtin_fabsf(frr * bf2f(a)) <= RMAXREV;
                    if (!(lg < ng && nrm)) ca[j * 4 + k] = NB * 4u + ((t8 >> 1) & 0xfcu);
                    if (lg < ng && !nrm) {
                        ready();
                        __builtin_amdgcn_raw_buffer_store_b32((uint32_t)((MAPT(tl0) * GR + lg) * 4 + k), rsR, spec0 - draw(vconst<M_LOG * 4 + 4>()) * 4u, 0, 0);
                    }
                }
            }
            cst = vconst<CSTEP>();
        };
        auto ticket = [&](int k) __attribute__((always_inline)) {
            if (LAST) return;
            cv[k] = lds_add(P_CNT + ca[k], cst);
            __builtin_amdgcn_sched_barrier(0);
        };
        auto insert = [&](int j) __attribute__((always_inline)) {
            const int k0 = 4 * j;
            const bool odd = __builtin_amdgcn_ballot_w64(max(max(cv[k0], cv[k0 + 1]), max(cv[k0 + 2], cv[k0 + 3])) >= 2u * CHV) != 0ull;
#pragma unroll
            for (int k = k0; k < k0 + 4; k += 2) {
                uint32_t x0, x1;
                asm volatile("v_lshrrev_b32 %6, 11, %8\n\tv_lshrrev_b32 %7, 11, %9\n\t"
                             "v_bitop3_b32 %6, %6, %10, 60 bitop3:0x28\n\tv_bitop3_b32 %7, %7, %11, 60 bitop3:0x28\n\t"
                             "v_lshl_or_b32 %0, %10, 4, %6\n\tv_lshl_or_b32 %1, %11, 4, %7\n\t"
                             "v_lshlrev_b32_sdwa %2, %8, %12 dst_sel:DWORD dst_unused:UNUSED_PAD src0_sel:BYTE_1 src1_sel:WORD_0\n\t"
                             "v_lshlrev_b32_sdwa %3, %9, %12 dst_sel:DWORD dst_unused:UNUSED_PAD src0_sel:BYTE_1 src1_sel:WORD_1\n\t"
                             "v_bitop3_b32 %6, %0, %10, 12 bitop3:0x72\n\tv_bitop3_b32 %7, %1, %11, 12 bitop3:0x72\n\t"
                             "v_cmpx_gt_u32 %4, %15, %8\n\tds_write_b32 %0, %13 offset:%16\n\tds_or_b32 %6, %2 offset:%16\n\ts_mov_b64 exec, -1\n\t"
                             "v_cmpx_gt_u32 %5, %15, %9\n\tds_write_b32 %1, %14 offset:%16\n\tds_or_b32 %7, %3 offset:%16\n\ts_mov_b64 exec, -1"
                             : "=&v"(aw[k]), "=&v"(aw[k + 1]), "=&v"(xv[k]), "=&v"(xv[k + 1]), "=&s"(nm[k]), "=&s"(nm[k + 1]), "=&v"(x0), "=&v"(x1)
                             : "v"(cv[k]), "v"(cv[k + 1]), "v"(ca[k]), "v"(ca[k + 1]), "v"(px[k >> 1]), "v"(wn[k]), "v"(wn[k + 1]), "s"(CHV), "n"(P_BUF) : "memory");
                w[k] = wn[k]; w[k + 1] = wn[k + 1];
                if (__builtin_expect(odd, 0)) {
#pragma unroll
                    for (int q = k; q < k + 2; q++) {
                        nm[q] |= __builtin_amdgcn_ballot_w64(cv[q] >= 2u * CHV);
                        if (cv[q] >= 2u * CHV && cv[q] < 0x40000000u) {
                            const uint32_t lq = (epoch + 2u) & 3u;
                            uint32_t p = LCAP;
                            if (cv[q] < 3u * CHV) p = lds_add(vconst<P_FLAG + 16>() + lq * 4u, vconst<1>());
                            if (p < LCAP) {
                                const u32x2 e = {aw[q], w[q]};
                                *(LDS u32x2*)(uintptr_t)(P_LST + (lq * LCAP + p) * 16u) = e;
                                lds_st(P_LST + (lq * LCAP + p) * 16u + 8u, xv[q]);
                            } else {
                                aw[q] = ca[q] | ((q & 1) ? (px[q >> 1] & 0xffff0000u) : (px[q >> 1] << 16)) | 0x80000000u;
                                xv[q] = cv[q] - CHV;
                                lds_st(vconst<P_FLAG>(), epoch);
                            }
                        }
                    }
                }
            }
        };
        constexpr int P0 = D ? 0 : 3, P1 = D ? 1 : 2, P2 = D ? 2 : 1, P3 = D ? 3 : 0;
        const uint32_t fl = lds_ld(vconst<P_FLAG>());
        if (!LAST) asm volatile("" : "+v"(rc.o[0]), "+v"(rc.o[1]), "+v"(rc.i[0]), "+v"(rc.i[1]), "+v"(rc.a[0]), "+v"(rc.a[1]));
        if (!LAST && (rd + 4 >= nstat || tl1 >= ntile) && wv == 2) {
            pin(tk);
            if ((t8 & 511u) == 0u) {
                if (tl1 >= ntile) {
                    if (rd + 3 <= nstat) ready();
                    (void)draw(vconst<M_MD * 4>());
                } else if (rd + 4 == nstat) tk = peek(vconst<M_BEAT * 4>());
                else {
                    if (rd + 3 == nstat) {
                        if (tk == lds_ld(vconst<P_FLAG + 8>())) ready();
                        tk = 0u;
                    }
                    lds_st(vconst<P_FLAG + 4>(), tk);
                    if ((int)tk < ntile - nstat * NWG) tk = draw(vconst<M_TILE * 4>());
                }
            }
        }
        if (!LAST && blockIdx.x == 0 && wv == 3 && (t8 & 511u) == 0u) beat();
        hook(12);
        pin(t8);
        const uint32_t tr = P_TAB + (t8 & ~63u), u16 = (t8 + t8) & 0x70u;
        convA(0);
        convB(0);
        const bool ag = __builtin_amdgcn_readfirstlane(fl) == epoch;
        if (!FIRST && __builtin_expect(LAST || ag || sm != 0ull, 0)) {
            if (ag) again(D);
            general(D, false, sm);
            Va = *(const LDS u32x4*)(uintptr_t)(P_TAB + (t8 & ~63u) + P0 * 16);
            Vb = *(const LDS u32x4*)(uintptr_t)(P_TAB + (t8 & ~63u) + P1 * 16);
            asm volatile("" : "+v"(Va), "+v"(Vb));
        }
        uint32_t pq = (uint32_t)pb2 | (uint32_t)(pb2 >> 32);
        pq = (pq | (pq >> 1)) & 0x55555555u;
        pq = __builtin_amdgcn_readfirstlane(pq + (pq & (far * NEARM)));
        uint32_t ad[8];
        if (!FIRST) ad[0] = take<4 * P0 + 0, D>(Va[0], u16, la, nul);
        convB(1);
        if (!FIRST) ad[1] = take<4 * P0 + 1, D>(Va[1], u16, la, nul);
        convD(0);
        convA(1);
        if (!FIRST) ad[2] = take<4 * P0 + 2, D>(Va[2], u16, la, nul);
        convB(2);
        hook(13);
        if (!FIRST) ad[3] = take<4 * P0 + 3, D>(Va[3], u16, la, nul);
        convB(3);
        if (!FIRST) put<4 * P0 + 0, LAST ? 3 : 3>(rsA, ad[0], pq);
        if (!FIRST) ad[4] = take<4 * P1 + 0, D>(Vb[0], u16, la, nul);
        convD(1);
        check(0);
        ticket(0);
        hook(4);
        if (!FIRST) put<4 * P0 + 1, LAST ? 3 : 4>(rsA, ad[1], pq);
        if (!FIRST) ad[5] = take<4 * P1 + 1, D>(Vb[1], u16, la, nul);
        convA(2);
        ticket(1);
        if (!FIRST) put<4 * P0 + 2, LAST ? 3 : 5>(rsA, ad[2], pq);
        if (!FIRST) ad[6] = take<4 * P1 + 2, D>(Vb[2], u16, la, nul);
        convB(4);
        ticket(2);
        if (!FIRST) put<4 * P0 + 3, LAST ? 3 : 6>(rsA, ad[3], pq);
        if (!FIRST) Va = *(const LDS u32x4*)(uintptr_t)(tr + P2 * 16);
        __builtin_amdgcn_sched_barrier(0);
        if (!FIRST) ad[7] = take<4 * P1 + 3, D>(Vb[3], u16, la, nul);
        convB(5);
        ticket(3);
        SETP(2);
        if (!FIRST) put<4 * P1 + 0, LAST ? 4 : 8>(rsA, ad[4], pq);
        if (!FIRST) ad[0] = take<4 * P2 + 0, D>(Va[0], u16, la, nul);
        convD(2);
        convA(3);
        hook(5);
        if (!FIRST) put<4 * P1 + 1, LAST ? 4 : 7>(rsA, ad[5], pq);
        if (!FIRST) ad[1] = take<4 * P2 + 1, D>(Va[1], u16, la, nul);
        convB(6);
        if (!FIRST) put<4 * P1 + 2, LAST ? 4 : 6>(rsA, ad[6], pq);
        if (!FIRST) ad[2] = take<4 * P2 + 2, D>(Va[2], u16, la, nul);
        convB(7);
        if (!FIRST) put<4 * P1 + 3, LAST ? 3 : 4>(rsA, ad[7], pq);
        if (!FIRST) Vb = *(const LDS u32x4*)(uintptr_t)(tr + P3 * 16);
        __builtin_amdgcn_sched_barrier(0);
        if (!FIRST) ad[3] = take<4 * P2 + 3, D>(Va[3], u16, la, nul);
        convD(3);
        check(1);
        ticket(4);
        SETP(1);
        hook(6);
        if (!FIRST) put<4 * P2 + 0, LAST ? 4 : 5>(rsA, ad[0], pq);
        if (!FIRST) ad[4] = take<4 * P3 + 0, D>(Vb[0], u16, la, nul);
        ticket(5);
        if (!FIRST) put<4 * P2 + 1, LAST ? 4 : 6>(rsA, ad[1], pq);
        if (!FIRST) ad[5] = take<4 * P3 + 1, D>(Vb[1], u16, la, nul);
        ticket(6);
        if (!FIRST) put<4 * P2 + 2, LAST ? 4 : 7>(rsA, ad[2], pq);
        if (!FIRST) ad[6] = take<4 * P3 + 2, D>(Vb[2], u16, la, nul);
        ticket(7);
        SETP(0);
        if (!FIRST) put<4 * P2 + 3, LAST ? 3 : 7>(rsA, ad[3], pq);
        if (!FIRST) ad[7] = take<4 * P3 + 3, D>(Vb[3], u16, la, nul);
        hook(7);
        SETP(3);
        asm volatile("s_waitcnt lgkmcnt(0)" ::: "memory");
        __syncthreads();
        u32x2 cn;
        uint32_t tv;
        if (!LAST) { cn = *(const LDS u32x2*)(uintptr_t)(P_CNT + t8); tv = lds_ld(vconst<P_FLAG + 4>()); }
        __builtin_amdgcn_sched_barrier(0);
        if (!FIRST) later2(0);
        if (!FIRST) put<4 * P3, 15>(rsA, ad[4], pq);
        if (!FIRST) later2(2);
        if (!FIRST) put<4 * P3 + 1, 15>(rsA, ad[5], pq);
        if (!FIRST) drain();
        if (!LAST) {
            tl2 = rd + 2 < nstat ? fixed_tile(rd + 2) : nstat * NWG + (int)__builtin_amdgcn_readfirstlane(tv);
            look(cn);
        }
        hook(0);
        SETP(2);
        if (!FIRST) later2(4);
        if (!FIRST) put<4 * P3 + 2, 15>(rsA, ad[6], pq);
        if (!FIRST) later2(6);
        if (!FIRST) put<4 * P3 + 3, 15>(rsA, ad[7], pq);
        if (!LAST) {
            hook(1);
            SETP(1);
            insert(0);
            hook(2);
            SETP(0);
            pin(t8);
            Va = *(const LDS u32x4*)(uintptr_t)(P_TAB + (t8 & ~63u) + P3 * 16);
            Vb = *(const LDS u32x4*)(uintptr_t)(P_TAB + (t8 & ~63u) + P2 * 16);
            __builtin_amdgcn_sched_barrier(0);
            insert(1);
            hook(3);
            SETP(3);
            asm volatile("s_waitcnt lgkmcnt(0)" ::: "memory");
            __syncthreads();
            tl0 = tl1; tl1 = tl2;
        }
    };
#define HMV(A, B) "v_mov_b64 v[" #A ":" #A "+1], v[" #B ":" #B "+1]\n\t"
#define HMV4(A) HMV(A + 8, A + 6) HMV(A + 6, A + 4) HMV(A + 4, A + 2) HMV(A + 2, A)
    round(IC<1>(), IC<2>(), raw[1], raw[0], 0);
    int rd = 1;
#pragma unroll 1
    for (; tl1 < ntile; rd += 2) {
        round(IC<0>(), IC<0>(), raw[0], raw[1], rd);
        round(IC<1>(), IC<0>(), raw[1], raw[0], rd + 1);
    }
    if (tl0 >= ntile) asm volatile(HMV4(88) HMV4(98) HMV4(108) HMV4(118));
    asm volatile("" : "+s"(tl0));
    if (tl0 < ntile) round(IC<0>(), IC<0>(), raw[0], raw[1], rd);
    RTS(1)
    round(IC<1>(), IC<1>(), raw[1], raw[0], 0);
    if (__builtin_amdgcn_readfirstlane(lds_ld(P_FLAG + 16 + ((epoch + 2u) & 3u) * 4u)) != 0u) {
        asm volatile("s_waitcnt lgkmcnt(0)" ::: "memory");
        __syncthreads();
        look(*(const LDS u32x2*)(uintptr_t)(P_CNT + t8));
        general(0, true, fm);
        __syncthreads();
        drain();
    }
    {
        uint32_t* tcnt = meta + W_TCNT / 4;
        const u32x2 c = *(const LDS u32x2*)(uintptr_t)(P_CNT + t8);
        const uint32_t tw = tab_at();
#pragma unroll
        for (int e = 0; e < 2; e++) {
            const uint32_t g = U[e] & 15u;
            lds_st(tw + e * 64u, g >= 8u ? 0x80000000u : ((U[e] + ((g & 1u) ? 0u : SK * 64u)) & 0x7fffff81u) | (((7u - g) << 1) & 12u));
            *(LDS uint8_t*)(uintptr_t)(P_CNT + (t8 & ~511u) + (((tw - P_TAB) >> 2) & 127u) + e * 16u) =
                (uint8_t)(g < 8u ? 0u : ((g - 8u) >> 1) + (((c[e] + 2u * CSTEP) >> 15) << 2) + (g & 1u) * 0x50u);
        }
        asm volatile("s_waitcnt lgkmcnt(0)" ::: "memory");
        __syncthreads();
        const uint32_t u16 = (t8 + t8) & 0x70u;
        u32x4 En[4];
#pragma unroll
        for (int p = 0; p < 4; p++) En[p] = *(const LDS u32x4*)(uintptr_t)(P_TAB + (t8 & ~63u) + p * 16u);
        const u32x4 cb = *(const LDS u32x4*)(uintptr_t)(P_CNT + (t8 & ~511u) + ((t8 >> 2) & 0x70u));
        u32x2 N[16];
#pragma unroll
        for (int s = 0; s < 16; s++) N[s] = *(const LDS u32x2*)(uintptr_t)(la + s * 256u);
        if (u16 == 0u) *(u32x4*)(tcnt + blockIdx.x * 512u + (t8 >> 4)) = cb;
        const uint32_t sg = P_BUF + ((t8 & ~63u) << 4);
        uint32_t ps;
        auto rest = [&](auto SL) __attribute__((always_inline)) {
            constexpr int s = decltype(SL)::value;
            const uint32_t en = En[s >> 2][s & 3], by = cb[s >> 2] >> ((s & 3) * 8);
            const u32x2 Hr = hget<HREG(s, 0)>();
            const uint32_t at = ps + ((la & 0x38u) ^ ((s & 3) << 4));
            *(LDS u32x2*)(uintptr_t)at = Hr;
            *(LDS u32x2*)(uintptr_t)(at + (by & 0x40u)) = N[s];
            ps += (by & 0x3cu) << 2;
            const u32x2 Hs = (en & 1u) ? Hr : nul;
            const u32x4 d = {Hs.x, Hs.y, N[s].x, N[s].y};
            uint32_t a = __builtin_amdgcn_bitop3_b32(en, u16, 15u, 0x54);
            {
                const u32x4 rs_ = rsA;
                const uint32_t far_ = ((NEARM >> (2 * s)) & 1u) ? far : 0u;
                asm volatile("s_cmp_eq_u32 %3, 0\n\ts_cbranch_scc0 1f\n\tbuffer_store_dwordx4 %0, %1, %2, 0 offen sc1\n\ts_branch 2f\n"
                             "1:\n\tbuffer_store_dwordx4 %0, %1, %2, 0 offen nt\n2:\n\ts_nop 1" :: "v"(d), "v"(a), "s"(rs_), "s"(far_) : "memory", "scc");
            }
            if (__builtin_expect(__builtin_amdgcn_ballot_w64((en & 12u) != 0u) != 0ull, 0)) {
                const u32x4 dn = {nul.x, nul.y, nul.x, nul.y};
#pragma unroll 1
                for (uint32_t k = (en >> 2) & 3u; k > 0u; k--) {
                    a += SK * 128u;
                    __builtin_amdgcn_raw_buffer_store_b128(dn, rsR, a, 0, AUX_SC1);
                }
            }
        };
        auto pack = [&](int p) __attribute__((always_inline)) {
            const uint32_t a = (SBASE + ((((t8 >> 1) & 0x60u) + (t8 >> 8)) * 2u + p) * (NWG * 9u) + blockIdx.x * 9u) * 128u + u16;
            asm volatile("" ::: "memory");
#pragma unroll 1
            for (uint32_t q = 0; __builtin_amdgcn_ballot_w64(sg + q < ps) != 0ull; q += 256u) {
                const u32x4 d0 = *(const LDS u32x4*)(uintptr_t)(sg + q + u16), d1 = *(const LDS u32x4*)(uintptr_t)(sg + q + 128u + u16);
                __builtin_amdgcn_raw_buffer_store_b128(d0, rsR, sg + q < ps ? a + q : 0xffffff00u, 0, AUX_SC1);
                __builtin_amdgcn_raw_buffer_store_b128(d1, rsR, sg + q + 128u < ps ? a + q + 128u : 0xffffff00u, 0, AUX_SC1);
            }
            asm volatile("" ::: "memory");
        };
        asm volatile("" : "+v"(N[0]), "+v"(N[1]), "+v"(N[2]), "+v"(N[3]), "+v"(N[4]), "+v"(N[5]), "+v"(N[6]), "+v"(N[7]) :: "memory");
        asm volatile("" : "+v"(N[8]), "+v"(N[9]), "+v"(N[10]), "+v"(N[11]), "+v"(N[12]), "+v"(N[13]), "+v"(N[14]), "+v"(N[15]) :: "memory");
        ps = sg;
        rest(IC<0>()); rest(IC<1>()); rest(IC<2>()); rest(IC<3>()); rest(IC<4>()); rest(IC<5>()); rest(IC<6>()); rest(IC<7>());
        pack(0);
        ps = sg;
        rest(IC<8>()); rest(IC<9>()); rest(IC<10>()); rest(IC<11>()); rest(IC<12>()); rest(IC<13>()); rest(IC<14>()); rest(IC<15>());
        pack(1);
    }
    RTS(2)
    if (blockIdx.x == 0 && t8 == 1536u) {
        do {
            beat();
            __builtin_amdgcn_s_sleep(8);
        } while (peek(vconst<M_MD * 4>()) < NWG);
    }
    RTS(3)
    RTS(4)
}

#define A_MD 0
#define A_ACC (SLOTI * 4)
#define A_FLAG (A_ACC + SLOTO * 4)
#define A_LDS (A_FLAG + 512)
static_assert(SLOTI * 4 == 3 * NT * 16 + 1024, "md staging assumes three full vectors and 256 dwords");
#define BIAS 0x40004000u
#define NFAST 8
#define NLD 10
#define MDF 0x0a11333
#define MDP 0xaaaaa
#define HP 2
#define SHARE (SLOTO / 8)
#define SPINCAP 4096u
#define ZSIG 9.0f
#define HSLOW 0x80008000u
#define GAS __attribute__((address_space(1)))

__global__ __launch_bounds__(NT) __attribute__((amdgpu_num_vgpr(53))) void acc_kernel(char* ws, int T, int E, int ks, int lcap, float zf, int pad,
                                                 const uint16_t* __restrict__ centers, const uint16_t* __restrict__ wrbf,
                                                 const uint16_t* __restrict__ wsbf, const uint16_t* __restrict__ wup,
                                                 const u32x2* __restrict__ m, const uint16_t* __restrict__ dist, u32x2* __restrict__ out,
                                                 const int* __restrict__ iin, const int* __restrict__ iout, const uint16_t* __restrict__ ang,
                                                 const uint16_t* __restrict__ wdown, const uint16_t* __restrict__ freq) {
    const char* recs = ws + W_RECS;
    const u32x4* tcnt = (const u32x4*)(ws + W_TCNT);
    const uint32_t* mdc = (const uint32_t*)(ws + W_MDC);
    const u32x4* stat = (const u32x4*)(ws + W_STAT);
    uint32_t* partial = (uint32_t*)(ws + W_PART);
    unsigned* meta = (unsigned*)ws;
    extern __shared__ __attribute__((aligned(16))) char smem[];
    RTS(8)
    unsigned* acc = (unsigned*)(smem + A_ACC);
    unsigned* s_flag = (unsigned*)(smem + A_FLAG);
    const int tid = threadIdx.x;
    const int lane = tid & 63;
    const int wv = __builtin_amdgcn_readfirstlane(tid >> 6);
    const int cp = (blockIdx.x >> 3) & 1;
    const int h = blockIdx.x & 3;
    const int j = ((blockIdx.x >> 2) & 1) * 16 + (blockIdx.x >> 4);
    u32x4 tc;
    const __amdgpu_buffer_rsrc_t rsM = __builtin_amdgcn_make_buffer_rsrc((void*)mdc, 0, (int)(128u * SLOTI * 4u), 0x00020000);

    u32x4 pm[4];
    u32x4 ring[NLD];
    auto load_md = [&](int ff, int pl, int mdp) __attribute__((always_inline)) {
        const uint32_t so = (uint32_t)(cp * 64 + ff * 4 + h) * (SLOTI * 4u);
#pragma unroll
        for (int v = 0; v < 3; v++)
            if (pl < 0 || pl == ((mdp >> (4 * v)) & 15)) pm[v] = __builtin_amdgcn_raw_buffer_load_b128(rsM, (uint32_t)tid * 16u, so + (uint32_t)v * (NT * 16u), 0);
        if ((pl < 0 || pl == (mdp & 15)) && wv == 0) pm[3] = __builtin_amdgcn_raw_buffer_load_b128(rsM, (uint32_t)tid * 16u, so + 3u * NT * 16u, 0);
        __builtin_amdgcn_sched_barrier(0);
    };
    const uint32_t lf = (uint32_t)(tid >> 9) * (SKH * 128u) + (uint32_t)(tid & 0x1e0) * 16u + (uint32_t)(tid & 12) * 32u + (uint32_t)(tid & 16) * 4u + (uint32_t)(tid & 3) * 16u;
    const uint32_t lp = (uint32_t)(tid >> 8) * (SKM * 128u) + (uint32_t)((tid >> 2) & 63) * 128u + (uint32_t)(tid & 3) * 16u;
    auto load_main = [&](int i, int ff) __attribute__((always_inline)) -> u32x4 {
        const uint32_t q = (uint32_t)(((i >> 1) - HP) & 3), SK = q ? SKM : SKH, SS = q ? SSM : SSH;
        const __amdgpu_buffer_rsrc_t rs = __builtin_amdgcn_make_buffer_rsrc((void*)(recs + (q * SQ + (uint32_t)ff * SS + (uint32_t)(h * 32 + j) * SC) * 128u), 0, ks * (int)(SK * 128u), 0x00020000);
        if (q == 0u) return __builtin_amdgcn_raw_buffer_load_b128(rs, lf, (i & 1) * (int)(2u * SK * 128u), AUX_NT);
        return __builtin_amdgcn_raw_buffer_load_b128(rs, lp + (uint32_t)(i & 1) * 64u, 0, 0);
    };
    const __amdgpu_buffer_rsrc_t rsT = __builtin_amdgcn_make_buffer_rsrc((void*)recs, 0, (int)(LBASE * 128u), 0x00020000);
    const uint32_t tof = (uint32_t)(lane & 2) * (TPL * 64u) + (uint32_t)(tid >> 2) * 128u + (uint32_t)(lane & 1) * 64u + 4u + (TBASE + (uint32_t)(h * 32 + j) * TST) * 128u;
    const uint32_t sp0 = (SBASE + (uint32_t)(h * 32 + j) * (2u * NWG * 9u) + (uint32_t)(tid >> 2) * 9u) * 128u;
    auto tail_rank = [&](uint32_t c, uint32_t sp, int ff, uint32_t t0, int& dst, uint32_t& en) __attribute__((always_inline)) -> int {
        const int k4 = (int)(((uint32_t)(lane & 3) + t0) * 4u) - (int)((c & 3u) * 8u);
        const int left = (int)((c >> 2) & 15u) - k4;
        const bool has = left > 0;
        uint32_t ea = tof + (uint32_t)ff * (128u * TST * 128u), eb = sp + (uint32_t)(k4 * 16 + min(max(left, 0), 4));
        asm volatile("" : "+v"(ea), "+v"(eb));
        en = k4 < 0 ? ea : eb;
        const u64 mask = __builtin_amdgcn_ballot_w64(has);
        const int n = __builtin_popcountll(mask);
        const int rank = __builtin_amdgcn_mbcnt_hi((uint32_t)(mask >> 32), __builtin_amdgcn_mbcnt_lo((uint32_t)mask, 0u));
        const int r = has ? rank : (n + lane - rank);
        dst = ((r & 15) << 4) | ((r >> 4) << 2);
        return n;
    };
    auto load_tail = [&](uint32_t en, u64& tm) __attribute__((always_inline)) -> u32x4 {
        const bool on = (uint32_t)(lane & 3) < (en & 7u);
        tm = __builtin_amdgcn_ballot_w64(on);
        return __builtin_amdgcn_raw_buffer_load_b128(rsT, on ? (en & ~15u) + (uint32_t)(lane & 3) * 16u : 0xffffff00u, 0, 0);
    };
    f32x2 fa[4][4];
    u32x4 em[2];
    u32x2 dw2;
    uint32_t wl;
    const __amdgpu_buffer_rsrc_t rsE = __builtin_amdgcn_make_buffer_rsrc((void*)m, 0, E * 8, 0x00020000);
    const __amdgpu_buffer_rsrc_t rsD = __builtin_amdgcn_make_buffer_rsrc((void*)dist, 0, E * 2, 0x00020000);
    auto acc_vec = [&](int i) __attribute__((always_inline)) -> u32x4 {
        const uint32_t a = A_ACC + (uint32_t)(i >> 4) * 256u + (uint32_t)(i & 15) * 8u;
        const u32x2 x = *(const LDS u32x2*)(uintptr_t)a, y = *(const LDS u32x2*)(uintptr_t)(a + 128u);
        const u32x4 v = {x.x, x.y, y.x, y.y};
        return v;
    };
    auto edge0 = [&](int sh) __attribute__((always_inline)) -> uint32_t {
        return (tid < SHARE / 4) ? (uint32_t)(((sh * (SHARE / 64) + (tid >> 4)) << 11) | (j << 6) | ((tid & 15) * 2)) : 0x0fffff00u;
    };
    auto load_m = [&](int sh) __attribute__((always_inline)) {
        em[0] = __builtin_amdgcn_raw_buffer_load_b128(rsE, edge0(sh) * 8u, 0, AUX_NT);
        em[1] = __builtin_amdgcn_raw_buffer_load_b128(rsE, edge0(sh) * 8u + 256u, 0, AUX_NT);
    };
    auto load_d = [&](int sh) __attribute__((always_inline)) {
        dw2.x = __builtin_amdgcn_raw_buffer_load_b32(rsD, edge0(sh) * 2u, 0, 0);
        dw2.y = __builtin_amdgcn_raw_buffer_load_b32(rsD, edge0(sh) * 2u + 64u, 0, 0);
    };
    auto load_wl = [&]() __attribute__((always_inline)) {
        const u64 p0 = (u64)wsbf, p1 = (u64)wrbf, p2 = (u64)centers, p3 = (u64)wup;
        const u64 pa = lane < 16 ? p0 : p1, pb = lane < 40 ? p2 : p3;
        wl = ((const GAS uint32_t*)(lane < 32 ? pa : pb))[lane & (lane < 32 ? 15 : lane < 40 ? 3 : 7)];
    };
    auto factor = [&](int r, bool first) __attribute__((always_inline)) {
        const float sq = 3.7982826f;
        const f32x2 d01 = {bf2f(dw2.x & 0xffffu), bf2f(dw2.x >> 16)}, d23 = {bf2f(dw2.y & 0xffffu), bf2f(dw2.y >> 16)};
        const uint32_t cw = __builtin_amdgcn_readlane(wl, 32 + (r >> 1));
        const float cn = bf2f((r & 1) ? (cw >> 16) : (cw & 0xffffu));
        f32x2 w[4];
#pragma unroll
        for (int k = 0; k < 4; k++) {
            const uint32_t x = __builtin_amdgcn_readlane(wl, (k >> 1) * 16 + r * 2 + (k & 1));
            w[k].x = bf2f(x & 0xffffu); w[k].y = bf2f(x >> 16);
        }
        const f32x2 y01 = (d01 - cn) * sq, y23 = (d23 - cn) * sq;
        const f32x2 t01 = -y01 * y01, t23 = -y23 * y23;
        const float rb[4] = {__builtin_amdgcn_exp2f(t01.x), __builtin_amdgcn_exp2f(t01.y), __builtin_amdgcn_exp2f(t23.x), __builtin_amdgcn_exp2f(t23.y)};
#pragma unroll
        for (int q = 0; q < 4; q++) {
#pragma unroll
            for (int k = 0; k < 4; k++) fa[q][k] = first ? w[k] * rb[q] : __builtin_elementwise_fma(w[k], (f32x2)(rb[q]), fa[q][k]);
        }
    };
    auto edges = [&](int sh) __attribute__((always_inline)) {
        load_d(sh);
        load_m(sh);
        factor(0, true);
#pragma unroll 1
        for (int r = 1; r < 8; r++) factor(r, false);
    };
#define ADT(V, O) "ds_write_addtid_b32 " V " offset:" #O "\n\t"
    auto store_md = [&]() __attribute__((always_inline)) {
        asm volatile("s_mov_b32 m0, %12\n\ts_nop 0\n\t" ADT("%0", 0) ADT("%1", 256) ADT("%2", 512) ADT("%3", 768) ADT("%4", 16384) ADT("%5", 16640) ADT("%6", 16896) ADT("%7", 17152)
                     ADT("%8", 32768) ADT("%9", 33024) ADT("%10", 33280) ADT("%11", 33536)
                     :: "v"(pm[0][0]), "v"(pm[0][1]), "v"(pm[0][2]), "v"(pm[0][3]), "v"(pm[1][0]), "v"(pm[1][1]), "v"(pm[1][2]), "v"(pm[1][3]),
                        "v"(pm[2][0]), "v"(pm[2][1]), "v"(pm[2][2]), "v"(pm[2][3]), "s"(wv * 1024) : "memory");
        if (wv == 0) asm volatile("s_mov_b32 m0, 0\n\ts_nop 0\n\t" ADT("%0", 49152) ADT("%1", 49408) ADT("%2", 49664) ADT("%3", 49920)
                                  :: "v"(pm[3][0]), "v"(pm[3][1]), "v"(pm[3][2]), "v"(pm[3][3]) : "memory");
    };
    const int me = h * 2 + cp;
    GAS unsigned* dw = (GAS unsigned*)(meta + M_DONE + j * 64);
    const bool fin = wv < (SHARE / 4 + 63) / 64;
    bool fast, more;
    uint32_t need, mg0, mg1;
    uint32_t guard = 0;
    uint32_t sgn = 0x8000u;
    pin(sgn);

#define GAT3(M) { uint32_t a0_, a1_, a2_; asm volatile( \
        "v_lshlrev_b16 %0, 2, %3\n\tv_lshlrev_b16 %1, 2, %4\n\tv_lshlrev_b16 %2, 2, %5\n\t" \
        "ds_read_b32 v[%6], %0\n\tds_read_b32 v[%6+1], %1\n\tds_read_b32 v[%6+2], %2" \
        : "=&v"(a0_), "=&v"(a1_), "=&v"(a2_) : "v"(w0), "v"(w1), "v"(w2), "n"(M) : "memory"); }
#define MIXL " op_sel_hi:[1,1,0]\n\t"
#define MIXH " op_sel:[0,1,0] op_sel_hi:[1,1,0]\n\t"
#define SDW " dst_sel:DWORD dst_unused:UNUSED_PAD src0_sel:DWORD src1_sel:WORD_1\n\t"
#define FOLD "v_bitop3_b32 %6, %6, v[%15-9], v[%15-8] bitop3:0xfe\n\tv_or_b32 %6, v[%15-7], %6\n\t"
#define SCAT(PRE, POST, M) { uint32_t c0_, c1_, c2_, a0_, a1_, a2_; asm volatile( \
        FOLD "v_and_b32 %0, 0x3ff, %7\n\tv_lshrrev_b32 %2, 22, %7\n\t" PRE \
        "v_bitop3_b32 %0, %11, %8, %0 bitop3:0xea\n\tv_bitop3_b32 %2, %11, %10, %2 bitop3:0xea\n\tv_bfe_u32 %1, %7, 11, 10\n\t" \
        "v_fma_mix_f32 %3, %0, v[%15], %12" MIXL "v_bitop3_b32 %1, %11, %9, %1 bitop3:0xea\n\tv_fma_mix_f32 %0, %0, v[%15], %13" MIXH \
        "v_fma_mix_f32 %4, %1, v[%15+1], %12" MIXL "v_lshl_add_u32 %0, %0, 16, %3\n\tv_lshlrev_b32_sdwa %3, 2, %8" SDW \
        "v_fma_mix_f32 %1, %1, v[%15+1], %13" MIXH "ds_add_rtn_u32 v[%15-9], %3, %0 offset:%14\n\t" \
        "v_fma_mix_f32 %5, %2, v[%15+2], %12" MIXL "v_lshl_add_u32 %1, %1, 16, %4\n\tv_lshlrev_b32_sdwa %4, 2, %9" SDW \
        "v_fma_mix_f32 %2, %2, v[%15+2], %13" MIXH "ds_add_rtn_u32 v[%15-8], %4, %1 offset:%14\n\t" \
        "v_lshl_add_u32 %2, %2, 16, %5\n\tv_lshlrev_b32_sdwa %5, 2, %10" SDW \
        "ds_add_rtn_u32 v[%15-7], %5, %2 offset:%14" POST \
        : "=&v"(c0_), "=&v"(c1_), "=&v"(c2_), "=&v"(a0_), "=&v"(a1_), "=&v"(a2_), "+v"(guard) \
        : "v"(xw), "v"(w0), "v"(w1), "v"(w2), "v"(sgn), "s"(mg0), "s"(mg1), "n"(A_ACC), "n"(M), "s"(tm) : "memory"); }
    asm volatile("v_mov_b32 v110, 0\n\tv_mov_b32 v111, 0\n\tv_mov_b32 v112, 0\n\tv_mov_b32 v113, 0\n\tv_mov_b32 v114, 0\n\tv_mov_b32 v115, 0\n\t"
                 "v_mov_b32 v116, 0\n\tv_mov_b32 v117, 0\n\tv_mov_b32 v118, 0" ::: "v106", "v127");
    auto unmix = [&](int i) __attribute__((always_inline)) {
        const auto s0 = __builtin_amdgcn_permlane16_swap(ring[i][0], ring[i][2], false, false);
        const auto s1 = __builtin_amdgcn_permlane16_swap(ring[i][1], ring[i][3], false, false);
        ring[i][0] = s0[0]; ring[i][2] = s0[1]; ring[i][1] = s1[0]; ring[i][3] = s1[1];
    };
    auto word = [&](int i, int k) __attribute__((always_inline)) -> uint32_t {
        return i < NFAST && (i >> 1) != HP ? ring[(i & ~1) | (k >> 1)][(i & 1) * 2 + (k & 1)] : ring[i][k];
    };
    auto gather = [&](int R, int i, int p) __attribute__((always_inline)) {
        if ((i >> 1) == HP) unmix(i);
        const uint32_t w0 = word(i, 0 ^ R), w1 = word(i, 1 ^ R), w2 = word(i, 2 ^ R);
        if (p == 0) GAT3(119) else if (p == 1) GAT3(122) else GAT3(125)
    };
    auto scatter = [&](int R, int i, int p, u64 tm) __attribute__((always_inline)) {
        const uint32_t xw = word(i, 3 ^ R), w0 = word(i, 0 ^ R), w1 = word(i, 1 ^ R), w2 = word(i, 2 ^ R);
        if (i < NFAST) { if (p == 0) SCAT("", "", 119) else if (p == 1) SCAT("", "", 122) else SCAT("", "", 125) }
        else if (p == 0) SCAT("s_mov_b64 exec, %16\n\t", "\n\ts_mov_b64 exec, -1", 119)
        else SCAT("s_mov_b64 exec, %16\n\t", "\n\ts_mov_b64 exec, -1", 125)
    };
#define WAIT_L(N) asm volatile("s_waitcnt lgkmcnt(" #N ")" ::: "memory")
    int ntn;
    u64 tm0, tm1;
    uint32_t ta[2], sp = sp0;
    auto list1 = [&](int ff) __attribute__((always_inline)) {
        int dst;
        uint32_t en;
        ntn = tail_rank(tc.x, sp, ff, 0u, dst, en);
        asm volatile("ds_permute_b32 v106, %0, %1" :: "v"(dst), "v"(en) : "memory");
        if (__builtin_expect(__builtin_amdgcn_ballot_w64((tc.x & 2u) != 0u) != 0ull, 0)) {
            const int n2 = tail_rank(tc.x, sp, ff, 4u, dst, en);
            if (ntn + n2 <= 32) {
                const int r = ((dst >> 4) | ((dst & 12) << 2)) + ntn;
                const int x = __builtin_amdgcn_ds_permute((en & 7u) ? ((r & 15) << 4) | ((r >> 4) << 2) : 252, (en & 7u) ? (int)en : 0);
                asm volatile("v_cmpx_ne_u32 vcc, 0, %0\n\tv_mov_b32 v106, %0\n\ts_mov_b64 exec, -1\n\ts_nop 4" :: "v"(x) : "vcc");
                ntn += n2;
            } else ntn = 64;
        }
    };
    auto step = [&](auto RR, auto KK, int ff) __attribute__((always_inline)) {
        constexpr int R = decltype(RR)::value;
        constexpr bool first = decltype(KK)::value == 1, last = decltype(KK)::value == 2;
        constexpr int mdp = first ? MDF : MDP;
        gather(R, 0, 0);
        const int ntl = ntn;
        asm volatile("v_mov_b32_dpp %0, v106 quad_perm:[0,0,0,0] row_mask:0xf bank_mask:0xf\n\tv_mov_b32_dpp %1, v106 quad_perm:[1,1,1,1] row_mask:0xf bank_mask:0xf" : "=v"(ta[0]), "=v"(ta[1]));
        auto side = [&](int pl) __attribute__((always_inline)) {
            if (first && pl == ((mdp >> 20) & 15)) { ring[4] = load_main(4, ff); ring[5] = load_main(5, ff); }
            if (first && pl == ((mdp >> 24) & 15)) { ring[6] = load_main(6, ff); ring[7] = load_main(7, ff); }
            if (pl == ((mdp >> 12) & 15)) ring[NFAST] = load_tail(ta[0], tm0);
            if (pl == ((mdp >> 16) & 15)) ring[NFAST + 1] = load_tail(ta[1], tm1);
            __builtin_amdgcn_sched_barrier(0);
            if (!last) load_md(ff + 1, pl, mdp);
            if (!first && !last && pl == 5 && ff == 6) {
                load_d(me);
                load_wl();
                if (tid == NT - 128) (void)__hip_atomic_fetch_add((GAS unsigned*)((GAS char*)dw + vconst<0>()), 0x1000000u << me, __ATOMIC_RELAXED, __HIP_MEMORY_SCOPE_AGENT);
            }
            if (last && pl == 3) load_m(me);
            if (last && pl == 4 && wv == 15) { GAS unsigned* d_ = dw; asm volatile("s_nop 4\n\tglobal_load_dword v108, %0, %1 sc1" :: "v"(vconst<0>()), "s"(d_) : "memory"); }
            if (last && pl < 7 && fin) factor(pl, pl == 0);
            __builtin_amdgcn_sched_barrier(0);
        };
        side(8);
        gather(R, 1, 1);
        side(9);
        const uint32_t tcur = tc.x, spc = sp;
#pragma unroll
        for (int i = 0; i < NFAST - 1; i++) {
            gather(R, i + 2, (i + 2) % 3);
            if (i == 0) side(10);
            if (i == 0) WAIT_L(6); else if (i == 1) WAIT_L(9); else WAIT_L(12);
            scatter(R, i, i % 3, 0ull);
            if (!last && (i & 1)) { ring[i - 1] = load_main(i - 1, ff + 1); ring[i] = load_main(i, ff + 1); }
            side(i);
            if (i == 3) SETP(2);
            if (i == 4) SETP(1);
            if (i == 5) SETP(0);
            if (i == 0 && !last) {
                tc.x = __builtin_amdgcn_alignbit(tc.y, tc.x, 8u); tc.y = __builtin_amdgcn_alignbit(tc.z, tc.y, 8u);
                tc.z = __builtin_amdgcn_alignbit(tc.w, tc.z, 8u); tc.w >>= 8;
                sp = ff == 7 ? sp0 + NWG * 9u * 128u : sp + ((tcur & 0x3cu) << 2);
                list1(ff + 1);
            }
            __builtin_amdgcn_sched_barrier(0);
        }
        if (last) asm volatile("" :: "v"(ring[NFAST + 1]));
        if (__builtin_expect(ntl > 16, 0)) {
            gather(R, NFAST + 1, 0);
            WAIT_L(0);
            scatter(R, NFAST + 1, 0, tm1);
        }
        if (__builtin_expect(ntl > 32, 0)) {
#pragma unroll 1
            for (int e = 2; e < 6; e++) {
                int dst;
                uint32_t en;
                if (tail_rank(tcur, spc, ff, (uint32_t)e & 4u, dst, en) <= (e & 3) * 16) continue;
                const int cmp = __builtin_amdgcn_ds_permute(dst, (int)en);
                ring[NFAST + 1] = load_tail((uint32_t)__builtin_amdgcn_ds_bpermute(((lane & ~3) | (e & 3)) << 2, cmp), tm1);
                gather(R, NFAST + 1, 0);
                WAIT_L(0);
                scatter(R, NFAST + 1, 0, tm1);
            }
        }
        SETP(3);
        WAIT_L(3);
        if (!last) {
            __builtin_amdgcn_s_barrier();
            store_md();
            SETP(0);
        }
        scatter(R, NFAST - 1, 1, 0ull);
        if (!last) { ring[6] = load_main(6, ff + 1); ring[7] = load_main(7, ff + 1); }
        scatter(R, NFAST, 2, tm0);
        if (!last) {
            WAIT_L(6);
            __builtin_amdgcn_s_barrier();
            SETP(3);
        }
    };
    auto steps = [&](auto RR) __attribute__((always_inline)) {
        tc = tcnt[(wv * 16 + (lane >> 2)) * 128 + j * 4 + h];
        u32x4 sv[2];
        uint32_t e0;
        if (wv < 4) {
            sv[0] = (u32x4)(0u); sv[1] = sv[0];
            if (lane < SLOTI / 256) { sv[0] = stat[tid * 2]; sv[1] = stat[tid * 2 + 1]; }
            e0 = meta[M_E0 + wv];
        }
        const u32x2 extra = *(const u32x2*)(meta + M_LOG);
        load_md(0, -1, 0);
        {
            const u32x4 bi = {BIAS, BIAS, BIAS, BIAS};
#pragma unroll
            for (int k = 0; k < 6; k++) *(LDS u32x4*)(uintptr_t)(A_ACC + (k * NT + threadIdx.x) * 16) = bi;
            if (threadIdx.x < 128) *(LDS u32x4*)(uintptr_t)(A_ACC + (6 * NT + threadIdx.x) * 16) = bi;
            __builtin_amdgcn_sched_barrier(0);
        }
#pragma unroll
        for (int i = 0; i < 2; i++) ring[i] = load_main(i, 0);
        __builtin_amdgcn_sched_barrier(0);
        list1(0);
        if (wv < 4) {
            const uint32_t mxb = wave_max(max(max(sv[0].x & 0xffffu, sv[0].z & 0xffffu), max(sv[1].x & 0xffffu, sv[1].z & 0xffffu)));
            const uint32_t mnb = 0xffffu - (wave_max(max(max(sv[0].x, sv[0].z), max(sv[1].x, sv[1].z))) >> 16);
            const float s2 = wave_sum((__uint_as_float(sv[0].y) + __uint_as_float(sv[0].w)) + (__uint_as_float(sv[1].y) + __uint_as_float(sv[1].w)));
            const float rng = fmaxf(sqrtf(zf * s2), 2.0f * bf2f(mxb)) * 6.103515625e-05f;
            const uint32_t eb = mxb ? (__float_as_uint(rng) + 0x7fffffu) >> 23 : 127u;
            const bool ok = !mxb || (mxb < 0x4700u && eb >= 107u && eb <= 140u && mnb >= ((eb + 8u) << 7));
            if (lane == 0) {
                s_flag[8 + wv] = (eb & 0xffu) | (ok ? 0x100u : 0u) | (mxb ? 0x200u : 0u);
                s_flag[12 + wv] = __float_as_uint(__uint_as_float(((eb + e0) & 0xffu) << 23) * (1024.0f / 1023.0f));
                s_flag[wv] = 0u;
            }
        }
        store_md();
#pragma unroll
        for (int i = 2; i < 4; i++) ring[i] = load_main(i, 0);
        WAIT_L(0);
        __syncthreads();
        const uint32_t q0 = __builtin_amdgcn_readfirstlane(s_flag[8 + 2 * cp]), q1 = __builtin_amdgcn_readfirstlane(s_flag[9 + 2 * cp]);
        fast = (q0 & q1 & 0x100u) != 0u;
        need = (q0 >> 9) | ((q1 >> 9) << 1);
        more = (__builtin_amdgcn_readfirstlane(extra.x | extra.y) | (uint32_t)(T & 3)) != 0u;
        mg0 = (((q0 & 0xffu) + 9u) << 23) | 0x400000u;
        mg1 = ((((q1 & 0xffu) + 9u) << 23) | 0x400000u) + ((0u - (mg0 >> 16)) & 0xffffu);
        if (!fast) { __builtin_amdgcn_s_waitcnt(0x0f70); return; }
        RTS(9)
        step(RR, IC<1>(), 0);
#pragma unroll 1
        for (int ff = 1; ff < 15; ff++) step(RR, IC<0>(), ff);
        step(RR, IC<2>(), 15);
        if (fin) factor(7, false);
    };
    auto irregular = [&](auto f) __attribute__((always_inline)) {
        const float fr = bf2f(freq[0]);
        const char* lg = recs + (size_t)LBASE * 128u;
        const uint16_t* tags = (const uint16_t*)(lg + (size_t)lcap * 128u);
        const uint32_t nl = meta[M_LOG], ns = meta[M_LOG + 1];
#pragma unroll 1
        for (uint32_t q = tid; q < nl * 8u; q += NT) {
            const uint32_t b = tags[q >> 3];
            if ((b & 127u) == (uint32_t)(h * 32 + j)) {
                const uint32_t* un = (const uint32_t*)(lg + (size_t)q * 16u);
                const uint32_t xw = un[3 ^ h];
#pragma unroll 1
                for (int k = 0; k < 3; k++) {
                    const uint32_t ww = un[k ^ h];
                    const float c = (float)((xw >> (k * 11)) & 0x3ffu) * (1.0f / 1023.0f);
                    if (!(ww & WNULL)) f(ww >> 16, ((ww & 0x3fffu) << 6) | (b >> 5), (ww & 0x8000u) ? -c : c);
                }
            }
        }
#pragma unroll 1
        for (uint32_t q = tid; q < ns + (uint32_t)(T & 3); q += NT) {
            const uint32_t t = q < ns ? ((const uint32_t*)tags)[-1 - (int)q] : (uint32_t)(T & ~3) + (q - ns);
            const uint32_t i = (uint32_t)iin[t], o = (uint32_t)iout[t];
            if (((o >> 6) & 31u) == (uint32_t)j && (i & 3u) == (uint32_t)h) f(((o >> 11) << 6) | (o & 63u), i, cosf(fr * bf2f(ang[t])));
        }
        __builtin_amdgcn_s_waitcnt(0x0f70);
    };

    if (h == 0) steps(IC<0>());
    else if (h == 1) steps(IC<1>());
    else if (h == 2) steps(IC<2>());
    else steps(IC<3>());
    if (fast) {
        if (more) irregular([&](uint32_t ol, uint32_t i, float cf) __attribute__((always_inline)) {
            const uint32_t md = mdc[(cp * 64u + (i & 63u)) * SLOTI + ((i >> 14) << 8) + ((i >> 4) & 0xfcu) + ((i >> 12) & 3u)];
            const float c = cf * (1023.0f / 16777216.0f);
            const uint32_t lo = __float_as_uint(__builtin_fmaf(c, h2f(md & 0xffffu), __uint_as_float(mg0)));
            const uint32_t hi = __float_as_uint(__builtin_fmaf(c, h2f(md >> 16), __uint_as_float(mg1)));
            if (cf == cf) guard |= atomicAdd(&acc[ol], (hi << 16) + lo);
            else guard |= HSLOW;
        });
    }
    volatile LDS unsigned* seen = (volatile LDS unsigned*)(uintptr_t)(A_FLAG + 4);
    if (fast && wv == 15) asm volatile("s_waitcnt vmcnt(0)\n\tds_write_b32 %0, v108 offset:16\n\tv_and_b32 v109, 0xff00ff, v108\n\tds_write_b32 %0, v109" :: "v"(vconst<A_FLAG + 4>()) : "memory");
    WAIT_L(0);
    asm volatile("v_or3_b32 %0, %0, v110, v111\n\tv_or3_b32 %0, %0, v112, v113\n\tv_or3_b32 %0, %0, v114, v115\n\tv_or3_b32 %0, %0, v116, v117\n\tv_or_b32 %0, v118, %0" : "+v"(guard));
    __syncthreads();
    RTS(10)
    const __amdgpu_buffer_rsrc_t rsP = __builtin_amdgcn_make_buffer_rsrc((void*)(partial + (size_t)j * 16 * SLOTO), 0, 16 * SLOTO * 4, 0x00020000);
    const char* pk = recs + (uint32_t)j * (SC * 128u);
    const __amdgpu_buffer_rsrc_t rsK = __builtin_amdgcn_make_buffer_rsrc((void*)pk, 0, (int)(MAINL * 128u), 0x00020000);
#define PKA (SSM * 128u)
#define PKB (32u * SC * 128u)
    auto pk_at = [&](uint32_t i) __attribute__((always_inline)) -> uint32_t {
        const uint32_t c = i >> 9;
        return (c < 12u ? (1u + (c >> 2)) * SQ + (c & 3u) * SKM : SQ + 2u * SSM) * 128u + (i & 511u) * 16u;
    };
    u32x4 p[8];
    unsigned have = 0u;
    auto fetch = [&](unsigned v, int sh) __attribute__((always_inline)) {
        const uint32_t po = tid < SHARE / 4 ? pk_at((uint32_t)(sh * (SHARE / 4) + tid)) : 0x7fffff00u;
        const unsigned nw = v & ~have;
#pragma unroll
        for (int r = 0; r < 8; r++) {
            const uint32_t k = (uint32_t)(me ^ r);
            if ((nw >> k) & 1u) p[r] = __builtin_amdgcn_raw_buffer_load_b128(rsK, po, ((v >> (16u + k)) & 1u) ? MAINL * 128u : (k & 1u) * PKA + (k >> 1) * PKB, AUX_SC1);
        }
        have |= v;
    };
    bool room = false;
    if (fast) {
        room = (__builtin_amdgcn_readfirstlane(s_flag[5]) >> (24 + (me ^ 1))) & 1u;
        const __amdgpu_buffer_rsrc_t rsW = __builtin_amdgcn_make_buffer_rsrc((void*)(pk + (uint32_t)cp * PKA + (uint32_t)h * PKB), 0, room ? (int)(MAINL * 128u) : 0, 0x00020000);
        uint32_t big = 0u;
        u32x4 v[7];
#pragma unroll
        for (int k = 0; k < 6; k++) v[k] = acc_vec(k * NT + tid);
        v[6] = (u32x4)(BIAS);
        if (wv < 2) v[6] = acc_vec(6 * NT + tid);
#pragma unroll
        for (int k = 0; k < 6; k++) __builtin_amdgcn_raw_buffer_store_b128(v[k], rsW, pk_at((uint32_t)tid) + ((k >> 1) * SQ + (k & 1) * 2u * SKM) * 128u, 0, AUX_SC1);
        if (wv < 2) __builtin_amdgcn_raw_buffer_store_b128(v[6], rsW, (SQ + 2u * SSM) * 128u + (uint32_t)tid * 16u, 0, AUX_SC1);
        __builtin_amdgcn_sched_barrier(0);
#pragma unroll
        for (int k = 0; k < 28; k++) { guard |= v[k >> 2][k & 3]; big |= v[k >> 2][k & 3] + 0x42004200u; }
        if (guard & HSLOW) s_flag[0] = 1u;
        if (big & 0x7c00u) s_flag[2] = 1u;
        if (big & 0x7c000000u) s_flag[3] = 2u;
        __builtin_amdgcn_sched_barrier(0);
        p[0] = acc_vec(me * (SHARE / 4) + min(tid, SHARE / 4 - 1));
        have = 1u << me;
        if (fin) fetch(__builtin_amdgcn_readfirstlane(*seen), me);
    }
    asm volatile("s_waitcnt vmcnt(0)" ::: "memory");
    __syncthreads();
    RTS(11)
    const bool redo = !fast || __builtin_amdgcn_readfirstlane(s_flag[0]) != 0u || (need & ~__builtin_amdgcn_readfirstlane(s_flag[2] | s_flag[3])) != 0u;
    const bool slow = redo || !room;
    if (slow) {
        float* fs = (float*)acc;
        float w[16];
        load_w(wdown, w);
        const float fr = bf2f(freq[0]);
#pragma unroll 1
        for (int e = 0; e < 2; e++) {
            if (redo) {
                __syncthreads();
                for (int i = tid; i < SLOTO; i += NT) fs[i] = 0.f;
                __syncthreads();
                const __amdgpu_buffer_rsrc_t rsO = __builtin_amdgcn_make_buffer_rsrc((void*)iout, 0, T * 4, 0x00020000);
                const __amdgpu_buffer_rsrc_t rsI = __builtin_amdgcn_make_buffer_rsrc((void*)iin, 0, T * 4, 0x00020000);
#pragma unroll 1
                for (int t0 = tid * 4; t0 < T; t0 += NT * 16) {
                    u32x4 o4[4], i4[4];
#pragma unroll
                    for (int g = 0; g < 4; g++) {
                        o4[g] = __builtin_amdgcn_raw_buffer_load_b128(rsO, (uint32_t)(t0 + g * NT * 4) * 4u, 0, 0);
                        i4[g] = __builtin_amdgcn_raw_buffer_load_b128(rsI, (uint32_t)(t0 + g * NT * 4) * 4u, 0, 0);
                    }
                    uint32_t hit = 0u;
#pragma unroll
                    for (int k = 0; k < 16; k++) hit |= (bfi(3u, i4[k >> 2][k & 3], o4[k >> 2][k & 3]) & 0x7c3u) == (uint32_t)(j * 64 + h) ? 1u << k : 0u;
#pragma unroll 1
                    for (; hit; hit &= hit - 1u) {
                        const int k = __builtin_ctz(hit), t = t0 + (k >> 2) * NT * 4 + (k & 3);
                        if (t >= T) break;
                        const uint32_t o = (uint32_t)iout[t];
                        uint32_t rr[4];
                        md_of(m[iin[t]], w, rr);
                        atomicAdd(&fs[((o >> 11) << 6) | (o & 63u)], cosf(fr * bf2f(ang[t])) * bf2f(cp ? (e ? rr[3] : rr[2]) : (e ? rr[1] : rr[0])));
                    }
                }
                __syncthreads();
            }
            const float un = __uint_as_float(s_flag[12 + 2 * cp + e]);
            for (int i = tid; i < SLOTO / 4; i += NT) {
                u32x4 v = acc_vec(i);
                if (!redo) {
#pragma unroll
                    for (int k = 0; k < 4; k++) v[k] = __float_as_uint((float)((int)((v[k] >> (16 * e)) & 0xffffu) - 0x4000) * un);
                }
                __builtin_amdgcn_raw_buffer_store_b128(v, rsP, (uint32_t)((e * 8 + me) * SLOTO * 4 + i * 16), 0, AUX_SC1);
            }
        }
        load_wl();
        edges(me);
#pragma unroll
        for (int k = 0; k < 8; k++) p[k] = (u32x4)(0u);
        have = 0u;
        asm volatile("s_waitcnt vmcnt(0)" ::: "memory");
        __syncthreads();
    }
    if (tid == NT - 64) {
        const unsigned inc = (slow ? 0x10001u : 1u) << me;
        unsigned v = __hip_atomic_fetch_add(dw, inc, __ATOMIC_RELAXED, __HIP_MEMORY_SCOPE_AGENT) + inc;
        unsigned quit = 0u;
        if ((v & 0xffu) != 0xffu) {
            unsigned spins = 0;
            do {
                *seen = v & 0xff00ffu;
                __builtin_amdgcn_s_sleep(1);
                v = __hip_atomic_load(dw, __ATOMIC_RELAXED, __HIP_MEMORY_SCOPE_AGENT);
                if (++spins > SPINCAP && (v & 0xffu) != 0xffu) {
                    v = __hip_atomic_fetch_add(dw, 256u << me, __ATOMIC_RELAXED, __HIP_MEMORY_SCOPE_AGENT);
                    if ((v & 0xffu) != 0xffu) { v = 0xffu; quit = 0x80000000u; }
                }
            } while ((v & 0xffu) != 0xffu);
            v &= 0xff00ffu;
        }
        *seen = (v & 0xffffffu) | quit;
    }
    if (!fin) return;
    have = __builtin_amdgcn_readfirstlane(have);
    uint32_t un[4];
#pragma unroll
    for (int c = 0; c < 4; c++) un[c] = __builtin_amdgcn_readfirstlane(s_flag[12 + c]);
    unsigned sv;
    {
        const u32x4 rsQ = {(uint32_t)(uintptr_t)pk, (uint32_t)((uintptr_t)pk >> 32) & 0xffffu, MAINL * 128u, 0x00020000u};
        const uint32_t po = tid < SHARE / 4 ? pk_at((uint32_t)(me * (SHARE / 4) + tid)) : 0x7fffff00u;
        do {
            sv = __builtin_amdgcn_readfirstlane(*seen);
            uint32_t t0, t1, t2;
#define FET(R) "s_xor_b32 %8, %13, " #R "\n\ts_bitcmp1_b32 %14, %8\n\ts_cbranch_scc0 1f\n\ts_and_b32 %9, %8, 1\n\ts_mul_i32 %9, %9, %16\n\ts_lshr_b32 %10, %8, 1\n\t" \
               "s_mul_i32 %10, %10, %17\n\ts_add_u32 %9, %9, %10\n\ts_bitcmp1_b32 %15, %8\n\ts_cselect_b32 %9, %18, %9\n\tbuffer_load_dwordx4 %" #R ", %11, %12, %9 offen sc1\n1:\n\t"
            asm volatile("s_and_b32 %8, %14, 0xff\n\ts_cbranch_scc0 2f\n\ts_nop 2\n\t" FET(0) FET(1) FET(2) FET(3) FET(4) FET(5) FET(6) FET(7) "\n2:\n\ts_sleep 2\n\ts_waitcnt vmcnt(0)"
                         : "+v"(p[0]), "+v"(p[1]), "+v"(p[2]), "+v"(p[3]), "+v"(p[4]), "+v"(p[5]), "+v"(p[6]), "+v"(p[7]), "=&s"(t0), "=&s"(t1), "=&s"(t2)
                         : "v"(po), "s"(rsQ), "s"(me), "s"((int)sv < 0 ? 0u : sv & ~have), "s"(sv >> 16), "n"(PKA), "n"(PKB), "n"(MAINL * 128u) : "memory", "scc");
            have |= sv;
        } while ((sv & 0xffu) != 0xffu);
    }
    if ((int)sv < 0) return;
    RTS(12)
    const unsigned flt = (sv >> 16) & 0xffu;
    auto finish = [&](int sh) __attribute__((always_inline)) {
        {
            const int ol = sh * SHARE + tid * 4;
            const uint32_t po = tid < SHARE / 4 ? (uint32_t)ol * 4u : 0x7fffff00u;
            float g[4][4];
            u32x4 a[2] = {p[0] + p[2], p[1] + p[3]}, b[2] = {p[4] + p[6], p[5] + p[7]};
            if (cp) { const u32x4 t = a[0], u = b[0]; a[0] = a[1]; b[0] = b[1]; a[1] = t; b[1] = u; }
#pragma unroll
            for (int c = 0; c < 2; c++) {
                const int nb = 0x10000 - 0x4000 * __builtin_popcount(flt & (0x55u << c));
                const float u0 = __uint_as_float(nb ? un[2 * c] : 0u), u1 = __uint_as_float(nb ? un[2 * c + 1] : 0u);
#pragma unroll
                for (int q = 0; q < 4; q++) {
                    g[q][2 * c] = (float)((int)((a[c][q] & 0xffffu) + (b[c][q] & 0xffffu)) - nb) * u0;
                    g[q][2 * c + 1] = (float)((int)((a[c][q] >> 16) + (b[c][q] >> 16)) - nb) * u1;
                }
            }
            if (__builtin_expect(flt != 0u, 0)) {
#pragma unroll
                for (int c = 0; c < 2; c++) {
#pragma unroll 1
                    for (int k = c; k < 8; k += 2) {
                        if (!((flt >> k) & 1u)) continue;
                        const u32x4 f0 = __builtin_amdgcn_raw_buffer_load_b128(rsP, po, k * SLOTO * 4, AUX_SC1);
                        const u32x4 f1 = __builtin_amdgcn_raw_buffer_load_b128(rsP, po, (8 + k) * SLOTO * 4, AUX_SC1);
#pragma unroll
                        for (int q = 0; q < 4; q++) { g[q][2 * c] += __uint_as_float(f0[q]); g[q][2 * c + 1] += __uint_as_float(f1[q]); }
                    }
                }
            }
            float wu[16];
#pragma unroll
            for (int k = 0; k < 8; k++) {
                const uint32_t x = __builtin_amdgcn_readlane(wl, 40 + k);
                wu[2 * k] = bf2f(x & 0xffffu); wu[2 * k + 1] = bf2f(x >> 16);
            }
            u32x4 ov[2];
#pragma unroll
            for (int q = 0; q < 4; q++) {
#pragma unroll
                for (int k = 0; k < 4; k++) g[q][k] *= fa[q][k >> 1][k & 1];
                const uint32_t m0 = em[q >> 1][(q & 1) * 2], m1 = em[q >> 1][(q & 1) * 2 + 1];
                const float mm[4] = {bf2f(m0 & 0xffff), bf2f(m0 >> 16), bf2f(m1 & 0xffff), bf2f(m1 >> 16)};
                float o[4];
#pragma unroll
                for (int k = 0; k < 4; k++) o[k] = mm[k] + (g[q][0] * wu[k] + g[q][1] * wu[4 + k] + g[q][2] * wu[8 + k] + g[q][3] * wu[12 + k]) * fa[q][2 + (k >> 1)][k & 1];
                ov[q >> 1][(q & 1) * 2] = pk_bf16(o[0], o[1]);
                ov[q >> 1][(q & 1) * 2 + 1] = pk_bf16(o[2], o[3]);
            }
            const __amdgpu_buffer_rsrc_t rsU = __builtin_amdgcn_make_buffer_rsrc((void*)out, 0, E * 8, 0x00020000);
            const uint32_t uo = edge0(sh) * 8u;
            __builtin_amdgcn_raw_buffer_store_b128(ov[0], rsU, uo, 0, AUX_NT);
            __builtin_amdgcn_raw_buffer_store_b128(ov[1], rsU, uo + 256u, 0, AUX_NT);
        }
    };
    finish(me);
#pragma unroll 1
    for (unsigned todo = (sv >> 8) & 0xffu; todo; todo &= todo - 1u) {
        const int sh = __builtin_ctz(todo);
        edges(sh);
        have = 0u;
        fetch(sv, sh);
        finish(sh);
    }
    RTS(13)
}

__global__ __launch_bounds__(256) void gen_scatter_kernel(const int* __restrict__ iin, const int* __restrict__ iout,
                                                          const uint16_t* __restrict__ ang, const uint16_t* __restrict__ freq,
                                                          const u32x2* __restrict__ m, const uint16_t* __restrict__ wdown,
                                                          float* __restrict__ agg, int T) {
    float w[16];
    load_w(wdown, w);
    const float fr = bf2f(freq[0]);
    for (long t = (long)blockIdx.x * 256 + threadIdx.x; t < T; t += (long)gridDim.x * 256) {
        const float c = cosf(fr * bf2f(ang[t]));
        uint32_t r[4];
        md_of(m[iin[t]], w, r);
        float* q = agg + (size_t)iout[t] * 4;
#pragma unroll
        for (int k = 0; k < 4; k++) atomicAdd(q + k, c * bf2f(r[k]));
    }
}
__global__ __launch_bounds__(256) void gen_final_kernel(const u32x2* __restrict__ m, const uint16_t* __restrict__ dist,
                                                        const uint16_t* __restrict__ centers, const uint16_t* __restrict__ wrbf,
                                                        const uint16_t* __restrict__ wsbf, const uint16_t* __restrict__ wup,
                                                        const float* __restrict__ agg, u32x2* __restrict__ out, int E) {
    const int e = blockIdx.x * 256 + threadIdx.x;
    if (e >= E) return;
    const float d = bf2f(dist[e]);
    float rs[4] = {0, 0, 0, 0}, rg[4] = {0, 0, 0, 0};
#pragma unroll 1
    for (int r = 0; r < 8; r++) {
        const float x = d - bf2f(centers[r]);
        const float rb = __expf(-10.0f * x * x);
#pragma unroll
        for (int k = 0; k < 4; k++) { rs[k] += rb * bf2f(wsbf[r * 4 + k]); rg[k] += rb * bf2f(wrbf[r * 4 + k]); }
    }
    float wu[16];
    load_w(wup, wu);
    const u32x2 mv = m[e];
    const float mm[4] = {bf2f(mv.x & 0xffff), bf2f(mv.x >> 16), bf2f(mv.y & 0xffff), bf2f(mv.y >> 16)};
    float g[4], o[4];
#pragma unroll
    for (int k = 0; k < 4; k++) g[k] = agg[(size_t)e * 4 + k] * rs[k];
#pragma unroll
    for (int k = 0; k < 4; k++) o[k] = mm[k] + (g[0] * wu[k] + g[1] * wu[4 + k] + g[2] * wu[8 + k] + g[3] * wu[12 + k]) * rg[k];
    const u32x2 ov = {f2bf(o[0]) | (f2bf(o[1]) << 16), f2bf(o[2]) | (f2bf(o[3]) << 16)};
    out[e] = ov;
}

extern "C" void kernel_launch(void* const* d_in, const int* in_sizes, int n_in,
                              void* d_out, int out_size, void* d_ws, size_t ws_size,
                              hipStream_t stream) {
    const u32x2* m = (const u32x2*)d_in[0];
    const int* iin = (const int*)d_in[1];
    const int* iout = (const int*)d_in[2];
    const uint16_t* dist = (const uint16_t*)d_in[3];
    const uint16_t* ang = (const uint16_t*)d_in[4];
    const uint16_t* centers = (const uint16_t*)d_in[5];
    const uint16_t* freq = (const uint16_t*)d_in[6];
    const uint16_t* wrbf = (const uint16_t*)d_in[7];
    const uint16_t* wsbf = (const uint16_t*)d_in[8];
    const uint16_t* wdown = (const uint16_t*)d_in[9];
    const uint16_t* wup = (const uint16_t*)d_in[10];
    const int E = in_sizes[3];
    const int T = in_sizes[1];
    char* ws = (char*)d_ws;
    const int G = T >> 2;
    const int ntile = (G + GR - 1) / GR;
    const int nstat = ntile / NWG - NDYN > 4 ? ntile / NWG - NDYN : 4;
    const int lcap = G / 6 + 2;
    char* recs = ws + W_RECS;
    size_t off = W_RECS;
    const size_t rbytes = (size_t)LBASE * 128 + (size_t)lcap * 130;
    off += rbytes;
    if (E > EMAX || (E & 3) || E < 1 || rbytes > 0x70000000u || off > ws_size) {
        (void)hipMemsetAsync(ws, 0, (size_t)E * 16, stream);
        gen_scatter_kernel<<<4096, 256, 0, stream>>>(iin, iout, ang, freq, m, wdown, (float*)ws, T);
        gen_final_kernel<<<(E + 255) / 256, 256, 0, stream>>>(m, dist, centers, wrbf, wsbf, wup, (const float*)ws, (u32x2*)d_out, E);
        return;
    }
    int ks = T / (NWG * NB * 24);
    if (ks > KMAX) ks = KMAX;
    const float zf = ZSIG * ZSIG * (float)T / (8.0f * (float)E * (float)E);
    (void)hipFuncSetAttribute((const void*)part_kernel, hipFuncAttributeMaxDynamicSharedMemorySize, P_LDS);
    (void)hipFuncSetAttribute((const void*)acc_kernel, hipFuncAttributeMaxDynamicSharedMemorySize, A_LDS);
    part_kernel<<<NWG, NT, P_LDS, stream>>>(iin, iout, ang, freq, m, wdown, recs, T, E, ntile, nstat, ks, lcap);
    acc_kernel<<<NWG, NT, A_LDS, stream>>>(ws, T, E, ks, lcap, zf, 0, centers, wrbf, wsbf, wup, m, dist, (u32x2*)d_out, iin, iout, ang, wdown, freq);
}
```

```cpp
#include <hip/hip_runtime.h>
#include <hip/hip_bf16.h>
#include <stdint.h>
#pragma clang diagnostic ignored "-Winline-asm"

typedef unsigned int u32x4 __attribute__((ext_vector_type(4)));
typedef unsigned int u32x2 __attribute__((ext_vector_type(2)));
typedef unsigned short u16x2 __attribute__((ext_vector_type(2)));
typedef __bf16 bf16x2 __attribute__((ext_vector_type(2)));
typedef float f32x2 __attribute__((ext_vector_type(2)));
typedef unsigned long long u64;

static __device__ __forceinline__ float bf2f(uint32_t bits16) { return __uint_as_float(bits16 << 16); }
static __device__ __forceinline__ uint32_t f2bf(float f) {
    uint32_t u = __float_as_uint(f);
    if ((u & 0x7fffffffu) > 0x7f800000u) return (u >> 16) | 0x40;
    return (u + 0x7fffu + ((u >> 16) & 1u)) >> 16;
}
static __device__ __forceinline__ float h2f(uint32_t bits16) { return (float)__builtin_bit_cast(_Float16, (unsigned short)bits16); }

#define NB 2048
#define WNULL 0x80000000u
#define CSTEP 0x2aabu
#define CINIT 205u
#define CHV (12u * CSTEP)
#define SLOTO 25088
#define SLOTI 12544
#define EMAX (SLOTO * 32)
#define NT 1024
#define NWG 256
#define KMAX 4
#define NTAIL 4
#define SC 64u
#define SKM (128u * SC + 2u)
#define SSM (4u * SKM + 1u)
#define SSH (128u * SC + 1u)
#define SKH (16u * SSH + 2u)
#define SQ (16u * SSM)
#define MAINL (4u * SQ)
#define BPL(b) ((((b) >> 3) & 3u) * SQ + ((b) & 7u) + (((b) >> 5) << 3))
#define TST 257u
#define TBASE MAINL
#define TPL (NB * TST)
#define SBASE (TBASE + 2u * TPL)
#define LBASE (SBASE + 128u * 2u * NWG * 9u)
#define NDYN 3
#define NEARM 0x55555554u
#define GR 2048
#define M_DONE 0
#define M_TILE (32 * 64)
#define M_LOG (M_TILE + 64)
#define M_MD (M_LOG + 64)
#define M_BEAT (M_MD + 64)
#define M_E0 (M_BEAT + 64)
#define M_WORDS (M_E0 + 64)
#define W_TCNT (M_WORDS * 4)
#define W_STAT (W_TCNT + NWG * NB)
#define W_MDC (W_STAT + 4 * NWG * 8)
#define W_PART (W_MDC + 128 * SLOTI * 4)
#define W_RECS (W_PART + 32 * 16 * SLOTO * 4)
#define AUX_NT 2
#define AUX_SC1 16

static __device__ __forceinline__ uint32_t row_max(uint32_t x) {
    x = max(x, (uint32_t)__builtin_amdgcn_update_dpp(0, x, 0x111, 0xf, 0xf, false));
    x = max(x, (uint32_t)__builtin_amdgcn_update_dpp(0, x, 0x112, 0xf, 0xf, false));
    x = max(x, (uint32_t)__builtin_amdgcn_update_dpp(0, x, 0x114, 0xf, 0xf, false));
    return max(x, (uint32_t)__builtin_amdgcn_update_dpp(0, x, 0x118, 0xf, 0xf, false));
}
static __device__ __forceinline__ uint32_t wave_max(uint32_t x) {
    x = row_max(x);
    x = max(x, (uint32_t)__builtin_amdgcn_update_dpp(0, x, 0x142, 0xa, 0xf, false));
    x = max(x, (uint32_t)__builtin_amdgcn_update_dpp(0, x, 0x143, 0xc, 0xf, false));
    return __builtin_amdgcn_readlane(x, 63);
}
static __device__ __forceinline__ float row_sum(float x) {
    x += __int_as_float(__builtin_amdgcn_update_dpp(0, __float_as_int(x), 0x111, 0xf, 0xf, false));
    x += __int_as_float(__builtin_amdgcn_update_dpp(0, __float_as_int(x), 0x112, 0xf, 0xf, false));
    x += __int_as_float(__builtin_amdgcn_update_dpp(0, __float_as_int(x), 0x114, 0xf, 0xf, false));
    return x + __int_as_float(__builtin_amdgcn_update_dpp(0, __float_as_int(x), 0x118, 0xf, 0xf, false));
}
static __device__ __forceinline__ float wave_sum(float x) {
    x = row_sum(x);
    x += __int_as_float(__builtin_amdgcn_update_dpp(0, __float_as_int(x), 0x142, 0xa, 0xf, false));
    x += __int_as_float(__builtin_amdgcn_update_dpp(0, __float_as_int(x), 0x143, 0xc, 0xf, false));
    return __int_as_float(__builtin_amdgcn_readlane(__float_as_int(x), 63));
}
static __device__ __forceinline__ uint32_t bfi(uint32_t mask, uint32_t a, uint32_t b) {
    return __builtin_amdgcn_bitop3_b32(mask, a, b, 0xca);
}
template <uint32_t C> static __device__ __forceinline__ uint32_t vconst() {
    uint32_t r;
    asm volatile("v_mov_b32 %0, %1" : "=v"(r) : "n"(C));
    return r;
}
static __device__ __forceinline__ uint32_t pk_bf16(float a, float b) {
    const f32x2 v = {a, b};
    return __builtin_bit_cast(uint32_t, __builtin_convertvector(v, bf16x2));
}
static __device__ __forceinline__ void pin(uint32_t& x) { asm volatile("" : "+v"(x)); }


static __device__ __forceinline__ void md_of(u32x2 v, const float* w, uint32_t* r) {
    float a0 = bf2f(v.x & 0xffff), a1 = bf2f(v.x >> 16), a2 = bf2f(v.y & 0xffff), a3 = bf2f(v.y >> 16);
#pragma unroll
    for (int j = 0; j < 4; j++) r[j] = f2bf(a0 * w[j] + a1 * w[4 + j] + a2 * w[8 + j] + a3 * w[12 + j]);
}
static __device__ __forceinline__ void load_w(const uint16_t* __restrict__ wdown, float* w) {
    u32x4 wv0 = *(const u32x4*)wdown, wv1 = *(const u32x4*)(wdown + 8);
#pragma unroll
    for (int i = 0; i < 4; i++) {
        w[2 * i] = bf2f(wv0[i] & 0xffffu); w[2 * i + 1] = bf2f(wv0[i] >> 16);
        w[8 + 2 * i] = bf2f(wv1[i] & 0xffffu); w[8 + 2 * i + 1] = bf2f(wv1[i] >> 16);
    }
}
#define LDS __attribute__((address_space(3)))
#define P_CNT 0
#define P_FLAG ((NB + 64) * 4)
#define P_LST (P_FLAG + 256)
#define LCAP 32
#define P_TAB (P_LST + 4 * LCAP * 16)
#define P_BUF (P_TAB + NB * 4)
#define P_LDS (P_BUF + NB * 64)
#define PD 2
#define LPOS 0x448763
#define SETP(n) __builtin_amdgcn_s_setprio(n)
#define MDL 3
#define RMAXREV 40.0f
#define HB 88
#define HREG(S, D) (HB + ((S) & 3) * 10 + (((S) >> 2) + (D)) * 2)

template <int N> struct IC { static constexpr int value = N; };
struct Raw { u32x4 o[2], i[2]; u32x2 a[2]; };
static __device__ __forceinline__ uint32_t lds_ld(uint32_t a) { return *(const LDS uint32_t*)(uintptr_t)a; }
static __device__ __forceinline__ void lds_st(uint32_t a, uint32_t v) { *(LDS uint32_t*)(uintptr_t)a = v; }
static __device__ __forceinline__ void lds_or(uint32_t a, uint32_t v) {
    __hip_atomic_fetch_or((LDS uint32_t*)(uintptr_t)a, v, __ATOMIC_RELAXED, __HIP_MEMORY_SCOPE_WORKGROUP);
}
static __device__ __forceinline__ uint32_t lds_add(uint32_t a, uint32_t v) {
    return __hip_atomic_fetch_add((LDS uint32_t*)(uintptr_t)a, v, __ATOMIC_RELAXED, __HIP_MEMORY_SCOPE_WORKGROUP);
}
static __device__ __forceinline__ uint32_t slot_w(uint32_t ca, uint32_t cv) {
    return (ca << 4) | __builtin_amdgcn_bitop3_b32(cv >> 11, ca, 0x3cu, 0x28);
}
static __device__ __forceinline__ uint32_t slot_x2(uint32_t aw, uint32_t ca) {
    return __builtin_amdgcn_bitop3_b32(aw, ca, 0xcu, 0x72);
}
static __device__ __forceinline__ uint32_t slot_x(uint32_t aw) { return slot_x2(aw, aw >> 4); }
template <int R> static __device__ __forceinline__ u32x2 hget() {
    u32x2 d;
    asm volatile("v_mov_b64 %0, v[%1:%2]" : "=v"(d) : "n"(R), "n"(R + 1));
    return d;
}
static __device__ __forceinline__ u32x2 hget_at(int r) {
    u32x2 d;
    asm volatile("s_set_gpr_idx_on %2, gpr_idx(SRC0)\n\ts_nop 1\n\tv_mov_b32 %0, v88\n\tv_mov_b32 %1, v89\n\ts_set_gpr_idx_off"
                 : "=&v"(d.x), "=&v"(d.y) : "s"(r));
    return d;
}
static __device__ __forceinline__ void hset_at(int r, u32x2 d) {
    asm volatile("s_set_gpr_idx_on %2, gpr_idx(DST)\n\ts_nop 1\n\tv_mov_b32 v88, %0\n\tv_mov_b32 v89, %1\n\ts_set_gpr_idx_off"
                 :: "v"(d.x), "v"(d.y), "s"(r));
}

template <int S, int D> static __device__ __forceinline__ uint32_t take(uint32_t V, uint32_t u16, uint32_t la, u32x2 nul) {
    const uint32_t a = __builtin_amdgcn_bitop3_b32(V, u16, 15u, 0x54);
    asm volatile("v_mov_b64 v[%4:%4+1], v[%3:%3+1]\n\tv_cmpx_ne_u32 vcc, -16, %0\n\tds_wrxchg_rtn_b64 v[%4:%4+1], %1, %2 offset:%5\n\ts_mov_b64 exec, -1"
                 :: "v"(a), "v"(la), "v"(nul), "n"(HREG(S, D)), "n"(HREG(S, 1 - D)), "n"(S * 256) : "memory", "vcc");
    return a;
}
#define PUT(P) asm volatile("s_bitcmp1_b32 %3, %4\n\ts_cbranch_scc0 1f\n\ts_waitcnt lgkmcnt(%5)\n\tbuffer_store_dwordx4 v[%2:%2+3], %0, %1, 0 offen sc1\n" \
                 "1:\n\ts_bitcmp1_b32 %3, %4+1\n\ts_cbranch_scc0 2f\n\ts_waitcnt lgkmcnt(%5)\n\tbuffer_store_dwordx4 v[%2:%2+3], %0, %1, 0 offen " P "\n\ts_nop 1\n2:" \
                 :: "v"(ad), "s"(rsA), "n"(HREG(S, 0)), "s"(pm), "n"(2 * S), "n"(W) : "memory", "scc")
template <int S, int W, bool WT> static __device__ __forceinline__ void put(u32x4 rsA, uint32_t ad, uint32_t pm) {
    if (WT) PUT("sc1 nt"); else PUT("nt");
}

__global__ __launch_bounds__(NT, 4) __attribute__((amdgpu_num_vgpr(HB / 2))) void part_kernel(const int* __restrict__ iin, const int* __restrict__ iout,
                                                  const uint16_t* __restrict__ ang, const uint16_t* __restrict__ freq,
                                                  const u32x2* __restrict__ m, const uint16_t* __restrict__ wdown,
                                                  char* __restrict__ recs,
                                                  int T, int E, int ntile, int nstat, int ks, int lcap) {
    const int tid0 = threadIdx.x;
    const int wv = __builtin_amdgcn_readfirstlane(tid0 >> 6);
    const int G = T >> 2;
    const int pos = (int)((blockIdx.x & 7) << 5) | (int)(blockIdx.x >> 3);
    float frr;
    unsigned* meta = (unsigned*)(recs - W_RECS);
    const uint32_t gbase = (LBASE + (uint32_t)lcap) * 128u;
    const uint32_t rbytes = gbase + (uint32_t)lcap * 2u;
    const __amdgpu_buffer_rsrc_t rsR = __builtin_amdgcn_make_buffer_rsrc(recs, 0, (int)rbytes, 0x00020000);
    const u32x4 rsA = {(uint32_t)(uintptr_t)recs, (uint32_t)((uintptr_t)recs >> 32) & 0xffffu, rbytes, 0x00020000u};
    const uint32_t g2 = 2u * (uint32_t)ks;
    const uint32_t far = 1u >> ((blockIdx.x >> 3) & 3u);
    const uint32_t SK = SKM + far * (SKH - SKM), SS = SSM + far * (SSH - SSM), UINC = 0x80000001u + SK * 64u;
    const uint32_t spec0 = gbase - 4u;
    auto fixed_tile = [&](int r) __attribute__((always_inline)) -> int { return r * NWG + ((pos + r * 37) & (NWG - 1)); };
    int tl0 = fixed_tile(0), tl1 = fixed_tile(1), tl2;
    uint32_t tk = 0u;
    const __amdgpu_buffer_rsrc_t rsO = __builtin_amdgcn_make_buffer_rsrc((void*)iout, 0, G * 16, 0x00020000);
    const __amdgpu_buffer_rsrc_t rsI = __builtin_amdgcn_make_buffer_rsrc((void*)iin, 0, G * 16, 0x00020000);
    const __amdgpu_buffer_rsrc_t rsG = __builtin_amdgcn_make_buffer_rsrc((void*)ang, 0, G * 8, 0x00020000);
    uint32_t t8 = (uint32_t)tid0 * 8u;
    auto load_one = [&](Raw& r, int tl, int idx) __attribute__((always_inline)) {
        const int j = idx < 4 ? idx >> 1 : idx & 1;
        const uint32_t g0 = (uint32_t)(tl * GR + j * NT);
        __builtin_amdgcn_sched_barrier(0);
        if (idx >= 4) r.a[j] = __builtin_amdgcn_raw_buffer_load_b64(rsG, t8, g0 * 8u, AUX_NT);
        else if (idx & 1) r.i[j] = __builtin_amdgcn_raw_buffer_load_b128(rsI, t8 + t8, g0 * 16u, AUX_NT);
        else r.o[j] = __builtin_amdgcn_raw_buffer_load_b128(rsO, t8 + t8, g0 * 16u, AUX_NT);
        __builtin_amdgcn_sched_barrier(0);
    };
    auto peek = [&](uint32_t at) __attribute__((always_inline)) -> uint32_t {
        return __hip_atomic_load((unsigned*)((char*)meta + at), __ATOMIC_RELAXED, __HIP_MEMORY_SCOPE_AGENT);
    };
    auto ready = [&]() __attribute__((always_inline)) {
        if (blockIdx.x != 0) {
            const uint32_t x = lds_ld(vconst<P_FLAG + 8>());
            while (peek(vconst<M_BEAT * 4>()) == x) __builtin_amdgcn_s_sleep(8);
        }
    };
    auto beat = [&]() __attribute__((always_inline)) {
        (void)__hip_atomic_fetch_add((unsigned*)((char*)meta + vconst<M_BEAT * 4>()), vconst<1>(), __ATOMIC_RELAXED, __HIP_MEMORY_SCOPE_AGENT);
    };
    auto draw = [&](uint32_t at) __attribute__((always_inline)) -> uint32_t {
        return __hip_atomic_fetch_add((unsigned*)((char*)meta + at), vconst<1>(), __ATOMIC_RELAXED, __HIP_MEMORY_SCOPE_AGENT);
    };
    uint32_t la;
    auto tab_at = [&]() __attribute__((always_inline)) -> uint32_t {
        return P_TAB + bfi(0xffu, bfi(0x3cu, t8 >> 2, t8 << 4), t8);
    };
    u32x2 nul;
    uint32_t U[2];
    {
        const uint32_t t = (uint32_t)tid0;
        la = P_BUF + ((t >> 5) << 12) + ((t & 0x18u) << 3) + ((t & 3u) << 4) + ((t & 4u) << 1);
        const uint32_t q = ((t >> 3) ^ (t >> 1 & 2u)) & 3u;
        nul.x = (0x7u >> q) << 31; nul.y = (0xbu >> q) << 31;
        U[0] = (((t >> 1) & 15u) * SS + ((t & 1u) * 64u + (t >> 5)) * SC + BPL(blockIdx.x)) * 128u + (0x80000000u - SK * 64u) + 8u - g2;
        U[1] = U[0] + 32u * SC * 128u;
    }
    auto log_line = [&](bool lg, uint32_t b) __attribute__((always_inline)) -> uint32_t {
        uint32_t lp = 0u;
        if (lg && (t8 & 0x38u) == 0u) {
            ready();
            lp = draw(vconst<M_LOG * 4>());
            __builtin_amdgcn_raw_buffer_store_b16((uint16_t)b, rsR, gbase + lp * 2u, 0, 0);
        }
        return LBASE + (uint32_t)__builtin_amdgcn_ds_bpermute((int)((t8 >> 1) & 0xe0u), (int)lp);
    };
    auto general = [&](int D, bool all, u64 sm) __attribute__((always_inline)) {
        uint32_t todo = __builtin_amdgcn_readfirstlane((uint32_t)sm | (uint32_t)(sm >> 32));
#pragma unroll 1
        while (todo) {
            const uint32_t S = (uint32_t)__builtin_ctz(todo) >> 1;
            todo &= ~(3u << (2u * S));
            pin(t8); pin(la);
            const uint32_t V = lds_ld(P_TAB + (t8 & ~63u) + S * 4u), g = V & 15u;
            const uint32_t col = S * 128u + ((t8 >> 1) & 0x60u) + (t8 >> 8);
            const bool act = all ? V != 0xffffffffu : (V & 0x80000009u) == 9u;
            const int hr = HREG((int)S, D) - HB;
            uint32_t ln = log_line(act && g > 8u + NTAIL, col);
            if (act) {
                const u32x2 N = *(const LDS u32x2*)(uintptr_t)(la + S * 256u);
                *(LDS u32x2*)(uintptr_t)(la + S * 256u) = nul;
                lds_st(P_TAB + (t8 & ~63u) + S * 4u, vconst<0xffffffffu>());
                if (!(g & 1u)) hset_at(hr, N);
                else {
                    uint32_t a, st;
                    if (g < 8u) { a = (V & ~15u) + ((t8 & 0x38u) << 1); st = 8u; }
                    else {
                        if (g <= 8u + NTAIL) ln = TBASE + ((g - 8u) >> 1) * TPL + col * TST + blockIdx.x;
                        a = ln * 128u + (la & 0x38u); st = 64u;
                    }
                    __builtin_amdgcn_raw_buffer_store_b64(hget_at(hr), rsR, a, 0, AUX_SC1);
                    __builtin_amdgcn_raw_buffer_store_b64(N, rsR, a + st, 0, AUX_SC1);
                }
            }
        }
    };
    unsigned epoch = 0;
    u64 pb2 = 0ull;
    u64 fm = 0ull, sm = 0ull;
    auto look = [&](u32x2 c) __attribute__((always_inline)) {
        uint32_t V[2];
        const bool late = __builtin_amdgcn_ballot_w64(((U[0] | U[1]) & 8u) != 0u) != 0ull;
        epoch++;
        asm volatile("v_mov_b32 %0, -1\n\tv_mov_b32 %1, -1\n\t"
                     "v_cmpx_le_u32 %6, %7, %2\n\tv_mov_b32 %0, %4\n\tv_add_u32 %4, %9, %4\n\tv_subrev_u32 %2, %8, %2\n\ts_mov_b64 exec, -1\n\t"
                     "v_cmpx_le_u32 vcc, %8, %3\n\tv_mov_b32 %1, %5\n\tv_add_u32 %5, %9, %5\n\tv_subrev_u32 %3, %8, %3\n\t"
                     "s_or_b64 %6, %6, exec\n\ts_mov_b64 exec, -1"
                     : "=&v"(V[0]), "=&v"(V[1]), "+v"(c.x), "+v"(c.y), "+v"(U[0]), "+v"(U[1]), "=&s"(fm) : "s"(CHV), "n"(CHV), "s"(UINC) : "vcc", "scc");
        *(LDS u32x2*)(uintptr_t)(P_CNT + t8) = c;
        pin(t8);
        const uint32_t tw = tab_at();
        lds_st(tw, V[0]);
        lds_st(tw + 64u, V[1]);
        if (wv == 1) lds_st(P_CNT + NB * 4 - 256 + (t8 >> 1), vconst<0x40000000u>());
        pb2 = __builtin_amdgcn_ballot_w64((int)(V[0] & V[1]) >= 0);
        sm = 0ull;
        if (__builtin_expect(late, 0)) {
            sm = __builtin_amdgcn_ballot_w64((V[0] & 0x80000009u) == 9u || (V[1] & 0x80000009u) == 9u);
#pragma unroll
            for (int e = 0; e < 2; e++)
                if ((V[e] & 0x8000000fu) == 9u + NTAIL) U[e] -= 2u * UINC;
        }
    };
    u32x4 Va, Vb;

    Raw raw[PD];
    {
        const int tid = tid0;
        asm volatile("" ::: "v88", "v127");
        const uint16_t fq = freq[0];
        if (tid == 128) tk = peek(vconst<M_BEAT * 4>());
        if (blockIdx.x == 0 && wv == 3 && (tid & 63) < M_BEAT / 64) {
            __hip_atomic_store(meta + (tid & 63) * 64, 0u, __ATOMIC_RELAXED, __HIP_MEMORY_SCOPE_AGENT);
            __hip_atomic_store(meta + (tid & 63) * 64 + 1, 0u, __ATOMIC_RELAXED, __HIP_MEMORY_SCOPE_AGENT);
        }
        const __amdgpu_buffer_rsrc_t rsE = __builtin_amdgcn_make_buffer_rsrc((void*)m, 0, E * 8, 0x00020000);
        u32x2 mv[5];
        mv[4] = __builtin_amdgcn_raw_buffer_load_b64(rsE, (t8 & 511u) * (uint32_t)(E >> 6), 0, 0);
#pragma unroll
        for (int q = 0; q < 4; q++)
            mv[q] = __builtin_amdgcn_raw_buffer_load_b64(rsE, t8 & 511u, ((blockIdx.x >> 2) * 256u + (blockIdx.x & 3u) * 16u + (uint32_t)wv + 64u * q) * 512u, AUX_NT);
#pragma unroll
        for (int d = 0; d < MDL; d++) load_one(raw[1], tl0, d);
        const u32x2 ci = {CINIT, CINIT};
        *(LDS u32x2*)(uintptr_t)(P_CNT + tid * 8) = ci;
        if (tid < 64) lds_st(P_CNT + (NB + tid) * 4, 0x40000000u);
        if (tid < 8 && tid != 2) lds_st(P_FLAG + tid * 4, tid == 0 ? 0xffffffffu : 0u);
        if (tid == 128) lds_st(P_FLAG + 8, tk);
        asm("s_nop 0\n\tv_readfirstlane_b32 %0, %1" : "=s"(frr) : "v"(bf2f(fq) * 0.15915494309189535f));
        const uint32_t lane = (uint32_t)tid & 63u;
        int e0[4];
        if (blockIdx.x < SLOTI / 64) {
            float w[16];
            load_w(wdown, w);
            auto dot = [&](u32x2 v, int c) __attribute__((always_inline)) -> f32x2 {
                const f32x2 w0 = {w[c], w[c + 1]}, w1 = {w[4 + c], w[5 + c]}, w2 = {w[8 + c], w[9 + c]}, w3 = {w[12 + c], w[13 + c]};
                return w0 * bf2f(v.x & 0xffffu) + w1 * bf2f(v.x >> 16) + w2 * bf2f(v.y & 0xffffu) + w3 * bf2f(v.y >> 16);
            };
#pragma unroll
            for (int cp = 0; cp < 2; cp++) {
                const f32x2 sd = dot(mv[4], 2 * cp);
#pragma unroll
                for (int e = 0; e < 2; e++) {
                    const int ex = (int)(wave_max(__float_as_uint(sd[e]) & 0x7fffffffu) >> 23);
                    e0[2 * cp + e] = __builtin_amdgcn_readfirstlane(ex == 0 ? 0 : min(max(ex - 129, -100), 100));
                }
                const f32x2 sc = {__uint_as_float((uint32_t)(127 - e0[2 * cp]) << 23), __uint_as_float((uint32_t)(127 - e0[2 * cp + 1]) << 23)};
                u32x4 o;
                u16x2 mx = {0, 0};
                f32x2 sq = {0.f, 0.f};
#pragma unroll
                for (int q = 0; q < 4; q++) {
                    const f32x2 y = dot(mv[q], 2 * cp) * sc;
                    const uint32_t hb = pk_bf16(y.x, y.y);
                    const f32x2 x = {__uint_as_float(hb << 16), __uint_as_float(hb & 0xffff0000u)};
                    o[q] = __builtin_bit_cast(uint32_t, __builtin_amdgcn_cvt_pkrtz(x.x, x.y));
                    mx = __builtin_elementwise_max(mx, __builtin_bit_cast(u16x2, hb & 0x7fff7fffu));
                    sq += x * x;
                }
                *(LDS u32x4*)(uintptr_t)(P_BUF + (cp * 64u + lane) * 272u + wv * 16) = o;
#pragma unroll
                for (int e = 0; e < 2; e++) {
                    const u32x2 st = {wave_max(mx[e]), __float_as_uint(wave_sum(sq[e]))};
                    if (lane == 0u) *(LDS u32x2*)(uintptr_t)(P_LST + ((2 * cp + e) * 16 + wv) * 8) = st;
                }
            }
        }
#pragma unroll
        for (int d = MDL; d < 6; d++) load_one(raw[1], tl0, d);
#pragma unroll
        for (int d = 0; d < 6; d++)
            if (((LPOS >> (4 * d)) & 15) < 4) load_one(raw[0], tl1, d);
        __syncthreads();
        u32x4 mo[2];
#pragma unroll
        for (int cp = 0; cp < 2; cp++) mo[cp] = *(const LDS u32x4*)(uintptr_t)(P_BUF + (cp * 64u + (t8 >> 7)) * 272u + ((t8 + t8) & 0xf0u));
        if (blockIdx.x == 0 && wv == 3) asm volatile("s_waitcnt vmcnt(0)" ::: "memory");
        __syncthreads();
#pragma unroll
        for (int s = 0; s < 16; s++) *(LDS u32x2*)(uintptr_t)(la + s * 256) = nul;
        asm volatile("" : "+v"(raw[1].o[0]), "+v"(raw[1].o[1]), "+v"(raw[1].i[0]), "+v"(raw[1].i[1]), "+v"(raw[1].a[0]), "+v"(raw[1].a[1]));
        if (blockIdx.x < SLOTI / 64) {
            const __amdgpu_buffer_rsrc_t rsM = __builtin_amdgcn_make_buffer_rsrc((void*)(meta + W_MDC / 4), 0, (int)(128u * SLOTI * 4u), 0x00020000);
#pragma unroll
            for (int cp = 0; cp < 2; cp++) {
                const uint32_t row = cp * 64u + (t8 >> 7), c16 = (t8 + t8) & 0xf0u;
                __builtin_amdgcn_raw_buffer_store_b128(mo[cp], rsM, row * (SLOTI * 4u) + c16 + blockIdx.x * 256u, 0, AUX_SC1);
            }
            if (wv == 0) {
                const u32x2 st = *(const LDS u32x2*)(uintptr_t)(P_LST + t8);
                const u32x2 r = {row_max(st.x) | (row_max(st.x ? 0xffffu - st.x : 0u) << 16), __float_as_uint(row_sum(__uint_as_float(st.y)))};
                if ((lane & 15u) == 15u) ((u32x2*)(meta + W_STAT / 4))[(lane >> 4) * NWG + blockIdx.x] = r;
                if (blockIdx.x == 0 && lane < 4u) meta[M_E0 + lane] = (uint32_t)(lane == 0u ? e0[0] : lane == 1u ? e0[1] : lane == 2u ? e0[2] : e0[3]);
            }
        }
    }
    pin(la);

    uint32_t w[8], aw[8], xv[8];
    u64 nm[8];
    auto later2 = [&](int k) __attribute__((always_inline)) {
        uint32_t x0, x1;
        asm volatile("v_lshrrev_b32 %0, 4, %2\n\tv_lshrrev_b32 %1, 4, %5\n\tv_bitop3_b32 %0, %2, %0, 12 bitop3:0x72\n\tv_bitop3_b32 %1, %5, %1, 12 bitop3:0x72\n\t"
                     "s_not_b64 exec, %8\n\tds_write_b32 %2, %3 offset:%10\n\tds_or_b32 %0, %4 offset:%10\n\t"
                     "s_not_b64 exec, %9\n\tds_write_b32 %5, %6 offset:%10\n\tds_or_b32 %1, %7 offset:%10\n\ts_mov_b64 exec, -1"
                     : "=&v"(x0), "=&v"(x1)
                     : "v"(aw[k]), "v"(w[k]), "v"(xv[k]), "v"(aw[k + 1]), "v"(w[k + 1]), "v"(xv[k + 1]), "s"(nm[k]), "s"(nm[k + 1]), "n"(P_BUF) : "memory", "scc");
    };
    auto drain = [&]() __attribute__((always_inline)) {
        if (wv == 1) {
            const uint32_t q = (epoch + 1u) & 3u;
            const uint32_t n = __builtin_amdgcn_readfirstlane(lds_ld(vconst<P_FLAG + 16>() + q * 4u));
            if (__builtin_expect(n != 0u, 0)) {
                pin(t8);
                const uint32_t l = (t8 >> 3) & 63u;
                if (l < min(n, (uint32_t)LCAP)) {
                    const u32x4 e = *(const LDS u32x4*)(uintptr_t)(P_LST + (q * LCAP + l) * 16u);
                    lds_st(P_BUF + e.x, e.y);
                    lds_or(P_BUF + slot_x(e.x), e.z);
                }
                lds_st(vconst<P_FLAG + 16>() + q * 4u, vconst<0>());
            }
        }
    };
    auto again = [&](int D) __attribute__((always_inline)) {
        do {
            general(D, true, fm);
            __syncthreads();
            later2(0); later2(2); later2(4); later2(6);
            drain();
#pragma unroll
            for (int k = 0; k < 8; k++) {
                pin(aw[k]);
                bool due = false;
                if (aw[k] & 0x80000000u) {
                    xv[k] -= CHV;
                    if (xv[k] < CHV) {
                        const uint32_t f = (aw[k] >> 16) & 0x3ffu;
                        aw[k] = slot_w(aw[k] & 0xffffu, xv[k]);
                        xv[k] = f << ((xv[k] >> 8) & 31u);
                        due = true;
                    } else lds_st(vconst<P_FLAG>(), epoch + 1u);
                }
                nm[k] = ~__builtin_amdgcn_ballot_w64(due);
            }
            look(*(const LDS u32x2*)(uintptr_t)(P_CNT + t8));
            asm volatile("s_waitcnt lgkmcnt(0)" ::: "memory");
            __syncthreads();
        } while (__builtin_amdgcn_readfirstlane(lds_ld(vconst<P_FLAG>())) == epoch);
    };
    auto round = [&](auto DD, auto LL, Raw& rc, Raw& rn, int rd) __attribute__((always_inline)) {
        constexpr int D = decltype(DD)::value;
        constexpr bool LAST = decltype(LL)::value == 1, FIRST = decltype(LL)::value == 2;
        const int ng = min(GR, G - tl0 * GR);
        auto hook = [&](int p) __attribute__((always_inline)) {
#pragma unroll
            for (int q = 0; q < 6; q++)
            {
                const int lp = (LPOS >> (4 * q)) & 15;
                if (!LAST && lp < 4 && lp == p) load_one(rc, tl2, q);
                if (!LAST && lp >= 4 && (lp < 8 || lp >= 12 ? lp : lp - 8) == p) load_one(rn, tl1, q);
            }
        };
        uint32_t wn[8], ca[8], px[4], cv[8];
        float mx;
        float cs[2];
        auto convA = [&](int q) __attribute__((always_inline)) {
            if (LAST) return;
            const uint32_t a = rc.a[q >> 1][q & 1];
            const f32x2 av = {__uint_as_float(a << 16), __uint_as_float(a & 0xffff0000u)};
            f32x2 rv;
            asm("v_pk_mul_f32 %0, %1, %2 op_sel_hi:[1,0]" : "=v"(rv) : "v"(av), "s"((u64)__float_as_uint(frr)));
            mx = __builtin_elementwise_maximum(__builtin_elementwise_maximum(__builtin_fabsf(rv.x), __builtin_fabsf(rv.y)), (q & 1) ? mx : 0.f);
            cs[0] = __builtin_amdgcn_cosf(rv.x); cs[1] = __builtin_amdgcn_cosf(rv.y);
            asm volatile("" : "+v"(mx), "+v"(cs[0]), "+v"(cs[1]));
            __builtin_amdgcn_sched_barrier(0);
        };
        auto convB = [&](int k) __attribute__((always_inline)) {
            if (LAST) return;
            const uint32_t o = rc.o[k >> 2][k & 3], i = rc.i[k >> 2][k & 3];
            wn[k] = (__builtin_amdgcn_alignbit(bfi(63u, o, o >> 5), __float_as_uint(cs[k & 1]), 30u) << 14) | (i >> 6);
            ca[k] = (bfi(63u, i, o) << 2) & 0x1ffcu;
            pin(wn[k]); pin(ca[k]);
            __builtin_amdgcn_sched_barrier(0);
        };
        auto convD = [&](int q) __attribute__((always_inline)) {
            if (LAST) return;
            asm volatile("v_cvt_pk_f16_f32 %0, |%1|, |%2|\n\tv_pk_mul_f16 %0, %0, %3 op_sel_hi:[1,0]" : "=v"(px[q]) : "v"(cs[0]), "v"(cs[1]), "s"(0x3ffu));
        };
        uint32_t cst;
        auto check = [&](int j) __attribute__((always_inline)) {
            if (LAST) return;
            if (__builtin_expect((__builtin_amdgcn_ballot_w64(!(mx <= RMAXREV)) | (u64)(uint32_t)(GR - ng)) != 0ull, 0)) {
                pin(t8);
                asm volatile("" : "+v"(rc.a[j]));
                const int lg = j * NT + (int)(t8 >> 3);
#pragma unroll
                for (int k = 0; k < 4; k++) {
                    const uint32_t a = (k & 1) ? (rc.a[j][k >> 1] >> 16) : (rc.a[j][k >> 1] & 0xffffu);
                    const bool nrm = __builtin_fabsf(frr * bf2f(a)) <= RMAXREV;
                    if (!(lg < ng && nrm)) ca[j * 4 + k] = NB * 4u + ((t8 >> 1) & 0xfcu);
                    if (lg < ng && !nrm) {
                        ready();
                        __builtin_amdgcn_raw_buffer_store_b32((uint32_t)((tl0 * GR + lg) * 4 + k), rsR, spec0 - draw(vconst<M_LOG * 4 + 4>()) * 4u, 0, 0);
                    }
                }
            }
            cst = vconst<CSTEP>();
        };
        auto ticket = [&](int k) __attribute__((always_inline)) {
            if (LAST) return;
            cv[k] = lds_add(P_CNT + ca[k], cst);
            __builtin_amdgcn_sched_barrier(0);
        };
        auto insert = [&](int j) __attribute__((always_inline)) {
            const int k0 = 4 * j;
            const bool odd = __builtin_amdgcn_ballot_w64(max(max(cv[k0], cv[k0 + 1]), max(cv[k0 + 2], cv[k0 + 3])) >= 2u * CHV) != 0ull;
#pragma unroll
            for (int k = k0; k < k0 + 4; k += 2) {
                uint32_t x0, x1;
                asm volatile("v_lshrrev_b32 %6, 11, %8\n\tv_lshrrev_b32 %7, 11, %9\n\t"
                             "v_bitop3_b32 %6, %6, %10, 60 bitop3:0x28\n\tv_bitop3_b32 %7, %7, %11, 60 bitop3:0x28\n\t"
                             "v_lshl_or_b32 %0, %10, 4, %6\n\tv_lshl_or_b32 %1, %11, 4, %7\n\t"
                             "v_lshlrev_b32_sdwa %2, %8, %12 dst_sel:DWORD dst_unused:UNUSED_PAD src0_sel:BYTE_1 src1_sel:WORD_0\n\t"
                             "v_lshlrev_b32_sdwa %3, %9, %12 dst_sel:DWORD dst_unused:UNUSED_PAD src0_sel:BYTE_1 src1_sel:WORD_1\n\t"
                             "v_bitop3_b32 %6, %0, %10, 12 bitop3:0x72\n\tv_bitop3_b32 %7, %1, %11, 12 bitop3:0x72\n\t"
                             "v_cmpx_gt_u32 %4, %15, %8\n\tds_write_b32 %0, %13 offset:%16\n\tds_or_b32 %6, %2 offset:%16\n\ts_mov_b64 exec, -1\n\t"
                             "v_cmpx_gt_u32 %5, %15, %9\n\tds_write_b32 %1, %14 offset:%16\n\tds_or_b32 %7, %3 offset:%16\n\ts_mov_b64 exec, -1"
                             : "=&v"(aw[k]), "=&v"(aw[k + 1]), "=&v"(xv[k]), "=&v"(xv[k + 1]), "=&s"(nm[k]), "=&s"(nm[k + 1]), "=&v"(x0), "=&v"(x1)
                             : "v"(cv[k]), "v"(cv[k + 1]), "v"(ca[k]), "v"(ca[k + 1]), "v"(px[k >> 1]), "v"(wn[k]), "v"(wn[k + 1]), "s"(CHV), "n"(P_BUF) : "memory");
                w[k] = wn[k]; w[k + 1] = wn[k + 1];
                if (__builtin_expect(odd, 0)) {
#pragma unroll
                    for (int q = k; q < k + 2; q++) {
                        nm[q] |= __builtin_amdgcn_ballot_w64(cv[q] >= 2u * CHV);
                        if (cv[q] >= 2u * CHV && cv[q] < 0x40000000u) {
                            const uint32_t lq = (epoch + 2u) & 3u;
                            uint32_t p = LCAP;
                            if (cv[q] < 3u * CHV) p = lds_add(vconst<P_FLAG + 16>() + lq * 4u, vconst<1>());
                            if (p < LCAP) {
                                const u32x2 e = {aw[q], w[q]};
                                *(LDS u32x2*)(uintptr_t)(P_LST + (lq * LCAP + p) * 16u) = e;
                                lds_st(P_LST + (lq * LCAP + p) * 16u + 8u, xv[q]);
                            } else {
                                aw[q] = ca[q] | ((q & 1) ? (px[q >> 1] & 0xffff0000u) : (px[q >> 1] << 16)) | 0x80000000u;
                                xv[q] = cv[q] - CHV;
                                lds_st(vconst<P_FLAG>(), epoch);
                            }
                        }
                    }
                }
            }
        };
        constexpr int P0 = D ? 0 : 3, P1 = D ? 1 : 2, P2 = D ? 2 : 1, P3 = D ? 3 : 0;
        const uint32_t fl = lds_ld(vconst<P_FLAG>());
        if (!LAST) asm volatile("" : "+v"(rc.o[0]), "+v"(rc.o[1]), "+v"(rc.i[0]), "+v"(rc.i[1]), "+v"(rc.a[0]), "+v"(rc.a[1]));
        if (!LAST && (rd + 4 >= nstat || tl1 >= ntile) && wv == 2) {
            pin(tk);
            if ((t8 & 511u) == 0u) {
                if (tl1 >= ntile) {
                    if (rd + 3 <= nstat) ready();
                    (void)draw(vconst<M_MD * 4>());
                } else if (rd + 4 == nstat) tk = peek(vconst<M_BEAT * 4>());
                else {
                    if (rd + 3 == nstat) {
                        if (tk == lds_ld(vconst<P_FLAG + 8>())) ready();
                        tk = 0u;
                    }
                    lds_st(vconst<P_FLAG + 4>(), tk);
                    if ((int)tk < ntile - nstat * NWG) tk = draw(vconst<M_TILE * 4>());
                }
            }
        }
        if (!LAST && blockIdx.x == 0 && wv == 3 && (t8 & 511u) == 0u) beat();
        hook(12);
        pin(t8);
        const uint32_t tr = P_TAB + (t8 & ~63u), u16 = (t8 + t8) & 0x70u;
        convA(0);
        convB(0);
        const bool ag = __builtin_amdgcn_readfirstlane(fl) == epoch;
        if (!FIRST && __builtin_expect(LAST || ag || sm != 0ull, 0)) {
            if (ag) again(D);
            general(D, false, sm);
            Va = *(const LDS u32x4*)(uintptr_t)(P_TAB + (t8 & ~63u) + P0 * 16);
            Vb = *(const LDS u32x4*)(uintptr_t)(P_TAB + (t8 & ~63u) + P1 * 16);
            asm volatile("" : "+v"(Va), "+v"(Vb));
        }
        uint32_t pq = (uint32_t)pb2 | (uint32_t)(pb2 >> 32);
        pq = (pq | (pq >> 1)) & 0x55555555u;
        pq = __builtin_amdgcn_readfirstlane(pq + (pq & (far * NEARM)));
        uint32_t ad[8];
        if (!FIRST) ad[0] = take<4 * P0 + 0, D>(Va[0], u16, la, nul);
        convB(1);
        if (!FIRST) ad[1] = take<4 * P0 + 1, D>(Va[1], u16, la, nul);
        convD(0);
        convA(1);
        if (!FIRST) ad[2] = take<4 * P0 + 2, D>(Va[2], u16, la, nul);
        convB(2);
        hook(13);
        if (!FIRST) ad[3] = take<4 * P0 + 3, D>(Va[3], u16, la, nul);
        convB(3);
        if (!FIRST) put<4 * P0 + 0, LAST ? 3 : 3, LAST>(rsA, ad[0], pq);
        if (!FIRST) ad[4] = take<4 * P1 + 0, D>(Vb[0], u16, la, nul);
        convD(1);
        check(0);
        ticket(0);
        hook(4);
        if (!FIRST) put<4 * P0 + 1, LAST ? 3 : 4, LAST>(rsA, ad[1], pq);
        if (!FIRST) ad[5] = take<4 * P1 + 1, D>(Vb[1], u16, la, nul);
        convA(2);
        ticket(1);
        if (!FIRST) put<4 * P0 + 2, LAST ? 3 : 5, LAST>(rsA, ad[2], pq);
        if (!FIRST) ad[6] = take<4 * P1 + 2, D>(Vb[2], u16, la, nul);
        convB(4);
        ticket(2);
        if (!FIRST) put<4 * P0 + 3, LAST ? 3 : 6, LAST>(rsA, ad[3], pq);
        if (!FIRST) Va = *(const LDS u32x4*)(uintptr_t)(tr + P2 * 16);
        __builtin_amdgcn_sched_barrier(0);
        if (!FIRST) ad[7] = take<4 * P1 + 3, D>(Vb[3], u16, la, nul);
        convB(5);
        ticket(3);
        SETP(2);
        if (!FIRST) put<4 * P1 + 0, LAST ? 4 : 8, LAST>(rsA, ad[4], pq);
        if (!FIRST) ad[0] = take<4 * P2 + 0, D>(Va[0], u16, la, nul);
        convD(2);
        convA(3);
        hook(5);
        if (!FIRST) put<4 * P1 + 1, LAST ? 4 : 7, LAST>(rsA, ad[5], pq);
        if (!FIRST) ad[1] = take<4 * P2 + 1, D>(Va[1], u16, la, nul);
        convB(6);
        if (!FIRST) put<4 * P1 + 2, LAST ? 4 : 6, LAST>(rsA, ad[6], pq);
        if (!FIRST) ad[2] = take<4 * P2 + 2, D>(Va[2], u16, la, nul);
        convB(7);
        if (!FIRST) put<4 * P1 + 3, LAST ? 3 : 4, LAST>(rsA, ad[7], pq);
        if (!FIRST) Vb = *(const LDS u32x4*)(uintptr_t)(tr + P3 * 16);
        __builtin_amdgcn_sched_barrier(0);
        if (!FIRST) ad[3] = take<4 * P2 + 3, D>(Va[3], u16, la, nul);
        convD(3);
        check(1);
        ticket(4);
        SETP(1);
        hook(6);
        if (!FIRST) put<4 * P2 + 0, LAST ? 4 : 5, LAST>(rsA, ad[0], pq);
        if (!FIRST) ad[4] = take<4 * P3 + 0, D>(Vb[0], u16, la, nul);
        ticket(5);
        if (!FIRST) put<4 * P2 + 1, LAST ? 4 : 6, LAST>(rsA, ad[1], pq);
        if (!FIRST) ad[5] = take<4 * P3 + 1, D>(Vb[1], u16, la, nul);
        ticket(6);
        if (!FIRST) put<4 * P2 + 2, LAST ? 4 : 7, LAST>(rsA, ad[2], pq);
        if (!FIRST) ad[6] = take<4 * P3 + 2, D>(Vb[2], u16, la, nul);
        ticket(7);
        SETP(0);
        if (!FIRST) put<4 * P2 + 3, LAST ? 3 : 7, LAST>(rsA, ad[3], pq);
        if (!FIRST) ad[7] = take<4 * P3 + 3, D>(Vb[3], u16, la, nul);
        hook(7);
        SETP(3);
        asm volatile("s_waitcnt lgkmcnt(0)" ::: "memory");
        __syncthreads();
        u32x2 cn;
        uint32_t tv;
        if (!LAST) { cn = *(const LDS u32x2*)(uintptr_t)(P_CNT + t8); tv = lds_ld(vconst<P_FLAG + 4>()); }
        __builtin_amdgcn_sched_barrier(0);
        if (!FIRST) later2(0);
        if (!FIRST) put<4 * P3, 15, LAST>(rsA, ad[4], pq);
        if (!FIRST) later2(2);
        if (!FIRST) put<4 * P3 + 1, 15, LAST>(rsA, ad[5], pq);
        if (!FIRST) drain();
        if (!LAST) {
            tl2 = rd + 2 < nstat ? fixed_tile(rd + 2) : nstat * NWG + (int)__builtin_amdgcn_readfirstlane(tv);
            look(cn);
        }
        hook(0);
        SETP(2);
        if (!FIRST) later2(4);
        if (!FIRST) put<4 * P3 + 2, 15, LAST>(rsA, ad[6], pq);
        if (!FIRST) later2(6);
        if (!FIRST) put<4 * P3 + 3, 15, LAST>(rsA, ad[7], pq);
        if (!LAST) {
            hook(1);
            SETP(1);
            insert(0);
            hook(2);
            SETP(0);
            pin(t8);
            Va = *(const LDS u32x4*)(uintptr_t)(P_TAB + (t8 & ~63u) + P3 * 16);
            Vb = *(const LDS u32x4*)(uintptr_t)(P_TAB + (t8 & ~63u) + P2 * 16);
            __builtin_amdgcn_sched_barrier(0);
            insert(1);
            hook(3);
            SETP(3);
            asm volatile("s_waitcnt lgkmcnt(0)" ::: "memory");
            __syncthreads();
            tl0 = tl1; tl1 = tl2;
        }
    };
#define HMV(A, B) "v_mov_b64 v[" #A ":" #A "+1], v[" #B ":" #B "+1]\n\t"
#define HMV4(A) HMV(A + 8, A + 6) HMV(A + 6, A + 4) HMV(A + 4, A + 2) HMV(A + 2, A)
    round(IC<1>(), IC<2>(), raw[1], raw[0], 0);
    int rd = 1;
#pragma unroll 1
    for (; tl1 < ntile; rd += 2) {
        round(IC<0>(), IC<0>(), raw[0], raw[1], rd);
        round(IC<1>(), IC<0>(), raw[1], raw[0], rd + 1);
    }
    if (tl0 >= ntile) asm volatile(HMV4(88) HMV4(98) HMV4(108) HMV4(118));
    asm volatile("" : "+s"(tl0));
    if (tl0 < ntile) round(IC<0>(), IC<0>(), raw[0], raw[1], rd);
    round(IC<1>(), IC<1>(), raw[1], raw[0], 0);
    if (__builtin_amdgcn_readfirstlane(lds_ld(P_FLAG + 16 + ((epoch + 2u) & 3u) * 4u)) != 0u) {
        asm volatile("s_waitcnt lgkmcnt(0)" ::: "memory");
        __syncthreads();
        look(*(const LDS u32x2*)(uintptr_t)(P_CNT + t8));
        general(0, true, fm);
        __syncthreads();
        drain();
    }
    {
        uint32_t* tcnt = meta + W_TCNT / 4;
        const u32x2 c = *(const LDS u32x2*)(uintptr_t)(P_CNT + t8);
        const uint32_t tw = tab_at();
#pragma unroll
        for (int e = 0; e < 2; e++) {
            const uint32_t g = U[e] & 15u;
            lds_st(tw + e * 64u, g >= 8u ? 0x80000000u : ((U[e] + ((g & 1u) ? 0u : SK * 64u)) & 0x7fffff81u) | (((7u - g) << 1) & 12u));
            *(LDS uint8_t*)(uintptr_t)(P_CNT + (t8 & ~511u) + (((tw - P_TAB) >> 2) & 127u) + e * 16u) =
                (uint8_t)(g < 8u ? 0u : ((g - 8u) >> 1) + (((c[e] + 2u * CSTEP) >> 15) << 2) + (g & 1u) * 0x50u);
        }
        asm volatile("s_waitcnt lgkmcnt(0)" ::: "memory");
        __syncthreads();
        const uint32_t u16 = (t8 + t8) & 0x70u;
        u32x4 En[4];
#pragma unroll
        for (int p = 0; p < 4; p++) En[p] = *(const LDS u32x4*)(uintptr_t)(P_TAB + (t8 & ~63u) + p * 16u);
        const u32x4 cb = *(const LDS u32x4*)(uintptr_t)(P_CNT + (t8 & ~511u) + ((t8 >> 2) & 0x70u));
        u32x2 N[16];
#pragma unroll
        for (int s = 0; s < 16; s++) N[s] = *(const LDS u32x2*)(uintptr_t)(la + s * 256u);
        if (u16 == 0u) *(u32x4*)(tcnt + blockIdx.x * 512u + (t8 >> 4)) = cb;
        const uint32_t sg = P_BUF + ((t8 & ~63u) << 4);
        uint32_t ps;
        auto rest = [&](auto SL) __attribute__((always_inline)) {
            constexpr int s = decltype(SL)::value;
            const uint32_t en = En[s >> 2][s & 3], by = cb[s >> 2] >> ((s & 3) * 8);
            const u32x2 Hr = hget<HREG(s, 0)>();
            const uint32_t at = ps + ((la & 0x38u) ^ ((s & 3) << 4));
            *(LDS u32x2*)(uintptr_t)at = Hr;
            *(LDS u32x2*)(uintptr_t)(at + (by & 0x40u)) = N[s];
            ps += (by & 0x3cu) << 2;
            const u32x2 Hs = (en & 1u) ? Hr : nul;
            const u32x4 d = {Hs.x, Hs.y, N[s].x, N[s].y};
            uint32_t a = __builtin_amdgcn_bitop3_b32(en, u16, 15u, 0x54);
            {
                const u32x4 rs_ = rsA;
                const uint32_t far_ = ((NEARM >> (2 * s)) & 1u) ? far : 0u;
                asm volatile("s_cmp_eq_u32 %3, 0\n\ts_cbranch_scc0 1f\n\tbuffer_store_dwordx4 %0, %1, %2, 0 offen sc1\n\ts_branch 2f\n"
                             "1:\n\tbuffer_store_dwordx4 %0, %1, %2, 0 offen sc1 nt\n2:\n\ts_nop 1" :: "v"(d), "v"(a), "s"(rs_), "s"(far_) : "memory", "scc");
            }
            if (__builtin_expect(__builtin_amdgcn_ballot_w64((en & 12u) != 0u) != 0ull, 0)) {
                const u32x4 dn = {nul.x, nul.y, nul.x, nul.y};
#pragma unroll 1
                for (uint32_t k = (en >> 2) & 3u; k > 0u; k--) {
                    a += SK * 128u;
                    __builtin_amdgcn_raw_buffer_store_b128(dn, rsR, a, 0, AUX_SC1);
                }
            }
        };
        auto pack = [&](int p) __attribute__((always_inline)) {
            const uint32_t a = (SBASE + ((((t8 >> 1) & 0x60u) + (t8 >> 8)) * 2u + p) * (NWG * 9u) + blockIdx.x * 9u) * 128u + u16;
            asm volatile("" ::: "memory");
#pragma unroll 1
            for (uint32_t q = 0; __builtin_amdgcn_ballot_w64(sg + q < ps) != 0ull; q += 256u) {
                const u32x4 d0 = *(const LDS u32x4*)(uintptr_t)(sg + q + u16), d1 = *(const LDS u32x4*)(uintptr_t)(sg + q + 128u + u16);
                __builtin_amdgcn_raw_buffer_store_b128(d0, rsR, sg + q < ps ? a + q : 0xffffff00u, 0, AUX_SC1);
                __builtin_amdgcn_raw_buffer_store_b128(d1, rsR, sg + q + 128u < ps ? a + q + 128u : 0xffffff00u, 0, AUX_SC1);
            }
            asm volatile("" ::: "memory");
        };
        asm volatile("" : "+v"(N[0]), "+v"(N[1]), "+v"(N[2]), "+v"(N[3]), "+v"(N[4]), "+v"(N[5]), "+v"(N[6]), "+v"(N[7]) :: "memory");
        asm volatile("" : "+v"(N[8]), "+v"(N[9]), "+v"(N[10]), "+v"(N[11]), "+v"(N[12]), "+v"(N[13]), "+v"(N[14]), "+v"(N[15]) :: "memory");
        ps = sg;
        rest(IC<0>()); rest(IC<1>()); rest(IC<2>()); rest(IC<3>()); rest(IC<4>()); rest(IC<5>()); rest(IC<6>()); rest(IC<7>());
        pack(0);
        ps = sg;
        rest(IC<8>()); rest(IC<9>()); rest(IC<10>()); rest(IC<11>()); rest(IC<12>()); rest(IC<13>()); rest(IC<14>()); rest(IC<15>());
        pack(1);
    }
    if (blockIdx.x == 0 && t8 == 1536u) {
        do {
            beat();
            __builtin_amdgcn_s_sleep(8);
        } while (peek(vconst<M_MD * 4>()) < NWG);
    }
}

#define A_MD 0
#define A_ACC (SLOTI * 4)
#define A_FLAG (A_ACC + SLOTO * 4)
#define A_LDS (A_FLAG + 512)
static_assert(SLOTI * 4 == 3 * NT * 16 + 1024, "md staging assumes three full vectors and 256 dwords");
#define BIAS 0x40004000u
#define NFAST 8
#define NLD 10
#define MDF 0x0a11333
#define MDP 0xaaaaa
#define HP 2
#define SHARE (SLOTO / 8)
#define SPINCAP 4096u
#define ZSIG 9.0f
#define HSLOW 0x80008000u
#define GAS __attribute__((address_space(1)))

__global__ __launch_bounds__(NT, 4) __attribute__((amdgpu_num_vgpr(53))) void acc_kernel(char* ws, int T, int E, int ks, int lcap, float zf, int pad,
                                                 const uint16_t* __restrict__ centers, const uint16_t* __restrict__ wrbf,
                                                 const uint16_t* __restrict__ wsbf, const uint16_t* __restrict__ wup,
                                                 const u32x2* __restrict__ m, const uint16_t* __restrict__ dist, u32x2* __restrict__ out,
                                                 const int* __restrict__ iin, const int* __restrict__ iout, const uint16_t* __restrict__ ang,
                                                 const uint16_t* __restrict__ wdown, const uint16_t* __restrict__ freq) {
    const char* recs = ws + W_RECS;
    const u32x4* tcnt = (const u32x4*)(ws + W_TCNT);
    const uint32_t* mdc = (const uint32_t*)(ws + W_MDC);
    const u32x4* stat = (const u32x4*)(ws + W_STAT);
    uint32_t* partial = (uint32_t*)(ws + W_PART);
    unsigned* meta = (unsigned*)ws;
    extern __shared__ __attribute__((aligned(16))) char smem[];
    unsigned* acc = (unsigned*)(smem + A_ACC);
    unsigned* s_flag = (unsigned*)(smem + A_FLAG);
    const int tid = threadIdx.x;
    const int lane = tid & 63;
    const int wv = __builtin_amdgcn_readfirstlane(tid >> 6);
    const int cp = (blockIdx.x >> 3) & 1;
    const int h = blockIdx.x & 3;
    const int j = ((blockIdx.x >> 2) & 1) * 16 + (blockIdx.x >> 4);
    u32x4 tc;
    const __amdgpu_buffer_rsrc_t rsM = __builtin_amdgcn_make_buffer_rsrc((void*)mdc, 0, (int)(128u * SLOTI * 4u), 0x00020000);

    u32x4 pm[4];
    u32x4 ring[NLD];
    auto load_md = [&](int ff, int pl, int mdp) __attribute__((always_inline)) {
        const uint32_t so = (uint32_t)(cp * 64 + ff * 4 + h) * (SLOTI * 4u);
#pragma unroll
        for (int v = 0; v < 3; v++)
            if (pl < 0 || pl == ((mdp >> (4 * v)) & 15)) pm[v] = __builtin_amdgcn_raw_buffer_load_b128(rsM, (uint32_t)tid * 16u, so + (uint32_t)v * (NT * 16u), 0);
        if ((pl < 0 || pl == (mdp & 15)) && wv == 0) pm[3] = __builtin_amdgcn_raw_buffer_load_b128(rsM, (uint32_t)tid * 16u, so + 3u * NT * 16u, 0);
        __builtin_amdgcn_sched_barrier(0);
    };
    const uint32_t lf = (uint32_t)(tid >> 9) * (SKH * 128u) + (uint32_t)(tid & 0x1e0) * 16u + (uint32_t)(tid & 12) * 32u + (uint32_t)(tid & 16) * 4u + (uint32_t)(tid & 3) * 16u;
    const uint32_t lp = (uint32_t)(tid >> 8) * (SKM * 128u) + (uint32_t)((tid >> 2) & 63) * 128u + (uint32_t)(tid & 3) * 16u;
    auto load_main = [&](int i, int ff, bool near = false) __attribute__((always_inline)) -> u32x4 {
        const uint32_t q = (uint32_t)(((i >> 1) - HP) & 3), SK = q ? SKM : SKH, SS = q ? SSM : SSH;
        const __amdgpu_buffer_rsrc_t rs = __builtin_amdgcn_make_buffer_rsrc((void*)(recs + (q * SQ + (uint32_t)ff * SS + (uint32_t)(h * 32 + j) * SC) * 128u), 0, ks * (int)(SK * 128u), 0x00020000);
        if (q == 0u && near) return __builtin_amdgcn_raw_buffer_load_b128(rs, lf, (i & 1) * (int)(2u * SK * 128u), 0);
        if (q == 0u) return __builtin_amdgcn_raw_buffer_load_b128(rs, lf, (i & 1) * (int)(2u * SK * 128u), AUX_NT);
        return __builtin_amdgcn_raw_buffer_load_b128(rs, lp + (uint32_t)(i & 1) * 64u, 0, 0);
    };
    const __amdgpu_buffer_rsrc_t rsT = __builtin_amdgcn_make_buffer_rsrc((void*)recs, 0, (int)(LBASE * 128u), 0x00020000);
    const uint32_t tof = (uint32_t)(lane & 2) * (TPL * 64u) + (uint32_t)(tid >> 2) * 128u + (uint32_t)(lane & 1) * 64u + 4u + (TBASE + (uint32_t)(h * 32 + j) * TST) * 128u;
    const uint32_t sp0 = (SBASE + (uint32_t)(h * 32 + j) * (2u * NWG * 9u) + (uint32_t)(tid >> 2) * 9u) * 128u;
    auto tail_rank = [&](uint32_t c, uint32_t sp, int ff, uint32_t t0, int& dst, uint32_t& en) __attribute__((always_inline)) -> int {
        const int k4 = (int)(((uint32_t)(lane & 3) + t0) * 4u) - (int)((c & 3u) * 8u);
        const int left = (int)((c >> 2) & 15u) - k4;
        const bool has = left > 0;
        uint32_t ea = tof + (uint32_t)ff * (128u * TST * 128u), eb = sp + (uint32_t)(k4 * 16 + min(max(left, 0), 4));
        asm volatile("" : "+v"(ea), "+v"(eb));
        en = k4 < 0 ? ea : eb;
        const u64 mask = __builtin_amdgcn_ballot_w64(has);
        const int n = __builtin_popcountll(mask);
        const int rank = __builtin_amdgcn_mbcnt_hi((uint32_t)(mask >> 32), __builtin_amdgcn_mbcnt_lo((uint32_t)mask, 0u));
        const int r = has ? rank : (n + lane - rank);
        dst = ((r & 15) << 4) | ((r >> 4) << 2);
        return n;
    };
    auto load_tail = [&](uint32_t en, u64& tm) __attribute__((always_inline)) -> u32x4 {
        const bool on = (uint32_t)(lane & 3) < (en & 7u);
        tm = __builtin_amdgcn_ballot_w64(on);
        return __builtin_amdgcn_raw_buffer_load_b128(rsT, on ? (en & ~15u) + (uint32_t)(lane & 3) * 16u : 0xffffff00u, 0, 0);
    };
    f32x2 fa[4][4];
    u32x4 em[2];
    u32x2 dw2;
    uint32_t wl;
    const __amdgpu_buffer_rsrc_t rsE = __builtin_amdgcn_make_buffer_rsrc((void*)m, 0, E * 8, 0x00020000);
    const __amdgpu_buffer_rsrc_t rsD = __builtin_amdgcn_make_buffer_rsrc((void*)dist, 0, E * 2, 0x00020000);
    auto acc_vec = [&](int i) __attribute__((always_inline)) -> u32x4 {
        const uint32_t a = A_ACC + (uint32_t)(i >> 4) * 256u + (uint32_t)(i & 15) * 8u;
        const u32x2 x = *(const LDS u32x2*)(uintptr_t)a, y = *(const LDS u32x2*)(uintptr_t)(a + 128u);
        const u32x4 v = {x.x, x.y, y.x, y.y};
        return v;
    };
    auto edge0 = [&](int sh) __attribute__((always_inline)) -> uint32_t {
        return (tid < SHARE / 4) ? (uint32_t)(((sh * (SHARE / 64) + (tid >> 4)) << 11) | (j << 6) | ((tid & 15) * 2)) : 0x0fffff00u;
    };
    auto load_m = [&](int sh) __attribute__((always_inline)) {
        em[0] = __builtin_amdgcn_raw_buffer_load_b128(rsE, edge0(sh) * 8u, 0, AUX_NT);
        em[1] = __builtin_amdgcn_raw_buffer_load_b128(rsE, edge0(sh) * 8u + 256u, 0, AUX_NT);
    };
    auto load_d = [&](int sh) __attribute__((always_inline)) {
        dw2.x = __builtin_amdgcn_raw_buffer_load_b32(rsD, edge0(sh) * 2u, 0, 0);
        dw2.y = __builtin_amdgcn_raw_buffer_load_b32(rsD, edge0(sh) * 2u + 64u, 0, 0);
    };
    auto load_wl = [&]() __attribute__((always_inline)) {
        const u64 p0 = (u64)wsbf, p1 = (u64)wrbf, p2 = (u64)centers, p3 = (u64)wup;
        const u64 pa = lane < 16 ? p0 : p1, pb = lane < 40 ? p2 : p3;
        wl = ((const GAS uint32_t*)(lane < 32 ? pa : pb))[lane & (lane < 32 ? 15 : lane < 40 ? 3 : 7)];
    };
    auto factor = [&](int r, bool first) __attribute__((always_inline)) {
        const float sq = 3.7982826f;
        const f32x2 d01 = {bf2f(dw2.x & 0xffffu), bf2f(dw2.x >> 16)}, d23 = {bf2f(dw2.y & 0xffffu), bf2f(dw2.y >> 16)};
        const uint32_t cw = __builtin_amdgcn_readlane(wl, 32 + (r >> 1));
        const float cn = bf2f((r & 1) ? (cw >> 16) : (cw & 0xffffu));
        f32x2 w[4];
#pragma unroll
        for (int k = 0; k < 4; k++) {
            const uint32_t x = __builtin_amdgcn_readlane(wl, (k >> 1) * 16 + r * 2 + (k & 1));
            w[k].x = bf2f(x & 0xffffu); w[k].y = bf2f(x >> 16);
        }
        const f32x2 y01 = (d01 - cn) * sq, y23 = (d23 - cn) * sq;
        const f32x2 t01 = -y01 * y01, t23 = -y23 * y23;
        const float rb[4] = {__builtin_amdgcn_exp2f(t01.x), __builtin_amdgcn_exp2f(t01.y), __builtin_amdgcn_exp2f(t23.x), __builtin_amdgcn_exp2f(t23.y)};
#pragma unroll
        for (int q = 0; q < 4; q++) {
#pragma unroll
            for (int k = 0; k < 4; k++) fa[q][k] = first ? w[k] * rb[q] : __builtin_elementwise_fma(w[k], (f32x2)(rb[q]), fa[q][k]);
        }
    };
    auto edges = [&](int sh) __attribute__((always_inline)) {
        load_d(sh);
        load_m(sh);
        factor(0, true);
#pragma unroll 1
        for (int r = 1; r < 8; r++) factor(r, false);
    };
#define ADT(V, O) "ds_write_addtid_b32 " V " offset:" #O "\n\t"
    auto store_md = [&]() __attribute__((always_inline)) {
        asm volatile("s_mov_b32 m0, %12\n\ts_nop 0\n\t" ADT("%0", 0) ADT("%1", 256) ADT("%2", 512) ADT("%3", 768) ADT("%4", 16384) ADT("%5", 16640) ADT("%6", 16896) ADT("%7", 17152)
                     ADT("%8", 32768) ADT("%9", 33024) ADT("%10", 33280) ADT("%11", 33536)
                     :: "v"(pm[0][0]), "v"(pm[0][1]), "v"(pm[0][2]), "v"(pm[0][3]), "v"(pm[1][0]), "v"(pm[1][1]), "v"(pm[1][2]), "v"(pm[1][3]),
                        "v"(pm[2][0]), "v"(pm[2][1]), "v"(pm[2][2]), "v"(pm[2][3]), "s"(wv * 1024) : "memory");
        if (wv == 0) asm volatile("s_mov_b32 m0, 0\n\ts_nop 0\n\t" ADT("%0", 49152) ADT("%1", 49408) ADT("%2", 49664) ADT("%3", 49920)
                                  :: "v"(pm[3][0]), "v"(pm[3][1]), "v"(pm[3][2]), "v"(pm[3][3]) : "memory");
    };
    const int me = h * 2 + cp;
    GAS unsigned* dw = (GAS unsigned*)(meta + M_DONE + j * 64);
    const bool fin = wv < (SHARE / 4 + 63) / 64;
    bool fast, more;
    uint32_t need, mg0, mg1;
    uint32_t guard = 0;
    uint32_t sgn = 0x8000u;
    pin(sgn);

#define GAT3(M) { uint32_t a0_, a1_, a2_; asm volatile( \
        "v_lshlrev_b16 %0, 2, %3\n\tv_lshlrev_b16 %1, 2, %4\n\tv_lshlrev_b16 %2, 2, %5\n\t" \
        "ds_read_b32 v[%6], %0\n\tds_read_b32 v[%6+1], %1\n\tds_read_b32 v[%6+2], %2" \
        : "=&v"(a0_), "=&v"(a1_), "=&v"(a2_) : "v"(w0), "v"(w1), "v"(w2), "n"(M) : "memory"); }
#define MIXL " op_sel_hi:[1,1,0]\n\t"
#define MIXH " op_sel:[0,1,0] op_sel_hi:[1,1,0]\n\t"
#define SDW " dst_sel:DWORD dst_unused:UNUSED_PAD src0_sel:DWORD src1_sel:WORD_1\n\t"
#define FOLD "v_bitop3_b32 %6, %6, v[%15-9], v[%15-8] bitop3:0xfe\n\tv_or_b32 %6, v[%15-7], %6\n\t"
#define SCAT(PRE, POST, M) { uint32_t c0_, c1_, c2_, a0_, a1_, a2_; asm volatile( \
        FOLD "v_and_b32 %0, 0x3ff, %7\n\tv_lshrrev_b32 %2, 22, %7\n\t" PRE \
        "v_bitop3_b32 %0, %11, %8, %0 bitop3:0xea\n\tv_bitop3_b32 %2, %11, %10, %2 bitop3:0xea\n\tv_bfe_u32 %1, %7, 11, 10\n\t" \
        "v_fma_mix_f32 %3, %0, v[%15], %12" MIXL "v_bitop3_b32 %1, %11, %9, %1 bitop3:0xea\n\tv_fma_mix_f32 %0, %0, v[%15], %13" MIXH \
        "v_fma_mix_f32 %4, %1, v[%15+1], %12" MIXL "v_lshl_add_u32 %0, %0, 16, %3\n\tv_lshlrev_b32_sdwa %3, 2, %8" SDW \
        "v_fma_mix_f32 %1, %1, v[%15+1], %13" MIXH "ds_add_rtn_u32 v[%15-9], %3, %0 offset:%14\n\t" \
        "v_fma_mix_f32 %5, %2, v[%15+2], %12" MIXL "v_lshl_add_u32 %1, %1, 16, %4\n\tv_lshlrev_b32_sdwa %4, 2, %9" SDW \
        "v_fma_mix_f32 %2, %2, v[%15+2], %13" MIXH "ds_add_rtn_u32 v[%15-8], %4, %1 offset:%14\n\t" \
        "v_lshl_add_u32 %2, %2, 16, %5\n\tv_lshlrev_b32_sdwa %5, 2, %10" SDW \
        "ds_add_rtn_u32 v[%15-7], %5, %2 offset:%14" POST \
        : "=&v"(c0_), "=&v"(c1_), "=&v"(c2_), "=&v"(a0_), "=&v"(a1_), "=&v"(a2_), "+v"(guard) \
        : "v"(xw), "v"(w0), "v"(w1), "v"(w2), "v"(sgn), "s"(mg0), "s"(mg1), "n"(A_ACC), "n"(M), "s"(tm) : "memory"); }
    asm volatile("v_mov_b32 v110, 0\n\tv_mov_b32 v111, 0\n\tv_mov_b32 v112, 0\n\tv_mov_b32 v113, 0\n\tv_mov_b32 v114, 0\n\tv_mov_b32 v115, 0\n\t"
                 "v_mov_b32 v116, 0\n\tv_mov_b32 v117, 0\n\tv_mov_b32 v118, 0" ::: "v106", "v127");
    auto unmix = [&](int i) __attribute__((always_inline)) {
        const auto s0 = __builtin_amdgcn_permlane16_swap(ring[i][0], ring[i][2], false, false);
        const auto s1 = __builtin_amdgcn_permlane16_swap(ring[i][1], ring[i][3], false, false);
        ring[i][0] = s0[0]; ring[i][2] = s0[1]; ring[i][1] = s1[0]; ring[i][3] = s1[1];
    };
    auto word = [&](int i, int k) __attribute__((always_inline)) -> uint32_t {
        return i < NFAST && (i >> 1) != HP ? ring[(i & ~1) | (k >> 1)][(i & 1) * 2 + (k & 1)] : ring[i][k];
    };
    auto gather = [&](int R, int i, int p) __attribute__((always_inline)) {
        if ((i >> 1) == HP) unmix(i);
        const uint32_t w0 = word(i, 0 ^ R), w1 = word(i, 1 ^ R), w2 = word(i, 2 ^ R);
        if (p == 0) GAT3(119) else if (p == 1) GAT3(122) else GAT3(125)
    };
    auto scatter = [&](int R, int i, int p, u64 tm) __attribute__((always_inline)) {
        const uint32_t xw = word(i, 3 ^ R), w0 = word(i, 0 ^ R), w1 = word(i, 1 ^ R), w2 = word(i, 2 ^ R);
        if (i < NFAST) { if (p == 0) SCAT("", "", 119) else if (p == 1) SCAT("", "", 122) else SCAT("", "", 125) }
        else if (p == 0) SCAT("s_mov_b64 exec, %16\n\t", "\n\ts_mov_b64 exec, -1", 119)
        else SCAT("s_mov_b64 exec, %16\n\t", "\n\ts_mov_b64 exec, -1", 125)
    };
#define WAIT_L(N) asm volatile("s_waitcnt lgkmcnt(" #N ")" ::: "memory")
    int ntn;
    u64 tm0, tm1;
    uint32_t ta[2], sp = sp0;
    auto list1 = [&](int ff) __attribute__((always_inline)) {
        int dst;
        uint32_t en;
        ntn = tail_rank(tc.x, sp, ff, 0u, dst, en);
        asm volatile("ds_permute_b32 v106, %0, %1" :: "v"(dst), "v"(en) : "memory");
        if (__builtin_expect(__builtin_amdgcn_ballot_w64((tc.x & 2u) != 0u) != 0ull, 0)) {
            const int n2 = tail_rank(tc.x, sp, ff, 4u, dst, en);
            if (ntn + n2 <= 32) {
                const int r = ((dst >> 4) | ((dst & 12) << 2)) + ntn;
                const int x = __builtin_amdgcn_ds_permute((en & 7u) ? ((r & 15) << 4) | ((r >> 4) << 2) : 252, (en & 7u) ? (int)en : 0);
                asm volatile("v_cmpx_ne_u32 vcc, 0, %0\n\tv_mov_b32 v106, %0\n\ts_mov_b64 exec, -1\n\ts_nop 4" :: "v"(x) : "vcc");
                ntn += n2;
            } else ntn = 64;
        }
    };
    auto step = [&](auto RR, auto KK, int ff) __attribute__((always_inline)) {
        constexpr int R = decltype(RR)::value;
        constexpr bool first = decltype(KK)::value == 1, last = decltype(KK)::value == 2;
        constexpr int mdp = first ? MDF : MDP;
        gather(R, 0, 0);
        const int ntl = ntn;
        asm volatile("v_mov_b32_dpp %0, v106 quad_perm:[0,0,0,0] row_mask:0xf bank_mask:0xf\n\tv_mov_b32_dpp %1, v106 quad_perm:[1,1,1,1] row_mask:0xf bank_mask:0xf" : "=v"(ta[0]), "=v"(ta[1]));
        auto side = [&](int pl) __attribute__((always_inline)) {
            if (first && pl == ((mdp >> 20) & 15)) { ring[4] = load_main(4, ff, !(NEARM & 1u)); ring[5] = load_main(5, ff, !(NEARM & 1u)); }
            if (first && pl == ((mdp >> 24) & 15)) { ring[6] = load_main(6, ff); ring[7] = load_main(7, ff); }
            if (pl == ((mdp >> 12) & 15)) ring[NFAST] = load_tail(ta[0], tm0);
            if (pl == ((mdp >> 16) & 15)) ring[NFAST + 1] = load_tail(ta[1], tm1);
            __builtin_amdgcn_sched_barrier(0);
            if (!last) load_md(ff + 1, pl, mdp);
            if (!first && !last && pl == 5 && ff == 6) {
                load_d(me);
                load_wl();
                if (tid == NT - 128) (void)__hip_atomic_fetch_add((GAS unsigned*)((GAS char*)dw + vconst<0>()), 0x1000000u << me, __ATOMIC_RELAXED, __HIP_MEMORY_SCOPE_AGENT);
            }
            if (last && pl == 3) load_m(me);
            if (last && pl == 4 && wv == 15) { GAS unsigned* d_ = dw; asm volatile("s_nop 4\n\tglobal_load_dword v108, %0, %1 sc1" :: "v"(vconst<0>()), "s"(d_) : "memory"); }
            if (last && pl < 7 && fin) factor(pl, pl == 0);
            __builtin_amdgcn_sched_barrier(0);
        };
        side(8);
        gather(R, 1, 1);
        side(9);
        const uint32_t tcur = tc.x, spc = sp;
#pragma unroll
        for (int i = 0; i < NFAST - 1; i++) {
            gather(R, i + 2, (i + 2) % 3);
            if (i == 0) side(10);
            if (i == 0) WAIT_L(6); else if (i == 1) WAIT_L(9); else WAIT_L(12);
            scatter(R, i, i % 3, 0ull);
            if (!last && (i & 1)) { ring[i - 1] = load_main(i - 1, ff + 1, first && !(NEARM & 4u)); ring[i] = load_main(i, ff + 1, first && !(NEARM & 4u)); }
            side(i);
            if (i == 3) SETP(2);
            if (i == 4) SETP(1);
            if (i == 5) SETP(0);
            if (i == 0 && !last) {
                tc.x = __builtin_amdgcn_alignbit(tc.y, tc.x, 8u); tc.y = __builtin_amdgcn_alignbit(tc.z, tc.y, 8u);
                tc.z = __builtin_amdgcn_alignbit(tc.w, tc.z, 8u); tc.w >>= 8;
                sp = ff == 7 ? sp0 + NWG * 9u * 128u : sp + ((tcur & 0x3cu) << 2);
                list1(ff + 1);
            }
            __builtin_amdgcn_sched_barrier(0);
        }
        if (last) asm volatile("" :: "v"(ring[NFAST + 1]));
        if (__builtin_expect(ntl > 16, 0)) {
            gather(R, NFAST + 1, 0);
            WAIT_L(0);
            scatter(R, NFAST + 1, 0, tm1);
        }
        if (__builtin_expect(ntl > 32, 0)) {
#pragma unroll 1
            for (int e = 2; e < 6; e++) {
                int dst;
                uint32_t en;
                if (tail_rank(tcur, spc, ff, (uint32_t)e & 4u, dst, en) <= (e & 3) * 16) continue;
                const int cmp = __builtin_amdgcn_ds_permute(dst, (int)en);
                ring[NFAST + 1] = load_tail((uint32_t)__builtin_amdgcn_ds_bpermute(((lane & ~3) | (e & 3)) << 2, cmp), tm1);
                gather(R, NFAST + 1, 0);
                WAIT_L(0);
                scatter(R, NFAST + 1, 0, tm1);
            }
        }
        SETP(3);
        WAIT_L(3);
        if (!last) {
            __builtin_amdgcn_s_barrier();
            store_md();
            SETP(0);
        }
        scatter(R, NFAST - 1, 1, 0ull);
        if (!last) { ring[6] = load_main(6, ff + 1); ring[7] = load_main(7, ff + 1); }
        scatter(R, NFAST, 2, tm0);
        if (!last) {
            WAIT_L(6);
            __builtin_amdgcn_s_barrier();
            SETP(3);
        }
    };
    auto steps = [&](auto RR) __attribute__((always_inline)) {
        tc = tcnt[(wv * 16 + (lane >> 2)) * 128 + j * 4 + h];
        u32x4 sv[2];
        uint32_t e0;
        if (wv < 4) {
            sv[0] = (u32x4)(0u); sv[1] = sv[0];
            if (lane < SLOTI / 256) { sv[0] = stat[tid * 2]; sv[1] = stat[tid * 2 + 1]; }
            e0 = meta[M_E0 + wv];
        }
        const u32x2 extra = *(const u32x2*)(meta + M_LOG);
        load_md(0, -1, 0);
        {
            const u32x4 bi = {BIAS, BIAS, BIAS, BIAS};
#pragma unroll
            for (int k = 0; k < 6; k++) *(LDS u32x4*)(uintptr_t)(A_ACC + (k * NT + threadIdx.x) * 16) = bi;
            if (threadIdx.x < 128) *(LDS u32x4*)(uintptr_t)(A_ACC + (6 * NT + threadIdx.x) * 16) = bi;
            __builtin_amdgcn_sched_barrier(0);
        }
#pragma unroll
        for (int i = 0; i < 2; i++) ring[i] = load_main(i, 0);
        __builtin_amdgcn_sched_barrier(0);
        list1(0);
        if (wv < 4) {
            const uint32_t mxb = wave_max(max(max(sv[0].x & 0xffffu, sv[0].z & 0xffffu), max(sv[1].x & 0xffffu, sv[1].z & 0xffffu)));
            const uint32_t mnb = 0xffffu - (wave_max(max(max(sv[0].x, sv[0].z), max(sv[1].x, sv[1].z))) >> 16);
            const float s2 = wave_sum((__uint_as_float(sv[0].y) + __uint_as_float(sv[0].w)) + (__uint_as_float(sv[1].y) + __uint_as_float(sv[1].w)));
            const float rng = fmaxf(sqrtf(zf * s2), 2.0f * bf2f(mxb)) * 6.103515625e-05f;
            const uint32_t eb = mxb ? (__float_as_uint(rng) + 0x7fffffu) >> 23 : 127u;
            const bool ok = !mxb || (mxb < 0x4700u && eb >= 107u && eb <= 140u && mnb >= ((eb + 8u) << 7));
            if (lane == 0) {
                s_flag[8 + wv] = (eb & 0xffu) | (ok ? 0x100u : 0u) | (mxb ? 0x200u : 0u);
                s_flag[12 + wv] = __float_as_uint(__uint_as_float(((eb + e0) & 0xffu) << 23) * (1024.0f / 1023.0f));
                s_flag[wv] = 0u;
            }
        }
        store_md();
#pragma unroll
        for (int i = 2; i < 4; i++) ring[i] = load_main(i, 0);
        WAIT_L(0);
        __syncthreads();
        const uint32_t q0 = __builtin_amdgcn_readfirstlane(s_flag[8 + 2 * cp]), q1 = __builtin_amdgcn_readfirstlane(s_flag[9 + 2 * cp]);
        fast = (q0 & q1 & 0x100u) != 0u;
        need = (q0 >> 9) | ((q1 >> 9) << 1);
        more = (__builtin_amdgcn_readfirstlane(extra.x | extra.y) | (uint32_t)(T & 3)) != 0u;
        mg0 = (((q0 & 0xffu) + 9u) << 23) | 0x400000u;
        mg1 = ((((q1 & 0xffu) + 9u) << 23) | 0x400000u) + ((0u - (mg0 >> 16)) & 0xffffu);
        if (!fast) { __builtin_amdgcn_s_waitcnt(0x0f70); return; }
        step(RR, IC<1>(), 0);
#pragma unroll 1
        for (int ff = 1; ff < 15; ff++) step(RR, IC<0>(), ff);
        step(RR, IC<2>(), 15);
        if (fin) factor(7, false);
    };
    auto irregular = [&](auto f) __attribute__((always_inline)) {
        const float fr = bf2f(freq[0]);
        const char* lg = recs + (size_t)LBASE * 128u;
        const uint16_t* tags = (const uint16_t*)(lg + (size_t)lcap * 128u);
        const uint32_t nl = meta[M_LOG], ns = meta[M_LOG + 1];
#pragma unroll 1
        for (uint32_t q = tid; q < nl * 8u; q += NT) {
            const uint32_t b = tags[q >> 3];
            if ((b & 127u) == (uint32_t)(h * 32 + j)) {
                const uint32_t* un = (const uint32_t*)(lg + (size_t)q * 16u);
                const uint32_t xw = un[3 ^ h];
#pragma unroll 1
                for (int k = 0; k < 3; k++) {
                    const uint32_t ww = un[k ^ h];
                    const float c = (float)((xw >> (k * 11)) & 0x3ffu) * (1.0f / 1023.0f);
                    if (!(ww & WNULL)) f(ww >> 16, ((ww & 0x3fffu) << 6) | (b >> 5), (ww & 0x8000u) ? -c : c);
                }
            }
        }
#pragma unroll 1
        for (uint32_t q = tid; q < ns + (uint32_t)(T & 3); q += NT) {
            const uint32_t t = q < ns ? ((const uint32_t*)tags)[-1 - (int)q] : (uint32_t)(T & ~3) + (q - ns);
            const uint32_t i = (uint32_t)iin[t], o = (uint32_t)iout[t];
            if (((o >> 6) & 31u) == (uint32_t)j && (i & 3u) == (uint32_t)h) f(((o >> 11) << 6) | (o & 63u), i, cosf(fr * bf2f(ang[t])));
        }
        __builtin_amdgcn_s_waitcnt(0x0f70);
    };

    if (h == 0) steps(IC<0>());
    else if (h == 1) steps(IC<1>());
    else if (h == 2) steps(IC<2>());
    else steps(IC<3>());
    if (fast) {
        if (more) irregular([&](uint32_t ol, uint32_t i, float cf) __attribute__((always_inline)) {
            const uint32_t md = mdc[(cp * 64u + (i & 63u)) * SLOTI + ((i >> 14) << 8) + ((i >> 4) & 0xfcu) + ((i >> 12) & 3u)];
            const float c = cf * (1023.0f / 16777216.0f);
            const uint32_t lo = __float_as_uint(__builtin_fmaf(c, h2f(md & 0xffffu), __uint_as_float(mg0)));
            const uint32_t hi = __float_as_uint(__builtin_fmaf(c, h2f(md >> 16), __uint_as_float(mg1)));
            if (cf == cf) guard |= atomicAdd(&acc[ol], (hi << 16) + lo);
            else guard |= HSLOW;
        });
    }
    volatile LDS unsigned* seen = (volatile LDS unsigned*)(uintptr_t)(A_FLAG + 4);
    if (fast && wv == 15) asm volatile("s_waitcnt vmcnt(0)\n\tds_write_b32 %0, v108 offset:16\n\tv_and_b32 v109, 0xff00ff, v108\n\tds_write_b32 %0, v109" :: "v"(vconst<A_FLAG + 4>()) : "memory");
    WAIT_L(0);
    asm volatile("v_or3_b32 %0, %0, v110, v111\n\tv_or3_b32 %0, %0, v112, v113\n\tv_or3_b32 %0, %0, v114, v115\n\tv_or3_b32 %0, %0, v116, v117\n\tv_or_b32 %0, v118, %0" : "+v"(guard));
    __syncthreads();
    const __amdgpu_buffer_rsrc_t rsP = __builtin_amdgcn_make_buffer_rsrc((void*)(partial + (size_t)j * 16 * SLOTO), 0, 16 * SLOTO * 4, 0x00020000);
    const char* pk = recs + (uint32_t)j * (SC * 128u);
    const __amdgpu_buffer_rsrc_t rsK = __builtin_amdgcn_make_buffer_rsrc((void*)pk, 0, (int)(MAINL * 128u), 0x00020000);
#define PKA (SSM * 128u)
#define PKB (32u * SC * 128u)
    auto pk_at = [&](uint32_t i) __attribute__((always_inline)) -> uint32_t {
        const uint32_t c = i >> 9;
        return (c < 12u ? (1u + (c >> 2)) * SQ + (c & 3u) * SKM : SQ + 2u * SSM) * 128u + (i & 511u) * 16u;
    };
    u32x4 p[8];
    unsigned have = 0u;
    auto fetch = [&](unsigned v, int sh) __attribute__((always_inline)) {
        const uint32_t po = tid < SHARE / 4 ? pk_at((uint32_t)(sh * (SHARE / 4) + tid)) : 0x7fffff00u;
        const unsigned nw = v & ~have;
#pragma unroll
        for (int r = 0; r < 8; r++) {
            const uint32_t k = (uint32_t)(me ^ r);
            if ((nw >> k) & 1u) p[r] = __builtin_amdgcn_raw_buffer_load_b128(rsK, po, ((v >> (16u + k)) & 1u) ? MAINL * 128u : (k & 1u) * PKA + (k >> 1) * PKB, AUX_SC1);
        }
        have |= v;
    };
    bool room = false;
    if (fast) {
        room = (__builtin_amdgcn_readfirstlane(s_flag[5]) >> (24 + (me ^ 1))) & 1u;
        const __amdgpu_buffer_rsrc_t rsW = __builtin_amdgcn_make_buffer_rsrc((void*)(pk + (uint32_t)cp * PKA + (uint32_t)h * PKB), 0, room ? (int)(MAINL * 128u) : 0, 0x00020000);
        uint32_t big = 0u;
        u32x4 v[7];
#pragma unroll
        for (int k = 0; k < 6; k++) v[k] = acc_vec(k * NT + tid);
        v[6] = (u32x4)(BIAS);
        if (wv < 2) v[6] = acc_vec(6 * NT + tid);
#pragma unroll
        for (int k = 0; k < 6; k++) __builtin_amdgcn_raw_buffer_store_b128(v[k], rsW, pk_at((uint32_t)tid) + ((k >> 1) * SQ + (k & 1) * 2u * SKM) * 128u, 0, AUX_SC1);
        if (wv < 2) __builtin_amdgcn_raw_buffer_store_b128(v[6], rsW, (SQ + 2u * SSM) * 128u + (uint32_t)tid * 16u, 0, AUX_SC1);
        __builtin_amdgcn_sched_barrier(0);
#pragma unroll
        for (int k = 0; k < 28; k++) { guard |= v[k >> 2][k & 3]; big |= v[k >> 2][k & 3] + 0x42004200u; }
        if (guard & HSLOW) s_flag[0] = 1u;
        if (big & 0x7c00u) s_flag[2] = 1u;
        if (big & 0x7c000000u) s_flag[3] = 2u;
        __builtin_amdgcn_sched_barrier(0);
        p[0] = acc_vec(me * (SHARE / 4) + min(tid, SHARE / 4 - 1));
        have = 1u << me;
        if (fin) fetch(__builtin_amdgcn_readfirstlane(*seen), me);
    }
    asm volatile("s_waitcnt vmcnt(0)" ::: "memory");
    __syncthreads();
    const bool redo = !fast || __builtin_amdgcn_readfirstlane(s_flag[0]) != 0u || (need & ~__builtin_amdgcn_readfirstlane(s_flag[2] | s_flag[3])) != 0u;
    const bool slow = redo || !room;
    if (slow) {
        float* fs = (float*)acc;
        float w[16];
        load_w(wdown, w);
        const float fr = bf2f(freq[0]);
#pragma unroll 1
        for (int e = 0; e < 2; e++) {
            if (redo) {
                __syncthreads();
                for (int i = tid; i < SLOTO; i += NT) fs[i] = 0.f;
                __syncthreads();
                const __amdgpu_buffer_rsrc_t rsO = __builtin_amdgcn_make_buffer_rsrc((void*)iout, 0, T * 4, 0x00020000);
                const __amdgpu_buffer_rsrc_t rsI = __builtin_amdgcn_make_buffer_rsrc((void*)iin, 0, T * 4, 0x00020000);
#pragma unroll 1
                for (int t0 = tid * 4; t0 < T; t0 += NT * 16) {
                    u32x4 o4[4], i4[4];
#pragma unroll
                    for (int g = 0; g < 4; g++) {
                        o4[g] = __builtin_amdgcn_raw_buffer_load_b128(rsO, (uint32_t)(t0 + g * NT * 4) * 4u, 0, 0);
                        i4[g] = __builtin_amdgcn_raw_buffer_load_b128(rsI, (uint32_t)(t0 + g * NT * 4) * 4u, 0, 0);
                    }
                    uint32_t hit = 0u;
#pragma unroll
                    for (int k = 0; k < 16; k++) hit |= (bfi(3u, i4[k >> 2][k & 3], o4[k >> 2][k & 3]) & 0x7c3u) == (uint32_t)(j * 64 + h) ? 1u << k : 0u;
#pragma unroll 1
                    for (; hit; hit &= hit - 1u) {
                        const int k = __builtin_ctz(hit), t = t0 + (k >> 2) * NT * 4 + (k & 3);
                        if (t >= T) break;
                        const uint32_t o = (uint32_t)iout[t];
                        uint32_t rr[4];
                        md_of(m[iin[t]], w, rr);
                        atomicAdd(&fs[((o >> 11) << 6) | (o & 63u)], cosf(fr * bf2f(ang[t])) * bf2f(cp ? (e ? rr[3] : rr[2]) : (e ? rr[1] : rr[0])));
                    }
                }
                __syncthreads();
            }
            const float un = __uint_as_float(s_flag[12 + 2 * cp + e]);
            for (int i = tid; i < SLOTO / 4; i += NT) {
                u32x4 v = acc_vec(i);
                if (!redo) {
#pragma unroll
                    for (int k = 0; k < 4; k++) v[k] = __float_as_uint((float)((int)((v[k] >> (16 * e)) & 0xffffu) - 0x4000) * un);
                }
                __builtin_amdgcn_raw_buffer_store_b128(v, rsP, (uint32_t)((e * 8 + me) * SLOTO * 4 + i * 16), 0, AUX_SC1);
            }
        }
        load_wl();
        edges(me);
#pragma unroll
        for (int k = 0; k < 8; k++) p[k] = (u32x4)(0u);
        have = 0u;
        asm volatile("s_waitcnt vmcnt(0)" ::: "memory");
        __syncthreads();
    }
    if (tid == NT - 64) {
        const unsigned inc = (slow ? 0x10001u : 1u) << me;
        unsigned v = __hip_atomic_fetch_add(dw, inc, __ATOMIC_RELAXED, __HIP_MEMORY_SCOPE_AGENT) + inc;
        unsigned quit = 0u;
        if ((v & 0xffu) != 0xffu) {
            unsigned spins = 0;
            do {
                *seen = v & 0xff00ffu;
                __builtin_amdgcn_s_sleep(1);
                v = __hip_atomic_load(dw, __ATOMIC_RELAXED, __HIP_MEMORY_SCOPE_AGENT);
                if (++spins > SPINCAP && (v & 0xffu) != 0xffu) {
                    v = __hip_atomic_fetch_add(dw, 256u << me, __ATOMIC_RELAXED, __HIP_MEMORY_SCOPE_AGENT);
                    if ((v & 0xffu) != 0xffu) { v = 0xffu; quit = 0x80000000u; }
                }
            } while ((v & 0xffu) != 0xffu);
            v &= 0xff00ffu;
        }
        *seen = (v & 0xffffffu) | quit;
    }
    if (!fin) return;
    have = __builtin_amdgcn_readfirstlane(have);
    uint32_t un[4];
#pragma unroll
    for (int c = 0; c < 4; c++) un[c] = __builtin_amdgcn_readfirstlane(s_flag[12 + c]);
    unsigned sv;
    {
        const u32x4 rsQ = {(uint32_t)(uintptr_t)pk, (uint32_t)((uintptr_t)pk >> 32) & 0xffffu, MAINL * 128u, 0x00020000u};
        const uint32_t po = tid < SHARE / 4 ? pk_at((uint32_t)(me * (SHARE / 4) + tid)) : 0x7fffff00u;
        do {
            sv = __builtin_amdgcn_readfirstlane(*seen);
            uint32_t t0, t1, t2;
#define FET(R) "s_xor_b32 %8, %13, " #R "\n\ts_bitcmp1_b32 %14, %8\n\ts_cbranch_scc0 1f\n\ts_and_b32 %9, %8, 1\n\ts_mul_i32 %9, %9, %16\n\ts_lshr_b32 %10, %8, 1\n\t" \
               "s_mul_i32 %10, %10, %17\n\ts_add_u32 %9, %9, %10\n\ts_bitcmp1_b32 %15, %8\n\ts_cselect_b32 %9, %18, %9\n\tbuffer_load_dwordx4 %" #R ", %11, %12, %9 offen sc1\n1:\n\t"
            asm volatile("s_and_b32 %8, %14, 0xff\n\ts_cbranch_scc0 2f\n\ts_nop 2\n\t" FET(0) FET(1) FET(2) FET(3) FET(4) FET(5) FET(6) FET(7) "\n2:\n\ts_sleep 2\n\ts_waitcnt vmcnt(0)"
                         : "+v"(p[0]), "+v"(p[1]), "+v"(p[2]), "+v"(p[3]), "+v"(p[4]), "+v"(p[5]), "+v"(p[6]), "+v"(p[7]), "=&s"(t0), "=&s"(t1), "=&s"(t2)
                         : "v"(po), "s"(rsQ), "s"(me), "s"((int)sv < 0 ? 0u : sv & ~have), "s"(sv >> 16), "n"(PKA), "n"(PKB), "n"(MAINL * 128u) : "memory", "scc");
            have |= sv;
        } while ((sv & 0xffu) != 0xffu);
    }
    if ((int)sv < 0) return;
    const unsigned flt = (sv >> 16) & 0xffu;
    auto finish = [&](int sh) __attribute__((always_inline)) {
        {
            const int ol = sh * SHARE + tid * 4;
            const uint32_t po = tid < SHARE / 4 ? (uint32_t)ol * 4u : 0x7fffff00u;
            float g[4][4];
            u32x4 a[2] = {p[0] + p[2], p[1] + p[3]}, b[2] = {p[4] + p[6], p[5] + p[7]};
            if (cp) { const u32x4 t = a[0], u = b[0]; a[0] = a[1]; b[0] = b[1]; a[1] = t; b[1] = u; }
#pragma unroll
            for (int c = 0; c < 2; c++) {
                const int nb = 0x10000 - 0x4000 * __builtin_popcount(flt & (0x55u << c));
                const float u0 = __uint_as_float(nb ? un[2 * c] : 0u), u1 = __uint_as_float(nb ? un[2 * c + 1] : 0u);
#pragma unroll
                for (int q = 0; q < 4; q++) {
                    g[q][2 * c] = (float)((int)((a[c][q] & 0xffffu) + (b[c][q] & 0xffffu)) - nb) * u0;
                    g[q][2 * c + 1] = (float)((int)((a[c][q] >> 16) + (b[c][q] >> 16)) - nb) * u1;
                }
            }
            if (__builtin_expect(flt != 0u, 0)) {
#pragma unroll
                for (int c = 0; c < 2; c++) {
#pragma unroll 1
                    for (int k = c; k < 8; k += 2) {
                        if (!((flt >> k) & 1u)) continue;
                        const u32x4 f0 = __builtin_amdgcn_raw_buffer_load_b128(rsP, po, k * SLOTO * 4, AUX_SC1);
                        const u32x4 f1 = __builtin_amdgcn_raw_buffer_load_b128(rsP, po, (8 + k) * SLOTO * 4, AUX_SC1);
#pragma unroll
                        for (int q = 0; q < 4; q++) { g[q][2 * c] += __uint_as_float(f0[q]); g[q][2 * c + 1] += __uint_as_float(f1[q]); }
                    }
                }
            }
            float wu[16];
#pragma unroll
            for (int k = 0; k < 8; k++) {
                const uint32_t x = __builtin_amdgcn_readlane(wl, 40 + k);
                wu[2 * k] = bf2f(x & 0xffffu); wu[2 * k + 1] = bf2f(x >> 16);
            }
            u32x4 ov[2];
#pragma unroll
            for (int q = 0; q < 4; q++) {
#pragma unroll
                for (int k = 0; k < 4; k++) g[q][k] *= fa[q][k >> 1][k & 1];
                const uint32_t m0 = em[q >> 1][(q & 1) * 2], m1 = em[q >> 1][(q & 1) * 2 + 1];
                const float mm[4] = {bf2f(m0 & 0xffff), bf2f(m0 >> 16), bf2f(m1 & 0xffff), bf2f(m1 >> 16)};
                float o[4];
#pragma unroll
                for (int k = 0; k < 4; k++) o[k] = mm[k] + (g[q][0] * wu[k] + g[q][1] * wu[4 + k] + g[q][2] * wu[8 + k] + g[q][3] * wu[12 + k]) * fa[q][2 + (k >> 1)][k & 1];
                ov[q >> 1][(q & 1) * 2] = pk_bf16(o[0], o[1]);
                ov[q >> 1][(q & 1) * 2 + 1] = pk_bf16(o[2], o[3]);
            }
            const __amdgpu_buffer_rsrc_t rsU = __builtin_amdgcn_make_buffer_rsrc((void*)out, 0, E * 8, 0x00020000);
            const uint32_t uo = edge0(sh) * 8u;
            __builtin_amdgcn_raw_buffer_store_b128(ov[0], rsU, uo, 0, AUX_NT);
            __builtin_amdgcn_raw_buffer_store_b128(ov[1], rsU, uo + 256u, 0, AUX_NT);
        }
    };
    finish(me);
#pragma unroll 1
    for (unsigned todo = (sv >> 8) & 0xffu; todo; todo &= todo - 1u) {
        const int sh = __builtin_ctz(todo);
        edges(sh);
        have = 0u;
        fetch(sv, sh);
        finish(sh);
    }
}

__global__ __launch_bounds__(256) void gen_scatter_kernel(const int* __restrict__ iin, const int* __restrict__ iout,
                                                          const uint16_t* __restrict__ ang, const uint16_t* __restrict__ freq,
                                                          const u32x2* __restrict__ m, const uint16_t* __restrict__ wdown,
                                                          float* __restrict__ agg, int T) {
    float w[16];
    load_w(wdown, w);
    const float fr = bf2f(freq[0]);
    for (long t = (long)blockIdx.x * 256 + threadIdx.x; t < T; t += (long)gridDim.x * 256) {
        const float c = cosf(fr * bf2f(ang[t]));
        uint32_t r[4];
        md_of(m[iin[t]], w, r);
        float* q = agg + (size_t)iout[t] * 4;
#pragma unroll
        for (int k = 0; k < 4; k++) atomicAdd(q + k, c * bf2f(r[k]));
    }
}
__global__ __launch_bounds__(256) void gen_final_kernel(const u32x2* __restrict__ m, const uint16_t* __restrict__ dist,
                                                        const uint16_t* __restrict__ centers, const uint16_t* __restrict__ wrbf,
                                                        const uint16_t* __restrict__ wsbf, const uint16_t* __restrict__ wup,
                                                        const float* __restrict__ agg, u32x2* __restrict__ out, int E) {
    const int e = blockIdx.x * 256 + threadIdx.x;
    if (e >= E) return;
    const float d = bf2f(dist[e]);
    float rs[4] = {0, 0, 0, 0}, rg[4] = {0, 0, 0, 0};
#pragma unroll 1
    for (int r = 0; r < 8; r++) {
        const float x = d - bf2f(centers[r]);
        const float rb = __expf(-10.0f * x * x);
#pragma unroll
        for (int k = 0; k < 4; k++) { rs[k] += rb * bf2f(wsbf[r * 4 + k]); rg[k] += rb * bf2f(wrbf[r * 4 + k]); }
    }
    float wu[16];
    load_w(wup, wu);
    const u32x2 mv = m[e];
    const float mm[4] = {bf2f(mv.x & 0xffff), bf2f(mv.x >> 16), bf2f(mv.y & 0xffff), bf2f(mv.y >> 16)};
    float g[4], o[4];
#pragma unroll
    for (int k = 0; k < 4; k++) g[k] = agg[(size_t)e * 4 + k] * rs[k];
#pragma unroll
    for (int k = 0; k < 4; k++) o[k] = mm[k] + (g[0] * wu[k] + g[1] * wu[4 + k] + g[2] * wu[8 + k] + g[3] * wu[12 + k]) * rg[k];
    const u32x2 ov = {f2bf(o[0]) | (f2bf(o[1]) << 16), f2bf(o[2]) | (f2bf(o[3]) << 16)};
    out[e] = ov;
}

extern "C" void kernel_launch(void* const* d_in, const int* in_sizes, int n_in,
                              void* d_out, int out_size, void* d_ws, size_t ws_size,
                              hipStream_t stream) {
    const u32x2* m = (const u32x2*)d_in[0];
    const int* iin = (const int*)d_in[1];
    const int* iout = (const int*)d_in[2];
    const uint16_t* dist = (const uint16_t*)d_in[3];
    const uint16_t* ang = (const uint16_t*)d_in[4];
    const uint16_t* centers = (const uint16_t*)d_in[5];
    const uint16_t* freq = (const uint16_t*)d_in[6];
    const uint16_t* wrbf = (const uint16_t*)d_in[7];
    const uint16_t* wsbf = (const uint16_t*)d_in[8];
    const uint16_t* wdown = (const uint16_t*)d_in[9];
    const uint16_t* wup = (const uint16_t*)d_in[10];
    const int E = in_sizes[3];
    const int T = in_sizes[1];
    char* ws = (char*)d_ws;
    const int G = T >> 2;
    const int ntile = (G + GR - 1) / GR;
    const int nstat = ntile / NWG - NDYN > 4 ? ntile / NWG - NDYN : 4;
    const int lcap = G / 6 + 2;
    char* recs = ws + W_RECS;
    size_t off = W_RECS;
    const size_t rbytes = (size_t)LBASE * 128 + (size_t)lcap * 130;
    off += rbytes;
    if (E > EMAX || (E & 3) || E < 1 || rbytes > 0x70000000u || off > ws_size) {
        (void)hipMemsetAsync(ws, 0, (size_t)E * 16, stream);
        gen_scatter_kernel<<<4096, 256, 0, stream>>>(iin, iout, ang, freq, m, wdown, (float*)ws, T);
        gen_final_kernel<<<(E + 255) / 256, 256, 0, stream>>>(m, dist, centers, wrbf, wsbf, wup, (const float*)ws, (u32x2*)d_out, E);
        return;
    }
    int ks = T / (NWG * NB * 24);
    if (ks > KMAX) ks = KMAX;
    const float zf = ZSIG * ZSIG * (float)T / (8.0f * (float)E * (float)E);
    (void)hipFuncSetAttribute((const void*)part_kernel, hipFuncAttributeMaxDynamicSharedMemorySize, P_LDS);
    (void)hipFuncSetAttribute((const void*)acc_kernel, hipFuncAttributeMaxDynamicSharedMemorySize, A_LDS);
    part_kernel<<<NWG, NT, P_LDS, stream>>>(iin, iout, ang, freq, m, wdown, recs, T, E, ntile, nstat, ks, lcap);
    acc_kernel<<<NWG, NT, A_LDS, stream>>>(ws, T, E, ks, lcap, zf, 0, centers, wrbf, wsbf, wup, m, dist, (u32x2*)d_out, iin, iout, ang, wdown, freq);
}
```

```cpp
#include <hip/hip_runtime.h>
#include <hip/hip_bf16.h>
#include <stdint.h>
#pragma clang diagnostic ignored "-Winline-asm"

typedef unsigned int u32x4 __attribute__((ext_vector_type(4)));
typedef unsigned int u32x2 __attribute__((ext_vector_type(2)));
typedef unsigned short u16x2 __attribute__((ext_vector_type(2)));
typedef __bf16 bf16x2 __attribute__((ext_vector_type(2)));
typedef float f32x2 __attribute__((ext_vector_type(2)));
typedef unsigned long long u64;

static __device__ __forceinline__ float bf2f(uint32_t bits16) { return __uint_as_float(bits16 << 16); }
static __device__ __forceinline__ uint32_t f2bf(float f) {
    uint32_t u = __float_as_uint(f);
    if ((u & 0x7fffffffu) > 0x7f800000u) return (u >> 16) | 0x40;
    return (u + 0x7fffu + ((u >> 16) & 1u)) >> 16;
}
static __device__ __forceinline__ float h2f(uint32_t bits16) { return (float)__builtin_bit_cast(_Float16, (unsigned short)bits16); }

#define NB 2048
#define WNULL 0x80000000u
#define CSTEP 0x2aabu
#define CINIT 205u
#define CHV (12u * CSTEP)
#define SLOTO 25088
#define SLOTI 12544
#define EMAX (SLOTO * 32)
#define NT 1024
#define NWG 256
#define KMAX 4
#define NTAIL 4
#define SC 64u
#define SKM (128u * SC + 2u)
#define SSM (4u * SKM + 1u)
#define SSH (128u * SC + 1u)
#define SKH (16u * SSH + 2u)
#define SQ (16u * SSM)
#define MAINL (4u * SQ)
#define BPL(b) ((((b) >> 3) & 3u) * SQ + ((b) & 7u) + (((b) >> 5) << 3))
#define TST 257u
#define TBASE MAINL
#define TPL (NB * TST)
#define SBASE (TBASE + 2u * TPL)
#define LBASE (SBASE + 128u * 2u * NWG * 9u)
#define NDYN 3
#define NEARM 0x55555554u
#define GR 2048
#define M_DONE 0
#define M_TILE (32 * 64)
#define M_LOG (M_TILE + 64)
#define M_MD (M_LOG + 64)
#define M_BEAT (M_MD + 64)
#define M_E0 (M_BEAT + 64)
#define M_WORDS (M_E0 + 64)
#define W_TCNT (M_WORDS * 4)
#define W_STAT (W_TCNT + NWG * NB)
#define W_SUM (W_STAT + 4 * NWG * 8)
#define W_MDC (W_SUM + 6 * NWG * 4)
#define W_PART (W_MDC + 128 * SLOTI * 4)
#define W_RECS (W_PART + 32 * 16 * SLOTO * 4)
#define AUX_NT 2
#define AUX_SC1 16

static __device__ __forceinline__ uint32_t row_max(uint32_t x) {
    x = max(x, (uint32_t)__builtin_amdgcn_update_dpp(0, x, 0x111, 0xf, 0xf, false));
    x = max(x, (uint32_t)__builtin_amdgcn_update_dpp(0, x, 0x112, 0xf, 0xf, false));
    x = max(x, (uint32_t)__builtin_amdgcn_update_dpp(0, x, 0x114, 0xf, 0xf, false));
    return max(x, (uint32_t)__builtin_amdgcn_update_dpp(0, x, 0x118, 0xf, 0xf, false));
}
static __device__ __forceinline__ uint32_t wave_max(uint32_t x) {
    x = row_max(x);
    x = max(x, (uint32_t)__builtin_amdgcn_update_dpp(0, x, 0x142, 0xa, 0xf, false));
    x = max(x, (uint32_t)__builtin_amdgcn_update_dpp(0, x, 0x143, 0xc, 0xf, false));
    return __builtin_amdgcn_readlane(x, 63);
}
static __device__ __forceinline__ float row_sum(float x) {
    x += __int_as_float(__builtin_amdgcn_update_dpp(0, __float_as_int(x), 0x111, 0xf, 0xf, false));
    x += __int_as_float(__builtin_amdgcn_update_dpp(0, __float_as_int(x), 0x112, 0xf, 0xf, false));
    x += __int_as_float(__builtin_amdgcn_update_dpp(0, __float_as_int(x), 0x114, 0xf, 0xf, false));
    return x + __int_as_float(__builtin_amdgcn_update_dpp(0, __float_as_int(x), 0x118, 0xf, 0xf, false));
}
static __device__ __forceinline__ float wave_sum(float x) {
    x = row_sum(x);
    x += __int_as_float(__builtin_amdgcn_update_dpp(0, __float_as_int(x), 0x142, 0xa, 0xf, false));
    x += __int_as_float(__builtin_amdgcn_update_dpp(0, __float_as_int(x), 0x143, 0xc, 0xf, false));
    return __int_as_float(__builtin_amdgcn_readlane(__float_as_int(x), 63));
}
static __device__ __forceinline__ uint32_t bfi(uint32_t mask, uint32_t a, uint32_t b) {
    return __builtin_amdgcn_bitop3_b32(mask, a, b, 0xca);
}
template <uint32_t C> static __device__ __forceinline__ uint32_t vconst() {
    uint32_t r;
    asm volatile("v_mov_b32 %0, %1" : "=v"(r) : "n"(C));
    return r;
}
static __device__ __forceinline__ uint32_t pk_bf16(float a, float b) {
    const f32x2 v = {a, b};
    return __builtin_bit_cast(uint32_t, __builtin_convertvector(v, bf16x2));
}
static __device__ __forceinline__ void pin(uint32_t& x) { asm volatile("" : "+v"(x)); }


static __device__ __forceinline__ void md_of(u32x2 v, const float* w, uint32_t* r) {
    float a0 = bf2f(v.x & 0xffff), a1 = bf2f(v.x >> 16), a2 = bf2f(v.y & 0xffff), a3 = bf2f(v.y >> 16);
#pragma unroll
    for (int j = 0; j < 4; j++) r[j] = f2bf(a0 * w[j] + a1 * w[4 + j] + a2 * w[8 + j] + a3 * w[12 + j]);
}
static __device__ __forceinline__ void load_w(const uint16_t* __restrict__ wdown, float* w) {
    u32x4 wv0 = *(const u32x4*)wdown, wv1 = *(const u32x4*)(wdown + 8);
#pragma unroll
    for (int i = 0; i < 4; i++) {
        w[2 * i] = bf2f(wv0[i] & 0xffffu); w[2 * i + 1] = bf2f(wv0[i] >> 16);
        w[8 + 2 * i] = bf2f(wv1[i] & 0xffffu); w[8 + 2 * i + 1] = bf2f(wv1[i] >> 16);
    }
}
#define LDS __attribute__((address_space(3)))
#define P_CNT 0
#define P_FLAG ((NB + 64) * 4)
#define P_LST (P_FLAG + 256)
#define LCAP 32
#define P_TAB (P_LST + 4 * LCAP * 16)
#define P_BUF (P_TAB + NB * 4)
#define P_LDS (P_BUF + NB * 64)
#define PD 2
#define LPOS 0x448763
#define SETP(n) __builtin_amdgcn_s_setprio(n)
#define MDL 1
#define MDM 3
#define RMAXREV 40.0f
#define HB 88
#define HREG(S, D) (HB + ((S) & 3) * 10 + (((S) >> 2) + (D)) * 2)

template <int N> struct IC { static constexpr int value = N; };
struct Raw { u32x4 o[2], i[2]; u32x2 a[2]; };
static __device__ __forceinline__ uint32_t lds_ld(uint32_t a) { return *(const LDS uint32_t*)(uintptr_t)a; }
static __device__ __forceinline__ void lds_st(uint32_t a, uint32_t v) { *(LDS uint32_t*)(uintptr_t)a = v; }
static __device__ __forceinline__ void lds_or(uint32_t a, uint32_t v) {
    __hip_atomic_fetch_or((LDS uint32_t*)(uintptr_t)a, v, __ATOMIC_RELAXED, __HIP_MEMORY_SCOPE_WORKGROUP);
}
static __device__ __forceinline__ uint32_t lds_add(uint32_t a, uint32_t v) {
    return __hip_atomic_fetch_add((LDS uint32_t*)(uintptr_t)a, v, __ATOMIC_RELAXED, __HIP_MEMORY_SCOPE_WORKGROUP);
}
static __device__ __forceinline__ uint32_t slot_w(uint32_t ca, uint32_t cv) {
    return (ca << 4) | __builtin_amdgcn_bitop3_b32(cv >> 11, ca, 0x3cu, 0x28);
}
static __device__ __forceinline__ uint32_t slot_x2(uint32_t aw, uint32_t ca) {
    return __builtin_amdgcn_bitop3_b32(aw, ca, 0xcu, 0x72);
}
static __device__ __forceinline__ uint32_t slot_x(uint32_t aw) { return slot_x2(aw, aw >> 4); }
template <int R> static __device__ __forceinline__ u32x2 hget() {
    u32x2 d;
    asm volatile("v_mov_b64 %0, v[%1:%2]" : "=v"(d) : "n"(R), "n"(R + 1));
    return d;
}
static __device__ __forceinline__ u32x2 hget_at(int r) {
    u32x2 d;
    asm volatile("s_set_gpr_idx_on %2, gpr_idx(SRC0)\n\ts_nop 1\n\tv_mov_b32 %0, v88\n\tv_mov_b32 %1, v89\n\ts_set_gpr_idx_off"
                 : "=&v"(d.x), "=&v"(d.y) : "s"(r));
    return d;
}
static __device__ __forceinline__ void hset_at(int r, u32x2 d) {
    asm volatile("s_set_gpr_idx_on %2, gpr_idx(DST)\n\ts_nop 1\n\tv_mov_b32 v88, %0\n\tv_mov_b32 v89, %1\n\ts_set_gpr_idx_off"
                 :: "v"(d.x), "v"(d.y), "s"(r));
}

template <int S, int D> static __device__ __forceinline__ uint32_t take(uint32_t V, uint32_t u16, uint32_t la, u32x2 nul) {
    const uint32_t a = __builtin_amdgcn_bitop3_b32(V, u16, 15u, 0x54);
    asm volatile("v_mov_b64 v[%4:%4+1], v[%3:%3+1]\n\tv_cmpx_ne_u32 vcc, -16, %0\n\tds_wrxchg_rtn_b64 v[%4:%4+1], %1, %2 offset:%5\n\ts_mov_b64 exec, -1"
                 :: "v"(a), "v"(la), "v"(nul), "n"(HREG(S, D)), "n"(HREG(S, 1 - D)), "n"(S * 256) : "memory", "vcc");
    return a;
}
#define PUT(P) asm volatile("s_bitcmp1_b32 %3, %4\n\ts_cbranch_scc0 1f\n\ts_waitcnt lgkmcnt(%5)\n\tbuffer_store_dwordx4 v[%2:%2+3], %0, %1, 0 offen sc1\n" \
                 "1:\n\ts_bitcmp1_b32 %3, %4+1\n\ts_cbranch_scc0 2f\n\ts_waitcnt lgkmcnt(%5)\n\tbuffer_store_dwordx4 v[%2:%2+3], %0, %1, 0 offen " P "\n\ts_nop 1\n2:" \
                 :: "v"(ad), "s"(rsA), "n"(HREG(S, 0)), "s"(pm), "n"(2 * S), "n"(W) : "memory", "scc")
template <int S, int W, bool WT> static __device__ __forceinline__ void put(u32x4 rsA, uint32_t ad, uint32_t pm) {
    if (WT) PUT("sc1 nt"); else PUT("nt");
}

__global__ __launch_bounds__(NT, 4) __attribute__((amdgpu_num_vgpr(HB / 2))) void part_kernel(const int* __restrict__ iin, const int* __restrict__ iout,
                                                  const uint16_t* __restrict__ ang, const uint16_t* __restrict__ freq,
                                                  const u32x2* __restrict__ m, const uint16_t* __restrict__ wdown,
                                                  char* __restrict__ recs,
                                                  int T, int E, int ntile, int nstat, int ks, int lcap) {
    asm volatile("" :: "s"(iin), "s"(iout), "s"(ang), "s"(freq), "s"(m), "s"(wdown), "s"(recs), "s"(T), "s"(E), "s"(ntile), "s"(nstat), "s"(ks), "s"(lcap));
    const int tid0 = threadIdx.x;
    const int wv = __builtin_amdgcn_readfirstlane(tid0 >> 6);
    const int G = T >> 2;
    const int pos = (int)((blockIdx.x & 7) << 5) | (int)(blockIdx.x >> 3);
    float frr;
    unsigned* meta = (unsigned*)(recs - W_RECS);
    const uint32_t gbase = (LBASE + (uint32_t)lcap) * 128u;
    const uint32_t rbytes = gbase + (uint32_t)lcap * 2u;
    const __amdgpu_buffer_rsrc_t rsR = __builtin_amdgcn_make_buffer_rsrc(recs, 0, (int)rbytes, 0x00020000);
    const u32x4 rsA = {(uint32_t)(uintptr_t)recs, (uint32_t)((uintptr_t)recs >> 32) & 0xffffu, rbytes, 0x00020000u};
    const uint32_t g2 = 2u * (uint32_t)ks;
    const uint32_t far = 1u >> ((blockIdx.x >> 3) & 3u);
    const uint32_t SK = SKM + far * (SKH - SKM), SS = SSM + far * (SSH - SSM), UINC = 0x80000001u + SK * 64u;
    const uint32_t spec0 = gbase - 4u;
    auto fixed_tile = [&](int r) __attribute__((always_inline)) -> int { return r * NWG + ((pos + r * 37) & (NWG - 1)); };
    int tl0 = fixed_tile(0), tl1 = fixed_tile(1), tl2;
    uint32_t tk = 0u;
    const __amdgpu_buffer_rsrc_t rsO = __builtin_amdgcn_make_buffer_rsrc((void*)iout, 0, G * 16, 0x00020000);
    const __amdgpu_buffer_rsrc_t rsI = __builtin_amdgcn_make_buffer_rsrc((void*)iin, 0, G * 16, 0x00020000);
    const __amdgpu_buffer_rsrc_t rsG = __builtin_amdgcn_make_buffer_rsrc((void*)ang, 0, G * 8, 0x00020000);
    uint32_t t8 = (uint32_t)tid0 * 8u;
    auto load_one = [&](Raw& r, int tl, int idx) __attribute__((always_inline)) {
        const int j = idx < 4 ? idx >> 1 : idx & 1;
        const uint32_t g0 = (uint32_t)(tl * GR + j * NT);
        __builtin_amdgcn_sched_barrier(0);
        if (idx >= 4) r.a[j] = __builtin_amdgcn_raw_buffer_load_b64(rsG, t8, g0 * 8u, AUX_NT);
        else if (idx & 1) r.i[j] = __builtin_amdgcn_raw_buffer_load_b128(rsI, t8 + t8, g0 * 16u, AUX_NT);
        else r.o[j] = __builtin_amdgcn_raw_buffer_load_b128(rsO, t8 + t8, g0 * 16u, AUX_NT);
        __builtin_amdgcn_sched_barrier(0);
    };
    auto peek = [&](uint32_t at) __attribute__((always_inline)) -> uint32_t {
        return __hip_atomic_load((unsigned*)((char*)meta + at), __ATOMIC_RELAXED, __HIP_MEMORY_SCOPE_AGENT);
    };
    auto ready = [&]() __attribute__((always_inline)) {
        if (blockIdx.x != 0) {
            const uint32_t x = lds_ld(vconst<P_FLAG + 8>());
            while (peek(vconst<M_BEAT * 4>()) == x) __builtin_amdgcn_s_sleep(8);
        }
    };
    auto beat = [&]() __attribute__((always_inline)) {
        (void)__hip_atomic_fetch_add((unsigned*)((char*)meta + vconst<M_BEAT * 4>()), vconst<1>(), __ATOMIC_RELAXED, __HIP_MEMORY_SCOPE_AGENT);
    };
    auto draw = [&](uint32_t at) __attribute__((always_inline)) -> uint32_t {
        return __hip_atomic_fetch_add((unsigned*)((char*)meta + at), vconst<1>(), __ATOMIC_RELAXED, __HIP_MEMORY_SCOPE_AGENT);
    };
    uint32_t la;
    auto tab_at = [&]() __attribute__((always_inline)) -> uint32_t {
        return P_TAB + bfi(0xffu, bfi(0x3cu, t8 >> 2, t8 << 4), t8);
    };
    u32x2 nul;
    uint32_t U[2];
    {
        const uint32_t t = (uint32_t)tid0;
        la = P_BUF + ((t >> 5) << 12) + ((t & 0x18u) << 3) + ((t & 3u) << 4) + ((t & 4u) << 1);
        const uint32_t q = ((t >> 3) ^ (t >> 1 & 2u)) & 3u;
        nul.x = (0x7u >> q) << 31; nul.y = (0xbu >> q) << 31;
        U[0] = (((t >> 1) & 15u) * SS + ((t & 1u) * 64u + (t >> 5)) * SC + BPL(blockIdx.x)) * 128u + (0x80000000u - SK * 64u) + 8u - g2;
        U[1] = U[0] + 32u * SC * 128u;
    }
    auto log_line = [&](bool lg, uint32_t b) __attribute__((always_inline)) -> uint32_t {
        uint32_t lp = 0u;
        if (lg && (t8 & 0x38u) == 0u) {
            ready();
            lp = draw(vconst<M_LOG * 4>());
            __builtin_amdgcn_raw_buffer_store_b16((uint16_t)b, rsR, gbase + lp * 2u, 0, 0);
        }
        return LBASE + (uint32_t)__builtin_amdgcn_ds_bpermute((int)((t8 >> 1) & 0xe0u), (int)lp);
    };
    auto general = [&](int D, bool all, u64 sm) __attribute__((always_inline)) {
        uint32_t todo = __builtin_amdgcn_readfirstlane((uint32_t)sm | (uint32_t)(sm >> 32));
#pragma unroll 1
        while (todo) {
            const uint32_t S = (uint32_t)__builtin_ctz(todo) >> 1;
            todo &= ~(3u << (2u * S));
            pin(t8); pin(la);
            const uint32_t V = lds_ld(P_TAB + (t8 & ~63u) + S * 4u), g = V & 15u;
            const uint32_t col = S * 128u + ((t8 >> 1) & 0x60u) + (t8 >> 8);
            const bool act = all ? V != 0xffffffffu : (V & 0x80000009u) == 9u;
            const int hr = HREG((int)S, D) - HB;
            uint32_t ln = log_line(act && g > 8u + NTAIL, col);
            if (act) {
                const u32x2 N = *(const LDS u32x2*)(uintptr_t)(la + S * 256u);
                *(LDS u32x2*)(uintptr_t)(la + S * 256u) = nul;
                lds_st(P_TAB + (t8 & ~63u) + S * 4u, vconst<0xffffffffu>());
                if (!(g & 1u)) hset_at(hr, N);
                else {
                    uint32_t a, st;
                    if (g < 8u) { a = (V & ~15u) + ((t8 & 0x38u) << 1); st = 8u; }
                    else {
                        if (g <= 8u + NTAIL) ln = TBASE + ((g - 8u) >> 1) * TPL + col * TST + blockIdx.x;
                        a = ln * 128u + (la & 0x38u); st = 64u;
                    }
                    __builtin_amdgcn_raw_buffer_store_b64(hget_at(hr), rsR, a, 0, AUX_SC1);
                    __builtin_amdgcn_raw_buffer_store_b64(N, rsR, a + st, 0, AUX_SC1);
                }
            }
        }
    };
    unsigned epoch = 0;
    u64 pb2 = 0ull;
    u64 fm = 0ull, sm = 0ull;
    auto look = [&](u32x2 c) __attribute__((always_inline)) {
        uint32_t V[2];
        const bool late = __builtin_amdgcn_ballot_w64(((U[0] | U[1]) & 8u) != 0u) != 0ull;
        epoch++;
        asm volatile("v_mov_b32 %0, -1\n\tv_mov_b32 %1, -1\n\t"
                     "v_cmpx_le_u32 %6, %7, %2\n\tv_mov_b32 %0, %4\n\tv_add_u32 %4, %9, %4\n\tv_subrev_u32 %2, %8, %2\n\ts_mov_b64 exec, -1\n\t"
                     "v_cmpx_le_u32 vcc, %8, %3\n\tv_mov_b32 %1, %5\n\tv_add_u32 %5, %9, %5\n\tv_subrev_u32 %3, %8, %3\n\t"
                     "s_or_b64 %6, %6, exec\n\ts_mov_b64 exec, -1"
                     : "=&v"(V[0]), "=&v"(V[1]), "+v"(c.x), "+v"(c.y), "+v"(U[0]), "+v"(U[1]), "=&s"(fm) : "s"(CHV), "n"(CHV), "s"(UINC) : "vcc", "scc");
        *(LDS u32x2*)(uintptr_t)(P_CNT + t8) = c;
        pin(t8);
        const uint32_t tw = tab_at();
        lds_st(tw, V[0]);
        lds_st(tw + 64u, V[1]);
        if (wv == 1) lds_st(P_CNT + NB * 4 - 256 + (t8 >> 1), vconst<0x40000000u>());
        pb2 = __builtin_amdgcn_ballot_w64((int)(V[0] & V[1]) >= 0);
        sm = 0ull;
        if (__builtin_expect(late, 0)) {
            sm = __builtin_amdgcn_ballot_w64((V[0] & 0x80000009u) == 9u || (V[1] & 0x80000009u) == 9u);
#pragma unroll
            for (int e = 0; e < 2; e++)
                if ((V[e] & 0x8000000fu) == 9u + NTAIL) U[e] -= 2u * UINC;
        }
    };
    u32x4 Va, Vb;

    Raw raw[PD];
    {
        const int tid = tid0;
        asm volatile("" ::: "v88", "v127");
        typedef const __attribute__((address_space(4))) u32x4* cptr;
        u32x4 wd0 = ((cptr)(uintptr_t)wdown)[0], wd1 = ((cptr)(uintptr_t)wdown)[1];
        __builtin_amdgcn_sched_barrier(0);
        if (tid == 128) tk = peek(vconst<M_BEAT * 4>());
        if (blockIdx.x == 0 && wv == 3 && (tid & 63) < M_BEAT / 64) {
            __hip_atomic_store(meta + (tid & 63) * 64, 0u, __ATOMIC_RELAXED, __HIP_MEMORY_SCOPE_AGENT);
            __hip_atomic_store(meta + (tid & 63) * 64 + 1, 0u, __ATOMIC_RELAXED, __HIP_MEMORY_SCOPE_AGENT);
        }
        const __amdgpu_buffer_rsrc_t rsE = __builtin_amdgcn_make_buffer_rsrc((void*)m, 0, E * 8, 0x00020000);
        u32x2 mv[5];
        mv[4] = __builtin_amdgcn_raw_buffer_load_b64(rsE, (t8 & 511u) * (uint32_t)(E >> 6), 0, 0);
#pragma unroll
        for (int q = 0; q < 4; q++)
            mv[q] = __builtin_amdgcn_raw_buffer_load_b64(rsE, t8 & 511u, ((blockIdx.x >> 2) * 256u + (blockIdx.x & 3u) * 16u + (uint32_t)wv + 64u * q) * 512u, AUX_NT);
#pragma unroll
        for (int d = 0; d < MDL; d++) load_one(raw[1], tl0, d);
        asm volatile("" : "+s"(wd0), "+s"(wd1));
        const u32x2 ci = {CINIT, CINIT};
        *(LDS u32x2*)(uintptr_t)(P_CNT + tid * 8) = ci;
        if (tid < 64) lds_st(P_CNT + (NB + tid) * 4, 0x40000000u);
        if (tid < 8 && tid != 2) lds_st(P_FLAG + tid * 4, tid == 0 ? 0xffffffffu : 0u);
        if (tid == 128) lds_st(P_FLAG + 8, tk);
        const uint32_t lane = (uint32_t)tid & 63u;
        int e0[4];
        {
            float w[16];
#pragma unroll
            for (int i = 0; i < 4; i++) {
                w[2 * i] = bf2f(wd0[i] & 0xffffu); w[2 * i + 1] = bf2f(wd0[i] >> 16);
                w[8 + 2 * i] = bf2f(wd1[i] & 0xffffu); w[8 + 2 * i + 1] = bf2f(wd1[i] >> 16);
            }
            auto dot = [&](u32x2 v, int c) __attribute__((always_inline)) -> f32x2 {
                const f32x2 w0 = {w[c], w[c + 1]}, w1 = {w[4 + c], w[5 + c]}, w2 = {w[8 + c], w[9 + c]}, w3 = {w[12 + c], w[13 + c]};
                return w0 * bf2f(v.x & 0xffffu) + w1 * bf2f(v.x >> 16) + w2 * bf2f(v.y & 0xffffu) + w3 * bf2f(v.y >> 16);
            };
#pragma unroll
            for (int cp = 0; cp < 2; cp++) {
                if (cp == 1) {
#pragma unroll
                    for (int d = MDL; d < MDM; d++) load_one(raw[1], tl0, d);
                }
                if (blockIdx.x >= SLOTI / 64) continue;
                const f32x2 sd = dot(mv[4], 2 * cp);
#pragma unroll
                for (int e = 0; e < 2; e++) {
                    const int ex = (int)(wave_max(__float_as_uint(sd[e]) & 0x7fffffffu) >> 23);
                    e0[2 * cp + e] = __builtin_amdgcn_readfirstlane(ex == 0 ? 0 : min(max(ex - 129, -100), 100));
                }
                const f32x2 sc = {__uint_as_float((uint32_t)(127 - e0[2 * cp]) << 23), __uint_as_float((uint32_t)(127 - e0[2 * cp + 1]) << 23)};
                u32x4 o;
                u16x2 mx = {0, 0};
                f32x2 sq = {0.f, 0.f}, sg = {0.f, 0.f};
#pragma unroll
                for (int q = 0; q < 4; q++) {
                    const f32x2 y = dot(mv[q], 2 * cp) * sc;
                    const uint32_t hb = pk_bf16(y.x, y.y);
                    const f32x2 x = {__uint_as_float(hb << 16), __uint_as_float(hb & 0xffff0000u)};
                    o[q] = __builtin_bit_cast(uint32_t, __builtin_amdgcn_cvt_pkrtz(x.x, x.y));
                    mx = __builtin_elementwise_max(mx, __builtin_bit_cast(u16x2, hb & 0x7fff7fffu));
                    sq += x * x;
                    sg += x;
                }
                *(LDS u32x4*)(uintptr_t)(P_BUF + (cp * 64u + lane) * 272u + wv * 16) = o;
#pragma unroll
                for (int e = 0; e < 2; e++) {
                    const u32x4 st = {wave_max(mx[e]), __float_as_uint(wave_sum(sq[e])), __float_as_uint(wave_sum(sg[e])), 0u};
                    if (lane == 0u) *(LDS u32x4*)(uintptr_t)(P_LST + ((2 * cp + e) * 16 + wv) * 16) = st;
                }
            }
        }
        __builtin_amdgcn_sched_barrier(0);
        const uint16_t fq = freq[0];
#pragma unroll
        for (int d = MDM; d < 6; d++) load_one(raw[1], tl0, d);
#pragma unroll
        for (int d = 0; d < 6; d++)
            if (((LPOS >> (4 * d)) & 15) < 4) load_one(raw[0], tl1, d);
        __syncthreads();
        u32x4 mo[2];
#pragma unroll
        for (int cp = 0; cp < 2; cp++) mo[cp] = *(const LDS u32x4*)(uintptr_t)(P_BUF + (cp * 64u + (t8 >> 7)) * 272u + ((t8 + t8) & 0xf0u));
        if (blockIdx.x == 0 && wv == 3) asm volatile("s_waitcnt vmcnt(0)" ::: "memory");
        __syncthreads();
#pragma unroll
        for (int s = 0; s < 16; s++) *(LDS u32x2*)(uintptr_t)(la + s * 256) = nul;
        asm volatile("" : "+v"(raw[1].o[0]), "+v"(raw[1].o[1]), "+v"(raw[1].i[0]), "+v"(raw[1].i[1]), "+v"(raw[1].a[0]), "+v"(raw[1].a[1]));
        asm volatile("s_nop 0\n\tv_readfirstlane_b32 %0, %1" : "=s"(frr) : "v"(bf2f(fq) * 0.15915494309189535f));
        if (blockIdx.x < SLOTI / 64) {
            const __amdgpu_buffer_rsrc_t rsM = __builtin_amdgcn_make_buffer_rsrc((void*)(meta + W_MDC / 4), 0, (int)(128u * SLOTI * 4u), 0x00020000);
#pragma unroll
            for (int cp = 0; cp < 2; cp++) {
                const uint32_t row = cp * 64u + (t8 >> 7), c16 = (t8 + t8) & 0xf0u;
                __builtin_amdgcn_raw_buffer_store_b128(mo[cp], rsM, row * (SLOTI * 4u) + c16 + blockIdx.x * 256u, 0, AUX_SC1);
            }
            if (wv == 0) {
                const u32x4 st = *(const LDS u32x4*)(uintptr_t)(P_LST + t8 * 2u);
                const float sm1 = row_sum(__uint_as_float(st.z));
                const u32x2 r = {row_max(st.x) | (row_max(st.x ? 0xffffu - st.x : 0u) << 16), __float_as_uint(row_sum(__uint_as_float(st.y)))};
                if ((lane & 15u) == 15u) {
                    ((u32x2*)(meta + W_STAT / 4))[(lane >> 4) * NWG + blockIdx.x] = r;
                    meta[W_SUM / 4 + (lane >> 4) * NWG + blockIdx.x] = __float_as_uint(sm1);
                }
                if (blockIdx.x == 0 && lane < 4u) meta[M_E0 + lane] = (uint32_t)(lane == 0u ? e0[0] : lane == 1u ? e0[1] : lane == 2u ? e0[2] : e0[3]);
            }
        }
    }
    pin(la);

    uint32_t w[8], aw[8], xv[8];
    u64 nm[8];
    auto later2 = [&](int k) __attribute__((always_inline)) {
        uint32_t x0, x1;
        asm volatile("v_lshrrev_b32 %0, 4, %2\n\tv_lshrrev_b32 %1, 4, %5\n\tv_bitop3_b32 %0, %2, %0, 12 bitop3:0x72\n\tv_bitop3_b32 %1, %5, %1, 12 bitop3:0x72\n\t"
                     "s_not_b64 exec, %8\n\tds_write_b32 %2, %3 offset:%10\n\tds_or_b32 %0, %4 offset:%10\n\t"
                     "s_not_b64 exec, %9\n\tds_write_b32 %5, %6 offset:%10\n\tds_or_b32 %1, %7 offset:%10\n\ts_mov_b64 exec, -1"
                     : "=&v"(x0), "=&v"(x1)
                     : "v"(aw[k]), "v"(w[k]), "v"(xv[k]), "v"(aw[k + 1]), "v"(w[k + 1]), "v"(xv[k + 1]), "s"(nm[k]), "s"(nm[k + 1]), "n"(P_BUF) : "memory", "scc");
    };
    auto drain = [&]() __attribute__((always_inline)) {
        if (wv == 1) {
            const uint32_t q = (epoch + 1u) & 3u;
            const uint32_t n = __builtin_amdgcn_readfirstlane(lds_ld(vconst<P_FLAG + 16>() + q * 4u));
            if (__builtin_expect(n != 0u, 0)) {
                pin(t8);
                const uint32_t l = (t8 >> 3) & 63u;
                if (l < min(n, (uint32_t)LCAP)) {
                    const u32x4 e = *(const LDS u32x4*)(uintptr_t)(P_LST + (q * LCAP + l) * 16u);
                    lds_st(P_BUF + e.x, e.y);
                    lds_or(P_BUF + slot_x(e.x), e.z);
                }
                lds_st(vconst<P_FLAG + 16>() + q * 4u, vconst<0>());
            }
        }
    };
    auto again = [&](int D) __attribute__((always_inline)) {
        do {
            general(D, true, fm);
            __syncthreads();
            later2(0); later2(2); later2(4); later2(6);
            drain();
#pragma unroll
            for (int k = 0; k < 8; k++) {
                pin(aw[k]);
                bool due = false;
                if (aw[k] & 0x80000000u) {
                    xv[k] -= CHV;
                    if (xv[k] < CHV) {
                        const uint32_t f = (aw[k] >> 16) & 0x3ffu;
                        aw[k] = slot_w(aw[k] & 0xffffu, xv[k]);
                        xv[k] = f << ((xv[k] >> 8) & 31u);
                        due = true;
                    } else lds_st(vconst<P_FLAG>(), epoch + 1u);
                }
                nm[k] = ~__builtin_amdgcn_ballot_w64(due);
            }
            look(*(const LDS u32x2*)(uintptr_t)(P_CNT + t8));
            asm volatile("s_waitcnt lgkmcnt(0)" ::: "memory");
            __syncthreads();
        } while (__builtin_amdgcn_readfirstlane(lds_ld(vconst<P_FLAG>())) == epoch);
    };
    auto round = [&](auto DD, auto LL, Raw& rc, Raw& rn, int rd) __attribute__((always_inline)) {
        constexpr int D = decltype(DD)::value;
        constexpr bool LAST = decltype(LL)::value == 1, FIRST = decltype(LL)::value == 2;
        const int ng = min(GR, G - tl0 * GR);
        auto hook = [&](int p) __attribute__((always_inline)) {
#pragma unroll
            for (int q = 0; q < 6; q++)
            {
                const int lp = (LPOS >> (4 * q)) & 15;
                if (!LAST && lp < 4 && lp == p) load_one(rc, tl2, q);
                if (!LAST && lp >= 4 && (lp < 8 || lp >= 12 ? lp : lp - 8) == p) load_one(rn, tl1, q);
            }
        };
        uint32_t wn[8], ca[8], px[4], cv[8];
        float mx;
        float cs[2], cm[2] = {0.f, 0.f};
        auto convA = [&](int q) __attribute__((always_inline)) {
            if (LAST) return;
            const uint32_t a = rc.a[q >> 1][q & 1];
            const f32x2 av = {__uint_as_float(a << 16), __uint_as_float(a & 0xffff0000u)};
            f32x2 rv;
            asm("v_pk_mul_f32 %0, %1, %2 op_sel_hi:[1,0]" : "=v"(rv) : "v"(av), "s"((u64)__float_as_uint(frr)));
            mx = __builtin_elementwise_maximum(__builtin_elementwise_maximum(__builtin_fabsf(rv.x), __builtin_fabsf(rv.y)), (q & 1) ? mx : 0.f);
            cs[0] = __builtin_amdgcn_cosf(rv.x); cs[1] = __builtin_amdgcn_cosf(rv.y);
            asm volatile("" : "+v"(mx), "+v"(cs[0]), "+v"(cs[1]));
            if (FIRST) {
                const float c0 = fminf(fmaxf(cs[0], -1.f), 1.f), c1 = fminf(fmaxf(cs[1], -1.f), 1.f);
                cm[0] += c0 + c1; cm[1] += c0 * c0 + c1 * c1;
                if (q == 3 && wv == 4) {
                    const float a0 = wave_sum(cm[0]), a1 = wave_sum(cm[1]);
                    if ((t8 & 0x1f0u) == 0u) meta[W_SUM / 4 + (4 + ((t8 >> 3) & 1u)) * NWG + blockIdx.x] = __float_as_uint((t8 & 8u) ? a1 : a0);
                }
            }
            __builtin_amdgcn_sched_barrier(0);
        };
        auto convB = [&](int k) __attribute__((always_inline)) {
            if (LAST) return;
            const uint32_t o = rc.o[k >> 2][k & 3], i = rc.i[k >> 2][k & 3];
            wn[k] = (__builtin_amdgcn_alignbit(bfi(63u, o, o >> 5), __float_as_uint(cs[k & 1]), 30u) << 14) | (i >> 6);
            ca[k] = (bfi(63u, i, o) << 2) & 0x1ffcu;
            pin(wn[k]); pin(ca[k]);
            __builtin_amdgcn_sched_barrier(0);
        };
        auto convD = [&](int q) __attribute__((always_inline)) {
            if (LAST) return;
            asm volatile("v_cvt_pk_f16_f32 %0, |%1|, |%2|\n\tv_pk_mul_f16 %0, %0, %3 op_sel_hi:[1,0]" : "=v"(px[q]) : "v"(cs[0]), "v"(cs[1]), "s"(0x3ffu));
        };
        uint32_t cst;
        auto check = [&](int j) __attribute__((always_inline)) {
            if (LAST) return;
            if (__builtin_expect((__builtin_amdgcn_ballot_w64(!(mx <= RMAXREV)) | (u64)(uint32_t)(GR - ng)) != 0ull, 0)) {
                pin(t8);
                asm volatile("" : "+v"(rc.a[j]));
                const int lg = j * NT + (int)(t8 >> 3);
#pragma unroll
                for (int k = 0; k < 4; k++) {
                    const uint32_t a = (k & 1) ? (rc.a[j][k >> 1] >> 16) : (rc.a[j][k >> 1] & 0xffffu);
                    const bool nrm = __builtin_fabsf(frr * bf2f(a)) <= RMAXREV;
                    if (!(lg < ng && nrm)) ca[j * 4 + k] = NB * 4u + ((t8 >> 1) & 0xfcu);
                    if (lg < ng && !nrm) {
                        ready();
                        __builtin_amdgcn_raw_buffer_store_b32((uint32_t)((tl0 * GR + lg) * 4 + k), rsR, spec0 - draw(vconst<M_LOG * 4 + 4>()) * 4u, 0, 0);
                    }
                }
            }
            cst = vconst<CSTEP>();
        };
        auto ticket = [&](int k) __attribute__((always_inline)) {
            if (LAST) return;
            cv[k] = lds_add(P_CNT + ca[k], cst);
            __builtin_amdgcn_sched_barrier(0);
        };
        auto insert = [&](int j) __attribute__((always_inline)) {
            const int k0 = 4 * j;
            const bool odd = __builtin_amdgcn_ballot_w64(max(max(cv[k0], cv[k0 + 1]), max(cv[k0 + 2], cv[k0 + 3])) >= 2u * CHV) != 0ull;
#pragma unroll
            for (int k = k0; k < k0 + 4; k += 2) {
                uint32_t x0, x1;
                asm volatile("v_lshrrev_b32 %6, 11, %8\n\tv_lshrrev_b32 %7, 11, %9\n\t"
                             "v_bitop3_b32 %6, %6, %10, 60 bitop3:0x28\n\tv_bitop3_b32 %7, %7, %11, 60 bitop3:0x28\n\t"
                             "v_lshl_or_b32 %0, %10, 4, %6\n\tv_lshl_or_b32 %1, %11, 4, %7\n\t"
                             "v_lshlrev_b32_sdwa %2, %8, %12 dst_sel:DWORD dst_unused:UNUSED_PAD src0_sel:BYTE_1 src1_sel:WORD_0\n\t"
                             "v_lshlrev_b32_sdwa %3, %9, %12 dst_sel:DWORD dst_unused:UNUSED_PAD src0_sel:BYTE_1 src1_sel:WORD_1\n\t"
                             "v_bitop3_b32 %6, %0, %10, 12 bitop3:0x72\n\tv_bitop3_b32 %7, %1, %11, 12 bitop3:0x72\n\t"
                             "v_cmpx_gt_u32 %4, %15, %8\n\tds_write_b32 %0, %13 offset:%16\n\tds_or_b32 %6, %2 offset:%16\n\ts_mov_b64 exec, -1\n\t"
                             "v_cmpx_gt_u32 %5, %15, %9\n\tds_write_b32 %1, %14 offset:%16\n\tds_or_b32 %7, %3 offset:%16\n\ts_mov_b64 exec, -1"
                             : "=&v"(aw[k]), "=&v"(aw[k + 1]), "=&v"(xv[k]), "=&v"(xv[k + 1]), "=&s"(nm[k]), "=&s"(nm[k + 1]), "=&v"(x0), "=&v"(x1)
                             : "v"(cv[k]), "v"(cv[k + 1]), "v"(ca[k]), "v"(ca[k + 1]), "v"(px[k >> 1]), "v"(wn[k]), "v"(wn[k + 1]), "s"(CHV), "n"(P_BUF) : "memory");
                w[k] = wn[k]; w[k + 1] = wn[k + 1];
                if (__builtin_expect(odd, 0)) {
#pragma unroll
                    for (int q = k; q < k + 2; q++) {
                        nm[q] |= __builtin_amdgcn_ballot_w64(cv[q] >= 2u * CHV);
                        if (cv[q] >= 2u * CHV && cv[q] < 0x40000000u) {
                            const uint32_t lq = (epoch + 2u) & 3u;
                            uint32_t p = LCAP;
                            if (cv[q] < 3u * CHV) p = lds_add(vconst<P_FLAG + 16>() + lq * 4u, vconst<1>());
                            if (p < LCAP) {
                                const u32x2 e = {aw[q], w[q]};
                                *(LDS u32x2*)(uintptr_t)(P_LST + (lq * LCAP + p) * 16u) = e;
                                lds_st(P_LST + (lq * LCAP + p) * 16u + 8u, xv[q]);
                            } else {
                                aw[q] = ca[q] | ((q & 1) ? (px[q >> 1] & 0xffff0000u) : (px[q >> 1] << 16)) | 0x80000000u;
                                xv[q] = cv[q] - CHV;
                                lds_st(vconst<P_FLAG>(), epoch);
                            }
                        }
                    }
                }
            }
        };
        constexpr int P0 = D ? 0 : 3, P1 = D ? 1 : 2, P2 = D ? 2 : 1, P3 = D ? 3 : 0;
        const uint32_t fl = lds_ld(vconst<P_FLAG>());
        if (!LAST) asm volatile("" : "+v"(rc.o[0]), "+v"(rc.o[1]), "+v"(rc.i[0]), "+v"(rc.i[1]), "+v"(rc.a[0]), "+v"(rc.a[1]));
        if (!LAST && (rd + 4 >= nstat || tl1 >= ntile) && wv == 2) {
            pin(tk);
            if ((t8 & 511u) == 0u) {
                if (tl1 >= ntile) {
                    if (rd + 3 <= nstat) ready();
                    (void)draw(vconst<M_MD * 4>());
                } else if (rd + 4 == nstat) tk = peek(vconst<M_BEAT * 4>());
                else {
                    if (rd + 3 == nstat) {
                        if (tk == lds_ld(vconst<P_FLAG + 8>())) ready();
                        tk = 0u;
                    }
                    lds_st(vconst<P_FLAG + 4>(), tk);
                    if ((int)tk < ntile - nstat * NWG) tk = draw(vconst<M_TILE * 4>());
                }
            }
        }
        if (!LAST && blockIdx.x == 0 && wv == 3 && (t8 & 511u) == 0u) beat();
        hook(12);
        pin(t8);
        const uint32_t tr = P_TAB + (t8 & ~63u), u16 = (t8 + t8) & 0x70u;
        convA(0);
        convB(0);
        const bool ag = __builtin_amdgcn_readfirstlane(fl) == epoch;
        if (!FIRST && __builtin_expect(LAST || ag || sm != 0ull, 0)) {
            if (ag) again(D);
            general(D, false, sm);
            Va = *(const LDS u32x4*)(uintptr_t)(P_TAB + (t8 & ~63u) + P0 * 16);
            Vb = *(const LDS u32x4*)(uintptr_t)(P_TAB + (t8 & ~63u) + P1 * 16);
            asm volatile("" : "+v"(Va), "+v"(Vb));
        }
        uint32_t pq = (uint32_t)pb2 | (uint32_t)(pb2 >> 32);
        pq = (pq | (pq >> 1)) & 0x55555555u;
        pq = __builtin_amdgcn_readfirstlane(pq + (pq & (far * NEARM)));
        uint32_t ad[8];
        if (!FIRST) ad[0] = take<4 * P0 + 0, D>(Va[0], u16, la, nul);
        convB(1);
        if (!FIRST) ad[1] = take<4 * P0 + 1, D>(Va[1], u16, la, nul);
        convD(0);
        convA(1);
        if (!FIRST) ad[2] = take<4 * P0 + 2, D>(Va[2], u16, la, nul);
        convB(2);
        hook(13);
        if (!FIRST) ad[3] = take<4 * P0 + 3, D>(Va[3], u16, la, nul);
        convB(3);
        if (!FIRST) put<4 * P0 + 0, LAST ? 3 : 3, LAST>(rsA, ad[0], pq);
        if (!FIRST) ad[4] = take<4 * P1 + 0, D>(Vb[0], u16, la, nul);
        convD(1);
        check(0);
        ticket(0);
        hook(4);
        if (!FIRST) put<4 * P0 + 1, LAST ? 3 : 4, LAST>(rsA, ad[1], pq);
        if (!FIRST) ad[5] = take<4 * P1 + 1, D>(Vb[1], u16, la, nul);
        convA(2);
        ticket(1);
        if (!FIRST) put<4 * P0 + 2, LAST ? 3 : 5, LAST>(rsA, ad[2], pq);
        if (!FIRST) ad[6] = take<4 * P1 + 2, D>(Vb[2], u16, la, nul);
        convB(4);
        ticket(2);
        if (!FIRST) put<4 * P0 + 3, LAST ? 3 : 6, LAST>(rsA, ad[3], pq);
        if (!FIRST) Va = *(const LDS u32x4*)(uintptr_t)(tr + P2 * 16);
        __builtin_amdgcn_sched_barrier(0);
        if (!FIRST) ad[7] = take<4 * P1 + 3, D>(Vb[3], u16, la, nul);
        convB(5);
        ticket(3);
        SETP(2);
        if (!FIRST) put<4 * P1 + 0, LAST ? 4 : 8, LAST>(rsA, ad[4], pq);
        if (!FIRST) ad[0] = take<4 * P2 + 0, D>(Va[0], u16, la, nul);
        convD(2);
        convA(3);
        hook(5);
        if (!FIRST) put<4 * P1 + 1, LAST ? 4 : 7, LAST>(rsA, ad[5], pq);
        if (!FIRST) ad[1] = take<4 * P2 + 1, D>(Va[1], u16, la, nul);
        convB(6);
        if (!FIRST) put<4 * P1 + 2, LAST ? 4 : 6, LAST>(rsA, ad[6], pq);
        if (!FIRST) ad[2] = take<4 * P2 + 2, D>(Va[2], u16, la, nul);
        convB(7);
        if (!FIRST) put<4 * P1 + 3, LAST ? 3 : 4, LAST>(rsA, ad[7], pq);
        if (!FIRST) Vb = *(const LDS u32x4*)(uintptr_t)(tr + P3 * 16);
        __builtin_amdgcn_sched_barrier(0);
        if (!FIRST) ad[3] = take<4 * P2 + 3, D>(Va[3], u16, la, nul);
        convD(3);
        check(1);
        ticket(4);
        SETP(1);
        hook(6);
        if (!FIRST) put<4 * P2 + 0, LAST ? 4 : 5, LAST>(rsA, ad[0], pq);
        if (!FIRST) ad[4] = take<4 * P3 + 0, D>(Vb[0], u16, la, nul);
        ticket(5);
        if (!FIRST) put<4 * P2 + 1, LAST ? 4 : 6, LAST>(rsA, ad[1], pq);
        if (!FIRST) ad[5] = take<4 * P3 + 1, D>(Vb[1], u16, la, nul);
        ticket(6);
        if (!FIRST) put<4 * P2 + 2, LAST ? 4 : 7, LAST>(rsA, ad[2], pq);
        if (!FIRST) ad[6] = take<4 * P3 + 2, D>(Vb[2], u16, la, nul);
        ticket(7);
        SETP(0);
        if (!FIRST) put<4 * P2 + 3, LAST ? 3 : 7, LAST>(rsA, ad[3], pq);
        if (!FIRST) ad[7] = take<4 * P3 + 3, D>(Vb[3], u16, la, nul);
        hook(7);
        SETP(3);
        asm volatile("s_waitcnt lgkmcnt(0)" ::: "memory");
        __syncthreads();
        u32x2 cn;
        uint32_t tv;
        if (!LAST) { cn = *(const LDS u32x2*)(uintptr_t)(P_CNT + t8); tv = lds_ld(vconst<P_FLAG + 4>()); }
        __builtin_amdgcn_sched_barrier(0);
        if (!FIRST) later2(0);
        if (!FIRST) put<4 * P3, 15, LAST>(rsA, ad[4], pq);
        if (!FIRST) later2(2);
        if (!FIRST) put<4 * P3 + 1, 15, LAST>(rsA, ad[5], pq);
        if (!FIRST) drain();
        if (!LAST) {
            tl2 = rd + 2 < nstat ? fixed_tile(rd + 2) : nstat * NWG + (int)__builtin_amdgcn_readfirstlane(tv);
            look(cn);
        }
        hook(0);
        SETP(2);
        if (!FIRST) later2(4);
        if (!FIRST) put<4 * P3 + 2, 15, LAST>(rsA, ad[6], pq);
        if (!FIRST) later2(6);
        if (!FIRST) put<4 * P3 + 3, 15, LAST>(rsA, ad[7], pq);
        if (!LAST) {
            hook(1);
            SETP(1);
            insert(0);
            hook(2);
            SETP(0);
            pin(t8);
            Va = *(const LDS u32x4*)(uintptr_t)(P_TAB + (t8 & ~63u) + P3 * 16);
            Vb = *(const LDS u32x4*)(uintptr_t)(P_TAB + (t8 & ~63u) + P2 * 16);
            __builtin_amdgcn_sched_barrier(0);
            insert(1);
            hook(3);
            SETP(3);
            asm volatile("s_waitcnt lgkmcnt(0)" ::: "memory");
            __syncthreads();
            tl0 = tl1; tl1 = tl2;
        }
    };
#define HMV(A, B) "v_mov_b64 v[" #A ":" #A "+1], v[" #B ":" #B "+1]\n\t"
#define HMV4(A) HMV(A + 8, A + 6) HMV(A + 6, A + 4) HMV(A + 4, A + 2) HMV(A + 2, A)
    round(IC<1>(), IC<2>(), raw[1], raw[0], 0);
    int rd = 1;
#pragma unroll 1
    for (; tl1 < ntile; rd += 2) {
        round(IC<0>(), IC<0>(), raw[0], raw[1], rd);
        round(IC<1>(), IC<0>(), raw[1], raw[0], rd + 1);
    }
    if (tl0 >= ntile) asm volatile(HMV4(88) HMV4(98) HMV4(108) HMV4(118));
    asm volatile("" : "+s"(tl0));
    if (tl0 < ntile) round(IC<0>(), IC<0>(), raw[0], raw[1], rd);
    round(IC<1>(), IC<1>(), raw[1], raw[0], 0);
    if (__builtin_amdgcn_readfirstlane(lds_ld(P_FLAG + 16 + ((epoch + 2u) & 3u) * 4u)) != 0u) {
        asm volatile("s_waitcnt lgkmcnt(0)" ::: "memory");
        __syncthreads();
        look(*(const LDS u32x2*)(uintptr_t)(P_CNT + t8));
        general(0, true, fm);
        __syncthreads();
        drain();
    }
    {
        uint32_t* tcnt = meta + W_TCNT / 4;
        const u32x2 c = *(const LDS u32x2*)(uintptr_t)(P_CNT + t8);
        const uint32_t tw = tab_at();
#pragma unroll
        for (int e = 0; e < 2; e++) {
            const uint32_t g = U[e] & 15u;
            lds_st(tw + e * 64u, g >= 8u ? 0x80000000u : ((U[e] + ((g & 1u) ? 0u : SK * 64u)) & 0x7fffff81u) | (((7u - g) << 1) & 12u));
            *(LDS uint8_t*)(uintptr_t)(P_CNT + (t8 & ~511u) + (((tw - P_TAB) >> 2) & 127u) + e * 16u) =
                (uint8_t)(g < 8u ? 0u : ((g - 8u) >> 1) + (((c[e] + 2u * CSTEP) >> 15) << 2) + (g & 1u) * 0x50u);
        }
        asm volatile("s_waitcnt lgkmcnt(0)" ::: "memory");
        __syncthreads();
        const uint32_t u16 = (t8 + t8) & 0x70u;
        u32x4 En[4];
#pragma unroll
        for (int p = 0; p < 4; p++) En[p] = *(const LDS u32x4*)(uintptr_t)(P_TAB + (t8 & ~63u) + p * 16u);
        const u32x4 cb = *(const LDS u32x4*)(uintptr_t)(P_CNT + (t8 & ~511u) + ((t8 >> 2) & 0x70u));
        u32x2 N[16];
#pragma unroll
        for (int s = 0; s < 16; s++) N[s] = *(const LDS u32x2*)(uintptr_t)(la + s * 256u);
        if (u16 == 0u) *(u32x4*)(tcnt + blockIdx.x * 512u + (t8 >> 4)) = cb;
        const uint32_t sg = P_BUF + ((t8 & ~63u) << 4);
        uint32_t ps;
        auto rest = [&](auto SL) __attribute__((always_inline)) {
            constexpr int s = decltype(SL)::value;
            const uint32_t en = En[s >> 2][s & 3], by = cb[s >> 2] >> ((s & 3) * 8);
            const u32x2 Hr = hget<HREG(s, 0)>();
            const uint32_t at = ps + ((la & 0x38u) ^ ((s & 3) << 4));
            *(LDS u32x2*)(uintptr_t)at = Hr;
            *(LDS u32x2*)(uintptr_t)(at + (by & 0x40u)) = N[s];
            ps += (by & 0x3cu) << 2;
            const u32x2 Hs = (en & 1u) ? Hr : nul;
            const u32x4 d = {Hs.x, Hs.y, N[s].x, N[s].y};
            uint32_t a = __builtin_amdgcn_bitop3_b32(en, u16, 15u, 0x54);
            {
                const u32x4 rs_ = rsA;
                const uint32_t far_ = ((NEARM >> (2 * s)) & 1u) ? far : 0u;
                asm volatile("s_cmp_eq_u32 %3, 0\n\ts_cbranch_scc0 1f\n\tbuffer_store_dwordx4 %0, %1, %2, 0 offen sc1\n\ts_branch 2f\n"
                             "1:\n\tbuffer_store_dwordx4 %0, %1, %2, 0 offen sc1 nt\n2:\n\ts_nop 1" :: "v"(d), "v"(a), "s"(rs_), "s"(far_) : "memory", "scc");
            }
            if (__builtin_expect(__builtin_amdgcn_ballot_w64((en & 12u) != 0u) != 0ull, 0)) {
                const u32x4 dn = {nul.x, nul.y, nul.x, nul.y};
#pragma unroll 1
                for (uint32_t k = (en >> 2) & 3u; k > 0u; k--) {
                    a += SK * 128u;
                    __builtin_amdgcn_raw_buffer_store_b128(dn, rsR, a, 0, AUX_SC1);
                }
            }
        };
        auto pack = [&](int p) __attribute__((always_inline)) {
            const uint32_t a = (SBASE + ((((t8 >> 1) & 0x60u) + (t8 >> 8)) * 2u + p) * (NWG * 9u) + blockIdx.x * 9u) * 128u + u16;
            asm volatile("" ::: "memory");
#pragma unroll 1
            for (uint32_t q = 0; __builtin_amdgcn_ballot_w64(sg + q < ps) != 0ull; q += 256u) {
                const u32x4 d0 = *(const LDS u32x4*)(uintptr_t)(sg + q + u16), d1 = *(const LDS u32x4*)(uintptr_t)(sg + q + 128u + u16);
                __builtin_amdgcn_raw_buffer_store_b128(d0, rsR, sg + q < ps ? a + q : 0xffffff00u, 0, AUX_SC1);
                __builtin_amdgcn_raw_buffer_store_b128(d1, rsR, sg + q + 128u < ps ? a + q + 128u : 0xffffff00u, 0, AUX_SC1);
            }
            asm volatile("" ::: "memory");
        };
        asm volatile("" : "+v"(N[0]), "+v"(N[1]), "+v"(N[2]), "+v"(N[3]), "+v"(N[4]), "+v"(N[5]), "+v"(N[6]), "+v"(N[7]) :: "memory");
        asm volatile("" : "+v"(N[8]), "+v"(N[9]), "+v"(N[10]), "+v"(N[11]), "+v"(N[12]), "+v"(N[13]), "+v"(N[14]), "+v"(N[15]) :: "memory");
        ps = sg;
        rest(IC<0>()); rest(IC<1>()); rest(IC<2>()); rest(IC<3>()); rest(IC<4>()); rest(IC<5>()); rest(IC<6>()); rest(IC<7>());
        pack(0);
        ps = sg;
        rest(IC<8>()); rest(IC<9>()); rest(IC<10>()); rest(IC<11>()); rest(IC<12>()); rest(IC<13>()); rest(IC<14>()); rest(IC<15>());
        pack(1);
    }
    if (blockIdx.x == 0 && t8 == 1536u) {
        do {
            beat();
            __builtin_amdgcn_s_sleep(8);
        } while (peek(vconst<M_MD * 4>()) < NWG);
    }
}

#define A_MD 0
#define A_ACC (SLOTI * 4)
#define A_FLAG (A_ACC + SLOTO * 4)
#define A_LDS (A_FLAG + 512)
static_assert(SLOTI * 4 == 3 * NT * 16 + 1024, "md staging assumes three full vectors and 256 dwords");
#define BIAS 0x40004000u
#define NFAST 8
#define NLD 10
#define MDF 0x0a11333
#define MDP 0xaaaaa
#define HP 2
#define SHARE (SLOTO / 8)
#define SPINCAP 4096u
#define ZSIG 9.0f
#define HSLOW 0x80008000u
#define GAS __attribute__((address_space(1)))

__global__ __launch_bounds__(NT, 4) __attribute__((amdgpu_num_vgpr(53))) void acc_kernel(char* ws, int T, int E, int ks, int lcap, float zf, int pad,
                                                 const uint16_t* __restrict__ centers, const uint16_t* __restrict__ wrbf,
                                                 const uint16_t* __restrict__ wsbf, const uint16_t* __restrict__ wup,
                                                 const u32x2* __restrict__ m, const uint16_t* __restrict__ dist, u32x2* __restrict__ out,
                                                 const int* __restrict__ iin, const int* __restrict__ iout, const uint16_t* __restrict__ ang,
                                                 const uint16_t* __restrict__ wdown, const uint16_t* __restrict__ freq) {
    const char* recs = ws + W_RECS;
    const u32x4* tcnt = (const u32x4*)(ws + W_TCNT);
    const uint32_t* mdc = (const uint32_t*)(ws + W_MDC);
    const u32x4* stat = (const u32x4*)(ws + W_STAT);
    uint32_t* partial = (uint32_t*)(ws + W_PART);
    unsigned* meta = (unsigned*)ws;
    extern __shared__ __attribute__((aligned(16))) char smem[];
    unsigned* acc = (unsigned*)(smem + A_ACC);
    unsigned* s_flag = (unsigned*)(smem + A_FLAG);
    const int tid = threadIdx.x;
    const int lane = tid & 63;
    const int wv = __builtin_amdgcn_readfirstlane(tid >> 6);
    const int cp = (blockIdx.x >> 3) & 1;
    const int h = blockIdx.x & 3;
    const int j = ((blockIdx.x >> 2) & 1) * 16 + (blockIdx.x >> 4);
    u32x4 tc;
    const __amdgpu_buffer_rsrc_t rsM = __builtin_amdgcn_make_buffer_rsrc((void*)mdc, 0, (int)(128u * SLOTI * 4u), 0x00020000);

    u32x4 pm[4];
    u32x4 ring[NLD];
    auto load_md = [&](int ff, int pl, int mdp) __attribute__((always_inline)) {
        const uint32_t so = (uint32_t)(cp * 64 + ff * 4 + h) * (SLOTI * 4u);
#pragma unroll
        for (int v = 0; v < 3; v++)
            if (pl < 0 || pl == ((mdp >> (4 * v)) & 15)) pm[v] = __builtin_amdgcn_raw_buffer_load_b128(rsM, (uint32_t)tid * 16u, so + (uint32_t)v * (NT * 16u), 0);
        if ((pl < 0 || pl == (mdp & 15)) && wv == 0) pm[3] = __builtin_amdgcn_raw_buffer_load_b128(rsM, (uint32_t)tid * 16u, so + 3u * NT * 16u, 0);
        __builtin_amdgcn_sched_barrier(0);
    };
    const uint32_t lf = (uint32_t)(tid >> 9) * (SKH * 128u) + (uint32_t)(tid & 0x1e0) * 16u + (uint32_t)(tid & 12) * 32u + (uint32_t)(tid & 16) * 4u + (uint32_t)(tid & 3) * 16u;
    const uint32_t lp = (uint32_t)(tid >> 8) * (SKM * 128u) + (uint32_t)((tid >> 2) & 63) * 128u + (uint32_t)(tid & 3) * 16u;
    auto load_main = [&](int i, int ff, bool near = false) __attribute__((always_inline)) -> u32x4 {
        const uint32_t q = (uint32_t)(((i >> 1) - HP) & 3), SK = q ? SKM : SKH, SS = q ? SSM : SSH;
        const __amdgpu_buffer_rsrc_t rs = __builtin_amdgcn_make_buffer_rsrc((void*)(recs + (q * SQ + (uint32_t)ff * SS + (uint32_t)(h * 32 + j) * SC) * 128u), 0, ks * (int)(SK * 128u), 0x00020000);
        if (q == 0u && near) return __builtin_amdgcn_raw_buffer_load_b128(rs, lf, (i & 1) * (int)(2u * SK * 128u), 0);
        if (q == 0u) return __builtin_amdgcn_raw_buffer_load_b128(rs, lf, (i & 1) * (int)(2u * SK * 128u), AUX_NT);
        return __builtin_amdgcn_raw_buffer_load_b128(rs, lp + (uint32_t)(i & 1) * 64u, 0, 0);
    };
    const __amdgpu_buffer_rsrc_t rsT = __builtin_amdgcn_make_buffer_rsrc((void*)recs, 0, (int)(LBASE * 128u), 0x00020000);
    const uint32_t tof = (uint32_t)(lane & 2) * (TPL * 64u) + (uint32_t)(tid >> 2) * 128u + (uint32_t)(lane & 1) * 64u + 4u + (TBASE + (uint32_t)(h * 32 + j) * TST) * 128u;
    const uint32_t sp0 = (SBASE + (uint32_t)(h * 32 + j) * (2u * NWG * 9u) + (uint32_t)(tid >> 2) * 9u) * 128u;
    auto tail_rank = [&](uint32_t c, uint32_t sp, int ff, uint32_t t0, int& dst, uint32_t& en) __attribute__((always_inline)) -> int {
        const int k4 = (int)(((uint32_t)(lane & 3) + t0) * 4u) - (int)((c & 3u) * 8u);
        const int left = (int)((c >> 2) & 15u) - k4;
        const bool has = left > 0;
        uint32_t ea = tof + (uint32_t)ff * (128u * TST * 128u), eb = sp + (uint32_t)(k4 * 16 + min(max(left, 0), 4));
        asm volatile("" : "+v"(ea), "+v"(eb));
        en = k4 < 0 ? ea : eb;
        const u64 mask = __builtin_amdgcn_ballot_w64(has);
        const int n = __builtin_popcountll(mask);
        const int rank = __builtin_amdgcn_mbcnt_hi((uint32_t)(mask >> 32), __builtin_amdgcn_mbcnt_lo((uint32_t)mask, 0u));
        const int r = has ? rank : (n + lane - rank);
        dst = ((r & 15) << 4) | ((r >> 4) << 2);
        return n;
    };
    auto load_tail = [&](uint32_t en, u64& tm) __attribute__((always_inline)) -> u32x4 {
        const bool on = (uint32_t)(lane & 3) < (en & 7u);
        tm = __builtin_amdgcn_ballot_w64(on);
        return __builtin_amdgcn_raw_buffer_load_b128(rsT, on ? (en & ~15u) + (uint32_t)(lane & 3) * 16u : 0xffffff00u, 0, 0);
    };
    f32x2 fa[4][4];
    u32x4 em[2];
    u32x2 dw2;
    uint32_t wl;
    const __amdgpu_buffer_rsrc_t rsE = __builtin_amdgcn_make_buffer_rsrc((void*)m, 0, E * 8, 0x00020000);
    const __amdgpu_buffer_rsrc_t rsD = __builtin_amdgcn_make_buffer_rsrc((void*)dist, 0, E * 2, 0x00020000);
    auto acc_vec = [&](int i) __attribute__((always_inline)) -> u32x4 {
        const uint32_t a = A_ACC + (uint32_t)(i >> 4) * 256u + (uint32_t)(i & 15) * 8u;
        const u32x2 x = *(const LDS u32x2*)(uintptr_t)a, y = *(const LDS u32x2*)(uintptr_t)(a + 128u);
        const u32x4 v = {x.x, x.y, y.x, y.y};
        return v;
    };
    auto edge0 = [&](int sh) __attribute__((always_inline)) -> uint32_t {
        return (tid < SHARE / 4) ? (uint32_t)(((sh * (SHARE / 64) + (tid >> 4)) << 11) | (j << 6) | ((tid & 15) * 2)) : 0x0fffff00u;
    };
    auto load_m = [&](int sh) __attribute__((always_inline)) {
        em[0] = __builtin_amdgcn_raw_buffer_load_b128(rsE, edge0(sh) * 8u, 0, AUX_NT);
        em[1] = __builtin_amdgcn_raw_buffer_load_b128(rsE, edge0(sh) * 8u + 256u, 0, AUX_NT);
    };
    auto load_d = [&](int sh) __attribute__((always_inline)) {
        dw2.x = __builtin_amdgcn_raw_buffer_load_b32(rsD, edge0(sh) * 2u, 0, 0);
        dw2.y = __builtin_amdgcn_raw_buffer_load_b32(rsD, edge0(sh) * 2u + 64u, 0, 0);
    };
    auto load_wl = [&]() __attribute__((always_inline)) {
        const u64 p0 = (u64)wsbf, p1 = (u64)wrbf, p2 = (u64)centers, p3 = (u64)wup;
        const u64 pa = lane < 16 ? p0 : p1, pb = lane < 40 ? p2 : p3;
        wl = ((const GAS uint32_t*)(lane < 32 ? pa : pb))[lane & (lane < 32 ? 15 : lane < 40 ? 3 : 7)];
    };
    auto factor = [&](int r, bool first) __attribute__((always_inline)) {
        const float sq = 3.7982826f;
        const f32x2 d01 = {bf2f(dw2.x & 0xffffu), bf2f(dw2.x >> 16)}, d23 = {bf2f(dw2.y & 0xffffu), bf2f(dw2.y >> 16)};
        const uint32_t cw = __builtin_amdgcn_readlane(wl, 32 + (r >> 1));
        const float cn = bf2f((r & 1) ? (cw >> 16) : (cw & 0xffffu));
        f32x2 w[4];
#pragma unroll
        for (int k = 0; k < 4; k++) {
            const uint32_t x = __builtin_amdgcn_readlane(wl, (k >> 1) * 16 + r * 2 + (k & 1));
            w[k].x = bf2f(x & 0xffffu); w[k].y = bf2f(x >> 16);
        }
        const f32x2 y01 = (d01 - cn) * sq, y23 = (d23 - cn) * sq;
        const f32x2 t01 = -y01 * y01, t23 = -y23 * y23;
        const float rb[4] = {__builtin_amdgcn_exp2f(t01.x), __builtin_amdgcn_exp2f(t01.y), __builtin_amdgcn_exp2f(t23.x), __builtin_amdgcn_exp2f(t23.y)};
#pragma unroll
        for (int q = 0; q < 4; q++) {
#pragma unroll
            for (int k = 0; k < 4; k++) fa[q][k] = first ? w[k] * rb[q] : __builtin_elementwise_fma(w[k], (f32x2)(rb[q]), fa[q][k]);
        }
    };
    auto edges = [&](int sh) __attribute__((always_inline)) {
        load_d(sh);
        load_m(sh);
        factor(0, true);
#pragma unroll 1
        for (int r = 1; r < 8; r++) factor(r, false);
    };
#define ADT(V, O) "ds_write_addtid_b32 " V " offset:" #O "\n\t"
    auto store_md = [&]() __attribute__((always_inline)) {
        asm volatile("s_mov_b32 m0, %12\n\ts_nop 0\n\t" ADT("%0", 0) ADT("%1", 256) ADT("%2", 512) ADT("%3", 768) ADT("%4", 16384) ADT("%5", 16640) ADT("%6", 16896) ADT("%7", 17152)
                     ADT("%8", 32768) ADT("%9", 33024) ADT("%10", 33280) ADT("%11", 33536)
                     :: "v"(pm[0][0]), "v"(pm[0][1]), "v"(pm[0][2]), "v"(pm[0][3]), "v"(pm[1][0]), "v"(pm[1][1]), "v"(pm[1][2]), "v"(pm[1][3]),
                        "v"(pm[2][0]), "v"(pm[2][1]), "v"(pm[2][2]), "v"(pm[2][3]), "s"(wv * 1024) : "memory");
        if (wv == 0) asm volatile("s_mov_b32 m0, 0\n\ts_nop 0\n\t" ADT("%0", 49152) ADT("%1", 49408) ADT("%2", 49664) ADT("%3", 49920)
                                  :: "v"(pm[3][0]), "v"(pm[3][1]), "v"(pm[3][2]), "v"(pm[3][3]) : "memory");
    };
    const int me = h * 2 + cp;
    GAS unsigned* dw = (GAS unsigned*)(meta + M_DONE + j * 64);
    const bool fin = wv < (SHARE / 4 + 63) / 64;
    bool fast, more;
    uint32_t need, mg0, mg1;
    uint32_t guard = 0;
    uint32_t sgn = 0x8000u;
    pin(sgn);

#define GAT3(M) { uint32_t a0_, a1_, a2_; asm volatile( \
        "v_lshlrev_b16 %0, 2, %3\n\tv_lshlrev_b16 %1, 2, %4\n\tv_lshlrev_b16 %2, 2, %5\n\t" \
        "ds_read_b32 v[%6], %0\n\tds_read_b32 v[%6+1], %1\n\tds_read_b32 v[%6+2], %2" \
        : "=&v"(a0_), "=&v"(a1_), "=&v"(a2_) : "v"(w0), "v"(w1), "v"(w2), "n"(M) : "memory"); }
#define MIXL " op_sel_hi:[1,1,0]\n\t"
#define MIXH " op_sel:[0,1,0] op_sel_hi:[1,1,0]\n\t"
#define SDW " dst_sel:DWORD dst_unused:UNUSED_PAD src0_sel:DWORD src1_sel:WORD_1\n\t"
#define FOLD "v_bitop3_b32 %6, %6, v[%15-9], v[%15-8] bitop3:0xfe\n\tv_or_b32 %6, v[%15-7], %6\n\t"
#define SCAT(PRE, POST, M) { uint32_t c0_, c1_, c2_, a0_, a1_, a2_; asm volatile( \
        FOLD "v_and_b32 %0, 0x3ff, %7\n\tv_lshrrev_b32 %2, 22, %7\n\t" PRE \
        "v_bitop3_b32 %0, %11, %8, %0 bitop3:0xea\n\tv_bitop3_b32 %2, %11, %10, %2 bitop3:0xea\n\tv_bfe_u32 %1, %7, 11, 10\n\t" \
        "v_fma_mix_f32 %3, %0, v[%15], %12" MIXL "v_bitop3_b32 %1, %11, %9, %1 bitop3:0xea\n\tv_fma_mix_f32 %0, %0, v[%15], %13" MIXH \
        "v_fma_mix_f32 %4, %1, v[%15+1], %12" MIXL "v_lshl_add_u32 %0, %0, 16, %3\n\tv_lshlrev_b32_sdwa %3, 2, %8" SDW \
        "v_fma_mix_f32 %1, %1, v[%15+1], %13" MIXH "ds_add_rtn_u32 v[%15-9], %3, %0 offset:%14\n\t" \
        "v_fma_mix_f32 %5, %2, v[%15+2], %12" MIXL "v_lshl_add_u32 %1, %1, 16, %4\n\tv_lshlrev_b32_sdwa %4, 2, %9" SDW \
        "v_fma_mix_f32 %2, %2, v[%15+2], %13" MIXH "ds_add_rtn_u32 v[%15-8], %4, %1 offset:%14\n\t" \
        "v_lshl_add_u32 %2, %2, 16, %5\n\tv_lshlrev_b32_sdwa %5, 2, %10" SDW \
        "ds_add_rtn_u32 v[%15-7], %5, %2 offset:%14" POST \
        : "=&v"(c0_), "=&v"(c1_), "=&v"(c2_), "=&v"(a0_), "=&v"(a1_), "=&v"(a2_), "+v"(guard) \
        : "v"(xw), "v"(w0), "v"(w1), "v"(w2), "v"(sgn), "s"(mg0), "s"(mg1), "n"(A_ACC), "n"(M), "s"(tm) : "memory"); }
    asm volatile("v_mov_b32 v110, 0\n\tv_mov_b32 v111, 0\n\tv_mov_b32 v112, 0\n\tv_mov_b32 v113, 0\n\tv_mov_b32 v114, 0\n\tv_mov_b32 v115, 0\n\t"
                 "v_mov_b32 v116, 0\n\tv_mov_b32 v117, 0\n\tv_mov_b32 v118, 0" ::: "v106", "v127");
    auto unmix = [&](int i) __attribute__((always_inline)) {
        const auto s0 = __builtin_amdgcn_permlane16_swap(ring[i][0], ring[i][2], false, false);
        const auto s1 = __builtin_amdgcn_permlane16_swap(ring[i][1], ring[i][3], false, false);
        ring[i][0] = s0[0]; ring[i][2] = s0[1]; ring[i][1] = s1[0]; ring[i][3] = s1[1];
    };
    auto word = [&](int i, int k) __attribute__((always_inline)) -> uint32_t {
        return i < NFAST && (i >> 1) != HP ? ring[(i & ~1) | (k >> 1)][(i & 1) * 2 + (k & 1)] : ring[i][k];
    };
    auto gather = [&](int R, int i, int p) __attribute__((always_inline)) {
        if ((i >> 1) == HP) unmix(i);
        const uint32_t w0 = word(i, 0 ^ R), w1 = word(i, 1 ^ R), w2 = word(i, 2 ^ R);
        if (p == 0) GAT3(119) else if (p == 1) GAT3(122) else GAT3(125)
    };
    auto scatter = [&](int R, int i, int p, u64 tm) __attribute__((always_inline)) {
        const uint32_t xw = word(i, 3 ^ R), w0 = word(i, 0 ^ R), w1 = word(i, 1 ^ R), w2 = word(i, 2 ^ R);
        if (i < NFAST) { if (p == 0) SCAT("", "", 119) else if (p == 1) SCAT("", "", 122) else SCAT("", "", 125) }
        else if (p == 0) SCAT("s_mov_b64 exec, %16\n\t", "\n\ts_mov_b64 exec, -1", 119)
        else SCAT("s_mov_b64 exec, %16\n\t", "\n\ts_mov_b64 exec, -1", 125)
    };
#define WAIT_L(N) asm volatile("s_waitcnt lgkmcnt(" #N ")" ::: "memory")
    int ntn;
    u64 tm0, tm1;
    uint32_t ta[2], sp = sp0;
    auto list1 = [&](int ff) __attribute__((always_inline)) {
        int dst;
        uint32_t en;
        ntn = tail_rank(tc.x, sp, ff, 0u, dst, en);
        asm volatile("ds_permute_b32 v106, %0, %1" :: "v"(dst), "v"(en) : "memory");
        if (__builtin_expect(__builtin_amdgcn_ballot_w64((tc.x & 2u) != 0u) != 0ull, 0)) {
            const int n2 = tail_rank(tc.x, sp, ff, 4u, dst, en);
            if (ntn + n2 <= 32) {
                const int r = ((dst >> 4) | ((dst & 12) << 2)) + ntn;
                const int x = __builtin_amdgcn_ds_permute((en & 7u) ? ((r & 15) << 4) | ((r >> 4) << 2) : 252, (en & 7u) ? (int)en : 0);
                asm volatile("v_cmpx_ne_u32 vcc, 0, %0\n\tv_mov_b32 v106, %0\n\ts_mov_b64 exec, -1\n\ts_nop 4" :: "v"(x) : "vcc");
                ntn += n2;
            } else ntn = 64;
        }
    };
    auto step = [&](auto RR, auto KK, int ff) __attribute__((always_inline)) {
        constexpr int R = decltype(RR)::value;
        constexpr bool first = decltype(KK)::value == 1, last = decltype(KK)::value == 2;
        constexpr int mdp = first ? MDF : MDP;
        gather(R, 0, 0);
        const int ntl = ntn;
        asm volatile("v_mov_b32_dpp %0, v106 quad_perm:[0,0,0,0] row_mask:0xf bank_mask:0xf\n\tv_mov_b32_dpp %1, v106 quad_perm:[1,1,1,1] row_mask:0xf bank_mask:0xf" : "=v"(ta[0]), "=v"(ta[1]));
        auto side = [&](int pl) __attribute__((always_inline)) {
            if (first && pl == ((mdp >> 20) & 15)) { ring[4] = load_main(4, ff, !(NEARM & 1u)); ring[5] = load_main(5, ff, !(NEARM & 1u)); }
            if (first && pl == ((mdp >> 24) & 15)) { ring[6] = load_main(6, ff); ring[7] = load_main(7, ff); }
            if (pl == ((mdp >> 12) & 15)) ring[NFAST] = load_tail(ta[0], tm0);
            if (pl == ((mdp >> 16) & 15)) ring[NFAST + 1] = load_tail(ta[1], tm1);
            __builtin_amdgcn_sched_barrier(0);
            if (!last) load_md(ff + 1, pl, mdp);
            if (!first && !last && pl == 5 && ff == 6) {
                load_d(me);
                load_wl();
                if (tid == NT - 128) (void)__hip_atomic_fetch_add((GAS unsigned*)((GAS char*)dw + vconst<0>()), 0x1000000u << me, __ATOMIC_RELAXED, __HIP_MEMORY_SCOPE_AGENT);
            }
            if (last && pl == 3) load_m(me);
            if (last && pl == 4 && wv == 15) { GAS unsigned* d_ = dw; asm volatile("s_nop 4\n\tglobal_load_dword v108, %0, %1 sc1" :: "v"(vconst<0>()), "s"(d_) : "memory"); }
            if (last && pl < 7 && fin) factor(pl, pl == 0);
            __builtin_amdgcn_sched_barrier(0);
        };
        side(8);
        gather(R, 1, 1);
        side(9);
        const uint32_t tcur = tc.x, spc = sp;
#pragma unroll
        for (int i = 0; i < NFAST - 1; i++) {
            gather(R, i + 2, (i + 2) % 3);
            if (i == 0) side(10);
            if (i == 0) WAIT_L(6); else if (i == 1) WAIT_L(9); else WAIT_L(12);
            scatter(R, i, i % 3, 0ull);
            if (!last && (i & 1)) { ring[i - 1] = load_main(i - 1, ff + 1, first && !(NEARM & 4u)); ring[i] = load_main(i, ff + 1, first && !(NEARM & 4u)); }
            side(i);
            if (i == 3) SETP(2);
            if (i == 4) SETP(1);
            if (i == 5) SETP(0);
            if (i == 0 && !last) {
                tc.x = __builtin_amdgcn_alignbit(tc.y, tc.x, 8u); tc.y = __builtin_amdgcn_alignbit(tc.z, tc.y, 8u);
                tc.z = __builtin_amdgcn_alignbit(tc.w, tc.z, 8u); tc.w >>= 8;
                sp = ff == 7 ? sp0 + NWG * 9u * 128u : sp + ((tcur & 0x3cu) << 2);
                list1(ff + 1);
            }
            __builtin_amdgcn_sched_barrier(0);
        }
        if (last) asm volatile("" :: "v"(ring[NFAST + 1]));
        if (__builtin_expect(ntl > 16, 0)) {
            gather(R, NFAST + 1, 0);
            WAIT_L(0);
            scatter(R, NFAST + 1, 0, tm1);
        }
        if (__builtin_expect(ntl > 32, 0)) {
#pragma unroll 1
            for (int e = 2; e < 6; e++) {
                int dst;
                uint32_t en;
                if (tail_rank(tcur, spc, ff, (uint32_t)e & 4u, dst, en) <= (e & 3) * 16) continue;
                const int cmp = __builtin_amdgcn_ds_permute(dst, (int)en);
                ring[NFAST + 1] = load_tail((uint32_t)__builtin_amdgcn_ds_bpermute(((lane & ~3) | (e & 3)) << 2, cmp), tm1);
                gather(R, NFAST + 1, 0);
                WAIT_L(0);
                scatter(R, NFAST + 1, 0, tm1);
            }
        }
        SETP(3);
        WAIT_L(3);
        if (!last) {
            __builtin_amdgcn_s_barrier();
            store_md();
            SETP(0);
        }
        scatter(R, NFAST - 1, 1, 0ull);
        if (!last) { ring[6] = load_main(6, ff + 1); ring[7] = load_main(7, ff + 1); }
        scatter(R, NFAST, 2, tm0);
        if (!last) {
            WAIT_L(6);
            __builtin_amdgcn_s_barrier();
            SETP(3);
        }
    };
    auto steps = [&](auto RR) __attribute__((always_inline)) {
        tc = tcnt[(wv * 16 + (lane >> 2)) * 128 + j * 4 + h];
        u32x4 sv[2];
        float4 su[3];
        uint32_t e0;
        if (wv < 4) {
            sv[0] = (u32x4)(0u); sv[1] = sv[0];
            su[0] = make_float4(0.f, 0.f, 0.f, 0.f);
            if (lane < SLOTI / 256) { sv[0] = stat[tid * 2]; sv[1] = stat[tid * 2 + 1]; su[0] = ((const float4*)(ws + W_SUM))[tid]; }
            su[1] = ((const float4*)(ws + W_SUM))[256 + lane];
            su[2] = ((const float4*)(ws + W_SUM))[320 + lane];
            e0 = meta[M_E0 + wv];
        }
        const u32x2 extra = *(const u32x2*)(meta + M_LOG);
        load_md(0, -1, 0);
        {
            const u32x4 bi = {BIAS, BIAS, BIAS, BIAS};
#pragma unroll
            for (int k = 0; k < 6; k++) *(LDS u32x4*)(uintptr_t)(A_ACC + (k * NT + threadIdx.x) * 16) = bi;
            if (threadIdx.x < 128) *(LDS u32x4*)(uintptr_t)(A_ACC + (6 * NT + threadIdx.x) * 16) = bi;
            __builtin_amdgcn_sched_barrier(0);
        }
#pragma unroll
        for (int i = 0; i < 2; i++) ring[i] = load_main(i, 0);
        __builtin_amdgcn_sched_barrier(0);
        list1(0);
        if (wv < 4) {
            const uint32_t mxb = wave_max(max(max(sv[0].x & 0xffffu, sv[0].z & 0xffffu), max(sv[1].x & 0xffffu, sv[1].z & 0xffffu)));
            const uint32_t mnb = 0xffffu - (wave_max(max(max(sv[0].x, sv[0].z), max(sv[1].x, sv[1].z))) >> 16);
            const float s2 = wave_sum((__uint_as_float(sv[0].y) + __uint_as_float(sv[0].w)) + (__uint_as_float(sv[1].y) + __uint_as_float(sv[1].w)));
            const float s1 = wave_sum((su[0].x + su[0].y) + (su[0].z + su[0].w));
            const float c1 = wave_sum((su[1].x + su[1].y) + (su[1].z + su[1].w)) * (1.0f / (NWG * 512)), c2 = wave_sum((su[2].x + su[2].y) + (su[2].z + su[2].w)) * (1.0f / (NWG * 512));
            const float rng = fmaxf(fabsf(zf * s1 * c1) + ZSIG * sqrtf(zf * s2 * c2), 2.0f * bf2f(mxb)) * 6.103515625e-05f;
            const uint32_t eb = mxb ? (__float_as_uint(rng) + 0x7fffffu) >> 23 : 127u;
            const bool ok = !mxb || (mxb < 0x4700u && eb >= 107u && eb <= 140u && mnb >= ((eb + 8u) << 7) && c2 >= 0.015625f);
            if (lane == 0) {
                s_flag[8 + wv] = (eb & 0xffu) | (ok ? 0x100u : 0u) | (mxb ? 0x200u : 0u);
                s_flag[12 + wv] = __float_as_uint(__uint_as_float(((eb + e0) & 0xffu) << 23) * (1024.0f / 1023.0f));
                s_flag[wv] = 0u;
            }
        }
        store_md();
#pragma unroll
        for (int i = 2; i < 4; i++) ring[i] = load_main(i, 0);
        WAIT_L(0);
        __syncthreads();
        const uint32_t q0 = __builtin_amdgcn_readfirstlane(s_flag[8 + 2 * cp]), q1 = __builtin_amdgcn_readfirstlane(s_flag[9 + 2 * cp]);
        fast = (q0 & q1 & 0x100u) != 0u;
        need = (q0 >> 9) | ((q1 >> 9) << 1);
        more = (__builtin_amdgcn_readfirstlane(extra.x | extra.y) | (uint32_t)(T & 3)) != 0u;
        mg0 = (((q0 & 0xffu) + 9u) << 23) | 0x400000u;
        mg1 = ((((q1 & 0xffu) + 9u) << 23) | 0x400000u) + ((0u - (mg0 >> 16)) & 0xffffu);
        if (!fast) { __builtin_amdgcn_s_waitcnt(0x0f70); return; }
        step(RR, IC<1>(), 0);
#pragma unroll 1
        for (int ff = 1; ff < 15; ff++) step(RR, IC<0>(), ff);
        step(RR, IC<2>(), 15);
        if (fin) factor(7, false);
    };
    auto irregular = [&](auto f) __attribute__((always_inline)) {
        const float fr = bf2f(freq[0]);
        const char* lg = recs + (size_t)LBASE * 128u;
        const uint16_t* tags = (const uint16_t*)(lg + (size_t)lcap * 128u);
        const uint32_t nl = meta[M_LOG], ns = meta[M_LOG + 1];
#pragma unroll 1
        for (uint32_t q = tid; q < nl * 8u; q += NT) {
            const uint32_t b = tags[q >> 3];
            if ((b & 127u) == (uint32_t)(h * 32 + j)) {
                const uint32_t* un = (const uint32_t*)(lg + (size_t)q * 16u);
                const uint32_t xw = un[3 ^ h];
#pragma unroll 1
                for (int k = 0; k < 3; k++) {
                    const uint32_t ww = un[k ^ h];
                    const float c = (float)((xw >> (k * 11)) & 0x3ffu) * (1.0f / 1023.0f);
                    if (!(ww & WNULL)) f(ww >> 16, ((ww & 0x3fffu) << 6) | (b >> 5), (ww & 0x8000u) ? -c : c);
                }
            }
        }
#pragma unroll 1
        for (uint32_t q = tid; q < ns + (uint32_t)(T & 3); q += NT) {
            const uint32_t t = q < ns ? ((const uint32_t*)tags)[-1 - (int)q] : (uint32_t)(T & ~3) + (q - ns);
            const uint32_t i = (uint32_t)iin[t], o = (uint32_t)iout[t];
            if (((o >> 6) & 31u) == (uint32_t)j && (i & 3u) == (uint32_t)h) f(((o >> 11) << 6) | (o & 63u), i, cosf(fr * bf2f(ang[t])));
        }
        __builtin_amdgcn_s_waitcnt(0x0f70);
    };

    if (h == 0) steps(IC<0>());
    else if (h == 1) steps(IC<1>());
    else if (h == 2) steps(IC<2>());
    else steps(IC<3>());
    if (fast) {
        if (more) irregular([&](uint32_t ol, uint32_t i, float cf) __attribute__((always_inline)) {
            const uint32_t md = mdc[(cp * 64u + (i & 63u)) * SLOTI + ((i >> 14) << 8) + ((i >> 4) & 0xfcu) + ((i >> 12) & 3u)];
            const float c = cf * (1023.0f / 16777216.0f);
            const uint32_t lo = __float_as_uint(__builtin_fmaf(c, h2f(md & 0xffffu), __uint_as_float(mg0)));
            const uint32_t hi = __float_as_uint(__builtin_fmaf(c, h2f(md >> 16), __uint_as_float(mg1)));
            if (cf == cf) guard |= atomicAdd(&acc[ol], (hi << 16) + lo);
            else guard |= HSLOW;
        });
    }
    volatile LDS unsigned* seen = (volatile LDS unsigned*)(uintptr_t)(A_FLAG + 4);
    if (fast && wv == 15) asm volatile("s_waitcnt vmcnt(0)\n\tds_write_b32 %0, v108 offset:16\n\tv_and_b32 v109, 0xff00ff, v108\n\tds_write_b32 %0, v109" :: "v"(vconst<A_FLAG + 4>()) : "memory");
    WAIT_L(0);
    asm volatile("v_or3_b32 %0, %0, v110, v111\n\tv_or3_b32 %0, %0, v112, v113\n\tv_or3_b32 %0, %0, v114, v115\n\tv_or3_b32 %0, %0, v116, v117\n\tv_or_b32 %0, v118, %0" : "+v"(guard));
    __syncthreads();
    const __amdgpu_buffer_rsrc_t rsP = __builtin_amdgcn_make_buffer_rsrc((void*)(partial + (size_t)j * 16 * SLOTO), 0, 16 * SLOTO * 4, 0x00020000);
    const char* pk = recs + (uint32_t)j * (SC * 128u);
    const __amdgpu_buffer_rsrc_t rsK = __builtin_amdgcn_make_buffer_rsrc((void*)pk, 0, (int)(MAINL * 128u), 0x00020000);
#define PKA (SSM * 128u)
#define PKB (32u * SC * 128u)
    auto pk_at = [&](uint32_t i) __attribute__((always_inline)) -> uint32_t {
        const uint32_t c = i >> 9;
        return (c < 12u ? (1u + (c >> 2)) * SQ + (c & 3u) * SKM : SQ + 2u * SSM) * 128u + (i & 511u) * 16u;
    };
    u32x4 p[8];
    unsigned have = 0u;
    auto fetch = [&](unsigned v, int sh) __attribute__((always_inline)) {
        const uint32_t po = tid < SHARE / 4 ? pk_at((uint32_t)(sh * (SHARE / 4) + tid)) : 0x7fffff00u;
        const unsigned nw = v & ~have;
#pragma unroll
        for (int r = 0; r < 8; r++) {
            const uint32_t k = (uint32_t)(me ^ r);
            if ((nw >> k) & 1u) p[r] = __builtin_amdgcn_raw_buffer_load_b128(rsK, po, ((v >> (16u + k)) & 1u) ? MAINL * 128u : (k & 1u) * PKA + (k >> 1) * PKB, AUX_SC1);
        }
        have |= v;
    };
    bool room = false;
    if (fast) {
        room = (__builtin_amdgcn_readfirstlane(s_flag[5]) >> (24 + (me ^ 1))) & 1u;
        const __amdgpu_buffer_rsrc_t rsW = __builtin_amdgcn_make_buffer_rsrc((void*)(pk + (uint32_t)cp * PKA + (uint32_t)h * PKB), 0, room ? (int)(MAINL * 128u) : 0, 0x00020000);
        uint32_t big = 0u;
        u32x4 v[7];
#pragma unroll
        for (int k = 0; k < 6; k++) v[k] = acc_vec(k * NT + tid);
        v[6] = (u32x4)(BIAS);
        if (wv < 2) v[6] = acc_vec(6 * NT + tid);
#pragma unroll
        for (int k = 0; k < 6; k++) __builtin_amdgcn_raw_buffer_store_b128(v[k], rsW, pk_at((uint32_t)tid) + ((k >> 1) * SQ + (k & 1) * 2u * SKM) * 128u, 0, AUX_SC1);
        if (wv < 2) __builtin_amdgcn_raw_buffer_store_b128(v[6], rsW, (SQ + 2u * SSM) * 128u + (uint32_t)tid * 16u, 0, AUX_SC1);
        __builtin_amdgcn_sched_barrier(0);
#pragma unroll
        for (int k = 0; k < 28; k++) { guard |= v[k >> 2][k & 3]; big |= v[k >> 2][k & 3] + 0x42004200u; }
        if (guard & HSLOW) s_flag[0] = 1u;
        if (big & 0x7c00u) s_flag[2] = 1u;
        if (big & 0x7c000000u) s_flag[3] = 2u;
        __builtin_amdgcn_sched_barrier(0);
        p[0] = acc_vec(me * (SHARE / 4) + min(tid, SHARE / 4 - 1));
        have = 1u << me;
        if (fin) fetch(__builtin_amdgcn_readfirstlane(*seen), me);
    }
    asm volatile("s_waitcnt vmcnt(0)" ::: "memory");
    __syncthreads();
    const bool redo = !fast || __builtin_amdgcn_readfirstlane(s_flag[0]) != 0u || (need & ~__builtin_amdgcn_readfirstlane(s_flag[2] | s_flag[3])) != 0u;
    const bool slow = redo || !room;
    if (slow) {
        float* fs = (float*)acc;
        float w[16];
        load_w(wdown, w);
        const float fr = bf2f(freq[0]);
#pragma unroll 1
        for (int e = 0; e < 2; e++) {
            if (redo) {
                __syncthreads();
                for (int i = tid; i < SLOTO; i += NT) fs[i] = 0.f;
                __syncthreads();
                const __amdgpu_buffer_rsrc_t rsO = __builtin_amdgcn_make_buffer_rsrc((void*)iout, 0, T * 4, 0x00020000);
                const __amdgpu_buffer_rsrc_t rsI = __builtin_amdgcn_make_buffer_rsrc((void*)iin, 0, T * 4, 0x00020000);
#pragma unroll 1
                for (int t0 = tid * 4; t0 < T; t0 += NT * 16) {
                    u32x4 o4[4], i4[4];
#pragma unroll
                    for (int g = 0; g < 4; g++) {
                        o4[g] = __builtin_amdgcn_raw_buffer_load_b128(rsO, (uint32_t)(t0 + g * NT * 4) * 4u, 0, 0);
                        i4[g] = __builtin_amdgcn_raw_buffer_load_b128(rsI, (uint32_t)(t0 + g * NT * 4) * 4u, 0, 0);
                    }
                    uint32_t hit = 0u;
#pragma unroll
                    for (int k = 0; k < 16; k++) hit |= (bfi(3u, i4[k >> 2][k & 3], o4[k >> 2][k & 3]) & 0x7c3u) == (uint32_t)(j * 64 + h) ? 1u << k : 0u;
#pragma unroll 1
                    for (; hit; hit &= hit - 1u) {
                        const int k = __builtin_ctz(hit), t = t0 + (k >> 2) * NT * 4 + (k & 3);
                        if (t >= T) break;
                        const uint32_t o = (uint32_t)iout[t];
                        uint32_t rr[4];
                        md_of(m[iin[t]], w, rr);
                        atomicAdd(&fs[((o >> 11) << 6) | (o & 63u)], cosf(fr * bf2f(ang[t])) * bf2f(cp ? (e ? rr[3] : rr[2]) : (e ? rr[1] : rr[0])));
                    }
                }
                __syncthreads();
            }
            const float un = __uint_as_float(s_flag[12 + 2 * cp + e]);
            for (int i = tid; i < SLOTO / 4; i += NT) {
                u32x4 v = acc_vec(i);
                if (!redo) {
#pragma unroll
                    for (int k = 0; k < 4; k++) v[k] = __float_as_uint((float)((int)((v[k] >> (16 * e)) & 0xffffu) - 0x4000) * un);
                }
                __builtin_amdgcn_raw_buffer_store_b128(v, rsP, (uint32_t)((e * 8 + me) * SLOTO * 4 + i * 16), 0, AUX_SC1);
            }
        }
        load_wl();
        edges(me);
#pragma unroll
        for (int k = 0; k < 8; k++) p[k] = (u32x4)(0u);
        have = 0u;
        asm volatile("s_waitcnt vmcnt(0)" ::: "memory");
        __syncthreads();
    }
    if (tid == NT - 64) {
        const unsigned inc = (slow ? 0x10001u : 1u) << me;
        unsigned v = __hip_atomic_fetch_add(dw, inc, __ATOMIC_RELAXED, __HIP_MEMORY_SCOPE_AGENT) + inc;
        unsigned quit = 0u;
        if ((v & 0xffu) != 0xffu) {
            unsigned spins = 0;
            do {
                *seen = v & 0xff00ffu;
                __builtin_amdgcn_s_sleep(1);
                v = __hip_atomic_load(dw, __ATOMIC_RELAXED, __HIP_MEMORY_SCOPE_AGENT);
                if (++spins > SPINCAP && (v & 0xffu) != 0xffu) {
                    v = __hip_atomic_fetch_add(dw, 256u << me, __ATOMIC_RELAXED, __HIP_MEMORY_SCOPE_AGENT);
                    if ((v & 0xffu) != 0xffu) { v = 0xffu; quit = 0x80000000u; }
                }
            } while ((v & 0xffu) != 0xffu);
            v &= 0xff00ffu;
        }
        *seen = (v & 0xffffffu) | quit;
    }
    if (!fin) return;
    have = __builtin_amdgcn_readfirstlane(have);
    uint32_t un[4];
#pragma unroll
    for (int c = 0; c < 4; c++) un[c] = __builtin_amdgcn_readfirstlane(s_flag[12 + c]);
    unsigned sv;
    {
        const u32x4 rsQ = {(uint32_t)(uintptr_t)pk, (uint32_t)((uintptr_t)pk >> 32) & 0xffffu, MAINL * 128u, 0x00020000u};
        const uint32_t po = tid < SHARE / 4 ? pk_at((uint32_t)(me * (SHARE / 4) + tid)) : 0x7fffff00u;
        do {
            sv = __builtin_amdgcn_readfirstlane(*seen);
            uint32_t t0, t1, t2;
#define FET(R) "s_xor_b32 %8, %13, " #R "\n\ts_bitcmp1_b32 %14, %8\n\ts_cbranch_scc0 1f\n\ts_and_b32 %9, %8, 1\n\ts_mul_i32 %9, %9, %16\n\ts_lshr_b32 %10, %8, 1\n\t" \
               "s_mul_i32 %10, %10, %17\n\ts_add_u32 %9, %9, %10\n\ts_bitcmp1_b32 %15, %8\n\ts_cselect_b32 %9, %18, %9\n\tbuffer_load_dwordx4 %" #R ", %11, %12, %9 offen sc1\n1:\n\t"
            asm volatile("s_and_b32 %8, %14, 0xff\n\ts_cbranch_scc0 2f\n\ts_nop 2\n\t" FET(0) FET(1) FET(2) FET(3) FET(4) FET(5) FET(6) FET(7) "\n2:\n\ts_sleep 2\n\ts_waitcnt vmcnt(0)"
                         : "+v"(p[0]), "+v"(p[1]), "+v"(p[2]), "+v"(p[3]), "+v"(p[4]), "+v"(p[5]), "+v"(p[6]), "+v"(p[7]), "=&s"(t0), "=&s"(t1), "=&s"(t2)
                         : "v"(po), "s"(rsQ), "s"(me), "s"((int)sv < 0 ? 0u : sv & ~have), "s"(sv >> 16), "n"(PKA), "n"(PKB), "n"(MAINL * 128u) : "memory", "scc");
            have |= sv;
        } while ((sv & 0xffu) != 0xffu);
    }
    if ((int)sv < 0) return;
    const unsigned flt = (sv >> 16) & 0xffu;
    auto finish = [&](int sh) __attribute__((always_inline)) {
        {
            const int ol = sh * SHARE + tid * 4;
            const uint32_t po = tid < SHARE / 4 ? (uint32_t)ol * 4u : 0x7fffff00u;
            float g[4][4];
            u32x4 a[2] = {p[0] + p[2], p[1] + p[3]}, b[2] = {p[4] + p[6], p[5] + p[7]};
            if (cp) { const u32x4 t = a[0], u = b[0]; a[0] = a[1]; b[0] = b[1]; a[1] = t; b[1] = u; }
#pragma unroll
            for (int c = 0; c < 2; c++) {
                const int nb = 0x10000 - 0x4000 * __builtin_popcount(flt & (0x55u << c));
                const float u0 = __uint_as_float(nb ? un[2 * c] : 0u), u1 = __uint_as_float(nb ? un[2 * c + 1] : 0u);
#pragma unroll
                for (int q = 0; q < 4; q++) {
                    g[q][2 * c] = (float)((int)((a[c][q] & 0xffffu) + (b[c][q] & 0xffffu)) - nb) * u0;
                    g[q][2 * c + 1] = (float)((int)((a[c][q] >> 16) + (b[c][q] >> 16)) - nb) * u1;
                }
            }
            if (__builtin_expect(flt != 0u, 0)) {
#pragma unroll
                for (int c = 0; c < 2; c++) {
#pragma unroll 1
                    for (int k = c; k < 8; k += 2) {
                        if (!((flt >> k) & 1u)) continue;
                        const u32x4 f0 = __builtin_amdgcn_raw_buffer_load_b128(rsP, po, k * SLOTO * 4, AUX_SC1);
                        const u32x4 f1 = __builtin_amdgcn_raw_buffer_load_b128(rsP, po, (8 + k) * SLOTO * 4, AUX_SC1);
#pragma unroll
                        for (int q = 0; q < 4; q++) { g[q][2 * c] += __uint_as_float(f0[q]); g[q][2 * c + 1] += __uint_as_float(f1[q]); }
                    }
                }
            }
            float wu[16];
#pragma unroll
            for (int k = 0; k < 8; k++) {
                const uint32_t x = __builtin_amdgcn_readlane(wl, 40 + k);
                wu[2 * k] = bf2f(x & 0xffffu); wu[2 * k + 1] = bf2f(x >> 16);
            }
            u32x4 ov[2];
#pragma unroll
            for (int q = 0; q < 4; q++) {
#pragma unroll
                for (int k = 0; k < 4; k++) g[q][k] *= fa[q][k >> 1][k & 1];
                const uint32_t m0 = em[q >> 1][(q & 1) * 2], m1 = em[q >> 1][(q & 1) * 2 + 1];
                const float mm[4] = {bf2f(m0 & 0xffff), bf2f(m0 >> 16), bf2f(m1 & 0xffff), bf2f(m1 >> 16)};
                float o[4];
#pragma unroll
                for (int k = 0; k < 4; k++) o[k] = mm[k] + (g[q][0] * wu[k] + g[q][1] * wu[4 + k] + g[q][2] * wu[8 + k] + g[q][3] * wu[12 + k]) * fa[q][2 + (k >> 1)][k & 1];
                ov[q >> 1][(q & 1) * 2] = pk_bf16(o[0], o[1]);
                ov[q >> 1][(q & 1) * 2 + 1] = pk_bf16(o[2], o[3]);
            }
            const __amdgpu_buffer_rsrc_t rsU = __builtin_amdgcn_make_buffer_rsrc((void*)out, 0, E * 8, 0x00020000);
            const uint32_t uo = edge0(sh) * 8u;
            __builtin_amdgcn_raw_buffer_store_b128(ov[0], rsU, uo, 0, AUX_NT);
            __builtin_amdgcn_raw_buffer_store_b128(ov[1], rsU, uo + 256u, 0, AUX_NT);
        }
    };
    finish(me);
#pragma unroll 1
    for (unsigned todo = (sv >> 8) & 0xffu; todo; todo &= todo - 1u) {
        const int sh = __builtin_ctz(todo);
        edges(sh);
        have = 0u;
        fetch(sv, sh);
        finish(sh);
    }
}

__global__ __launch_bounds__(256) void gen_scatter_kernel(const int* __restrict__ iin, const int* __restrict__ iout,
                                                          const uint16_t* __restrict__ ang, const uint16_t* __restrict__ freq,
                                                          const u32x2* __restrict__ m, const uint16_t* __restrict__ wdown,
                                                          float* __restrict__ agg, int T) {
    float w[16];
    load_w(wdown, w);
    const float fr = bf2f(freq[0]);
    for (long t = (long)blockIdx.x * 256 + threadIdx.x; t < T; t += (long)gridDim.x * 256) {
        const float c = cosf(fr * bf2f(ang[t]));
        uint32_t r[4];
        md_of(m[iin[t]], w, r);
        float* q = agg + (size_t)iout[t] * 4;
#pragma unroll
        for (int k = 0; k < 4; k++) atomicAdd(q + k, c * bf2f(r[k]));
    }
}
__global__ __launch_bounds__(256) void gen_final_kernel(const u32x2* __restrict__ m, const uint16_t* __restrict__ dist,
                                                        const uint16_t* __restrict__ centers, const uint16_t* __restrict__ wrbf,
                                                        const uint16_t* __restrict__ wsbf, const uint16_t* __restrict__ wup,
                                                        const float* __restrict__ agg, u32x2* __restrict__ out, int E) {
    const int e = blockIdx.x * 256 + threadIdx.x;
    if (e >= E) return;
    const float d = bf2f(dist[e]);
    float rs[4] = {0, 0, 0, 0}, rg[4] = {0, 0, 0, 0};
#pragma unroll 1
    for (int r = 0; r < 8; r++) {
        const float x = d - bf2f(centers[r]);
        const float rb = __expf(-10.0f * x * x);
#pragma unroll
        for (int k = 0; k < 4; k++) { rs[k] += rb * bf2f(wsbf[r * 4 + k]); rg[k] += rb * bf2f(wrbf[r * 4 + k]); }
    }
    float wu[16];
    load_w(wup, wu);
    const u32x2 mv = m[e];
    const float mm[4] = {bf2f(mv.x & 0xffff), bf2f(mv.x >> 16), bf2f(mv.y & 0xffff), bf2f(mv.y >> 16)};
    float g[4], o[4];
#pragma unroll
    for (int k = 0; k < 4; k++) g[k] = agg[(size_t)e * 4 + k] * rs[k];
#pragma unroll
    for (int k = 0; k < 4; k++) o[k] = mm[k] + (g[0] * wu[k] + g[1] * wu[4 + k] + g[2] * wu[8 + k] + g[3] * wu[12 + k]) * rg[k];
    const u32x2 ov = {f2bf(o[0]) | (f2bf(o[1]) << 16), f2bf(o[2]) | (f2bf(o[3]) << 16)};
    out[e] = ov;
}

extern "C" void kernel_launch(void* const* d_in, const int* in_sizes, int n_in,
                              void* d_out, int out_size, void* d_ws, size_t ws_size,
                              hipStream_t stream) {
    const u32x2* m = (const u32x2*)d_in[0];
    const int* iin = (const int*)d_in[1];
    const int* iout = (const int*)d_in[2];
    const uint16_t* dist = (const uint16_t*)d_in[3];
    const uint16_t* ang = (const uint16_t*)d_in[4];
    const uint16_t* centers = (const uint16_t*)d_in[5];
    const uint16_t* freq = (const uint16_t*)d_in[6];
    const uint16_t* wrbf = (const uint16_t*)d_in[7];
    const uint16_t* wsbf = (const uint16_t*)d_in[8];
    const uint16_t* wdown = (const uint16_t*)d_in[9];
    const uint16_t* wup = (const uint16_t*)d_in[10];
    const int E = in_sizes[3];
    const int T = in_sizes[1];
    char* ws = (char*)d_ws;
    const int G = T >> 2;
    const int ntile = (G + GR - 1) / GR;
    const int nstat = ntile / NWG - NDYN > 4 ? ntile / NWG - NDYN : 4;
    const int lcap = G / 6 + 2;
    char* recs = ws + W_RECS;
    size_t off = W_RECS;
    const size_t rbytes = (size_t)LBASE * 128 + (size_t)lcap * 130;
    off += rbytes;
    if (E > EMAX || (E & 3) || E < 1 || rbytes > 0x70000000u || off > ws_size) {
        (void)hipMemsetAsync(ws, 0, (size_t)E * 16, stream);
        gen_scatter_kernel<<<4096, 256, 0, stream>>>(iin, iout, ang, freq, m, wdown, (float*)ws, T);
        gen_final_kernel<<<(E + 255) / 256, 256, 0, stream>>>(m, dist, centers, wrbf, wsbf, wup, (const float*)ws, (u32x2*)d_out, E);
        return;
    }
    int ks = T / (NWG * NB * 24);
    if (ks > KMAX) ks = KMAX;
    const float zf = (float)T / (4.0f * (float)E * (float)E);
    (void)hipFuncSetAttribute((const void*)part_kernel, hipFuncAttributeMaxDynamicSharedMemorySize, P_LDS);
    (void)hipFuncSetAttribute((const void*)acc_kernel, hipFuncAttributeMaxDynamicSharedMemorySize, A_LDS);
    part_kernel<<<NWG, NT, P_LDS, stream>>>(iin, iout, ang, freq, m, wdown, recs, T, E, ntile, nstat, ks, lcap);
    acc_kernel<<<NWG, NT, A_LDS, stream>>>(ws, T, E, ks, lcap, zf, 0, centers, wrbf, wsbf, wup, m, dist, (u32x2*)d_out, iin, iout, ang, wdown, freq);
}
```

```cpp
#include <hip/hip_runtime.h>
#include <hip/hip_bf16.h>
#include <stdint.h>
#pragma clang diagnostic ignored "-Winline-asm"

typedef unsigned int u32x4 __attribute__((ext_vector_type(4)));
typedef unsigned int u32x2 __attribute__((ext_vector_type(2)));
typedef unsigned short u16x2 __attribute__((ext_vector_type(2)));
typedef __bf16 bf16x2 __attribute__((ext_vector_type(2)));
typedef float f32x2 __attribute__((ext_vector_type(2)));
typedef unsigned long long u64;

static __device__ __forceinline__ float bf2f(uint32_t bits16) { return __uint_as_float(bits16 << 16); }
static __device__ __forceinline__ uint32_t f2bf(float f) {
    uint32_t u = __float_as_uint(f);
    if ((u & 0x7fffffffu) > 0x7f800000u) return (u >> 16) | 0x40;
    return (u + 0x7fffu + ((u >> 16) & 1u)) >> 16;
}
static __device__ __forceinline__ float h2f(uint32_t bits16) { return (float)__builtin_bit_cast(_Float16, (unsigned short)bits16); }

#define NB 2048
#define WNULL 0x80000000u
#define CSTEP 0x2aabu
#define CINIT 205u
#define CHV (12u * CSTEP)
#define SLOTO 25088
#define SLOTI 12544
#define EMAX (SLOTO * 32)
#define NT 1024
#define NWG 256
#define KMAX 4
#define NTAIL 4
#define SC 64u
#define SKM (128u * SC + 2u)
#define SSM (4u * SKM + 1u)
#define SSH (128u * SC + 1u)
#define SKH (16u * SSH + 2u)
#define SQ (16u * SSM)
#define MAINL (4u * SQ)
#define BPL(b) ((((b) >> 3) & 3u) * SQ + ((b) & 7u) + (((b) >> 5) << 3))
#define TST 257u
#define TBASE MAINL
#define TPL (NB * TST)
#define SBASE (TBASE + 2u * TPL)
#define LBASE (SBASE + 128u * 2u * NWG * 9u)
#define NDYN 3
#define NEARM 0x55555554u
#define GR 2048
#define M_DONE 0
#define M_TILE (32 * 64)
#define M_LOG (M_TILE + 64)
#define M_MD (M_LOG + 64)
#define M_BEAT (M_MD + 64)
#define M_E0 (M_BEAT + 64)
#define M_WORDS (M_E0 + 64)
#define W_TCNT (M_WORDS * 4)
#define W_STAT (W_TCNT + NWG * NB)
#define W_SUM (W_STAT + 4 * NWG * 8)
#define W_MDC (W_SUM + 6 * NWG * 4)
#define W_PART (W_MDC + 128 * SLOTI * 4)
#define W_RECS (W_PART + 32 * 16 * SLOTO * 4)
#define AUX_NT 2
#define AUX_SC1 16

static __device__ __forceinline__ uint32_t row_max(uint32_t x) {
    x = max(x, (uint32_t)__builtin_amdgcn_update_dpp(0, x, 0x111, 0xf, 0xf, false));
    x = max(x, (uint32_t)__builtin_amdgcn_update_dpp(0, x, 0x112, 0xf, 0xf, false));
    x = max(x, (uint32_t)__builtin_amdgcn_update_dpp(0, x, 0x114, 0xf, 0xf, false));
    return max(x, (uint32_t)__builtin_amdgcn_update_dpp(0, x, 0x118, 0xf, 0xf, false));
}
static __device__ __forceinline__ uint32_t wave_max(uint32_t x) {
    x = row_max(x);
    x = max(x, (uint32_t)__builtin_amdgcn_update_dpp(0, x, 0x142, 0xa, 0xf, false));
    x = max(x, (uint32_t)__builtin_amdgcn_update_dpp(0, x, 0x143, 0xc, 0xf, false));
    return __builtin_amdgcn_readlane(x, 63);
}
static __device__ __forceinline__ float row_sum(float x) {
    x += __int_as_float(__builtin_amdgcn_update_dpp(0, __float_as_int(x), 0x111, 0xf, 0xf, false));
    x += __int_as_float(__builtin_amdgcn_update_dpp(0, __float_as_int(x), 0x112, 0xf, 0xf, false));
    x += __int_as_float(__builtin_amdgcn_update_dpp(0, __float_as_int(x), 0x114, 0xf, 0xf, false));
    return x + __int_as_float(__builtin_amdgcn_update_dpp(0, __float_as_int(x), 0x118, 0xf, 0xf, false));
}
static __device__ __forceinline__ float wave_sum(float x) {
    x = row_sum(x);
    x += __int_as_float(__builtin_amdgcn_update_dpp(0, __float_as_int(x), 0x142, 0xa, 0xf, false));
    x += __int_as_float(__builtin_amdgcn_update_dpp(0, __float_as_int(x), 0x143, 0xc, 0xf, false));
    return __int_as_float(__builtin_amdgcn_readlane(__float_as_int(x), 63));
}
static __device__ __forceinline__ uint32_t bfi(uint32_t mask, uint32_t a, uint32_t b) {
    return __builtin_amdgcn_bitop3_b32(mask, a, b, 0xca);
}
template <uint32_t C> static __device__ __forceinline__ uint32_t vconst() {
    uint32_t r;
    asm volatile("v_mov_b32 %0, %1" : "=v"(r) : "n"(C));
    return r;
}
static __device__ __forceinline__ uint32_t pk_bf16(float a, float b) {
    const f32x2 v = {a, b};
    return __builtin_bit_cast(uint32_t, __builtin_convertvector(v, bf16x2));
}
static __device__ __forceinline__ void pin(uint32_t& x) { asm volatile("" : "+v"(x)); }

#define RTS(k)

static __device__ __forceinline__ void md_of(u32x2 v, const float* w, uint32_t* r) {
    float a0 = bf2f(v.x & 0xffff), a1 = bf2f(v.x >> 16), a2 = bf2f(v.y & 0xffff), a3 = bf2f(v.y >> 16);
#pragma unroll
    for (int j = 0; j < 4; j++) r[j] = f2bf(a0 * w[j] + a1 * w[4 + j] + a2 * w[8 + j] + a3 * w[12 + j]);
}
static __device__ __forceinline__ void load_w(const uint16_t* __restrict__ wdown, float* w) {
    u32x4 wv0 = *(const u32x4*)wdown, wv1 = *(const u32x4*)(wdown + 8);
#pragma unroll
    for (int i = 0; i < 4; i++) {
        w[2 * i] = bf2f(wv0[i] & 0xffffu); w[2 * i + 1] = bf2f(wv0[i] >> 16);
        w[8 + 2 * i] = bf2f(wv1[i] & 0xffffu); w[8 + 2 * i + 1] = bf2f(wv1[i] >> 16);
    }
}
#define LDS __attribute__((address_space(3)))
#define P_CNT 0
#define P_FLAG ((NB + 64) * 4)
#define P_LST (P_FLAG + 256)
#define LCAP 32
#define P_TAB (P_LST + 4 * LCAP * 16)
#define P_BUF (P_TAB + NB * 4)
#define P_LDS (P_BUF + NB * 64)
#define PD 2
#define LPOS 0x448763
#define SETP(n) __builtin_amdgcn_s_setprio(n)
#define MDL 1
#define MDM 3
#define RMAXREV 40.0f
#define HB 88
#define HREG(S, D) (HB + ((S) & 3) * 10 + (((S) >> 2) + (D)) * 2)

template <int N> struct IC { static constexpr int value = N; };
struct Raw { u32x4 o[2], i[2]; u32x2 a[2]; };
static __device__ __forceinline__ uint32_t lds_ld(uint32_t a) { return *(const LDS uint32_t*)(uintptr_t)a; }
static __device__ __forceinline__ void lds_st(uint32_t a, uint32_t v) { *(LDS uint32_t*)(uintptr_t)a = v; }
static __device__ __forceinline__ void lds_or(uint32_t a, uint32_t v) {
    __hip_atomic_fetch_or((LDS uint32_t*)(uintptr_t)a, v, __ATOMIC_RELAXED, __HIP_MEMORY_SCOPE_WORKGROUP);
}
static __device__ __forceinline__ uint32_t lds_add(uint32_t a, uint32_t v) {
    return __hip_atomic_fetch_add((LDS uint32_t*)(uintptr_t)a, v, __ATOMIC_RELAXED, __HIP_MEMORY_SCOPE_WORKGROUP);
}
static __device__ __forceinline__ uint32_t slot_w(uint32_t ca, uint32_t cv) {
    return (ca << 4) | __builtin_amdgcn_bitop3_b32(cv >> 11, ca, 0x3cu, 0x28);
}
static __device__ __forceinline__ uint32_t slot_x2(uint32_t aw, uint32_t ca) {
    return __builtin_amdgcn_bitop3_b32(aw, ca, 0xcu, 0x72);
}
static __device__ __forceinline__ uint32_t slot_x(uint32_t aw) { return slot_x2(aw, aw >> 4); }
template <int R> static __device__ __forceinline__ u32x2 hget() {
    u32x2 d;
    asm volatile("v_mov_b64 %0, v[%1:%2]" : "=v"(d) : "n"(R), "n"(R + 1));
    return d;
}
static __device__ __forceinline__ u32x2 hget_at(int r) {
    u32x2 d;
    asm volatile("s_set_gpr_idx_on %2, gpr_idx(SRC0)\n\ts_nop 1\n\tv_mov_b32 %0, v88\n\tv_mov_b32 %1, v89\n\ts_set_gpr_idx_off"
                 : "=&v"(d.x), "=&v"(d.y) : "s"(r));
    return d;
}
static __device__ __forceinline__ void hset_at(int r, u32x2 d) {
    asm volatile("s_set_gpr_idx_on %2, gpr_idx(DST)\n\ts_nop 1\n\tv_mov_b32 v88, %0\n\tv_mov_b32 v89, %1\n\ts_set_gpr_idx_off"
                 :: "v"(d.x), "v"(d.y), "s"(r));
}

template <int S, int D> static __device__ __forceinline__ uint32_t take(uint32_t V, uint32_t u16, uint32_t la, u32x2 nul) {
    const uint32_t a = __builtin_amdgcn_bitop3_b32(V, u16, 15u, 0x54);
    asm volatile("v_mov_b64 v[%4:%4+1], v[%3:%3+1]\n\tv_cmpx_ne_u32 vcc, -16, %0\n\tds_wrxchg_rtn_b64 v[%4:%4+1], %1, %2 offset:%5\n\ts_mov_b64 exec, -1"
                 :: "v"(a), "v"(la), "v"(nul), "n"(HREG(S, D)), "n"(HREG(S, 1 - D)), "n"(S * 256) : "memory", "vcc");
    return a;
}
#define PUT(P) asm volatile("s_bitcmp1_b32 %3, %4\n\ts_cbranch_scc0 1f\n\ts_waitcnt lgkmcnt(%5)\n\tbuffer_store_dwordx4 v[%2:%2+3], %0, %1, 0 offen sc1\n" \
                 "1:\n\ts_bitcmp1_b32 %3, %4+1\n\ts_cbranch_scc0 2f\n\ts_waitcnt lgkmcnt(%5)\n\tbuffer_store_dwordx4 v[%2:%2+3], %0, %1, 0 offen " P "\n\ts_nop 1\n2:" \
                 :: "v"(ad), "s"(rsA), "n"(HREG(S, 0)), "s"(pm), "n"(2 * S), "n"(W) : "memory", "scc")
template <int S, int W, bool WT> static __device__ __forceinline__ void put(u32x4 rsA, uint32_t ad, uint32_t pm) {
    if (WT) PUT("sc1 nt"); else PUT("nt");
}

__global__ __launch_bounds__(NT, 4) __attribute__((amdgpu_num_vgpr(HB / 2))) void part_kernel(const int* __restrict__ iin, const int* __restrict__ iout,
                                                  const uint16_t* __restrict__ ang, const uint16_t* __restrict__ freq,
                                                  const u32x2* __restrict__ m, const uint16_t* __restrict__ wdown,
                                                  char* __restrict__ recs,
                                                  int T, int E, int ntile, int nstat, int ks, int lcap) {
    RTS(0)
    asm volatile("" :: "s"(iin), "s"(iout), "s"(ang), "s"(freq), "s"(m), "s"(wdown), "s"(recs), "s"(T), "s"(E), "s"(ntile), "s"(nstat), "s"(ks), "s"(lcap));
    const int tid0 = threadIdx.x;
    const int wv = __builtin_amdgcn_readfirstlane(tid0 >> 6);
    const int G = T >> 2;
    const int pos = (int)((blockIdx.x & 7) << 5) | (int)(blockIdx.x >> 3);
    float frr;
    unsigned* meta = (unsigned*)(recs - W_RECS);
    const uint32_t gbase = (LBASE + (uint32_t)lcap) * 128u;
    const uint32_t rbytes = gbase + (uint32_t)lcap * 2u;
    const __amdgpu_buffer_rsrc_t rsR = __builtin_amdgcn_make_buffer_rsrc(recs, 0, (int)rbytes, 0x00020000);
    const u32x4 rsA = {(uint32_t)(uintptr_t)recs, (uint32_t)((uintptr_t)recs >> 32) & 0xffffu, rbytes, 0x00020000u};
    const uint32_t g2 = 2u * (uint32_t)ks;
    const uint32_t far = 1u >> ((blockIdx.x >> 3) & 3u);
    const uint32_t SK = SKM + far * (SKH - SKM), SS = SSM + far * (SSH - SSM), UINC = 0x80000001u + SK * 64u;
    const uint32_t spec0 = gbase - 4u;
    auto fixed_tile = [&](int r) __attribute__((always_inline)) -> int { return r * NWG + ((pos + r * 37) & (NWG - 1)); };
    int tl0 = fixed_tile(0), tl1 = fixed_tile(1), tl2;
    uint32_t tk = 0u;
    const __amdgpu_buffer_rsrc_t rsO = __builtin_amdgcn_make_buffer_rsrc((void*)iout, 0, G * 16, 0x00020000);
    const __amdgpu_buffer_rsrc_t rsI = __builtin_amdgcn_make_buffer_rsrc((void*)iin, 0, G * 16, 0x00020000);
    const __amdgpu_buffer_rsrc_t rsG = __builtin_amdgcn_make_buffer_rsrc((void*)ang, 0, G * 8, 0x00020000);
    uint32_t t8 = (uint32_t)tid0 * 8u;
    auto load_one = [&](Raw& r, int tl, int idx) __attribute__((always_inline)) {
        const int j = idx < 4 ? idx >> 1 : idx & 1;
        const uint32_t g0 = (uint32_t)(tl * GR + j * NT);
        __builtin_amdgcn_sched_barrier(0);
        if (idx >= 4) r.a[j] = __builtin_amdgcn_raw_buffer_load_b64(rsG, t8, g0 * 8u, AUX_NT);
        else if (idx & 1) r.i[j] = __builtin_amdgcn_raw_buffer_load_b128(rsI, t8 + t8, g0 * 16u, AUX_NT);
        else r.o[j] = __builtin_amdgcn_raw_buffer_load_b128(rsO, t8 + t8, g0 * 16u, AUX_NT);
        __builtin_amdgcn_sched_barrier(0);
    };
    auto peek = [&](uint32_t at) __attribute__((always_inline)) -> uint32_t {
        return __hip_atomic_load((unsigned*)((char*)meta + at), __ATOMIC_RELAXED, __HIP_MEMORY_SCOPE_AGENT);
    };
    auto ready = [&]() __attribute__((always_inline)) {
        if (blockIdx.x != 0) {
            const uint32_t x = lds_ld(vconst<P_FLAG + 8>());
            while (peek(vconst<M_BEAT * 4>()) == x) __builtin_amdgcn_s_sleep(8);
        }
    };
    auto beat = [&]() __attribute__((always_inline)) {
        (void)__hip_atomic_fetch_add((unsigned*)((char*)meta + vconst<M_BEAT * 4>()), vconst<1>(), __ATOMIC_RELAXED, __HIP_MEMORY_SCOPE_AGENT);
    };
    auto draw = [&](uint32_t at) __attribute__((always_inline)) -> uint32_t {
        return __hip_atomic_fetch_add((unsigned*)((char*)meta + at), vconst<1>(), __ATOMIC_RELAXED, __HIP_MEMORY_SCOPE_AGENT);
    };
    uint32_t la;
    auto tab_at = [&]() __attribute__((always_inline)) -> uint32_t {
        return P_TAB + bfi(0xffu, bfi(0x3cu, t8 >> 2, t8 << 4), t8);
    };
    u32x2 nul;
    uint32_t U[2];
    {
        const uint32_t t = (uint32_t)tid0;
        la = P_BUF + ((t >> 5) << 12) + ((t & 0x18u) << 3) + ((t & 3u) << 4) + ((t & 4u) << 1);
        const uint32_t q = ((t >> 3) ^ (t >> 1 & 2u)) & 3u;
        nul.x = (0x7u >> q) << 31; nul.y = (0xbu >> q) << 31;
        U[0] = (((t >> 1) & 15u) * SS + ((t & 1u) * 64u + (t >> 5)) * SC + BPL(blockIdx.x)) * 128u + (0x80000000u - SK * 64u) + 8u - g2;
        U[1] = U[0] + 32u * SC * 128u;
    }
    auto log_line = [&](bool lg, uint32_t b) __attribute__((always_inline)) -> uint32_t {
        uint32_t lp = 0u;
        if (lg && (t8 & 0x38u) == 0u) {
            ready();
            lp = draw(vconst<M_LOG * 4>());
            __builtin_amdgcn_raw_buffer_store_b16((uint16_t)b, rsR, gbase + lp * 2u, 0, 0);
        }
        return LBASE + (uint32_t)__builtin_amdgcn_ds_bpermute((int)((t8 >> 1) & 0xe0u), (int)lp);
    };
    auto general = [&](int D, bool all, u64 sm) __attribute__((always_inline)) {
        uint32_t todo = __builtin_amdgcn_readfirstlane((uint32_t)sm | (uint32_t)(sm >> 32));
#pragma unroll 1
        while (todo) {
            const uint32_t S = (uint32_t)__builtin_ctz(todo) >> 1;
            todo &= ~(3u << (2u * S));
            pin(t8); pin(la);
            const uint32_t V = lds_ld(P_TAB + (t8 & ~63u) + S * 4u), g = V & 15u;
            const uint32_t col = S * 128u + ((t8 >> 1) & 0x60u) + (t8 >> 8);
            const bool act = all ? V != 0xffffffffu : (V & 0x80000009u) == 9u;
            const int hr = HREG((int)S, D) - HB;
            uint32_t ln = log_line(act && g > 8u + NTAIL, col);
            if (act) {
                const u32x2 N = *(const LDS u32x2*)(uintptr_t)(la + S * 256u);
                *(LDS u32x2*)(uintptr_t)(la + S * 256u) = nul;
                lds_st(P_TAB + (t8 & ~63u) + S * 4u, vconst<0xffffffffu>());
                if (!(g & 1u)) hset_at(hr, N);
                else {
                    uint32_t a, st;
                    if (g < 8u) { a = (V & ~15u) + ((t8 & 0x38u) << 1); st = 8u; }
                    else {
                        if (g <= 8u + NTAIL) ln = TBASE + ((g - 8u) >> 1) * TPL + col * TST + blockIdx.x;
                        a = ln * 128u + (la & 0x38u); st = 64u;
                    }
                    __builtin_amdgcn_raw_buffer_store_b64(hget_at(hr), rsR, a, 0, AUX_SC1);
                    __builtin_amdgcn_raw_buffer_store_b64(N, rsR, a + st, 0, AUX_SC1);
                }
            }
        }
    };
    unsigned epoch = 0;
    u64 pb2 = 0ull;
    u64 fm = 0ull, sm = 0ull;
    auto look = [&](u32x2 c) __attribute__((always_inline)) {
        uint32_t V[2];
        const bool late = __builtin_amdgcn_ballot_w64(((U[0] | U[1]) & 8u) != 0u) != 0ull;
        epoch++;
        asm volatile("v_mov_b32 %0, -1\n\tv_mov_b32 %1, -1\n\t"
                     "v_cmpx_le_u32 %6, %7, %2\n\tv_mov_b32 %0, %4\n\tv_add_u32 %4, %9, %4\n\tv_subrev_u32 %2, %8, %2\n\ts_mov_b64 exec, -1\n\t"
                     "v_cmpx_le_u32 vcc, %8, %3\n\tv_mov_b32 %1, %5\n\tv_add_u32 %5, %9, %5\n\tv_subrev_u32 %3, %8, %3\n\t"
                     "s_or_b64 %6, %6, exec\n\ts_mov_b64 exec, -1"
                     : "=&v"(V[0]), "=&v"(V[1]), "+v"(c.x), "+v"(c.y), "+v"(U[0]), "+v"(U[1]), "=&s"(fm) : "s"(CHV), "n"(CHV), "s"(UINC) : "vcc", "scc");
        *(LDS u32x2*)(uintptr_t)(P_CNT + t8) = c;
        pin(t8);
        const uint32_t tw = tab_at();
        lds_st(tw, V[0]);
        lds_st(tw + 64u, V[1]);
        if (wv == 1) lds_st(P_CNT + NB * 4 - 256 + (t8 >> 1), vconst<0x40000000u>());
        pb2 = __builtin_amdgcn_ballot_w64((int)(V[0] & V[1]) >= 0);
        sm = 0ull;
        if (__builtin_expect(late, 0)) {
            sm = __builtin_amdgcn_ballot_w64((V[0] & 0x80000009u) == 9u || (V[1] & 0x80000009u) == 9u);
#pragma unroll
            for (int e = 0; e < 2; e++)
                if ((V[e] & 0x8000000fu) == 9u + NTAIL) U[e] -= 2u * UINC;
        }
    };
    u32x4 Va, Vb;

    Raw raw[PD];
    {
        const int tid = tid0;
        asm volatile("" ::: "v88", "v127");
        typedef const __attribute__((address_space(4))) u32x4* cptr;
        u32x4 wd0 = ((cptr)(uintptr_t)wdown)[0], wd1 = ((cptr)(uintptr_t)wdown)[1];
        __builtin_amdgcn_sched_barrier(0);
        if (tid == 128) tk = peek(vconst<M_BEAT * 4>());
        if (blockIdx.x == 0 && wv == 3 && (tid & 63) < M_BEAT / 64) {
            __hip_atomic_store(meta + (tid & 63) * 64, 0u, __ATOMIC_RELAXED, __HIP_MEMORY_SCOPE_AGENT);
            __hip_atomic_store(meta + (tid & 63) * 64 + 1, 0u, __ATOMIC_RELAXED, __HIP_MEMORY_SCOPE_AGENT);
        }
        const __amdgpu_buffer_rsrc_t rsE = __builtin_amdgcn_make_buffer_rsrc((void*)m, 0, E * 8, 0x00020000);
        u32x2 mv[5];
        mv[4] = __builtin_amdgcn_raw_buffer_load_b64(rsE, (t8 & 511u) * (uint32_t)(E >> 6), 0, 0);
#pragma unroll
        for (int q = 0; q < 4; q++)
            mv[q] = __builtin_amdgcn_raw_buffer_load_b64(rsE, t8 & 511u, ((blockIdx.x >> 2) * 256u + (blockIdx.x & 3u) * 16u + (uint32_t)wv + 64u * q) * 512u, AUX_NT);
#pragma unroll
        for (int d = 0; d < MDL; d++) load_one(raw[1], tl0, d);
        asm volatile("" : "+s"(wd0), "+s"(wd1));
        const u32x2 ci = {CINIT, CINIT};
        *(LDS u32x2*)(uintptr_t)(P_CNT + tid * 8) = ci;
        if (tid < 64) lds_st(P_CNT + (NB + tid) * 4, 0x40000000u);
        if (tid < 8 && tid != 2) lds_st(P_FLAG + tid * 4, tid == 0 ? 0xffffffffu : 0u);
        if (tid == 128) lds_st(P_FLAG + 8, tk);
        const uint32_t lane = (uint32_t)tid & 63u;
        int e0[4];
        {
            float w[16];
#pragma unroll
            for (int i = 0; i < 4; i++) {
                w[2 * i] = bf2f(wd0[i] & 0xffffu); w[2 * i + 1] = bf2f(wd0[i] >> 16);
                w[8 + 2 * i] = bf2f(wd1[i] & 0xffffu); w[8 + 2 * i + 1] = bf2f(wd1[i] >> 16);
            }
            auto dot = [&](u32x2 v, int c) __attribute__((always_inline)) -> f32x2 {
                const f32x2 w0 = {w[c], w[c + 1]}, w1 = {w[4 + c], w[5 + c]}, w2 = {w[8 + c], w[9 + c]}, w3 = {w[12 + c], w[13 + c]};
                return w0 * bf2f(v.x & 0xffffu) + w1 * bf2f(v.x >> 16) + w2 * bf2f(v.y & 0xffffu) + w3 * bf2f(v.y >> 16);
            };
#pragma unroll
            for (int cp = 0; cp < 2; cp++) {
                if (cp == 1) {
#pragma unroll
                    for (int d = MDL; d < MDM; d++) load_one(raw[1], tl0, d);
                }
                if (blockIdx.x >= SLOTI / 64) continue;
                const f32x2 sd = dot(mv[4], 2 * cp);
#pragma unroll
                for (int e = 0; e < 2; e++) {
                    const int ex = (int)(wave_max(__float_as_uint(sd[e]) & 0x7fffffffu) >> 23);
                    e0[2 * cp + e] = __builtin_amdgcn_readfirstlane(ex == 0 ? 0 : min(max(ex - 129, -100), 100));
                }
                const f32x2 sc = {__uint_as_float((uint32_t)(127 - e0[2 * cp]) << 23), __uint_as_float((uint32_t)(127 - e0[2 * cp + 1]) << 23)};
                u32x4 o;
                u16x2 mx = {0, 0};
                f32x2 sq = {0.f, 0.f}, sg = {0.f, 0.f};
#pragma unroll
                for (int q = 0; q < 4; q++) {
                    const f32x2 y = dot(mv[q], 2 * cp) * sc;
                    const uint32_t hb = pk_bf16(y.x, y.y);
                    const f32x2 x = {__uint_as_float(hb << 16), __uint_as_float(hb & 0xffff0000u)};
                    o[q] = __builtin_bit_cast(uint32_t, __builtin_amdgcn_cvt_pkrtz(x.x, x.y));
                    mx = __builtin_elementwise_max(mx, __builtin_bit_cast(u16x2, hb & 0x7fff7fffu));
                    sq += x * x;
                    sg += x;
                }
                *(LDS u32x4*)(uintptr_t)(P_BUF + (cp * 64u + lane) * 272u + wv * 16) = o;
#pragma unroll
                for (int e = 0; e < 2; e++) {
                    const u32x4 st = {wave_max(mx[e]), __float_as_uint(wave_sum(sq[e])), __float_as_uint(wave_sum(sg[e])), 0u};
                    if (lane == 0u) *(LDS u32x4*)(uintptr_t)(P_LST + ((2 * cp + e) * 16 + wv) * 16) = st;
                }
            }
        }
        __builtin_amdgcn_sched_barrier(0);
        const uint16_t fq = freq[0];
#pragma unroll
        for (int d = MDM; d < 6; d++) load_one(raw[1], tl0, d);
#pragma unroll
        for (int d = 0; d < 6; d++)
            if (((LPOS >> (4 * d)) & 15) < 4) load_one(raw[0], tl1, d);
        __syncthreads();
        u32x4 mo[2];
#pragma unroll
        for (int cp = 0; cp < 2; cp++) mo[cp] = *(const LDS u32x4*)(uintptr_t)(P_BUF + (cp * 64u + (t8 >> 7)) * 272u + ((t8 + t8) & 0xf0u));
        if (blockIdx.x == 0 && wv == 3) asm volatile("s_waitcnt vmcnt(0)" ::: "memory");
        __syncthreads();
#pragma unroll
        for (int s = 0; s < 16; s++) *(LDS u32x2*)(uintptr_t)(la + s * 256) = nul;
        asm volatile("" : "+v"(raw[1].o[0]), "+v"(raw[1].o[1]), "+v"(raw[1].i[0]), "+v"(raw[1].i[1]), "+v"(raw[1].a[0]), "+v"(raw[1].a[1]));
        asm volatile("s_nop 0\n\tv_readfirstlane_b32 %0, %1" : "=s"(frr) : "v"(bf2f(fq) * 0.15915494309189535f));
        if (blockIdx.x < SLOTI / 64) {
            const __amdgpu_buffer_rsrc_t rsM = __builtin_amdgcn_make_buffer_rsrc((void*)(meta + W_MDC / 4), 0, (int)(128u * SLOTI * 4u), 0x00020000);
#pragma unroll
            for (int cp = 0; cp < 2; cp++) {
                const uint32_t row = cp * 64u + (t8 >> 7), c16 = (t8 + t8) & 0xf0u;
                __builtin_amdgcn_raw_buffer_store_b128(mo[cp], rsM, row * (SLOTI * 4u) + c16 + blockIdx.x * 256u, 0, AUX_SC1);
            }
            if (wv == 0) {
                const u32x4 st = *(const LDS u32x4*)(uintptr_t)(P_LST + t8 * 2u);
                const float sm1 = row_sum(__uint_as_float(st.z));
                const u32x2 r = {row_max(st.x) | (row_max(st.x ? 0xffffu - st.x : 0u) << 16), __float_as_uint(row_sum(__uint_as_float(st.y)))};
                if ((lane & 15u) == 15u) {
                    ((u32x2*)(meta + W_STAT / 4))[(lane >> 4) * NWG + blockIdx.x] = r;
                    meta[W_SUM / 4 + (lane >> 4) * NWG + blockIdx.x] = __float_as_uint(sm1);
                }
                if (blockIdx.x == 0 && lane < 4u) meta[M_E0 + lane] = (uint32_t)(lane == 0u ? e0[0] : lane == 1u ? e0[1] : lane == 2u ? e0[2] : e0[3]);
            }
        }
    }
    pin(la);
    RTS(5)

    uint32_t w[8], aw[8], xv[8];
    u64 nm[8];
    auto later2 = [&](int k) __attribute__((always_inline)) {
        uint32_t x0, x1;
        asm volatile("v_lshrrev_b32 %0, 4, %2\n\tv_lshrrev_b32 %1, 4, %5\n\tv_bitop3_b32 %0, %2, %0, 12 bitop3:0x72\n\tv_bitop3_b32 %1, %5, %1, 12 bitop3:0x72\n\t"
                     "s_not_b64 exec, %8\n\tds_write_b32 %2, %3 offset:%10\n\tds_or_b32 %0, %4 offset:%10\n\t"
                     "s_not_b64 exec, %9\n\tds_write_b32 %5, %6 offset:%10\n\tds_or_b32 %1, %7 offset:%10\n\ts_mov_b64 exec, -1"
                     : "=&v"(x0), "=&v"(x1)
                     : "v"(aw[k]), "v"(w[k]), "v"(xv[k]), "v"(aw[k + 1]), "v"(w[k + 1]), "v"(xv[k + 1]), "s"(nm[k]), "s"(nm[k + 1]), "n"(P_BUF) : "memory", "scc");
    };
    auto drain = [&]() __attribute__((always_inline)) {
        if (wv == 1) {
            const uint32_t q = (epoch + 1u) & 3u;
            const uint32_t n = __builtin_amdgcn_readfirstlane(lds_ld(vconst<P_FLAG + 16>() + q * 4u));
            if (__builtin_expect(n != 0u, 0)) {
                pin(t8);
                const uint32_t l = (t8 >> 3) & 63u;
                if (l < min(n, (uint32_t)LCAP)) {
                    const u32x4 e = *(const LDS u32x4*)(uintptr_t)(P_LST + (q * LCAP + l) * 16u);
                    lds_st(P_BUF + e.x, e.y);
                    lds_or(P_BUF + slot_x(e.x), e.z);
                }
                lds_st(vconst<P_FLAG + 16>() + q * 4u, vconst<0>());
            }
        }
    };
    auto again = [&](int D) __attribute__((always_inline)) {
        do {
            general(D, true, fm);
            __syncthreads();
            later2(0); later2(2); later2(4); later2(6);
            drain();
#pragma unroll
            for (int k = 0; k < 8; k++) {
                pin(aw[k]);
                bool due = false;
                if (aw[k] & 0x80000000u) {
                    xv[k] -= CHV;
                    if (xv[k] < CHV) {
                        const uint32_t f = (aw[k] >> 16) & 0x3ffu;
                        aw[k] = slot_w(aw[k] & 0xffffu, xv[k]);
                        xv[k] = f << ((xv[k] >> 8) & 31u);
                        due = true;
                    } else lds_st(vconst<P_FLAG>(), epoch + 1u);
                }
                nm[k] = ~__builtin_amdgcn_ballot_w64(due);
            }
            look(*(const LDS u32x2*)(uintptr_t)(P_CNT + t8));
            asm volatile("s_waitcnt lgkmcnt(0)" ::: "memory");
            __syncthreads();
        } while (__builtin_amdgcn_readfirstlane(lds_ld(vconst<P_FLAG>())) == epoch);
    };
    auto round = [&](auto DD, auto LL, Raw& rc, Raw& rn, int rd) __attribute__((always_inline)) {
        constexpr int D = decltype(DD)::value;
        constexpr bool LAST = decltype(LL)::value == 1, FIRST = decltype(LL)::value == 2;
        const int ng = min(GR, G - tl0 * GR);
        auto hook = [&](int p) __attribute__((always_inline)) {
#pragma unroll
            for (int q = 0; q < 6; q++)
            {
                const int lp = (LPOS >> (4 * q)) & 15;
                if (!LAST && lp < 4 && lp == p) load_one(rc, tl2, q);
                if (!LAST && lp >= 4 && (lp < 8 || lp >= 12 ? lp : lp - 8) == p) load_one(rn, tl1, q);
            }
        };
        uint32_t wn[8], ca[8], px[4], cv[8];
        float mx;
        float cs[2], cm[2] = {0.f, 0.f};
        auto convA = [&](int q) __attribute__((always_inline)) {
            if (LAST) return;
            const uint32_t a = rc.a[q >> 1][q & 1];
            const f32x2 av = {__uint_as_float(a << 16), __uint_as_float(a & 0xffff0000u)};
            f32x2 rv;
            asm("v_pk_mul_f32 %0, %1, %2 op_sel_hi:[1,0]" : "=v"(rv) : "v"(av), "s"((u64)__float_as_uint(frr)));
            mx = __builtin_elementwise_maximum(__builtin_elementwise_maximum(__builtin_fabsf(rv.x), __builtin_fabsf(rv.y)), (q & 1) ? mx : 0.f);
            cs[0] = __builtin_amdgcn_cosf(rv.x); cs[1] = __builtin_amdgcn_cosf(rv.y);
            asm volatile("" : "+v"(mx), "+v"(cs[0]), "+v"(cs[1]));
            if (FIRST) {
                const float c0 = fminf(fmaxf(cs[0], -1.f), 1.f), c1 = fminf(fmaxf(cs[1], -1.f), 1.f);
                cm[0] += c0 + c1; cm[1] += c0 * c0 + c1 * c1;
                if (q == 3 && wv == 4) {
                    const float a0 = wave_sum(cm[0]), a1 = wave_sum(cm[1]);
                    if ((t8 & 0x1f0u) == 0u) meta[W_SUM / 4 + (4 + ((t8 >> 3) & 1u)) * NWG + blockIdx.x] = __float_as_uint((t8 & 8u) ? a1 : a0);
                }
            }
            __builtin_amdgcn_sched_barrier(0);
        };
        auto convB = [&](int k) __attribute__((always_inline)) {
            if (LAST) return;
            const uint32_t o = rc.o[k >> 2][k & 3], i = rc.i[k >> 2][k & 3];
            wn[k] = (__builtin_amdgcn_alignbit(bfi(63u, o, o >> 5), __float_as_uint(cs[k & 1]), 30u) << 14) | (i >> 6);
            ca[k] = (bfi(63u, i, o) << 2) & 0x1ffcu;
            pin(wn[k]); pin(ca[k]);
            __builtin_amdgcn_sched_barrier(0);
        };
        auto convD = [&](int q) __attribute__((always_inline)) {
            if (LAST) return;
            asm volatile("v_cvt_pk_f16_f32 %0, |%1|, |%2|\n\tv_pk_mul_f16 %0, %0, %3 op_sel_hi:[1,0]" : "=v"(px[q]) : "v"(cs[0]), "v"(cs[1]), "s"(0x3ffu));
        };
        uint32_t cst;
        auto check = [&](int j) __attribute__((always_inline)) {
            if (LAST) return;
            if (__builtin_expect((__builtin_amdgcn_ballot_w64(!(mx <= RMAXREV)) | (u64)(uint32_t)(GR - ng)) != 0ull, 0)) {
                pin(t8);
                asm volatile("" : "+v"(rc.a[j]));
                const int lg = j * NT + (int)(t8 >> 3);
#pragma unroll
                for (int k = 0; k < 4; k++) {
                    const uint32_t a = (k & 1) ? (rc.a[j][k >> 1] >> 16) : (rc.a[j][k >> 1] & 0xffffu);
                    const bool nrm = __builtin_fabsf(frr * bf2f(a)) <= RMAXREV;
                    if (!(lg < ng && nrm)) ca[j * 4 + k] = NB * 4u + ((t8 >> 1) & 0xfcu);
                    if (lg < ng && !nrm) {
                        ready();
                        __builtin_amdgcn_raw_buffer_store_b32((uint32_t)((tl0 * GR + lg) * 4 + k), rsR, spec0 - draw(vconst<M_LOG * 4 + 4>()) * 4u, 0, 0);
                    }
                }
            }
            cst = vconst<CSTEP>();
        };
        auto ticket = [&](int k) __attribute__((always_inline)) {
            if (LAST) return;
            cv[k] = lds_add(P_CNT + ca[k], cst);
            __builtin_amdgcn_sched_barrier(0);
        };
        auto insert = [&](int j) __attribute__((always_inline)) {
            const int k0 = 4 * j;
            const bool odd = __builtin_amdgcn_ballot_w64(max(max(cv[k0], cv[k0 + 1]), max(cv[k0 + 2], cv[k0 + 3])) >= 2u * CHV) != 0ull;
#pragma unroll
            for (int k = k0; k < k0 + 4; k += 2) {
                uint32_t x0, x1;
                asm volatile("v_lshrrev_b32 %6, 11, %8\n\tv_lshrrev_b32 %7, 11, %9\n\t"
                             "v_bitop3_b32 %6, %6, %10, 60 bitop3:0x28\n\tv_bitop3_b32 %7, %7, %11, 60 bitop3:0x28\n\t"
                             "v_lshl_or_b32 %0, %10, 4, %6\n\tv_lshl_or_b32 %1, %11, 4, %7\n\t"
                             "v_lshlrev_b32_sdwa %2, %8, %12 dst_sel:DWORD dst_unused:UNUSED_PAD src0_sel:BYTE_1 src1_sel:WORD_0\n\t"
                             "v_lshlrev_b32_sdwa %3, %9, %12 dst_sel:DWORD dst_unused:UNUSED_PAD src0_sel:BYTE_1 src1_sel:WORD_1\n\t"
                             "v_bitop3_b32 %6, %0, %10, 12 bitop3:0x72\n\tv_bitop3_b32 %7, %1, %11, 12 bitop3:0x72\n\t"
                             "v_cmpx_gt_u32 %4, %15, %8\n\tds_write_b32 %0, %13 offset:%16\n\tds_or_b32 %6, %2 offset:%16\n\ts_mov_b64 exec, -1\n\t"
                             "v_cmpx_gt_u32 %5, %15, %9\n\tds_write_b32 %1, %14 offset:%16\n\tds_or_b32 %7, %3 offset:%16\n\ts_mov_b64 exec, -1"
                             : "=&v"(aw[k]), "=&v"(aw[k + 1]), "=&v"(xv[k]), "=&v"(xv[k + 1]), "=&s"(nm[k]), "=&s"(nm[k + 1]), "=&v"(x0), "=&v"(x1)
                             : "v"(cv[k]), "v"(cv[k + 1]), "v"(ca[k]), "v"(ca[k + 1]), "v"(px[k >> 1]), "v"(wn[k]), "v"(wn[k + 1]), "s"(CHV), "n"(P_BUF) : "memory");
                w[k] = wn[k]; w[k + 1] = wn[k + 1];
                if (__builtin_expect(odd, 0)) {
#pragma unroll
                    for (int q = k; q < k + 2; q++) {
                        nm[q] |= __builtin_amdgcn_ballot_w64(cv[q] >= 2u * CHV);
                        if (cv[q] >= 2u * CHV && cv[q] < 0x40000000u) {
                            const uint32_t lq = (epoch + 2u) & 3u;
                            uint32_t p = LCAP;
                            if (cv[q] < 3u * CHV) p = lds_add(vconst<P_FLAG + 16>() + lq * 4u, vconst<1>());
                            if (p < LCAP) {
                                const u32x2 e = {aw[q], w[q]};
                                *(LDS u32x2*)(uintptr_t)(P_LST + (lq * LCAP + p) * 16u) = e;
                                lds_st(P_LST + (lq * LCAP + p) * 16u + 8u, xv[q]);
                            } else {
                                aw[q] = ca[q] | ((q & 1) ? (px[q >> 1] & 0xffff0000u) : (px[q >> 1] << 16)) | 0x80000000u;
                                xv[q] = cv[q] - CHV;
                                lds_st(vconst<P_FLAG>(), epoch);
                            }
                        }
                    }
                }
            }
        };
        constexpr int P0 = D ? 0 : 3, P1 = D ? 1 : 2, P2 = D ? 2 : 1, P3 = D ? 3 : 0;
        const uint32_t fl = lds_ld(vconst<P_FLAG>());
        if (!LAST) asm volatile("" : "+v"(rc.o[0]), "+v"(rc.o[1]), "+v"(rc.i[0]), "+v"(rc.i[1]), "+v"(rc.a[0]), "+v"(rc.a[1]));
        if (!LAST && (rd + 4 >= nstat || tl1 >= ntile) && wv == 2) {
            pin(tk);
            if ((t8 & 511u) == 0u) {
                if (tl1 >= ntile) {
                    if (rd + 3 <= nstat) ready();
                    (void)draw(vconst<M_MD * 4>());
                } else if (rd + 4 == nstat) tk = peek(vconst<M_BEAT * 4>());
                else {
                    if (rd + 3 == nstat) {
                        if (tk == lds_ld(vconst<P_FLAG + 8>())) ready();
                        tk = 0u;
                    }
                    lds_st(vconst<P_FLAG + 4>(), tk);
                    if ((int)tk < ntile - nstat * NWG) tk = draw(vconst<M_TILE * 4>());
                }
            }
        }
        if (!LAST && blockIdx.x == 0 && wv == 3 && (t8 & 511u) == 0u) beat();
        hook(12);
        pin(t8);
        const uint32_t tr = P_TAB + (t8 & ~63u), u16 = (t8 + t8) & 0x70u;
        convA(0);
        convB(0);
        const bool ag = __builtin_amdgcn_readfirstlane(fl) == epoch;
        if (!FIRST && __builtin_expect(LAST || ag || sm != 0ull, 0)) {
            if (ag) again(D);
            general(D, false, sm);
            Va = *(const LDS u32x4*)(uintptr_t)(P_TAB + (t8 & ~63u) + P0 * 16);
            Vb = *(const LDS u32x4*)(uintptr_t)(P_TAB + (t8 & ~63u) + P1 * 16);
            asm volatile("" : "+v"(Va), "+v"(Vb));
        }
        uint32_t pq = (uint32_t)pb2 | (uint32_t)(pb2 >> 32);
        pq = (pq | (pq >> 1)) & 0x55555555u;
        pq = __builtin_amdgcn_readfirstlane(pq + (pq & (far * NEARM)));
        uint32_t ad[8];
        if (!FIRST) ad[0] = take<4 * P0 + 0, D>(Va[0], u16, la, nul);
        convB(1);
        if (!FIRST) ad[1] = take<4 * P0 + 1, D>(Va[1], u16, la, nul);
        convD(0);
        convA(1);
        if (!FIRST) ad[2] = take<4 * P0 + 2, D>(Va[2], u16, la, nul);
        convB(2);
        hook(13);
        if (!FIRST) ad[3] = take<4 * P0 + 3, D>(Va[3], u16, la, nul);
        convB(3);
        if (!FIRST) put<4 * P0 + 0, LAST ? 3 : 3, LAST>(rsA, ad[0], pq);
        if (!FIRST) ad[4] = take<4 * P1 + 0, D>(Vb[0], u16, la, nul);
        convD(1);
        check(0);
        ticket(0);
        hook(4);
        if (!FIRST) put<4 * P0 + 1, LAST ? 3 : 4, LAST>(rsA, ad[1], pq);
        if (!FIRST) ad[5] = take<4 * P1 + 1, D>(Vb[1], u16, la, nul);
        convA(2);
        ticket(1);
        if (!FIRST) put<4 * P0 + 2, LAST ? 3 : 5, LAST>(rsA, ad[2], pq);
        if (!FIRST) ad[6] = take<4 * P1 + 2, D>(Vb[2], u16, la, nul);
        convB(4);
        ticket(2);
        if (!FIRST) put<4 * P0 + 3, LAST ? 3 : 6, LAST>(rsA, ad[3], pq);
        if (!FIRST) Va = *(const LDS u32x4*)(uintptr_t)(tr + P2 * 16);
        __builtin_amdgcn_sched_barrier(0);
        if (!FIRST) ad[7] = take<4 * P1 + 3, D>(Vb[3], u16, la, nul);
        convB(5);
        ticket(3);
        SETP(2);
        if (!FIRST) put<4 * P1 + 0, LAST ? 4 : 8, LAST>(rsA, ad[4], pq);
        if (!FIRST) ad[0] = take<4 * P2 + 0, D>(Va[0], u16, la, nul);
        convD(2);
        convA(3);
        hook(5);
        if (!FIRST) put<4 * P1 + 1, LAST ? 4 : 7, LAST>(rsA, ad[5], pq);
        if (!FIRST) ad[1] = take<4 * P2 + 1, D>(Va[1], u16, la, nul);
        convB(6);
        if (!FIRST) put<4 * P1 + 2, LAST ? 4 : 6, LAST>(rsA, ad[6], pq);
        if (!FIRST) ad[2] = take<4 * P2 + 2, D>(Va[2], u16, la, nul);
        convB(7);
        if (!FIRST) put<4 * P1 + 3, LAST ? 3 : 4, LAST>(rsA, ad[7], pq);
        if (!FIRST) Vb = *(const LDS u32x4*)(uintptr_t)(tr + P3 * 16);
        __builtin_amdgcn_sched_barrier(0);
        if (!FIRST) ad[3] = take<4 * P2 + 3, D>(Va[3], u16, la, nul);
        convD(3);
        check(1);
        ticket(4);
        SETP(1);
        hook(6);
        if (!FIRST) put<4 * P2 + 0, LAST ? 4 : 5, LAST>(rsA, ad[0], pq);
        if (!FIRST) ad[4] = take<4 * P3 + 0, D>(Vb[0], u16, la, nul);
        ticket(5);
        if (!FIRST) put<4 * P2 + 1, LAST ? 4 : 6, LAST>(rsA, ad[1], pq);
        if (!FIRST) ad[5] = take<4 * P3 + 1, D>(Vb[1], u16, la, nul);
        ticket(6);
        if (!FIRST) put<4 * P2 + 2, LAST ? 4 : 7, LAST>(rsA, ad[2], pq);
        if (!FIRST) ad[6] = take<4 * P3 + 2, D>(Vb[2], u16, la, nul);
        ticket(7);
        SETP(0);
        if (!FIRST) put<4 * P2 + 3, LAST ? 3 : 7, LAST>(rsA, ad[3], pq);
        if (!FIRST) ad[7] = take<4 * P3 + 3, D>(Vb[3], u16, la, nul);
        hook(7);
        SETP(3);
        asm volatile("s_waitcnt lgkmcnt(0)" ::: "memory");
        __syncthreads();
        u32x2 cn;
        uint32_t tv;
        if (!LAST) { cn = *(const LDS u32x2*)(uintptr_t)(P_CNT + t8); tv = lds_ld(vconst<P_FLAG + 4>()); }
        __builtin_amdgcn_sched_barrier(0);
        if (!FIRST) later2(0);
        if (!FIRST) put<4 * P3, 15, LAST>(rsA, ad[4], pq);
        if (!FIRST) later2(2);
        if (!FIRST) put<4 * P3 + 1, 15, LAST>(rsA, ad[5], pq);
        if (!FIRST) drain();
        if (!LAST) {
            tl2 = rd + 2 < nstat ? fixed_tile(rd + 2) : nstat * NWG + (int)__builtin_amdgcn_readfirstlane(tv);
            look(cn);
        }
        hook(0);
        SETP(2);
        if (!FIRST) later2(4);
        if (!FIRST) put<4 * P3 + 2, 15, LAST>(rsA, ad[6], pq);
        if (!FIRST) later2(6);
        if (!FIRST) put<4 * P3 + 3, 15, LAST>(rsA, ad[7], pq);
        if (!LAST) {
            hook(1);
            SETP(1);
            insert(0);
            hook(2);
            SETP(0);
            pin(t8);
            Va = *(const LDS u32x4*)(uintptr_t)(P_TAB + (t8 & ~63u) + P3 * 16);
            Vb = *(const LDS u32x4*)(uintptr_t)(P_TAB + (t8 & ~63u) + P2 * 16);
            __builtin_amdgcn_sched_barrier(0);
            insert(1);
            hook(3);
            SETP(3);
            asm volatile("s_waitcnt lgkmcnt(0)" ::: "memory");
            __syncthreads();
            tl0 = tl1; tl1 = tl2;
        }
    };
#define HMV(A, B) "v_mov_b64 v[" #A ":" #A "+1], v[" #B ":" #B "+1]\n\t"
#define HMV4(A) HMV(A + 8, A + 6) HMV(A + 6, A + 4) HMV(A + 4, A + 2) HMV(A + 2, A)
    round(IC<1>(), IC<2>(), raw[1], raw[0], 0);
    int rd = 1;
#pragma unroll 1
    for (; tl1 < ntile; rd += 2) {
        round(IC<0>(), IC<0>(), raw[0], raw[1], rd);
        round(IC<1>(), IC<0>(), raw[1], raw[0], rd + 1);
    }
    if (tl0 >= ntile) asm volatile(HMV4(88) HMV4(98) HMV4(108) HMV4(118));
    asm volatile("" : "+s"(tl0));
    if (tl0 < ntile) round(IC<0>(), IC<0>(), raw[0], raw[1], rd);
    RTS(1)
    round(IC<1>(), IC<1>(), raw[1], raw[0], 0);
    if (__builtin_amdgcn_readfirstlane(lds_ld(P_FLAG + 16 + ((epoch + 2u) & 3u) * 4u)) != 0u) {
        asm volatile("s_waitcnt lgkmcnt(0)" ::: "memory");
        __syncthreads();
        look(*(const LDS u32x2*)(uintptr_t)(P_CNT + t8));
        general(0, true, fm);
        __syncthreads();
        drain();
    }
    {
        uint32_t* tcnt = meta + W_TCNT / 4;
        const u32x2 c = *(const LDS u32x2*)(uintptr_t)(P_CNT + t8);
        const uint32_t tw = tab_at();
#pragma unroll
        for (int e = 0; e < 2; e++) {
            const uint32_t g = U[e] & 15u;
            lds_st(tw + e * 64u, g >= 8u ? 0x80000000u : ((U[e] + ((g & 1u) ? 0u : SK * 64u)) & 0x7fffff81u) | (((7u - g) << 1) & 12u));
            *(LDS uint8_t*)(uintptr_t)(P_CNT + (t8 & ~511u) + (((tw - P_TAB) >> 2) & 127u) + e * 16u) =
                (uint8_t)(g < 8u ? 0u : ((g - 8u) >> 1) + (((c[e] + 2u * CSTEP) >> 15) << 2) + (g & 1u) * 0x50u);
        }
        asm volatile("s_waitcnt lgkmcnt(0)" ::: "memory");
        __syncthreads();
        const uint32_t u16 = (t8 + t8) & 0x70u;
        u32x4 En[4];
#pragma unroll
        for (int p = 0; p < 4; p++) En[p] = *(const LDS u32x4*)(uintptr_t)(P_TAB + (t8 & ~63u) + p * 16u);
        const u32x4 cb = *(const LDS u32x4*)(uintptr_t)(P_CNT + (t8 & ~511u) + ((t8 >> 2) & 0x70u));
        u32x2 N[16];
#pragma unroll
        for (int s = 0; s < 16; s++) N[s] = *(const LDS u32x2*)(uintptr_t)(la + s * 256u);
        if (u16 == 0u) *(u32x4*)(tcnt + blockIdx.x * 512u + (t8 >> 4)) = cb;
        const uint32_t sg = P_BUF + ((t8 & ~63u) << 4);
        uint32_t ps;
        auto rest = [&](auto SL) __attribute__((always_inline)) {
            constexpr int s = decltype(SL)::value;
            const uint32_t en = En[s >> 2][s & 3], by = cb[s >> 2] >> ((s & 3) * 8);
            const u32x2 Hr = hget<HREG(s, 0)>();
            const uint32_t at = ps + ((la & 0x38u) ^ ((s & 3) << 4));
            *(LDS u32x2*)(uintptr_t)at = Hr;
            *(LDS u32x2*)(uintptr_t)(at + (by & 0x40u)) = N[s];
            ps += (by & 0x3cu) << 2;
            const u32x2 Hs = (en & 1u) ? Hr : nul;
            const u32x4 d = {Hs.x, Hs.y, N[s].x, N[s].y};
            uint32_t a = __builtin_amdgcn_bitop3_b32(en, u16, 15u, 0x54);
            {
                const u32x4 rs_ = rsA;
                const uint32_t far_ = ((NEARM >> (2 * s)) & 1u) ? far : 0u;
                asm volatile("s_cmp_eq_u32 %3, 0\n\ts_cbranch_scc0 1f\n\tbuffer_store_dwordx4 %0, %1, %2, 0 offen sc1\n\ts_branch 2f\n"
                             "1:\n\tbuffer_store_dwordx4 %0, %1, %2, 0 offen sc1 nt\n2:\n\ts_nop 1" :: "v"(d), "v"(a), "s"(rs_), "s"(far_) : "memory", "scc");
            }
            if (__builtin_expect(__builtin_amdgcn_ballot_w64((en & 12u) != 0u) != 0ull, 0)) {
                const u32x4 dn = {nul.x, nul.y, nul.x, nul.y};
#pragma unroll 1
                for (uint32_t k = (en >> 2) & 3u; k > 0u; k--) {
                    a += SK * 128u;
                    __builtin_amdgcn_raw_buffer_store_b128(dn, rsR, a, 0, AUX_SC1);
                }
            }
        };
        auto pack = [&](int p) __attribute__((always_inline)) {
            const uint32_t a = (SBASE + ((((t8 >> 1) & 0x60u) + (t8 >> 8)) * 2u + p) * (NWG * 9u) + blockIdx.x * 9u) * 128u + u16;
            asm volatile("" ::: "memory");
#pragma unroll 1
            for (uint32_t q = 0; __builtin_amdgcn_ballot_w64(sg + q < ps) != 0ull; q += 256u) {
                const u32x4 d0 = *(const LDS u32x4*)(uintptr_t)(sg + q + u16), d1 = *(const LDS u32x4*)(uintptr_t)(sg + q + 128u + u16);
                __builtin_amdgcn_raw_buffer_store_b128(d0, rsR, sg + q < ps ? a + q : 0xffffff00u, 0, AUX_SC1);
                __builtin_amdgcn_raw_buffer_store_b128(d1, rsR, sg + q + 128u < ps ? a + q + 128u : 0xffffff00u, 0, AUX_SC1);
            }
            asm volatile("" ::: "memory");
        };
        asm volatile("" : "+v"(N[0]), "+v"(N[1]), "+v"(N[2]), "+v"(N[3]), "+v"(N[4]), "+v"(N[5]), "+v"(N[6]), "+v"(N[7]) :: "memory");
        asm volatile("" : "+v"(N[8]), "+v"(N[9]), "+v"(N[10]), "+v"(N[11]), "+v"(N[12]), "+v"(N[13]), "+v"(N[14]), "+v"(N[15]) :: "memory");
        ps = sg;
        rest(IC<0>()); rest(IC<1>()); rest(IC<2>()); rest(IC<3>()); rest(IC<4>()); rest(IC<5>()); rest(IC<6>()); rest(IC<7>());
        pack(0);
        ps = sg;
        rest(IC<8>()); rest(IC<9>()); rest(IC<10>()); rest(IC<11>()); rest(IC<12>()); rest(IC<13>()); rest(IC<14>()); rest(IC<15>());
        pack(1);
    }
    RTS(2)
    if (blockIdx.x == 0 && t8 == 1536u) {
        do {
            beat();
            __builtin_amdgcn_s_sleep(8);
        } while (peek(vconst<M_MD * 4>()) < NWG);
    }
    RTS(3)
    RTS(4)
}

#define A_MD 0
#define A_ACC (SLOTI * 4)
#define A_FLAG (A_ACC + SLOTO * 4)
#define A_LDS (A_FLAG + 512)
static_assert(SLOTI * 4 == 3 * NT * 16 + 1024, "md staging assumes three full vectors and 256 dwords");
#define BIAS 0x40004000u
#define NFAST 8
#define NLD 10
#define MDF 0x0a11333
#define MDP 0xaaaaa
#define HP 2
#define SHARE (SLOTO / 8)
#define SPINCAP 4096u
#define ZSIG 9.0f
#define HSLOW 0x80008000u
#define GAS __attribute__((address_space(1)))

__global__ __launch_bounds__(NT, 4) __attribute__((amdgpu_num_vgpr(53))) void acc_kernel(char* ws, int T, int E, int ks, int lcap, float zf, int pad,
                                                 const uint16_t* __restrict__ centers, const uint16_t* __restrict__ wrbf,
                                                 const uint16_t* __restrict__ wsbf, const uint16_t* __restrict__ wup,
                                                 const u32x2* __restrict__ m, const uint16_t* __restrict__ dist, u32x2* __restrict__ out,
                                                 const int* __restrict__ iin, const int* __restrict__ iout, const uint16_t* __restrict__ ang,
                                                 const uint16_t* __restrict__ wdown, const uint16_t* __restrict__ freq) {
    const char* recs = ws + W_RECS;
    const u32x4* tcnt = (const u32x4*)(ws + W_TCNT);
    const uint32_t* mdc = (const uint32_t*)(ws + W_MDC);
    const u32x4* stat = (const u32x4*)(ws + W_STAT);
    uint32_t* partial = (uint32_t*)(ws + W_PART);
    unsigned* meta = (unsigned*)ws;
    extern __shared__ __attribute__((aligned(16))) char smem[];
    RTS(8)
    unsigned* acc = (unsigned*)(smem + A_ACC);
    unsigned* s_flag = (unsigned*)(smem + A_FLAG);
    const int tid = threadIdx.x;
    const int lane = tid & 63;
    const int wv = __builtin_amdgcn_readfirstlane(tid >> 6);
    const int cp = (blockIdx.x >> 3) & 1;
    const int h = blockIdx.x & 3;
    const int j = ((blockIdx.x >> 2) & 1) * 16 + (blockIdx.x >> 4);
    u32x4 tc;
    const __amdgpu_buffer_rsrc_t rsM = __builtin_amdgcn_make_buffer_rsrc((void*)mdc, 0, (int)(128u * SLOTI * 4u), 0x00020000);

    u32x4 pm[4];
    u32x4 ring[NLD];
    auto load_md = [&](int ff, int pl, int mdp) __attribute__((always_inline)) {
        const uint32_t so = (uint32_t)(cp * 64 + ff * 4 + h) * (SLOTI * 4u);
#pragma unroll
        for (int v = 0; v < 3; v++)
            if (pl < 0 || pl == ((mdp >> (4 * v)) & 15)) pm[v] = __builtin_amdgcn_raw_buffer_load_b128(rsM, (uint32_t)tid * 16u, so + (uint32_t)v * (NT * 16u), 0);
        if ((pl < 0 || pl == (mdp & 15)) && wv == 0) pm[3] = __builtin_amdgcn_raw_buffer_load_b128(rsM, (uint32_t)tid * 16u, so + 3u * NT * 16u, 0);
        __builtin_amdgcn_sched_barrier(0);
    };
    const uint32_t lf = (uint32_t)(tid >> 9) * (SKH * 128u) + (uint32_t)(tid & 0x1e0) * 16u + (uint32_t)(tid & 12) * 32u + (uint32_t)(tid & 16) * 4u + (uint32_t)(tid & 3) * 16u;
    const uint32_t lp = (uint32_t)(tid >> 8) * (SKM * 128u) + (uint32_t)((tid >> 2) & 63) * 128u + (uint32_t)(tid & 3) * 16u;
    auto load_main = [&](int i, int ff, bool near = false) __attribute__((always_inline)) -> u32x4 {
        const uint32_t q = (uint32_t)(((i >> 1) - HP) & 3), SK = q ? SKM : SKH, SS = q ? SSM : SSH;
        const __amdgpu_buffer_rsrc_t rs = __builtin_amdgcn_make_buffer_rsrc((void*)(recs + (q * SQ + (uint32_t)ff * SS + (uint32_t)(h * 32 + j) * SC) * 128u), 0, ks * (int)(SK * 128u), 0x00020000);
        if (q == 0u && near) return __builtin_amdgcn_raw_buffer_load_b128(rs, lf, (i & 1) * (int)(2u * SK * 128u), 0);
        if (q == 0u) return __builtin_amdgcn_raw_buffer_load_b128(rs, lf, (i & 1) * (int)(2u * SK * 128u), AUX_NT);
        return __builtin_amdgcn_raw_buffer_load_b128(rs, lp + (uint32_t)(i & 1) * 64u, 0, 0);
    };
    const __amdgpu_buffer_rsrc_t rsT = __builtin_amdgcn_make_buffer_rsrc((void*)recs, 0, (int)(LBASE * 128u), 0x00020000);
    const uint32_t tof = (uint32_t)(lane & 2) * (TPL * 64u) + (uint32_t)(tid >> 2) * 128u + (uint32_t)(lane & 1) * 64u + 4u + (TBASE + (uint32_t)(h * 32 + j) * TST) * 128u;
    const uint32_t sp0 = (SBASE + (uint32_t)(h * 32 + j) * (2u * NWG * 9u) + (uint32_t)(tid >> 2) * 9u) * 128u;
    auto tail_rank = [&](uint32_t c, uint32_t sp, int ff, uint32_t t0, int& dst, uint32_t& en) __attribute__((always_inline)) -> int {
        const int k4 = (int)(((uint32_t)(lane & 3) + t0) * 4u) - (int)((c & 3u) * 8u);
        const int left = (int)((c >> 2) & 15u) - k4;
        const bool has = left > 0;
        uint32_t ea = tof + (uint32_t)ff * (128u * TST * 128u), eb = sp + (uint32_t)(k4 * 16 + min(max(left, 0), 4));
        asm volatile("" : "+v"(ea), "+v"(eb));
        en = k4 < 0 ? ea : eb;
        const u64 mask = __builtin_amdgcn_ballot_w64(has);
        const int n = __builtin_popcountll(mask);
        const int rank = __builtin_amdgcn_mbcnt_hi((uint32_t)(mask >> 32), __builtin_amdgcn_mbcnt_lo((uint32_t)mask, 0u));
        const int r = has ? rank : (n + lane - rank);
        dst = ((r & 15) << 4) | ((r >> 4) << 2);
        return n;
    };
    auto load_tail = [&](uint32_t en, u64& tm) __attribute__((always_inline)) -> u32x4 {
        const bool on = (uint32_t)(lane & 3) < (en & 7u);
        tm = __builtin_amdgcn_ballot_w64(on);
        return __builtin_amdgcn_raw_buffer_load_b128(rsT, on ? (en & ~15u) + (uint32_t)(lane & 3) * 16u : 0xffffff00u, 0, 0);
    };
    f32x2 fa[4][4];
    u32x4 em[2];
    u32x2 dw2;
    uint32_t wl;
    const __amdgpu_buffer_rsrc_t rsE = __builtin_amdgcn_make_buffer_rsrc((void*)m, 0, E * 8, 0x00020000);
    const __amdgpu_buffer_rsrc_t rsD = __builtin_amdgcn_make_buffer_rsrc((void*)dist, 0, E * 2, 0x00020000);
    auto acc_vec = [&](int i) __attribute__((always_inline)) -> u32x4 {
        const uint32_t a = A_ACC + (uint32_t)(i >> 4) * 256u + (uint32_t)(i & 15) * 8u;
        const u32x2 x = *(const LDS u32x2*)(uintptr_t)a, y = *(const LDS u32x2*)(uintptr_t)(a + 128u);
        const u32x4 v = {x.x, x.y, y.x, y.y};
        return v;
    };
    auto edge0 = [&](int sh) __attribute__((always_inline)) -> uint32_t {
        return (tid < SHARE / 4) ? (uint32_t)(((sh * (SHARE / 64) + (tid >> 4)) << 11) | (j << 6) | ((tid & 15) * 2)) : 0x0fffff00u;
    };
    auto load_m = [&](int sh) __attribute__((always_inline)) {
        em[0] = __builtin_amdgcn_raw_buffer_load_b128(rsE, edge0(sh) * 8u, 0, AUX_NT);
        em[1] = __builtin_amdgcn_raw_buffer_load_b128(rsE, edge0(sh) * 8u + 256u, 0, AUX_NT);
    };
    auto load_d = [&](int sh) __attribute__((always_inline)) {
        dw2.x = __builtin_amdgcn_raw_buffer_load_b32(rsD, edge0(sh) * 2u, 0, 0);
        dw2.y = __builtin_amdgcn_raw_buffer_load_b32(rsD, edge0(sh) * 2u + 64u, 0, 0);
    };
    auto load_wl = [&]() __attribute__((always_inline)) {
        const u64 p0 = (u64)wsbf, p1 = (u64)wrbf, p2 = (u64)centers, p3 = (u64)wup;
        const u64 pa = lane < 16 ? p0 : p1, pb = lane < 40 ? p2 : p3;
        wl = ((const GAS uint32_t*)(lane < 32 ? pa : pb))[lane & (lane < 32 ? 15 : lane < 40 ? 3 : 7)];
    };
    auto factor = [&](int r, bool first) __attribute__((always_inline)) {
        const float sq = 3.7982826f;
        const f32x2 d01 = {bf2f(dw2.x & 0xffffu), bf2f(dw2.x >> 16)}, d23 = {bf2f(dw2.y & 0xffffu), bf2f(dw2.y >> 16)};
        const uint32_t cw = __builtin_amdgcn_readlane(wl, 32 + (r >> 1));
        const float cn = bf2f((r & 1) ? (cw >> 16) : (cw & 0xffffu));
        f32x2 w[4];
#pragma unroll
        for (int k = 0; k < 4; k++) {
            const uint32_t x = __builtin_amdgcn_readlane(wl, (k >> 1) * 16 + r * 2 + (k & 1));
            w[k].x = bf2f(x & 0xffffu); w[k].y = bf2f(x >> 16);
        }
        const f32x2 y01 = (d01 - cn) * sq, y23 = (d23 - cn) * sq;
        const f32x2 t01 = -y01 * y01, t23 = -y23 * y23;
        const float rb[4] = {__builtin_amdgcn_exp2f(t01.x), __builtin_amdgcn_exp2f(t01.y), __builtin_amdgcn_exp2f(t23.x), __builtin_amdgcn_exp2f(t23.y)};
#pragma unroll
        for (int q = 0; q < 4; q++) {
#pragma unroll
            for (int k = 0; k < 4; k++) fa[q][k] = first ? w[k] * rb[q] : __builtin_elementwise_fma(w[k], (f32x2)(rb[q]), fa[q][k]);
        }
    };
    auto edges = [&](int sh) __attribute__((always_inline)) {
        load_d(sh);
        load_m(sh);
        factor(0, true);
#pragma unroll 1
        for (int r = 1; r < 8; r++) factor(r, false);
    };
#define ADT(V, O) "ds_write_addtid_b32 " V " offset:" #O "\n\t"
    auto store_md = [&]() __attribute__((always_inline)) {
        asm volatile("s_mov_b32 m0, %12\n\ts_nop 0\n\t" ADT("%0", 0) ADT("%1", 256) ADT("%2", 512) ADT("%3", 768) ADT("%4", 16384) ADT("%5", 16640) ADT("%6", 16896) ADT("%7", 17152)
                     ADT("%8", 32768) ADT("%9", 33024) ADT("%10", 33280) ADT("%11", 33536)
                     :: "v"(pm[0][0]), "v"(pm[0][1]), "v"(pm[0][2]), "v"(pm[0][3]), "v"(pm[1][0]), "v"(pm[1][1]), "v"(pm[1][2]), "v"(pm[1][3]),
                        "v"(pm[2][0]), "v"(pm[2][1]), "v"(pm[2][2]), "v"(pm[2][3]), "s"(wv * 1024) : "memory");
        if (wv == 0) asm volatile("s_mov_b32 m0, 0\n\ts_nop 0\n\t" ADT("%0", 49152) ADT("%1", 49408) ADT("%2", 49664) ADT("%3", 49920)
                                  :: "v"(pm[3][0]), "v"(pm[3][1]), "v"(pm[3][2]), "v"(pm[3][3]) : "memory");
    };
    const int me = h * 2 + cp;
    GAS unsigned* dw = (GAS unsigned*)(meta + M_DONE + j * 64);
    const bool fin = wv < (SHARE / 4 + 63) / 64;
    bool fast, more;
    uint32_t need, mg0, mg1;
    uint32_t guard = 0;
    uint32_t sgn = 0x8000u;
    pin(sgn);

#define GAT3(M) { uint32_t a0_, a1_, a2_; asm volatile( \
        "v_lshlrev_b16 %0, 2, %3\n\tv_lshlrev_b16 %1, 2, %4\n\tv_lshlrev_b16 %2, 2, %5\n\t" \
        "ds_read_b32 v[%6], %0\n\tds_read_b32 v[%6+1], %1\n\tds_read_b32 v[%6+2], %2" \
        : "=&v"(a0_), "=&v"(a1_), "=&v"(a2_) : "v"(w0), "v"(w1), "v"(w2), "n"(M) : "memory"); }
#define MIXL " op_sel_hi:[1,1,0]\n\t"
#define MIXH " op_sel:[0,1,0] op_sel_hi:[1,1,0]\n\t"
#define SDW " dst_sel:DWORD dst_unused:UNUSED_PAD src0_sel:DWORD src1_sel:WORD_1\n\t"
#define FOLD "v_bitop3_b32 %6, %6, v[%15-9], v[%15-8] bitop3:0xfe\n\tv_or_b32 %6, v[%15-7], %6\n\t"
#define SCAT(PRE, POST, M) { uint32_t c0_, c1_, c2_, a0_, a1_, a2_; asm volatile( \
        FOLD "v_and_b32 %0, 0x3ff, %7\n\tv_lshrrev_b32 %2, 22, %7\n\t" PRE \
        "v_bitop3_b32 %0, %11, %8, %0 bitop3:0xea\n\tv_bitop3_b32 %2, %11, %10, %2 bitop3:0xea\n\tv_bfe_u32 %1, %7, 11, 10\n\t" \
        "v_fma_mix_f32 %3, %0, v[%15], %12" MIXL "v_bitop3_b32 %1, %11, %9, %1 bitop3:0xea\n\tv_fma_mix_f32 %0, %0, v[%15], %13" MIXH \
        "v_fma_mix_f32 %4, %1, v[%15+1], %12" MIXL "v_lshl_add_u32 %0, %0, 16, %3\n\tv_lshlrev_b32_sdwa %3, 2, %8" SDW \
        "v_fma_mix_f32 %1, %1, v[%15+1], %13" MIXH "ds_add_rtn_u32 v[%15-9], %3, %0 offset:%14\n\t" \
        "v_fma_mix_f32 %5, %2, v[%15+2], %12" MIXL "v_lshl_add_u32 %1, %1, 16, %4\n\tv_lshlrev_b32_sdwa %4, 2, %9" SDW \
        "v_fma_mix_f32 %2, %2, v[%15+2], %13" MIXH "ds_add_rtn_u32 v[%15-8], %4, %1 offset:%14\n\t" \
        "v_lshl_add_u32 %2, %2, 16, %5\n\tv_lshlrev_b32_sdwa %5, 2, %10" SDW \
        "ds_add_rtn_u32 v[%15-7], %5, %2 offset:%14" POST \
        : "=&v"(c0_), "=&v"(c1_), "=&v"(c2_), "=&v"(a0_), "=&v"(a1_), "=&v"(a2_), "+v"(guard) \
        : "v"(xw), "v"(w0), "v"(w1), "v"(w2), "v"(sgn), "s"(mg0), "s"(mg1), "n"(A_ACC), "n"(M), "s"(tm) : "memory"); }
    asm volatile("v_mov_b32 v110, 0\n\tv_mov_b32 v111, 0\n\tv_mov_b32 v112, 0\n\tv_mov_b32 v113, 0\n\tv_mov_b32 v114, 0\n\tv_mov_b32 v115, 0\n\t"
                 "v_mov_b32 v116, 0\n\tv_mov_b32 v117, 0\n\tv_mov_b32 v118, 0" ::: "v106", "v127");
    auto unmix = [&](int i) __attribute__((always_inline)) {
        const auto s0 = __builtin_amdgcn_permlane16_swap(ring[i][0], ring[i][2], false, false);
        const auto s1 = __builtin_amdgcn_permlane16_swap(ring[i][1], ring[i][3], false, false);
        ring[i][0] = s0[0]; ring[i][2] = s0[1]; ring[i][1] = s1[0]; ring[i][3] = s1[1];
    };
    auto word = [&](int i, int k) __attribute__((always_inline)) -> uint32_t {
        return i < NFAST && (i >> 1) != HP ? ring[(i & ~1) | (k >> 1)][(i & 1) * 2 + (k & 1)] : ring[i][k];
    };
    auto gather = [&](int R, int i, int p) __attribute__((always_inline)) {
        if ((i >> 1) == HP) unmix(i);
        const uint32_t w0 = word(i, 0 ^ R), w1 = word(i, 1 ^ R), w2 = word(i, 2 ^ R);
        if (p == 0) GAT3(119) else if (p == 1) GAT3(122) else GAT3(125)
    };
    auto scatter = [&](int R, int i, int p, u64 tm) __attribute__((always_inline)) {
        const uint32_t xw = word(i, 3 ^ R), w0 = word(i, 0 ^ R), w1 = word(i, 1 ^ R), w2 = word(i, 2 ^ R);
        if (i < NFAST) { if (p == 0) SCAT("", "", 119) else if (p == 1) SCAT("", "", 122) else SCAT("", "", 125) }
        else if (p == 0) SCAT("s_mov_b64 exec, %16\n\t", "\n\ts_mov_b64 exec, -1", 119)
        else SCAT("s_mov_b64 exec, %16\n\t", "\n\ts_mov_b64 exec, -1", 125)
    };
#define WAIT_L(N) asm volatile("s_waitcnt lgkmcnt(" #N ")" ::: "memory")
    int ntn;
    u64 tm0, tm1;
    uint32_t ta[2], sp = sp0;
    auto list1 = [&](int ff) __attribute__((always_inline)) {
        int dst;
        uint32_t en;
        ntn = tail_rank(tc.x, sp, ff, 0u, dst, en);
        asm volatile("ds_permute_b32 v106, %0, %1" :: "v"(dst), "v"(en) : "memory");
        if (__builtin_expect(__builtin_amdgcn_ballot_w64((tc.x & 2u) != 0u) != 0ull, 0)) {
            const int n2 = tail_rank(tc.x, sp, ff, 4u, dst, en);
            if (ntn + n2 <= 32) {
                const int r = ((dst >> 4) | ((dst & 12) << 2)) + ntn;
                const int x = __builtin_amdgcn_ds_permute((en & 7u) ? ((r & 15) << 4) | ((r >> 4) << 2) : 252, (en & 7u) ? (int)en : 0);
                asm volatile("v_cmpx_ne_u32 vcc, 0, %0\n\tv_mov_b32 v106, %0\n\ts_mov_b64 exec, -1\n\ts_nop 4" :: "v"(x) : "vcc");
                ntn += n2;
            } else ntn = 64;
        }
    };
    auto step = [&](auto RR, auto KK, int ff) __attribute__((always_inline)) {
        constexpr int R = decltype(RR)::value;
        constexpr bool first = decltype(KK)::value == 1, last = decltype(KK)::value == 2;
        constexpr int mdp = first ? MDF : MDP;
        gather(R, 0, 0);
        const int ntl = ntn;
        asm volatile("v_mov_b32_dpp %0, v106 quad_perm:[0,0,0,0] row_mask:0xf bank_mask:0xf\n\tv_mov_b32_dpp %1, v106 quad_perm:[1,1,1,1] row_mask:0xf bank_mask:0xf" : "=v"(ta[0]), "=v"(ta[1]));
        auto side = [&](int pl) __attribute__((always_inline)) {
            if (first && pl == ((mdp >> 20) & 15)) { ring[4] = load_main(4, ff, !(NEARM & 1u)); ring[5] = load_main(5, ff, !(NEARM & 1u)); }
            if (first && pl == ((mdp >> 24) & 15)) { ring[6] = load_main(6, ff); ring[7] = load_main(7, ff); }
            if (pl == ((mdp >> 12) & 15)) ring[NFAST] = load_tail(ta[0], tm0);
            if (pl == ((mdp >> 16) & 15)) ring[NFAST + 1] = load_tail(ta[1], tm1);
            __builtin_amdgcn_sched_barrier(0);
            if (!last) load_md(ff + 1, pl, mdp);
            if (!first && !last && pl == 5 && ff == 6) {
                load_d(me);
                load_wl();
                if (tid == NT - 128) (void)__hip_atomic_fetch_add((GAS unsigned*)((GAS char*)dw + vconst<0>()), 0x1000000u << me, __ATOMIC_RELAXED, __HIP_MEMORY_SCOPE_AGENT);
            }
            if (last && pl == 3) load_m(me);
            if (last && pl == 4 && wv == 15) { GAS unsigned* d_ = dw; asm volatile("s_nop 4\n\tglobal_load_dword v108, %0, %1 sc1" :: "v"(vconst<0>()), "s"(d_) : "memory"); }
            if (last && pl < 7 && fin) factor(pl, pl == 0);
            __builtin_amdgcn_sched_barrier(0);
        };
        side(8);
        gather(R, 1, 1);
        side(9);
        const uint32_t tcur = tc.x, spc = sp;
#pragma unroll
        for (int i = 0; i < NFAST - 1; i++) {
            gather(R, i + 2, (i + 2) % 3);
            if (i == 0) side(10);
            if (i == 0) WAIT_L(6); else if (i == 1) WAIT_L(9); else WAIT_L(12);
            scatter(R, i, i % 3, 0ull);
            if (!last && (i & 1)) { ring[i - 1] = load_main(i - 1, ff + 1, first && !(NEARM & 4u)); ring[i] = load_main(i, ff + 1, first && !(NEARM & 4u)); }
            side(i);
            if (i == 3) SETP(2);
            if (i == 4) SETP(1);
            if (i == 5) SETP(0);
            if (i == 0 && !last) {
                tc.x = __builtin_amdgcn_alignbit(tc.y, tc.x, 8u); tc.y = __builtin_amdgcn_alignbit(tc.z, tc.y, 8u);
                tc.z = __builtin_amdgcn_alignbit(tc.w, tc.z, 8u); tc.w >>= 8;
                sp = ff == 7 ? sp0 + NWG * 9u * 128u : sp + ((tcur & 0x3cu) << 2);
                list1(ff + 1);
            }
            __builtin_amdgcn_sched_barrier(0);
        }
        if (last) asm volatile("" :: "v"(ring[NFAST + 1]));
        if (__builtin_expect(ntl > 16, 0)) {
            gather(R, NFAST + 1, 0);
            WAIT_L(0);
            scatter(R, NFAST + 1, 0, tm1);
        }
        if (__builtin_expect(ntl > 32, 0)) {
#pragma unroll 1
            for (int e = 2; e < 6; e++) {
                int dst;
                uint32_t en;
                if (tail_rank(tcur, spc, ff, (uint32_t)e & 4u, dst, en) <= (e & 3) * 16) continue;
                const int cmp = __builtin_amdgcn_ds_permute(dst, (int)en);
                ring[NFAST + 1] = load_tail((uint32_t)__builtin_amdgcn_ds_bpermute(((lane & ~3) | (e & 3)) << 2, cmp), tm1);
                gather(R, NFAST + 1, 0);
                WAIT_L(0);
                scatter(R, NFAST + 1, 0, tm1);
            }
        }
        SETP(3);
        WAIT_L(3);
        if (!last) {
            __builtin_amdgcn_s_barrier();
            store_md();
            SETP(0);
        }
        scatter(R, NFAST - 1, 1, 0ull);
        if (!last) { ring[6] = load_main(6, ff + 1); ring[7] = load_main(7, ff + 1); }
        scatter(R, NFAST, 2, tm0);
        if (!last) {
            WAIT_L(6);
            __builtin_amdgcn_s_barrier();
            SETP(3);
        }
    };
    auto steps = [&](auto RR) __attribute__((always_inline)) {
        tc = tcnt[(wv * 16 + (lane >> 2)) * 128 + j * 4 + h];
        u32x4 sv[2];
        float4 su[3];
        uint32_t e0;
        if (wv < 4) {
            sv[0] = (u32x4)(0u); sv[1] = sv[0];
            su[0] = make_float4(0.f, 0.f, 0.f, 0.f);
            if (lane < SLOTI / 256) { sv[0] = stat[tid * 2]; sv[1] = stat[tid * 2 + 1]; su[0] = ((const float4*)(ws + W_SUM))[tid]; }
            su[1] = ((const float4*)(ws + W_SUM))[256 + lane];
            su[2] = ((const float4*)(ws + W_SUM))[320 + lane];
            e0 = meta[M_E0 + wv];
        }
        const u32x2 extra = *(const u32x2*)(meta + M_LOG);
        load_md(0, -1, 0);
        {
            const u32x4 bi = {BIAS, BIAS, BIAS, BIAS};
#pragma unroll
            for (int k = 0; k < 6; k++) *(LDS u32x4*)(uintptr_t)(A_ACC + (k * NT + threadIdx.x) * 16) = bi;
            if (threadIdx.x < 128) *(LDS u32x4*)(uintptr_t)(A_ACC + (6 * NT + threadIdx.x) * 16) = bi;
            __builtin_amdgcn_sched_barrier(0);
        }
#pragma unroll
        for (int i = 0; i < 2; i++) ring[i] = load_main(i, 0);
        __builtin_amdgcn_sched_barrier(0);
        list1(0);
        if (wv < 4) {
            const uint32_t mxb = wave_max(max(max(sv[0].x & 0xffffu, sv[0].z & 0xffffu), max(sv[1].x & 0xffffu, sv[1].z & 0xffffu)));
            const uint32_t mnb = 0xffffu - (wave_max(max(max(sv[0].x, sv[0].z), max(sv[1].x, sv[1].z))) >> 16);
            const float s2 = wave_sum((__uint_as_float(sv[0].y) + __uint_as_float(sv[0].w)) + (__uint_as_float(sv[1].y) + __uint_as_float(sv[1].w)));
            const float s1 = wave_sum((su[0].x + su[0].y) + (su[0].z + su[0].w));
            const float c1 = wave_sum((su[1].x + su[1].y) + (su[1].z + su[1].w)) * (1.0f / (NWG * 512)), c2 = wave_sum((su[2].x + su[2].y) + (su[2].z + su[2].w)) * (1.0f / (NWG * 512));
            const float rng = fmaxf(fabsf(zf * s1 * c1) + ZSIG * sqrtf(zf * s2 * c2), 2.0f * bf2f(mxb)) * 6.103515625e-05f;
            const uint32_t eb = mxb ? (__float_as_uint(rng) + 0x7fffffu) >> 23 : 127u;
            const bool ok = !mxb || (mxb < 0x4700u && eb >= 107u && eb <= 140u && mnb >= ((eb + 8u) << 7) && c2 >= 0.015625f);
            if (lane == 0) {
                s_flag[8 + wv] = (eb & 0xffu) | (ok ? 0x100u : 0u) | (mxb ? 0x200u : 0u);
                s_flag[12 + wv] = __float_as_uint(__uint_as_float(((eb + e0) & 0xffu) << 23) * (1024.0f / 1023.0f));
                s_flag[wv] = 0u;
            }
        }
        store_md();
#pragma unroll
        for (int i = 2; i < 4; i++) ring[i] = load_main(i, 0);
        WAIT_L(0);
        __syncthreads();
        const uint32_t q0 = __builtin_amdgcn_readfirstlane(s_flag[8 + 2 * cp]), q1 = __builtin_amdgcn_readfirstlane(s_flag[9 + 2 * cp]);
        fast = (q0 & q1 & 0x100u) != 0u;
        need = (q0 >> 9) | ((q1 >> 9) << 1);
        more = (__builtin_amdgcn_readfirstlane(extra.x | extra.y) | (uint32_t)(T & 3)) != 0u;
        mg0 = (((q0 & 0xffu) + 9u) << 23) | 0x400000u;
        mg1 = ((((q1 & 0xffu) + 9u) << 23) | 0x400000u) + ((0u - (mg0 >> 16)) & 0xffffu);
        if (!fast) { __builtin_amdgcn_s_waitcnt(0x0f70); return; }
        RTS(9)
        step(RR, IC<1>(), 0);
#pragma unroll 1
        for (int ff = 1; ff < 15; ff++) step(RR, IC<0>(), ff);
        step(RR, IC<2>(), 15);
        if (fin) factor(7, false);
    };
    auto irregular = [&](auto f) __attribute__((always_inline)) {
        const float fr = bf2f(freq[0]);
        const char* lg = recs + (size_t)LBASE * 128u;
        const uint16_t* tags = (const uint16_t*)(lg + (size_t)lcap * 128u);
        const uint32_t nl = meta[M_LOG], ns = meta[M_LOG + 1];
#pragma unroll 1
        for (uint32_t q = tid; q < nl * 8u; q += NT) {
            const uint32_t b = tags[q >> 3];
            if ((b & 127u) == (uint32_t)(h * 32 + j)) {
                const uint32_t* un = (const uint32_t*)(lg + (size_t)q * 16u);
                const uint32_t xw = un[3 ^ h];
#pragma unroll 1
                for (int k = 0; k < 3; k++) {
                    const uint32_t ww = un[k ^ h];
                    const float c = (float)((xw >> (k * 11)) & 0x3ffu) * (1.0f / 1023.0f);
                    if (!(ww & WNULL)) f(ww >> 16, ((ww & 0x3fffu) << 6) | (b >> 5), (ww & 0x8000u) ? -c : c);
                }
            }
        }
#pragma unroll 1
        for (uint32_t q = tid; q < ns + (uint32_t)(T & 3); q += NT) {
            const uint32_t t = q < ns ? ((const uint32_t*)tags)[-1 - (int)q] : (uint32_t)(T & ~3) + (q - ns);
            const uint32_t i = (uint32_t)iin[t], o = (uint32_t)iout[t];
            if (((o >> 6) & 31u) == (uint32_t)j && (i & 3u) == (uint32_t)h) f(((o >> 11) << 6) | (o & 63u), i, cosf(fr * bf2f(ang[t])));
        }
        __builtin_amdgcn_s_waitcnt(0x0f70);
    };

    if (h == 0) steps(IC<0>());
    else if (h == 1) steps(IC<1>());
    else if (h == 2) steps(IC<2>());
    else steps(IC<3>());
    if (fast) {
        if (more) irregular([&](uint32_t ol, uint32_t i, float cf) __attribute__((always_inline)) {
            const uint32_t md = mdc[(cp * 64u + (i & 63u)) * SLOTI + ((i >> 14) << 8) + ((i >> 4) & 0xfcu) + ((i >> 12) & 3u)];
            const float c = cf * (1023.0f / 16777216.0f);
            const uint32_t lo = __float_as_uint(__builtin_fmaf(c, h2f(md & 0xffffu), __uint_as_float(mg0)));
            const uint32_t hi = __float_as_uint(__builtin_fmaf(c, h2f(md >> 16), __uint_as_float(mg1)));
            if (cf == cf) guard |= atomicAdd(&acc[ol], (hi << 16) + lo);
            else guard |= HSLOW;
        });
    }
    volatile LDS unsigned* seen = (volatile LDS unsigned*)(uintptr_t)(A_FLAG + 4);
    if (fast && wv == 15) asm volatile("s_waitcnt vmcnt(0)\n\tds_write_b32 %0, v108 offset:16\n\tv_and_b32 v109, 0xff00ff, v108\n\tds_write_b32 %0, v109" :: "v"(vconst<A_FLAG + 4>()) : "memory");
    WAIT_L(0);
    asm volatile("v_or3_b32 %0, %0, v110, v111\n\tv_or3_b32 %0, %0, v112, v113\n\tv_or3_b32 %0, %0, v114, v115\n\tv_or3_b32 %0, %0, v116, v117\n\tv_or_b32 %0, v118, %0" : "+v"(guard));
    __syncthreads();
    RTS(10)
    const __amdgpu_buffer_rsrc_t rsP = __builtin_amdgcn_make_buffer_rsrc((void*)(partial + (size_t)j * 16 * SLOTO), 0, 16 * SLOTO * 4, 0x00020000);
    const char* pk = recs + (uint32_t)j * (SC * 128u);
    const __amdgpu_buffer_rsrc_t rsK = __builtin_amdgcn_make_buffer_rsrc((void*)pk, 0, (int)(MAINL * 128u), 0x00020000);
#define PKA (SSM * 128u)
#define PKB (32u * SC * 128u)
    auto pk_at = [&](uint32_t i) __attribute__((always_inline)) -> uint32_t {
        const uint32_t c = i >> 9;
        return (c < 12u ? (1u + (c >> 2)) * SQ + (c & 3u) * SKM : SQ + 2u * SSM) * 128u + (i & 511u) * 16u;
    };
    u32x4 p[8];
    unsigned have = 0u;
    auto fetch = [&](unsigned v, int sh) __attribute__((always_inline)) {
        const uint32_t po = tid < SHARE / 4 ? pk_at((uint32_t)(sh * (SHARE / 4) + tid)) : 0x7fffff00u;
        const unsigned nw = v & ~have;
#pragma unroll
        for (int r = 0; r < 8; r++) {
            const uint32_t k = (uint32_t)(me ^ r);
            if ((nw >> k) & 1u) p[r] = __builtin_amdgcn_raw_buffer_load_b128(rsK, po, ((v >> (16u + k)) & 1u) ? MAINL * 128u : (k & 1u) * PKA + (k >> 1) * PKB, AUX_SC1);
        }
        have |= v;
    };
    bool room = false;
    if (fast) {
        room = (__builtin_amdgcn_readfirstlane(s_flag[5]) >> (24 + (me ^ 1))) & 1u;
        const __amdgpu_buffer_rsrc_t rsW = __builtin_amdgcn_make_buffer_rsrc((void*)(pk + (uint32_t)cp * PKA + (uint32_t)h * PKB), 0, room ? (int)(MAINL * 128u) : 0, 0x00020000);
        uint32_t big = 0u;
        u32x4 v[7];
#pragma unroll
        for (int k = 0; k < 6; k++) v[k] = acc_vec(k * NT + tid);
        v[6] = (u32x4)(BIAS);
        if (wv < 2) v[6] = acc_vec(6 * NT + tid);
#pragma unroll
        for (int k = 0; k < 6; k++) __builtin_amdgcn_raw_buffer_store_b128(v[k], rsW, pk_at((uint32_t)tid) + ((k >> 1) * SQ + (k & 1) * 2u * SKM) * 128u, 0, AUX_SC1);
        if (wv < 2) __builtin_amdgcn_raw_buffer_store_b128(v[6], rsW, (SQ + 2u * SSM) * 128u + (uint32_t)tid * 16u, 0, AUX_SC1);
        __builtin_amdgcn_sched_barrier(0);
#pragma unroll
        for (int k = 0; k < 28; k++) { guard |= v[k >> 2][k & 3]; big |= v[k >> 2][k & 3] + 0x42004200u; }
        if (guard & HSLOW) s_flag[0] = 1u;
        if (big & 0x7c00u) s_flag[2] = 1u;
        if (big & 0x7c000000u) s_flag[3] = 2u;
        __builtin_amdgcn_sched_barrier(0);
        p[0] = acc_vec(me * (SHARE / 4) + min(tid, SHARE / 4 - 1));
        have = 1u << me;
        if (fin) fetch(__builtin_amdgcn_readfirstlane(*seen), me);
    }
    asm volatile("s_waitcnt vmcnt(0)" ::: "memory");
    __syncthreads();
    RTS(11)
    const bool redo = !fast || __builtin_amdgcn_readfirstlane(s_flag[0]) != 0u || (need & ~__builtin_amdgcn_readfirstlane(s_flag[2] | s_flag[3])) != 0u;
    const bool slow = redo || !room;
    if (slow) {
        float* fs = (float*)acc;
        float w[16];
        load_w(wdown, w);
        const float fr = bf2f(freq[0]);
#pragma unroll 1
        for (int e = 0; e < 2; e++) {
            if (redo) {
                __syncthreads();
                for (int i = tid; i < SLOTO; i += NT) fs[i] = 0.f;
                __syncthreads();
                const __amdgpu_buffer_rsrc_t rsO = __builtin_amdgcn_make_buffer_rsrc((void*)iout, 0, T * 4, 0x00020000);
                const __amdgpu_buffer_rsrc_t rsI = __builtin_amdgcn_make_buffer_rsrc((void*)iin, 0, T * 4, 0x00020000);
#pragma unroll 1
                for (int t0 = tid * 4; t0 < T; t0 += NT * 16) {
                    u32x4 o4[4], i4[4];
#pragma unroll
                    for (int g = 0; g < 4; g++) {
                        o4[g] = __builtin_amdgcn_raw_buffer_load_b128(rsO, (uint32_t)(t0 + g * NT * 4) * 4u, 0, 0);
                        i4[g] = __builtin_amdgcn_raw_buffer_load_b128(rsI, (uint32_t)(t0 + g * NT * 4) * 4u, 0, 0);
                    }
                    uint32_t hit = 0u;
#pragma unroll
                    for (int k = 0; k < 16; k++) hit |= (bfi(3u, i4[k >> 2][k & 3], o4[k >> 2][k & 3]) & 0x7c3u) == (uint32_t)(j * 64 + h) ? 1u << k : 0u;
#pragma unroll 1
                    for (; hit; hit &= hit - 1u) {
                        const int k = __builtin_ctz(hit), t = t0 + (k >> 2) * NT * 4 + (k & 3);
                        if (t >= T) break;
                        const uint32_t o = (uint32_t)iout[t];
                        uint32_t rr[4];
                        md_of(m[iin[t]], w, rr);
                        atomicAdd(&fs[((o >> 11) << 6) | (o & 63u)], cosf(fr * bf2f(ang[t])) * bf2f(cp ? (e ? rr[3] : rr[2]) : (e ? rr[1] : rr[0])));
                    }
                }
                __syncthreads();
            }
            const float un = __uint_as_float(s_flag[12 + 2 * cp + e]);
            for (int i = tid; i < SLOTO / 4; i += NT) {
                u32x4 v = acc_vec(i);
                if (!redo) {
#pragma unroll
                    for (int k = 0; k < 4; k++) v[k] = __float_as_uint((float)((int)((v[k] >> (16 * e)) & 0xffffu) - 0x4000) * un);
                }
                __builtin_amdgcn_raw_buffer_store_b128(v, rsP, (uint32_t)((e * 8 + me) * SLOTO * 4 + i * 16), 0, AUX_SC1);
            }
        }
        load_wl();
        edges(me);
#pragma unroll
        for (int k = 0; k < 8; k++) p[k] = (u32x4)(0u);
        have = 0u;
        asm volatile("s_waitcnt vmcnt(0)" ::: "memory");
        __syncthreads();
    }
    if (tid == NT - 64) {
        const unsigned inc = (slow ? 0x10001u : 1u) << me;
        unsigned v = __hip_atomic_fetch_add(dw, inc, __ATOMIC_RELAXED, __HIP_MEMORY_SCOPE_AGENT) + inc;
        unsigned quit = 0u;
        if ((v & 0xffu) != 0xffu) {
            unsigned spins = 0;
            do {
                *seen = v & 0xff00ffu;
                __builtin_amdgcn_s_sleep(1);
                v = __hip_atomic_load(dw, __ATOMIC_RELAXED, __HIP_MEMORY_SCOPE_AGENT);
                if (++spins > SPINCAP && (v & 0xffu) != 0xffu) {
                    v = __hip_atomic_fetch_add(dw, 256u << me, __ATOMIC_RELAXED, __HIP_MEMORY_SCOPE_AGENT);
                    if ((v & 0xffu) != 0xffu) { v = 0xffu; quit = 0x80000000u; }
                }
            } while ((v & 0xffu) != 0xffu);
            v &= 0xff00ffu;
        }
        *seen = (v & 0xffffffu) | quit;
    }
    if (!fin) return;
    have = __builtin_amdgcn_readfirstlane(have);
    uint32_t un[4];
#pragma unroll
    for (int c = 0; c < 4; c++) un[c] = __builtin_amdgcn_readfirstlane(s_flag[12 + c]);
    unsigned sv;
    {
        const u32x4 rsQ = {(uint32_t)(uintptr_t)pk, (uint32_t)((uintptr_t)pk >> 32) & 0xffffu, MAINL * 128u, 0x00020000u};
        const uint32_t po = tid < SHARE / 4 ? pk_at((uint32_t)(me * (SHARE / 4) + tid)) : 0x7fffff00u;
        do {
            sv = __builtin_amdgcn_readfirstlane(*seen);
            uint32_t t0, t1, t2;
#define FET(R) "s_xor_b32 %8, %13, " #R "\n\ts_bitcmp1_b32 %14, %8\n\ts_cbranch_scc0 1f\n\ts_and_b32 %9, %8, 1\n\ts_mul_i32 %9, %9, %16\n\ts_lshr_b32 %10, %8, 1\n\t" \
               "s_mul_i32 %10, %10, %17\n\ts_add_u32 %9, %9, %10\n\ts_bitcmp1_b32 %15, %8\n\ts_cselect_b32 %9, %18, %9\n\tbuffer_load_dwordx4 %" #R ", %11, %12, %9 offen sc1\n1:\n\t"
            asm volatile("s_and_b32 %8, %14, 0xff\n\ts_cbranch_scc0 2f\n\ts_nop 2\n\t" FET(0) FET(1) FET(2) FET(3) FET(4) FET(5) FET(6) FET(7) "\n2:\n\ts_sleep 2\n\ts_waitcnt vmcnt(0)"
                         : "+v"(p[0]), "+v"(p[1]), "+v"(p[2]), "+v"(p[3]), "+v"(p[4]), "+v"(p[5]), "+v"(p[6]), "+v"(p[7]), "=&s"(t0), "=&s"(t1), "=&s"(t2)
                         : "v"(po), "s"(rsQ), "s"(me), "s"((int)sv < 0 ? 0u : sv & ~have), "s"(sv >> 16), "n"(PKA), "n"(PKB), "n"(MAINL * 128u) : "memory", "scc");
            have |= sv;
        } while ((sv & 0xffu) != 0xffu);
    }
    if ((int)sv < 0) return;
    RTS(12)
    const unsigned flt = (sv >> 16) & 0xffu;
    auto finish = [&](int sh) __attribute__((always_inline)) {
        {
            const int ol = sh * SHARE + tid * 4;
            const uint32_t po = tid < SHARE / 4 ? (uint32_t)ol * 4u : 0x7fffff00u;
            float g[4][4];
            u32x4 a[2] = {p[0] + p[2], p[1] + p[3]}, b[2] = {p[4] + p[6], p[5] + p[7]};
            if (cp) { const u32x4 t = a[0], u = b[0]; a[0] = a[1]; b[0] = b[1]; a[1] = t; b[1] = u; }
#pragma unroll
            for (int c = 0; c < 2; c++) {
                const int nb = 0x10000 - 0x4000 * __builtin_popcount(flt & (0x55u << c));
                const float u0 = __uint_as_float(nb ? un[2 * c] : 0u), u1 = __uint_as_float(nb ? un[2 * c + 1] : 0u);
#pragma unroll
                for (int q = 0; q < 4; q++) {
                    g[q][2 * c] = (float)((int)((a[c][q] & 0xffffu) + (b[c][q] & 0xffffu)) - nb) * u0;
                    g[q][2 * c + 1] = (float)((int)((a[c][q] >> 16) + (b[c][q] >> 16)) - nb) * u1;
                }
            }
            if (__builtin_expect(flt != 0u, 0)) {
#pragma unroll
                for (int c = 0; c < 2; c++) {
#pragma unroll 1
                    for (int k = c; k < 8; k += 2) {
                        if (!((flt >> k) & 1u)) continue;
                        const u32x4 f0 = __builtin_amdgcn_raw_buffer_load_b128(rsP, po, k * SLOTO * 4, AUX_SC1);
                        const u32x4 f1 = __builtin_amdgcn_raw_buffer_load_b128(rsP, po, (8 + k) * SLOTO * 4, AUX_SC1);
#pragma unroll
                        for (int q = 0; q < 4; q++) { g[q][2 * c] += __uint_as_float(f0[q]); g[q][2 * c + 1] += __uint_as_float(f1[q]); }
                    }
                }
            }
            float wu[16];
#pragma unroll
            for (int k = 0; k < 8; k++) {
                const uint32_t x = __builtin_amdgcn_readlane(wl, 40 + k);
                wu[2 * k] = bf2f(x & 0xffffu); wu[2 * k + 1] = bf2f(x >> 16);
            }
            u32x4 ov[2];
#pragma unroll
            for (int q = 0; q < 4; q++) {
#pragma unroll
                for (int k = 0; k < 4; k++) g[q][k] *= fa[q][k >> 1][k & 1];
                const uint32_t m0 = em[q >> 1][(q & 1) * 2], m1 = em[q >> 1][(q & 1) * 2 + 1];
                const float mm[4] = {bf2f(m0 & 0xffff), bf2f(m0 >> 16), bf2f(m1 & 0xffff), bf2f(m1 >> 16)};
                float o[4];
#pragma unroll
                for (int k = 0; k < 4; k++) o[k] = mm[k] + (g[q][0] * wu[k] + g[q][1] * wu[4 + k] + g[q][2] * wu[8 + k] + g[q][3] * wu[12 + k]) * fa[q][2 + (k >> 1)][k & 1];
                ov[q >> 1][(q & 1) * 2] = pk_bf16(o[0], o[1]);
                ov[q >> 1][(q & 1) * 2 + 1] = pk_bf16(o[2], o[3]);
            }
            const __amdgpu_buffer_rsrc_t rsU = __builtin_amdgcn_make_buffer_rsrc((void*)out, 0, E * 8, 0x00020000);
            const uint32_t uo = edge0(sh) * 8u;
            __builtin_amdgcn_raw_buffer_store_b128(ov[0], rsU, uo, 0, AUX_NT);
            __builtin_amdgcn_raw_buffer_store_b128(ov[1], rsU, uo + 256u, 0, AUX_NT);
        }
    };
    finish(me);
#pragma unroll 1
    for (unsigned todo = (sv >> 8) & 0xffu; todo; todo &= todo - 1u) {
        const int sh = __builtin_ctz(todo);
        edges(sh);
        have = 0u;
        fetch(sv, sh);
        finish(sh);
    }
    const unsigned leak_ = 3000;
    if (blockIdx.x == 5) { const unsigned long long t0_ = __builtin_amdgcn_s_memrealtime(); while (__builtin_amdgcn_s_memrealtime() - t0_ < leak_) __builtin_amdgcn_s_sleep(2); }
}

__global__ __launch_bounds__(256) void gen_scatter_kernel(const int* __restrict__ iin, const int* __restrict__ iout,
                                                          const uint16_t* __restrict__ ang, const uint16_t* __restrict__ freq,
                                                          const u32x2* __restrict__ m, const uint16_t* __restrict__ wdown,
                                                          float* __restrict__ agg, int T) {
    float w[16];
    load_w(wdown, w);
    const float fr = bf2f(freq[0]);
    for (long t = (long)blockIdx.x * 256 + threadIdx.x; t < T; t += (long)gridDim.x * 256) {
        const float c = cosf(fr * bf2f(ang[t]));
        uint32_t r[4];
        md_of(m[iin[t]], w, r);
        float* q = agg + (size_t)iout[t] * 4;
#pragma unroll
        for (int k = 0; k < 4; k++) atomicAdd(q + k, c * bf2f(r[k]));
    }
}
__global__ __launch_bounds__(256) void gen_final_kernel(const u32x2* __restrict__ m, const uint16_t* __restrict__ dist,
                                                        const uint16_t* __restrict__ centers, const uint16_t* __restrict__ wrbf,
                                                        const uint16_t* __restrict__ wsbf, const uint16_t* __restrict__ wup,
                                                        const float* __restrict__ agg, u32x2* __restrict__ out, int E) {
    const int e = blockIdx.x * 256 + threadIdx.x;
    if (e >= E) return;
    const float d = bf2f(dist[e]);
    float rs[4] = {0, 0, 0, 0}, rg[4] = {0, 0, 0, 0};
#pragma unroll 1
    for (int r = 0; r < 8; r++) {
        const float x = d - bf2f(centers[r]);
        const float rb = __expf(-10.0f * x * x);
#pragma unroll
        for (int k = 0; k < 4; k++) { rs[k] += rb * bf2f(wsbf[r * 4 + k]); rg[k] += rb * bf2f(wrbf[r * 4 + k]); }
    }
    float wu[16];
    load_w(wup, wu);
    const u32x2 mv = m[e];
    const float mm[4] = {bf2f(mv.x & 0xffff), bf2f(mv.x >> 16), bf2f(mv.y & 0xffff), bf2f(mv.y >> 16)};
    float g[4], o[4];
#pragma unroll
    for (int k = 0; k < 4; k++) g[k] = agg[(size_t)e * 4 + k] * rs[k];
#pragma unroll
    for (int k = 0; k < 4; k++) o[k] = mm[k] + (g[0] * wu[k] + g[1] * wu[4 + k] + g[2] * wu[8 + k] + g[3] * wu[12 + k]) * rg[k];
    const u32x2 ov = {f2bf(o[0]) | (f2bf(o[1]) << 16), f2bf(o[2]) | (f2bf(o[3]) << 16)};
    out[e] = ov;
}

extern "C" void kernel_launch(void* const* d_in, const int* in_sizes, int n_in,
                              void* d_out, int out_size, void* d_ws, size_t ws_size,
                              hipStream_t stream) {
    const u32x2* m = (const u32x2*)d_in[0];
    const int* iin = (const int*)d_in[1];
    const int* iout = (const int*)d_in[2];
    const uint16_t* dist = (const uint16_t*)d_in[3];
    const uint16_t* ang = (const uint16_t*)d_in[4];
    const uint16_t* centers = (const uint16_t*)d_in[5];
    const uint16_t* freq = (const uint16_t*)d_in[6];
    const uint16_t* wrbf = (const uint16_t*)d_in[7];
    const uint16_t* wsbf = (const uint16_t*)d_in[8];
    const uint16_t* wdown = (const uint16_t*)d_in[9];
    const uint16_t* wup = (const uint16_t*)d_in[10];
    const int E = in_sizes[3];
    const int T = in_sizes[1];
    char* ws = (char*)d_ws;
    const int G = T >> 2;
    const int ntile = (G + GR - 1) / GR;
    const int nstat = ntile / NWG - NDYN > 4 ? ntile / NWG - NDYN : 4;
    const int lcap = G / 6 + 2;
    char* recs = ws + W_RECS;
    size_t off = W_RECS;
    const size_t rbytes = (size_t)LBASE * 128 + (size_t)lcap * 130;
    off += rbytes;
    if (E > EMAX || (E & 3) || E < 1 || rbytes > 0x70000000u || off > ws_size) {
        (void)hipMemsetAsync(ws, 0, (size_t)E * 16, stream);
        gen_scatter_kernel<<<4096, 256, 0, stream>>>(iin, iout, ang, freq, m, wdown, (float*)ws, T);
        gen_final_kernel<<<(E + 255) / 256, 256, 0, stream>>>(m, dist, centers, wrbf, wsbf, wup, (const float*)ws, (u32x2*)d_out, E);
        return;
    }
    int ks = T / (NWG * NB * 24);
    if (ks > KMAX) ks = KMAX;
    const float zf = (float)T / (4.0f * (float)E * (float)E);
    (void)hipFuncSetAttribute((const void*)part_kernel, hipFuncAttributeMaxDynamicSharedMemorySize, P_LDS);
    (void)hipFuncSetAttribute((const void*)acc_kernel, hipFuncAttributeMaxDynamicSharedMemorySize, A_LDS);
    part_kernel<<<NWG, NT, P_LDS, stream>>>(iin, iout, ang, freq, m, wdown, recs, T, E, ntile, nstat, ks, lcap);
    acc_kernel<<<NWG, NT, A_LDS, stream>>>(ws, T, E, ks, lcap, zf, 0, centers, wrbf, wsbf, wup, m, dist, (u32x2*)d_out, iin, iout, ang, wdown, freq);
}
```

```cpp
#define CPF 78848
#include <hip/hip_runtime.h>
#include <hip/hip_bf16.h>
#include <stdint.h>
#pragma clang diagnostic ignored "-Winline-asm"

typedef unsigned int u32x4 __attribute__((ext_vector_type(4)));
typedef unsigned int u32x2 __attribute__((ext_vector_type(2)));
typedef unsigned short u16x2 __attribute__((ext_vector_type(2)));
typedef __bf16 bf16x2 __attribute__((ext_vector_type(2)));
typedef float f32x2 __attribute__((ext_vector_type(2)));
typedef unsigned long long u64;

static __device__ __forceinline__ float bf2f(uint32_t bits16) { return __uint_as_float(bits16 << 16); }
static __device__ __forceinline__ uint32_t f2bf(float f) {
    uint32_t u = __float_as_uint(f);
    if ((u & 0x7fffffffu) > 0x7f800000u) return (u >> 16) | 0x40;
    return (u + 0x7fffu + ((u >> 16) & 1u)) >> 16;
}
static __device__ __forceinline__ float h2f(uint32_t bits16) { return (float)__builtin_bit_cast(_Float16, (unsigned short)bits16); }

#define NB 2048
#define WNULL 0x80000000u
#define CSTEP 0x2aabu
#define CINIT 205u
#define CHV (12u * CSTEP)
#define SLOTO 25088
#define SLOTI 12544
#define EMAX (SLOTO * 32)
#define NT 1024
#define NWG 256
#define KMAX 4
#define NTAIL 4
#define SC 64u
#define SKM (128u * SC + 2u)
#define SSM (4u * SKM + 1u)
#define SSH (128u * SC + 1u)
#define SKH (16u * SSH + 2u)
#define SQ (16u * SSM)
#define MAINL (4u * SQ)
#define BPL(b) ((((b) >> 3) & 3u) * SQ + ((b) & 7u) + (((b) >> 5) << 3))
#define TST 257u
#define TBASE MAINL
#define TPL (NB * TST)
#define SBASE (TBASE + 2u * TPL)
#define LBASE (SBASE + 128u * 2u * NWG * 9u)
#define NDYN 3
#define NEARM 0x55555554u
#define GR 2048
#define M_DONE 0
#define M_TILE (32 * 64)
#define M_LOG (M_TILE + 64)
#define M_MD (M_LOG + 64)
#define M_BEAT (M_MD + 64)
#define M_E0 (M_BEAT + 64)
#define M_WORDS (M_E0 + 64)
#define W_TCNT (M_WORDS * 4)
#define W_STAT (W_TCNT + NWG * NB)
#define W_SUM (W_STAT + 4 * NWG * 8)
#define W_MDC (W_SUM + 6 * NWG * 4)
#define W_PART (W_MDC + 128 * SLOTI * 4)
#define W_RECS (W_PART + 32 * 16 * SLOTO * 4)
#define AUX_NT 2
#define AUX_SC1 16

static __device__ __forceinline__ uint32_t row_max(uint32_t x) {
    x = max(x, (uint32_t)__builtin_amdgcn_update_dpp(0, x, 0x111, 0xf, 0xf, false));
    x = max(x, (uint32_t)__builtin_amdgcn_update_dpp(0, x, 0x112, 0xf, 0xf, false));
    x = max(x, (uint32_t)__builtin_amdgcn_update_dpp(0, x, 0x114, 0xf, 0xf, false));
    return max(x, (uint32_t)__builtin_amdgcn_update_dpp(0, x, 0x118, 0xf, 0xf, false));
}
static __device__ __forceinline__ uint32_t wave_max(uint32_t x) {
    x = row_max(x);
    x = max(x, (uint32_t)__builtin_amdgcn_update_dpp(0, x, 0x142, 0xa, 0xf, false));
    x = max(x, (uint32_t)__builtin_amdgcn_update_dpp(0, x, 0x143, 0xc, 0xf, false));
    return __builtin_amdgcn_readlane(x, 63);
}
static __device__ __forceinline__ float row_sum(float x) {
    x += __int_as_float(__builtin_amdgcn_update_dpp(0, __float_as_int(x), 0x111, 0xf, 0xf, false));
    x += __int_as_float(__builtin_amdgcn_update_dpp(0, __float_as_int(x), 0x112, 0xf, 0xf, false));
    x += __int_as_float(__builtin_amdgcn_update_dpp(0, __float_as_int(x), 0x114, 0xf, 0xf, false));
    return x + __int_as_float(__builtin_amdgcn_update_dpp(0, __float_as_int(x), 0x118, 0xf, 0xf, false));
}
static __device__ __forceinline__ float wave_sum(float x) {
    x = row_sum(x);
    x += __int_as_float(__builtin_amdgcn_update_dpp(0, __float_as_int(x), 0x142, 0xa, 0xf, false));
    x += __int_as_float(__builtin_amdgcn_update_dpp(0, __float_as_int(x), 0x143, 0xc, 0xf, false));
    return __int_as_float(__builtin_amdgcn_readlane(__float_as_int(x), 63));
}
static __device__ __forceinline__ uint32_t bfi(uint32_t mask, uint32_t a, uint32_t b) {
    return __builtin_amdgcn_bitop3_b32(mask, a, b, 0xca);
}
template <uint32_t C> static __device__ __forceinline__ uint32_t vconst() {
    uint32_t r;
    asm volatile("v_mov_b32 %0, %1" : "=v"(r) : "n"(C));
    return r;
}
static __device__ __forceinline__ uint32_t pk_bf16(float a, float b) {
    const f32x2 v = {a, b};
    return __builtin_bit_cast(uint32_t, __builtin_convertvector(v, bf16x2));
}
static __device__ __forceinline__ void pin(uint32_t& x) { asm volatile("" : "+v"(x)); }

#define RTS(k)

static __device__ __forceinline__ void md_of(u32x2 v, const float* w, uint32_t* r) {
    float a0 = bf2f(v.x & 0xffff), a1 = bf2f(v.x >> 16), a2 = bf2f(v.y & 0xffff), a3 = bf2f(v.y >> 16);
#pragma unroll
    for (int j = 0; j < 4; j++) r[j] = f2bf(a0 * w[j] + a1 * w[4 + j] + a2 * w[8 + j] + a3 * w[12 + j]);
}
static __device__ __forceinline__ void load_w(const uint16_t* __restrict__ wdown, float* w) {
    u32x4 wv0 = *(const u32x4*)wdown, wv1 = *(const u32x4*)(wdown + 8);
#pragma unroll
    for (int i = 0; i < 4; i++) {
        w[2 * i] = bf2f(wv0[i] & 0xffffu); w[2 * i + 1] = bf2f(wv0[i] >> 16);
        w[8 + 2 * i] = bf2f(wv1[i] & 0xffffu); w[8 + 2 * i + 1] = bf2f(wv1[i] >> 16);
    }
}
#define LDS __attribute__((address_space(3)))
#define P_CNT 0
#define P_FLAG ((NB + 64) * 4)
#define P_LST (P_FLAG + 256)
#define LCAP 32
#define P_TAB (P_LST + 4 * LCAP * 16)
#define P_BUF (P_TAB + NB * 4)
#define P_LDS (P_BUF + NB * 64)
#define PD 2
#define LPOS 0x448763
#define SETP(n) __builtin_amdgcn_s_setprio(n)
#define MDL 1
#define MDM 3
#define RMAXREV 40.0f
#define HB 88
#define HREG(S, D) (HB + ((S) & 3) * 10 + (((S) >> 2) + (D)) * 2)

template <int N> struct IC { static constexpr int value = N; };
struct Raw { u32x4 o[2], i[2]; u32x2 a[2]; };
static __device__ __forceinline__ uint32_t lds_ld(uint32_t a) { return *(const LDS uint32_t*)(uintptr_t)a; }
static __device__ __forceinline__ void lds_st(uint32_t a, uint32_t v) { *(LDS uint32_t*)(uintptr_t)a = v; }
static __device__ __forceinline__ void lds_or(uint32_t a, uint32_t v) {
    __hip_atomic_fetch_or((LDS uint32_t*)(uintptr_t)a, v, __ATOMIC_RELAXED, __HIP_MEMORY_SCOPE_WORKGROUP);
}
static __device__ __forceinline__ uint32_t lds_add(uint32_t a, uint32_t v) {
    return __hip_atomic_fetch_add((LDS uint32_t*)(uintptr_t)a, v, __ATOMIC_RELAXED, __HIP_MEMORY_SCOPE_WORKGROUP);
}
static __device__ __forceinline__ uint32_t slot_w(uint32_t ca, uint32_t cv) {
    return (ca << 4) | __builtin_amdgcn_bitop3_b32(cv >> 11, ca, 0x3cu, 0x28);
}
static __device__ __forceinline__ uint32_t slot_x2(uint32_t aw, uint32_t ca) {
    return __builtin_amdgcn_bitop3_b32(aw, ca, 0xcu, 0x72);
}
static __device__ __forceinline__ uint32_t slot_x(uint32_t aw) { return slot_x2(aw, aw >> 4); }
template <int R> static __device__ __forceinline__ u32x2 hget() {
    u32x2 d;
    asm volatile("v_mov_b64 %0, v[%1:%2]" : "=v"(d) : "n"(R), "n"(R + 1));
    return d;
}
static __device__ __forceinline__ u32x2 hget_at(int r) {
    u32x2 d;
    asm volatile("s_set_gpr_idx_on %2, gpr_idx(SRC0)\n\ts_nop 1\n\tv_mov_b32 %0, v88\n\tv_mov_b32 %1, v89\n\ts_set_gpr_idx_off"
                 : "=&v"(d.x), "=&v"(d.y) : "s"(r));
    return d;
}
static __device__ __forceinline__ void hset_at(int r, u32x2 d) {
    asm volatile("s_set_gpr_idx_on %2, gpr_idx(DST)\n\ts_nop 1\n\tv_mov_b32 v88, %0\n\tv_mov_b32 v89, %1\n\ts_set_gpr_idx_off"
                 :: "v"(d.x), "v"(d.y), "s"(r));
}

template <int S, int D> static __device__ __forceinline__ uint32_t take(uint32_t V, uint32_t u16, uint32_t la, u32x2 nul) {
    const uint32_t a = __builtin_amdgcn_bitop3_b32(V, u16, 15u, 0x54);
    asm volatile("v_mov_b64 v[%4:%4+1], v[%3:%3+1]\n\tv_cmpx_ne_u32 vcc, -16, %0\n\tds_wrxchg_rtn_b64 v[%4:%4+1], %1, %2 offset:%5\n\ts_mov_b64 exec, -1"
                 :: "v"(a), "v"(la), "v"(nul), "n"(HREG(S, D)), "n"(HREG(S, 1 - D)), "n"(S * 256) : "memory", "vcc");
    return a;
}
#define PUT(P) asm volatile("s_bitcmp1_b32 %3, %4\n\ts_cbranch_scc0 1f\n\ts_waitcnt lgkmcnt(%5)\n\tbuffer_store_dwordx4 v[%2:%2+3], %0, %1, 0 offen sc1\n" \
                 "1:\n\ts_bitcmp1_b32 %3, %4+1\n\ts_cbranch_scc0 2f\n\ts_waitcnt lgkmcnt(%5)\n\tbuffer_store_dwordx4 v[%2:%2+3], %0, %1, 0 offen " P "\n\ts_nop 1\n2:" \
                 :: "v"(ad), "s"(rsA), "n"(HREG(S, 0)), "s"(pm), "n"(2 * S), "n"(W) : "memory", "scc")
template <int S, int W, bool WT> static __device__ __forceinline__ void put(u32x4 rsA, uint32_t ad, uint32_t pm) {
    if (WT) PUT("sc1 nt"); else PUT("nt");
}

__global__ __launch_bounds__(NT, 4) __attribute__((amdgpu_num_vgpr(HB / 2))) void part_kernel(const int* __restrict__ iin, const int* __restrict__ iout,
                                                  const uint16_t* __restrict__ ang, const uint16_t* __restrict__ freq,
                                                  const u32x2* __restrict__ m, const uint16_t* __restrict__ wdown,
                                                  char* __restrict__ recs,
                                                  int T, int E, int ntile, int nstat, int ks, int lcap) {
    RTS(0)
    asm volatile("" :: "s"(iin), "s"(iout), "s"(ang), "s"(freq), "s"(m), "s"(wdown), "s"(recs), "s"(T), "s"(E), "s"(ntile), "s"(nstat), "s"(ks), "s"(lcap));
    const int tid0 = threadIdx.x;
    const int wv = __builtin_amdgcn_readfirstlane(tid0 >> 6);
    const int G = T >> 2;
    const int pos = (int)((blockIdx.x & 7) << 5) | (int)(blockIdx.x >> 3);
    float frr;
    unsigned* meta = (unsigned*)(recs - W_RECS);
    const uint32_t gbase = (LBASE + (uint32_t)lcap) * 128u;
    const uint32_t rbytes = gbase + (uint32_t)lcap * 2u;
    const __amdgpu_buffer_rsrc_t rsR = __builtin_amdgcn_make_buffer_rsrc(recs, 0, (int)rbytes, 0x00020000);
    const u32x4 rsA = {(uint32_t)(uintptr_t)recs, (uint32_t)((uintptr_t)recs >> 32) & 0xffffu, rbytes, 0x00020000u};
    const uint32_t g2 = 2u * (uint32_t)ks;
    const uint32_t far = 1u >> ((blockIdx.x >> 3) & 3u);
    const uint32_t SK = SKM + far * (SKH - SKM), SS = SSM + far * (SSH - SSM), UINC = 0x80000001u + SK * 64u;
    const uint32_t spec0 = gbase - 4u;
    auto fixed_tile = [&](int r) __attribute__((always_inline)) -> int { return r * NWG + ((pos + r * 37) & (NWG - 1)); };
    int tl0 = fixed_tile(0), tl1 = fixed_tile(1), tl2;
    uint32_t tk = 0u;
    const __amdgpu_buffer_rsrc_t rsO = __builtin_amdgcn_make_buffer_rsrc((void*)iout, 0, G * 16, 0x00020000);
    const __amdgpu_buffer_rsrc_t rsI = __builtin_amdgcn_make_buffer_rsrc((void*)iin, 0, G * 16, 0x00020000);
    const __amdgpu_buffer_rsrc_t rsG = __builtin_amdgcn_make_buffer_rsrc((void*)ang, 0, G * 8, 0x00020000);
    uint32_t t8 = (uint32_t)tid0 * 8u;
    auto load_one = [&](Raw& r, int tl, int idx) __attribute__((always_inline)) {
        const int j = idx < 4 ? idx >> 1 : idx & 1;
        const uint32_t g0 = (uint32_t)(tl * GR + j * NT);
        __builtin_amdgcn_sched_barrier(0);
        if (idx >= 4) r.a[j] = __builtin_amdgcn_raw_buffer_load_b64(rsG, t8, g0 * 8u, AUX_NT);
        else if (idx & 1) r.i[j] = __builtin_amdgcn_raw_buffer_load_b128(rsI, t8 + t8, g0 * 16u, AUX_NT);
        else r.o[j] = __builtin_amdgcn_raw_buffer_load_b128(rsO, t8 + t8, g0 * 16u, AUX_NT);
        __builtin_amdgcn_sched_barrier(0);
    };
    auto peek = [&](uint32_t at) __attribute__((always_inline)) -> uint32_t {
        return __hip_atomic_load((unsigned*)((char*)meta + at), __ATOMIC_RELAXED, __HIP_MEMORY_SCOPE_AGENT);
    };
    auto ready = [&]() __attribute__((always_inline)) {
        if (blockIdx.x != 0) {
            const uint32_t x = lds_ld(vconst<P_FLAG + 8>());
            while (peek(vconst<M_BEAT * 4>()) == x) __builtin_amdgcn_s_sleep(8);
        }
    };
    auto beat = [&]() __attribute__((always_inline)) {
        (void)__hip_atomic_fetch_add((unsigned*)((char*)meta + vconst<M_BEAT * 4>()), vconst<1>(), __ATOMIC_RELAXED, __HIP_MEMORY_SCOPE_AGENT);
    };
    auto draw = [&](uint32_t at) __attribute__((always_inline)) -> uint32_t {
        return __hip_atomic_fetch_add((unsigned*)((char*)meta + at), vconst<1>(), __ATOMIC_RELAXED, __HIP_MEMORY_SCOPE_AGENT);
    };
    uint32_t la;
    auto tab_at = [&]() __attribute__((always_inline)) -> uint32_t {
        return P_TAB + bfi(0xffu, bfi(0x3cu, t8 >> 2, t8 << 4), t8);
    };
    u32x2 nul;
    uint32_t U[2];
    {
        const uint32_t t = (uint32_t)tid0;
        la = P_BUF + ((t >> 5) << 12) + ((t & 0x18u) << 3) + ((t & 3u) << 4) + ((t & 4u) << 1);
        const uint32_t q = ((t >> 3) ^ (t >> 1 & 2u)) & 3u;
        nul.x = (0x7u >> q) << 31; nul.y = (0xbu >> q) << 31;
        U[0] = (((t >> 1) & 15u) * SS + ((t & 1u) * 64u + (t >> 5)) * SC + BPL(blockIdx.x)) * 128u + (0x80000000u - SK * 64u) + 8u - g2;
        U[1] = U[0] + 32u * SC * 128u;
    }
    auto log_line = [&](bool lg, uint32_t b) __attribute__((always_inline)) -> uint32_t {
        uint32_t lp = 0u;
        if (lg && (t8 & 0x38u) == 0u) {
            ready();
            lp = draw(vconst<M_LOG * 4>());
            __builtin_amdgcn_raw_buffer_store_b16((uint16_t)b, rsR, gbase + lp * 2u, 0, 0);
        }
        return LBASE + (uint32_t)__builtin_amdgcn_ds_bpermute((int)((t8 >> 1) & 0xe0u), (int)lp);
    };
    auto general = [&](int D, bool all, u64 sm) __attribute__((always_inline)) {
        uint32_t todo = __builtin_amdgcn_readfirstlane((uint32_t)sm | (uint32_t)(sm >> 32));
#pragma unroll 1
        while (todo) {
            const uint32_t S = (uint32_t)__builtin_ctz(todo) >> 1;
            todo &= ~(3u << (2u * S));
            pin(t8); pin(la);
            const uint32_t V = lds_ld(P_TAB + (t8 & ~63u) + S * 4u), g = V & 15u;
            const uint32_t col = S * 128u + ((t8 >> 1) & 0x60u) + (t8 >> 8);
            const bool act = all ? V != 0xffffffffu : (V & 0x80000009u) == 9u;
            const int hr = HREG((int)S, D) - HB;
            uint32_t ln = log_line(act && g > 8u + NTAIL, col);
            if (act) {
                const u32x2 N = *(const LDS u32x2*)(uintptr_t)(la + S * 256u);
                *(LDS u32x2*)(uintptr_t)(la + S * 256u) = nul;
                lds_st(P_TAB + (t8 & ~63u) + S * 4u, vconst<0xffffffffu>());
                if (!(g & 1u)) hset_at(hr, N);
                else {
                    uint32_t a, st;
                    if (g < 8u) { a = (V & ~15u) + ((t8 & 0x38u) << 1); st = 8u; }
                    else {
                        if (g <= 8u + NTAIL) ln = TBASE + ((g - 8u) >> 1) * TPL + col * TST + blockIdx.x;
                        a = ln * 128u + (la & 0x38u); st = 64u;
                    }
                    __builtin_amdgcn_raw_buffer_store_b64(hget_at(hr), rsR, a, 0, AUX_SC1);
                    __builtin_amdgcn_raw_buffer_store_b64(N, rsR, a + st, 0, AUX_SC1);
                }
            }
        }
    };
    unsigned epoch = 0;
    u64 pb2 = 0ull;
    u64 fm = 0ull, sm = 0ull;
    auto look = [&](u32x2 c) __attribute__((always_inline)) {
        uint32_t V[2];
        const bool late = __builtin_amdgcn_ballot_w64(((U[0] | U[1]) & 8u) != 0u) != 0ull;
        epoch++;
        asm volatile("v_mov_b32 %0, -1\n\tv_mov_b32 %1, -1\n\t"
                     "v_cmpx_le_u32 %6, %7, %2\n\tv_mov_b32 %0, %4\n\tv_add_u32 %4, %9, %4\n\tv_subrev_u32 %2, %8, %2\n\ts_mov_b64 exec, -1\n\t"
                     "v_cmpx_le_u32 vcc, %8, %3\n\tv_mov_b32 %1, %5\n\tv_add_u32 %5, %9, %5\n\tv_subrev_u32 %3, %8, %3\n\t"
                     "s_or_b64 %6, %6, exec\n\ts_mov_b64 exec, -1"
                     : "=&v"(V[0]), "=&v"(V[1]), "+v"(c.x), "+v"(c.y), "+v"(U[0]), "+v"(U[1]), "=&s"(fm) : "s"(CHV), "n"(CHV), "s"(UINC) : "vcc", "scc");
        *(LDS u32x2*)(uintptr_t)(P_CNT + t8) = c;
        pin(t8);
        const uint32_t tw = tab_at();
        lds_st(tw, V[0]);
        lds_st(tw + 64u, V[1]);
        if (wv == 1) lds_st(P_CNT + NB * 4 - 256 + (t8 >> 1), vconst<0x40000000u>());
        pb2 = __builtin_amdgcn_ballot_w64((int)(V[0] & V[1]) >= 0);
        sm = 0ull;
        if (__builtin_expect(late, 0)) {
            sm = __builtin_amdgcn_ballot_w64((V[0] & 0x80000009u) == 9u || (V[1] & 0x80000009u) == 9u);
#pragma unroll
            for (int e = 0; e < 2; e++)
                if ((V[e] & 0x8000000fu) == 9u + NTAIL) U[e] -= 2u * UINC;
        }
    };
    u32x4 Va, Vb;

    Raw raw[PD];
    {
        const int tid = tid0;
        asm volatile("" ::: "v88", "v127");
        typedef const __attribute__((address_space(4))) u32x4* cptr;
        u32x4 wd0 = ((cptr)(uintptr_t)wdown)[0], wd1 = ((cptr)(uintptr_t)wdown)[1];
        __builtin_amdgcn_sched_barrier(0);
        if (tid == 128) tk = peek(vconst<M_BEAT * 4>());
        if (blockIdx.x == 0 && wv == 3 && (tid & 63) < M_BEAT / 64) {
            __hip_atomic_store(meta + (tid & 63) * 64, 0u, __ATOMIC_RELAXED, __HIP_MEMORY_SCOPE_AGENT);
            __hip_atomic_store(meta + (tid & 63) * 64 + 1, 0u, __ATOMIC_RELAXED, __HIP_MEMORY_SCOPE_AGENT);
        }
        const __amdgpu_buffer_rsrc_t rsE = __builtin_amdgcn_make_buffer_rsrc((void*)m, 0, E * 8, 0x00020000);
        u32x2 mv[5];
        mv[4] = __builtin_amdgcn_raw_buffer_load_b64(rsE, (t8 & 511u) * (uint32_t)(E >> 6), 0, 0);
#pragma unroll
        for (int q = 0; q < 4; q++)
            mv[q] = __builtin_amdgcn_raw_buffer_load_b64(rsE, t8 & 511u, ((blockIdx.x >> 2) * 256u + (blockIdx.x & 3u) * 16u + (uint32_t)wv + 64u * q) * 512u, AUX_NT);
#pragma unroll
        for (int d = 0; d < MDL; d++) load_one(raw[1], tl0, d);
        asm volatile("" : "+s"(wd0), "+s"(wd1));
        const u32x2 ci = {CINIT, CINIT};
        *(LDS u32x2*)(uintptr_t)(P_CNT + tid * 8) = ci;
        if (tid < 64) lds_st(P_CNT + (NB + tid) * 4, 0x40000000u);
        if (tid < 8 && tid != 2) lds_st(P_FLAG + tid * 4, tid == 0 ? 0xffffffffu : 0u);
        if (tid == 128) lds_st(P_FLAG + 8, tk);
        const uint32_t lane = (uint32_t)tid & 63u;
        int e0[4];
        {
            float w[16];
#pragma unroll
            for (int i = 0; i < 4; i++) {
                w[2 * i] = bf2f(wd0[i] & 0xffffu); w[2 * i + 1] = bf2f(wd0[i] >> 16);
                w[8 + 2 * i] = bf2f(wd1[i] & 0xffffu); w[8 + 2 * i + 1] = bf2f(wd1[i] >> 16);
            }
            auto dot = [&](u32x2 v, int c) __attribute__((always_inline)) -> f32x2 {
                const f32x2 w0 = {w[c], w[c + 1]}, w1 = {w[4 + c], w[5 + c]}, w2 = {w[8 + c], w[9 + c]}, w3 = {w[12 + c], w[13 + c]};
                return w0 * bf2f(v.x & 0xffffu) + w1 * bf2f(v.x >> 16) + w2 * bf2f(v.y & 0xffffu) + w3 * bf2f(v.y >> 16);
            };
#pragma unroll
            for (int cp = 0; cp < 2; cp++) {
                if (cp == 1) {
#pragma unroll
                    for (int d = MDL; d < MDM; d++) load_one(raw[1], tl0, d);
                }
                if (blockIdx.x >= SLOTI / 64) continue;
                const f32x2 sd = dot(mv[4], 2 * cp);
#pragma unroll
                for (int e = 0; e < 2; e++) {
                    const int ex = (int)(wave_max(__float_as_uint(sd[e]) & 0x7fffffffu) >> 23);
                    e0[2 * cp + e] = __builtin_amdgcn_readfirstlane(ex == 0 ? 0 : min(max(ex - 129, -100), 100));
                }
                const f32x2 sc = {__uint_as_float((uint32_t)(127 - e0[2 * cp]) << 23), __uint_as_float((uint32_t)(127 - e0[2 * cp + 1]) << 23)};
                u32x4 o;
                u16x2 mx = {0, 0};
                f32x2 sq = {0.f, 0.f}, sg = {0.f, 0.f};
#pragma unroll
                for (int q = 0; q < 4; q++) {
                    const f32x2 y = dot(mv[q], 2 * cp) * sc;
                    const uint32_t hb = pk_bf16(y.x, y.y);
                    const f32x2 x = {__uint_as_float(hb << 16), __uint_as_float(hb & 0xffff0000u)};
                    o[q] = __builtin_bit_cast(uint32_t, __builtin_amdgcn_cvt_pkrtz(x.x, x.y));
                    mx = __builtin_elementwise_max(mx, __builtin_bit_cast(u16x2, hb & 0x7fff7fffu));
                    sq += x * x;
                    sg += x;
                }
                *(LDS u32x4*)(uintptr_t)(P_BUF + (cp * 64u + lane) * 272u + wv * 16) = o;
#pragma unroll
                for (int e = 0; e < 2; e++) {
                    const u32x4 st = {wave_max(mx[e]), __float_as_uint(wave_sum(sq[e])), __float_as_uint(wave_sum(sg[e])), 0u};
                    if (lane == 0u) *(LDS u32x4*)(uintptr_t)(P_LST + ((2 * cp + e) * 16 + wv) * 16) = st;
                }
            }
        }
        __builtin_amdgcn_sched_barrier(0);
        const uint16_t fq = freq[0];
#pragma unroll
        for (int d = MDM; d < 6; d++) load_one(raw[1], tl0, d);
#pragma unroll
        for (int d = 0; d < 6; d++)
            if (((LPOS >> (4 * d)) & 15) < 4) load_one(raw[0], tl1, d);
        __syncthreads();
        u32x4 mo[2];
#pragma unroll
        for (int cp = 0; cp < 2; cp++) mo[cp] = *(const LDS u32x4*)(uintptr_t)(P_BUF + (cp * 64u + (t8 >> 7)) * 272u + ((t8 + t8) & 0xf0u));
        if (blockIdx.x == 0 && wv == 3) asm volatile("s_waitcnt vmcnt(0)" ::: "memory");
        __syncthreads();
#pragma unroll
        for (int s = 0; s < 16; s++) *(LDS u32x2*)(uintptr_t)(la + s * 256) = nul;
        asm volatile("" : "+v"(raw[1].o[0]), "+v"(raw[1].o[1]), "+v"(raw[1].i[0]), "+v"(raw[1].i[1]), "+v"(raw[1].a[0]), "+v"(raw[1].a[1]));
        asm volatile("s_nop 0\n\tv_readfirstlane_b32 %0, %1" : "=s"(frr) : "v"(bf2f(fq) * 0.15915494309189535f));
        if (blockIdx.x < SLOTI / 64) {
            const __amdgpu_buffer_rsrc_t rsM = __builtin_amdgcn_make_buffer_rsrc((void*)(meta + W_MDC / 4), 0, (int)(128u * SLOTI * 4u), 0x00020000);
#pragma unroll
            for (int cp = 0; cp < 2; cp++) {
                const uint32_t row = cp * 64u + (t8 >> 7), c16 = (t8 + t8) & 0xf0u;
                __builtin_amdgcn_raw_buffer_store_b128(mo[cp], rsM, row * (SLOTI * 4u) + c16 + blockIdx.x * 256u, 0, AUX_SC1);
            }
            if (wv == 0) {
                const u32x4 st = *(const LDS u32x4*)(uintptr_t)(P_LST + t8 * 2u);
                const float sm1 = row_sum(__uint_as_float(st.z));
                const u32x2 r = {row_max(st.x) | (row_max(st.x ? 0xffffu - st.x : 0u) << 16), __float_as_uint(row_sum(__uint_as_float(st.y)))};
                if ((lane & 15u) == 15u) {
                    ((u32x2*)(meta + W_STAT / 4))[(lane >> 4) * NWG + blockIdx.x] = r;
                    meta[W_SUM / 4 + (lane >> 4) * NWG + blockIdx.x] = __float_as_uint(sm1);
                }
                if (blockIdx.x == 0 && lane < 4u) meta[M_E0 + lane] = (uint32_t)(lane == 0u ? e0[0] : lane == 1u ? e0[1] : lane == 2u ? e0[2] : e0[3]);
            }
        }
    }
    pin(la);
    RTS(5)

    uint32_t w[8], aw[8], xv[8];
    u64 nm[8];
    auto later2 = [&](int k) __attribute__((always_inline)) {
        uint32_t x0, x1;
        asm volatile("v_lshrrev_b32 %0, 4, %2\n\tv_lshrrev_b32 %1, 4, %5\n\tv_bitop3_b32 %0, %2, %0, 12 bitop3:0x72\n\tv_bitop3_b32 %1, %5, %1, 12 bitop3:0x72\n\t"
                     "s_not_b64 exec, %8\n\tds_write_b32 %2, %3 offset:%10\n\tds_or_b32 %0, %4 offset:%10\n\t"
                     "s_not_b64 exec, %9\n\tds_write_b32 %5, %6 offset:%10\n\tds_or_b32 %1, %7 offset:%10\n\ts_mov_b64 exec, -1"
                     : "=&v"(x0), "=&v"(x1)
                     : "v"(aw[k]), "v"(w[k]), "v"(xv[k]), "v"(aw[k + 1]), "v"(w[k + 1]), "v"(xv[k + 1]), "s"(nm[k]), "s"(nm[k + 1]), "n"(P_BUF) : "memory", "scc");
    };
    auto drain = [&]() __attribute__((always_inline)) {
        if (wv == 1) {
            const uint32_t q = (epoch + 1u) & 3u;
            const uint32_t n = __builtin_amdgcn_readfirstlane(lds_ld(vconst<P_FLAG + 16>() + q * 4u));
            if (__builtin_expect(n != 0u, 0)) {
                pin(t8);
                const uint32_t l = (t8 >> 3) & 63u;
                if (l < min(n, (uint32_t)LCAP)) {
                    const u32x4 e = *(const LDS u32x4*)(uintptr_t)(P_LST + (q * LCAP + l) * 16u);
                    lds_st(P_BUF + e.x, e.y);
                    lds_or(P_BUF + slot_x(e.x), e.z);
                }
                lds_st(vconst<P_FLAG + 16>() + q * 4u, vconst<0>());
            }
        }
    };
    auto again = [&](int D) __attribute__((always_inline)) {
        do {
            general(D, true, fm);
            __syncthreads();
            later2(0); later2(2); later2(4); later2(6);
            drain();
#pragma unroll
            for (int k = 0; k < 8; k++) {
                pin(aw[k]);
                bool due = false;
                if (aw[k] & 0x80000000u) {
                    xv[k] -= CHV;
                    if (xv[k] < CHV) {
                        const uint32_t f = (aw[k] >> 16) & 0x3ffu;
                        aw[k] = slot_w(aw[k] & 0xffffu, xv[k]);
                        xv[k] = f << ((xv[k] >> 8) & 31u);
                        due = true;
                    } else lds_st(vconst<P_FLAG>(), epoch + 1u);
                }
                nm[k] = ~__builtin_amdgcn_ballot_w64(due);
            }
            look(*(const LDS u32x2*)(uintptr_t)(P_CNT + t8));
            asm volatile("s_waitcnt lgkmcnt(0)" ::: "memory");
            __syncthreads();
        } while (__builtin_amdgcn_readfirstlane(lds_ld(vconst<P_FLAG>())) == epoch);
    };
    auto round = [&](auto DD, auto LL, Raw& rc, Raw& rn, int rd) __attribute__((always_inline)) {
        constexpr int D = decltype(DD)::value;
        constexpr bool LAST = decltype(LL)::value == 1, FIRST = decltype(LL)::value == 2;
        const int ng = min(GR, G - tl0 * GR);
        auto hook = [&](int p) __attribute__((always_inline)) {
#pragma unroll
            for (int q = 0; q < 6; q++)
            {
                const int lp = (LPOS >> (4 * q)) & 15;
                if (!LAST && lp < 4 && lp == p) load_one(rc, tl2, q);
                if (!LAST && lp >= 4 && (lp < 8 || lp >= 12 ? lp : lp - 8) == p) load_one(rn, tl1, q);
            }
        };
        uint32_t wn[8], ca[8], px[4], cv[8];
        float mx;
        float cs[2], cm[2] = {0.f, 0.f};
        auto convA = [&](int q) __attribute__((always_inline)) {
            if (LAST) return;
            const uint32_t a = rc.a[q >> 1][q & 1];
            const f32x2 av = {__uint_as_float(a << 16), __uint_as_float(a & 0xffff0000u)};
            f32x2 rv;
            asm("v_pk_mul_f32 %0, %1, %2 op_sel_hi:[1,0]" : "=v"(rv) : "v"(av), "s"((u64)__float_as_uint(frr)));
            mx = __builtin_elementwise_maximum(__builtin_elementwise_maximum(__builtin_fabsf(rv.x), __builtin_fabsf(rv.y)), (q & 1) ? mx : 0.f);
            cs[0] = __builtin_amdgcn_cosf(rv.x); cs[1] = __builtin_amdgcn_cosf(rv.y);
            asm volatile("" : "+v"(mx), "+v"(cs[0]), "+v"(cs[1]));
            if (FIRST) {
                const float c0 = fminf(fmaxf(cs[0], -1.f), 1.f), c1 = fminf(fmaxf(cs[1], -1.f), 1.f);
                cm[0] += c0 + c1; cm[1] += c0 * c0 + c1 * c1;
                if (q == 3 && wv == 4) {
                    const float a0 = wave_sum(cm[0]), a1 = wave_sum(cm[1]);
                    if ((t8 & 0x1f0u) == 0u) meta[W_SUM / 4 + (4 + ((t8 >> 3) & 1u)) * NWG + blockIdx.x] = __float_as_uint((t8 & 8u) ? a1 : a0);
                }
            }
            __builtin_amdgcn_sched_barrier(0);
        };
        auto convB = [&](int k) __attribute__((always_inline)) {
            if (LAST) return;
            const uint32_t o = rc.o[k >> 2][k & 3], i = rc.i[k >> 2][k & 3];
            wn[k] = (__builtin_amdgcn_alignbit(bfi(63u, o, o >> 5), __float_as_uint(cs[k & 1]), 30u) << 14) | (i >> 6);
            ca[k] = (bfi(63u, i, o) << 2) & 0x1ffcu;
            pin(wn[k]); pin(ca[k]);
            __builtin_amdgcn_sched_barrier(0);
        };
        auto convD = [&](int q) __attribute__((always_inline)) {
            if (LAST) return;
            asm volatile("v_cvt_pk_f16_f32 %0, |%1|, |%2|\n\tv_pk_mul_f16 %0, %0, %3 op_sel_hi:[1,0]" : "=v"(px[q]) : "v"(cs[0]), "v"(cs[1]), "s"(0x3ffu));
        };
        uint32_t cst;
        auto check = [&](int j) __attribute__((always_inline)) {
            if (LAST) return;
            if (__builtin_expect((__builtin_amdgcn_ballot_w64(!(mx <= RMAXREV)) | (u64)(uint32_t)(GR - ng)) != 0ull, 0)) {
                pin(t8);
                asm volatile("" : "+v"(rc.a[j]));
                const int lg = j * NT + (int)(t8 >> 3);
#pragma unroll
                for (int k = 0; k < 4; k++) {
                    const uint32_t a = (k & 1) ? (rc.a[j][k >> 1] >> 16) : (rc.a[j][k >> 1] & 0xffffu);
                    const bool nrm = __builtin_fabsf(frr * bf2f(a)) <= RMAXREV;
                    if (!(lg < ng && nrm)) ca[j * 4 + k] = NB * 4u + ((t8 >> 1) & 0xfcu);
                    if (lg < ng && !nrm) {
                        ready();
                        __builtin_amdgcn_raw_buffer_store_b32((uint32_t)((tl0 * GR + lg) * 4 + k), rsR, spec0 - draw(vconst<M_LOG * 4 + 4>()) * 4u, 0, 0);
                    }
                }
            }
            cst = vconst<CSTEP>();
        };
        auto ticket = [&](int k) __attribute__((always_inline)) {
            if (LAST) return;
            cv[k] = lds_add(P_CNT + ca[k], cst);
            __builtin_amdgcn_sched_barrier(0);
        };
        auto insert = [&](int j) __attribute__((always_inline)) {
            const int k0 = 4 * j;
            const bool odd = __builtin_amdgcn_ballot_w64(max(max(cv[k0], cv[k0 + 1]), max(cv[k0 + 2], cv[k0 + 3])) >= 2u * CHV) != 0ull;
#pragma unroll
            for (int k = k0; k < k0 + 4; k += 2) {
                uint32_t x0, x1;
                asm volatile("v_lshrrev_b32 %6, 11, %8\n\tv_lshrrev_b32 %7, 11, %9\n\t"
                             "v_bitop3_b32 %6, %6, %10, 60 bitop3:0x28\n\tv_bitop3_b32 %7, %7, %11, 60 bitop3:0x28\n\t"
                             "v_lshl_or_b32 %0, %10, 4, %6\n\tv_lshl_or_b32 %1, %11, 4, %7\n\t"
                             "v_lshlrev_b32_sdwa %2, %8, %12 dst_sel:DWORD dst_unused:UNUSED_PAD src0_sel:BYTE_1 src1_sel:WORD_0\n\t"
                             "v_lshlrev_b32_sdwa %3, %9, %12 dst_sel:DWORD dst_unused:UNUSED_PAD src0_sel:BYTE_1 src1_sel:WORD_1\n\t"
                             "v_bitop3_b32 %6, %0, %10, 12 bitop3:0x72\n\tv_bitop3_b32 %7, %1, %11, 12 bitop3:0x72\n\t"
                             "v_cmpx_gt_u32 %4, %15, %8\n\tds_write_b32 %0, %13 offset:%16\n\tds_or_b32 %6, %2 offset:%16\n\ts_mov_b64 exec, -1\n\t"
                             "v_cmpx_gt_u32 %5, %15, %9\n\tds_write_b32 %1, %14 offset:%16\n\tds_or_b32 %7, %3 offset:%16\n\ts_mov_b64 exec, -1"
                             : "=&v"(aw[k]), "=&v"(aw[k + 1]), "=&v"(xv[k]), "=&v"(xv[k + 1]), "=&s"(nm[k]), "=&s"(nm[k + 1]), "=&v"(x0), "=&v"(x1)
                             : "v"(cv[k]), "v"(cv[k + 1]), "v"(ca[k]), "v"(ca[k + 1]), "v"(px[k >> 1]), "v"(wn[k]), "v"(wn[k + 1]), "s"(CHV), "n"(P_BUF) : "memory");
                w[k] = wn[k]; w[k + 1] = wn[k + 1];
                if (__builtin_expect(odd, 0)) {
#pragma unroll
                    for (int q = k; q < k + 2; q++) {
                        nm[q] |= __builtin_amdgcn_ballot_w64(cv[q] >= 2u * CHV);
                        if (cv[q] >= 2u * CHV && cv[q] < 0x40000000u) {
                            const uint32_t lq = (epoch + 2u) & 3u;
                            uint32_t p = LCAP;
                            if (cv[q] < 3u * CHV) p = lds_add(vconst<P_FLAG + 16>() + lq * 4u, vconst<1>());
                            if (p < LCAP) {
                                const u32x2 e = {aw[q], w[q]};
                                *(LDS u32x2*)(uintptr_t)(P_LST + (lq * LCAP + p) * 16u) = e;
                                lds_st(P_LST + (lq * LCAP + p) * 16u + 8u, xv[q]);
                            } else {
                                aw[q] = ca[q] | ((q & 1) ? (px[q >> 1] & 0xffff0000u) : (px[q >> 1] << 16)) | 0x80000000u;
                                xv[q] = cv[q] - CHV;
                                lds_st(vconst<P_FLAG>(), epoch);
                            }
                        }
                    }
                }
            }
        };
        constexpr int P0 = D ? 0 : 3, P1 = D ? 1 : 2, P2 = D ? 2 : 1, P3 = D ? 3 : 0;
        const uint32_t fl = lds_ld(vconst<P_FLAG>());
        if (!LAST) asm volatile("" : "+v"(rc.o[0]), "+v"(rc.o[1]), "+v"(rc.i[0]), "+v"(rc.i[1]), "+v"(rc.a[0]), "+v"(rc.a[1]));
        if (!LAST && (rd + 4 >= nstat || tl1 >= ntile) && wv == 2) {
            pin(tk);
            if ((t8 & 511u) == 0u) {
                if (tl1 >= ntile) {
                    if (rd + 3 <= nstat) ready();
                    (void)draw(vconst<M_MD * 4>());
                } else if (rd + 4 == nstat) tk = peek(vconst<M_BEAT * 4>());
                else {
                    if (rd + 3 == nstat) {
                        if (tk == lds_ld(vconst<P_FLAG + 8>())) ready();
                        tk = 0u;
                    }
                    lds_st(vconst<P_FLAG + 4>(), tk);
                    if ((int)tk < ntile - nstat * NWG) tk = draw(vconst<M_TILE * 4>());
                }
            }
        }
        if (!LAST && blockIdx.x == 0 && wv == 3 && (t8 & 511u) == 0u) beat();
        hook(12);
        pin(t8);
        const uint32_t tr = P_TAB + (t8 & ~63u), u16 = (t8 + t8) & 0x70u;
        convA(0);
        convB(0);
        const bool ag = __builtin_amdgcn_readfirstlane(fl) == epoch;
        if (!FIRST && __builtin_expect(LAST || ag || sm != 0ull, 0)) {
            if (ag) again(D);
            general(D, false, sm);
            Va = *(const LDS u32x4*)(uintptr_t)(P_TAB + (t8 & ~63u) + P0 * 16);
            Vb = *(const LDS u32x4*)(uintptr_t)(P_TAB + (t8 & ~63u) + P1 * 16);
            asm volatile("" : "+v"(Va), "+v"(Vb));
        }
        uint32_t pq = (uint32_t)pb2 | (uint32_t)(pb2 >> 32);
        pq = (pq | (pq >> 1)) & 0x55555555u;
        pq = __builtin_amdgcn_readfirstlane(pq + (pq & (far * NEARM)));
        uint32_t ad[8];
        if (!FIRST) ad[0] = take<4 * P0 + 0, D>(Va[0], u16, la, nul);
        convB(1);
        if (!FIRST) ad[1] = take<4 * P0 + 1, D>(Va[1], u16, la, nul);
        convD(0);
        convA(1);
        if (!FIRST) ad[2] = take<4 * P0 + 2, D>(Va[2], u16, la, nul);
        convB(2);
        hook(13);
        if (!FIRST) ad[3] = take<4 * P0 + 3, D>(Va[3], u16, la, nul);
        convB(3);
        if (!FIRST) put<4 * P0 + 0, LAST ? 3 : 3, LAST>(rsA, ad[0], pq);
        if (!FIRST) ad[4] = take<4 * P1 + 0, D>(Vb[0], u16, la, nul);
        convD(1);
        check(0);
        ticket(0);
        hook(4);
        if (!FIRST) put<4 * P0 + 1, LAST ? 3 : 4, LAST>(rsA, ad[1], pq);
        if (!FIRST) ad[5] = take<4 * P1 + 1, D>(Vb[1], u16, la, nul);
        convA(2);
        ticket(1);
        if (!FIRST) put<4 * P0 + 2, LAST ? 3 : 5, LAST>(rsA, ad[2], pq);
        if (!FIRST) ad[6] = take<4 * P1 + 2, D>(Vb[2], u16, la, nul);
        convB(4);
        ticket(2);
        if (!FIRST) put<4 * P0 + 3, LAST ? 3 : 6, LAST>(rsA, ad[3], pq);
        if (!FIRST) Va = *(const LDS u32x4*)(uintptr_t)(tr + P2 * 16);
        __builtin_amdgcn_sched_barrier(0);
        if (!FIRST) ad[7] = take<4 * P1 + 3, D>(Vb[3], u16, la, nul);
        convB(5);
        ticket(3);
        SETP(2);
        if (!FIRST) put<4 * P1 + 0, LAST ? 4 : 8, LAST>(rsA, ad[4], pq);
        if (!FIRST) ad[0] = take<4 * P2 + 0, D>(Va[0], u16, la, nul);
        convD(2);
        convA(3);
        hook(5);
        if (!FIRST) put<4 * P1 + 1, LAST ? 4 : 7, LAST>(rsA, ad[5], pq);
        if (!FIRST) ad[1] = take<4 * P2 + 1, D>(Va[1], u16, la, nul);
        convB(6);
        if (!FIRST) put<4 * P1 + 2, LAST ? 4 : 6, LAST>(rsA, ad[6], pq);
        if (!FIRST) ad[2] = take<4 * P2 + 2, D>(Va[2], u16, la, nul);
        convB(7);
        if (!FIRST) put<4 * P1 + 3, LAST ? 3 : 4, LAST>(rsA, ad[7], pq);
        if (!FIRST) Vb = *(const LDS u32x4*)(uintptr_t)(tr + P3 * 16);
        __builtin_amdgcn_sched_barrier(0);
        if (!FIRST) ad[3] = take<4 * P2 + 3, D>(Va[3], u16, la, nul);
        convD(3);
        check(1);
        ticket(4);
        SETP(1);
        hook(6);
        if (!FIRST) put<4 * P2 + 0, LAST ? 4 : 5, LAST>(rsA, ad[0], pq);
        if (!FIRST) ad[4] = take<4 * P3 + 0, D>(Vb[0], u16, la, nul);
        ticket(5);
        if (!FIRST) put<4 * P2 + 1, LAST ? 4 : 6, LAST>(rsA, ad[1], pq);
        if (!FIRST) ad[5] = take<4 * P3 + 1, D>(Vb[1], u16, la, nul);
        ticket(6);
        if (!FIRST) put<4 * P2 + 2, LAST ? 4 : 7, LAST>(rsA, ad[2], pq);
        if (!FIRST) ad[6] = take<4 * P3 + 2, D>(Vb[2], u16, la, nul);
        ticket(7);
        SETP(0);
        if (!FIRST) put<4 * P2 + 3, LAST ? 3 : 7, LAST>(rsA, ad[3], pq);
        if (!FIRST) ad[7] = take<4 * P3 + 3, D>(Vb[3], u16, la, nul);
        hook(7);
        SETP(3);
        asm volatile("s_waitcnt lgkmcnt(0)" ::: "memory");
        __syncthreads();
        u32x2 cn;
        uint32_t tv;
        if (!LAST) { cn = *(const LDS u32x2*)(uintptr_t)(P_CNT + t8); tv = lds_ld(vconst<P_FLAG + 4>()); }
        __builtin_amdgcn_sched_barrier(0);
        if (!FIRST) later2(0);
        if (!FIRST) put<4 * P3, 15, LAST>(rsA, ad[4], pq);
        if (!FIRST) later2(2);
        if (!FIRST) put<4 * P3 + 1, 15, LAST>(rsA, ad[5], pq);
        if (!FIRST) drain();
        if (!LAST) {
            tl2 = rd + 2 < nstat ? fixed_tile(rd + 2) : nstat * NWG + (int)__builtin_amdgcn_readfirstlane(tv);
            look(cn);
        }
        hook(0);
        SETP(2);
        if (!FIRST) later2(4);
        if (!FIRST) put<4 * P3 + 2, 15, LAST>(rsA, ad[6], pq);
        if (!FIRST) later2(6);
        if (!FIRST) put<4 * P3 + 3, 15, LAST>(rsA, ad[7], pq);
        if (!LAST) {
            hook(1);
            SETP(1);
            insert(0);
            hook(2);
            SETP(0);
            pin(t8);
            Va = *(const LDS u32x4*)(uintptr_t)(P_TAB + (t8 & ~63u) + P3 * 16);
            Vb = *(const LDS u32x4*)(uintptr_t)(P_TAB + (t8 & ~63u) + P2 * 16);
            __builtin_amdgcn_sched_barrier(0);
            insert(1);
            hook(3);
            SETP(3);
            asm volatile("s_waitcnt lgkmcnt(0)" ::: "memory");
            __syncthreads();
            tl0 = tl1; tl1 = tl2;
        }
    };
#define HMV(A, B) "v_mov_b64 v[" #A ":" #A "+1], v[" #B ":" #B "+1]\n\t"
#define HMV4(A) HMV(A + 8, A + 6) HMV(A + 6, A + 4) HMV(A + 4, A + 2) HMV(A + 2, A)
    round(IC<1>(), IC<2>(), raw[1], raw[0], 0);
    int rd = 1;
#pragma unroll 1
    for (; tl1 < ntile; rd += 2) {
        round(IC<0>(), IC<0>(), raw[0], raw[1], rd);
        round(IC<1>(), IC<0>(), raw[1], raw[0], rd + 1);
    }
    if (tl0 >= ntile) asm volatile(HMV4(88) HMV4(98) HMV4(108) HMV4(118));
    asm volatile("" : "+s"(tl0));
    if (tl0 < ntile) round(IC<0>(), IC<0>(), raw[0], raw[1], rd);
    RTS(1)
    uint32_t cpf = 0u;
    if (wv == 5) {
        u64 ca;
        asm volatile("s_getpc_b64 vcc\n\ts_add_u32 vcc_lo, vcc_lo, acc_kernel@rel32@lo+4\n\ts_addc_u32 vcc_hi, vcc_hi, acc_kernel@rel32@hi+12\n\ts_mov_b64 %0, vcc" : "=s"(ca) :: "scc", "vcc");
        cpf = __builtin_amdgcn_raw_buffer_load_b32(__builtin_amdgcn_make_buffer_rsrc((void*)ca, 0, CPF, 0x00020000), ((blockIdx.x >> 3) * 64u + ((t8 >> 3) & 63u)) * 64u, 0, 0);
    }
    round(IC<1>(), IC<1>(), raw[1], raw[0], 0);
    if (__builtin_amdgcn_readfirstlane(lds_ld(P_FLAG + 16 + ((epoch + 2u) & 3u) * 4u)) != 0u) {
        asm volatile("s_waitcnt lgkmcnt(0)" ::: "memory");
        __syncthreads();
        look(*(const LDS u32x2*)(uintptr_t)(P_CNT + t8));
        general(0, true, fm);
        __syncthreads();
        drain();
    }
    {
        uint32_t* tcnt = meta + W_TCNT / 4;
        const u32x2 c = *(const LDS u32x2*)(uintptr_t)(P_CNT + t8);
        const uint32_t tw = tab_at();
#pragma unroll
        for (int e = 0; e < 2; e++) {
            const uint32_t g = U[e] & 15u;
            lds_st(tw + e * 64u, g >= 8u ? 0x80000000u : ((U[e] + ((g & 1u) ? 0u : SK * 64u)) & 0x7fffff81u) | (((7u - g) << 1) & 12u));
            *(LDS uint8_t*)(uintptr_t)(P_CNT + (t8 & ~511u) + (((tw - P_TAB) >> 2) & 127u) + e * 16u) =
                (uint8_t)(g < 8u ? 0u : ((g - 8u) >> 1) + (((c[e] + 2u * CSTEP) >> 15) << 2) + (g & 1u) * 0x50u);
        }
        asm volatile("s_waitcnt lgkmcnt(0)" ::: "memory");
        __syncthreads();
        const uint32_t u16 = (t8 + t8) & 0x70u;
        u32x4 En[4];
#pragma unroll
        for (int p = 0; p < 4; p++) En[p] = *(const LDS u32x4*)(uintptr_t)(P_TAB + (t8 & ~63u) + p * 16u);
        const u32x4 cb = *(const LDS u32x4*)(uintptr_t)(P_CNT + (t8 & ~511u) + ((t8 >> 2) & 0x70u));
        u32x2 N[16];
#pragma unroll
        for (int s = 0; s < 16; s++) N[s] = *(const LDS u32x2*)(uintptr_t)(la + s * 256u);
        if (u16 == 0u) *(u32x4*)(tcnt + blockIdx.x * 512u + (t8 >> 4)) = cb;
        const uint32_t sg = P_BUF + ((t8 & ~63u) << 4);
        uint32_t ps;
        auto rest = [&](auto SL) __attribute__((always_inline)) {
            constexpr int s = decltype(SL)::value;
            const uint32_t en = En[s >> 2][s & 3], by = cb[s >> 2] >> ((s & 3) * 8);
            const u32x2 Hr = hget<HREG(s, 0)>();
            const uint32_t at = ps + ((la & 0x38u) ^ ((s & 3) << 4));
            *(LDS u32x2*)(uintptr_t)at = Hr;
            *(LDS u32x2*)(uintptr_t)(at + (by & 0x40u)) = N[s];
            ps += (by & 0x3cu) << 2;
            const u32x2 Hs = (en & 1u) ? Hr : nul;
            const u32x4 d = {Hs.x, Hs.y, N[s].x, N[s].y};
            uint32_t a = __builtin_amdgcn_bitop3_b32(en, u16, 15u, 0x54);
            {
                const u32x4 rs_ = rsA;
                const uint32_t far_ = ((NEARM >> (2 * s)) & 1u) ? far : 0u;
                asm volatile("s_cmp_eq_u32 %3, 0\n\ts_cbranch_scc0 1f\n\tbuffer_store_dwordx4 %0, %1, %2, 0 offen sc1\n\ts_branch 2f\n"
                             "1:\n\tbuffer_store_dwordx4 %0, %1, %2, 0 offen sc1 nt\n2:\n\ts_nop 1" :: "v"(d), "v"(a), "s"(rs_), "s"(far_) : "memory", "scc");
            }
            if (__builtin_expect(__builtin_amdgcn_ballot_w64((en & 12u) != 0u) != 0ull, 0)) {
                const u32x4 dn = {nul.x, nul.y, nul.x, nul.y};
#pragma unroll 1
                for (uint32_t k = (en >> 2) & 3u; k > 0u; k--) {
                    a += SK * 128u;
                    __builtin_amdgcn_raw_buffer_store_b128(dn, rsR, a, 0, AUX_SC1);
                }
            }
        };
        auto pack = [&](int p) __attribute__((always_inline)) {
            const uint32_t a = (SBASE + ((((t8 >> 1) & 0x60u) + (t8 >> 8)) * 2u + p) * (NWG * 9u) + blockIdx.x * 9u) * 128u + u16;
            asm volatile("" ::: "memory");
#pragma unroll 1
            for (uint32_t q = 0; __builtin_amdgcn_ballot_w64(sg + q < ps) != 0ull; q += 256u) {
                const u32x4 d0 = *(const LDS u32x4*)(uintptr_t)(sg + q + u16), d1 = *(const LDS u32x4*)(uintptr_t)(sg + q + 128u + u16);
                __builtin_amdgcn_raw_buffer_store_b128(d0, rsR, sg + q < ps ? a + q : 0xffffff00u, 0, AUX_SC1);
                __builtin_amdgcn_raw_buffer_store_b128(d1, rsR, sg + q + 128u < ps ? a + q + 128u : 0xffffff00u, 0, AUX_SC1);
            }
            asm volatile("" ::: "memory");
        };
        asm volatile("" : "+v"(N[0]), "+v"(N[1]), "+v"(N[2]), "+v"(N[3]), "+v"(N[4]), "+v"(N[5]), "+v"(N[6]), "+v"(N[7]) :: "memory");
        asm volatile("" : "+v"(N[8]), "+v"(N[9]), "+v"(N[10]), "+v"(N[11]), "+v"(N[12]), "+v"(N[13]), "+v"(N[14]), "+v"(N[15]) :: "memory");
        ps = sg;
        rest(IC<0>()); rest(IC<1>()); rest(IC<2>()); rest(IC<3>()); rest(IC<4>()); rest(IC<5>()); rest(IC<6>()); rest(IC<7>());
        pack(0);
        ps = sg;
        rest(IC<8>()); rest(IC<9>()); rest(IC<10>()); rest(IC<11>()); rest(IC<12>()); rest(IC<13>()); rest(IC<14>()); rest(IC<15>());
        pack(1);
    }
    RTS(2)
    asm volatile("" :: "v"(cpf));
    if (blockIdx.x == 0 && t8 == 1536u) {
        do {
            beat();
            __builtin_amdgcn_s_sleep(8);
        } while (peek(vconst<M_MD * 4>()) < NWG);
    }
    RTS(3)
    RTS(4)
}

#define A_MD 0
#define A_ACC (SLOTI * 4)
#define A_FLAG (A_ACC + SLOTO * 4)
#define A_LDS (A_FLAG + 512)
static_assert(SLOTI * 4 == 3 * NT * 16 + 1024, "md staging assumes three full vectors and 256 dwords");
#define BIAS 0x40004000u
#define NFAST 8
#define NLD 10
#define MDF 0x0a11333
#define MDP 0xaaaaa
#define HP 2
#define SHARE (SLOTO / 8)
#define SPINCAP 4096u
#define ZSIG 9.0f
#define HSLOW 0x80008000u
#define GAS __attribute__((address_space(1)))

extern "C" __global__ __launch_bounds__(NT, 4) __attribute__((amdgpu_num_vgpr(53))) void acc_kernel(char* ws, int T, int E, int ks, int lcap, float zf, int pad,
                                                 const uint16_t* __restrict__ centers, const uint16_t* __restrict__ wrbf,
                                                 const uint16_t* __restrict__ wsbf, const uint16_t* __restrict__ wup,
                                                 const u32x2* __restrict__ m, const uint16_t* __restrict__ dist, u32x2* __restrict__ out,
                                                 const int* __restrict__ iin, const int* __restrict__ iout, const uint16_t* __restrict__ ang,
                                                 const uint16_t* __restrict__ wdown, const uint16_t* __restrict__ freq) {
    unsigned long long tf_; asm volatile("s_memrealtime %0" : "=s"(tf_));
    unsigned leak_ = 0;
    const char* recs = ws + W_RECS;
    const u32x4* tcnt = (const u32x4*)(ws + W_TCNT);
    const uint32_t* mdc = (const uint32_t*)(ws + W_MDC);
    const u32x4* stat = (const u32x4*)(ws + W_STAT);
    uint32_t* partial = (uint32_t*)(ws + W_PART);
    unsigned* meta = (unsigned*)ws;
    extern __shared__ __attribute__((aligned(16))) char smem[];
    RTS(8)
    unsigned* acc = (unsigned*)(smem + A_ACC);
    unsigned* s_flag = (unsigned*)(smem + A_FLAG);
    const int tid = threadIdx.x;
    const int lane = tid & 63;
    const int wv = __builtin_amdgcn_readfirstlane(tid >> 6);
    const int cp = (blockIdx.x >> 3) & 1;
    const int h = blockIdx.x & 3;
    const int j = ((blockIdx.x >> 2) & 1) * 16 + (blockIdx.x >> 4);
    u32x4 tc;
    const __amdgpu_buffer_rsrc_t rsM = __builtin_amdgcn_make_buffer_rsrc((void*)mdc, 0, (int)(128u * SLOTI * 4u), 0x00020000);

    u32x4 pm[4];
    u32x4 ring[NLD];
    auto load_md = [&](int ff, int pl, int mdp) __attribute__((always_inline)) {
        const uint32_t so = (uint32_t)(cp * 64 + ff * 4 + h) * (SLOTI * 4u);
#pragma unroll
        for (int v = 0; v < 3; v++)
            if (pl < 0 || pl == ((mdp >> (4 * v)) & 15)) pm[v] = __builtin_amdgcn_raw_buffer_load_b128(rsM, (uint32_t)tid * 16u, so + (uint32_t)v * (NT * 16u), 0);
        if ((pl < 0 || pl == (mdp & 15)) && wv == 0) pm[3] = __builtin_amdgcn_raw_buffer_load_b128(rsM, (uint32_t)tid * 16u, so + 3u * NT * 16u, 0);
        __builtin_amdgcn_sched_barrier(0);
    };
    const uint32_t lf = (uint32_t)(tid >> 9) * (SKH * 128u) + (uint32_t)(tid & 0x1e0) * 16u + (uint32_t)(tid & 12) * 32u + (uint32_t)(tid & 16) * 4u + (uint32_t)(tid & 3) * 16u;
    const uint32_t lp = (uint32_t)(tid >> 8) * (SKM * 128u) + (uint32_t)((tid >> 2) & 63) * 128u + (uint32_t)(tid & 3) * 16u;
    auto load_main = [&](int i, int ff, bool near = false) __attribute__((always_inline)) -> u32x4 {
        const uint32_t q = (uint32_t)(((i >> 1) - HP) & 3), SK = q ? SKM : SKH, SS = q ? SSM : SSH;
        const __amdgpu_buffer_rsrc_t rs = __builtin_amdgcn_make_buffer_rsrc((void*)(recs + (q * SQ + (uint32_t)ff * SS + (uint32_t)(h * 32 + j) * SC) * 128u), 0, ks * (int)(SK * 128u), 0x00020000);
        if (q == 0u && near) return __builtin_amdgcn_raw_buffer_load_b128(rs, lf, (i & 1) * (int)(2u * SK * 128u), 0);
        if (q == 0u) return __builtin_amdgcn_raw_buffer_load_b128(rs, lf, (i & 1) * (int)(2u * SK * 128u), AUX_NT);
        return __builtin_amdgcn_raw_buffer_load_b128(rs, lp + (uint32_t)(i & 1) * 64u, 0, 0);
    };
    const __amdgpu_buffer_rsrc_t rsT = __builtin_amdgcn_make_buffer_rsrc((void*)recs, 0, (int)(LBASE * 128u), 0x00020000);
    const uint32_t tof = (uint32_t)(lane & 2) * (TPL * 64u) + (uint32_t)(tid >> 2) * 128u + (uint32_t)(lane & 1) * 64u + 4u + (TBASE + (uint32_t)(h * 32 + j) * TST) * 128u;
    const uint32_t sp0 = (SBASE + (uint32_t)(h * 32 + j) * (2u * NWG * 9u) + (uint32_t)(tid >> 2) * 9u) * 128u;
    auto tail_rank = [&](uint32_t c, uint32_t sp, int ff, uint32_t t0, int& dst, uint32_t& en) __attribute__((always_inline)) -> int {
        const int k4 = (int)(((uint32_t)(lane & 3) + t0) * 4u) - (int)((c & 3u) * 8u);
        const int left = (int)((c >> 2) & 15u) - k4;
        const bool has = left > 0;
        uint32_t ea = tof + (uint32_t)ff * (128u * TST * 128u), eb = sp + (uint32_t)(k4 * 16 + min(max(left, 0), 4));
        asm volatile("" : "+v"(ea), "+v"(eb));
        en = k4 < 0 ? ea : eb;
        const u64 mask = __builtin_amdgcn_ballot_w64(has);
        const int n = __builtin_popcountll(mask);
        const int rank = __builtin_amdgcn_mbcnt_hi((uint32_t)(mask >> 32), __builtin_amdgcn_mbcnt_lo((uint32_t)mask, 0u));
        const int r = has ? rank : (n + lane - rank);
        dst = ((r & 15) << 4) | ((r >> 4) << 2);
        return n;
    };
    auto load_tail = [&](uint32_t en, u64& tm) __attribute__((always_inline)) -> u32x4 {
        const bool on = (uint32_t)(lane & 3) < (en & 7u);
        tm = __builtin_amdgcn_ballot_w64(on);
        return __builtin_amdgcn_raw_buffer_load_b128(rsT, on ? (en & ~15u) + (uint32_t)(lane & 3) * 16u : 0xffffff00u, 0, 0);
    };
    f32x2 fa[4][4];
    u32x4 em[2];
    u32x2 dw2;
    uint32_t wl;
    const __amdgpu_buffer_rsrc_t rsE = __builtin_amdgcn_make_buffer_rsrc((void*)m, 0, E * 8, 0x00020000);
    const __amdgpu_buffer_rsrc_t rsD = __builtin_amdgcn_make_buffer_rsrc((void*)dist, 0, E * 2, 0x00020000);
    auto acc_vec = [&](int i) __attribute__((always_inline)) -> u32x4 {
        const uint32_t a = A_ACC + (uint32_t)(i >> 4) * 256u + (uint32_t)(i & 15) * 8u;
        const u32x2 x = *(const LDS u32x2*)(uintptr_t)a, y = *(const LDS u32x2*)(uintptr_t)(a + 128u);
        const u32x4 v = {x.x, x.y, y.x, y.y};
        return v;
    };
    auto edge0 = [&](int sh) __attribute__((always_inline)) -> uint32_t {
        return (tid < SHARE / 4) ? (uint32_t)(((sh * (SHARE / 64) + (tid >> 4)) << 11) | (j << 6) | ((tid & 15) * 2)) : 0x0fffff00u;
    };
    auto load_m = [&](int sh) __attribute__((always_inline)) {
        em[0] = __builtin_amdgcn_raw_buffer_load_b128(rsE, edge0(sh) * 8u, 0, AUX_NT);
        em[1] = __builtin_amdgcn_raw_buffer_load_b128(rsE, edge0(sh) * 8u + 256u, 0, AUX_NT);
    };
    auto load_d = [&](int sh) __attribute__((always_inline)) {
        dw2.x = __builtin_amdgcn_raw_buffer_load_b32(rsD, edge0(sh) * 2u, 0, 0);
        dw2.y = __builtin_amdgcn_raw_buffer_load_b32(rsD, edge0(sh) * 2u + 64u, 0, 0);
    };
    auto load_wl = [&]() __attribute__((always_inline)) {
        const u64 p0 = (u64)wsbf, p1 = (u64)wrbf, p2 = (u64)centers, p3 = (u64)wup;
        const u64 pa = lane < 16 ? p0 : p1, pb = lane < 40 ? p2 : p3;
        wl = ((const GAS uint32_t*)(lane < 32 ? pa : pb))[lane & (lane < 32 ? 15 : lane < 40 ? 3 : 7)];
    };
    auto factor = [&](int r, bool first) __attribute__((always_inline)) {
        const float sq = 3.7982826f;
        const f32x2 d01 = {bf2f(dw2.x & 0xffffu), bf2f(dw2.x >> 16)}, d23 = {bf2f(dw2.y & 0xffffu), bf2f(dw2.y >> 16)};
        const uint32_t cw = __builtin_amdgcn_readlane(wl, 32 + (r >> 1));
        const float cn = bf2f((r & 1) ? (cw >> 16) : (cw & 0xffffu));
        f32x2 w[4];
#pragma unroll
        for (int k = 0; k < 4; k++) {
            const uint32_t x = __builtin_amdgcn_readlane(wl, (k >> 1) * 16 + r * 2 + (k & 1));
            w[k].x = bf2f(x & 0xffffu); w[k].y = bf2f(x >> 16);
        }
        const f32x2 y01 = (d01 - cn) * sq, y23 = (d23 - cn) * sq;
        const f32x2 t01 = -y01 * y01, t23 = -y23 * y23;
        const float rb[4] = {__builtin_amdgcn_exp2f(t01.x), __builtin_amdgcn_exp2f(t01.y), __builtin_amdgcn_exp2f(t23.x), __builtin_amdgcn_exp2f(t23.y)};
#pragma unroll
        for (int q = 0; q < 4; q++) {
#pragma unroll
            for (int k = 0; k < 4; k++) fa[q][k] = first ? w[k] * rb[q] : __builtin_elementwise_fma(w[k], (f32x2)(rb[q]), fa[q][k]);
        }
    };
    auto edges = [&](int sh) __attribute__((always_inline)) {
        load_d(sh);
        load_m(sh);
        factor(0, true);
#pragma unroll 1
        for (int r = 1; r < 8; r++) factor(r, false);
    };
#define ADT(V, O) "ds_write_addtid_b32 " V " offset:" #O "\n\t"
    auto store_md = [&]() __attribute__((always_inline)) {
        asm volatile("s_mov_b32 m0, %12\n\ts_nop 0\n\t" ADT("%0", 0) ADT("%1", 256) ADT("%2", 512) ADT("%3", 768) ADT("%4", 16384) ADT("%5", 16640) ADT("%6", 16896) ADT("%7", 17152)
                     ADT("%8", 32768) ADT("%9", 33024) ADT("%10", 33280) ADT("%11", 33536)
                     :: "v"(pm[0][0]), "v"(pm[0][1]), "v"(pm[0][2]), "v"(pm[0][3]), "v"(pm[1][0]), "v"(pm[1][1]), "v"(pm[1][2]), "v"(pm[1][3]),
                        "v"(pm[2][0]), "v"(pm[2][1]), "v"(pm[2][2]), "v"(pm[2][3]), "s"(wv * 1024) : "memory");
        if (wv == 0) asm volatile("s_mov_b32 m0, 0\n\ts_nop 0\n\t" ADT("%0", 49152) ADT("%1", 49408) ADT("%2", 49664) ADT("%3", 49920)
                                  :: "v"(pm[3][0]), "v"(pm[3][1]), "v"(pm[3][2]), "v"(pm[3][3]) : "memory");
    };
    const int me = h * 2 + cp;
    GAS unsigned* dw = (GAS unsigned*)(meta + M_DONE + j * 64);
    const bool fin = wv < (SHARE / 4 + 63) / 64;
    bool fast, more;
    uint32_t need, mg0, mg1;
    uint32_t guard = 0;
    uint32_t sgn = 0x8000u;
    pin(sgn);

#define GAT3(M) { uint32_t a0_, a1_, a2_; asm volatile( \
        "v_lshlrev_b16 %0, 2, %3\n\tv_lshlrev_b16 %1, 2, %4\n\tv_lshlrev_b16 %2, 2, %5\n\t" \
        "ds_read_b32 v[%6], %0\n\tds_read_b32 v[%6+1], %1\n\tds_read_b32 v[%6+2], %2" \
        : "=&v"(a0_), "=&v"(a1_), "=&v"(a2_) : "v"(w0), "v"(w1), "v"(w2), "n"(M) : "memory"); }
#define MIXL " op_sel_hi:[1,1,0]\n\t"
#define MIXH " op_sel:[0,1,0] op_sel_hi:[1,1,0]\n\t"
#define SDW " dst_sel:DWORD dst_unused:UNUSED_PAD src0_sel:DWORD src1_sel:WORD_1\n\t"
#define FOLD "v_bitop3_b32 %6, %6, v[%15-9], v[%15-8] bitop3:0xfe\n\tv_or_b32 %6, v[%15-7], %6\n\t"
#define SCAT(PRE, POST, M) { uint32_t c0_, c1_, c2_, a0_, a1_, a2_; asm volatile( \
        FOLD "v_and_b32 %0, 0x3ff, %7\n\tv_lshrrev_b32 %2, 22, %7\n\t" PRE \
        "v_bitop3_b32 %0, %11, %8, %0 bitop3:0xea\n\tv_bitop3_b32 %2, %11, %10, %2 bitop3:0xea\n\tv_bfe_u32 %1, %7, 11, 10\n\t" \
        "v_fma_mix_f32 %3, %0, v[%15], %12" MIXL "v_bitop3_b32 %1, %11, %9, %1 bitop3:0xea\n\tv_fma_mix_f32 %0, %0, v[%15], %13" MIXH \
        "v_fma_mix_f32 %4, %1, v[%15+1], %12" MIXL "v_lshl_add_u32 %0, %0, 16, %3\n\tv_lshlrev_b32_sdwa %3, 2, %8" SDW \
        "v_fma_mix_f32 %1, %1, v[%15+1], %13" MIXH "ds_add_rtn_u32 v[%15-9], %3, %0 offset:%14\n\t" \
        "v_fma_mix_f32 %5, %2, v[%15+2], %12" MIXL "v_lshl_add_u32 %1, %1, 16, %4\n\tv_lshlrev_b32_sdwa %4, 2, %9" SDW \
        "v_fma_mix_f32 %2, %2, v[%15+2], %13" MIXH "ds_add_rtn_u32 v[%15-8], %4, %1 offset:%14\n\t" \
        "v_lshl_add_u32 %2, %2, 16, %5\n\tv_lshlrev_b32_sdwa %5, 2, %10" SDW \
        "ds_add_rtn_u32 v[%15-7], %5, %2 offset:%14" POST \
        : "=&v"(c0_), "=&v"(c1_), "=&v"(c2_), "=&v"(a0_), "=&v"(a1_), "=&v"(a2_), "+v"(guard) \
        : "v"(xw), "v"(w0), "v"(w1), "v"(w2), "v"(sgn), "s"(mg0), "s"(mg1), "n"(A_ACC), "n"(M), "s"(tm) : "memory"); }
    asm volatile("v_mov_b32 v110, 0\n\tv_mov_b32 v111, 0\n\tv_mov_b32 v112, 0\n\tv_mov_b32 v113, 0\n\tv_mov_b32 v114, 0\n\tv_mov_b32 v115, 0\n\t"
                 "v_mov_b32 v116, 0\n\tv_mov_b32 v117, 0\n\tv_mov_b32 v118, 0" ::: "v106", "v127");
    auto unmix = [&](int i) __attribute__((always_inline)) {
        const auto s0 = __builtin_amdgcn_permlane16_swap(ring[i][0], ring[i][2], false, false);
        const auto s1 = __builtin_amdgcn_permlane16_swap(ring[i][1], ring[i][3], false, false);
        ring[i][0] = s0[0]; ring[i][2] = s0[1]; ring[i][1] = s1[0]; ring[i][3] = s1[1];
    };
    auto word = [&](int i, int k) __attribute__((always_inline)) -> uint32_t {
        return i < NFAST && (i >> 1) != HP ? ring[(i & ~1) | (k >> 1)][(i & 1) * 2 + (k & 1)] : ring[i][k];
    };
    auto gather = [&](int R, int i, int p) __attribute__((always_inline)) {
        if ((i >> 1) == HP) unmix(i);
        const uint32_t w0 = word(i, 0 ^ R), w1 = word(i, 1 ^ R), w2 = word(i, 2 ^ R);
        if (p == 0) GAT3(119) else if (p == 1) GAT3(122) else GAT3(125)
    };
    auto scatter = [&](int R, int i, int p, u64 tm) __attribute__((always_inline)) {
        const uint32_t xw = word(i, 3 ^ R), w0 = word(i, 0 ^ R), w1 = word(i, 1 ^ R), w2 = word(i, 2 ^ R);
        if (i < NFAST) { if (p == 0) SCAT("", "", 119) else if (p == 1) SCAT("", "", 122) else SCAT("", "", 125) }
        else if (p == 0) SCAT("s_mov_b64 exec, %16\n\t", "\n\ts_mov_b64 exec, -1", 119)
        else SCAT("s_mov_b64 exec, %16\n\t", "\n\ts_mov_b64 exec, -1", 125)
    };
#define WAIT_L(N) asm volatile("s_waitcnt lgkmcnt(" #N ")" ::: "memory")
    int ntn;
    u64 tm0, tm1;
    uint32_t ta[2], sp = sp0;
    auto list1 = [&](int ff) __attribute__((always_inline)) {
        int dst;
        uint32_t en;
        ntn = tail_rank(tc.x, sp, ff, 0u, dst, en);
        asm volatile("ds_permute_b32 v106, %0, %1" :: "v"(dst), "v"(en) : "memory");
        if (__builtin_expect(__builtin_amdgcn_ballot_w64((tc.x & 2u) != 0u) != 0ull, 0)) {
            const int n2 = tail_rank(tc.x, sp, ff, 4u, dst, en);
            if (ntn + n2 <= 32) {
                const int r = ((dst >> 4) | ((dst & 12) << 2)) + ntn;
                const int x = __builtin_amdgcn_ds_permute((en & 7u) ? ((r & 15) << 4) | ((r >> 4) << 2) : 252, (en & 7u) ? (int)en : 0);
                asm volatile("v_cmpx_ne_u32 vcc, 0, %0\n\tv_mov_b32 v106, %0\n\ts_mov_b64 exec, -1\n\ts_nop 4" :: "v"(x) : "vcc");
                ntn += n2;
            } else ntn = 64;
        }
    };
    auto step = [&](auto RR, auto KK, int ff) __attribute__((always_inline)) {
        constexpr int R = decltype(RR)::value;
        constexpr bool first = decltype(KK)::value == 1, last = decltype(KK)::value == 2;
        constexpr int mdp = first ? MDF : MDP;
        gather(R, 0, 0);
        const int ntl = ntn;
        asm volatile("v_mov_b32_dpp %0, v106 quad_perm:[0,0,0,0] row_mask:0xf bank_mask:0xf\n\tv_mov_b32_dpp %1, v106 quad_perm:[1,1,1,1] row_mask:0xf bank_mask:0xf" : "=v"(ta[0]), "=v"(ta[1]));
        auto side = [&](int pl) __attribute__((always_inline)) {
            if (first && pl == ((mdp >> 20) & 15)) { ring[4] = load_main(4, ff, !(NEARM & 1u)); ring[5] = load_main(5, ff, !(NEARM & 1u)); }
            if (first && pl == ((mdp >> 24) & 15)) { ring[6] = load_main(6, ff); ring[7] = load_main(7, ff); }
            if (pl == ((mdp >> 12) & 15)) ring[NFAST] = load_tail(ta[0], tm0);
            if (pl == ((mdp >> 16) & 15)) ring[NFAST + 1] = load_tail(ta[1], tm1);
            __builtin_amdgcn_sched_barrier(0);
            if (!last) load_md(ff + 1, pl, mdp);
            if (!first && !last && pl == 5 && ff == 6) {
                load_d(me);
                load_wl();
                if (tid == NT - 128) (void)__hip_atomic_fetch_add((GAS unsigned*)((GAS char*)dw + vconst<0>()), 0x1000000u << me, __ATOMIC_RELAXED, __HIP_MEMORY_SCOPE_AGENT);
            }
            if (last && pl == 3) load_m(me);
            if (last && pl == 4 && wv == 15) { GAS unsigned* d_ = dw; asm volatile("s_nop 4\n\tglobal_load_dword v108, %0, %1 sc1" :: "v"(vconst<0>()), "s"(d_) : "memory"); }
            if (last && pl < 7 && fin) factor(pl, pl == 0);
            __builtin_amdgcn_sched_barrier(0);
        };
        side(8);
        gather(R, 1, 1);
        side(9);
        const uint32_t tcur = tc.x, spc = sp;
#pragma unroll
        for (int i = 0; i < NFAST - 1; i++) {
            gather(R, i + 2, (i + 2) % 3);
            if (i == 0) side(10);
            if (i == 0) WAIT_L(6); else if (i == 1) WAIT_L(9); else WAIT_L(12);
            scatter(R, i, i % 3, 0ull);
            if (!last && (i & 1)) { ring[i - 1] = load_main(i - 1, ff + 1, first && !(NEARM & 4u)); ring[i] = load_main(i, ff + 1, first && !(NEARM & 4u)); }
            side(i);
            if (i == 3) SETP(2);
            if (i == 4) SETP(1);
            if (i == 5) SETP(0);
            if (i == 0 && !last) {
                tc.x = __builtin_amdgcn_alignbit(tc.y, tc.x, 8u); tc.y = __builtin_amdgcn_alignbit(tc.z, tc.y, 8u);
                tc.z = __builtin_amdgcn_alignbit(tc.w, tc.z, 8u); tc.w >>= 8;
                sp = ff == 7 ? sp0 + NWG * 9u * 128u : sp + ((tcur & 0x3cu) << 2);
                list1(ff + 1);
            }
            __builtin_amdgcn_sched_barrier(0);
        }
        if (last) asm volatile("" :: "v"(ring[NFAST + 1]));
        if (__builtin_expect(ntl > 16, 0)) {
            gather(R, NFAST + 1, 0);
            WAIT_L(0);
            scatter(R, NFAST + 1, 0, tm1);
        }
        if (__builtin_expect(ntl > 32, 0)) {
#pragma unroll 1
            for (int e = 2; e < 6; e++) {
                int dst;
                uint32_t en;
                if (tail_rank(tcur, spc, ff, (uint32_t)e & 4u, dst, en) <= (e & 3) * 16) continue;
                const int cmp = __builtin_amdgcn_ds_permute(dst, (int)en);
                ring[NFAST + 1] = load_tail((uint32_t)__builtin_amdgcn_ds_bpermute(((lane & ~3) | (e & 3)) << 2, cmp), tm1);
                gather(R, NFAST + 1, 0);
                WAIT_L(0);
                scatter(R, NFAST + 1, 0, tm1);
            }
        }
        SETP(3);
        WAIT_L(3);
        if (!last) {
            __builtin_amdgcn_s_barrier();
            store_md();
            SETP(0);
        }
        scatter(R, NFAST - 1, 1, 0ull);
        if (!last) { ring[6] = load_main(6, ff + 1); ring[7] = load_main(7, ff + 1); }
        scatter(R, NFAST, 2, tm0);
        if (!last) {
            WAIT_L(6);
            __builtin_amdgcn_s_barrier();
            SETP(3);
        }
    };
    auto steps = [&](auto RR) __attribute__((always_inline)) {
        tc = tcnt[(wv * 16 + (lane >> 2)) * 128 + j * 4 + h];
        u32x4 sv[2];
        float4 su[3];
        uint32_t e0;
        if (wv < 4) {
            sv[0] = (u32x4)(0u); sv[1] = sv[0];
            su[0] = make_float4(0.f, 0.f, 0.f, 0.f);
            if (lane < SLOTI / 256) { sv[0] = stat[tid * 2]; sv[1] = stat[tid * 2 + 1]; su[0] = ((const float4*)(ws + W_SUM))[tid]; }
            su[1] = ((const float4*)(ws + W_SUM))[256 + lane];
            su[2] = ((const float4*)(ws + W_SUM))[320 + lane];
            e0 = meta[M_E0 + wv];
        }
        const u32x2 extra = *(const u32x2*)(meta + M_LOG);
        load_md(0, -1, 0);
        {
            const u32x4 bi = {BIAS, BIAS, BIAS, BIAS};
#pragma unroll
            for (int k = 0; k < 6; k++) *(LDS u32x4*)(uintptr_t)(A_ACC + (k * NT + threadIdx.x) * 16) = bi;
            if (threadIdx.x < 128) *(LDS u32x4*)(uintptr_t)(A_ACC + (6 * NT + threadIdx.x) * 16) = bi;
            __builtin_amdgcn_sched_barrier(0);
        }
#pragma unroll
        for (int i = 0; i < 2; i++) ring[i] = load_main(i, 0);
        __builtin_amdgcn_sched_barrier(0);
        list1(0);
        if (wv < 4) {
            const uint32_t mxb = wave_max(max(max(sv[0].x & 0xffffu, sv[0].z & 0xffffu), max(sv[1].x & 0xffffu, sv[1].z & 0xffffu)));
            const uint32_t mnb = 0xffffu - (wave_max(max(max(sv[0].x, sv[0].z), max(sv[1].x, sv[1].z))) >> 16);
            const float s2 = wave_sum((__uint_as_float(sv[0].y) + __uint_as_float(sv[0].w)) + (__uint_as_float(sv[1].y) + __uint_as_float(sv[1].w)));
            const float s1 = wave_sum((su[0].x + su[0].y) + (su[0].z + su[0].w));
            const float c1 = wave_sum((su[1].x + su[1].y) + (su[1].z + su[1].w)) * (1.0f / (NWG * 512)), c2 = wave_sum((su[2].x + su[2].y) + (su[2].z + su[2].w)) * (1.0f / (NWG * 512));
            const float rng = fmaxf(fabsf(zf * s1 * c1) + ZSIG * sqrtf(zf * s2 * c2), 2.0f * bf2f(mxb)) * 6.103515625e-05f;
            const uint32_t eb = mxb ? (__float_as_uint(rng) + 0x7fffffu) >> 23 : 127u;
            const bool ok = !mxb || (mxb < 0x4700u && eb >= 107u && eb <= 140u && mnb >= ((eb + 8u) << 7) && c2 >= 0.015625f);
            if (lane == 0) {
                s_flag[8 + wv] = (eb & 0xffu) | (ok ? 0x100u : 0u) | (mxb ? 0x200u : 0u);
                s_flag[12 + wv] = __float_as_uint(__uint_as_float(((eb + e0) & 0xffu) << 23) * (1024.0f / 1023.0f));
                s_flag[wv] = 0u;
            }
        }
        store_md();
#pragma unroll
        for (int i = 2; i < 4; i++) ring[i] = load_main(i, 0);
        WAIT_L(0);
        __syncthreads();
        const uint32_t q0 = __builtin_amdgcn_readfirstlane(s_flag[8 + 2 * cp]), q1 = __builtin_amdgcn_readfirstlane(s_flag[9 + 2 * cp]);
        fast = (q0 & q1 & 0x100u) != 0u;
        need = (q0 >> 9) | ((q1 >> 9) << 1);
        more = (__builtin_amdgcn_readfirstlane(extra.x | extra.y) | (uint32_t)(T & 3)) != 0u;
        mg0 = (((q0 & 0xffu) + 9u) << 23) | 0x400000u;
        mg1 = ((((q1 & 0xffu) + 9u) << 23) | 0x400000u) + ((0u - (mg0 >> 16)) & 0xffffu);
        if (!fast) { __builtin_amdgcn_s_waitcnt(0x0f70); return; }
        RTS(9)
        step(RR, IC<1>(), 0);
        leak_ = ((unsigned)__builtin_amdgcn_s_memrealtime() - (unsigned)tf_) * 10u;
        asm volatile("" : "+s"(leak_));
#pragma unroll 1
        for (int ff = 1; ff < 15; ff++) step(RR, IC<0>(), ff);
        step(RR, IC<2>(), 15);
        if (fin) factor(7, false);
    };
    auto irregular = [&](auto f) __attribute__((always_inline)) {
        const float fr = bf2f(freq[0]);
        const char* lg = recs + (size_t)LBASE * 128u;
        const uint16_t* tags = (const uint16_t*)(lg + (size_t)lcap * 128u);
        const uint32_t nl = meta[M_LOG], ns = meta[M_LOG + 1];
#pragma unroll 1
        for (uint32_t q = tid; q < nl * 8u; q += NT) {
            const uint32_t b = tags[q >> 3];
            if ((b & 127u) == (uint32_t)(h * 32 + j)) {
                const uint32_t* un = (const uint32_t*)(lg + (size_t)q * 16u);
                const uint32_t xw = un[3 ^ h];
#pragma unroll 1
                for (int k = 0; k < 3; k++) {
                    const uint32_t ww = un[k ^ h];
                    const float c = (float)((xw >> (k * 11)) & 0x3ffu) * (1.0f / 1023.0f);
                    if (!(ww & WNULL)) f(ww >> 16, ((ww & 0x3fffu) << 6) | (b >> 5), (ww & 0x8000u) ? -c : c);
                }
            }
        }
#pragma unroll 1
        for (uint32_t q = tid; q < ns + (uint32_t)(T & 3); q += NT) {
            const uint32_t t = q < ns ? ((const uint32_t*)tags)[-1 - (int)q] : (uint32_t)(T & ~3) + (q - ns);
            const uint32_t i = (uint32_t)iin[t], o = (uint32_t)iout[t];
            if (((o >> 6) & 31u) == (uint32_t)j && (i & 3u) == (uint32_t)h) f(((o >> 11) << 6) | (o & 63u), i, cosf(fr * bf2f(ang[t])));
        }
        __builtin_amdgcn_s_waitcnt(0x0f70);
    };

    if (h == 0) steps(IC<0>());
    else if (h == 1) steps(IC<1>());
    else if (h == 2) steps(IC<2>());
    else steps(IC<3>());
    if (fast) {
        if (more) irregular([&](uint32_t ol, uint32_t i, float cf) __attribute__((always_inline)) {
            const uint32_t md = mdc[(cp * 64u + (i & 63u)) * SLOTI + ((i >> 14) << 8) + ((i >> 4) & 0xfcu) + ((i >> 12) & 3u)];
            const float c = cf * (1023.0f / 16777216.0f);
            const uint32_t lo = __float_as_uint(__builtin_fmaf(c, h2f(md & 0xffffu), __uint_as_float(mg0)));
            const uint32_t hi = __float_as_uint(__builtin_fmaf(c, h2f(md >> 16), __uint_as_float(mg1)));
            if (cf == cf) guard |= atomicAdd(&acc[ol], (hi << 16) + lo);
            else guard |= HSLOW;
        });
    }
    volatile LDS unsigned* seen = (volatile LDS unsigned*)(uintptr_t)(A_FLAG + 4);
    if (fast && wv == 15) asm volatile("s_waitcnt vmcnt(0)\n\tds_write_b32 %0, v108 offset:16\n\tv_and_b32 v109, 0xff00ff, v108\n\tds_write_b32 %0, v109" :: "v"(vconst<A_FLAG + 4>()) : "memory");
    WAIT_L(0);
    asm volatile("v_or3_b32 %0, %0, v110, v111\n\tv_or3_b32 %0, %0, v112, v113\n\tv_or3_b32 %0, %0, v114, v115\n\tv_or3_b32 %0, %0, v116, v117\n\tv_or_b32 %0, v118, %0" : "+v"(guard));
    __syncthreads();
    RTS(10)
    const __amdgpu_buffer_rsrc_t rsP = __builtin_amdgcn_make_buffer_rsrc((void*)(partial + (size_t)j * 16 * SLOTO), 0, 16 * SLOTO * 4, 0x00020000);
    const char* pk = recs + (uint32_t)j * (SC * 128u);
    const __amdgpu_buffer_rsrc_t rsK = __builtin_amdgcn_make_buffer_rsrc((void*)pk, 0, (int)(MAINL * 128u), 0x00020000);
#define PKA (SSM * 128u)
#define PKB (32u * SC * 128u)
    auto pk_at = [&](uint32_t i) __attribute__((always_inline)) -> uint32_t {
        const uint32_t c = i >> 9;
        return (c < 12u ? (1u + (c >> 2)) * SQ + (c & 3u) * SKM : SQ + 2u * SSM) * 128u + (i & 511u) * 16u;
    };
    u32x4 p[8];
    unsigned have = 0u;
    auto fetch = [&](unsigned v, int sh) __attribute__((always_inline)) {
        const uint32_t po = tid < SHARE / 4 ? pk_at((uint32_t)(sh * (SHARE / 4) + tid)) : 0x7fffff00u;
        const unsigned nw = v & ~have;
#pragma unroll
        for (int r = 0; r < 8; r++) {
            const uint32_t k = (uint32_t)(me ^ r);
            if ((nw >> k) & 1u) p[r] = __builtin_amdgcn_raw_buffer_load_b128(rsK, po, ((v >> (16u + k)) & 1u) ? MAINL * 128u : (k & 1u) * PKA + (k >> 1) * PKB, AUX_SC1);
        }
        have |= v;
    };
    bool room = false;
    if (fast) {
        room = (__builtin_amdgcn_readfirstlane(s_flag[5]) >> (24 + (me ^ 1))) & 1u;
        const __amdgpu_buffer_rsrc_t rsW = __builtin_amdgcn_make_buffer_rsrc((void*)(pk + (uint32_t)cp * PKA + (uint32_t)h * PKB), 0, room ? (int)(MAINL * 128u) : 0, 0x00020000);
        uint32_t big = 0u;
        u32x4 v[7];
#pragma unroll
        for (int k = 0; k < 6; k++) v[k] = acc_vec(k * NT + tid);
        v[6] = (u32x4)(BIAS);
        if (wv < 2) v[6] = acc_vec(6 * NT + tid);
#pragma unroll
        for (int k = 0; k < 6; k++) __builtin_amdgcn_raw_buffer_store_b128(v[k], rsW, pk_at((uint32_t)tid) + ((k >> 1) * SQ + (k & 1) * 2u * SKM) * 128u, 0, AUX_SC1);
        if (wv < 2) __builtin_amdgcn_raw_buffer_store_b128(v[6], rsW, (SQ + 2u * SSM) * 128u + (uint32_t)tid * 16u, 0, AUX_SC1);
        __builtin_amdgcn_sched_barrier(0);
#pragma unroll
        for (int k = 0; k < 28; k++) { guard |= v[k >> 2][k & 3]; big |= v[k >> 2][k & 3] + 0x42004200u; }
        if (guard & HSLOW) s_flag[0] = 1u;
        if (big & 0x7c00u) s_flag[2] = 1u;
        if (big & 0x7c000000u) s_flag[3] = 2u;
        __builtin_amdgcn_sched_barrier(0);
        p[0] = acc_vec(me * (SHARE / 4) + min(tid, SHARE / 4 - 1));
        have = 1u << me;
        if (fin) fetch(__builtin_amdgcn_readfirstlane(*seen), me);
    }
    asm volatile("s_waitcnt vmcnt(0)" ::: "memory");
    __syncthreads();
    RTS(11)
    const bool redo = !fast || __builtin_amdgcn_readfirstlane(s_flag[0]) != 0u || (need & ~__builtin_amdgcn_readfirstlane(s_flag[2] | s_flag[3])) != 0u;
    const bool slow = redo || !room;
    if (slow) {
        float* fs = (float*)acc;
        float w[16];
        load_w(wdown, w);
        const float fr = bf2f(freq[0]);
#pragma unroll 1
        for (int e = 0; e < 2; e++) {
            if (redo) {
                __syncthreads();
                for (int i = tid; i < SLOTO; i += NT) fs[i] = 0.f;
                __syncthreads();
                const __amdgpu_buffer_rsrc_t rsO = __builtin_amdgcn_make_buffer_rsrc((void*)iout, 0, T * 4, 0x00020000);
                const __amdgpu_buffer_rsrc_t rsI = __builtin_amdgcn_make_buffer_rsrc((void*)iin, 0, T * 4, 0x00020000);
#pragma unroll 1
                for (int t0 = tid * 4; t0 < T; t0 += NT * 16) {
                    u32x4 o4[4], i4[4];
#pragma unroll
                    for (int g = 0; g < 4; g++) {
                        o4[g] = __builtin_amdgcn_raw_buffer_load_b128(rsO, (uint32_t)(t0 + g * NT * 4) * 4u, 0, 0);
                        i4[g] = __builtin_amdgcn_raw_buffer_load_b128(rsI, (uint32_t)(t0 + g * NT * 4) * 4u, 0, 0);
                    }
                    uint32_t hit = 0u;
#pragma unroll
                    for (int k = 0; k < 16; k++) hit |= (bfi(3u, i4[k >> 2][k & 3], o4[k >> 2][k & 3]) & 0x7c3u) == (uint32_t)(j * 64 + h) ? 1u << k : 0u;
#pragma unroll 1
                    for (; hit; hit &= hit - 1u) {
                        const int k = __builtin_ctz(hit), t = t0 + (k >> 2) * NT * 4 + (k & 3);
                        if (t >= T) break;
                        const uint32_t o = (uint32_t)iout[t];
                        uint32_t rr[4];
                        md_of(m[iin[t]], w, rr);
                        atomicAdd(&fs[((o >> 11) << 6) | (o & 63u)], cosf(fr * bf2f(ang[t])) * bf2f(cp ? (e ? rr[3] : rr[2]) : (e ? rr[1] : rr[0])));
                    }
                }
                __syncthreads();
            }
            const float un = __uint_as_float(s_flag[12 + 2 * cp + e]);
            for (int i = tid; i < SLOTO / 4; i += NT) {
                u32x4 v = acc_vec(i);
                if (!redo) {
#pragma unroll
                    for (int k = 0; k < 4; k++) v[k] = __float_as_uint((float)((int)((v[k] >> (16 * e)) & 0xffffu) - 0x4000) * un);
                }
                __builtin_amdgcn_raw_buffer_store_b128(v, rsP, (uint32_t)((e * 8 + me) * SLOTO * 4 + i * 16), 0, AUX_SC1);
            }
        }
        load_wl();
        edges(me);
#pragma unroll
        for (int k = 0; k < 8; k++) p[k] = (u32x4)(0u);
        have = 0u;
        asm volatile("s_waitcnt vmcnt(0)" ::: "memory");
        __syncthreads();
    }
    if (tid == NT - 64) {
        const unsigned inc = (slow ? 0x10001u : 1u) << me;
        unsigned v = __hip_atomic_fetch_add(dw, inc, __ATOMIC_RELAXED, __HIP_MEMORY_SCOPE_AGENT) + inc;
        unsigned quit = 0u;
        if ((v & 0xffu) != 0xffu) {
            unsigned spins = 0;
            do {
                *seen = v & 0xff00ffu;
                __builtin_amdgcn_s_sleep(1);
                v = __hip_atomic_load(dw, __ATOMIC_RELAXED, __HIP_MEMORY_SCOPE_AGENT);
                if (++spins > SPINCAP && (v & 0xffu) != 0xffu) {
                    v = __hip_atomic_fetch_add(dw, 256u << me, __ATOMIC_RELAXED, __HIP_MEMORY_SCOPE_AGENT);
                    if ((v & 0xffu) != 0xffu) { v = 0xffu; quit = 0x80000000u; }
                }
            } while ((v & 0xffu) != 0xffu);
            v &= 0xff00ffu;
        }
        *seen = (v & 0xffffffu) | quit;
    }
    if (!fin) return;
    have = __builtin_amdgcn_readfirstlane(have);
    uint32_t un[4];
#pragma unroll
    for (int c = 0; c < 4; c++) un[c] = __builtin_amdgcn_readfirstlane(s_flag[12 + c]);
    unsigned sv;
    {
        const u32x4 rsQ = {(uint32_t)(uintptr_t)pk, (uint32_t)((uintptr_t)pk >> 32) & 0xffffu, MAINL * 128u, 0x00020000u};
        const uint32_t po = tid < SHARE / 4 ? pk_at((uint32_t)(me * (SHARE / 4) + tid)) : 0x7fffff00u;
        do {
            sv = __builtin_amdgcn_readfirstlane(*seen);
            uint32_t t0, t1, t2;
#define FET(R) "s_xor_b32 %8, %13, " #R "\n\ts_bitcmp1_b32 %14, %8\n\ts_cbranch_scc0 1f\n\ts_and_b32 %9, %8, 1\n\ts_mul_i32 %9, %9, %16\n\ts_lshr_b32 %10, %8, 1\n\t" \
               "s_mul_i32 %10, %10, %17\n\ts_add_u32 %9, %9, %10\n\ts_bitcmp1_b32 %15, %8\n\ts_cselect_b32 %9, %18, %9\n\tbuffer_load_dwordx4 %" #R ", %11, %12, %9 offen sc1\n1:\n\t"
            asm volatile("s_and_b32 %8, %14, 0xff\n\ts_cbranch_scc0 2f\n\ts_nop 2\n\t" FET(0) FET(1) FET(2) FET(3) FET(4) FET(5) FET(6) FET(7) "\n2:\n\ts_sleep 2\n\ts_waitcnt vmcnt(0)"
                         : "+v"(p[0]), "+v"(p[1]), "+v"(p[2]), "+v"(p[3]), "+v"(p[4]), "+v"(p[5]), "+v"(p[6]), "+v"(p[7]), "=&s"(t0), "=&s"(t1), "=&s"(t2)
                         : "v"(po), "s"(rsQ), "s"(me), "s"((int)sv < 0 ? 0u : sv & ~have), "s"(sv >> 16), "n"(PKA), "n"(PKB), "n"(MAINL * 128u) : "memory", "scc");
            have |= sv;
        } while ((sv & 0xffu) != 0xffu);
    }
    if ((int)sv < 0) return;
    RTS(12)
    const unsigned flt = (sv >> 16) & 0xffu;
    auto finish = [&](int sh) __attribute__((always_inline)) {
        {
            const int ol = sh * SHARE + tid * 4;
            const uint32_t po = tid < SHARE / 4 ? (uint32_t)ol * 4u : 0x7fffff00u;
            float g[4][4];
            u32x4 a[2] = {p[0] + p[2], p[1] + p[3]}, b[2] = {p[4] + p[6], p[5] + p[7]};
            if (cp) { const u32x4 t = a[0], u = b[0]; a[0] = a[1]; b[0] = b[1]; a[1] = t; b[1] = u; }
#pragma unroll
            for (int c = 0; c < 2; c++) {
                const int nb = 0x10000 - 0x4000 * __builtin_popcount(flt & (0x55u << c));
                const float u0 = __uint_as_float(nb ? un[2 * c] : 0u), u1 = __uint_as_float(nb ? un[2 * c + 1] : 0u);
#pragma unroll
                for (int q = 0; q < 4; q++) {
                    g[q][2 * c] = (float)((int)((a[c][q] & 0xffffu) + (b[c][q] & 0xffffu)) - nb) * u0;
                    g[q][2 * c + 1] = (float)((int)((a[c][q] >> 16) + (b[c][q] >> 16)) - nb) * u1;
                }
            }
            if (__builtin_expect(flt != 0u, 0)) {
#pragma unroll
                for (int c = 0; c < 2; c++) {
#pragma unroll 1
                    for (int k = c; k < 8; k += 2) {
                        if (!((flt >> k) & 1u)) continue;
                        const u32x4 f0 = __builtin_amdgcn_raw_buffer_load_b128(rsP, po, k * SLOTO * 4, AUX_SC1);
                        const u32x4 f1 = __builtin_amdgcn_raw_buffer_load_b128(rsP, po, (8 + k) * SLOTO * 4, AUX_SC1);
#pragma unroll
                        for (int q = 0; q < 4; q++) { g[q][2 * c] += __uint_as_float(f0[q]); g[q][2 * c + 1] += __uint_as_float(f1[q]); }
                    }
                }
            }
            float wu[16];
#pragma unroll
            for (int k = 0; k < 8; k++) {
                const uint32_t x = __builtin_amdgcn_readlane(wl, 40 + k);
                wu[2 * k] = bf2f(x & 0xffffu); wu[2 * k + 1] = bf2f(x >> 16);
            }
            u32x4 ov[2];
#pragma unroll
            for (int q = 0; q < 4; q++) {
#pragma unroll
                for (int k = 0; k < 4; k++) g[q][k] *= fa[q][k >> 1][k & 1];
                const uint32_t m0 = em[q >> 1][(q & 1) * 2], m1 = em[q >> 1][(q & 1) * 2 + 1];
                const float mm[4] = {bf2f(m0 & 0xffff), bf2f(m0 >> 16), bf2f(m1 & 0xffff), bf2f(m1 >> 16)};
                float o[4];
#pragma unroll
                for (int k = 0; k < 4; k++) o[k] = mm[k] + (g[q][0] * wu[k] + g[q][1] * wu[4 + k] + g[q][2] * wu[8 + k] + g[q][3] * wu[12 + k]) * fa[q][2 + (k >> 1)][k & 1];
                ov[q >> 1][(q & 1) * 2] = pk_bf16(o[0], o[1]);
                ov[q >> 1][(q & 1) * 2 + 1] = pk_bf16(o[2], o[3]);
            }
            const __amdgpu_buffer_rsrc_t rsU = __builtin_amdgcn_make_buffer_rsrc((void*)out, 0, E * 8, 0x00020000);
            const uint32_t uo = edge0(sh) * 8u;
            __builtin_amdgcn_raw_buffer_store_b128(ov[0], rsU, uo, 0, AUX_NT);
            __builtin_amdgcn_raw_buffer_store_b128(ov[1], rsU, uo + 256u, 0, AUX_NT);
        }
    };
    finish(me);
#pragma unroll 1
    for (unsigned todo = (sv >> 8) & 0xffu; todo; todo &= todo - 1u) {
        const int sh = __builtin_ctz(todo);
        edges(sh);
        have = 0u;
        fetch(sv, sh);
        finish(sh);
    }
    if (blockIdx.x == 5) { const unsigned long long t0_ = __builtin_amdgcn_s_memrealtime(); while (__builtin_amdgcn_s_memrealtime() - t0_ < leak_) __builtin_amdgcn_s_sleep(2); }
}

__global__ __launch_bounds__(256) void gen_scatter_kernel(const int* __restrict__ iin, const int* __restrict__ iout,
                                                          const uint16_t* __restrict__ ang, const uint16_t* __restrict__ freq,
                                                          const u32x2* __restrict__ m, const uint16_t* __restrict__ wdown,
                                                          float* __restrict__ agg, int T) {
    float w[16];
    load_w(wdown, w);
    const float fr = bf2f(freq[0]);
    for (long t = (long)blockIdx.x * 256 + threadIdx.x; t < T; t += (long)gridDim.x * 256) {
        const float c = cosf(fr * bf2f(ang[t]));
        uint32_t r[4];
        md_of(m[iin[t]], w, r);
        float* q = agg + (size_t)iout[t] * 4;
#pragma unroll
        for (int k = 0; k < 4; k++) atomicAdd(q + k, c * bf2f(r[k]));
    }
}
__global__ __launch_bounds__(256) void gen_final_kernel(const u32x2* __restrict__ m, const uint16_t* __restrict__ dist,
                                                        const uint16_t* __restrict__ centers, const uint16_t* __restrict__ wrbf,
                                                        const uint16_t* __restrict__ wsbf, const uint16_t* __restrict__ wup,
                                                        const float* __restrict__ agg, u32x2* __restrict__ out, int E) {
    const int e = blockIdx.x * 256 + threadIdx.x;
    if (e >= E) return;
    const float d = bf2f(dist[e]);
    float rs[4] = {0, 0, 0, 0}, rg[4] = {0, 0, 0, 0};
#pragma unroll 1
    for (int r = 0; r < 8; r++) {
        const float x = d - bf2f(centers[r]);
        const float rb = __expf(-10.0f * x * x);
#pragma unroll
        for (int k = 0; k < 4; k++) { rs[k] += rb * bf2f(wsbf[r * 4 + k]); rg[k] += rb * bf2f(wrbf[r * 4 + k]); }
    }
    float wu[16];
    load_w(wup, wu);
    const u32x2 mv = m[e];
    const float mm[4] = {bf2f(mv.x & 0xffff), bf2f(mv.x >> 16), bf2f(mv.y & 0xffff), bf2f(mv.y >> 16)};
    float g[4], o[4];
#pragma unroll
    for (int k = 0; k < 4; k++) g[k] = agg[(size_t)e * 4 + k] * rs[k];
#pragma unroll
    for (int k = 0; k < 4; k++) o[k] = mm[k] + (g[0] * wu[k] + g[1] * wu[4 + k] + g[2] * wu[8 + k] + g[3] * wu[12 + k]) * rg[k];
    const u32x2 ov = {f2bf(o[0]) | (f2bf(o[1]) << 16), f2bf(o[2]) | (f2bf(o[3]) << 16)};
    out[e] = ov;
}

extern "C" void kernel_launch(void* const* d_in, const int* in_sizes, int n_in,
                              void* d_out, int out_size, void* d_ws, size_t ws_size,
                              hipStream_t stream) {
    const u32x2* m = (const u32x2*)d_in[0];
    const int* iin = (const int*)d_in[1];
    const int* iout = (const int*)d_in[2];
    const uint16_t* dist = (const uint16_t*)d_in[3];
    const uint16_t* ang = (const uint16_t*)d_in[4];
    const uint16_t* centers = (const uint16_t*)d_in[5];
    const uint16_t* freq = (const uint16_t*)d_in[6];
    const uint16_t* wrbf = (const uint16_t*)d_in[7];
    const uint16_t* wsbf = (const uint16_t*)d_in[8];
    const uint16_t* wdown = (const uint16_t*)d_in[9];
    const uint16_t* wup = (const uint16_t*)d_in[10];
    const int E = in_sizes[3];
    const int T = in_sizes[1];
    char* ws = (char*)d_ws;
    const int G = T >> 2;
    const int ntile = (G + GR - 1) / GR;
    const int nstat = ntile / NWG - NDYN > 4 ? ntile / NWG - NDYN : 4;
    const int lcap = G / 6 + 2;
    char* recs = ws + W_RECS;
    size_t off = W_RECS;
    const size_t rbytes = (size_t)LBASE * 128 + (size_t)lcap * 130;
    off += rbytes;
    if (E > EMAX || (E & 3) || E < 1 || rbytes > 0x70000000u || off > ws_size) {
        (void)hipMemsetAsync(ws, 0, (size_t)E * 16, stream);
        gen_scatter_kernel<<<4096, 256, 0, stream>>>(iin, iout, ang, freq, m, wdown, (float*)ws, T);
        gen_final_kernel<<<(E + 255) / 256, 256, 0, stream>>>(m, dist, centers, wrbf, wsbf, wup, (const float*)ws, (u32x2*)d_out, E);
        return;
    }
    int ks = T / (NWG * NB * 24);
    if (ks > KMAX) ks = KMAX;
    const float zf = (float)T / (4.0f * (float)E * (float)E);
    (void)hipFuncSetAttribute((const void*)part_kernel, hipFuncAttributeMaxDynamicSharedMemorySize, P_LDS);
    (void)hipFuncSetAttribute((const void*)acc_kernel, hipFuncAttributeMaxDynamicSharedMemorySize, A_LDS);
    part_kernel<<<NWG, NT, P_LDS, stream>>>(iin, iout, ang, freq, m, wdown, recs, T, E, ntile, nstat, ks, lcap);
    acc_kernel<<<NWG, NT, A_LDS, stream>>>(ws, T, E, ks, lcap, zf, 0, centers, wrbf, wsbf, wup, m, dist, (u32x2*)d_out, iin, iout, ang, wdown, freq);
}
```

```cpp
#define PPF 0
#include <hip/hip_runtime.h>
#include <hip/hip_bf16.h>
#include <stdint.h>
#pragma clang diagnostic ignored "-Winline-asm"

typedef unsigned int u32x4 __attribute__((ext_vector_type(4)));
typedef unsigned int u32x2 __attribute__((ext_vector_type(2)));
typedef unsigned short u16x2 __attribute__((ext_vector_type(2)));
typedef __bf16 bf16x2 __attribute__((ext_vector_type(2)));
typedef float f32x2 __attribute__((ext_vector_type(2)));
typedef unsigned long long u64;

static __device__ __forceinline__ float bf2f(uint32_t bits16) { return __uint_as_float(bits16 << 16); }
static __device__ __forceinline__ uint32_t f2bf(float f) {
    uint32_t u = __float_as_uint(f);
    if ((u & 0x7fffffffu) > 0x7f800000u) return (u >> 16) | 0x40;
    return (u + 0x7fffu + ((u >> 16) & 1u)) >> 16;
}
static __device__ __forceinline__ float h2f(uint32_t bits16) { return (float)__builtin_bit_cast(_Float16, (unsigned short)bits16); }

#define NB 2048
#define WNULL 0x80000000u
#define CSTEP 0x2aabu
#define CINIT 205u
#define CHV (12u * CSTEP)
#define SLOTO 25088
#define SLOTI 12544
#define EMAX (SLOTO * 32)
#define NT 1024
#define NWG 256
#define KMAX 4
#define NTAIL 4
#define SC 64u
#define SKM (128u * SC + 2u)
#define SSM (4u * SKM + 1u)
#define SSH (128u * SC + 1u)
#define SKH (16u * SSH + 2u)
#define SQ (16u * SSM)
#define MAINL (4u * SQ)
#define BPL(b) ((((b) >> 3) & 3u) * SQ + ((b) & 7u) + (((b) >> 5) << 3))
#define TST 257u
#define TBASE MAINL
#define TPL (NB * TST)
#define SBASE (TBASE + 2u * TPL)
#define LBASE (SBASE + 128u * 2u * NWG * 9u)
#define NDYN 3
#define NEARM 0x55555554u
#define GR 2048
#define M_DONE 0
#define M_TILE (32 * 64)
#define M_LOG (M_TILE + 64)
#define M_MD (M_LOG + 64)
#define M_BEAT (M_MD + 64)
#define M_E0 (M_BEAT + 64)
#define M_WORDS (M_E0 + 64)
#define W_TCNT (M_WORDS * 4)
#define W_STAT (W_TCNT + NWG * NB)
#define W_SUM (W_STAT + 4 * NWG * 8)
#define W_MDC (W_SUM + 6 * NWG * 4)
#define W_PART (W_MDC + 128 * SLOTI * 4)
#define W_RECS (W_PART + 32 * 16 * SLOTO * 4)
#define AUX_NT 2
#define AUX_SC1 16

static __device__ __forceinline__ uint32_t row_max(uint32_t x) {
    x = max(x, (uint32_t)__builtin_amdgcn_update_dpp(0, x, 0x111, 0xf, 0xf, false));
    x = max(x, (uint32_t)__builtin_amdgcn_update_dpp(0, x, 0x112, 0xf, 0xf, false));
    x = max(x, (uint32_t)__builtin_amdgcn_update_dpp(0, x, 0x114, 0xf, 0xf, false));
    return max(x, (uint32_t)__builtin_amdgcn_update_dpp(0, x, 0x118, 0xf, 0xf, false));
}
static __device__ __forceinline__ uint32_t wave_max(uint32_t x) {
    x = row_max(x);
    x = max(x, (uint32_t)__builtin_amdgcn_update_dpp(0, x, 0x142, 0xa, 0xf, false));
    x = max(x, (uint32_t)__builtin_amdgcn_update_dpp(0, x, 0x143, 0xc, 0xf, false));
    return __builtin_amdgcn_readlane(x, 63);
}
static __device__ __forceinline__ float row_sum(float x) {
    x += __int_as_float(__builtin_amdgcn_update_dpp(0, __float_as_int(x), 0x111, 0xf, 0xf, false));
    x += __int_as_float(__builtin_amdgcn_update_dpp(0, __float_as_int(x), 0x112, 0xf, 0xf, false));
    x += __int_as_float(__builtin_amdgcn_update_dpp(0, __float_as_int(x), 0x114, 0xf, 0xf, false));
    return x + __int_as_float(__builtin_amdgcn_update_dpp(0, __float_as_int(x), 0x118, 0xf, 0xf, false));
}
static __device__ __forceinline__ float wave_sum(float x) {
    x = row_sum(x);
    x += __int_as_float(__builtin_amdgcn_update_dpp(0, __float_as_int(x), 0x142, 0xa, 0xf, false));
    x += __int_as_float(__builtin_amdgcn_update_dpp(0, __float_as_int(x), 0x143, 0xc, 0xf, false));
    return __int_as_float(__builtin_amdgcn_readlane(__float_as_int(x), 63));
}
static __device__ __forceinline__ uint32_t bfi(uint32_t mask, uint32_t a, uint32_t b) {
    return __builtin_amdgcn_bitop3_b32(mask, a, b, 0xca);
}
template <uint32_t C> static __device__ __forceinline__ uint32_t vconst() {
    uint32_t r;
    asm volatile("v_mov_b32 %0, %1" : "=v"(r) : "n"(C));
    return r;
}
static __device__ __forceinline__ uint32_t pk_bf16(float a, float b) {
    const f32x2 v = {a, b};
    return __builtin_bit_cast(uint32_t, __builtin_convertvector(v, bf16x2));
}
static __device__ __forceinline__ void pin(uint32_t& x) { asm volatile("" : "+v"(x)); }

#define RTS(k)

static __device__ __forceinline__ void md_of(u32x2 v, const float* w, uint32_t* r) {
    float a0 = bf2f(v.x & 0xffff), a1 = bf2f(v.x >> 16), a2 = bf2f(v.y & 0xffff), a3 = bf2f(v.y >> 16);
#pragma unroll
    for (int j = 0; j < 4; j++) r[j] = f2bf(a0 * w[j] + a1 * w[4 + j] + a2 * w[8 + j] + a3 * w[12 + j]);
}
static __device__ __forceinline__ void load_w(const uint16_t* __restrict__ wdown, float* w) {
    u32x4 wv0 = *(const u32x4*)wdown, wv1 = *(const u32x4*)(wdown + 8);
#pragma unroll
    for (int i = 0; i < 4; i++) {
        w[2 * i] = bf2f(wv0[i] & 0xffffu); w[2 * i + 1] = bf2f(wv0[i] >> 16);
        w[8 + 2 * i] = bf2f(wv1[i] & 0xffffu); w[8 + 2 * i + 1] = bf2f(wv1[i] >> 16);
    }
}
#define LDS __attribute__((address_space(3)))
#define P_CNT 0
#define P_FLAG ((NB + 64) * 4)
#define P_LST (P_FLAG + 256)
#define LCAP 32
#define P_TAB (P_LST + 4 * LCAP * 16)
#define P_BUF (P_TAB + NB * 4)
#define P_LDS (P_BUF + NB * 64)
#define PD 2
#define LPOS 0x448763
#define SETP(n) __builtin_amdgcn_s_setprio(n)
#define MDL 1
#define MDM 3
#define CPF 1
#define PPF2 1
#define APF 0
#define RMAXREV 40.0f
#define HB 88
#define HREG(S, D) (HB + ((S) & 3) * 10 + (((S) >> 2) + (D)) * 2)

template <int N> struct IC { static constexpr int value = N; };
struct Raw { u32x4 o[2], i[2]; u32x2 a[2]; };
static __device__ __forceinline__ uint32_t lds_ld(uint32_t a) { return *(const LDS uint32_t*)(uintptr_t)a; }
static __device__ __forceinline__ void lds_st(uint32_t a, uint32_t v) { *(LDS uint32_t*)(uintptr_t)a = v; }
static __device__ __forceinline__ void lds_or(uint32_t a, uint32_t v) {
    __hip_atomic_fetch_or((LDS uint32_t*)(uintptr_t)a, v, __ATOMIC_RELAXED, __HIP_MEMORY_SCOPE_WORKGROUP);
}
static __device__ __forceinline__ uint32_t lds_add(uint32_t a, uint32_t v) {
    return __hip_atomic_fetch_add((LDS uint32_t*)(uintptr_t)a, v, __ATOMIC_RELAXED, __HIP_MEMORY_SCOPE_WORKGROUP);
}
static __device__ __forceinline__ uint32_t slot_w(uint32_t ca, uint32_t cv) {
    return (ca << 4) | __builtin_amdgcn_bitop3_b32(cv >> 11, ca, 0x3cu, 0x28);
}
static __device__ __forceinline__ uint32_t slot_x2(uint32_t aw, uint32_t ca) {
    return __builtin_amdgcn_bitop3_b32(aw, ca, 0xcu, 0x72);
}
static __device__ __forceinline__ uint32_t slot_x(uint32_t aw) { return slot_x2(aw, aw >> 4); }
template <int R> static __device__ __forceinline__ u32x2 hget() {
    u32x2 d;
    asm volatile("v_mov_b64 %0, v[%1:%2]" : "=v"(d) : "n"(R), "n"(R + 1));
    return d;
}
static __device__ __forceinline__ u32x2 hget_at(int r) {
    u32x2 d;
    asm volatile("s_set_gpr_idx_on %2, gpr_idx(SRC0)\n\ts_nop 1\n\tv_mov_b32 %0, v88\n\tv_mov_b32 %1, v89\n\ts_set_gpr_idx_off"
                 : "=&v"(d.x), "=&v"(d.y) : "s"(r));
    return d;
}
static __device__ __forceinline__ void hset_at(int r, u32x2 d) {
    asm volatile("s_set_gpr_idx_on %2, gpr_idx(DST)\n\ts_nop 1\n\tv_mov_b32 v88, %0\n\tv_mov_b32 v89, %1\n\ts_set_gpr_idx_off"
                 :: "v"(d.x), "v"(d.y), "s"(r));
}

template <int S, int D> static __device__ __forceinline__ uint32_t take(uint32_t V, uint32_t u16, uint32_t la, u32x2 nul) {
    const uint32_t a = __builtin_amdgcn_bitop3_b32(V, u16, 15u, 0x54);
    asm volatile("v_mov_b64 v[%4:%4+1], v[%3:%3+1]\n\tv_cmpx_ne_u32 vcc, -16, %0\n\tds_wrxchg_rtn_b64 v[%4:%4+1], %1, %2 offset:%5\n\ts_mov_b64 exec, -1"
                 :: "v"(a), "v"(la), "v"(nul), "n"(HREG(S, D)), "n"(HREG(S, 1 - D)), "n"(S * 256) : "memory", "vcc");
    return a;
}
#define PUT(P) asm volatile("s_bitcmp1_b32 %3, %4\n\ts_cbranch_scc0 1f\n\ts_waitcnt lgkmcnt(%5)\n\tbuffer_store_dwordx4 v[%2:%2+3], %0, %1, 0 offen sc1\n" \
                 "1:\n\ts_bitcmp1_b32 %3, %4+1\n\ts_cbranch_scc0 2f\n\ts_waitcnt lgkmcnt(%5)\n\tbuffer_store_dwordx4 v[%2:%2+3], %0, %1, 0 offen " P "\n\ts_nop 1\n2:" \
                 :: "v"(ad), "s"(rsA), "n"(HREG(S, 0)), "s"(pm), "n"(2 * S), "n"(W) : "memory", "scc")
template <int S, int W, bool WT> static __device__ __forceinline__ void put(u32x4 rsA, uint32_t ad, uint32_t pm) {
    if (WT) PUT("sc1 nt"); else PUT("nt");
}

#define CODE_AHEAD(SYM, BYTES, LANE) ({ u64 ca_; \
    asm volatile("s_getpc_b64 vcc\n\ts_add_u32 vcc_lo, vcc_lo, " #SYM "@rel32@lo+4\n\ts_addc_u32 vcc_hi, vcc_hi, " #SYM "@rel32@hi+12\n\ts_mov_b64 %0, vcc" : "=s"(ca_) :: "scc", "vcc"); \
    __builtin_amdgcn_raw_buffer_load_b32(__builtin_amdgcn_make_buffer_rsrc((void*)ca_, 0, BYTES, 0x00020000), ((blockIdx.x >> 3) * 64u + (LANE)) * 64u, 0, 0); })
#define PART_BYTES 56320
#define ACC_BYTES 78848
extern "C" __global__ __launch_bounds__(NT, 4) __attribute__((amdgpu_num_vgpr(HB / 2))) void part_kernel(const int* __restrict__ iin, const int* __restrict__ iout,
                                                  const uint16_t* __restrict__ ang, const uint16_t* __restrict__ freq,
                                                  const u32x2* __restrict__ m, const uint16_t* __restrict__ wdown,
                                                  char* __restrict__ recs,
                                                  int T, int E, int ntile, int nstat, int ks, int lcap) {
    unsigned long long tf_; asm volatile("s_memrealtime %0" : "=s"(tf_));
    asm volatile("" :: "s"(iin), "s"(iout), "s"(ang), "s"(freq), "s"(m), "s"(wdown), "s"(recs), "s"(T), "s"(E), "s"(ntile), "s"(nstat), "s"(ks), "s"(lcap));
    const int tid0 = threadIdx.x;
    const int wv = __builtin_amdgcn_readfirstlane(tid0 >> 6);
    const int G = T >> 2;
    const int pos = (int)((blockIdx.x & 7) << 5) | (int)(blockIdx.x >> 3);
    float frr;
    unsigned* meta = (unsigned*)(recs - W_RECS);
    const uint32_t gbase = (LBASE + (uint32_t)lcap) * 128u;
    const uint32_t rbytes = gbase + (uint32_t)lcap * 2u;
    const __amdgpu_buffer_rsrc_t rsR = __builtin_amdgcn_make_buffer_rsrc(recs, 0, (int)rbytes, 0x00020000);
    const u32x4 rsA = {(uint32_t)(uintptr_t)recs, (uint32_t)((uintptr_t)recs >> 32) & 0xffffu, rbytes, 0x00020000u};
    const uint32_t g2 = 2u * (uint32_t)ks;
    const uint32_t far = 1u >> ((blockIdx.x >> 3) & 3u);
    const uint32_t SK = SKM + far * (SKH - SKM), SS = SSM + far * (SSH - SSM), UINC = 0x80000001u + SK * 64u;
    const uint32_t spec0 = gbase - 4u;
    auto fixed_tile = [&](int r) __attribute__((always_inline)) -> int { return r * NWG + ((pos + r * 37) & (NWG - 1)); };
    int tl0 = fixed_tile(0), tl1 = fixed_tile(1), tl2;
    uint32_t tk = 0u;
    const __amdgpu_buffer_rsrc_t rsO = __builtin_amdgcn_make_buffer_rsrc((void*)iout, 0, G * 16, 0x00020000);
    const __amdgpu_buffer_rsrc_t rsI = __builtin_amdgcn_make_buffer_rsrc((void*)iin, 0, G * 16, 0x00020000);
    const __amdgpu_buffer_rsrc_t rsG = __builtin_amdgcn_make_buffer_rsrc((void*)ang, 0, G * 8, 0x00020000);
    uint32_t t8 = (uint32_t)tid0 * 8u;
    auto load_one = [&](Raw& r, int tl, int idx) __attribute__((always_inline)) {
        const int j = idx < 4 ? idx >> 1 : idx & 1;
        const uint32_t g0 = (uint32_t)(tl * GR + j * NT);
        __builtin_amdgcn_sched_barrier(0);
        if (idx >= 4) r.a[j] = __builtin_amdgcn_raw_buffer_load_b64(rsG, t8, g0 * 8u, AUX_NT);
        else if (idx & 1) r.i[j] = __builtin_amdgcn_raw_buffer_load_b128(rsI, t8 + t8, g0 * 16u, AUX_NT);
        else r.o[j] = __builtin_amdgcn_raw_buffer_load_b128(rsO, t8 + t8, g0 * 16u, AUX_NT);
        __builtin_amdgcn_sched_barrier(0);
    };
    auto peek = [&](uint32_t at) __attribute__((always_inline)) -> uint32_t {
        return __hip_atomic_load((unsigned*)((char*)meta + at), __ATOMIC_RELAXED, __HIP_MEMORY_SCOPE_AGENT);
    };
    auto ready = [&]() __attribute__((always_inline)) {
        if (blockIdx.x != 0) {
            const uint32_t x = lds_ld(vconst<P_FLAG + 8>());
            while (peek(vconst<M_BEAT * 4>()) == x) __builtin_amdgcn_s_sleep(8);
        }
    };
    auto beat = [&]() __attribute__((always_inline)) {
        (void)__hip_atomic_fetch_add((unsigned*)((char*)meta + vconst<M_BEAT * 4>()), vconst<1>(), __ATOMIC_RELAXED, __HIP_MEMORY_SCOPE_AGENT);
    };
    auto draw = [&](uint32_t at) __attribute__((always_inline)) -> uint32_t {
        return __hip_atomic_fetch_add((unsigned*)((char*)meta + at), vconst<1>(), __ATOMIC_RELAXED, __HIP_MEMORY_SCOPE_AGENT);
    };
    uint32_t la;
    auto tab_at = [&]() __attribute__((always_inline)) -> uint32_t {
        return P_TAB + bfi(0xffu, bfi(0x3cu, t8 >> 2, t8 << 4), t8);
    };
    u32x2 nul;
    uint32_t U[2];
    {
        const uint32_t t = (uint32_t)tid0;
        la = P_BUF + ((t >> 5) << 12) + ((t & 0x18u) << 3) + ((t & 3u) << 4) + ((t & 4u) << 1);
        const uint32_t q = ((t >> 3) ^ (t >> 1 & 2u)) & 3u;
        nul.x = (0x7u >> q) << 31; nul.y = (0xbu >> q) << 31;
        U[0] = (((t >> 1) & 15u) * SS + ((t & 1u) * 64u + (t >> 5)) * SC + BPL(blockIdx.x)) * 128u + (0x80000000u - SK * 64u) + 8u - g2;
        U[1] = U[0] + 32u * SC * 128u;
    }
    auto log_line = [&](bool lg, uint32_t b) __attribute__((always_inline)) -> uint32_t {
        uint32_t lp = 0u;
        if (lg && (t8 & 0x38u) == 0u) {
            ready();
            lp = draw(vconst<M_LOG * 4>());
            __builtin_amdgcn_raw_buffer_store_b16((uint16_t)b, rsR, gbase + lp * 2u, 0, 0);
        }
        return LBASE + (uint32_t)__builtin_amdgcn_ds_bpermute((int)((t8 >> 1) & 0xe0u), (int)lp);
    };
    auto general = [&](int D, bool all, u64 sm) __attribute__((always_inline)) {
        uint32_t todo = __builtin_amdgcn_readfirstlane((uint32_t)sm | (uint32_t)(sm >> 32));
#pragma unroll 1
        while (todo) {
            const uint32_t S = (uint32_t)__builtin_ctz(todo) >> 1;
            todo &= ~(3u << (2u * S));
            pin(t8); pin(la);
            const uint32_t V = lds_ld(P_TAB + (t8 & ~63u) + S * 4u), g = V & 15u;
            const uint32_t col = S * 128u + ((t8 >> 1) & 0x60u) + (t8 >> 8);
            const bool act = all ? V != 0xffffffffu : (V & 0x80000009u) == 9u;
            const int hr = HREG((int)S, D) - HB;
            uint32_t ln = log_line(act && g > 8u + NTAIL, col);
            if (act) {
                const u32x2 N = *(const LDS u32x2*)(uintptr_t)(la + S * 256u);
                *(LDS u32x2*)(uintptr_t)(la + S * 256u) = nul;
                lds_st(P_TAB + (t8 & ~63u) + S * 4u, vconst<0xffffffffu>());
                if (!(g & 1u)) hset_at(hr, N);
                else {
                    uint32_t a, st;
                    if (g < 8u) { a = (V & ~15u) + ((t8 & 0x38u) << 1); st = 8u; }
                    else {
                        if (g <= 8u + NTAIL) ln = TBASE + ((g - 8u) >> 1) * TPL + col * TST + blockIdx.x;
                        a = ln * 128u + (la & 0x38u); st = 64u;
                    }
                    __builtin_amdgcn_raw_buffer_store_b64(hget_at(hr), rsR, a, 0, AUX_SC1);
                    __builtin_amdgcn_raw_buffer_store_b64(N, rsR, a + st, 0, AUX_SC1);
                }
            }
        }
    };
    unsigned epoch = 0;
    u64 pb2 = 0ull;
    u64 fm = 0ull, sm = 0ull;
    auto look = [&](u32x2 c) __attribute__((always_inline)) {
        uint32_t V[2];
        const bool late = __builtin_amdgcn_ballot_w64(((U[0] | U[1]) & 8u) != 0u) != 0ull;
        epoch++;
        asm volatile("v_mov_b32 %0, -1\n\tv_mov_b32 %1, -1\n\t"
                     "v_cmpx_le_u32 %6, %7, %2\n\tv_mov_b32 %0, %4\n\tv_add_u32 %4, %9, %4\n\tv_subrev_u32 %2, %8, %2\n\ts_mov_b64 exec, -1\n\t"
                     "v_cmpx_le_u32 vcc, %8, %3\n\tv_mov_b32 %1, %5\n\tv_add_u32 %5, %9, %5\n\tv_subrev_u32 %3, %8, %3\n\t"
                     "s_or_b64 %6, %6, exec\n\ts_mov_b64 exec, -1"
                     : "=&v"(V[0]), "=&v"(V[1]), "+v"(c.x), "+v"(c.y), "+v"(U[0]), "+v"(U[1]), "=&s"(fm) : "s"(CHV), "n"(CHV), "s"(UINC) : "vcc", "scc");
        *(LDS u32x2*)(uintptr_t)(P_CNT + t8) = c;
        pin(t8);
        const uint32_t tw = tab_at();
        lds_st(tw, V[0]);
        lds_st(tw + 64u, V[1]);
        if (wv == 1) lds_st(P_CNT + NB * 4 - 256 + (t8 >> 1), vconst<0x40000000u>());
        pb2 = __builtin_amdgcn_ballot_w64((int)(V[0] & V[1]) >= 0);
        sm = 0ull;
        if (__builtin_expect(late, 0)) {
            sm = __builtin_amdgcn_ballot_w64((V[0] & 0x80000009u) == 9u || (V[1] & 0x80000009u) == 9u);
#pragma unroll
            for (int e = 0; e < 2; e++)
                if ((V[e] & 0x8000000fu) == 9u + NTAIL) U[e] -= 2u * UINC;
        }
    };
    u32x4 Va, Vb;

    Raw raw[PD];
    {
        const int tid = tid0;
        asm volatile("" ::: "v88", "v127");
        typedef const __attribute__((address_space(4))) u32x4* cptr;
        u32x4 wd0 = ((cptr)(uintptr_t)wdown)[0], wd1 = ((cptr)(uintptr_t)wdown)[1];
        __builtin_amdgcn_sched_barrier(0);
        if (PPF && wv == 5) tk = CODE_AHEAD(part_kernel, PART_BYTES, (uint32_t)tid & 63u);
        if (tid == 128) tk = peek(vconst<M_BEAT * 4>());
        if (blockIdx.x == 0 && wv == 3 && (tid & 63) < M_BEAT / 64) {
            __hip_atomic_store(meta + (tid & 63) * 64, 0u, __ATOMIC_RELAXED, __HIP_MEMORY_SCOPE_AGENT);
            __hip_atomic_store(meta + (tid & 63) * 64 + 1, 0u, __ATOMIC_RELAXED, __HIP_MEMORY_SCOPE_AGENT);
        }
        const __amdgpu_buffer_rsrc_t rsE = __builtin_amdgcn_make_buffer_rsrc((void*)m, 0, E * 8, 0x00020000);
        u32x2 mv[5];
        mv[4] = __builtin_amdgcn_raw_buffer_load_b64(rsE, (t8 & 511u) * (uint32_t)(E >> 6), 0, 0);
#pragma unroll
        for (int q = 0; q < 4; q++)
            mv[q] = __builtin_amdgcn_raw_buffer_load_b64(rsE, t8 & 511u, ((blockIdx.x >> 2) * 256u + (blockIdx.x & 3u) * 16u + (uint32_t)wv + 64u * q) * 512u, AUX_NT);
#pragma unroll
        for (int d = 0; d < MDL; d++) load_one(raw[1], tl0, d);
        asm volatile("" : "+s"(wd0), "+s"(wd1));
        const u32x2 ci = {CINIT, CINIT};
        *(LDS u32x2*)(uintptr_t)(P_CNT + tid * 8) = ci;
        if (tid < 64) lds_st(P_CNT + (NB + tid) * 4, 0x40000000u);
        if (tid < 8 && tid != 2) lds_st(P_FLAG + tid * 4, tid == 0 ? 0xffffffffu : 0u);
        if (tid == 128) lds_st(P_FLAG + 8, tk);
        const uint32_t lane = (uint32_t)tid & 63u;
        int e0[4];
        {
            float w[16];
#pragma unroll
            for (int i = 0; i < 4; i++) {
                w[2 * i] = bf2f(wd0[i] & 0xffffu); w[2 * i + 1] = bf2f(wd0[i] >> 16);
                w[8 + 2 * i] = bf2f(wd1[i] & 0xffffu); w[8 + 2 * i + 1] = bf2f(wd1[i] >> 16);
            }
            auto dot = [&](u32x2 v, int c) __attribute__((always_inline)) -> f32x2 {
                const f32x2 w0 = {w[c], w[c + 1]}, w1 = {w[4 + c], w[5 + c]}, w2 = {w[8 + c], w[9 + c]}, w3 = {w[12 + c], w[13 + c]};
                return w0 * bf2f(v.x & 0xffffu) + w1 * bf2f(v.x >> 16) + w2 * bf2f(v.y & 0xffffu) + w3 * bf2f(v.y >> 16);
            };
#pragma unroll
            for (int cp = 0; cp < 2; cp++) {
                if (cp == 1) {
#pragma unroll
                    for (int d = MDL; d < MDM; d++) load_one(raw[1], tl0, d);
                }
                if (blockIdx.x >= SLOTI / 64) continue;
                const f32x2 sd = dot(mv[4], 2 * cp);
#pragma unroll
                for (int e = 0; e < 2; e++) {
                    const int ex = (int)(wave_max(__float_as_uint(sd[e]) & 0x7fffffffu) >> 23);
                    e0[2 * cp + e] = __builtin_amdgcn_readfirstlane(ex == 0 ? 0 : min(max(ex - 129, -100), 100));
                }
                const f32x2 sc = {__uint_as_float((uint32_t)(127 - e0[2 * cp]) << 23), __uint_as_float((uint32_t)(127 - e0[2 * cp + 1]) << 23)};
                u32x4 o;
                u16x2 mx = {0, 0};
                f32x2 sq = {0.f, 0.f}, sg = {0.f, 0.f};
#pragma unroll
                for (int q = 0; q < 4; q++) {
                    const f32x2 y = dot(mv[q], 2 * cp) * sc;
                    const uint32_t hb = pk_bf16(y.x, y.y);
                    const f32x2 x = {__uint_as_float(hb << 16), __uint_as_float(hb & 0xffff0000u)};
                    o[q] = __builtin_bit_cast(uint32_t, __builtin_amdgcn_cvt_pkrtz(x.x, x.y));
                    mx = __builtin_elementwise_max(mx, __builtin_bit_cast(u16x2, hb & 0x7fff7fffu));
                    sq += x * x;
                    sg += x;
                }
                *(LDS u32x4*)(uintptr_t)(P_BUF + (cp * 64u + lane) * 272u + wv * 16) = o;
#pragma unroll
                for (int e = 0; e < 2; e++) {
                    const u32x4 st = {wave_max(mx[e]), __float_as_uint(wave_sum(sq[e])), __float_as_uint(wave_sum(sg[e])), 0u};
                    if (lane == 0u) *(LDS u32x4*)(uintptr_t)(P_LST + ((2 * cp + e) * 16 + wv) * 16) = st;
                }
            }
        }
        __builtin_amdgcn_sched_barrier(0);
        const uint16_t fq = freq[0];
#pragma unroll
        for (int d = MDM; d < 6; d++) load_one(raw[1], tl0, d);
#pragma unroll
        for (int d = 0; d < 6; d++)
            if (((LPOS >> (4 * d)) & 15) < 4) load_one(raw[0], tl1, d);
        __syncthreads();
        u32x4 mo[2];
#pragma unroll
        for (int cp = 0; cp < 2; cp++) mo[cp] = *(const LDS u32x4*)(uintptr_t)(P_BUF + (cp * 64u + (t8 >> 7)) * 272u + ((t8 + t8) & 0xf0u));
        if (blockIdx.x == 0 && wv == 3) asm volatile("s_waitcnt vmcnt(0)" ::: "memory");
        __syncthreads();
#pragma unroll
        for (int s = 0; s < 16; s++) *(LDS u32x2*)(uintptr_t)(la + s * 256) = nul;
        asm volatile("" : "+v"(raw[1].o[0]), "+v"(raw[1].o[1]), "+v"(raw[1].i[0]), "+v"(raw[1].i[1]), "+v"(raw[1].a[0]), "+v"(raw[1].a[1]));
        asm volatile("s_nop 0\n\tv_readfirstlane_b32 %0, %1" : "=s"(frr) : "v"(bf2f(fq) * 0.15915494309189535f));
        if (blockIdx.x < SLOTI / 64) {
            const __amdgpu_buffer_rsrc_t rsM = __builtin_amdgcn_make_buffer_rsrc((void*)(meta + W_MDC / 4), 0, (int)(128u * SLOTI * 4u), 0x00020000);
#pragma unroll
            for (int cp = 0; cp < 2; cp++) {
                const uint32_t row = cp * 64u + (t8 >> 7), c16 = (t8 + t8) & 0xf0u;
                __builtin_amdgcn_raw_buffer_store_b128(mo[cp], rsM, row * (SLOTI * 4u) + c16 + blockIdx.x * 256u, 0, AUX_SC1);
            }
            if (wv == 0) {
                const u32x4 st = *(const LDS u32x4*)(uintptr_t)(P_LST + t8 * 2u);
                const float sm1 = row_sum(__uint_as_float(st.z));
                const u32x2 r = {row_max(st.x) | (row_max(st.x ? 0xffffu - st.x : 0u) << 16), __float_as_uint(row_sum(__uint_as_float(st.y)))};
                if ((lane & 15u) == 15u) {
                    ((u32x2*)(meta + W_STAT / 4))[(lane >> 4) * NWG + blockIdx.x] = r;
                    meta[W_SUM / 4 + (lane >> 4) * NWG + blockIdx.x] = __float_as_uint(sm1);
                }
                if (blockIdx.x == 0 && lane < 4u) meta[M_E0 + lane] = (uint32_t)(lane == 0u ? e0[0] : lane == 1u ? e0[1] : lane == 2u ? e0[2] : e0[3]);
            }
        }
    }
    pin(la);
    RTS(5)

    uint32_t w[8], aw[8], xv[8];
    u64 nm[8];
    auto later2 = [&](int k) __attribute__((always_inline)) {
        uint32_t x0, x1;
        asm volatile("v_lshrrev_b32 %0, 4, %2\n\tv_lshrrev_b32 %1, 4, %5\n\tv_bitop3_b32 %0, %2, %0, 12 bitop3:0x72\n\tv_bitop3_b32 %1, %5, %1, 12 bitop3:0x72\n\t"
                     "s_not_b64 exec, %8\n\tds_write_b32 %2, %3 offset:%10\n\tds_or_b32 %0, %4 offset:%10\n\t"
                     "s_not_b64 exec, %9\n\tds_write_b32 %5, %6 offset:%10\n\tds_or_b32 %1, %7 offset:%10\n\ts_mov_b64 exec, -1"
                     : "=&v"(x0), "=&v"(x1)
                     : "v"(aw[k]), "v"(w[k]), "v"(xv[k]), "v"(aw[k + 1]), "v"(w[k + 1]), "v"(xv[k + 1]), "s"(nm[k]), "s"(nm[k + 1]), "n"(P_BUF) : "memory", "scc");
    };
    auto drain = [&]() __attribute__((always_inline)) {
        if (wv == 1) {
            const uint32_t q = (epoch + 1u) & 3u;
            const uint32_t n = __builtin_amdgcn_readfirstlane(lds_ld(vconst<P_FLAG + 16>() + q * 4u));
            if (__builtin_expect(n != 0u, 0)) {
                pin(t8);
                const uint32_t l = (t8 >> 3) & 63u;
                if (l < min(n, (uint32_t)LCAP)) {
                    const u32x4 e = *(const LDS u32x4*)(uintptr_t)(P_LST + (q * LCAP + l) * 16u);
                    lds_st(P_BUF + e.x, e.y);
                    lds_or(P_BUF + slot_x(e.x), e.z);
                }
                lds_st(vconst<P_FLAG + 16>() + q * 4u, vconst<0>());
            }
        }
    };
    auto again = [&](int D) __attribute__((always_inline)) {
        do {
            general(D, true, fm);
            __syncthreads();
            later2(0); later2(2); later2(4); later2(6);
            drain();
#pragma unroll
            for (int k = 0; k < 8; k++) {
                pin(aw[k]);
                bool due = false;
                if (aw[k] & 0x80000000u) {
                    xv[k] -= CHV;
                    if (xv[k] < CHV) {
                        const uint32_t f = (aw[k] >> 16) & 0x3ffu;
                        aw[k] = slot_w(aw[k] & 0xffffu, xv[k]);
                        xv[k] = f << ((xv[k] >> 8) & 31u);
                        due = true;
                    } else lds_st(vconst<P_FLAG>(), epoch + 1u);
                }
                nm[k] = ~__builtin_amdgcn_ballot_w64(due);
            }
            look(*(const LDS u32x2*)(uintptr_t)(P_CNT + t8));
            asm volatile("s_waitcnt lgkmcnt(0)" ::: "memory");
            __syncthreads();
        } while (__builtin_amdgcn_readfirstlane(lds_ld(vconst<P_FLAG>())) == epoch);
    };
    auto round = [&](auto DD, auto LL, Raw& rc, Raw& rn, int rd) __attribute__((always_inline)) {
        constexpr int D = decltype(DD)::value;
        constexpr bool LAST = decltype(LL)::value == 1, FIRST = decltype(LL)::value == 2;
        const int ng = min(GR, G - tl0 * GR);
        auto hook = [&](int p) __attribute__((always_inline)) {
#pragma unroll
            for (int q = 0; q < 6; q++)
            {
                const int lp = (LPOS >> (4 * q)) & 15;
                if (!LAST && lp < 4 && lp == p) load_one(rc, tl2, q);
                if (!LAST && lp >= 4 && (lp < 8 || lp >= 12 ? lp : lp - 8) == p) load_one(rn, tl1, q);
            }
        };
        uint32_t wn[8], ca[8], px[4], cv[8];
        float mx;
        float cs[2], cm[2] = {0.f, 0.f};
        auto convA = [&](int q) __attribute__((always_inline)) {
            if (LAST) return;
            const uint32_t a = rc.a[q >> 1][q & 1];
            const f32x2 av = {__uint_as_float(a << 16), __uint_as_float(a & 0xffff0000u)};
            f32x2 rv;
            asm("v_pk_mul_f32 %0, %1, %2 op_sel_hi:[1,0]" : "=v"(rv) : "v"(av), "s"((u64)__float_as_uint(frr)));
            mx = __builtin_elementwise_maximum(__builtin_elementwise_maximum(__builtin_fabsf(rv.x), __builtin_fabsf(rv.y)), (q & 1) ? mx : 0.f);
            cs[0] = __builtin_amdgcn_cosf(rv.x); cs[1] = __builtin_amdgcn_cosf(rv.y);
            asm volatile("" : "+v"(mx), "+v"(cs[0]), "+v"(cs[1]));
            if (FIRST) {
                const float c0 = fminf(fmaxf(cs[0], -1.f), 1.f), c1 = fminf(fmaxf(cs[1], -1.f), 1.f);
                cm[0] += c0 + c1; cm[1] += c0 * c0 + c1 * c1;
                if (q == 3 && wv == 4) {
                    const float a0 = wave_sum(cm[0]), a1 = wave_sum(cm[1]);
                    if ((t8 & 0x1f0u) == 0u) meta[W_SUM / 4 + (4 + ((t8 >> 3) & 1u)) * NWG + blockIdx.x] = __float_as_uint((t8 & 8u) ? a1 : a0);
                }
            }
            __builtin_amdgcn_sched_barrier(0);
        };
        auto convB = [&](int k) __attribute__((always_inline)) {
            if (LAST) return;
            const uint32_t o = rc.o[k >> 2][k & 3], i = rc.i[k >> 2][k & 3];
            wn[k] = (__builtin_amdgcn_alignbit(bfi(63u, o, o >> 5), __float_as_uint(cs[k & 1]), 30u) << 14) | (i >> 6);
            ca[k] = (bfi(63u, i, o) << 2) & 0x1ffcu;
            pin(wn[k]); pin(ca[k]);
            __builtin_amdgcn_sched_barrier(0);
        };
        auto convD = [&](int q) __attribute__((always_inline)) {
            if (LAST) return;
            asm volatile("v_cvt_pk_f16_f32 %0, |%1|, |%2|\n\tv_pk_mul_f16 %0, %0, %3 op_sel_hi:[1,0]" : "=v"(px[q]) : "v"(cs[0]), "v"(cs[1]), "s"(0x3ffu));
        };
        uint32_t cst;
        auto check = [&](int j) __attribute__((always_inline)) {
            if (LAST) return;
            if (__builtin_expect((__builtin_amdgcn_ballot_w64(!(mx <= RMAXREV)) | (u64)(uint32_t)(GR - ng)) != 0ull, 0)) {
                pin(t8);
                asm volatile("" : "+v"(rc.a[j]));
                const int lg = j * NT + (int)(t8 >> 3);
#pragma unroll
                for (int k = 0; k < 4; k++) {
                    const uint32_t a = (k & 1) ? (rc.a[j][k >> 1] >> 16) : (rc.a[j][k >> 1] & 0xffffu);
                    const bool nrm = __builtin_fabsf(frr * bf2f(a)) <= RMAXREV;
                    if (!(lg < ng && nrm)) ca[j * 4 + k] = NB * 4u + ((t8 >> 1) & 0xfcu);
                    if (lg < ng && !nrm) {
                        ready();
                        __builtin_amdgcn_raw_buffer_store_b32((uint32_t)((tl0 * GR + lg) * 4 + k), rsR, spec0 - draw(vconst<M_LOG * 4 + 4>()) * 4u, 0, 0);
                    }
                }
            }
            cst = vconst<CSTEP>();
        };
        auto ticket = [&](int k) __attribute__((always_inline)) {
            if (LAST) return;
            cv[k] = lds_add(P_CNT + ca[k], cst);
            __builtin_amdgcn_sched_barrier(0);
        };
        auto insert = [&](int j) __attribute__((always_inline)) {
            const int k0 = 4 * j;
            const bool odd = __builtin_amdgcn_ballot_w64(max(max(cv[k0], cv[k0 + 1]), max(cv[k0 + 2], cv[k0 + 3])) >= 2u * CHV) != 0ull;
#pragma unroll
            for (int k = k0; k < k0 + 4; k += 2) {
                uint32_t x0, x1;
                asm volatile("v_lshrrev_b32 %6, 11, %8\n\tv_lshrrev_b32 %7, 11, %9\n\t"
                             "v_bitop3_b32 %6, %6, %10, 60 bitop3:0x28\n\tv_bitop3_b32 %7, %7, %11, 60 bitop3:0x28\n\t"
                             "v_lshl_or_b32 %0, %10, 4, %6\n\tv_lshl_or_b32 %1, %11, 4, %7\n\t"
                             "v_lshlrev_b32_sdwa %2, %8, %12 dst_sel:DWORD dst_unused:UNUSED_PAD src0_sel:BYTE_1 src1_sel:WORD_0\n\t"
                             "v_lshlrev_b32_sdwa %3, %9, %12 dst_sel:DWORD dst_unused:UNUSED_PAD src0_sel:BYTE_1 src1_sel:WORD_1\n\t"
                             "v_bitop3_b32 %6, %0, %10, 12 bitop3:0x72\n\tv_bitop3_b32 %7, %1, %11, 12 bitop3:0x72\n\t"
                             "v_cmpx_gt_u32 %4, %15, %8\n\tds_write_b32 %0, %13 offset:%16\n\tds_or_b32 %6, %2 offset:%16\n\ts_mov_b64 exec, -1\n\t"
                             "v_cmpx_gt_u32 %5, %15, %9\n\tds_write_b32 %1, %14 offset:%16\n\tds_or_b32 %7, %3 offset:%16\n\ts_mov_b64 exec, -1"
                             : "=&v"(aw[k]), "=&v"(aw[k + 1]), "=&v"(xv[k]), "=&v"(xv[k + 1]), "=&s"(nm[k]), "=&s"(nm[k + 1]), "=&v"(x0), "=&v"(x1)
                             : "v"(cv[k]), "v"(cv[k + 1]), "v"(ca[k]), "v"(ca[k + 1]), "v"(px[k >> 1]), "v"(wn[k]), "v"(wn[k + 1]), "s"(CHV), "n"(P_BUF) : "memory");
                w[k] = wn[k]; w[k + 1] = wn[k + 1];
                if (__builtin_expect(odd, 0)) {
#pragma unroll
                    for (int q = k; q < k + 2; q++) {
                        nm[q] |= __builtin_amdgcn_ballot_w64(cv[q] >= 2u * CHV);
                        if (cv[q] >= 2u * CHV && cv[q] < 0x40000000u) {
                            const uint32_t lq = (epoch + 2u) & 3u;
                            uint32_t p = LCAP;
                            if (cv[q] < 3u * CHV) p = lds_add(vconst<P_FLAG + 16>() + lq * 4u, vconst<1>());
                            if (p < LCAP) {
                                const u32x2 e = {aw[q], w[q]};
                                *(LDS u32x2*)(uintptr_t)(P_LST + (lq * LCAP + p) * 16u) = e;
                                lds_st(P_LST + (lq * LCAP + p) * 16u + 8u, xv[q]);
                            } else {
                                aw[q] = ca[q] | ((q & 1) ? (px[q >> 1] & 0xffff0000u) : (px[q >> 1] << 16)) | 0x80000000u;
                                xv[q] = cv[q] - CHV;
                                lds_st(vconst<P_FLAG>(), epoch);
                            }
                        }
                    }
                }
            }
        };
        constexpr int P0 = D ? 0 : 3, P1 = D ? 1 : 2, P2 = D ? 2 : 1, P3 = D ? 3 : 0;
        const uint32_t fl = lds_ld(vconst<P_FLAG>());
        if (!LAST) asm volatile("" : "+v"(rc.o[0]), "+v"(rc.o[1]), "+v"(rc.i[0]), "+v"(rc.i[1]), "+v"(rc.a[0]), "+v"(rc.a[1]));
        if (!LAST && (rd + 4 >= nstat || tl1 >= ntile) && wv == 2) {
            pin(tk);
            if ((t8 & 511u) == 0u) {
                if (tl1 >= ntile) {
                    if (rd + 3 <= nstat) ready();
                    (void)draw(vconst<M_MD * 4>());
                } else if (rd + 4 == nstat) tk = peek(vconst<M_BEAT * 4>());
                else {
                    if (rd + 3 == nstat) {
                        if (tk == lds_ld(vconst<P_FLAG + 8>())) ready();
                        tk = 0u;
                    }
                    lds_st(vconst<P_FLAG + 4>(), tk);
                    if ((int)tk < ntile - nstat * NWG) tk = draw(vconst<M_TILE * 4>());
                }
            }
        }
        if (!LAST && blockIdx.x == 0 && wv == 3 && (t8 & 511u) == 0u) beat();
        if (PPF2 && !LAST && !FIRST && rd == nstat && wv == 5) { pin(tk); tk = CODE_AHEAD(part_kernel, PART_BYTES, (t8 >> 3) & 63u); }
        hook(12);
        pin(t8);
        const uint32_t tr = P_TAB + (t8 & ~63u), u16 = (t8 + t8) & 0x70u;
        convA(0);
        convB(0);
        const bool ag = __builtin_amdgcn_readfirstlane(fl) == epoch;
        if (!FIRST && __builtin_expect(LAST || ag || sm != 0ull, 0)) {
            if (ag) again(D);
            general(D, false, sm);
            Va = *(const LDS u32x4*)(uintptr_t)(P_TAB + (t8 & ~63u) + P0 * 16);
            Vb = *(const LDS u32x4*)(uintptr_t)(P_TAB + (t8 & ~63u) + P1 * 16);
            asm volatile("" : "+v"(Va), "+v"(Vb));
        }
        uint32_t pq = (uint32_t)pb2 | (uint32_t)(pb2 >> 32);
        pq = (pq | (pq >> 1)) & 0x55555555u;
        pq = __builtin_amdgcn_readfirstlane(pq + (pq & (far * NEARM)));
        uint32_t ad[8];
        if (!FIRST) ad[0] = take<4 * P0 + 0, D>(Va[0], u16, la, nul);
        convB(1);
        if (!FIRST) ad[1] = take<4 * P0 + 1, D>(Va[1], u16, la, nul);
        convD(0);
        convA(1);
        if (!FIRST) ad[2] = take<4 * P0 + 2, D>(Va[2], u16, la, nul);
        convB(2);
        hook(13);
        if (!FIRST) ad[3] = take<4 * P0 + 3, D>(Va[3], u16, la, nul);
        convB(3);
        if (!FIRST) put<4 * P0 + 0, LAST ? 3 : 3, LAST>(rsA, ad[0], pq);
        if (!FIRST) ad[4] = take<4 * P1 + 0, D>(Vb[0], u16, la, nul);
        convD(1);
        check(0);
        ticket(0);
        hook(4);
        if (!FIRST) put<4 * P0 + 1, LAST ? 3 : 4, LAST>(rsA, ad[1], pq);
        if (!FIRST) ad[5] = take<4 * P1 + 1, D>(Vb[1], u16, la, nul);
        convA(2);
        ticket(1);
        if (!FIRST) put<4 * P0 + 2, LAST ? 3 : 5, LAST>(rsA, ad[2], pq);
        if (!FIRST) ad[6] = take<4 * P1 + 2, D>(Vb[2], u16, la, nul);
        convB(4);
        ticket(2);
        if (!FIRST) put<4 * P0 + 3, LAST ? 3 : 6, LAST>(rsA, ad[3], pq);
        if (!FIRST) Va = *(const LDS u32x4*)(uintptr_t)(tr + P2 * 16);
        __builtin_amdgcn_sched_barrier(0);
        if (!FIRST) ad[7] = take<4 * P1 + 3, D>(Vb[3], u16, la, nul);
        convB(5);
        ticket(3);
        SETP(2);
        if (!FIRST) put<4 * P1 + 0, LAST ? 4 : 8, LAST>(rsA, ad[4], pq);
        if (!FIRST) ad[0] = take<4 * P2 + 0, D>(Va[0], u16, la, nul);
        convD(2);
        convA(3);
        hook(5);
        if (!FIRST) put<4 * P1 + 1, LAST ? 4 : 7, LAST>(rsA, ad[5], pq);
        if (!FIRST) ad[1] = take<4 * P2 + 1, D>(Va[1], u16, la, nul);
        convB(6);
        if (!FIRST) put<4 * P1 + 2, LAST ? 4 : 6, LAST>(rsA, ad[6], pq);
        if (!FIRST) ad[2] = take<4 * P2 + 2, D>(Va[2], u16, la, nul);
        convB(7);
        if (!FIRST) put<4 * P1 + 3, LAST ? 3 : 4, LAST>(rsA, ad[7], pq);
        if (!FIRST) Vb = *(const LDS u32x4*)(uintptr_t)(tr + P3 * 16);
        __builtin_amdgcn_sched_barrier(0);
        if (!FIRST) ad[3] = take<4 * P2 + 3, D>(Va[3], u16, la, nul);
        convD(3);
        check(1);
        ticket(4);
        SETP(1);
        hook(6);
        if (!FIRST) put<4 * P2 + 0, LAST ? 4 : 5, LAST>(rsA, ad[0], pq);
        if (!FIRST) ad[4] = take<4 * P3 + 0, D>(Vb[0], u16, la, nul);
        ticket(5);
        if (!FIRST) put<4 * P2 + 1, LAST ? 4 : 6, LAST>(rsA, ad[1], pq);
        if (!FIRST) ad[5] = take<4 * P3 + 1, D>(Vb[1], u16, la, nul);
        ticket(6);
        if (!FIRST) put<4 * P2 + 2, LAST ? 4 : 7, LAST>(rsA, ad[2], pq);
        if (!FIRST) ad[6] = take<4 * P3 + 2, D>(Vb[2], u16, la, nul);
        ticket(7);
        SETP(0);
        if (!FIRST) put<4 * P2 + 3, LAST ? 3 : 7, LAST>(rsA, ad[3], pq);
        if (!FIRST) ad[7] = take<4 * P3 + 3, D>(Vb[3], u16, la, nul);
        hook(7);
        SETP(3);
        asm volatile("s_waitcnt lgkmcnt(0)" ::: "memory");
        __syncthreads();
        u32x2 cn;
        uint32_t tv;
        if (!LAST) { cn = *(const LDS u32x2*)(uintptr_t)(P_CNT + t8); tv = lds_ld(vconst<P_FLAG + 4>()); }
        __builtin_amdgcn_sched_barrier(0);
        if (!FIRST) later2(0);
        if (!FIRST) put<4 * P3, 15, LAST>(rsA, ad[4], pq);
        if (!FIRST) later2(2);
        if (!FIRST) put<4 * P3 + 1, 15, LAST>(rsA, ad[5], pq);
        if (!FIRST) drain();
        if (!LAST) {
            tl2 = rd + 2 < nstat ? fixed_tile(rd + 2) : nstat * NWG + (int)__builtin_amdgcn_readfirstlane(tv);
            look(cn);
        }
        hook(0);
        SETP(2);
        if (!FIRST) later2(4);
        if (!FIRST) put<4 * P3 + 2, 15, LAST>(rsA, ad[6], pq);
        if (!FIRST) later2(6);
        if (!FIRST) put<4 * P3 + 3, 15, LAST>(rsA, ad[7], pq);
        if (!LAST) {
            hook(1);
            SETP(1);
            insert(0);
            hook(2);
            SETP(0);
            pin(t8);
            Va = *(const LDS u32x4*)(uintptr_t)(P_TAB + (t8 & ~63u) + P3 * 16);
            Vb = *(const LDS u32x4*)(uintptr_t)(P_TAB + (t8 & ~63u) + P2 * 16);
            __builtin_amdgcn_sched_barrier(0);
            insert(1);
            hook(3);
            SETP(3);
            asm volatile("s_waitcnt lgkmcnt(0)" ::: "memory");
            __syncthreads();
            tl0 = tl1; tl1 = tl2;
        }
    };
#define HMV(A, B) "v_mov_b64 v[" #A ":" #A "+1], v[" #B ":" #B "+1]\n\t"
#define HMV4(A) HMV(A + 8, A + 6) HMV(A + 6, A + 4) HMV(A + 4, A + 2) HMV(A + 2, A)
    round(IC<1>(), IC<2>(), raw[1], raw[0], 0);
    int rd = 1;
#pragma unroll 1
    for (; tl1 < ntile; rd += 2) {
        if (rd == 3) if (blockIdx.x == 5 && t8 == 0) meta[M_E0 + 8] = ((unsigned)__builtin_amdgcn_s_memrealtime() - (unsigned)tf_) * 10u;
        round(IC<0>(), IC<0>(), raw[0], raw[1], rd);
        round(IC<1>(), IC<0>(), raw[1], raw[0], rd + 1);
    }
    if (tl0 >= ntile) asm volatile(HMV4(88) HMV4(98) HMV4(108) HMV4(118));
    asm volatile("" : "+s"(tl0));
    if (tl0 < ntile) round(IC<0>(), IC<0>(), raw[0], raw[1], rd);
    RTS(1)
    if (CPF && wv == 5) tk = CODE_AHEAD(acc_kernel, ACC_BYTES, (t8 >> 3) & 63u);
    round(IC<1>(), IC<1>(), raw[1], raw[0], 0);
    if (__builtin_amdgcn_readfirstlane(lds_ld(P_FLAG + 16 + ((epoch + 2u) & 3u) * 4u)) != 0u) {
        asm volatile("s_waitcnt lgkmcnt(0)" ::: "memory");
        __syncthreads();
        look(*(const LDS u32x2*)(uintptr_t)(P_CNT + t8));
        general(0, true, fm);
        __syncthreads();
        drain();
    }
    {
        uint32_t* tcnt = meta + W_TCNT / 4;
        const u32x2 c = *(const LDS u32x2*)(uintptr_t)(P_CNT + t8);
        const uint32_t tw = tab_at();
#pragma unroll
        for (int e = 0; e < 2; e++) {
            const uint32_t g = U[e] & 15u;
            lds_st(tw + e * 64u, g >= 8u ? 0x80000000u : ((U[e] + ((g & 1u) ? 0u : SK * 64u)) & 0x7fffff81u) | (((7u - g) << 1) & 12u));
            *(LDS uint8_t*)(uintptr_t)(P_CNT + (t8 & ~511u) + (((tw - P_TAB) >> 2) & 127u) + e * 16u) =
                (uint8_t)(g < 8u ? 0u : ((g - 8u) >> 1) + (((c[e] + 2u * CSTEP) >> 15) << 2) + (g & 1u) * 0x50u);
        }
        asm volatile("s_waitcnt lgkmcnt(0)" ::: "memory");
        __syncthreads();
        const uint32_t u16 = (t8 + t8) & 0x70u;
        u32x4 En[4];
#pragma unroll
        for (int p = 0; p < 4; p++) En[p] = *(const LDS u32x4*)(uintptr_t)(P_TAB + (t8 & ~63u) + p * 16u);
        const u32x4 cb = *(const LDS u32x4*)(uintptr_t)(P_CNT + (t8 & ~511u) + ((t8 >> 2) & 0x70u));
        u32x2 N[16];
#pragma unroll
        for (int s = 0; s < 16; s++) N[s] = *(const LDS u32x2*)(uintptr_t)(la + s * 256u);
        if (u16 == 0u) *(u32x4*)(tcnt + blockIdx.x * 512u + (t8 >> 4)) = cb;
        const uint32_t sg = P_BUF + ((t8 & ~63u) << 4);
        uint32_t ps;
        auto rest = [&](auto SL) __attribute__((always_inline)) {
            constexpr int s = decltype(SL)::value;
            const uint32_t en = En[s >> 2][s & 3], by = cb[s >> 2] >> ((s & 3) * 8);
            const u32x2 Hr = hget<HREG(s, 0)>();
            const uint32_t at = ps + ((la & 0x38u) ^ ((s & 3) << 4));
            *(LDS u32x2*)(uintptr_t)at = Hr;
            *(LDS u32x2*)(uintptr_t)(at + (by & 0x40u)) = N[s];
            ps += (by & 0x3cu) << 2;
            const u32x2 Hs = (en & 1u) ? Hr : nul;
            const u32x4 d = {Hs.x, Hs.y, N[s].x, N[s].y};
            uint32_t a = __builtin_amdgcn_bitop3_b32(en, u16, 15u, 0x54);
            {
                const u32x4 rs_ = rsA;
                const uint32_t far_ = ((NEARM >> (2 * s)) & 1u) ? far : 0u;
                asm volatile("s_cmp_eq_u32 %3, 0\n\ts_cbranch_scc0 1f\n\tbuffer_store_dwordx4 %0, %1, %2, 0 offen sc1\n\ts_branch 2f\n"
                             "1:\n\tbuffer_store_dwordx4 %0, %1, %2, 0 offen sc1 nt\n2:\n\ts_nop 1" :: "v"(d), "v"(a), "s"(rs_), "s"(far_) : "memory", "scc");
            }
            if (__builtin_expect(__builtin_amdgcn_ballot_w64((en & 12u) != 0u) != 0ull, 0)) {
                const u32x4 dn = {nul.x, nul.y, nul.x, nul.y};
#pragma unroll 1
                for (uint32_t k = (en >> 2) & 3u; k > 0u; k--) {
                    a += SK * 128u;
                    __builtin_amdgcn_raw_buffer_store_b128(dn, rsR, a, 0, AUX_SC1);
                }
            }
        };
        auto pack = [&](int p) __attribute__((always_inline)) {
            const uint32_t a = (SBASE + ((((t8 >> 1) & 0x60u) + (t8 >> 8)) * 2u + p) * (NWG * 9u) + blockIdx.x * 9u) * 128u + u16;
            asm volatile("" ::: "memory");
#pragma unroll 1
            for (uint32_t q = 0; __builtin_amdgcn_ballot_w64(sg + q < ps) != 0ull; q += 256u) {
                const u32x4 d0 = *(const LDS u32x4*)(uintptr_t)(sg + q + u16), d1 = *(const LDS u32x4*)(uintptr_t)(sg + q + 128u + u16);
                __builtin_amdgcn_raw_buffer_store_b128(d0, rsR, sg + q < ps ? a + q : 0xffffff00u, 0, AUX_SC1);
                __builtin_amdgcn_raw_buffer_store_b128(d1, rsR, sg + q + 128u < ps ? a + q + 128u : 0xffffff00u, 0, AUX_SC1);
            }
            asm volatile("" ::: "memory");
        };
        asm volatile("" : "+v"(N[0]), "+v"(N[1]), "+v"(N[2]), "+v"(N[3]), "+v"(N[4]), "+v"(N[5]), "+v"(N[6]), "+v"(N[7]) :: "memory");
        asm volatile("" : "+v"(N[8]), "+v"(N[9]), "+v"(N[10]), "+v"(N[11]), "+v"(N[12]), "+v"(N[13]), "+v"(N[14]), "+v"(N[15]) :: "memory");
        ps = sg;
        rest(IC<0>()); rest(IC<1>()); rest(IC<2>()); rest(IC<3>()); rest(IC<4>()); rest(IC<5>()); rest(IC<6>()); rest(IC<7>());
        pack(0);
        ps = sg;
        rest(IC<8>()); rest(IC<9>()); rest(IC<10>()); rest(IC<11>()); rest(IC<12>()); rest(IC<13>()); rest(IC<14>()); rest(IC<15>());
        pack(1);
    }
    RTS(2)
    asm volatile("" :: "v"(tk));
    if (blockIdx.x == 0 && t8 == 1536u) {
        do {
            beat();
            __builtin_amdgcn_s_sleep(8);
        } while (peek(vconst<M_MD * 4>()) < NWG);
    }
    RTS(3)
    RTS(4)
}

#define A_MD 0
#define A_ACC (SLOTI * 4)
#define A_FLAG (A_ACC + SLOTO * 4)
#define A_LDS (A_FLAG + 512)
static_assert(SLOTI * 4 == 3 * NT * 16 + 1024, "md staging assumes three full vectors and 256 dwords");
#define BIAS 0x40004000u
#define NFAST 8
#define NLD 10
#define MDF 0x0a11333
#define MDP 0xaaaaa
#define HP 2
#define SHARE (SLOTO / 8)
#define SPINCAP 4096u
#define ZSIG 9.0f
#define HSLOW 0x80008000u
#define GAS __attribute__((address_space(1)))

extern "C" __global__ __launch_bounds__(NT, 4) __attribute__((amdgpu_num_vgpr(53))) void acc_kernel(char* ws, int T, int E, int ks, int lcap, float zf, int pad,
                                                 const uint16_t* __restrict__ centers, const uint16_t* __restrict__ wrbf,
                                                 const uint16_t* __restrict__ wsbf, const uint16_t* __restrict__ wup,
                                                 const u32x2* __restrict__ m, const uint16_t* __restrict__ dist, u32x2* __restrict__ out,
                                                 const int* __restrict__ iin, const int* __restrict__ iout, const uint16_t* __restrict__ ang,
                                                 const uint16_t* __restrict__ wdown, const uint16_t* __restrict__ freq) {
    const char* recs = ws + W_RECS;
    const u32x4* tcnt = (const u32x4*)(ws + W_TCNT);
    const uint32_t* mdc = (const uint32_t*)(ws + W_MDC);
    const u32x4* stat = (const u32x4*)(ws + W_STAT);
    uint32_t* partial = (uint32_t*)(ws + W_PART);
    unsigned* meta = (unsigned*)ws;
    extern __shared__ __attribute__((aligned(16))) char smem[];
    RTS(8)
    unsigned* acc = (unsigned*)(smem + A_ACC);
    unsigned* s_flag = (unsigned*)(smem + A_FLAG);
    const int tid = threadIdx.x;
    const int lane = tid & 63;
    const int wv = __builtin_amdgcn_readfirstlane(tid >> 6);
    const int cp = (blockIdx.x >> 3) & 1;
    const int h = blockIdx.x & 3;
    const int j = ((blockIdx.x >> 2) & 1) * 16 + (blockIdx.x >> 4);
    u32x4 tc;
    const __amdgpu_buffer_rsrc_t rsM = __builtin_amdgcn_make_buffer_rsrc((void*)mdc, 0, (int)(128u * SLOTI * 4u), 0x00020000);

    u32x4 pm[4];
    u32x4 ring[NLD];
    auto load_md = [&](int ff, int pl, int mdp) __attribute__((always_inline)) {
        const uint32_t so = (uint32_t)(cp * 64 + ff * 4 + h) * (SLOTI * 4u);
#pragma unroll
        for (int v = 0; v < 3; v++)
            if (pl < 0 || pl == ((mdp >> (4 * v)) & 15)) pm[v] = __builtin_amdgcn_raw_buffer_load_b128(rsM, (uint32_t)tid * 16u, so + (uint32_t)v * (NT * 16u), 0);
        if ((pl < 0 || pl == (mdp & 15)) && wv == 0) pm[3] = __builtin_amdgcn_raw_buffer_load_b128(rsM, (uint32_t)tid * 16u, so + 3u * NT * 16u, 0);
        __builtin_amdgcn_sched_barrier(0);
    };
    const uint32_t lf = (uint32_t)(tid >> 9) * (SKH * 128u) + (uint32_t)(tid & 0x1e0) * 16u + (uint32_t)(tid & 12) * 32u + (uint32_t)(tid & 16) * 4u + (uint32_t)(tid & 3) * 16u;
    const uint32_t lp = (uint32_t)(tid >> 8) * (SKM * 128u) + (uint32_t)((tid >> 2) & 63) * 128u + (uint32_t)(tid & 3) * 16u;
    auto load_main = [&](int i, int ff, bool near = false) __attribute__((always_inline)) -> u32x4 {
        const uint32_t q = (uint32_t)(((i >> 1) - HP) & 3), SK = q ? SKM : SKH, SS = q ? SSM : SSH;
        const __amdgpu_buffer_rsrc_t rs = __builtin_amdgcn_make_buffer_rsrc((void*)(recs + (q * SQ + (uint32_t)ff * SS + (uint32_t)(h * 32 + j) * SC) * 128u), 0, ks * (int)(SK * 128u), 0x00020000);
        if (q == 0u && near) return __builtin_amdgcn_raw_buffer_load_b128(rs, lf, (i & 1) * (int)(2u * SK * 128u), 0);
        if (q == 0u) return __builtin_amdgcn_raw_buffer_load_b128(rs, lf, (i & 1) * (int)(2u * SK * 128u), AUX_NT);
        return __builtin_amdgcn_raw_buffer_load_b128(rs, lp + (uint32_t)(i & 1) * 64u, 0, 0);
    };
    const __amdgpu_buffer_rsrc_t rsT = __builtin_amdgcn_make_buffer_rsrc((void*)recs, 0, (int)(LBASE * 128u), 0x00020000);
    const uint32_t tof = (uint32_t)(lane & 2) * (TPL * 64u) + (uint32_t)(tid >> 2) * 128u + (uint32_t)(lane & 1) * 64u + 4u + (TBASE + (uint32_t)(h * 32 + j) * TST) * 128u;
    const uint32_t sp0 = (SBASE + (uint32_t)(h * 32 + j) * (2u * NWG * 9u) + (uint32_t)(tid >> 2) * 9u) * 128u;
    auto tail_rank = [&](uint32_t c, uint32_t sp, int ff, uint32_t t0, int& dst, uint32_t& en) __attribute__((always_inline)) -> int {
        const int k4 = (int)(((uint32_t)(lane & 3) + t0) * 4u) - (int)((c & 3u) * 8u);
        const int left = (int)((c >> 2) & 15u) - k4;
        const bool has = left > 0;
        uint32_t ea = tof + (uint32_t)ff * (128u * TST * 128u), eb = sp + (uint32_t)(k4 * 16 + min(max(left, 0), 4));
        asm volatile("" : "+v"(ea), "+v"(eb));
        en = k4 < 0 ? ea : eb;
        const u64 mask = __builtin_amdgcn_ballot_w64(has);
        const int n = __builtin_popcountll(mask);
        const int rank = __builtin_amdgcn_mbcnt_hi((uint32_t)(mask >> 32), __builtin_amdgcn_mbcnt_lo((uint32_t)mask, 0u));
        const int r = has ? rank : (n + lane - rank);
        dst = ((r & 15) << 4) | ((r >> 4) << 2);
        return n;
    };
    auto load_tail = [&](uint32_t en, u64& tm) __attribute__((always_inline)) -> u32x4 {
        const bool on = (uint32_t)(lane & 3) < (en & 7u);
        tm = __builtin_amdgcn_ballot_w64(on);
        return __builtin_amdgcn_raw_buffer_load_b128(rsT, on ? (en & ~15u) + (uint32_t)(lane & 3) * 16u : 0xffffff00u, 0, 0);
    };
    f32x2 fa[4][4];
    u32x4 em[2];
    u32x2 dw2;
    uint32_t wl;
    const __amdgpu_buffer_rsrc_t rsE = __builtin_amdgcn_make_buffer_rsrc((void*)m, 0, E * 8, 0x00020000);
    const __amdgpu_buffer_rsrc_t rsD = __builtin_amdgcn_make_buffer_rsrc((void*)dist, 0, E * 2, 0x00020000);
    auto acc_vec = [&](int i) __attribute__((always_inline)) -> u32x4 {
        const uint32_t a = A_ACC + (uint32_t)(i >> 4) * 256u + (uint32_t)(i & 15) * 8u;
        const u32x2 x = *(const LDS u32x2*)(uintptr_t)a, y = *(const LDS u32x2*)(uintptr_t)(a + 128u);
        const u32x4 v = {x.x, x.y, y.x, y.y};
        return v;
    };
    auto edge0 = [&](int sh) __attribute__((always_inline)) -> uint32_t {
        return (tid < SHARE / 4) ? (uint32_t)(((sh * (SHARE / 64) + (tid >> 4)) << 11) | (j << 6) | ((tid & 15) * 2)) : 0x0fffff00u;
    };
    auto load_m = [&](int sh) __attribute__((always_inline)) {
        em[0] = __builtin_amdgcn_raw_buffer_load_b128(rsE, edge0(sh) * 8u, 0, AUX_NT);
        em[1] = __builtin_amdgcn_raw_buffer_load_b128(rsE, edge0(sh) * 8u + 256u, 0, AUX_NT);
    };
    auto load_d = [&](int sh) __attribute__((always_inline)) {
        dw2.x = __builtin_amdgcn_raw_buffer_load_b32(rsD, edge0(sh) * 2u, 0, 0);
        dw2.y = __builtin_amdgcn_raw_buffer_load_b32(rsD, edge0(sh) * 2u + 64u, 0, 0);
    };
    auto load_wl = [&]() __attribute__((always_inline)) {
        const u64 p0 = (u64)wsbf, p1 = (u64)wrbf, p2 = (u64)centers, p3 = (u64)wup;
        const u64 pa = lane < 16 ? p0 : p1, pb = lane < 40 ? p2 : p3;
        wl = ((const GAS uint32_t*)(lane < 32 ? pa : pb))[lane & (lane < 32 ? 15 : lane < 40 ? 3 : 7)];
    };
    auto factor = [&](int r, bool first) __attribute__((always_inline)) {
        const float sq = 3.7982826f;
        const f32x2 d01 = {bf2f(dw2.x & 0xffffu), bf2f(dw2.x >> 16)}, d23 = {bf2f(dw2.y & 0xffffu), bf2f(dw2.y >> 16)};
        const uint32_t cw = __builtin_amdgcn_readlane(wl, 32 + (r >> 1));
        const float cn = bf2f((r & 1) ? (cw >> 16) : (cw & 0xffffu));
        f32x2 w[4];
#pragma unroll
        for (int k = 0; k < 4; k++) {
            const uint32_t x = __builtin_amdgcn_readlane(wl, (k >> 1) * 16 + r * 2 + (k & 1));
            w[k].x = bf2f(x & 0xffffu); w[k].y = bf2f(x >> 16);
        }
        const f32x2 y01 = (d01 - cn) * sq, y23 = (d23 - cn) * sq;
        const f32x2 t01 = -y01 * y01, t23 = -y23 * y23;
        const float rb[4] = {__builtin_amdgcn_exp2f(t01.x), __builtin_amdgcn_exp2f(t01.y), __builtin_amdgcn_exp2f(t23.x), __builtin_amdgcn_exp2f(t23.y)};
#pragma unroll
        for (int q = 0; q < 4; q++) {
#pragma unroll
            for (int k = 0; k < 4; k++) fa[q][k] = first ? w[k] * rb[q] : __builtin_elementwise_fma(w[k], (f32x2)(rb[q]), fa[q][k]);
        }
    };
    auto edges = [&](int sh) __attribute__((always_inline)) {
        load_d(sh);
        load_m(sh);
        factor(0, true);
#pragma unroll 1
        for (int r = 1; r < 8; r++) factor(r, false);
    };
#define ADT(V, O) "ds_write_addtid_b32 " V " offset:" #O "\n\t"
    auto store_md = [&]() __attribute__((always_inline)) {
        asm volatile("s_mov_b32 m0, %12\n\ts_nop 0\n\t" ADT("%0", 0) ADT("%1", 256) ADT("%2", 512) ADT("%3", 768) ADT("%4", 16384) ADT("%5", 16640) ADT("%6", 16896) ADT("%7", 17152)
                     ADT("%8", 32768) ADT("%9", 33024) ADT("%10", 33280) ADT("%11", 33536)
                     :: "v"(pm[0][0]), "v"(pm[0][1]), "v"(pm[0][2]), "v"(pm[0][3]), "v"(pm[1][0]), "v"(pm[1][1]), "v"(pm[1][2]), "v"(pm[1][3]),
                        "v"(pm[2][0]), "v"(pm[2][1]), "v"(pm[2][2]), "v"(pm[2][3]), "s"(wv * 1024) : "memory");
        if (wv == 0) asm volatile("s_mov_b32 m0, 0\n\ts_nop 0\n\t" ADT("%0", 49152) ADT("%1", 49408) ADT("%2", 49664) ADT("%3", 49920)
                                  :: "v"(pm[3][0]), "v"(pm[3][1]), "v"(pm[3][2]), "v"(pm[3][3]) : "memory");
    };
    const int me = h * 2 + cp;
    GAS unsigned* dw = (GAS unsigned*)(meta + M_DONE + j * 64);
    const bool fin = wv < (SHARE / 4 + 63) / 64;
    bool fast, more;
    uint32_t need, mg0, mg1;
    uint32_t guard = 0;
    uint32_t sgn = 0x8000u;
    pin(sgn);

#define GAT3(M) { uint32_t a0_, a1_, a2_; asm volatile( \
        "v_lshlrev_b16 %0, 2, %3\n\tv_lshlrev_b16 %1, 2, %4\n\tv_lshlrev_b16 %2, 2, %5\n\t" \
        "ds_read_b32 v[%6], %0\n\tds_read_b32 v[%6+1], %1\n\tds_read_b32 v[%6+2], %2" \
        : "=&v"(a0_), "=&v"(a1_), "=&v"(a2_) : "v"(w0), "v"(w1), "v"(w2), "n"(M) : "memory"); }
#define MIXL " op_sel_hi:[1,1,0]\n\t"
#define MIXH " op_sel:[0,1,0] op_sel_hi:[1,1,0]\n\t"
#define SDW " dst_sel:DWORD dst_unused:UNUSED_PAD src0_sel:DWORD src1_sel:WORD_1\n\t"
#define FOLD "v_bitop3_b32 %6, %6, v[%15-9], v[%15-8] bitop3:0xfe\n\tv_or_b32 %6, v[%15-7], %6\n\t"
#define SCAT(PRE, POST, M) { uint32_t c0_, c1_, c2_, a0_, a1_, a2_; asm volatile( \
        FOLD "v_and_b32 %0, 0x3ff, %7\n\tv_lshrrev_b32 %2, 22, %7\n\t" PRE \
        "v_bitop3_b32 %0, %11, %8, %0 bitop3:0xea\n\tv_bitop3_b32 %2, %11, %10, %2 bitop3:0xea\n\tv_bfe_u32 %1, %7, 11, 10\n\t" \
        "v_fma_mix_f32 %3, %0, v[%15], %12" MIXL "v_bitop3_b32 %1, %11, %9, %1 bitop3:0xea\n\tv_fma_mix_f32 %0, %0, v[%15], %13" MIXH \
        "v_fma_mix_f32 %4, %1, v[%15+1], %12" MIXL "v_lshl_add_u32 %0, %0, 16, %3\n\tv_lshlrev_b32_sdwa %3, 2, %8" SDW \
        "v_fma_mix_f32 %1, %1, v[%15+1], %13" MIXH "ds_add_rtn_u32 v[%15-9], %3, %0 offset:%14\n\t" \
        "v_fma_mix_f32 %5, %2, v[%15+2], %12" MIXL "v_lshl_add_u32 %1, %1, 16, %4\n\tv_lshlrev_b32_sdwa %4, 2, %9" SDW \
        "v_fma_mix_f32 %2, %2, v[%15+2], %13" MIXH "ds_add_rtn_u32 v[%15-8], %4, %1 offset:%14\n\t" \
        "v_lshl_add_u32 %2, %2, 16, %5\n\tv_lshlrev_b32_sdwa %5, 2, %10" SDW \
        "ds_add_rtn_u32 v[%15-7], %5, %2 offset:%14" POST \
        : "=&v"(c0_), "=&v"(c1_), "=&v"(c2_), "=&v"(a0_), "=&v"(a1_), "=&v"(a2_), "+v"(guard) \
        : "v"(xw), "v"(w0), "v"(w1), "v"(w2), "v"(sgn), "s"(mg0), "s"(mg1), "n"(A_ACC), "n"(M), "s"(tm) : "memory"); }
    asm volatile("v_mov_b32 v110, 0\n\tv_mov_b32 v111, 0\n\tv_mov_b32 v112, 0\n\tv_mov_b32 v113, 0\n\tv_mov_b32 v114, 0\n\tv_mov_b32 v115, 0\n\t"
                 "v_mov_b32 v116, 0\n\tv_mov_b32 v117, 0\n\tv_mov_b32 v118, 0" ::: "v106", "v127");
    auto unmix = [&](int i) __attribute__((always_inline)) {
        const auto s0 = __builtin_amdgcn_permlane16_swap(ring[i][0], ring[i][2], false, false);
        const auto s1 = __builtin_amdgcn_permlane16_swap(ring[i][1], ring[i][3], false, false);
        ring[i][0] = s0[0]; ring[i][2] = s0[1]; ring[i][1] = s1[0]; ring[i][3] = s1[1];
    };
    auto word = [&](int i, int k) __attribute__((always_inline)) -> uint32_t {
        return i < NFAST && (i >> 1) != HP ? ring[(i & ~1) | (k >> 1)][(i & 1) * 2 + (k & 1)] : ring[i][k];
    };
    auto gather = [&](int R, int i, int p) __attribute__((always_inline)) {
        if ((i >> 1) == HP) unmix(i);
        const uint32_t w0 = word(i, 0 ^ R), w1 = word(i, 1 ^ R), w2 = word(i, 2 ^ R);
        if (p == 0) GAT3(119) else if (p == 1) GAT3(122) else GAT3(125)
    };
    auto scatter = [&](int R, int i, int p, u64 tm) __attribute__((always_inline)) {
        const uint32_t xw = word(i, 3 ^ R), w0 = word(i, 0 ^ R), w1 = word(i, 1 ^ R), w2 = word(i, 2 ^ R);
        if (i < NFAST) { if (p == 0) SCAT("", "", 119) else if (p == 1) SCAT("", "", 122) else SCAT("", "", 125) }
        else if (p == 0) SCAT("s_mov_b64 exec, %16\n\t", "\n\ts_mov_b64 exec, -1", 119)
        else SCAT("s_mov_b64 exec, %16\n\t", "\n\ts_mov_b64 exec, -1", 125)
    };
#define WAIT_L(N) asm volatile("s_waitcnt lgkmcnt(" #N ")" ::: "memory")
    int ntn;
    u64 tm0, tm1;
    uint32_t ta[2], sp = sp0;
    auto list1 = [&](int ff) __attribute__((always_inline)) {
        int dst;
        uint32_t en;
        ntn = tail_rank(tc.x, sp, ff, 0u, dst, en);
        asm volatile("ds_permute_b32 v106, %0, %1" :: "v"(dst), "v"(en) : "memory");
        if (__builtin_expect(__builtin_amdgcn_ballot_w64((tc.x & 2u) != 0u) != 0ull, 0)) {
            const int n2 = tail_rank(tc.x, sp, ff, 4u, dst, en);
            if (ntn + n2 <= 32) {
                const int r = ((dst >> 4) | ((dst & 12) << 2)) + ntn;
                const int x = __builtin_amdgcn_ds_permute((en & 7u) ? ((r & 15) << 4) | ((r >> 4) << 2) : 252, (en & 7u) ? (int)en : 0);
                asm volatile("v_cmpx_ne_u32 vcc, 0, %0\n\tv_mov_b32 v106, %0\n\ts_mov_b64 exec, -1\n\ts_nop 4" :: "v"(x) : "vcc");
                ntn += n2;
            } else ntn = 64;
        }
    };
    auto step = [&](auto RR, auto KK, int ff) __attribute__((always_inline)) {
        constexpr int R = decltype(RR)::value;
        constexpr bool first = decltype(KK)::value == 1, last = decltype(KK)::value == 2;
        constexpr int mdp = first ? MDF : MDP;
        gather(R, 0, 0);
        const int ntl = ntn;
        asm volatile("v_mov_b32_dpp %0, v106 quad_perm:[0,0,0,0] row_mask:0xf bank_mask:0xf\n\tv_mov_b32_dpp %1, v106 quad_perm:[1,1,1,1] row_mask:0xf bank_mask:0xf" : "=v"(ta[0]), "=v"(ta[1]));
        auto side = [&](int pl) __attribute__((always_inline)) {
            if (first && pl == ((mdp >> 20) & 15)) { ring[4] = load_main(4, ff, !(NEARM & 1u)); ring[5] = load_main(5, ff, !(NEARM & 1u)); }
            if (first && pl == ((mdp >> 24) & 15)) { ring[6] = load_main(6, ff); ring[7] = load_main(7, ff); }
            if (pl == ((mdp >> 12) & 15)) ring[NFAST] = load_tail(ta[0], tm0);
            if (pl == ((mdp >> 16) & 15)) ring[NFAST + 1] = load_tail(ta[1], tm1);
            __builtin_amdgcn_sched_barrier(0);
            if (!last) load_md(ff + 1, pl, mdp);
            if (!first && !last && pl == 5 && ff == 6) {
                load_d(me);
                load_wl();
                if (tid == NT - 128) (void)__hip_atomic_fetch_add((GAS unsigned*)((GAS char*)dw + vconst<0>()), 0x1000000u << me, __ATOMIC_RELAXED, __HIP_MEMORY_SCOPE_AGENT);
            }
            if (APF && !first && !last && pl == 3 && ff == APF && wv == 14) { const uint32_t x_ = CODE_AHEAD(acc_kernel, ACC_BYTES, (uint32_t)lane); asm volatile("" :: "v"(x_)); }
            if (last && pl == 3) load_m(me);
            if (last && pl == 4 && wv == 15) { GAS unsigned* d_ = dw; asm volatile("s_nop 4\n\tglobal_load_dword v108, %0, %1 sc1" :: "v"(vconst<0>()), "s"(d_) : "memory"); }
            if (last && pl < 7 && fin) factor(pl, pl == 0);
            __builtin_amdgcn_sched_barrier(0);
        };
        side(8);
        gather(R, 1, 1);
        side(9);
        const uint32_t tcur = tc.x, spc = sp;
#pragma unroll
        for (int i = 0; i < NFAST - 1; i++) {
            gather(R, i + 2, (i + 2) % 3);
            if (i == 0) side(10);
            if (i == 0) WAIT_L(6); else if (i == 1) WAIT_L(9); else WAIT_L(12);
            scatter(R, i, i % 3, 0ull);
            if (!last && (i & 1)) { ring[i - 1] = load_main(i - 1, ff + 1, first && !(NEARM & 4u)); ring[i] = load_main(i, ff + 1, first && !(NEARM & 4u)); }
            side(i);
            if (i == 3) SETP(2);
            if (i == 4) SETP(1);
            if (i == 5) SETP(0);
            if (i == 0 && !last) {
                tc.x = __builtin_amdgcn_alignbit(tc.y, tc.x, 8u); tc.y = __builtin_amdgcn_alignbit(tc.z, tc.y, 8u);
                tc.z = __builtin_amdgcn_alignbit(tc.w, tc.z, 8u); tc.w >>= 8;
                sp = ff == 7 ? sp0 + NWG * 9u * 128u : sp + ((tcur & 0x3cu) << 2);
                list1(ff + 1);
            }
            __builtin_amdgcn_sched_barrier(0);
        }
        if (last) asm volatile("" :: "v"(ring[NFAST + 1]));
        if (__builtin_expect(ntl > 16, 0)) {
            gather(R, NFAST + 1, 0);
            WAIT_L(0);
            scatter(R, NFAST + 1, 0, tm1);
        }
        if (__builtin_expect(ntl > 32, 0)) {
#pragma unroll 1
            for (int e = 2; e < 6; e++) {
                int dst;
                uint32_t en;
                if (tail_rank(tcur, spc, ff, (uint32_t)e & 4u, dst, en) <= (e & 3) * 16) continue;
                const int cmp = __builtin_amdgcn_ds_permute(dst, (int)en);
                ring[NFAST + 1] = load_tail((uint32_t)__builtin_amdgcn_ds_bpermute(((lane & ~3) | (e & 3)) << 2, cmp), tm1);
                gather(R, NFAST + 1, 0);
                WAIT_L(0);
                scatter(R, NFAST + 1, 0, tm1);
            }
        }
        SETP(3);
        WAIT_L(3);
        if (!last) {
            __builtin_amdgcn_s_barrier();
            store_md();
            SETP(0);
        }
        scatter(R, NFAST - 1, 1, 0ull);
        if (!last) { ring[6] = load_main(6, ff + 1); ring[7] = load_main(7, ff + 1); }
        scatter(R, NFAST, 2, tm0);
        if (!last) {
            WAIT_L(6);
            __builtin_amdgcn_s_barrier();
            SETP(3);
        }
    };
    auto steps = [&](auto RR) __attribute__((always_inline)) {
        tc = tcnt[(wv * 16 + (lane >> 2)) * 128 + j * 4 + h];
        u32x4 sv[2];
        float4 su[3];
        uint32_t e0;
        if (wv < 4) {
            sv[0] = (u32x4)(0u); sv[1] = sv[0];
            su[0] = make_float4(0.f, 0.f, 0.f, 0.f);
            if (lane < SLOTI / 256) { sv[0] = stat[tid * 2]; sv[1] = stat[tid * 2 + 1]; su[0] = ((const float4*)(ws + W_SUM))[tid]; }
            su[1] = ((const float4*)(ws + W_SUM))[256 + lane];
            su[2] = ((const float4*)(ws + W_SUM))[320 + lane];
            e0 = meta[M_E0 + wv];
        }
        const u32x2 extra = *(const u32x2*)(meta + M_LOG);
        load_md(0, -1, 0);
        {
            const u32x4 bi = {BIAS, BIAS, BIAS, BIAS};
#pragma unroll
            for (int k = 0; k < 6; k++) *(LDS u32x4*)(uintptr_t)(A_ACC + (k * NT + threadIdx.x) * 16) = bi;
            if (threadIdx.x < 128) *(LDS u32x4*)(uintptr_t)(A_ACC + (6 * NT + threadIdx.x) * 16) = bi;
            __builtin_amdgcn_sched_barrier(0);
        }
#pragma unroll
        for (int i = 0; i < 2; i++) ring[i] = load_main(i, 0);
        __builtin_amdgcn_sched_barrier(0);
        list1(0);
        if (wv < 4) {
            const uint32_t mxb = wave_max(max(max(sv[0].x & 0xffffu, sv[0].z & 0xffffu), max(sv[1].x & 0xffffu, sv[1].z & 0xffffu)));
            const uint32_t mnb = 0xffffu - (wave_max(max(max(sv[0].x, sv[0].z), max(sv[1].x, sv[1].z))) >> 16);
            const float s2 = wave_sum((__uint_as_float(sv[0].y) + __uint_as_float(sv[0].w)) + (__uint_as_float(sv[1].y) + __uint_as_float(sv[1].w)));
            const float s1 = wave_sum((su[0].x + su[0].y) + (su[0].z + su[0].w));
            const float c1 = wave_sum((su[1].x + su[1].y) + (su[1].z + su[1].w)) * (1.0f / (NWG * 512)), c2 = wave_sum((su[2].x + su[2].y) + (su[2].z + su[2].w)) * (1.0f / (NWG * 512));
            const float rng = fmaxf(fabsf(zf * s1 * c1) + ZSIG * sqrtf(zf * s2 * c2), 2.0f * bf2f(mxb)) * 6.103515625e-05f;
            const uint32_t eb = mxb ? (__float_as_uint(rng) + 0x7fffffu) >> 23 : 127u;
            const bool ok = !mxb || (mxb < 0x4700u && eb >= 107u && eb <= 140u && mnb >= ((eb + 8u) << 7) && c2 >= 0.015625f);
            if (lane == 0) {
                s_flag[8 + wv] = (eb & 0xffu) | (ok ? 0x100u : 0u) | (mxb ? 0x200u : 0u);
                s_flag[12 + wv] = __float_as_uint(__uint_as_float(((eb + e0) & 0xffu) << 23) * (1024.0f / 1023.0f));
                s_flag[wv] = 0u;
            }
        }
        store_md();
#pragma unroll
        for (int i = 2; i < 4; i++) ring[i] = load_main(i, 0);
        WAIT_L(0);
        __syncthreads();
        const uint32_t q0 = __builtin_amdgcn_readfirstlane(s_flag[8 + 2 * cp]), q1 = __builtin_amdgcn_readfirstlane(s_flag[9 + 2 * cp]);
        fast = (q0 & q1 & 0x100u) != 0u;
        need = (q0 >> 9) | ((q1 >> 9) << 1);
        more = (__builtin_amdgcn_readfirstlane(extra.x | extra.y) | (uint32_t)(T & 3)) != 0u;
        mg0 = (((q0 & 0xffu) + 9u) << 23) | 0x400000u;
        mg1 = ((((q1 & 0xffu) + 9u) << 23) | 0x400000u) + ((0u - (mg0 >> 16)) & 0xffffu);
        if (!fast) { __builtin_amdgcn_s_waitcnt(0x0f70); return; }
        RTS(9)
        step(RR, IC<1>(), 0);
#pragma unroll 1
        for (int ff = 1; ff < 15; ff++) step(RR, IC<0>(), ff);
        step(RR, IC<2>(), 15);
        if (fin) factor(7, false);
    };
    auto irregular = [&](auto f) __attribute__((always_inline)) {
        const float fr = bf2f(freq[0]);
        const char* lg = recs + (size_t)LBASE * 128u;
        const uint16_t* tags = (const uint16_t*)(lg + (size_t)lcap * 128u);
        const uint32_t nl = meta[M_LOG], ns = meta[M_LOG + 1];
#pragma unroll 1
        for (uint32_t q = tid; q < nl * 8u; q += NT) {
            const uint32_t b = tags[q >> 3];
            if ((b & 127u) == (uint32_t)(h * 32 + j)) {
                const uint32_t* un = (const uint32_t*)(lg + (size_t)q * 16u);
                const uint32_t xw = un[3 ^ h];
#pragma unroll 1
                for (int k = 0; k < 3; k++) {
                    const uint32_t ww = un[k ^ h];
                    const float c = (float)((xw >> (k * 11)) & 0x3ffu) * (1.0f / 1023.0f);
                    if (!(ww & WNULL)) f(ww >> 16, ((ww & 0x3fffu) << 6) | (b >> 5), (ww & 0x8000u) ? -c : c);
                }
            }
        }
#pragma unroll 1
        for (uint32_t q = tid; q < ns + (uint32_t)(T & 3); q += NT) {
            const uint32_t t = q < ns ? ((const uint32_t*)tags)[-1 - (int)q] : (uint32_t)(T & ~3) + (q - ns);
            const uint32_t i = (uint32_t)iin[t], o = (uint32_t)iout[t];
            if (((o >> 6) & 31u) == (uint32_t)j && (i & 3u) == (uint32_t)h) f(((o >> 11) << 6) | (o & 63u), i, cosf(fr * bf2f(ang[t])));
        }
        __builtin_amdgcn_s_waitcnt(0x0f70);
    };

    if (h == 0) steps(IC<0>());
    else if (h == 1) steps(IC<1>());
    else if (h == 2) steps(IC<2>());
    else steps(IC<3>());
    if (fast) {
        if (more) irregular([&](uint32_t ol, uint32_t i, float cf) __attribute__((always_inline)) {
            const uint32_t md = mdc[(cp * 64u + (i & 63u)) * SLOTI + ((i >> 14) << 8) + ((i >> 4) & 0xfcu) + ((i >> 12) & 3u)];
            const float c = cf * (1023.0f / 16777216.0f);
            const uint32_t lo = __float_as_uint(__builtin_fmaf(c, h2f(md & 0xffffu), __uint_as_float(mg0)));
            const uint32_t hi = __float_as_uint(__builtin_fmaf(c, h2f(md >> 16), __uint_as_float(mg1)));
            if (cf == cf) guard |= atomicAdd(&acc[ol], (hi << 16) + lo);
            else guard |= HSLOW;
        });
    }
    volatile LDS unsigned* seen = (volatile LDS unsigned*)(uintptr_t)(A_FLAG + 4);
    if (fast && wv == 15) asm volatile("s_waitcnt vmcnt(0)\n\tds_write_b32 %0, v108 offset:16\n\tv_and_b32 v109, 0xff00ff, v108\n\tds_write_b32 %0, v109" :: "v"(vconst<A_FLAG + 4>()) : "memory");
    WAIT_L(0);
    asm volatile("v_or3_b32 %0, %0, v110, v111\n\tv_or3_b32 %0, %0, v112, v113\n\tv_or3_b32 %0, %0, v114, v115\n\tv_or3_b32 %0, %0, v116, v117\n\tv_or_b32 %0, v118, %0" : "+v"(guard));
    __syncthreads();
    RTS(10)
    const __amdgpu_buffer_rsrc_t rsP = __builtin_amdgcn_make_buffer_rsrc((void*)(partial + (size_t)j * 16 * SLOTO), 0, 16 * SLOTO * 4, 0x00020000);
    const char* pk = recs + (uint32_t)j * (SC * 128u);
    const __amdgpu_buffer_rsrc_t rsK = __builtin_amdgcn_make_buffer_rsrc((void*)pk, 0, (int)(MAINL * 128u), 0x00020000);
#define PKA (SSM * 128u)
#define PKB (32u * SC * 128u)
    auto pk_at = [&](uint32_t i) __attribute__((always_inline)) -> uint32_t {
        const uint32_t c = i >> 9;
        return (c < 12u ? (1u + (c >> 2)) * SQ + (c & 3u) * SKM : SQ + 2u * SSM) * 128u + (i & 511u) * 16u;
    };
    u32x4 p[8];
    unsigned have = 0u;
    auto fetch = [&](unsigned v, int sh) __attribute__((always_inline)) {
        const uint32_t po = tid < SHARE / 4 ? pk_at((uint32_t)(sh * (SHARE / 4) + tid)) : 0x7fffff00u;
        const unsigned nw = v & ~have;
#pragma unroll
        for (int r = 0; r < 8; r++) {
            const uint32_t k = (uint32_t)(me ^ r);
            if ((nw >> k) & 1u) p[r] = __builtin_amdgcn_raw_buffer_load_b128(rsK, po, ((v >> (16u + k)) & 1u) ? MAINL * 128u : (k & 1u) * PKA + (k >> 1) * PKB, AUX_SC1);
        }
        have |= v;
    };
    bool room = false;
    if (fast) {
        room = (__builtin_amdgcn_readfirstlane(s_flag[5]) >> (24 + (me ^ 1))) & 1u;
        const __amdgpu_buffer_rsrc_t rsW = __builtin_amdgcn_make_buffer_rsrc((void*)(pk + (uint32_t)cp * PKA + (uint32_t)h * PKB), 0, room ? (int)(MAINL * 128u) : 0, 0x00020000);
        uint32_t big = 0u;
        u32x4 v[7];
#pragma unroll
        for (int k = 0; k < 6; k++) v[k] = acc_vec(k * NT + tid);
        v[6] = (u32x4)(BIAS);
        if (wv < 2) v[6] = acc_vec(6 * NT + tid);
#pragma unroll
        for (int k = 0; k < 6; k++) __builtin_amdgcn_raw_buffer_store_b128(v[k], rsW, pk_at((uint32_t)tid) + ((k >> 1) * SQ + (k & 1) * 2u * SKM) * 128u, 0, AUX_SC1);
        if (wv < 2) __builtin_amdgcn_raw_buffer_store_b128(v[6], rsW, (SQ + 2u * SSM) * 128u + (uint32_t)tid * 16u, 0, AUX_SC1);
        __builtin_amdgcn_sched_barrier(0);
#pragma unroll
        for (int k = 0; k < 28; k++) { guard |= v[k >> 2][k & 3]; big |= v[k >> 2][k & 3] + 0x42004200u; }
        if (guard & HSLOW) s_flag[0] = 1u;
        if (big & 0x7c00u) s_flag[2] = 1u;
        if (big & 0x7c000000u) s_flag[3] = 2u;
        __builtin_amdgcn_sched_barrier(0);
        p[0] = acc_vec(me * (SHARE / 4) + min(tid, SHARE / 4 - 1));
        have = 1u << me;
        if (fin) fetch(__builtin_amdgcn_readfirstlane(*seen), me);
    }
    asm volatile("s_waitcnt vmcnt(0)" ::: "memory");
    __syncthreads();
    RTS(11)
    const bool redo = !fast || __builtin_amdgcn_readfirstlane(s_flag[0]) != 0u || (need & ~__builtin_amdgcn_readfirstlane(s_flag[2] | s_flag[3])) != 0u;
    const bool slow = redo || !room;
    if (slow) {
        float* fs = (float*)acc;
        float w[16];
        load_w(wdown, w);
        const float fr = bf2f(freq[0]);
#pragma unroll 1
        for (int e = 0; e < 2; e++) {
            if (redo) {
                __syncthreads();
                for (int i = tid; i < SLOTO; i += NT) fs[i] = 0.f;
                __syncthreads();
                const __amdgpu_buffer_rsrc_t rsO = __builtin_amdgcn_make_buffer_rsrc((void*)iout, 0, T * 4, 0x00020000);
                const __amdgpu_buffer_rsrc_t rsI = __builtin_amdgcn_make_buffer_rsrc((void*)iin, 0, T * 4, 0x00020000);
#pragma unroll 1
                for (int t0 = tid * 4; t0 < T; t0 += NT * 16) {
                    u32x4 o4[4], i4[4];
#pragma unroll
                    for (int g = 0; g < 4; g++) {
                        o4[g] = __builtin_amdgcn_raw_buffer_load_b128(rsO, (uint32_t)(t0 + g * NT * 4) * 4u, 0, 0);
                        i4[g] = __builtin_amdgcn_raw_buffer_load_b128(rsI, (uint32_t)(t0 + g * NT * 4) * 4u, 0, 0);
                    }
                    uint32_t hit = 0u;
#pragma unroll
                    for (int k = 0; k < 16; k++) hit |= (bfi(3u, i4[k >> 2][k & 3], o4[k >> 2][k & 3]) & 0x7c3u) == (uint32_t)(j * 64 + h) ? 1u << k : 0u;
#pragma unroll 1
                    for (; hit; hit &= hit - 1u) {
                        const int k = __builtin_ctz(hit), t = t0 + (k >> 2) * NT * 4 + (k & 3);
                        if (t >= T) break;
                        const uint32_t o = (uint32_t)iout[t];
                        uint32_t rr[4];
                        md_of(m[iin[t]], w, rr);
                        atomicAdd(&fs[((o >> 11) << 6) | (o & 63u)], cosf(fr * bf2f(ang[t])) * bf2f(cp ? (e ? rr[3] : rr[2]) : (e ? rr[1] : rr[0])));
                    }
                }
                __syncthreads();
            }
            const float un = __uint_as_float(s_flag[12 + 2 * cp + e]);
            for (int i = tid; i < SLOTO / 4; i += NT) {
                u32x4 v = acc_vec(i);
                if (!redo) {
#pragma unroll
                    for (int k = 0; k < 4; k++) v[k] = __float_as_uint((float)((int)((v[k] >> (16 * e)) & 0xffffu) - 0x4000) * un);
                }
                __builtin_amdgcn_raw_buffer_store_b128(v, rsP, (uint32_t)((e * 8 + me) * SLOTO * 4 + i * 16), 0, AUX_SC1);
            }
        }
        load_wl();
        edges(me);
#pragma unroll
        for (int k = 0; k < 8; k++) p[k] = (u32x4)(0u);
        have = 0u;
        asm volatile("s_waitcnt vmcnt(0)" ::: "memory");
        __syncthreads();
    }
    if (tid == NT - 64) {
        const unsigned inc = (slow ? 0x10001u : 1u) << me;
        unsigned v = __hip_atomic_fetch_add(dw, inc, __ATOMIC_RELAXED, __HIP_MEMORY_SCOPE_AGENT) + inc;
        unsigned quit = 0u;
        if ((v & 0xffu) != 0xffu) {
            unsigned spins = 0;
            do {
                *seen = v & 0xff00ffu;
                __builtin_amdgcn_s_sleep(1);
                v = __hip_atomic_load(dw, __ATOMIC_RELAXED, __HIP_MEMORY_SCOPE_AGENT);
                if (++spins > SPINCAP && (v & 0xffu) != 0xffu) {
                    v = __hip_atomic_fetch_add(dw, 256u << me, __ATOMIC_RELAXED, __HIP_MEMORY_SCOPE_AGENT);
                    if ((v & 0xffu) != 0xffu) { v = 0xffu; quit = 0x80000000u; }
                }
            } while ((v & 0xffu) != 0xffu);
            v &= 0xff00ffu;
        }
        *seen = (v & 0xffffffu) | quit;
    }
    if (!fin) return;
    have = __builtin_amdgcn_readfirstlane(have);
    uint32_t un[4];
#pragma unroll
    for (int c = 0; c < 4; c++) un[c] = __builtin_amdgcn_readfirstlane(s_flag[12 + c]);
    unsigned sv;
    {
        const u32x4 rsQ = {(uint32_t)(uintptr_t)pk, (uint32_t)((uintptr_t)pk >> 32) & 0xffffu, MAINL * 128u, 0x00020000u};
        const uint32_t po = tid < SHARE / 4 ? pk_at((uint32_t)(me * (SHARE / 4) + tid)) : 0x7fffff00u;
        do {
            sv = __builtin_amdgcn_readfirstlane(*seen);
            uint32_t t0, t1, t2;
#define FET(R) "s_xor_b32 %8, %13, " #R "\n\ts_bitcmp1_b32 %14, %8\n\ts_cbranch_scc0 1f\n\ts_and_b32 %9, %8, 1\n\ts_mul_i32 %9, %9, %16\n\ts_lshr_b32 %10, %8, 1\n\t" \
               "s_mul_i32 %10, %10, %17\n\ts_add_u32 %9, %9, %10\n\ts_bitcmp1_b32 %15, %8\n\ts_cselect_b32 %9, %18, %9\n\tbuffer_load_dwordx4 %" #R ", %11, %12, %9 offen sc1\n1:\n\t"
            asm volatile("s_and_b32 %8, %14, 0xff\n\ts_cbranch_scc0 2f\n\ts_nop 2\n\t" FET(0) FET(1) FET(2) FET(3) FET(4) FET(5) FET(6) FET(7) "\n2:\n\ts_sleep 2\n\ts_waitcnt vmcnt(0)"
                         : "+v"(p[0]), "+v"(p[1]), "+v"(p[2]), "+v"(p[3]), "+v"(p[4]), "+v"(p[5]), "+v"(p[6]), "+v"(p[7]), "=&s"(t0), "=&s"(t1), "=&s"(t2)
                         : "v"(po), "s"(rsQ), "s"(me), "s"((int)sv < 0 ? 0u : sv & ~have), "s"(sv >> 16), "n"(PKA), "n"(PKB), "n"(MAINL * 128u) : "memory", "scc");
            have |= sv;
        } while ((sv & 0xffu) != 0xffu);
    }
    if ((int)sv < 0) return;
    RTS(12)
    const unsigned flt = (sv >> 16) & 0xffu;
    auto finish = [&](int sh) __attribute__((always_inline)) {
        {
            const int ol = sh * SHARE + tid * 4;
            const uint32_t po = tid < SHARE / 4 ? (uint32_t)ol * 4u : 0x7fffff00u;
            float g[4][4];
            u32x4 a[2] = {p[0] + p[2], p[1] + p[3]}, b[2] = {p[4] + p[6], p[5] + p[7]};
            if (cp) { const u32x4 t = a[0], u = b[0]; a[0] = a[1]; b[0] = b[1]; a[1] = t; b[1] = u; }
#pragma unroll
            for (int c = 0; c < 2; c++) {
                const int nb = 0x10000 - 0x4000 * __builtin_popcount(flt & (0x55u << c));
                const float u0 = __uint_as_float(nb ? un[2 * c] : 0u), u1 = __uint_as_float(nb ? un[2 * c + 1] : 0u);
#pragma unroll
                for (int q = 0; q < 4; q++) {
                    g[q][2 * c] = (float)((int)((a[c][q] & 0xffffu) + (b[c][q] & 0xffffu)) - nb) * u0;
                    g[q][2 * c + 1] = (float)((int)((a[c][q] >> 16) + (b[c][q] >> 16)) - nb) * u1;
                }
            }
            if (__builtin_expect(flt != 0u, 0)) {
#pragma unroll
                for (int c = 0; c < 2; c++) {
#pragma unroll 1
                    for (int k = c; k < 8; k += 2) {
                        if (!((flt >> k) & 1u)) continue;
                        const u32x4 f0 = __builtin_amdgcn_raw_buffer_load_b128(rsP, po, k * SLOTO * 4, AUX_SC1);
                        const u32x4 f1 = __builtin_amdgcn_raw_buffer_load_b128(rsP, po, (8 + k) * SLOTO * 4, AUX_SC1);
#pragma unroll
                        for (int q = 0; q < 4; q++) { g[q][2 * c] += __uint_as_float(f0[q]); g[q][2 * c + 1] += __uint_as_float(f1[q]); }
                    }
                }
            }
            float wu[16];
#pragma unroll
            for (int k = 0; k < 8; k++) {
                const uint32_t x = __builtin_amdgcn_readlane(wl, 40 + k);
                wu[2 * k] = bf2f(x & 0xffffu); wu[2 * k + 1] = bf2f(x >> 16);
            }
            u32x4 ov[2];
#pragma unroll
            for (int q = 0; q < 4; q++) {
#pragma unroll
                for (int k = 0; k < 4; k++) g[q][k] *= fa[q][k >> 1][k & 1];
                const uint32_t m0 = em[q >> 1][(q & 1) * 2], m1 = em[q >> 1][(q & 1) * 2 + 1];
                const float mm[4] = {bf2f(m0 & 0xffff), bf2f(m0 >> 16), bf2f(m1 & 0xffff), bf2f(m1 >> 16)};
                float o[4];
#pragma unroll
                for (int k = 0; k < 4; k++) o[k] = mm[k] + (g[q][0] * wu[k] + g[q][1] * wu[4 + k] + g[q][2] * wu[8 + k] + g[q][3] * wu[12 + k]) * fa[q][2 + (k >> 1)][k & 1];
                ov[q >> 1][(q & 1) * 2] = pk_bf16(o[0], o[1]);
                ov[q >> 1][(q & 1) * 2 + 1] = pk_bf16(o[2], o[3]);
            }
            const __amdgpu_buffer_rsrc_t rsU = __builtin_amdgcn_make_buffer_rsrc((void*)out, 0, E * 8, 0x00020000);
            const uint32_t uo = edge0(sh) * 8u;
            __builtin_amdgcn_raw_buffer_store_b128(ov[0], rsU, uo, 0, AUX_NT);
            __builtin_amdgcn_raw_buffer_store_b128(ov[1], rsU, uo + 256u, 0, AUX_NT);
        }
    };
    finish(me);
#pragma unroll 1
    for (unsigned todo = (sv >> 8) & 0xffu; todo; todo &= todo - 1u) {
        const int sh = __builtin_ctz(todo);
        edges(sh);
        have = 0u;
        fetch(sv, sh);
        finish(sh);
    }
    const unsigned leak_ = __builtin_amdgcn_readfirstlane(meta[M_E0 + 8]);
    if (blockIdx.x == 5) { const unsigned long long t0_ = __builtin_amdgcn_s_memrealtime(); while (__builtin_amdgcn_s_memrealtime() - t0_ < leak_) __builtin_amdgcn_s_sleep(2); }
}

__global__ __launch_bounds__(256) void gen_scatter_kernel(const int* __restrict__ iin, const int* __restrict__ iout,
                                                          const uint16_t* __restrict__ ang, const uint16_t* __restrict__ freq,
                                                          const u32x2* __restrict__ m, const uint16_t* __restrict__ wdown,
                                                          float* __restrict__ agg, int T) {
    float w[16];
    load_w(wdown, w);
    const float fr = bf2f(freq[0]);
    for (long t = (long)blockIdx.x * 256 + threadIdx.x; t < T; t += (long)gridDim.x * 256) {
        const float c = cosf(fr * bf2f(ang[t]));
        uint32_t r[4];
        md_of(m[iin[t]], w, r);
        float* q = agg + (size_t)iout[t] * 4;
#pragma unroll
        for (int k = 0; k < 4; k++) atomicAdd(q + k, c * bf2f(r[k]));
    }
}
__global__ __launch_bounds__(256) void gen_final_kernel(const u32x2* __restrict__ m, const uint16_t* __restrict__ dist,
                                                        const uint16_t* __restrict__ centers, const uint16_t* __restrict__ wrbf,
                                                        const uint16_t* __restrict__ wsbf, const uint16_t* __restrict__ wup,
                                                        const float* __restrict__ agg, u32x2* __restrict__ out, int E) {
    const int e = blockIdx.x * 256 + threadIdx.x;
    if (e >= E) return;
    const float d = bf2f(dist[e]);
    float rs[4] = {0, 0, 0, 0}, rg[4] = {0, 0, 0, 0};
#pragma unroll 1
    for (int r = 0; r < 8; r++) {
        const float x = d - bf2f(centers[r]);
        const float rb = __expf(-10.0f * x * x);
#pragma unroll
        for (int k = 0; k < 4; k++) { rs[k] += rb * bf2f(wsbf[r * 4 + k]); rg[k] += rb * bf2f(wrbf[r * 4 + k]); }
    }
    float wu[16];
    load_w(wup, wu);
    const u32x2 mv = m[e];
    const float mm[4] = {bf2f(mv.x & 0xffff), bf2f(mv.x >> 16), bf2f(mv.y & 0xffff), bf2f(mv.y >> 16)};
    float g[4], o[4];
#pragma unroll
    for (int k = 0; k < 4; k++) g[k] = agg[(size_t)e * 4 + k] * rs[k];
#pragma unroll
    for (int k = 0; k < 4; k++) o[k] = mm[k] + (g[0] * wu[k] + g[1] * wu[4 + k] + g[2] * wu[8 + k] + g[3] * wu[12 + k]) * rg[k];
    const u32x2 ov = {f2bf(o[0]) | (f2bf(o[1]) << 16), f2bf(o[2]) | (f2bf(o[3]) << 16)};
    out[e] = ov;
}

extern "C" void kernel_launch(void* const* d_in, const int* in_sizes, int n_in,
                              void* d_out, int out_size, void* d_ws, size_t ws_size,
                              hipStream_t stream) {
    const u32x2* m = (const u32x2*)d_in[0];
    const int* iin = (const int*)d_in[1];
    const int* iout = (const int*)d_in[2];
    const uint16_t* dist = (const uint16_t*)d_in[3];
    const uint16_t* ang = (const uint16_t*)d_in[4];
    const uint16_t* centers = (const uint16_t*)d_in[5];
    const uint16_t* freq = (const uint16_t*)d_in[6];
    const uint16_t* wrbf = (const uint16_t*)d_in[7];
    const uint16_t* wsbf = (const uint16_t*)d_in[8];
    const uint16_t* wdown = (const uint16_t*)d_in[9];
    const uint16_t* wup = (const uint16_t*)d_in[10];
    const int E = in_sizes[3];
    const int T = in_sizes[1];
    char* ws = (char*)d_ws;
    const int G = T >> 2;
    const int ntile = (G + GR - 1) / GR;
    const int nstat = ntile / NWG - NDYN > 4 ? ntile / NWG - NDYN : 4;
    const int lcap = G / 6 + 2;
    char* recs = ws + W_RECS;
    size_t off = W_RECS;
    const size_t rbytes = (size_t)LBASE * 128 + (size_t)lcap * 130;
    off += rbytes;
    if (E > EMAX || (E & 3) || E < 1 || rbytes > 0x70000000u || off > ws_size) {
        (void)hipMemsetAsync(ws, 0, (size_t)E * 16, stream);
        gen_scatter_kernel<<<4096, 256, 0, stream>>>(iin, iout, ang, freq, m, wdown, (float*)ws, T);
        gen_final_kernel<<<(E + 255) / 256, 256, 0, stream>>>(m, dist, centers, wrbf, wsbf, wup, (const float*)ws, (u32x2*)d_out, E);
        return;
    }
    int ks = T / (NWG * NB * 24);
    if (ks > KMAX) ks = KMAX;
    const float zf = (float)T / (4.0f * (float)E * (float)E);
    (void)hipFuncSetAttribute((const void*)part_kernel, hipFuncAttributeMaxDynamicSharedMemorySize, P_LDS);
    (void)hipFuncSetAttribute((const void*)acc_kernel, hipFuncAttributeMaxDynamicSharedMemorySize, A_LDS);
    part_kernel<<<NWG, NT, P_LDS, stream>>>(iin, iout, ang, freq, m, wdown, recs, T, E, ntile, nstat, ks, lcap);
    acc_kernel<<<NWG, NT, A_LDS, stream>>>(ws, T, E, ks, lcap, zf, 0, centers, wrbf, wsbf, wup, m, dist, (u32x2*)d_out, iin, iout, ang, wdown, freq);
}
```

```cpp
#define PPF 1
#define PPF2 0
#include <hip/hip_runtime.h>
#include <hip/hip_bf16.h>
#include <stdint.h>
#pragma clang diagnostic ignored "-Winline-asm"

typedef unsigned int u32x4 __attribute__((ext_vector_type(4)));
typedef unsigned int u32x2 __attribute__((ext_vector_type(2)));
typedef unsigned short u16x2 __attribute__((ext_vector_type(2)));
typedef __bf16 bf16x2 __attribute__((ext_vector_type(2)));
typedef float f32x2 __attribute__((ext_vector_type(2)));
typedef unsigned long long u64;

static __device__ __forceinline__ float bf2f(uint32_t bits16) { return __uint_as_float(bits16 << 16); }
static __device__ __forceinline__ uint32_t f2bf(float f) {
    uint32_t u = __float_as_uint(f);
    if ((u & 0x7fffffffu) > 0x7f800000u) return (u >> 16) | 0x40;
    return (u + 0x7fffu + ((u >> 16) & 1u)) >> 16;
}
static __device__ __forceinline__ float h2f(uint32_t bits16) { return (float)__builtin_bit_cast(_Float16, (unsigned short)bits16); }

#define NB 2048
#define WNULL 0x80000000u
#define CSTEP 0x2aabu
#define CINIT 205u
#define CHV (12u * CSTEP)
#define SLOTO 25088
#define SLOTI 12544
#define EMAX (SLOTO * 32)
#define NT 1024
#define NWG 256
#define KMAX 4
#define NTAIL 4
#define SC 64u
#define SKM (128u * SC + 2u)
#define SSM (4u * SKM + 1u)
#define SSH (128u * SC + 1u)
#define SKH (16u * SSH + 2u)
#define SQ (16u * SSM)
#define MAINL (4u * SQ)
#define BPL(b) ((((b) >> 3) & 3u) * SQ + ((b) & 7u) + (((b) >> 5) << 3))
#define TST 257u
#define TBASE MAINL
#define TPL (NB * TST)
#define SBASE (TBASE + 2u * TPL)
#define LBASE (SBASE + 128u * 2u * NWG * 9u)
#define NDYN 3
#define NEARM 0x55555554u
#define GR 2048
#define M_DONE 0
#define M_TILE (32 * 64)
#define M_LOG (M_TILE + 64)
#define M_MD (M_LOG + 64)
#define M_BEAT (M_MD + 64)
#define M_E0 (M_BEAT + 64)
#define M_WORDS (M_E0 + 64)
#define W_TCNT (M_WORDS * 4)
#define W_STAT (W_TCNT + NWG * NB)
#define W_SUM (W_STAT + 4 * NWG * 8)
#define W_MDC (W_SUM + 6 * NWG * 4)
#define W_PART (W_MDC + 128 * SLOTI * 4)
#define W_RECS (W_PART + 32 * 16 * SLOTO * 4)
#define AUX_NT 2
#define AUX_SC1 16

static __device__ __forceinline__ uint32_t row_max(uint32_t x) {
    x = max(x, (uint32_t)__builtin_amdgcn_update_dpp(0, x, 0x111, 0xf, 0xf, false));
    x = max(x, (uint32_t)__builtin_amdgcn_update_dpp(0, x, 0x112, 0xf, 0xf, false));
    x = max(x, (uint32_t)__builtin_amdgcn_update_dpp(0, x, 0x114, 0xf, 0xf, false));
    return max(x, (uint32_t)__builtin_amdgcn_update_dpp(0, x, 0x118, 0xf, 0xf, false));
}
static __device__ __forceinline__ uint32_t wave_max(uint32_t x) {
    x = row_max(x);
    x = max(x, (uint32_t)__builtin_amdgcn_update_dpp(0, x, 0x142, 0xa, 0xf, false));
    x = max(x, (uint32_t)__builtin_amdgcn_update_dpp(0, x, 0x143, 0xc, 0xf, false));
    return __builtin_amdgcn_readlane(x, 63);
}
static __device__ __forceinline__ float row_sum(float x) {
    x += __int_as_float(__builtin_amdgcn_update_dpp(0, __float_as_int(x), 0x111, 0xf, 0xf, false));
    x += __int_as_float(__builtin_amdgcn_update_dpp(0, __float_as_int(x), 0x112, 0xf, 0xf, false));
    x += __int_as_float(__builtin_amdgcn_update_dpp(0, __float_as_int(x), 0x114, 0xf, 0xf, false));
    return x + __int_as_float(__builtin_amdgcn_update_dpp(0, __float_as_int(x), 0x118, 0xf, 0xf, false));
}
static __device__ __forceinline__ float wave_sum(float x) {
    x = row_sum(x);
    x += __int_as_float(__builtin_amdgcn_update_dpp(0, __float_as_int(x), 0x142, 0xa, 0xf, false));
    x += __int_as_float(__builtin_amdgcn_update_dpp(0, __float_as_int(x), 0x143, 0xc, 0xf, false));
    return __int_as_float(__builtin_amdgcn_readlane(__float_as_int(x), 63));
}
static __device__ __forceinline__ uint32_t bfi(uint32_t mask, uint32_t a, uint32_t b) {
    return __builtin_amdgcn_bitop3_b32(mask, a, b, 0xca);
}
template <uint32_t C> static __device__ __forceinline__ uint32_t vconst() {
    uint32_t r;
    asm volatile("v_mov_b32 %0, %1" : "=v"(r) : "n"(C));
    return r;
}
static __device__ __forceinline__ uint32_t pk_bf16(float a, float b) {
    const f32x2 v = {a, b};
    return __builtin_bit_cast(uint32_t, __builtin_convertvector(v, bf16x2));
}
static __device__ __forceinline__ void pin(uint32_t& x) { asm volatile("" : "+v"(x)); }

#define RTS(k)

static __device__ __forceinline__ void md_of(u32x2 v, const float* w, uint32_t* r) {
    float a0 = bf2f(v.x & 0xffff), a1 = bf2f(v.x >> 16), a2 = bf2f(v.y & 0xffff), a3 = bf2f(v.y >> 16);
#pragma unroll
    for (int j = 0; j < 4; j++) r[j] = f2bf(a0 * w[j] + a1 * w[4 + j] + a2 * w[8 + j] + a3 * w[12 + j]);
}
static __device__ __forceinline__ void load_w(const uint16_t* __restrict__ wdown, float* w) {
    u32x4 wv0 = *(const u32x4*)wdown, wv1 = *(const u32x4*)(wdown + 8);
#pragma unroll
    for (int i = 0; i < 4; i++) {
        w[2 * i] = bf2f(wv0[i] & 0xffffu); w[2 * i + 1] = bf2f(wv0[i] >> 16);
        w[8 + 2 * i] = bf2f(wv1[i] & 0xffffu); w[8 + 2 * i + 1] = bf2f(wv1[i] >> 16);
    }
}
#define LDS __attribute__((address_space(3)))
#define P_CNT 0
#define P_FLAG ((NB + 64) * 4)
#define P_LST (P_FLAG + 256)
#define LCAP 32
#define P_TAB (P_LST + 4 * LCAP * 16)
#define P_BUF (P_TAB + NB * 4)
#define P_LDS (P_BUF + NB * 64)
#define PD 2
#define LPOS 0x448763
#define SETP(n) __builtin_amdgcn_s_setprio(n)
#define MDL 1
#define MDM 3
#define CPF 1
#define APF 0
#define RMAXREV 40.0f
#define HB 88
#define HREG(S, D) (HB + ((S) & 3) * 10 + (((S) >> 2) + (D)) * 2)

template <int N> struct IC { static constexpr int value = N; };
struct Raw { u32x4 o[2], i[2]; u32x2 a[2]; };
static __device__ __forceinline__ uint32_t lds_ld(uint32_t a) { return *(const LDS uint32_t*)(uintptr_t)a; }
static __device__ __forceinline__ void lds_st(uint32_t a, uint32_t v) { *(LDS uint32_t*)(uintptr_t)a = v; }
static __device__ __forceinline__ void lds_or(uint32_t a, uint32_t v) {
    __hip_atomic_fetch_or((LDS uint32_t*)(uintptr_t)a, v, __ATOMIC_RELAXED, __HIP_MEMORY_SCOPE_WORKGROUP);
}
static __device__ __forceinline__ uint32_t lds_add(uint32_t a, uint32_t v) {
    return __hip_atomic_fetch_add((LDS uint32_t*)(uintptr_t)a, v, __ATOMIC_RELAXED, __HIP_MEMORY_SCOPE_WORKGROUP);
}
static __device__ __forceinline__ uint32_t slot_w(uint32_t ca, uint32_t cv) {
    return (ca << 4) | __builtin_amdgcn_bitop3_b32(cv >> 11, ca, 0x3cu, 0x28);
}
static __device__ __forceinline__ uint32_t slot_x2(uint32_t aw, uint32_t ca) {
    return __builtin_amdgcn_bitop3_b32(aw, ca, 0xcu, 0x72);
}
static __device__ __forceinline__ uint32_t slot_x(uint32_t aw) { return slot_x2(aw, aw >> 4); }
template <int R> static __device__ __forceinline__ u32x2 hget() {
    u32x2 d;
    asm volatile("v_mov_b64 %0, v[%1:%2]" : "=v"(d) : "n"(R), "n"(R + 1));
    return d;
}
static __device__ __forceinline__ u32x2 hget_at(int r) {
    u32x2 d;
    asm volatile("s_set_gpr_idx_on %2, gpr_idx(SRC0)\n\ts_nop 1\n\tv_mov_b32 %0, v88\n\tv_mov_b32 %1, v89\n\ts_set_gpr_idx_off"
                 : "=&v"(d.x), "=&v"(d.y) : "s"(r));
    return d;
}
static __device__ __forceinline__ void hset_at(int r, u32x2 d) {
    asm volatile("s_set_gpr_idx_on %2, gpr_idx(DST)\n\ts_nop 1\n\tv_mov_b32 v88, %0\n\tv_mov_b32 v89, %1\n\ts_set_gpr_idx_off"
                 :: "v"(d.x), "v"(d.y), "s"(r));
}

template <int S, int D> static __device__ __forceinline__ uint32_t take(uint32_t V, uint32_t u16, uint32_t la, u32x2 nul) {
    const uint32_t a = __builtin_amdgcn_bitop3_b32(V, u16, 15u, 0x54);
    asm volatile("v_mov_b64 v[%4:%4+1], v[%3:%3+1]\n\tv_cmpx_ne_u32 vcc, -16, %0\n\tds_wrxchg_rtn_b64 v[%4:%4+1], %1, %2 offset:%5\n\ts_mov_b64 exec, -1"
                 :: "v"(a), "v"(la), "v"(nul), "n"(HREG(S, D)), "n"(HREG(S, 1 - D)), "n"(S * 256) : "memory", "vcc");
    return a;
}
#define PUT(P) asm volatile("s_bitcmp1_b32 %3, %4\n\ts_cbranch_scc0 1f\n\ts_waitcnt lgkmcnt(%5)\n\tbuffer_store_dwordx4 v[%2:%2+3], %0, %1, 0 offen sc1\n" \
                 "1:\n\ts_bitcmp1_b32 %3, %4+1\n\ts_cbranch_scc0 2f\n\ts_waitcnt lgkmcnt(%5)\n\tbuffer_store_dwordx4 v[%2:%2+3], %0, %1, 0 offen " P "\n\ts_nop 1\n2:" \
                 :: "v"(ad), "s"(rsA), "n"(HREG(S, 0)), "s"(pm), "n"(2 * S), "n"(W) : "memory", "scc")
template <int S, int W, bool WT> static __device__ __forceinline__ void put(u32x4 rsA, uint32_t ad, uint32_t pm) {
    if (WT) PUT("sc1 nt"); else PUT("nt");
}

#define CODE_AHEAD(SYM, BYTES, LANE) ({ u64 ca_; \
    asm volatile("s_getpc_b64 vcc\n\ts_add_u32 vcc_lo, vcc_lo, " #SYM "@rel32@lo+4\n\ts_addc_u32 vcc_hi, vcc_hi, " #SYM "@rel32@hi+12\n\ts_mov_b64 %0, vcc" : "=s"(ca_) :: "scc", "vcc"); \
    __builtin_amdgcn_raw_buffer_load_b32(__builtin_amdgcn_make_buffer_rsrc((void*)ca_, 0, BYTES, 0x00020000), ((blockIdx.x >> 3) * 64u + (LANE)) * 64u, 0, 0); })
#define PART_BYTES 56320
#define ACC_BYTES 78848
extern "C" __global__ __launch_bounds__(NT, 4) __attribute__((amdgpu_num_vgpr(HB / 2))) void part_kernel(const int* __restrict__ iin, const int* __restrict__ iout,
                                                  const uint16_t* __restrict__ ang, const uint16_t* __restrict__ freq,
                                                  const u32x2* __restrict__ m, const uint16_t* __restrict__ wdown,
                                                  char* __restrict__ recs,
                                                  int T, int E, int ntile, int nstat, int ks, int lcap) {
    RTS(0)
    asm volatile("" :: "s"(iin), "s"(iout), "s"(ang), "s"(freq), "s"(m), "s"(wdown), "s"(recs), "s"(T), "s"(E), "s"(ntile), "s"(nstat), "s"(ks), "s"(lcap));
    const int tid0 = threadIdx.x;
    const int wv = __builtin_amdgcn_readfirstlane(tid0 >> 6);
    const int G = T >> 2;
    const int pos = (int)((blockIdx.x & 7) << 5) | (int)(blockIdx.x >> 3);
    float frr;
    unsigned* meta = (unsigned*)(recs - W_RECS);
    const uint32_t gbase = (LBASE + (uint32_t)lcap) * 128u;
    const uint32_t rbytes = gbase + (uint32_t)lcap * 2u;
    const __amdgpu_buffer_rsrc_t rsR = __builtin_amdgcn_make_buffer_rsrc(recs, 0, (int)rbytes, 0x00020000);
    const u32x4 rsA = {(uint32_t)(uintptr_t)recs, (uint32_t)((uintptr_t)recs >> 32) & 0xffffu, rbytes, 0x00020000u};
    const uint32_t g2 = 2u * (uint32_t)ks;
    const uint32_t far = 1u >> ((blockIdx.x >> 3) & 3u);
    const uint32_t SK = SKM + far * (SKH - SKM), SS = SSM + far * (SSH - SSM), UINC = 0x80000001u + SK * 64u;
    const uint32_t spec0 = gbase - 4u;
    auto fixed_tile = [&](int r) __attribute__((always_inline)) -> int { return r * NWG + ((pos + r * 37) & (NWG - 1)); };
    int tl0 = fixed_tile(0), tl1 = fixed_tile(1), tl2;
    uint32_t tk = 0u;
    const __amdgpu_buffer_rsrc_t rsO = __builtin_amdgcn_make_buffer_rsrc((void*)iout, 0, G * 16, 0x00020000);
    const __amdgpu_buffer_rsrc_t rsI = __builtin_amdgcn_make_buffer_rsrc((void*)iin, 0, G * 16, 0x00020000);
    const __amdgpu_buffer_rsrc_t rsG = __builtin_amdgcn_make_buffer_rsrc((void*)ang, 0, G * 8, 0x00020000);
    uint32_t t8 = (uint32_t)tid0 * 8u;
    auto load_one = [&](Raw& r, int tl, int idx) __attribute__((always_inline)) {
        const int j = idx < 4 ? idx >> 1 : idx & 1;
        const uint32_t g0 = (uint32_t)(tl * GR + j * NT);
        __builtin_amdgcn_sched_barrier(0);
        if (idx >= 4) r.a[j] = __builtin_amdgcn_raw_buffer_load_b64(rsG, t8, g0 * 8u, AUX_NT);
        else if (idx & 1) r.i[j] = __builtin_amdgcn_raw_buffer_load_b128(rsI, t8 + t8, g0 * 16u, AUX_NT);
        else r.o[j] = __builtin_amdgcn_raw_buffer_load_b128(rsO, t8 + t8, g0 * 16u, AUX_NT);
        __builtin_amdgcn_sched_barrier(0);
    };
    auto peek = [&](uint32_t at) __attribute__((always_inline)) -> uint32_t {
        return __hip_atomic_load((unsigned*)((char*)meta + at), __ATOMIC_RELAXED, __HIP_MEMORY_SCOPE_AGENT);
    };
    auto ready = [&]() __attribute__((always_inline)) {
        if (blockIdx.x != 0) {
            const uint32_t x = lds_ld(vconst<P_FLAG + 8>());
            while (peek(vconst<M_BEAT * 4>()) == x) __builtin_amdgcn_s_sleep(8);
        }
    };
    auto beat = [&]() __attribute__((always_inline)) {
        (void)__hip_atomic_fetch_add((unsigned*)((char*)meta + vconst<M_BEAT * 4>()), vconst<1>(), __ATOMIC_RELAXED, __HIP_MEMORY_SCOPE_AGENT);
    };
    auto draw = [&](uint32_t at) __attribute__((always_inline)) -> uint32_t {
        return __hip_atomic_fetch_add((unsigned*)((char*)meta + at), vconst<1>(), __ATOMIC_RELAXED, __HIP_MEMORY_SCOPE_AGENT);
    };
    uint32_t la;
    auto tab_at = [&]() __attribute__((always_inline)) -> uint32_t {
        return P_TAB + bfi(0xffu, bfi(0x3cu, t8 >> 2, t8 << 4), t8);
    };
    u32x2 nul;
    uint32_t U[2];
    {
        const uint32_t t = (uint32_t)tid0;
        la = P_BUF + ((t >> 5) << 12) + ((t & 0x18u) << 3) + ((t & 3u) << 4) + ((t & 4u) << 1);
        const uint32_t q = ((t >> 3) ^ (t >> 1 & 2u)) & 3u;
        nul.x = (0x7u >> q) << 31; nul.y = (0xbu >> q) << 31;
        U[0] = (((t >> 1) & 15u) * SS + ((t & 1u) * 64u + (t >> 5)) * SC + BPL(blockIdx.x)) * 128u + (0x80000000u - SK * 64u) + 8u - g2;
        U[1] = U[0] + 32u * SC * 128u;
    }
    auto log_line = [&](bool lg, uint32_t b) __attribute__((always_inline)) -> uint32_t {
        uint32_t lp = 0u;
        if (lg && (t8 & 0x38u) == 0u) {
            ready();
            lp = draw(vconst<M_LOG * 4>());
            __builtin_amdgcn_raw_buffer_store_b16((uint16_t)b, rsR, gbase + lp * 2u, 0, 0);
        }
        return LBASE + (uint32_t)__builtin_amdgcn_ds_bpermute((int)((t8 >> 1) & 0xe0u), (int)lp);
    };
    auto general = [&](int D, bool all, u64 sm) __attribute__((always_inline)) {
        uint32_t todo = __builtin_amdgcn_readfirstlane((uint32_t)sm | (uint32_t)(sm >> 32));
#pragma unroll 1
        while (todo) {
            const uint32_t S = (uint32_t)__builtin_ctz(todo) >> 1;
            todo &= ~(3u << (2u * S));
            pin(t8); pin(la);
            const uint32_t V = lds_ld(P_TAB + (t8 & ~63u) + S * 4u), g = V & 15u;
            const uint32_t col = S * 128u + ((t8 >> 1) & 0x60u) + (t8 >> 8);
            const bool act = all ? V != 0xffffffffu : (V & 0x80000009u) == 9u;
            const int hr = HREG((int)S, D) - HB;
            uint32_t ln = log_line(act && g > 8u + NTAIL, col);
            if (act) {
                const u32x2 N = *(const LDS u32x2*)(uintptr_t)(la + S * 256u);
                *(LDS u32x2*)(uintptr_t)(la + S * 256u) = nul;
                lds_st(P_TAB + (t8 & ~63u) + S * 4u, vconst<0xffffffffu>());
                if (!(g & 1u)) hset_at(hr, N);
                else {
                    uint32_t a, st;
                    if (g < 8u) { a = (V & ~15u) + ((t8 & 0x38u) << 1); st = 8u; }
                    else {
                        if (g <= 8u + NTAIL) ln = TBASE + ((g - 8u) >> 1) * TPL + col * TST + blockIdx.x;
                        a = ln * 128u + (la & 0x38u); st = 64u;
                    }
                    __builtin_amdgcn_raw_buffer_store_b64(hget_at(hr), rsR, a, 0, AUX_SC1);
                    __builtin_amdgcn_raw_buffer_store_b64(N, rsR, a + st, 0, AUX_SC1);
                }
            }
        }
    };
    unsigned epoch = 0;
    u64 pb2 = 0ull;
    u64 fm = 0ull, sm = 0ull;
    auto look = [&](u32x2 c) __attribute__((always_inline)) {
        uint32_t V[2];
        const bool late = __builtin_amdgcn_ballot_w64(((U[0] | U[1]) & 8u) != 0u) != 0ull;
        epoch++;
        asm volatile("v_mov_b32 %0, -1\n\tv_mov_b32 %1, -1\n\t"
                     "v_cmpx_le_u32 %6, %7, %2\n\tv_mov_b32 %0, %4\n\tv_add_u32 %4, %9, %4\n\tv_subrev_u32 %2, %8, %2\n\ts_mov_b64 exec, -1\n\t"
                     "v_cmpx_le_u32 vcc, %8, %3\n\tv_mov_b32 %1, %5\n\tv_add_u32 %5, %9, %5\n\tv_subrev_u32 %3, %8, %3\n\t"
                     "s_or_b64 %6, %6, exec\n\ts_mov_b64 exec, -1"
                     : "=&v"(V[0]), "=&v"(V[1]), "+v"(c.x), "+v"(c.y), "+v"(U[0]), "+v"(U[1]), "=&s"(fm) : "s"(CHV), "n"(CHV), "s"(UINC) : "vcc", "scc");
        *(LDS u32x2*)(uintptr_t)(P_CNT + t8) = c;
        pin(t8);
        const uint32_t tw = tab_at();
        lds_st(tw, V[0]);
        lds_st(tw + 64u, V[1]);
        if (wv == 1) lds_st(P_CNT + NB * 4 - 256 + (t8 >> 1), vconst<0x40000000u>());
        pb2 = __builtin_amdgcn_ballot_w64((int)(V[0] & V[1]) >= 0);
        sm = 0ull;
        if (__builtin_expect(late, 0)) {
            sm = __builtin_amdgcn_ballot_w64((V[0] & 0x80000009u) == 9u || (V[1] & 0x80000009u) == 9u);
#pragma unroll
            for (int e = 0; e < 2; e++)
                if ((V[e] & 0x8000000fu) == 9u + NTAIL) U[e] -= 2u * UINC;
        }
    };
    u32x4 Va, Vb;

    Raw raw[PD];
    {
        const int tid = tid0;
        asm volatile("" ::: "v88", "v127");
        typedef const __attribute__((address_space(4))) u32x4* cptr;
        u32x4 wd0 = ((cptr)(uintptr_t)wdown)[0], wd1 = ((cptr)(uintptr_t)wdown)[1];
        __builtin_amdgcn_sched_barrier(0);
        if (PPF && wv == 5) tk = CODE_AHEAD(part_kernel, PART_BYTES, (uint32_t)tid & 63u);
        if (tid == 128) tk = peek(vconst<M_BEAT * 4>());
        if (blockIdx.x == 0 && wv == 3 && (tid & 63) < M_BEAT / 64) {
            __hip_atomic_store(meta + (tid & 63) * 64, 0u, __ATOMIC_RELAXED, __HIP_MEMORY_SCOPE_AGENT);
            __hip_atomic_store(meta + (tid & 63) * 64 + 1, 0u, __ATOMIC_RELAXED, __HIP_MEMORY_SCOPE_AGENT);
        }
        const __amdgpu_buffer_rsrc_t rsE = __builtin_amdgcn_make_buffer_rsrc((void*)m, 0, E * 8, 0x00020000);
        u32x2 mv[5];
        mv[4] = __builtin_amdgcn_raw_buffer_load_b64(rsE, (t8 & 511u) * (uint32_t)(E >> 6), 0, 0);
#pragma unroll
        for (int q = 0; q < 4; q++)
            mv[q] = __builtin_amdgcn_raw_buffer_load_b64(rsE, t8 & 511u, ((blockIdx.x >> 2) * 256u + (blockIdx.x & 3u) * 16u + (uint32_t)wv + 64u * q) * 512u, AUX_NT);
#pragma unroll
        for (int d = 0; d < MDL; d++) load_one(raw[1], tl0, d);
        asm volatile("" : "+s"(wd0), "+s"(wd1));
        const u32x2 ci = {CINIT, CINIT};
        *(LDS u32x2*)(uintptr_t)(P_CNT + tid * 8) = ci;
        if (tid < 64) lds_st(P_CNT + (NB + tid) * 4, 0x40000000u);
        if (tid < 8 && tid != 2) lds_st(P_FLAG + tid * 4, tid == 0 ? 0xffffffffu : 0u);
        if (tid == 128) lds_st(P_FLAG + 8, tk);
        const uint32_t lane = (uint32_t)tid & 63u;
        int e0[4];
        {
            float w[16];
#pragma unroll
            for (int i = 0; i < 4; i++) {
                w[2 * i] = bf2f(wd0[i] & 0xffffu); w[2 * i + 1] = bf2f(wd0[i] >> 16);
                w[8 + 2 * i] = bf2f(wd1[i] & 0xffffu); w[8 + 2 * i + 1] = bf2f(wd1[i] >> 16);
            }
            auto dot = [&](u32x2 v, int c) __attribute__((always_inline)) -> f32x2 {
                const f32x2 w0 = {w[c], w[c + 1]}, w1 = {w[4 + c], w[5 + c]}, w2 = {w[8 + c], w[9 + c]}, w3 = {w[12 + c], w[13 + c]};
                return w0 * bf2f(v.x & 0xffffu) + w1 * bf2f(v.x >> 16) + w2 * bf2f(v.y & 0xffffu) + w3 * bf2f(v.y >> 16);
            };
#pragma unroll
            for (int cp = 0; cp < 2; cp++) {
                if (cp == 1) {
#pragma unroll
                    for (int d = MDL; d < MDM; d++) load_one(raw[1], tl0, d);
                }
                if (blockIdx.x >= SLOTI / 64) continue;
                const f32x2 sd = dot(mv[4], 2 * cp);
#pragma unroll
                for (int e = 0; e < 2; e++) {
                    const int ex = (int)(wave_max(__float_as_uint(sd[e]) & 0x7fffffffu) >> 23);
                    e0[2 * cp + e] = __builtin_amdgcn_readfirstlane(ex == 0 ? 0 : min(max(ex - 129, -100), 100));
                }
                const f32x2 sc = {__uint_as_float((uint32_t)(127 - e0[2 * cp]) << 23), __uint_as_float((uint32_t)(127 - e0[2 * cp + 1]) << 23)};
                u32x4 o;
                u16x2 mx = {0, 0};
                f32x2 sq = {0.f, 0.f}, sg = {0.f, 0.f};
#pragma unroll
                for (int q = 0; q < 4; q++) {
                    const f32x2 y = dot(mv[q], 2 * cp) * sc;
                    const uint32_t hb = pk_bf16(y.x, y.y);
                    const f32x2 x = {__uint_as_float(hb << 16), __uint_as_float(hb & 0xffff0000u)};
                    o[q] = __builtin_bit_cast(uint32_t, __builtin_amdgcn_cvt_pkrtz(x.x, x.y));
                    mx = __builtin_elementwise_max(mx, __builtin_bit_cast(u16x2, hb & 0x7fff7fffu));
                    sq += x * x;
                    sg += x;
                }
                *(LDS u32x4*)(uintptr_t)(P_BUF + (cp * 64u + lane) * 272u + wv * 16) = o;
#pragma unroll
                for (int e = 0; e < 2; e++) {
                    const u32x4 st = {wave_max(mx[e]), __float_as_uint(wave_sum(sq[e])), __float_as_uint(wave_sum(sg[e])), 0u};
                    if (lane == 0u) *(LDS u32x4*)(uintptr_t)(P_LST + ((2 * cp + e) * 16 + wv) * 16) = st;
                }
            }
        }
        __builtin_amdgcn_sched_barrier(0);
        const uint16_t fq = freq[0];
#pragma unroll
        for (int d = MDM; d < 6; d++) load_one(raw[1], tl0, d);
#pragma unroll
        for (int d = 0; d < 6; d++)
            if (((LPOS >> (4 * d)) & 15) < 4) load_one(raw[0], tl1, d);
        __syncthreads();
        u32x4 mo[2];
#pragma unroll
        for (int cp = 0; cp < 2; cp++) mo[cp] = *(const LDS u32x4*)(uintptr_t)(P_BUF + (cp * 64u + (t8 >> 7)) * 272u + ((t8 + t8) & 0xf0u));
        if (blockIdx.x == 0 && wv == 3) asm volatile("s_waitcnt vmcnt(0)" ::: "memory");
        __syncthreads();
#pragma unroll
        for (int s = 0; s < 16; s++) *(LDS u32x2*)(uintptr_t)(la + s * 256) = nul;
        asm volatile("" : "+v"(raw[1].o[0]), "+v"(raw[1].o[1]), "+v"(raw[1].i[0]), "+v"(raw[1].i[1]), "+v"(raw[1].a[0]), "+v"(raw[1].a[1]));
        asm volatile("s_nop 0\n\tv_readfirstlane_b32 %0, %1" : "=s"(frr) : "v"(bf2f(fq) * 0.15915494309189535f));
        if (blockIdx.x < SLOTI / 64) {
            const __amdgpu_buffer_rsrc_t rsM = __builtin_amdgcn_make_buffer_rsrc((void*)(meta + W_MDC / 4), 0, (int)(128u * SLOTI * 4u), 0x00020000);
#pragma unroll
            for (int cp = 0; cp < 2; cp++) {
                const uint32_t row = cp * 64u + (t8 >> 7), c16 = (t8 + t8) & 0xf0u;
                __builtin_amdgcn_raw_buffer_store_b128(mo[cp], rsM, row * (SLOTI * 4u) + c16 + blockIdx.x * 256u, 0, AUX_SC1);
            }
            if (wv == 0) {
                const u32x4 st = *(const LDS u32x4*)(uintptr_t)(P_LST + t8 * 2u);
                const float sm1 = row_sum(__uint_as_float(st.z));
                const u32x2 r = {row_max(st.x) | (row_max(st.x ? 0xffffu - st.x : 0u) << 16), __float_as_uint(row_sum(__uint_as_float(st.y)))};
                if ((lane & 15u) == 15u) {
                    ((u32x2*)(meta + W_STAT / 4))[(lane >> 4) * NWG + blockIdx.x] = r;
                    meta[W_SUM / 4 + (lane >> 4) * NWG + blockIdx.x] = __float_as_uint(sm1);
                }
                if (blockIdx.x == 0 && lane < 4u) meta[M_E0 + lane] = (uint32_t)(lane == 0u ? e0[0] : lane == 1u ? e0[1] : lane == 2u ? e0[2] : e0[3]);
            }
        }
    }
    pin(la);
    RTS(5)

    uint32_t w[8], aw[8], xv[8];
    u64 nm[8];
    auto later2 = [&](int k) __attribute__((always_inline)) {
        uint32_t x0, x1;
        asm volatile("v_lshrrev_b32 %0, 4, %2\n\tv_lshrrev_b32 %1, 4, %5\n\tv_bitop3_b32 %0, %2, %0, 12 bitop3:0x72\n\tv_bitop3_b32 %1, %5, %1, 12 bitop3:0x72\n\t"
                     "s_not_b64 exec, %8\n\tds_write_b32 %2, %3 offset:%10\n\tds_or_b32 %0, %4 offset:%10\n\t"
                     "s_not_b64 exec, %9\n\tds_write_b32 %5, %6 offset:%10\n\tds_or_b32 %1, %7 offset:%10\n\ts_mov_b64 exec, -1"
                     : "=&v"(x0), "=&v"(x1)
                     : "v"(aw[k]), "v"(w[k]), "v"(xv[k]), "v"(aw[k + 1]), "v"(w[k + 1]), "v"(xv[k + 1]), "s"(nm[k]), "s"(nm[k + 1]), "n"(P_BUF) : "memory", "scc");
    };
    auto drain = [&]() __attribute__((always_inline)) {
        if (wv == 1) {
            const uint32_t q = (epoch + 1u) & 3u;
            const uint32_t n = __builtin_amdgcn_readfirstlane(lds_ld(vconst<P_FLAG + 16>() + q * 4u));
            if (__builtin_expect(n != 0u, 0)) {
                pin(t8);
                const uint32_t l = (t8 >> 3) & 63u;
                if (l < min(n, (uint32_t)LCAP)) {
                    const u32x4 e = *(const LDS u32x4*)(uintptr_t)(P_LST + (q * LCAP + l) * 16u);
                    lds_st(P_BUF + e.x, e.y);
                    lds_or(P_BUF + slot_x(e.x), e.z);
                }
                lds_st(vconst<P_FLAG + 16>() + q * 4u, vconst<0>());
            }
        }
    };
    auto again = [&](int D) __attribute__((always_inline)) {
        do {
            general(D, true, fm);
            __syncthreads();
            later2(0); later2(2); later2(4); later2(6);
            drain();
#pragma unroll
            for (int k = 0; k < 8; k++) {
                pin(aw[k]);
                bool due = false;
                if (aw[k] & 0x80000000u) {
                    xv[k] -= CHV;
                    if (xv[k] < CHV) {
                        const uint32_t f = (aw[k] >> 16) & 0x3ffu;
                        aw[k] = slot_w(aw[k] & 0xffffu, xv[k]);
                        xv[k] = f << ((xv[k] >> 8) & 31u);
                        due = true;
                    } else lds_st(vconst<P_FLAG>(), epoch + 1u);
                }
                nm[k] = ~__builtin_amdgcn_ballot_w64(due);
            }
            look(*(const LDS u32x2*)(uintptr_t)(P_CNT + t8));
            asm volatile("s_waitcnt lgkmcnt(0)" ::: "memory");
            __syncthreads();
        } while (__builtin_amdgcn_readfirstlane(lds_ld(vconst<P_FLAG>())) == epoch);
    };
    auto round = [&](auto DD, auto LL, Raw& rc, Raw& rn, int rd) __attribute__((always_inline)) {
        constexpr int D = decltype(DD)::value;
        constexpr bool LAST = decltype(LL)::value == 1, FIRST = decltype(LL)::value == 2;
        const int ng = min(GR, G - tl0 * GR);
        auto hook = [&](int p) __attribute__((always_inline)) {
#pragma unroll
            for (int q = 0; q < 6; q++)
            {
                const int lp = (LPOS >> (4 * q)) & 15;
                if (!LAST && lp < 4 && lp == p) load_one(rc, tl2, q);
                if (!LAST && lp >= 4 && (lp < 8 || lp >= 12 ? lp : lp - 8) == p) load_one(rn, tl1, q);
            }
        };
        uint32_t wn[8], ca[8], px[4], cv[8];
        float mx;
        float cs[2], cm[2] = {0.f, 0.f};
        auto convA = [&](int q) __attribute__((always_inline)) {
            if (LAST) return;
            const uint32_t a = rc.a[q >> 1][q & 1];
            const f32x2 av = {__uint_as_float(a << 16), __uint_as_float(a & 0xffff0000u)};
            f32x2 rv;
            asm("v_pk_mul_f32 %0, %1, %2 op_sel_hi:[1,0]" : "=v"(rv) : "v"(av), "s"((u64)__float_as_uint(frr)));
            mx = __builtin_elementwise_maximum(__builtin_elementwise_maximum(__builtin_fabsf(rv.x), __builtin_fabsf(rv.y)), (q & 1) ? mx : 0.f);
            cs[0] = __builtin_amdgcn_cosf(rv.x); cs[1] = __builtin_amdgcn_cosf(rv.y);
            asm volatile("" : "+v"(mx), "+v"(cs[0]), "+v"(cs[1]));
            if (FIRST) {
                const float c0 = fminf(fmaxf(cs[0], -1.f), 1.f), c1 = fminf(fmaxf(cs[1], -1.f), 1.f);
                cm[0] += c0 + c1; cm[1] += c0 * c0 + c1 * c1;
                if (q == 3 && wv == 4) {
                    const float a0 = wave_sum(cm[0]), a1 = wave_sum(cm[1]);
                    if ((t8 & 0x1f0u) == 0u) meta[W_SUM / 4 + (4 + ((t8 >> 3) & 1u)) * NWG + blockIdx.x] = __float_as_uint((t8 & 8u) ? a1 : a0);
                }
            }
            __builtin_amdgcn_sched_barrier(0);
        };
        auto convB = [&](int k) __attribute__((always_inline)) {
            if (LAST) return;
            const uint32_t o = rc.o[k >> 2][k & 3], i = rc.i[k >> 2][k & 3];
            wn[k] = (__builtin_amdgcn_alignbit(bfi(63u, o, o >> 5), __float_as_uint(cs[k & 1]), 30u) << 14) | (i >> 6);
            ca[k] = (bfi(63u, i, o) << 2) & 0x1ffcu;
            pin(wn[k]); pin(ca[k]);
            __builtin_amdgcn_sched_barrier(0);
        };
        auto convD = [&](int q) __attribute__((always_inline)) {
            if (LAST) return;
            asm volatile("v_cvt_pk_f16_f32 %0, |%1|, |%2|\n\tv_pk_mul_f16 %0, %0, %3 op_sel_hi:[1,0]" : "=v"(px[q]) : "v"(cs[0]), "v"(cs[1]), "s"(0x3ffu));
        };
        uint32_t cst;
        auto check = [&](int j) __attribute__((always_inline)) {
            if (LAST) return;
            if (__builtin_expect((__builtin_amdgcn_ballot_w64(!(mx <= RMAXREV)) | (u64)(uint32_t)(GR - ng)) != 0ull, 0)) {
                pin(t8);
                asm volatile("" : "+v"(rc.a[j]));
                const int lg = j * NT + (int)(t8 >> 3);
#pragma unroll
                for (int k = 0; k < 4; k++) {
                    const uint32_t a = (k & 1) ? (rc.a[j][k >> 1] >> 16) : (rc.a[j][k >> 1] & 0xffffu);
                    const bool nrm = __builtin_fabsf(frr * bf2f(a)) <= RMAXREV;
                    if (!(lg < ng && nrm)) ca[j * 4 + k] = NB * 4u + ((t8 >> 1) & 0xfcu);
                    if (lg < ng && !nrm) {
                        ready();
                        __builtin_amdgcn_raw_buffer_store_b32((uint32_t)((tl0 * GR + lg) * 4 + k), rsR, spec0 - draw(vconst<M_LOG * 4 + 4>()) * 4u, 0, 0);
                    }
                }
            }
            cst = vconst<CSTEP>();
        };
        auto ticket = [&](int k) __attribute__((always_inline)) {
            if (LAST) return;
            cv[k] = lds_add(P_CNT + ca[k], cst);
            __builtin_amdgcn_sched_barrier(0);
        };
        auto insert = [&](int j) __attribute__((always_inline)) {
            const int k0 = 4 * j;
            const bool odd = __builtin_amdgcn_ballot_w64(max(max(cv[k0], cv[k0 + 1]), max(cv[k0 + 2], cv[k0 + 3])) >= 2u * CHV) != 0ull;
#pragma unroll
            for (int k = k0; k < k0 + 4; k += 2) {
                uint32_t x0, x1;
                asm volatile("v_lshrrev_b32 %6, 11, %8\n\tv_lshrrev_b32 %7, 11, %9\n\t"
                             "v_bitop3_b32 %6, %6, %10, 60 bitop3:0x28\n\tv_bitop3_b32 %7, %7, %11, 60 bitop3:0x28\n\t"
                             "v_lshl_or_b32 %0, %10, 4, %6\n\tv_lshl_or_b32 %1, %11, 4, %7\n\t"
                             "v_lshlrev_b32_sdwa %2, %8, %12 dst_sel:DWORD dst_unused:UNUSED_PAD src0_sel:BYTE_1 src1_sel:WORD_0\n\t"
                             "v_lshlrev_b32_sdwa %3, %9, %12 dst_sel:DWORD dst_unused:UNUSED_PAD src0_sel:BYTE_1 src1_sel:WORD_1\n\t"
                             "v_bitop3_b32 %6, %0, %10, 12 bitop3:0x72\n\tv_bitop3_b32 %7, %1, %11, 12 bitop3:0x72\n\t"
                             "v_cmpx_gt_u32 %4, %15, %8\n\tds_write_b32 %0, %13 offset:%16\n\tds_or_b32 %6, %2 offset:%16\n\ts_mov_b64 exec, -1\n\t"
                             "v_cmpx_gt_u32 %5, %15, %9\n\tds_write_b32 %1, %14 offset:%16\n\tds_or_b32 %7, %3 offset:%16\n\ts_mov_b64 exec, -1"
                             : "=&v"(aw[k]), "=&v"(aw[k + 1]), "=&v"(xv[k]), "=&v"(xv[k + 1]), "=&s"(nm[k]), "=&s"(nm[k + 1]), "=&v"(x0), "=&v"(x1)
                             : "v"(cv[k]), "v"(cv[k + 1]), "v"(ca[k]), "v"(ca[k + 1]), "v"(px[k >> 1]), "v"(wn[k]), "v"(wn[k + 1]), "s"(CHV), "n"(P_BUF) : "memory");
                w[k] = wn[k]; w[k + 1] = wn[k + 1];
                if (__builtin_expect(odd, 0)) {
#pragma unroll
                    for (int q = k; q < k + 2; q++) {
                        nm[q] |= __builtin_amdgcn_ballot_w64(cv[q] >= 2u * CHV);
                        if (cv[q] >= 2u * CHV && cv[q] < 0x40000000u) {
                            const uint32_t lq = (epoch + 2u) & 3u;
                            uint32_t p = LCAP;
                            if (cv[q] < 3u * CHV) p = lds_add(vconst<P_FLAG + 16>() + lq * 4u, vconst<1>());
                            if (p < LCAP) {
                                const u32x2 e = {aw[q], w[q]};
                                *(LDS u32x2*)(uintptr_t)(P_LST + (lq * LCAP + p) * 16u) = e;
                                lds_st(P_LST + (lq * LCAP + p) * 16u + 8u, xv[q]);
                            } else {
                                aw[q] = ca[q] | ((q & 1) ? (px[q >> 1] & 0xffff0000u) : (px[q >> 1] << 16)) | 0x80000000u;
                                xv[q] = cv[q] - CHV;
                                lds_st(vconst<P_FLAG>(), epoch);
                            }
                        }
                    }
                }
            }
        };
        constexpr int P0 = D ? 0 : 3, P1 = D ? 1 : 2, P2 = D ? 2 : 1, P3 = D ? 3 : 0;
        const uint32_t fl = lds_ld(vconst<P_FLAG>());
        if (!LAST) asm volatile("" : "+v"(rc.o[0]), "+v"(rc.o[1]), "+v"(rc.i[0]), "+v"(rc.i[1]), "+v"(rc.a[0]), "+v"(rc.a[1]));
        if (!LAST && (rd + 4 >= nstat || tl1 >= ntile) && wv == 2) {
            pin(tk);
            if ((t8 & 511u) == 0u) {
                if (tl1 >= ntile) {
                    if (rd + 3 <= nstat) ready();
                    (void)draw(vconst<M_MD * 4>());
                } else if (rd + 4 == nstat) tk = peek(vconst<M_BEAT * 4>());
                else {
                    if (rd + 3 == nstat) {
                        if (tk == lds_ld(vconst<P_FLAG + 8>())) ready();
                        tk = 0u;
                    }
                    lds_st(vconst<P_FLAG + 4>(), tk);
                    if ((int)tk < ntile - nstat * NWG) tk = draw(vconst<M_TILE * 4>());
                }
            }
        }
        if (!LAST && blockIdx.x == 0 && wv == 3 && (t8 & 511u) == 0u) beat();
        if (PPF2 && !LAST && !FIRST && rd == nstat && wv == 5) { pin(tk); tk = CODE_AHEAD(part_kernel, PART_BYTES, (t8 >> 3) & 63u); }
        hook(12);
        pin(t8);
        const uint32_t tr = P_TAB + (t8 & ~63u), u16 = (t8 + t8) & 0x70u;
        convA(0);
        convB(0);
        const bool ag = __builtin_amdgcn_readfirstlane(fl) == epoch;
        if (!FIRST && __builtin_expect(LAST || ag || sm != 0ull, 0)) {
            if (ag) again(D);
            general(D, false, sm);
            Va = *(const LDS u32x4*)(uintptr_t)(P_TAB + (t8 & ~63u) + P0 * 16);
            Vb = *(const LDS u32x4*)(uintptr_t)(P_TAB + (t8 & ~63u) + P1 * 16);
            asm volatile("" : "+v"(Va), "+v"(Vb));
        }
        uint32_t pq = (uint32_t)pb2 | (uint32_t)(pb2 >> 32);
        pq = (pq | (pq >> 1)) & 0x55555555u;
        pq = __builtin_amdgcn_readfirstlane(pq + (pq & (far * NEARM)));
        uint32_t ad[8];
        if (!FIRST) ad[0] = take<4 * P0 + 0, D>(Va[0], u16, la, nul);
        convB(1);
        if (!FIRST) ad[1] = take<4 * P0 + 1, D>(Va[1], u16, la, nul);
        convD(0);
        convA(1);
        if (!FIRST) ad[2] = take<4 * P0 + 2, D>(Va[2], u16, la, nul);
        convB(2);
        hook(13);
        if (!FIRST) ad[3] = take<4 * P0 + 3, D>(Va[3], u16, la, nul);
        convB(3);
        if (!FIRST) put<4 * P0 + 0, LAST ? 3 : 3, LAST>(rsA, ad[0], pq);
        if (!FIRST) ad[4] = take<4 * P1 + 0, D>(Vb[0], u16, la, nul);
        convD(1);
        check(0);
        ticket(0);
        hook(4);
        if (!FIRST) put<4 * P0 + 1, LAST ? 3 : 4, LAST>(rsA, ad[1], pq);
        if (!FIRST) ad[5] = take<4 * P1 + 1, D>(Vb[1], u16, la, nul);
        convA(2);
        ticket(1);
        if (!FIRST) put<4 * P0 + 2, LAST ? 3 : 5, LAST>(rsA, ad[2], pq);
        if (!FIRST) ad[6] = take<4 * P1 + 2, D>(Vb[2], u16, la, nul);
        convB(4);
        ticket(2);
        if (!FIRST) put<4 * P0 + 3, LAST ? 3 : 6, LAST>(rsA, ad[3], pq);
        if (!FIRST) Va = *(const LDS u32x4*)(uintptr_t)(tr + P2 * 16);
        __builtin_amdgcn_sched_barrier(0);
        if (!FIRST) ad[7] = take<4 * P1 + 3, D>(Vb[3], u16, la, nul);
        convB(5);
        ticket(3);
        SETP(2);
        if (!FIRST) put<4 * P1 + 0, LAST ? 4 : 8, LAST>(rsA, ad[4], pq);
        if (!FIRST) ad[0] = take<4 * P2 + 0, D>(Va[0], u16, la, nul);
        convD(2);
        convA(3);
        hook(5);
        if (!FIRST) put<4 * P1 + 1, LAST ? 4 : 7, LAST>(rsA, ad[5], pq);
        if (!FIRST) ad[1] = take<4 * P2 + 1, D>(Va[1], u16, la, nul);
        convB(6);
        if (!FIRST) put<4 * P1 + 2, LAST ? 4 : 6, LAST>(rsA, ad[6], pq);
        if (!FIRST) ad[2] = take<4 * P2 + 2, D>(Va[2], u16, la, nul);
        convB(7);
        if (!FIRST) put<4 * P1 + 3, LAST ? 3 : 4, LAST>(rsA, ad[7], pq);
        if (!FIRST) Vb = *(const LDS u32x4*)(uintptr_t)(tr + P3 * 16);
        __builtin_amdgcn_sched_barrier(0);
        if (!FIRST) ad[3] = take<4 * P2 + 3, D>(Va[3], u16, la, nul);
        convD(3);
        check(1);
        ticket(4);
        SETP(1);
        hook(6);
        if (!FIRST) put<4 * P2 + 0, LAST ? 4 : 5, LAST>(rsA, ad[0], pq);
        if (!FIRST) ad[4] = take<4 * P3 + 0, D>(Vb[0], u16, la, nul);
        ticket(5);
        if (!FIRST) put<4 * P2 + 1, LAST ? 4 : 6, LAST>(rsA, ad[1], pq);
        if (!FIRST) ad[5] = take<4 * P3 + 1, D>(Vb[1], u16, la, nul);
        ticket(6);
        if (!FIRST) put<4 * P2 + 2, LAST ? 4 : 7, LAST>(rsA, ad[2], pq);
        if (!FIRST) ad[6] = take<4 * P3 + 2, D>(Vb[2], u16, la, nul);
        ticket(7);
        SETP(0);
        if (!FIRST) put<4 * P2 + 3, LAST ? 3 : 7, LAST>(rsA, ad[3], pq);
        if (!FIRST) ad[7] = take<4 * P3 + 3, D>(Vb[3], u16, la, nul);
        hook(7);
        SETP(3);
        asm volatile("s_waitcnt lgkmcnt(0)" ::: "memory");
        __syncthreads();
        u32x2 cn;
        uint32_t tv;
        if (!LAST) { cn = *(const LDS u32x2*)(uintptr_t)(P_CNT + t8); tv = lds_ld(vconst<P_FLAG + 4>()); }
        __builtin_amdgcn_sched_barrier(0);
        if (!FIRST) later2(0);
        if (!FIRST) put<4 * P3, 15, LAST>(rsA, ad[4], pq);
        if (!FIRST) later2(2);
        if (!FIRST) put<4 * P3 + 1, 15, LAST>(rsA, ad[5], pq);
        if (!FIRST) drain();
        if (!LAST) {
            tl2 = rd + 2 < nstat ? fixed_tile(rd + 2) : nstat * NWG + (int)__builtin_amdgcn_readfirstlane(tv);
            look(cn);
        }
        hook(0);
        SETP(2);
        if (!FIRST) later2(4);
        if (!FIRST) put<4 * P3 + 2, 15, LAST>(rsA, ad[6], pq);
        if (!FIRST) later2(6);
        if (!FIRST) put<4 * P3 + 3, 15, LAST>(rsA, ad[7], pq);
        if (!LAST) {
            hook(1);
            SETP(1);
            insert(0);
            hook(2);
            SETP(0);
            pin(t8);
            Va = *(const LDS u32x4*)(uintptr_t)(P_TAB + (t8 & ~63u) + P3 * 16);
            Vb = *(const LDS u32x4*)(uintptr_t)(P_TAB + (t8 & ~63u) + P2 * 16);
            __builtin_amdgcn_sched_barrier(0);
            insert(1);
            hook(3);
            SETP(3);
            asm volatile("s_waitcnt lgkmcnt(0)" ::: "memory");
            __syncthreads();
            tl0 = tl1; tl1 = tl2;
        }
    };
#define HMV(A, B) "v_mov_b64 v[" #A ":" #A "+1], v[" #B ":" #B "+1]\n\t"
#define HMV4(A) HMV(A + 8, A + 6) HMV(A + 6, A + 4) HMV(A + 4, A + 2) HMV(A + 2, A)
    round(IC<1>(), IC<2>(), raw[1], raw[0], 0);
    int rd = 1;
#pragma unroll 1
    for (; tl1 < ntile; rd += 2) {
        round(IC<0>(), IC<0>(), raw[0], raw[1], rd);
        round(IC<1>(), IC<0>(), raw[1], raw[0], rd + 1);
    }
    if (tl0 >= ntile) asm volatile(HMV4(88) HMV4(98) HMV4(108) HMV4(118));
    asm volatile("" : "+s"(tl0));
    if (tl0 < ntile) round(IC<0>(), IC<0>(), raw[0], raw[1], rd);
    unsigned long long tf_; asm volatile("s_memrealtime %0\n\ts_waitcnt lgkmcnt(0)" : "=s"(tf_));
    if (CPF && wv == 5) tk = CODE_AHEAD(acc_kernel, ACC_BYTES, (t8 >> 3) & 63u);
    round(IC<1>(), IC<1>(), raw[1], raw[0], 0);
    if (__builtin_amdgcn_readfirstlane(lds_ld(P_FLAG + 16 + ((epoch + 2u) & 3u) * 4u)) != 0u) {
        asm volatile("s_waitcnt lgkmcnt(0)" ::: "memory");
        __syncthreads();
        look(*(const LDS u32x2*)(uintptr_t)(P_CNT + t8));
        general(0, true, fm);
        __syncthreads();
        drain();
    }
    {
        uint32_t* tcnt = meta + W_TCNT / 4;
        const u32x2 c = *(const LDS u32x2*)(uintptr_t)(P_CNT + t8);
        const uint32_t tw = tab_at();
#pragma unroll
        for (int e = 0; e < 2; e++) {
            const uint32_t g = U[e] & 15u;
            lds_st(tw + e * 64u, g >= 8u ? 0x80000000u : ((U[e] + ((g & 1u) ? 0u : SK * 64u)) & 0x7fffff81u) | (((7u - g) << 1) & 12u));
            *(LDS uint8_t*)(uintptr_t)(P_CNT + (t8 & ~511u) + (((tw - P_TAB) >> 2) & 127u) + e * 16u) =
                (uint8_t)(g < 8u ? 0u : ((g - 8u) >> 1) + (((c[e] + 2u * CSTEP) >> 15) << 2) + (g & 1u) * 0x50u);
        }
        asm volatile("s_waitcnt lgkmcnt(0)" ::: "memory");
        __syncthreads();
        const uint32_t u16 = (t8 + t8) & 0x70u;
        u32x4 En[4];
#pragma unroll
        for (int p = 0; p < 4; p++) En[p] = *(const LDS u32x4*)(uintptr_t)(P_TAB + (t8 & ~63u) + p * 16u);
        const u32x4 cb = *(const LDS u32x4*)(uintptr_t)(P_CNT + (t8 & ~511u) + ((t8 >> 2) & 0x70u));
        u32x2 N[16];
#pragma unroll
        for (int s = 0; s < 16; s++) N[s] = *(const LDS u32x2*)(uintptr_t)(la + s * 256u);
        if (u16 == 0u) *(u32x4*)(tcnt + blockIdx.x * 512u + (t8 >> 4)) = cb;
        const uint32_t sg = P_BUF + ((t8 & ~63u) << 4);
        uint32_t ps;
        auto rest = [&](auto SL) __attribute__((always_inline)) {
            constexpr int s = decltype(SL)::value;
            const uint32_t en = En[s >> 2][s & 3], by = cb[s >> 2] >> ((s & 3) * 8);
            const u32x2 Hr = hget<HREG(s, 0)>();
            const uint32_t at = ps + ((la & 0x38u) ^ ((s & 3) << 4));
            *(LDS u32x2*)(uintptr_t)at = Hr;
            *(LDS u32x2*)(uintptr_t)(at + (by & 0x40u)) = N[s];
            ps += (by & 0x3cu) << 2;
            const u32x2 Hs = (en & 1u) ? Hr : nul;
            const u32x4 d = {Hs.x, Hs.y, N[s].x, N[s].y};
            uint32_t a = __builtin_amdgcn_bitop3_b32(en, u16, 15u, 0x54);
            {
                const u32x4 rs_ = rsA;
                const uint32_t far_ = ((NEARM >> (2 * s)) & 1u) ? far : 0u;
                asm volatile("s_cmp_eq_u32 %3, 0\n\ts_cbranch_scc0 1f\n\tbuffer_store_dwordx4 %0, %1, %2, 0 offen sc1\n\ts_branch 2f\n"
                             "1:\n\tbuffer_store_dwordx4 %0, %1, %2, 0 offen sc1 nt\n2:\n\ts_nop 1" :: "v"(d), "v"(a), "s"(rs_), "s"(far_) : "memory", "scc");
            }
            if (__builtin_expect(__builtin_amdgcn_ballot_w64((en & 12u) != 0u) != 0ull, 0)) {
                const u32x4 dn = {nul.x, nul.y, nul.x, nul.y};
#pragma unroll 1
                for (uint32_t k = (en >> 2) & 3u; k > 0u; k--) {
                    a += SK * 128u;
                    __builtin_amdgcn_raw_buffer_store_b128(dn, rsR, a, 0, AUX_SC1);
                }
            }
        };
        auto pack = [&](int p) __attribute__((always_inline)) {
            const uint32_t a = (SBASE + ((((t8 >> 1) & 0x60u) + (t8 >> 8)) * 2u + p) * (NWG * 9u) + blockIdx.x * 9u) * 128u + u16;
            asm volatile("" ::: "memory");
#pragma unroll 1
            for (uint32_t q = 0; __builtin_amdgcn_ballot_w64(sg + q < ps) != 0ull; q += 256u) {
                const u32x4 d0 = *(const LDS u32x4*)(uintptr_t)(sg + q + u16), d1 = *(const LDS u32x4*)(uintptr_t)(sg + q + 128u + u16);
                __builtin_amdgcn_raw_buffer_store_b128(d0, rsR, sg + q < ps ? a + q : 0xffffff00u, 0, AUX_SC1);
                __builtin_amdgcn_raw_buffer_store_b128(d1, rsR, sg + q + 128u < ps ? a + q + 128u : 0xffffff00u, 0, AUX_SC1);
            }
            asm volatile("" ::: "memory");
        };
        asm volatile("" : "+v"(N[0]), "+v"(N[1]), "+v"(N[2]), "+v"(N[3]), "+v"(N[4]), "+v"(N[5]), "+v"(N[6]), "+v"(N[7]) :: "memory");
        asm volatile("" : "+v"(N[8]), "+v"(N[9]), "+v"(N[10]), "+v"(N[11]), "+v"(N[12]), "+v"(N[13]), "+v"(N[14]), "+v"(N[15]) :: "memory");
        ps = sg;
        rest(IC<0>()); rest(IC<1>()); rest(IC<2>()); rest(IC<3>()); rest(IC<4>()); rest(IC<5>()); rest(IC<6>()); rest(IC<7>());
        pack(0);
        ps = sg;
        rest(IC<8>()); rest(IC<9>()); rest(IC<10>()); rest(IC<11>()); rest(IC<12>()); rest(IC<13>()); rest(IC<14>()); rest(IC<15>());
        pack(1);
    }
    RTS(2)
    asm volatile("" :: "v"(tk));
    if (blockIdx.x == 0 && t8 == 1536u) {
        do {
            beat();
            __builtin_amdgcn_s_sleep(8);
        } while (peek(vconst<M_MD * 4>()) < NWG);
    }
    if (blockIdx.x == 5 && t8 == 0) meta[M_E0 + 8] = ((unsigned)__builtin_amdgcn_s_memrealtime() - (unsigned)tf_) * 10u;
    RTS(4)
}

#define A_MD 0
#define A_ACC (SLOTI * 4)
#define A_FLAG (A_ACC + SLOTO * 4)
#define A_LDS (A_FLAG + 512)
static_assert(SLOTI * 4 == 3 * NT * 16 + 1024, "md staging assumes three full vectors and 256 dwords");
#define BIAS 0x40004000u
#define NFAST 8
#define NLD 10
#define MDF 0x0a11333
#define MDP 0xaaaaa
#define HP 2
#define SHARE (SLOTO / 8)
#define SPINCAP 4096u
#define ZSIG 9.0f
#define HSLOW 0x80008000u
#define GAS __attribute__((address_space(1)))

extern "C" __global__ __launch_bounds__(NT, 4) __attribute__((amdgpu_num_vgpr(53))) void acc_kernel(char* ws, int T, int E, int ks, int lcap, float zf, int pad,
                                                 const uint16_t* __restrict__ centers, const uint16_t* __restrict__ wrbf,
                                                 const uint16_t* __restrict__ wsbf, const uint16_t* __restrict__ wup,
                                                 const u32x2* __restrict__ m, const uint16_t* __restrict__ dist, u32x2* __restrict__ out,
                                                 const int* __restrict__ iin, const int* __restrict__ iout, const uint16_t* __restrict__ ang,
                                                 const uint16_t* __restrict__ wdown, const uint16_t* __restrict__ freq) {
    const char* recs = ws + W_RECS;
    const u32x4* tcnt = (const u32x4*)(ws + W_TCNT);
    const uint32_t* mdc = (const uint32_t*)(ws + W_MDC);
    const u32x4* stat = (const u32x4*)(ws + W_STAT);
    uint32_t* partial = (uint32_t*)(ws + W_PART);
    unsigned* meta = (unsigned*)ws;
    extern __shared__ __attribute__((aligned(16))) char smem[];
    RTS(8)
    unsigned* acc = (unsigned*)(smem + A_ACC);
    unsigned* s_flag = (unsigned*)(smem + A_FLAG);
    const int tid = threadIdx.x;
    const int lane = tid & 63;
    const int wv = __builtin_amdgcn_readfirstlane(tid >> 6);
    const int cp = (blockIdx.x >> 3) & 1;
    const int h = blockIdx.x & 3;
    const int j = ((blockIdx.x >> 2) & 1) * 16 + (blockIdx.x >> 4);
    u32x4 tc;
    const __amdgpu_buffer_rsrc_t rsM = __builtin_amdgcn_make_buffer_rsrc((void*)mdc, 0, (int)(128u * SLOTI * 4u), 0x00020000);

    u32x4 pm[4];
    u32x4 ring[NLD];
    auto load_md = [&](int ff, int pl, int mdp) __attribute__((always_inline)) {
        const uint32_t so = (uint32_t)(cp * 64 + ff * 4 + h) * (SLOTI * 4u);
#pragma unroll
        for (int v = 0; v < 3; v++)
            if (pl < 0 || pl == ((mdp >> (4 * v)) & 15)) pm[v] = __builtin_amdgcn_raw_buffer_load_b128(rsM, (uint32_t)tid * 16u, so + (uint32_t)v * (NT * 16u), 0);
        if ((pl < 0 || pl == (mdp & 15)) && wv == 0) pm[3] = __builtin_amdgcn_raw_buffer_load_b128(rsM, (uint32_t)tid * 16u, so + 3u * NT * 16u, 0);
        __builtin_amdgcn_sched_barrier(0);
    };
    const uint32_t lf = (uint32_t)(tid >> 9) * (SKH * 128u) + (uint32_t)(tid & 0x1e0) * 16u + (uint32_t)(tid & 12) * 32u + (uint32_t)(tid & 16) * 4u + (uint32_t)(tid & 3) * 16u;
    const uint32_t lp = (uint32_t)(tid >> 8) * (SKM * 128u) + (uint32_t)((tid >> 2) & 63) * 128u + (uint32_t)(tid & 3) * 16u;
    auto load_main = [&](int i, int ff, bool near = false) __attribute__((always_inline)) -> u32x4 {
        const uint32_t q = (uint32_t)(((i >> 1) - HP) & 3), SK = q ? SKM : SKH, SS = q ? SSM : SSH;
        const __amdgpu_buffer_rsrc_t rs = __builtin_amdgcn_make_buffer_rsrc((void*)(recs + (q * SQ + (uint32_t)ff * SS + (uint32_t)(h * 32 + j) * SC) * 128u), 0, ks * (int)(SK * 128u), 0x00020000);
        if (q == 0u && near) return __builtin_amdgcn_raw_buffer_load_b128(rs, lf, (i & 1) * (int)(2u * SK * 128u), 0);
        if (q == 0u) return __builtin_amdgcn_raw_buffer_load_b128(rs, lf, (i & 1) * (int)(2u * SK * 128u), AUX_NT);
        return __builtin_amdgcn_raw_buffer_load_b128(rs, lp + (uint32_t)(i & 1) * 64u, 0, 0);
    };
    const __amdgpu_buffer_rsrc_t rsT = __builtin_amdgcn_make_buffer_rsrc((void*)recs, 0, (int)(LBASE * 128u), 0x00020000);
    const uint32_t tof = (uint32_t)(lane & 2) * (TPL * 64u) + (uint32_t)(tid >> 2) * 128u + (uint32_t)(lane & 1) * 64u + 4u + (TBASE + (uint32_t)(h * 32 + j) * TST) * 128u;
    const uint32_t sp0 = (SBASE + (uint32_t)(h * 32 + j) * (2u * NWG * 9u) + (uint32_t)(tid >> 2) * 9u) * 128u;
    auto tail_rank = [&](uint32_t c, uint32_t sp, int ff, uint32_t t0, int& dst, uint32_t& en) __attribute__((always_inline)) -> int {
        const int k4 = (int)(((uint32_t)(lane & 3) + t0) * 4u) - (int)((c & 3u) * 8u);
        const int left = (int)((c >> 2) & 15u) - k4;
        const bool has = left > 0;
        uint32_t ea = tof + (uint32_t)ff * (128u * TST * 128u), eb = sp + (uint32_t)(k4 * 16 + min(max(left, 0), 4));
        asm volatile("" : "+v"(ea), "+v"(eb));
        en = k4 < 0 ? ea : eb;
        const u64 mask = __builtin_amdgcn_ballot_w64(has);
        const int n = __builtin_popcountll(mask);
        const int rank = __builtin_amdgcn_mbcnt_hi((uint32_t)(mask >> 32), __builtin_amdgcn_mbcnt_lo((uint32_t)mask, 0u));
        const int r = has ? rank : (n + lane - rank);
        dst = ((r & 15) << 4) | ((r >> 4) << 2);
        return n;
    };
    auto load_tail = [&](uint32_t en, u64& tm) __attribute__((always_inline)) -> u32x4 {
        const bool on = (uint32_t)(lane & 3) < (en & 7u);
        tm = __builtin_amdgcn_ballot_w64(on);
        return __builtin_amdgcn_raw_buffer_load_b128(rsT, on ? (en & ~15u) + (uint32_t)(lane & 3) * 16u : 0xffffff00u, 0, 0);
    };
    f32x2 fa[4][4];
    u32x4 em[2];
    u32x2 dw2;
    uint32_t wl;
    const __amdgpu_buffer_rsrc_t rsE = __builtin_amdgcn_make_buffer_rsrc((void*)m, 0, E * 8, 0x00020000);
    const __amdgpu_buffer_rsrc_t rsD = __builtin_amdgcn_make_buffer_rsrc((void*)dist, 0, E * 2, 0x00020000);
    auto acc_vec = [&](int i) __attribute__((always_inline)) -> u32x4 {
        const uint32_t a = A_ACC + (uint32_t)(i >> 4) * 256u + (uint32_t)(i & 15) * 8u;
        const u32x2 x = *(const LDS u32x2*)(uintptr_t)a, y = *(const LDS u32x2*)(uintptr_t)(a + 128u);
        const u32x4 v = {x.x, x.y, y.x, y.y};
        return v;
    };
    auto edge0 = [&](int sh) __attribute__((always_inline)) -> uint32_t {
        return (tid < SHARE / 4) ? (uint32_t)(((sh * (SHARE / 64) + (tid >> 4)) << 11) | (j << 6) | ((tid & 15) * 2)) : 0x0fffff00u;
    };
    auto load_m = [&](int sh) __attribute__((always_inline)) {
        em[0] = __builtin_amdgcn_raw_buffer_load_b128(rsE, edge0(sh) * 8u, 0, AUX_NT);
        em[1] = __builtin_amdgcn_raw_buffer_load_b128(rsE, edge0(sh) * 8u + 256u, 0, AUX_NT);
    };
    auto load_d = [&](int sh) __attribute__((always_inline)) {
        dw2.x = __builtin_amdgcn_raw_buffer_load_b32(rsD, edge0(sh) * 2u, 0, 0);
        dw2.y = __builtin_amdgcn_raw_buffer_load_b32(rsD, edge0(sh) * 2u + 64u, 0, 0);
    };
    auto load_wl = [&]() __attribute__((always_inline)) {
        const u64 p0 = (u64)wsbf, p1 = (u64)wrbf, p2 = (u64)centers, p3 = (u64)wup;
        const u64 pa = lane < 16 ? p0 : p1, pb = lane < 40 ? p2 : p3;
        wl = ((const GAS uint32_t*)(lane < 32 ? pa : pb))[lane & (lane < 32 ? 15 : lane < 40 ? 3 : 7)];
    };
    auto factor = [&](int r, bool first) __attribute__((always_inline)) {
        const float sq = 3.7982826f;
        const f32x2 d01 = {bf2f(dw2.x & 0xffffu), bf2f(dw2.x >> 16)}, d23 = {bf2f(dw2.y & 0xffffu), bf2f(dw2.y >> 16)};
        const uint32_t cw = __builtin_amdgcn_readlane(wl, 32 + (r >> 1));
        const float cn = bf2f((r & 1) ? (cw >> 16) : (cw & 0xffffu));
        f32x2 w[4];
#pragma unroll
        for (int k = 0; k < 4; k++) {
            const uint32_t x = __builtin_amdgcn_readlane(wl, (k >> 1) * 16 + r * 2 + (k & 1));
            w[k].x = bf2f(x & 0xffffu); w[k].y = bf2f(x >> 16);
        }
        const f32x2 y01 = (d01 - cn) * sq, y23 = (d23 - cn) * sq;
        const f32x2 t01 = -y01 * y01, t23 = -y23 * y23;
        const float rb[4] = {__builtin_amdgcn_exp2f(t01.x), __builtin_amdgcn_exp2f(t01.y), __builtin_amdgcn_exp2f(t23.x), __builtin_amdgcn_exp2f(t23.y)};
#pragma unroll
        for (int q = 0; q < 4; q++) {
#pragma unroll
            for (int k = 0; k < 4; k++) fa[q][k] = first ? w[k] * rb[q] : __builtin_elementwise_fma(w[k], (f32x2)(rb[q]), fa[q][k]);
        }
    };
    auto edges = [&](int sh) __attribute__((always_inline)) {
        load_d(sh);
        load_m(sh);
        factor(0, true);
#pragma unroll 1
        for (int r = 1; r < 8; r++) factor(r, false);
    };
#define ADT(V, O) "ds_write_addtid_b32 " V " offset:" #O "\n\t"
    auto store_md = [&]() __attribute__((always_inline)) {
        asm volatile("s_mov_b32 m0, %12\n\ts_nop 0\n\t" ADT("%0", 0) ADT("%1", 256) ADT("%2", 512) ADT("%3", 768) ADT("%4", 16384) ADT("%5", 16640) ADT("%6", 16896) ADT("%7", 17152)
                     ADT("%8", 32768) ADT("%9", 33024) ADT("%10", 33280) ADT("%11", 33536)
                     :: "v"(pm[0][0]), "v"(pm[0][1]), "v"(pm[0][2]), "v"(pm[0][3]), "v"(pm[1][0]), "v"(pm[1][1]), "v"(pm[1][2]), "v"(pm[1][3]),
                        "v"(pm[2][0]), "v"(pm[2][1]), "v"(pm[2][2]), "v"(pm[2][3]), "s"(wv * 1024) : "memory");
        if (wv == 0) asm volatile("s_mov_b32 m0, 0\n\ts_nop 0\n\t" ADT("%0", 49152) ADT("%1", 49408) ADT("%2", 49664) ADT("%3", 49920)
                                  :: "v"(pm[3][0]), "v"(pm[3][1]), "v"(pm[3][2]), "v"(pm[3][3]) : "memory");
    };
    const int me = h * 2 + cp;
    GAS unsigned* dw = (GAS unsigned*)(meta + M_DONE + j * 64);
    const bool fin = wv < (SHARE / 4 + 63) / 64;
    bool fast, more;
    uint32_t need, mg0, mg1;
    uint32_t guard = 0;
    uint32_t sgn = 0x8000u;
    pin(sgn);

#define GAT3(M) { uint32_t a0_, a1_, a2_; asm volatile( \
        "v_lshlrev_b16 %0, 2, %3\n\tv_lshlrev_b16 %1, 2, %4\n\tv_lshlrev_b16 %2, 2, %5\n\t" \
        "ds_read_b32 v[%6], %0\n\tds_read_b32 v[%6+1], %1\n\tds_read_b32 v[%6+2], %2" \
        : "=&v"(a0_), "=&v"(a1_), "=&v"(a2_) : "v"(w0), "v"(w1), "v"(w2), "n"(M) : "memory"); }
#define MIXL " op_sel_hi:[1,1,0]\n\t"
#define MIXH " op_sel:[0,1,0] op_sel_hi:[1,1,0]\n\t"
#define SDW " dst_sel:DWORD dst_unused:UNUSED_PAD src0_sel:DWORD src1_sel:WORD_1\n\t"
#define FOLD "v_bitop3_b32 %6, %6, v[%15-9], v[%15-8] bitop3:0xfe\n\tv_or_b32 %6, v[%15-7], %6\n\t"
#define SCAT(PRE, POST, M) { uint32_t c0_, c1_, c2_, a0_, a1_, a2_; asm volatile( \
        FOLD "v_and_b32 %0, 0x3ff, %7\n\tv_lshrrev_b32 %2, 22, %7\n\t" PRE \
        "v_bitop3_b32 %0, %11, %8, %0 bitop3:0xea\n\tv_bitop3_b32 %2, %11, %10, %2 bitop3:0xea\n\tv_bfe_u32 %1, %7, 11, 10\n\t" \
        "v_fma_mix_f32 %3, %0, v[%15], %12" MIXL "v_bitop3_b32 %1, %11, %9, %1 bitop3:0xea\n\tv_fma_mix_f32 %0, %0, v[%15], %13" MIXH \
        "v_fma_mix_f32 %4, %1, v[%15+1], %12" MIXL "v_lshl_add_u32 %0, %0, 16, %3\n\tv_lshlrev_b32_sdwa %3, 2, %8" SDW \
        "v_fma_mix_f32 %1, %1, v[%15+1], %13" MIXH "ds_add_rtn_u32 v[%15-9], %3, %0 offset:%14\n\t" \
        "v_fma_mix_f32 %5, %2, v[%15+2], %12" MIXL "v_lshl_add_u32 %1, %1, 16, %4\n\tv_lshlrev_b32_sdwa %4, 2, %9" SDW \
        "v_fma_mix_f32 %2, %2, v[%15+2], %13" MIXH "ds_add_rtn_u32 v[%15-8], %4, %1 offset:%14\n\t" \
        "v_lshl_add_u32 %2, %2, 16, %5\n\tv_lshlrev_b32_sdwa %5, 2, %10" SDW \
        "ds_add_rtn_u32 v[%15-7], %5, %2 offset:%14" POST \
        : "=&v"(c0_), "=&v"(c1_), "=&v"(c2_), "=&v"(a0_), "=&v"(a1_), "=&v"(a2_), "+v"(guard) \
        : "v"(xw), "v"(w0), "v"(w1), "v"(w2), "v"(sgn), "s"(mg0), "s"(mg1), "n"(A_ACC), "n"(M), "s"(tm) : "memory"); }
    asm volatile("v_mov_b32 v110, 0\n\tv_mov_b32 v111, 0\n\tv_mov_b32 v112, 0\n\tv_mov_b32 v113, 0\n\tv_mov_b32 v114, 0\n\tv_mov_b32 v115, 0\n\t"
                 "v_mov_b32 v116, 0\n\tv_mov_b32 v117, 0\n\tv_mov_b32 v118, 0" ::: "v106", "v127");
    auto unmix = [&](int i) __attribute__((always_inline)) {
        const auto s0 = __builtin_amdgcn_permlane16_swap(ring[i][0], ring[i][2], false, false);
        const auto s1 = __builtin_amdgcn_permlane16_swap(ring[i][1], ring[i][3], false, false);
        ring[i][0] = s0[0]; ring[i][2] = s0[1]; ring[i][1] = s1[0]; ring[i][3] = s1[1];
    };
    auto word = [&](int i, int k) __attribute__((always_inline)) -> uint32_t {
        return i < NFAST && (i >> 1) != HP ? ring[(i & ~1) | (k >> 1)][(i & 1) * 2 + (k & 1)] : ring[i][k];
    };
    auto gather = [&](int R, int i, int p) __attribute__((always_inline)) {
        if ((i >> 1) == HP) unmix(i);
        const uint32_t w0 = word(i, 0 ^ R), w1 = word(i, 1 ^ R), w2 = word(i, 2 ^ R);
        if (p == 0) GAT3(119) else if (p == 1) GAT3(122) else GAT3(125)
    };
    auto scatter = [&](int R, int i, int p, u64 tm) __attribute__((always_inline)) {
        const uint32_t xw = word(i, 3 ^ R), w0 = word(i, 0 ^ R), w1 = word(i, 1 ^ R), w2 = word(i, 2 ^ R);
        if (i < NFAST) { if (p == 0) SCAT("", "", 119) else if (p == 1) SCAT("", "", 122) else SCAT("", "", 125) }
        else if (p == 0) SCAT("s_mov_b64 exec, %16\n\t", "\n\ts_mov_b64 exec, -1", 119)
        else SCAT("s_mov_b64 exec, %16\n\t", "\n\ts_mov_b64 exec, -1", 125)
    };
#define WAIT_L(N) asm volatile("s_waitcnt lgkmcnt(" #N ")" ::: "memory")
    int ntn;
    u64 tm0, tm1;
    uint32_t ta[2], sp = sp0;
    auto list1 = [&](int ff) __attribute__((always_inline)) {
        int dst;
        uint32_t en;
        ntn = tail_rank(tc.x, sp, ff, 0u, dst, en);
        asm volatile("ds_permute_b32 v106, %0, %1" :: "v"(dst), "v"(en) : "memory");
        if (__builtin_expect(__builtin_amdgcn_ballot_w64((tc.x & 2u) != 0u) != 0ull, 0)) {
            const int n2 = tail_rank(tc.x, sp, ff, 4u, dst, en);
            if (ntn + n2 <= 32) {
                const int r = ((dst >> 4) | ((dst & 12) << 2)) + ntn;
                const int x = __builtin_amdgcn_ds_permute((en & 7u) ? ((r & 15) << 4) | ((r >> 4) << 2) : 252, (en & 7u) ? (int)en : 0);
                asm volatile("v_cmpx_ne_u32 vcc, 0, %0\n\tv_mov_b32 v106, %0\n\ts_mov_b64 exec, -1\n\ts_nop 4" :: "v"(x) : "vcc");
                ntn += n2;
            } else ntn = 64;
        }
    };
    auto step = [&](auto RR, auto KK, int ff) __attribute__((always_inline)) {
        constexpr int R = decltype(RR)::value;
        constexpr bool first = decltype(KK)::value == 1, last = decltype(KK)::value == 2;
        constexpr int mdp = first ? MDF : MDP;
        gather(R, 0, 0);
        const int ntl = ntn;
        asm volatile("v_mov_b32_dpp %0, v106 quad_perm:[0,0,0,0] row_mask:0xf bank_mask:0xf\n\tv_mov_b32_dpp %1, v106 quad_perm:[1,1,1,1] row_mask:0xf bank_mask:0xf" : "=v"(ta[0]), "=v"(ta[1]));
        auto side = [&](int pl) __attribute__((always_inline)) {
            if (first && pl == ((mdp >> 20) & 15)) { ring[4] = load_main(4, ff, !(NEARM & 1u)); ring[5] = load_main(5, ff, !(NEARM & 1u)); }
            if (first && pl == ((mdp >> 24) & 15)) { ring[6] = load_main(6, ff); ring[7] = load_main(7, ff); }
            if (pl == ((mdp >> 12) & 15)) ring[NFAST] = load_tail(ta[0], tm0);
            if (pl == ((mdp >> 16) & 15)) ring[NFAST + 1] = load_tail(ta[1], tm1);
            __builtin_amdgcn_sched_barrier(0);
            if (!last) load_md(ff + 1, pl, mdp);
            if (!first && !last && pl == 5 && ff == 6) {
                load_d(me);
                load_wl();
                if (tid == NT - 128) (void)__hip_atomic_fetch_add((GAS unsigned*)((GAS char*)dw + vconst<0>()), 0x1000000u << me, __ATOMIC_RELAXED, __HIP_MEMORY_SCOPE_AGENT);
            }
            if (APF && !first && !last && pl == 3 && ff == APF && wv == 14) { const uint32_t x_ = CODE_AHEAD(acc_kernel, ACC_BYTES, (uint32_t)lane); asm volatile("" :: "v"(x_)); }
            if (last && pl == 3) load_m(me);
            if (last && pl == 4 && wv == 15) { GAS unsigned* d_ = dw; asm volatile("s_nop 4\n\tglobal_load_dword v108, %0, %1 sc1" :: "v"(vconst<0>()), "s"(d_) : "memory"); }
            if (last && pl < 7 && fin) factor(pl, pl == 0);
            __builtin_amdgcn_sched_barrier(0);
        };
        side(8);
        gather(R, 1, 1);
        side(9);
        const uint32_t tcur = tc.x, spc = sp;
#pragma unroll
        for (int i = 0; i < NFAST - 1; i++) {
            gather(R, i + 2, (i + 2) % 3);
            if (i == 0) side(10);
            if (i == 0) WAIT_L(6); else if (i == 1) WAIT_L(9); else WAIT_L(12);
            scatter(R, i, i % 3, 0ull);
            if (!last && (i & 1)) { ring[i - 1] = load_main(i - 1, ff + 1, first && !(NEARM & 4u)); ring[i] = load_main(i, ff + 1, first && !(NEARM & 4u)); }
            side(i);
            if (i == 3) SETP(2);
            if (i == 4) SETP(1);
            if (i == 5) SETP(0);
            if (i == 0 && !last) {
                tc.x = __builtin_amdgcn_alignbit(tc.y, tc.x, 8u); tc.y = __builtin_amdgcn_alignbit(tc.z, tc.y, 8u);
                tc.z = __builtin_amdgcn_alignbit(tc.w, tc.z, 8u); tc.w >>= 8;
                sp = ff == 7 ? sp0 + NWG * 9u * 128u : sp + ((tcur & 0x3cu) << 2);
                list1(ff + 1);
            }
            __builtin_amdgcn_sched_barrier(0);
        }
        if (last) asm volatile("" :: "v"(ring[NFAST + 1]));
        if (__builtin_expect(ntl > 16, 0)) {
            gather(R, NFAST + 1, 0);
            WAIT_L(0);
            scatter(R, NFAST + 1, 0, tm1);
        }
        if (__builtin_expect(ntl > 32, 0)) {
#pragma unroll 1
            for (int e = 2; e < 6; e++) {
                int dst;
                uint32_t en;
                if (tail_rank(tcur, spc, ff, (uint32_t)e & 4u, dst, en) <= (e & 3) * 16) continue;
                const int cmp = __builtin_amdgcn_ds_permute(dst, (int)en);
                ring[NFAST + 1] = load_tail((uint32_t)__builtin_amdgcn_ds_bpermute(((lane & ~3) | (e & 3)) << 2, cmp), tm1);
                gather(R, NFAST + 1, 0);
                WAIT_L(0);
                scatter(R, NFAST + 1, 0, tm1);
            }
        }
        SETP(3);
        WAIT_L(3);
        if (!last) {
            __builtin_amdgcn_s_barrier();
            store_md();
            SETP(0);
        }
        scatter(R, NFAST - 1, 1, 0ull);
        if (!last) { ring[6] = load_main(6, ff + 1); ring[7] = load_main(7, ff + 1); }
        scatter(R, NFAST, 2, tm0);
        if (!last) {
            WAIT_L(6);
            __builtin_amdgcn_s_barrier();
            SETP(3);
        }
    };
    auto steps = [&](auto RR) __attribute__((always_inline)) {
        tc = tcnt[(wv * 16 + (lane >> 2)) * 128 + j * 4 + h];
        u32x4 sv[2];
        float4 su[3];
        uint32_t e0;
        if (wv < 4) {
            sv[0] = (u32x4)(0u); sv[1] = sv[0];
            su[0] = make_float4(0.f, 0.f, 0.f, 0.f);
            if (lane < SLOTI / 256) { sv[0] = stat[tid * 2]; sv[1] = stat[tid * 2 + 1]; su[0] = ((const float4*)(ws + W_SUM))[tid]; }
            su[1] = ((const float4*)(ws + W_SUM))[256 + lane];
            su[2] = ((const float4*)(ws + W_SUM))[320 + lane];
            e0 = meta[M_E0 + wv];
        }
        const u32x2 extra = *(const u32x2*)(meta + M_LOG);
        load_md(0, -1, 0);
        {
            const u32x4 bi = {BIAS, BIAS, BIAS, BIAS};
#pragma unroll
            for (int k = 0; k < 6; k++) *(LDS u32x4*)(uintptr_t)(A_ACC + (k * NT + threadIdx.x) * 16) = bi;
            if (threadIdx.x < 128) *(LDS u32x4*)(uintptr_t)(A_ACC + (6 * NT + threadIdx.x) * 16) = bi;
            __builtin_amdgcn_sched_barrier(0);
        }
#pragma unroll
        for (int i = 0; i < 2; i++) ring[i] = load_main(i, 0);
        __builtin_amdgcn_sched_barrier(0);
        list1(0);
        if (wv < 4) {
            const uint32_t mxb = wave_max(max(max(sv[0].x & 0xffffu, sv[0].z & 0xffffu), max(sv[1].x & 0xffffu, sv[1].z & 0xffffu)));
            const uint32_t mnb = 0xffffu - (wave_max(max(max(sv[0].x, sv[0].z), max(sv[1].x, sv[1].z))) >> 16);
            const float s2 = wave_sum((__uint_as_float(sv[0].y) + __uint_as_float(sv[0].w)) + (__uint_as_float(sv[1].y) + __uint_as_float(sv[1].w)));
            const float s1 = wave_sum((su[0].x + su[0].y) + (su[0].z + su[0].w));
            const float c1 = wave_sum((su[1].x + su[1].y) + (su[1].z + su[1].w)) * (1.0f / (NWG * 512)), c2 = wave_sum((su[2].x + su[2].y) + (su[2].z + su[2].w)) * (1.0f / (NWG * 512));
            const float rng = fmaxf(fabsf(zf * s1 * c1) + ZSIG * sqrtf(zf * s2 * c2), 2.0f * bf2f(mxb)) * 6.103515625e-05f;
            const uint32_t eb = mxb ? (__float_as_uint(rng) + 0x7fffffu) >> 23 : 127u;
            const bool ok = !mxb || (mxb < 0x4700u && eb >= 107u && eb <= 140u && mnb >= ((eb + 8u) << 7) && c2 >= 0.015625f);
            if (lane == 0) {
                s_flag[8 + wv] = (eb & 0xffu) | (ok ? 0x100u : 0u) | (mxb ? 0x200u : 0u);
                s_flag[12 + wv] = __float_as_uint(__uint_as_float(((eb + e0) & 0xffu) << 23) * (1024.0f / 1023.0f));
                s_flag[wv] = 0u;
            }
        }
        store_md();
#pragma unroll
        for (int i = 2; i < 4; i++) ring[i] = load_main(i, 0);
        WAIT_L(0);
        __syncthreads();
        const uint32_t q0 = __builtin_amdgcn_readfirstlane(s_flag[8 + 2 * cp]), q1 = __builtin_amdgcn_readfirstlane(s_flag[9 + 2 * cp]);
        fast = (q0 & q1 & 0x100u) != 0u;
        need = (q0 >> 9) | ((q1 >> 9) << 1);
        more = (__builtin_amdgcn_readfirstlane(extra.x | extra.y) | (uint32_t)(T & 3)) != 0u;
        mg0 = (((q0 & 0xffu) + 9u) << 23) | 0x400000u;
        mg1 = ((((q1 & 0xffu) + 9u) << 23) | 0x400000u) + ((0u - (mg0 >> 16)) & 0xffffu);
        if (!fast) { __builtin_amdgcn_s_waitcnt(0x0f70); return; }
        RTS(9)
        step(RR, IC<1>(), 0);
#pragma unroll 1
        for (int ff = 1; ff < 15; ff++) step(RR, IC<0>(), ff);
        step(RR, IC<2>(), 15);
        if (fin) factor(7, false);
    };
    auto irregular = [&](auto f) __attribute__((always_inline)) {
        const float fr = bf2f(freq[0]);
        const char* lg = recs + (size_t)LBASE * 128u;
        const uint16_t* tags = (const uint16_t*)(lg + (size_t)lcap * 128u);
        const uint32_t nl = meta[M_LOG], ns = meta[M_LOG + 1];
#pragma unroll 1
        for (uint32_t q = tid; q < nl * 8u; q += NT) {
            const uint32_t b = tags[q >> 3];
            if ((b & 127u) == (uint32_t)(h * 32 + j)) {
                const uint32_t* un = (const uint32_t*)(lg + (size_t)q * 16u);
                const uint32_t xw = un[3 ^ h];
#pragma unroll 1
                for (int k = 0; k < 3; k++) {
                    const uint32_t ww = un[k ^ h];
                    const float c = (float)((xw >> (k * 11)) & 0x3ffu) * (1.0f / 1023.0f);
                    if (!(ww & WNULL)) f(ww >> 16, ((ww & 0x3fffu) << 6) | (b >> 5), (ww & 0x8000u) ? -c : c);
                }
            }
        }
#pragma unroll 1
        for (uint32_t q = tid; q < ns + (uint32_t)(T & 3); q += NT) {
            const uint32_t t = q < ns ? ((const uint32_t*)tags)[-1 - (int)q] : (uint32_t)(T & ~3) + (q - ns);
            const uint32_t i = (uint32_t)iin[t], o = (uint32_t)iout[t];
            if (((o >> 6) & 31u) == (uint32_t)j && (i & 3u) == (uint32_t)h) f(((o >> 11) << 6) | (o & 63u), i, cosf(fr * bf2f(ang[t])));
        }
        __builtin_amdgcn_s_waitcnt(0x0f70);
    };

    if (h == 0) steps(IC<0>());
    else if (h == 1) steps(IC<1>());
    else if (h == 2) steps(IC<2>());
    else steps(IC<3>());
    if (fast) {
        if (more) irregular([&](uint32_t ol, uint32_t i, float cf) __attribute__((always_inline)) {
            const uint32_t md = mdc[(cp * 64u + (i & 63u)) * SLOTI + ((i >> 14) << 8) + ((i >> 4) & 0xfcu) + ((i >> 12) & 3u)];
            const float c = cf * (1023.0f / 16777216.0f);
            const uint32_t lo = __float_as_uint(__builtin_fmaf(c, h2f(md & 0xffffu), __uint_as_float(mg0)));
            const uint32_t hi = __float_as_uint(__builtin_fmaf(c, h2f(md >> 16), __uint_as_float(mg1)));
            if (cf == cf) guard |= atomicAdd(&acc[ol], (hi << 16) + lo);
            else guard |= HSLOW;
        });
    }
    volatile LDS unsigned* seen = (volatile LDS unsigned*)(uintptr_t)(A_FLAG + 4);
    if (fast && wv == 15) asm volatile("s_waitcnt vmcnt(0)\n\tds_write_b32 %0, v108 offset:16\n\tv_and_b32 v109, 0xff00ff, v108\n\tds_write_b32 %0, v109" :: "v"(vconst<A_FLAG + 4>()) : "memory");
    WAIT_L(0);
    asm volatile("v_or3_b32 %0, %0, v110, v111\n\tv_or3_b32 %0, %0, v112, v113\n\tv_or3_b32 %0, %0, v114, v115\n\tv_or3_b32 %0, %0, v116, v117\n\tv_or_b32 %0, v118, %0" : "+v"(guard));
    __syncthreads();
    RTS(10)
    const __amdgpu_buffer_rsrc_t rsP = __builtin_amdgcn_make_buffer_rsrc((void*)(partial + (size_t)j * 16 * SLOTO), 0, 16 * SLOTO * 4, 0x00020000);
    const char* pk = recs + (uint32_t)j * (SC * 128u);
    const __amdgpu_buffer_rsrc_t rsK = __builtin_amdgcn_make_buffer_rsrc((void*)pk, 0, (int)(MAINL * 128u), 0x00020000);
#define PKA (SSM * 128u)
#define PKB (32u * SC * 128u)
    auto pk_at = [&](uint32_t i) __attribute__((always_inline)) -> uint32_t {
        const uint32_t c = i >> 9;
        return (c < 12u ? (1u + (c >> 2)) * SQ + (c & 3u) * SKM : SQ + 2u * SSM) * 128u + (i & 511u) * 16u;
    };
    u32x4 p[8];
    unsigned have = 0u;
    auto fetch = [&](unsigned v, int sh) __attribute__((always_inline)) {
        const uint32_t po = tid < SHARE / 4 ? pk_at((uint32_t)(sh * (SHARE / 4) + tid)) : 0x7fffff00u;
        const unsigned nw = v & ~have;
#pragma unroll
        for (int r = 0; r < 8; r++) {
            const uint32_t k = (uint32_t)(me ^ r);
            if ((nw >> k) & 1u) p[r] = __builtin_amdgcn_raw_buffer_load_b128(rsK, po, ((v >> (16u + k)) & 1u) ? MAINL * 128u : (k & 1u) * PKA + (k >> 1) * PKB, AUX_SC1);
        }
        have |= v;
    };
    bool room = false;
    if (fast) {
        room = (__builtin_amdgcn_readfirstlane(s_flag[5]) >> (24 + (me ^ 1))) & 1u;
        const __amdgpu_buffer_rsrc_t rsW = __builtin_amdgcn_make_buffer_rsrc((void*)(pk + (uint32_t)cp * PKA + (uint32_t)h * PKB), 0, room ? (int)(MAINL * 128u) : 0, 0x00020000);
        uint32_t big = 0u;
        u32x4 v[7];
#pragma unroll
        for (int k = 0; k < 6; k++) v[k] = acc_vec(k * NT + tid);
        v[6] = (u32x4)(BIAS);
        if (wv < 2) v[6] = acc_vec(6 * NT + tid);
#pragma unroll
        for (int k = 0; k < 6; k++) __builtin_amdgcn_raw_buffer_store_b128(v[k], rsW, pk_at((uint32_t)tid) + ((k >> 1) * SQ + (k & 1) * 2u * SKM) * 128u, 0, AUX_SC1);
        if (wv < 2) __builtin_amdgcn_raw_buffer_store_b128(v[6], rsW, (SQ + 2u * SSM) * 128u + (uint32_t)tid * 16u, 0, AUX_SC1);
        __builtin_amdgcn_sched_barrier(0);
#pragma unroll
        for (int k = 0; k < 28; k++) { guard |= v[k >> 2][k & 3]; big |= v[k >> 2][k & 3] + 0x42004200u; }
        if (guard & HSLOW) s_flag[0] = 1u;
        if (big & 0x7c00u) s_flag[2] = 1u;
        if (big & 0x7c000000u) s_flag[3] = 2u;
        __builtin_amdgcn_sched_barrier(0);
        p[0] = acc_vec(me * (SHARE / 4) + min(tid, SHARE / 4 - 1));
        have = 1u << me;
        if (fin) fetch(__builtin_amdgcn_readfirstlane(*seen), me);
    }
    asm volatile("s_waitcnt vmcnt(0)" ::: "memory");
    __syncthreads();
    RTS(11)
    const bool redo = !fast || __builtin_amdgcn_readfirstlane(s_flag[0]) != 0u || (need & ~__builtin_amdgcn_readfirstlane(s_flag[2] | s_flag[3])) != 0u;
    const bool slow = redo || !room;
    if (slow) {
        float* fs = (float*)acc;
        float w[16];
        load_w(wdown, w);
        const float fr = bf2f(freq[0]);
#pragma unroll 1
        for (int e = 0; e < 2; e++) {
            if (redo) {
                __syncthreads();
                for (int i = tid; i < SLOTO; i += NT) fs[i] = 0.f;
                __syncthreads();
                const __amdgpu_buffer_rsrc_t rsO = __builtin_amdgcn_make_buffer_rsrc((void*)iout, 0, T * 4, 0x00020000);
                const __amdgpu_buffer_rsrc_t rsI = __builtin_amdgcn_make_buffer_rsrc((void*)iin, 0, T * 4, 0x00020000);
#pragma unroll 1
                for (int t0 = tid * 4; t0 < T; t0 += NT * 16) {
                    u32x4 o4[4], i4[4];
#pragma unroll
                    for (int g = 0; g < 4; g++) {
                        o4[g] = __builtin_amdgcn_raw_buffer_load_b128(rsO, (uint32_t)(t0 + g * NT * 4) * 4u, 0, 0);
                        i4[g] = __builtin_amdgcn_raw_buffer_load_b128(rsI, (uint32_t)(t0 + g * NT * 4) * 4u, 0, 0);
                    }
                    uint32_t hit = 0u;
#pragma unroll
                    for (int k = 0; k < 16; k++) hit |= (bfi(3u, i4[k >> 2][k & 3], o4[k >> 2][k & 3]) & 0x7c3u) == (uint32_t)(j * 64 + h) ? 1u << k : 0u;
#pragma unroll 1
                    for (; hit; hit &= hit - 1u) {
                        const int k = __builtin_ctz(hit), t = t0 + (k >> 2) * NT * 4 + (k & 3);
                        if (t >= T) break;
                        const uint32_t o = (uint32_t)iout[t];
                        uint32_t rr[4];
                        md_of(m[iin[t]], w, rr);
                        atomicAdd(&fs[((o >> 11) << 6) | (o & 63u)], cosf(fr * bf2f(ang[t])) * bf2f(cp ? (e ? rr[3] : rr[2]) : (e ? rr[1] : rr[0])));
                    }
                }
                __syncthreads();
            }
            const float un = __uint_as_float(s_flag[12 + 2 * cp + e]);
            for (int i = tid; i < SLOTO / 4; i += NT) {
                u32x4 v = acc_vec(i);
                if (!redo) {
#pragma unroll
                    for (int k = 0; k < 4; k++) v[k] = __float_as_uint((float)((int)((v[k] >> (16 * e)) & 0xffffu) - 0x4000) * un);
                }
                __builtin_amdgcn_raw_buffer_store_b128(v, rsP, (uint32_t)((e * 8 + me) * SLOTO * 4 + i * 16), 0, AUX_SC1);
            }
        }
        load_wl();
        edges(me);
#pragma unroll
        for (int k = 0; k < 8; k++) p[k] = (u32x4)(0u);
        have = 0u;
        asm volatile("s_waitcnt vmcnt(0)" ::: "memory");
        __syncthreads();
    }
    if (tid == NT - 64) {
        const unsigned inc = (slow ? 0x10001u : 1u) << me;
        unsigned v = __hip_atomic_fetch_add(dw, inc, __ATOMIC_RELAXED, __HIP_MEMORY_SCOPE_AGENT) + inc;
        unsigned quit = 0u;
        if ((v & 0xffu) != 0xffu) {
            unsigned spins = 0;
            do {
                *seen = v & 0xff00ffu;
                __builtin_amdgcn_s_sleep(1);
                v = __hip_atomic_load(dw, __ATOMIC_RELAXED, __HIP_MEMORY_SCOPE_AGENT);
                if (++spins > SPINCAP && (v & 0xffu) != 0xffu) {
                    v = __hip_atomic_fetch_add(dw, 256u << me, __ATOMIC_RELAXED, __HIP_MEMORY_SCOPE_AGENT);
                    if ((v & 0xffu) != 0xffu) { v = 0xffu; quit = 0x80000000u; }
                }
            } while ((v & 0xffu) != 0xffu);
            v &= 0xff00ffu;
        }
        *seen = (v & 0xffffffu) | quit;
    }
    if (!fin) return;
    have = __builtin_amdgcn_readfirstlane(have);
    uint32_t un[4];
#pragma unroll
    for (int c = 0; c < 4; c++) un[c] = __builtin_amdgcn_readfirstlane(s_flag[12 + c]);
    unsigned sv;
    {
        const u32x4 rsQ = {(uint32_t)(uintptr_t)pk, (uint32_t)((uintptr_t)pk >> 32) & 0xffffu, MAINL * 128u, 0x00020000u};
        const uint32_t po = tid < SHARE / 4 ? pk_at((uint32_t)(me * (SHARE / 4) + tid)) : 0x7fffff00u;
        do {
            sv = __builtin_amdgcn_readfirstlane(*seen);
            uint32_t t0, t1, t2;
#define FET(R) "s_xor_b32 %8, %13, " #R "\n\ts_bitcmp1_b32 %14, %8\n\ts_cbranch_scc0 1f\n\ts_and_b32 %9, %8, 1\n\ts_mul_i32 %9, %9, %16\n\ts_lshr_b32 %10, %8, 1\n\t" \
               "s_mul_i32 %10, %10, %17\n\ts_add_u32 %9, %9, %10\n\ts_bitcmp1_b32 %15, %8\n\ts_cselect_b32 %9, %18, %9\n\tbuffer_load_dwordx4 %" #R ", %11, %12, %9 offen sc1\n1:\n\t"
            asm volatile("s_and_b32 %8, %14, 0xff\n\ts_cbranch_scc0 2f\n\ts_nop 2\n\t" FET(0) FET(1) FET(2) FET(3) FET(4) FET(5) FET(6) FET(7) "\n2:\n\ts_sleep 2\n\ts_waitcnt vmcnt(0)"
                         : "+v"(p[0]), "+v"(p[1]), "+v"(p[2]), "+v"(p[3]), "+v"(p[4]), "+v"(p[5]), "+v"(p[6]), "+v"(p[7]), "=&s"(t0), "=&s"(t1), "=&s"(t2)
                         : "v"(po), "s"(rsQ), "s"(me), "s"((int)sv < 0 ? 0u : sv & ~have), "s"(sv >> 16), "n"(PKA), "n"(PKB), "n"(MAINL * 128u) : "memory", "scc");
            have |= sv;
        } while ((sv & 0xffu) != 0xffu);
    }
    if ((int)sv < 0) return;
    RTS(12)
    const unsigned flt = (sv >> 16) & 0xffu;
    auto finish = [&](int sh) __attribute__((always_inline)) {
        {
            const int ol = sh * SHARE + tid * 4;
            const uint32_t po = tid < SHARE / 4 ? (uint32_t)ol * 4u : 0x7fffff00u;
            float g[4][4];
            u32x4 a[2] = {p[0] + p[2], p[1] + p[3]}, b[2] = {p[4] + p[6], p[5] + p[7]};
            if (cp) { const u32x4 t = a[0], u = b[0]; a[0] = a[1]; b[0] = b[1]; a[1] = t; b[1] = u; }
#pragma unroll
            for (int c = 0; c < 2; c++) {
                const int nb = 0x10000 - 0x4000 * __builtin_popcount(flt & (0x55u << c));
                const float u0 = __uint_as_float(nb ? un[2 * c] : 0u), u1 = __uint_as_float(nb ? un[2 * c + 1] : 0u);
#pragma unroll
                for (int q = 0; q < 4; q++) {
                    g[q][2 * c] = (float)((int)((a[c][q] & 0xffffu) + (b[c][q] & 0xffffu)) - nb) * u0;
                    g[q][2 * c + 1] = (float)((int)((a[c][q] >> 16) + (b[c][q] >> 16)) - nb) * u1;
                }
            }
            if (__builtin_expect(flt != 0u, 0)) {
#pragma unroll
                for (int c = 0; c < 2; c++) {
#pragma unroll 1
                    for (int k = c; k < 8; k += 2) {
                        if (!((flt >> k) & 1u)) continue;
                        const u32x4 f0 = __builtin_amdgcn_raw_buffer_load_b128(rsP, po, k * SLOTO * 4, AUX_SC1);
                        const u32x4 f1 = __builtin_amdgcn_raw_buffer_load_b128(rsP, po, (8 + k) * SLOTO * 4, AUX_SC1);
#pragma unroll
                        for (int q = 0; q < 4; q++) { g[q][2 * c] += __uint_as_float(f0[q]); g[q][2 * c + 1] += __uint_as_float(f1[q]); }
                    }
                }
            }
            float wu[16];
#pragma unroll
            for (int k = 0; k < 8; k++) {
                const uint32_t x = __builtin_amdgcn_readlane(wl, 40 + k);
                wu[2 * k] = bf2f(x & 0xffffu); wu[2 * k + 1] = bf2f(x >> 16);
            }
            u32x4 ov[2];
#pragma unroll
            for (int q = 0; q < 4; q++) {
#pragma unroll
                for (int k = 0; k < 4; k++) g[q][k] *= fa[q][k >> 1][k & 1];
                const uint32_t m0 = em[q >> 1][(q & 1) * 2], m1 = em[q >> 1][(q & 1) * 2 + 1];
                const float mm[4] = {bf2f(m0 & 0xffff), bf2f(m0 >> 16), bf2f(m1 & 0xffff), bf2f(m1 >> 16)};
                float o[4];
#pragma unroll
                for (int k = 0; k < 4; k++) o[k] = mm[k] + (g[q][0] * wu[k] + g[q][1] * wu[4 + k] + g[q][2] * wu[8 + k] + g[q][3] * wu[12 + k]) * fa[q][2 + (k >> 1)][k & 1];
                ov[q >> 1][(q & 1) * 2] = pk_bf16(o[0], o[1]);
                ov[q >> 1][(q & 1) * 2 + 1] = pk_bf16(o[2], o[3]);
            }
            const __amdgpu_buffer_rsrc_t rsU = __builtin_amdgcn_make_buffer_rsrc((void*)out, 0, E * 8, 0x00020000);
            const uint32_t uo = edge0(sh) * 8u;
            __builtin_amdgcn_raw_buffer_store_b128(ov[0], rsU, uo, 0, AUX_NT);
            __builtin_amdgcn_raw_buffer_store_b128(ov[1], rsU, uo + 256u, 0, AUX_NT);
        }
    };
    finish(me);
#pragma unroll 1
    for (unsigned todo = (sv >> 8) & 0xffu; todo; todo &= todo - 1u) {
        const int sh = __builtin_ctz(todo);
        edges(sh);
        have = 0u;
        fetch(sv, sh);
        finish(sh);
    }
    const unsigned leak_ = __builtin_amdgcn_readfirstlane(meta[M_E0 + 8]);
    if (blockIdx.x == 5) { const unsigned long long t0_ = __builtin_amdgcn_s_memrealtime(); while (__builtin_amdgcn_s_memrealtime() - t0_ < min(leak_, 40000u)) __builtin_amdgcn_s_sleep(2); }
}

__global__ __launch_bounds__(256) void gen_scatter_kernel(const int* __restrict__ iin, const int* __restrict__ iout,
                                                          const uint16_t* __restrict__ ang, const uint16_t* __restrict__ freq,
                                                          const u32x2* __restrict__ m, const uint16_t* __restrict__ wdown,
                                                          float* __restrict__ agg, int T) {
    float w[16];
    load_w(wdown, w);
    const float fr = bf2f(freq[0]);
    for (long t = (long)blockIdx.x * 256 + threadIdx.x; t < T; t += (long)gridDim.x * 256) {
        const float c = cosf(fr * bf2f(ang[t]));
        uint32_t r[4];
        md_of(m[iin[t]], w, r);
        float* q = agg + (size_t)iout[t] * 4;
#pragma unroll
        for (int k = 0; k < 4; k++) atomicAdd(q + k, c * bf2f(r[k]));
    }
}
__global__ __launch_bounds__(256) void gen_final_kernel(const u32x2* __restrict__ m, const uint16_t* __restrict__ dist,
                                                        const uint16_t* __restrict__ centers, const uint16_t* __restrict__ wrbf,
                                                        const uint16_t* __restrict__ wsbf, const uint16_t* __restrict__ wup,
                                                        const float* __restrict__ agg, u32x2* __restrict__ out, int E) {
    const int e = blockIdx.x * 256 + threadIdx.x;
    if (e >= E) return;
    const float d = bf2f(dist[e]);
    float rs[4] = {0, 0, 0, 0}, rg[4] = {0, 0, 0, 0};
#pragma unroll 1
    for (int r = 0; r < 8; r++) {
        const float x = d - bf2f(centers[r]);
        const float rb = __expf(-10.0f * x * x);
#pragma unroll
        for (int k = 0; k < 4; k++) { rs[k] += rb * bf2f(wsbf[r * 4 + k]); rg[k] += rb * bf2f(wrbf[r * 4 + k]); }
    }
    float wu[16];
    load_w(wup, wu);
    const u32x2 mv = m[e];
    const float mm[4] = {bf2f(mv.x & 0xffff), bf2f(mv.x >> 16), bf2f(mv.y & 0xffff), bf2f(mv.y >> 16)};
    float g[4], o[4];
#pragma unroll
    for (int k = 0; k < 4; k++) g[k] = agg[(size_t)e * 4 + k] * rs[k];
#pragma unroll
    for (int k = 0; k < 4; k++) o[k] = mm[k] + (g[0] * wu[k] + g[1] * wu[4 + k] + g[2] * wu[8 + k] + g[3] * wu[12 + k]) * rg[k];
    const u32x2 ov = {f2bf(o[0]) | (f2bf(o[1]) << 16), f2bf(o[2]) | (f2bf(o[3]) << 16)};
    out[e] = ov;
}

extern "C" void kernel_launch(void* const* d_in, const int* in_sizes, int n_in,
                              void* d_out, int out_size, void* d_ws, size_t ws_size,
                              hipStream_t stream) {
    const u32x2* m = (const u32x2*)d_in[0];
    const int* iin = (const int*)d_in[1];
    const int* iout = (const int*)d_in[2];
    const uint16_t* dist = (const uint16_t*)d_in[3];
    const uint16_t* ang = (const uint16_t*)d_in[4];
    const uint16_t* centers = (const uint16_t*)d_in[5];
    const uint16_t* freq = (const uint16_t*)d_in[6];
    const uint16_t* wrbf = (const uint16_t*)d_in[7];
    const uint16_t* wsbf = (const uint16_t*)d_in[8];
    const uint16_t* wdown = (const uint16_t*)d_in[9];
    const uint16_t* wup = (const uint16_t*)d_in[10];
    const int E = in_sizes[3];
    const int T = in_sizes[1];
    char* ws = (char*)d_ws;
    const int G = T >> 2;
    const int ntile = (G + GR - 1) / GR;
    const int nstat = ntile / NWG - NDYN > 4 ? ntile / NWG - NDYN : 4;
    const int lcap = G / 6 + 2;
    char* recs = ws + W_RECS;
    size_t off = W_RECS;
    const size_t rbytes = (size_t)LBASE * 128 + (size_t)lcap * 130;
    off += rbytes;
    if (E > EMAX || (E & 3) || E < 1 || rbytes > 0x70000000u || off > ws_size) {
        (void)hipMemsetAsync(ws, 0, (size_t)E * 16, stream);
        gen_scatter_kernel<<<4096, 256, 0, stream>>>(iin, iout, ang, freq, m, wdown, (float*)ws, T);
        gen_final_kernel<<<(E + 255) / 256, 256, 0, stream>>>(m, dist, centers, wrbf, wsbf, wup, (const float*)ws, (u32x2*)d_out, E);
        return;
    }
    int ks = T / (NWG * NB * 24);
    if (ks > KMAX) ks = KMAX;
    const float zf = (float)T / (4.0f * (float)E * (float)E);
    (void)hipFuncSetAttribute((const void*)part_kernel, hipFuncAttributeMaxDynamicSharedMemorySize, P_LDS);
    (void)hipFuncSetAttribute((const void*)acc_kernel, hipFuncAttributeMaxDynamicSharedMemorySize, A_LDS);
    part_kernel<<<NWG, NT, P_LDS, stream>>>(iin, iout, ang, freq, m, wdown, recs, T, E, ntile, nstat, ks, lcap);
    acc_kernel<<<NWG, NT, A_LDS, stream>>>(ws, T, E, ks, lcap, zf, 0, centers, wrbf, wsbf, wup, m, dist, (u32x2*)d_out, iin, iout, ang, wdown, freq);
}
```

```cpp
#include <hip/hip_runtime.h>
#include <hip/hip_bf16.h>
#include <stdint.h>
#pragma clang diagnostic ignored "-Winline-asm"

typedef unsigned int u32x4 __attribute__((ext_vector_type(4)));
typedef unsigned int u32x2 __attribute__((ext_vector_type(2)));
typedef unsigned short u16x2 __attribute__((ext_vector_type(2)));
typedef __bf16 bf16x2 __attribute__((ext_vector_type(2)));
typedef float f32x2 __attribute__((ext_vector_type(2)));
typedef unsigned long long u64;

static __device__ __forceinline__ float bf2f(uint32_t bits16) { return __uint_as_float(bits16 << 16); }
static __device__ __forceinline__ uint32_t f2bf(float f) {
    uint32_t u = __float_as_uint(f);
    if ((u & 0x7fffffffu) > 0x7f800000u) return (u >> 16) | 0x40;
    return (u + 0x7fffu + ((u >> 16) & 1u)) >> 16;
}
static __device__ __forceinline__ float h2f(uint32_t bits16) { return (float)__builtin_bit_cast(_Float16, (unsigned short)bits16); }

#define NB 2048
#define WNULL 0x80000000u
#define CSTEP 0x2aabu
#define CINIT 205u
#define CHV (12u * CSTEP)
#define SLOTO 25088
#define SLOTI 12544
#define EMAX (SLOTO * 32)
#define NT 1024
#define NWG 256
#define KMAX 4
#define NTAIL 4
#define SC 64u
#define SKM (128u * SC + 2u)
#define SSM (4u * SKM + 1u)
#define SSH (128u * SC + 1u)
#define SKH (16u * SSH + 2u)
#define SQ (16u * SSM)
#define MAINL (4u * SQ)
#define BPL(b) ((((b) >> 3) & 3u) * SQ + ((b) & 7u) + (((b) >> 5) << 3))
#define TST 257u
#define TBASE MAINL
#define TPL (NB * TST)
#define SBASE (TBASE + 2u * TPL)
#define LBASE (SBASE + 128u * 2u * NWG * 9u)
#define NDYN 3
#define NEARM 0x55555554u
#define GR 2048
#define M_DONE 0
#define M_TILE (32 * 64)
#define M_LOG (M_TILE + 64)
#define M_MD (M_LOG + 64)
#define M_BEAT (M_MD + 64)
#define M_E0 (M_BEAT + 64)
#define M_WORDS (M_E0 + 64)
#define W_TCNT (M_WORDS * 4)
#define W_STAT (W_TCNT + NWG * NB)
#define W_SUM (W_STAT + 4 * NWG * 8)
#define W_MDC (W_SUM + 6 * NWG * 4)
#define W_PART (W_MDC + 128 * SLOTI * 4)
#define W_RECS (W_PART + 32 * 16 * SLOTO * 4)
#define AUX_NT 2
#define AUX_SC1 16

static __device__ __forceinline__ uint32_t row_max(uint32_t x) {
    x = max(x, (uint32_t)__builtin_amdgcn_update_dpp(0, x, 0x111, 0xf, 0xf, false));
    x = max(x, (uint32_t)__builtin_amdgcn_update_dpp(0, x, 0x112, 0xf, 0xf, false));
    x = max(x, (uint32_t)__builtin_amdgcn_update_dpp(0, x, 0x114, 0xf, 0xf, false));
    return max(x, (uint32_t)__builtin_amdgcn_update_dpp(0, x, 0x118, 0xf, 0xf, false));
}
static __device__ __forceinline__ uint32_t wave_max(uint32_t x) {
    x = row_max(x);
    x = max(x, (uint32_t)__builtin_amdgcn_update_dpp(0, x, 0x142, 0xa, 0xf, false));
    x = max(x, (uint32_t)__builtin_amdgcn_update_dpp(0, x, 0x143, 0xc, 0xf, false));
    return __builtin_amdgcn_readlane(x, 63);
}
static __device__ __forceinline__ float row_sum(float x) {
    x += __int_as_float(__builtin_amdgcn_update_dpp(0, __float_as_int(x), 0x111, 0xf, 0xf, false));
    x += __int_as_float(__builtin_amdgcn_update_dpp(0, __float_as_int(x), 0x112, 0xf, 0xf, false));
    x += __int_as_float(__builtin_amdgcn_update_dpp(0, __float_as_int(x), 0x114, 0xf, 0xf, false));
    return x + __int_as_float(__builtin_amdgcn_update_dpp(0, __float_as_int(x), 0x118, 0xf, 0xf, false));
}
static __device__ __forceinline__ float wave_sum(float x) {
    x = row_sum(x);
    x += __int_as_float(__builtin_amdgcn_update_dpp(0, __float_as_int(x), 0x142, 0xa, 0xf, false));
    x += __int_as_float(__builtin_amdgcn_update_dpp(0, __float_as_int(x), 0x143, 0xc, 0xf, false));
    return __int_as_float(__builtin_amdgcn_readlane(__float_as_int(x), 63));
}
static __device__ __forceinline__ uint32_t bfi(uint32_t mask, uint32_t a, uint32_t b) {
    return __builtin_amdgcn_bitop3_b32(mask, a, b, 0xca);
}
template <uint32_t C> static __device__ __forceinline__ uint32_t vconst() {
    uint32_t r;
    asm volatile("v_mov_b32 %0, %1" : "=v"(r) : "n"(C));
    return r;
}
static __device__ __forceinline__ uint32_t pk_bf16(float a, float b) {
    const f32x2 v = {a, b};
    return __builtin_bit_cast(uint32_t, __builtin_convertvector(v, bf16x2));
}
static __device__ __forceinline__ void pin(uint32_t& x) { asm volatile("" : "+v"(x)); }


static __device__ __forceinline__ void md_of(u32x2 v, const float* w, uint32_t* r) {
    float a0 = bf2f(v.x & 0xffff), a1 = bf2f(v.x >> 16), a2 = bf2f(v.y & 0xffff), a3 = bf2f(v.y >> 16);
#pragma unroll
    for (int j = 0; j < 4; j++) r[j] = f2bf(a0 * w[j] + a1 * w[4 + j] + a2 * w[8 + j] + a3 * w[12 + j]);
}
static __device__ __forceinline__ void load_w(const uint16_t* __restrict__ wdown, float* w) {
    u32x4 wv0 = *(const u32x4*)wdown, wv1 = *(const u32x4*)(wdown + 8);
#pragma unroll
    for (int i = 0; i < 4; i++) {
        w[2 * i] = bf2f(wv0[i] & 0xffffu); w[2 * i + 1] = bf2f(wv0[i] >> 16);
        w[8 + 2 * i] = bf2f(wv1[i] & 0xffffu); w[8 + 2 * i + 1] = bf2f(wv1[i] >> 16);
    }
}
#define LDS __attribute__((address_space(3)))
#define P_CNT 0
#define P_FLAG ((NB + 64) * 4)
#define P_LST (P_FLAG + 256)
#define LCAP 32
#define P_TAB (P_LST + 4 * LCAP * 16)
#define P_BUF (P_TAB + NB * 4)
#define P_LDS (P_BUF + NB * 64)
#define PD 2
#define LPOS 0x448763
#define SETP(n) __builtin_amdgcn_s_setprio(n)
#define MDL 1
#define MDM 3
#define RMAXREV 40.0f
#define HB 88
#define HREG(S, D) (HB + ((S) & 3) * 10 + (((S) >> 2) + (D)) * 2)

template <int N> struct IC { static constexpr int value = N; };
struct Raw { u32x4 o[2], i[2]; u32x2 a[2]; };
static __device__ __forceinline__ uint32_t lds_ld(uint32_t a) { return *(const LDS uint32_t*)(uintptr_t)a; }
static __device__ __forceinline__ void lds_st(uint32_t a, uint32_t v) { *(LDS uint32_t*)(uintptr_t)a = v; }
static __device__ __forceinline__ void lds_or(uint32_t a, uint32_t v) {
    __hip_atomic_fetch_or((LDS uint32_t*)(uintptr_t)a, v, __ATOMIC_RELAXED, __HIP_MEMORY_SCOPE_WORKGROUP);
}
static __device__ __forceinline__ uint32_t lds_add(uint32_t a, uint32_t v) {
    return __hip_atomic_fetch_add((LDS uint32_t*)(uintptr_t)a, v, __ATOMIC_RELAXED, __HIP_MEMORY_SCOPE_WORKGROUP);
}
static __device__ __forceinline__ uint32_t slot_w(uint32_t ca, uint32_t cv) {
    return (ca << 4) | __builtin_amdgcn_bitop3_b32(cv >> 11, ca, 0x3cu, 0x28);
}
static __device__ __forceinline__ uint32_t slot_x2(uint32_t aw, uint32_t ca) {
    return __builtin_amdgcn_bitop3_b32(aw, ca, 0xcu, 0x72);
}
static __device__ __forceinline__ uint32_t slot_x(uint32_t aw) { return slot_x2(aw, aw >> 4); }
template <int R> static __device__ __forceinline__ u32x2 hget() {
    u32x2 d;
    asm volatile("v_mov_b64 %0, v[%1:%2]" : "=v"(d) : "n"(R), "n"(R + 1));
    return d;
}
static __device__ __forceinline__ u32x2 hget_at(int r) {
    u32x2 d;
    asm volatile("s_set_gpr_idx_on %2, gpr_idx(SRC0)\n\ts_nop 1\n\tv_mov_b32 %0, v88\n\tv_mov_b32 %1, v89\n\ts_set_gpr_idx_off"
                 : "=&v"(d.x), "=&v"(d.y) : "s"(r));
    return d;
}
static __device__ __forceinline__ void hset_at(int r, u32x2 d) {
    asm volatile("s_set_gpr_idx_on %2, gpr_idx(DST)\n\ts_nop 1\n\tv_mov_b32 v88, %0\n\tv_mov_b32 v89, %1\n\ts_set_gpr_idx_off"
                 :: "v"(d.x), "v"(d.y), "s"(r));
}

template <int S, int D> static __device__ __forceinline__ uint32_t take(uint32_t V, uint32_t u16, uint32_t la, u32x2 nul) {
    const uint32_t a = __builtin_amdgcn_bitop3_b32(V, u16, 15u, 0x54);
    asm volatile("v_mov_b64 v[%4:%4+1], v[%3:%3+1]\n\tv_cmpx_ne_u32 vcc, -16, %0\n\tds_wrxchg_rtn_b64 v[%4:%4+1], %1, %2 offset:%5\n\ts_mov_b64 exec, -1"
                 :: "v"(a), "v"(la), "v"(nul), "n"(HREG(S, D)), "n"(HREG(S, 1 - D)), "n"(S * 256) : "memory", "vcc");
    return a;
}
#define PUT(P) asm volatile("s_bitcmp1_b32 %3, %4\n\ts_cbranch_scc0 1f\n\ts_waitcnt lgkmcnt(%5)\n\tbuffer_store_dwordx4 v[%2:%2+3], %0, %1, 0 offen sc1\n" \
                 "1:\n\ts_bitcmp1_b32 %3, %4+1\n\ts_cbranch_scc0 2f\n\ts_waitcnt lgkmcnt(%5)\n\tbuffer_store_dwordx4 v[%2:%2+3], %0, %1, 0 offen " P "\n\ts_nop 1\n2:" \
                 :: "v"(ad), "s"(rsA), "n"(HREG(S, 0)), "s"(pm), "n"(2 * S), "n"(W) : "memory", "scc")
template <int S, int W, bool WT> static __device__ __forceinline__ void put(u32x4 rsA, uint32_t ad, uint32_t pm) {
    if (WT) PUT("sc1 nt"); else PUT("nt");
}

#define CODE_AHEAD(SYM, NEXT, LANE) ({ u64 ca_; int cb_; \
    asm volatile("s_getpc_b64 vcc\n\ts_add_u32 vcc_lo, vcc_lo, " #SYM "@rel32@lo+4\n\ts_addc_u32 vcc_hi, vcc_hi, " #SYM "@rel32@hi+12\n\t" \
                 "s_mov_b64 %0, vcc\n\ts_max_i32 %1, " #NEXT "-" #SYM ", 0" : "=s"(ca_), "=s"(cb_) :: "scc", "vcc"); \
    __builtin_amdgcn_raw_buffer_load_b32(__builtin_amdgcn_make_buffer_rsrc((void*)ca_, 0, cb_, 0x00020000), ((blockIdx.x >> 3) * 64u + (LANE)) * 64u, 0, 0); })
extern "C" __global__ __launch_bounds__(NT, 4) __attribute__((amdgpu_num_vgpr(HB / 2))) void part_kernel(const int* __restrict__ iin, const int* __restrict__ iout,
                                                  const uint16_t* __restrict__ ang, const uint16_t* __restrict__ freq,
                                                  const u32x2* __restrict__ m, const uint16_t* __restrict__ wdown,
                                                  char* __restrict__ recs,
                                                  int T, int E, int ntile, int nstat, int ks, int lcap) {
    asm volatile("" :: "s"(iin), "s"(iout), "s"(ang), "s"(freq), "s"(m), "s"(wdown), "s"(recs), "s"(T), "s"(E), "s"(ntile), "s"(nstat), "s"(ks), "s"(lcap));
    const int tid0 = threadIdx.x;
    const int wv = __builtin_amdgcn_readfirstlane(tid0 >> 6);
    const int G = T >> 2;
    const int pos = (int)((blockIdx.x & 7) << 5) | (int)(blockIdx.x >> 3);
    float frr;
    unsigned* meta = (unsigned*)(recs - W_RECS);
    const uint32_t gbase = (LBASE + (uint32_t)lcap) * 128u;
    const uint32_t rbytes = gbase + (uint32_t)lcap * 2u;
    const __amdgpu_buffer_rsrc_t rsR = __builtin_amdgcn_make_buffer_rsrc(recs, 0, (int)rbytes, 0x00020000);
    const u32x4 rsA = {(uint32_t)(uintptr_t)recs, (uint32_t)((uintptr_t)recs >> 32) & 0xffffu, rbytes, 0x00020000u};
    const uint32_t g2 = 2u * (uint32_t)ks;
    const uint32_t far = 1u >> ((blockIdx.x >> 3) & 3u);
    const uint32_t SK = SKM + far * (SKH - SKM), SS = SSM + far * (SSH - SSM), UINC = 0x80000001u + SK * 64u;
    const uint32_t spec0 = gbase - 4u;
    auto fixed_tile = [&](int r) __attribute__((always_inline)) -> int { return r * NWG + ((pos + r * 37) & (NWG - 1)); };
    int tl0 = fixed_tile(0), tl1 = fixed_tile(1), tl2;
    uint32_t tk = 0u;
    const __amdgpu_buffer_rsrc_t rsO = __builtin_amdgcn_make_buffer_rsrc((void*)iout, 0, G * 16, 0x00020000);
    const __amdgpu_buffer_rsrc_t rsI = __builtin_amdgcn_make_buffer_rsrc((void*)iin, 0, G * 16, 0x00020000);
    const __amdgpu_buffer_rsrc_t rsG = __builtin_amdgcn_make_buffer_rsrc((void*)ang, 0, G * 8, 0x00020000);
    uint32_t t8 = (uint32_t)tid0 * 8u;
    auto load_one = [&](Raw& r, int tl, int idx) __attribute__((always_inline)) {
        const int j = idx < 4 ? idx >> 1 : idx & 1;
        const uint32_t g0 = (uint32_t)(tl * GR + j * NT);
        __builtin_amdgcn_sched_barrier(0);
        if (idx >= 4) r.a[j] = __builtin_amdgcn_raw_buffer_load_b64(rsG, t8, g0 * 8u, AUX_NT);
        else if (idx & 1) r.i[j] = __builtin_amdgcn_raw_buffer_load_b128(rsI, t8 + t8, g0 * 16u, AUX_NT);
        else r.o[j] = __builtin_amdgcn_raw_buffer_load_b128(rsO, t8 + t8, g0 * 16u, AUX_NT);
        __builtin_amdgcn_sched_barrier(0);
    };
    auto peek = [&](uint32_t at) __attribute__((always_inline)) -> uint32_t {
        return __hip_atomic_load((unsigned*)((char*)meta + at), __ATOMIC_RELAXED, __HIP_MEMORY_SCOPE_AGENT);
    };
    auto ready = [&]() __attribute__((always_inline)) {
        if (blockIdx.x != 0) {
            const uint32_t x = lds_ld(vconst<P_FLAG + 8>());
            while (peek(vconst<M_BEAT * 4>()) == x) __builtin_amdgcn_s_sleep(8);
        }
    };
    auto beat = [&]() __attribute__((always_inline)) {
        (void)__hip_atomic_fetch_add((unsigned*)((char*)meta + vconst<M_BEAT * 4>()), vconst<1>(), __ATOMIC_RELAXED, __HIP_MEMORY_SCOPE_AGENT);
    };
    auto draw = [&](uint32_t at) __attribute__((always_inline)) -> uint32_t {
        return __hip_atomic_fetch_add((unsigned*)((char*)meta + at), vconst<1>(), __ATOMIC_RELAXED, __HIP_MEMORY_SCOPE_AGENT);
    };
    uint32_t la;
    auto tab_at = [&]() __attribute__((always_inline)) -> uint32_t {
        return P_TAB + bfi(0xffu, bfi(0x3cu, t8 >> 2, t8 << 4), t8);
    };
    u32x2 nul;
    uint32_t U[2];
    {
        const uint32_t t = (uint32_t)tid0;
        la = P_BUF + ((t >> 5) << 12) + ((t & 0x18u) << 3) + ((t & 3u) << 4) + ((t & 4u) << 1);
        const uint32_t q = ((t >> 3) ^ (t >> 1 & 2u)) & 3u;
        nul.x = (0x7u >> q) << 31; nul.y = (0xbu >> q) << 31;
        U[0] = (((t >> 1) & 15u) * SS + ((t & 1u) * 64u + (t >> 5)) * SC + BPL(blockIdx.x)) * 128u + (0x80000000u - SK * 64u) + 8u - g2;
        U[1] = U[0] + 32u * SC * 128u;
    }
    auto log_line = [&](bool lg, uint32_t b) __attribute__((always_inline)) -> uint32_t {
        uint32_t lp = 0u;
        if (lg && (t8 & 0x38u) == 0u) {
            ready();
            lp = draw(vconst<M_LOG * 4>());
            __builtin_amdgcn_raw_buffer_store_b16((uint16_t)b, rsR, gbase + lp * 2u, 0, 0);
        }
        return LBASE + (uint32_t)__builtin_amdgcn_ds_bpermute((int)((t8 >> 1) & 0xe0u), (int)lp);
    };
    auto general = [&](int D, bool all, u64 sm) __attribute__((always_inline)) {
        uint32_t todo = __builtin_amdgcn_readfirstlane((uint32_t)sm | (uint32_t)(sm >> 32));
#pragma unroll 1
        while (todo) {
            const uint32_t S = (uint32_t)__builtin_ctz(todo) >> 1;
            todo &= ~(3u << (2u * S));
            pin(t8); pin(la);
            const uint32_t V = lds_ld(P_TAB + (t8 & ~63u) + S * 4u), g = V & 15u;
            const uint32_t col = S * 128u + ((t8 >> 1) & 0x60u) + (t8 >> 8);
            const bool act = all ? V != 0xffffffffu : (V & 0x80000009u) == 9u;
            const int hr = HREG((int)S, D) - HB;
            uint32_t ln = log_line(act && g > 8u + NTAIL, col);
            if (act) {
                const u32x2 N = *(const LDS u32x2*)(uintptr_t)(la + S * 256u);
                *(LDS u32x2*)(uintptr_t)(la + S * 256u) = nul;
                lds_st(P_TAB + (t8 & ~63u) + S * 4u, vconst<0xffffffffu>());
                if (!(g & 1u)) hset_at(hr, N);
                else {
                    uint32_t a, st;
                    if (g < 8u) { a = (V & ~15u) + ((t8 & 0x38u) << 1); st = 8u; }
                    else {
                        if (g <= 8u + NTAIL) ln = TBASE + ((g - 8u) >> 1) * TPL + col * TST + blockIdx.x;
                        a = ln * 128u + (la & 0x38u); st = 64u;
                    }
                    __builtin_amdgcn_raw_buffer_store_b64(hget_at(hr), rsR, a, 0, AUX_SC1);
                    __builtin_amdgcn_raw_buffer_store_b64(N, rsR, a + st, 0, AUX_SC1);
                }
            }
        }
    };
    unsigned epoch = 0;
    u64 pb2 = 0ull;
    u64 fm = 0ull, sm = 0ull;
    auto look = [&](u32x2 c) __attribute__((always_inline)) {
        uint32_t V[2];
        const bool late = __builtin_amdgcn_ballot_w64(((U[0] | U[1]) & 8u) != 0u) != 0ull;
        epoch++;
        asm volatile("v_mov_b32 %0, -1\n\tv_mov_b32 %1, -1\n\t"
                     "v_cmpx_le_u32 %6, %7, %2\n\tv_mov_b32 %0, %4\n\tv_add_u32 %4, %9, %4\n\tv_subrev_u32 %2, %8, %2\n\ts_mov_b64 exec, -1\n\t"
                     "v_cmpx_le_u32 vcc, %8, %3\n\tv_mov_b32 %1, %5\n\tv_add_u32 %5, %9, %5\n\tv_subrev_u32 %3, %8, %3\n\t"
                     "s_or_b64 %6, %6, exec\n\ts_mov_b64 exec, -1"
                     : "=&v"(V[0]), "=&v"(V[1]), "+v"(c.x), "+v"(c.y), "+v"(U[0]), "+v"(U[1]), "=&s"(fm) : "s"(CHV), "n"(CHV), "s"(UINC) : "vcc", "scc");
        *(LDS u32x2*)(uintptr_t)(P_CNT + t8) = c;
        pin(t8);
        const uint32_t tw = tab_at();
        lds_st(tw, V[0]);
        lds_st(tw + 64u, V[1]);
        if (wv == 1) lds_st(P_CNT + NB * 4 - 256 + (t8 >> 1), vconst<0x40000000u>());
        pb2 = __builtin_amdgcn_ballot_w64((int)(V[0] & V[1]) >= 0);
        sm = 0ull;
        if (__builtin_expect(late, 0)) {
            sm = __builtin_amdgcn_ballot_w64((V[0] & 0x80000009u) == 9u || (V[1] & 0x80000009u) == 9u);
#pragma unroll
            for (int e = 0; e < 2; e++)
                if ((V[e] & 0x8000000fu) == 9u + NTAIL) U[e] -= 2u * UINC;
        }
    };
    u32x4 Va, Vb;

    Raw raw[PD];
    {
        const int tid = tid0;
        asm volatile("" ::: "v88", "v127");
        typedef const __attribute__((address_space(4))) u32x4* cptr;
        u32x4 wd0 = ((cptr)(uintptr_t)wdown)[0], wd1 = ((cptr)(uintptr_t)wdown)[1];
        __builtin_amdgcn_sched_barrier(0);
        if (wv == 5) tk = CODE_AHEAD(part_kernel, acc_kernel, (uint32_t)tid & 63u);
        if (tid == 128) tk = peek(vconst<M_BEAT * 4>());
        if (blockIdx.x == 0 && wv == 3 && (tid & 63) < M_BEAT / 64) {
            __hip_atomic_store(meta + (tid & 63) * 64, 0u, __ATOMIC_RELAXED, __HIP_MEMORY_SCOPE_AGENT);
            __hip_atomic_store(meta + (tid & 63) * 64 + 1, 0u, __ATOMIC_RELAXED, __HIP_MEMORY_SCOPE_AGENT);
        }
        const __amdgpu_buffer_rsrc_t rsE = __builtin_amdgcn_make_buffer_rsrc((void*)m, 0, E * 8, 0x00020000);
        u32x2 mv[5];
        mv[4] = __builtin_amdgcn_raw_buffer_load_b64(rsE, (t8 & 511u) * (uint32_t)(E >> 6), 0, 0);
#pragma unroll
        for (int q = 0; q < 4; q++)
            mv[q] = __builtin_amdgcn_raw_buffer_load_b64(rsE, t8 & 511u, ((blockIdx.x >> 2) * 256u + (blockIdx.x & 3u) * 16u + (uint32_t)wv + 64u * q) * 512u, AUX_NT);
#pragma unroll
        for (int d = 0; d < MDL; d++) load_one(raw[1], tl0, d);
        asm volatile("" : "+s"(wd0), "+s"(wd1));
        const u32x2 ci = {CINIT, CINIT};
        *(LDS u32x2*)(uintptr_t)(P_CNT + tid * 8) = ci;
        if (tid < 64) lds_st(P_CNT + (NB + tid) * 4, 0x40000000u);
        if (tid < 8 && tid != 2) lds_st(P_FLAG + tid * 4, tid == 0 ? 0xffffffffu : 0u);
        if (tid == 128) lds_st(P_FLAG + 8, tk);
        const uint32_t lane = (uint32_t)tid & 63u;
        int e0[4];
        {
            float w[16];
#pragma unroll
            for (int i = 0; i < 4; i++) {
                w[2 * i] = bf2f(wd0[i] & 0xffffu); w[2 * i + 1] = bf2f(wd0[i] >> 16);
                w[8 + 2 * i] = bf2f(wd1[i] & 0xffffu); w[8 + 2 * i + 1] = bf2f(wd1[i] >> 16);
            }
            auto dot = [&](u32x2 v, int c) __attribute__((always_inline)) -> f32x2 {
                const f32x2 w0 = {w[c], w[c + 1]}, w1 = {w[4 + c], w[5 + c]}, w2 = {w[8 + c], w[9 + c]}, w3 = {w[12 + c], w[13 + c]};
                return w0 * bf2f(v.x & 0xffffu) + w1 * bf2f(v.x >> 16) + w2 * bf2f(v.y & 0xffffu) + w3 * bf2f(v.y >> 16);
            };
#pragma unroll
            for (int cp = 0; cp < 2; cp++) {
                if (cp == 1) {
#pragma unroll
                    for (int d = MDL; d < MDM; d++) load_one(raw[1], tl0, d);
                }
                if (blockIdx.x >= SLOTI / 64) continue;
                const f32x2 sd = dot(mv[4], 2 * cp);
#pragma unroll
                for (int e = 0; e < 2; e++) {
                    const int ex = (int)(wave_max(__float_as_uint(sd[e]) & 0x7fffffffu) >> 23);
                    e0[2 * cp + e] = __builtin_amdgcn_readfirstlane(ex == 0 ? 0 : min(max(ex - 129, -100), 100));
                }
                const f32x2 sc = {__uint_as_float((uint32_t)(127 - e0[2 * cp]) << 23), __uint_as_float((uint32_t)(127 - e0[2 * cp + 1]) << 23)};
                u32x4 o;
                u16x2 mx = {0, 0};
                f32x2 sq = {0.f, 0.f}, sg = {0.f, 0.f};
#pragma unroll
                for (int q = 0; q < 4; q++) {
                    const f32x2 y = dot(mv[q], 2 * cp) * sc;
                    const uint32_t hb = pk_bf16(y.x, y.y);
                    const f32x2 x = {__uint_as_float(hb << 16), __uint_as_float(hb & 0xffff0000u)};
                    o[q] = __builtin_bit_cast(uint32_t, __builtin_amdgcn_cvt_pkrtz(x.x, x.y));
                    mx = __builtin_elementwise_max(mx, __builtin_bit_cast(u16x2, hb & 0x7fff7fffu));
                    sq += x * x;
                    sg += x;
                }
                *(LDS u32x4*)(uintptr_t)(P_BUF + (cp * 64u + lane) * 272u + wv * 16) = o;
#pragma unroll
                for (int e = 0; e < 2; e++) {
                    const u32x4 st = {wave_max(mx[e]), __float_as_uint(wave_sum(sq[e])), __float_as_uint(wave_sum(sg[e])), 0u};
                    if (lane == 0u) *(LDS u32x4*)(uintptr_t)(P_LST + ((2 * cp + e) * 16 + wv) * 16) = st;
                }
            }
        }
        __builtin_amdgcn_sched_barrier(0);
        const uint16_t fq = freq[0];
#pragma unroll
        for (int d = MDM; d < 6; d++) load_one(raw[1], tl0, d);
#pragma unroll
        for (int d = 0; d < 6; d++)
            if (((LPOS >> (4 * d)) & 15) < 4) load_one(raw[0], tl1, d);
        __syncthreads();
        u32x4 mo[2];
#pragma unroll
        for (int cp = 0; cp < 2; cp++) mo[cp] = *(const LDS u32x4*)(uintptr_t)(P_BUF + (cp * 64u + (t8 >> 7)) * 272u + ((t8 + t8) & 0xf0u));
        if (blockIdx.x == 0 && wv == 3) asm volatile("s_waitcnt vmcnt(0)" ::: "memory");
        __syncthreads();
#pragma unroll
        for (int s = 0; s < 16; s++) *(LDS u32x2*)(uintptr_t)(la + s * 256) = nul;
        asm volatile("" : "+v"(raw[1].o[0]), "+v"(raw[1].o[1]), "+v"(raw[1].i[0]), "+v"(raw[1].i[1]), "+v"(raw[1].a[0]), "+v"(raw[1].a[1]));
        asm volatile("s_nop 0\n\tv_readfirstlane_b32 %0, %1" : "=s"(frr) : "v"(bf2f(fq) * 0.15915494309189535f));
        if (blockIdx.x < SLOTI / 64) {
            const __amdgpu_buffer_rsrc_t rsM = __builtin_amdgcn_make_buffer_rsrc((void*)(meta + W_MDC / 4), 0, (int)(128u * SLOTI * 4u), 0x00020000);
#pragma unroll
            for (int cp = 0; cp < 2; cp++) {
                const uint32_t row = cp * 64u + (t8 >> 7), c16 = (t8 + t8) & 0xf0u;
                __builtin_amdgcn_raw_buffer_store_b128(mo[cp], rsM, row * (SLOTI * 4u) + c16 + blockIdx.x * 256u, 0, AUX_SC1);
            }
            if (wv == 0) {
                const u32x4 st = *(const LDS u32x4*)(uintptr_t)(P_LST + t8 * 2u);
                const float sm1 = row_sum(__uint_as_float(st.z));
                const u32x2 r = {row_max(st.x) | (row_max(st.x ? 0xffffu - st.x : 0u) << 16), __float_as_uint(row_sum(__uint_as_float(st.y)))};
                if ((lane & 15u) == 15u) {
                    ((u32x2*)(meta + W_STAT / 4))[(lane >> 4) * NWG + blockIdx.x] = r;
                    meta[W_SUM / 4 + (lane >> 4) * NWG + blockIdx.x] = __float_as_uint(sm1);
                }
                if (blockIdx.x == 0 && lane < 4u) meta[M_E0 + lane] = (uint32_t)(lane == 0u ? e0[0] : lane == 1u ? e0[1] : lane == 2u ? e0[2] : e0[3]);
            }
        }
    }
    pin(la);

    uint32_t w[8], aw[8], xv[8];
    u64 nm[8];
    auto later2 = [&](int k) __attribute__((always_inline)) {
        uint32_t x0, x1;
        asm volatile("v_lshrrev_b32 %0, 4, %2\n\tv_lshrrev_b32 %1, 4, %5\n\tv_bitop3_b32 %0, %2, %0, 12 bitop3:0x72\n\tv_bitop3_b32 %1, %5, %1, 12 bitop3:0x72\n\t"
                     "s_not_b64 exec, %8\n\tds_write_b32 %2, %3 offset:%10\n\tds_or_b32 %0, %4 offset:%10\n\t"
                     "s_not_b64 exec, %9\n\tds_write_b32 %5, %6 offset:%10\n\tds_or_b32 %1, %7 offset:%10\n\ts_mov_b64 exec, -1"
                     : "=&v"(x0), "=&v"(x1)
                     : "v"(aw[k]), "v"(w[k]), "v"(xv[k]), "v"(aw[k + 1]), "v"(w[k + 1]), "v"(xv[k + 1]), "s"(nm[k]), "s"(nm[k + 1]), "n"(P_BUF) : "memory", "scc");
    };
    auto drain = [&]() __attribute__((always_inline)) {
        if (wv == 1) {
            const uint32_t q = (epoch + 1u) & 3u;
            const uint32_t n = __builtin_amdgcn_readfirstlane(lds_ld(vconst<P_FLAG + 16>() + q * 4u));
            if (__builtin_expect(n != 0u, 0)) {
                pin(t8);
                const uint32_t l = (t8 >> 3) & 63u;
                if (l < min(n, (uint32_t)LCAP)) {
                    const u32x4 e = *(const LDS u32x4*)(uintptr_t)(P_LST + (q * LCAP + l) * 16u);
                    lds_st(P_BUF + e.x, e.y);
                    lds_or(P_BUF + slot_x(e.x), e.z);
                }
                lds_st(vconst<P_FLAG + 16>() + q * 4u, vconst<0>());
            }
        }
    };
    auto again = [&](int D) __attribute__((always_inline)) {
        do {
            general(D, true, fm);
            __syncthreads();
            later2(0); later2(2); later2(4); later2(6);
            drain();
#pragma unroll
            for (int k = 0; k < 8; k++) {
                pin(aw[k]);
                bool due = false;
                if (aw[k] & 0x80000000u) {
                    xv[k] -= CHV;
                    if (xv[k] < CHV) {
                        const uint32_t f = (aw[k] >> 16) & 0x3ffu;
                        aw[k] = slot_w(aw[k] & 0xffffu, xv[k]);
                        xv[k] = f << ((xv[k] >> 8) & 31u);
                        due = true;
                    } else lds_st(vconst<P_FLAG>(), epoch + 1u);
                }
                nm[k] = ~__builtin_amdgcn_ballot_w64(due);
            }
            look(*(const LDS u32x2*)(uintptr_t)(P_CNT + t8));
            asm volatile("s_waitcnt lgkmcnt(0)" ::: "memory");
            __syncthreads();
        } while (__builtin_amdgcn_readfirstlane(lds_ld(vconst<P_FLAG>())) == epoch);
    };
    auto round = [&](auto DD, auto LL, Raw& rc, Raw& rn, int rd) __attribute__((always_inline)) {
        constexpr int D = decltype(DD)::value;
        constexpr bool LAST = decltype(LL)::value == 1, FIRST = decltype(LL)::value == 2;
        const int ng = min(GR, G - tl0 * GR);
        auto hook = [&](int p) __attribute__((always_inline)) {
#pragma unroll
            for (int q = 0; q < 6; q++)
            {
                const int lp = (LPOS >> (4 * q)) & 15;
                if (!LAST && lp < 4 && lp == p) load_one(rc, tl2, q);
                if (!LAST && lp >= 4 && (lp < 8 || lp >= 12 ? lp : lp - 8) == p) load_one(rn, tl1, q);
            }
        };
        uint32_t wn[8], ca[8], px[4], cv[8];
        float mx;
        float cs[2], cm[2] = {0.f, 0.f};
        auto convA = [&](int q) __attribute__((always_inline)) {
            if (LAST) return;
            const uint32_t a = rc.a[q >> 1][q & 1];
            const f32x2 av = {__uint_as_float(a << 16), __uint_as_float(a & 0xffff0000u)};
            f32x2 rv;
            asm("v_pk_mul_f32 %0, %1, %2 op_sel_hi:[1,0]" : "=v"(rv) : "v"(av), "s"((u64)__float_as_uint(frr)));
            mx = __builtin_elementwise_maximum(__builtin_elementwise_maximum(__builtin_fabsf(rv.x), __builtin_fabsf(rv.y)), (q & 1) ? mx : 0.f);
            cs[0] = __builtin_amdgcn_cosf(rv.x); cs[1] = __builtin_amdgcn_cosf(rv.y);
            asm volatile("" : "+v"(mx), "+v"(cs[0]), "+v"(cs[1]));
            if (FIRST) {
                const float c0 = fminf(fmaxf(cs[0], -1.f), 1.f), c1 = fminf(fmaxf(cs[1], -1.f), 1.f);
                cm[0] += c0 + c1; cm[1] += c0 * c0 + c1 * c1;
                if (q == 3 && wv == 4) {
                    const float a0 = wave_sum(cm[0]), a1 = wave_sum(cm[1]);
                    if ((t8 & 0x1f0u) == 0u) meta[W_SUM / 4 + (4 + ((t8 >> 3) & 1u)) * NWG + blockIdx.x] = __float_as_uint((t8 & 8u) ? a1 : a0);
                }
            }
            __builtin_amdgcn_sched_barrier(0);
        };
        auto convB = [&](int k) __attribute__((always_inline)) {
            if (LAST) return;
            const uint32_t o = rc.o[k >> 2][k & 3], i = rc.i[k >> 2][k & 3];
            wn[k] = (__builtin_amdgcn_alignbit(bfi(63u, o, o >> 5), __float_as_uint(cs[k & 1]), 30u) << 14) | (i >> 6);
            ca[k] = (bfi(63u, i, o) << 2) & 0x1ffcu;
            pin(wn[k]); pin(ca[k]);
            __builtin_amdgcn_sched_barrier(0);
        };
        auto convD = [&](int q) __attribute__((always_inline)) {
            if (LAST) return;
            asm volatile("v_cvt_pk_f16_f32 %0, |%1|, |%2|\n\tv_pk_mul_f16 %0, %0, %3 op_sel_hi:[1,0]" : "=v"(px[q]) : "v"(cs[0]), "v"(cs[1]), "s"(0x3ffu));
        };
        uint32_t cst;
        auto check = [&](int j) __attribute__((always_inline)) {
            if (LAST) return;
            if (__builtin_expect((__builtin_amdgcn_ballot_w64(!(mx <= RMAXREV)) | (u64)(uint32_t)(GR - ng)) != 0ull, 0)) {
                pin(t8);
                asm volatile("" : "+v"(rc.a[j]));
                const int lg = j * NT + (int)(t8 >> 3);
#pragma unroll
                for (int k = 0; k < 4; k++) {
                    const uint32_t a = (k & 1) ? (rc.a[j][k >> 1] >> 16) : (rc.a[j][k >> 1] & 0xffffu);
                    const bool nrm = __builtin_fabsf(frr * bf2f(a)) <= RMAXREV;
                    if (!(lg < ng && nrm)) ca[j * 4 + k] = NB * 4u + ((t8 >> 1) & 0xfcu);
                    if (lg < ng && !nrm) {
                        ready();
                        __builtin_amdgcn_raw_buffer_store_b32((uint32_t)((tl0 * GR + lg) * 4 + k), rsR, spec0 - draw(vconst<M_LOG * 4 + 4>()) * 4u, 0, 0);
                    }
                }
            }
            cst = vconst<CSTEP>();
        };
        auto ticket = [&](int k) __attribute__((always_inline)) {
            if (LAST) return;
            cv[k] = lds_add(P_CNT + ca[k], cst);
            __builtin_amdgcn_sched_barrier(0);
        };
        auto insert = [&](int j) __attribute__((always_inline)) {
            const int k0 = 4 * j;
            const bool odd = __builtin_amdgcn_ballot_w64(max(max(cv[k0], cv[k0 + 1]), max(cv[k0 + 2], cv[k0 + 3])) >= 2u * CHV) != 0ull;
#pragma unroll
            for (int k = k0; k < k0 + 4; k += 2) {
                uint32_t x0, x1;
                asm volatile("v_lshrrev_b32 %6, 11, %8\n\tv_lshrrev_b32 %7, 11, %9\n\t"
                             "v_bitop3_b32 %6, %6, %10, 60 bitop3:0x28\n\tv_bitop3_b32 %7, %7, %11, 60 bitop3:0x28\n\t"
                             "v_lshl_or_b32 %0, %10, 4, %6\n\tv_lshl_or_b32 %1, %11, 4, %7\n\t"
                             "v_lshlrev_b32_sdwa %2, %8, %12 dst_sel:DWORD dst_unused:UNUSED_PAD src0_sel:BYTE_1 src1_sel:WORD_0\n\t"
                             "v_lshlrev_b32_sdwa %3, %9, %12 dst_sel:DWORD dst_unused:UNUSED_PAD src0_sel:BYTE_1 src1_sel:WORD_1\n\t"
                             "v_bitop3_b32 %6, %0, %10, 12 bitop3:0x72\n\tv_bitop3_b32 %7, %1, %11, 12 bitop3:0x72\n\t"
                             "v_cmpx_gt_u32 %4, %15, %8\n\tds_write_b32 %0, %13 offset:%16\n\tds_or_b32 %6, %2 offset:%16\n\ts_mov_b64 exec, -1\n\t"
                             "v_cmpx_gt_u32 %5, %15, %9\n\tds_write_b32 %1, %14 offset:%16\n\tds_or_b32 %7, %3 offset:%16\n\ts_mov_b64 exec, -1"
                             : "=&v"(aw[k]), "=&v"(aw[k + 1]), "=&v"(xv[k]), "=&v"(xv[k + 1]), "=&s"(nm[k]), "=&s"(nm[k + 1]), "=&v"(x0), "=&v"(x1)
                             : "v"(cv[k]), "v"(cv[k + 1]), "v"(ca[k]), "v"(ca[k + 1]), "v"(px[k >> 1]), "v"(wn[k]), "v"(wn[k + 1]), "s"(CHV), "n"(P_BUF) : "memory");
                w[k] = wn[k]; w[k + 1] = wn[k + 1];
                if (__builtin_expect(odd, 0)) {
#pragma unroll
                    for (int q = k; q < k + 2; q++) {
                        nm[q] |= __builtin_amdgcn_ballot_w64(cv[q] >= 2u * CHV);
                        if (cv[q] >= 2u * CHV && cv[q] < 0x40000000u) {
                            const uint32_t lq = (epoch + 2u) & 3u;
                            uint32_t p = LCAP;
                            if (cv[q] < 3u * CHV) p = lds_add(vconst<P_FLAG + 16>() + lq * 4u, vconst<1>());
                            if (p < LCAP) {
                                const u32x2 e = {aw[q], w[q]};
                                *(LDS u32x2*)(uintptr_t)(P_LST + (lq * LCAP + p) * 16u) = e;
                                lds_st(P_LST + (lq * LCAP + p) * 16u + 8u, xv[q]);
                            } else {
                                aw[q] = ca[q] | ((q & 1) ? (px[q >> 1] & 0xffff0000u) : (px[q >> 1] << 16)) | 0x80000000u;
                                xv[q] = cv[q] - CHV;
                                lds_st(vconst<P_FLAG>(), epoch);
                            }
                        }
                    }
                }
            }
        };
        constexpr int P0 = D ? 0 : 3, P1 = D ? 1 : 2, P2 = D ? 2 : 1, P3 = D ? 3 : 0;
        const uint32_t fl = lds_ld(vconst<P_FLAG>());
        if (!LAST) asm volatile("" : "+v"(rc.o[0]), "+v"(rc.o[1]), "+v"(rc.i[0]), "+v"(rc.i[1]), "+v"(rc.a[0]), "+v"(rc.a[1]));
        if (!LAST && (rd + 4 >= nstat || tl1 >= ntile) && wv == 2) {
            pin(tk);
            if ((t8 & 511u) == 0u) {
                if (tl1 >= ntile) {
                    if (rd + 3 <= nstat) ready();
                    (void)draw(vconst<M_MD * 4>());
                } else if (rd + 4 == nstat) tk = peek(vconst<M_BEAT * 4>());
                else {
                    if (rd + 3 == nstat) {
                        if (tk == lds_ld(vconst<P_FLAG + 8>())) ready();
                        tk = 0u;
                    }
                    lds_st(vconst<P_FLAG + 4>(), tk);
                    if ((int)tk < ntile - nstat * NWG) tk = draw(vconst<M_TILE * 4>());
                }
            }
        }
        if (!LAST && blockIdx.x == 0 && wv == 3 && (t8 & 511u) == 0u) beat();
        hook(12);
        pin(t8);
        const uint32_t tr = P_TAB + (t8 & ~63u), u16 = (t8 + t8) & 0x70u;
        convA(0);
        convB(0);
        const bool ag = __builtin_amdgcn_readfirstlane(fl) == epoch;
        if (!FIRST && __builtin_expect(LAST || ag || sm != 0ull, 0)) {
            if (ag) again(D);
            general(D, false, sm);
            Va = *(const LDS u32x4*)(uintptr_t)(P_TAB + (t8 & ~63u) + P0 * 16);
            Vb = *(const LDS u32x4*)(uintptr_t)(P_TAB + (t8 & ~63u) + P1 * 16);
            asm volatile("" : "+v"(Va), "+v"(Vb));
        }
        uint32_t pq = (uint32_t)pb2 | (uint32_t)(pb2 >> 32);
        pq = (pq | (pq >> 1)) & 0x55555555u;
        pq = __builtin_amdgcn_readfirstlane(pq + (pq & (far * NEARM)));
        uint32_t ad[8];
        if (!FIRST) ad[0] = take<4 * P0 + 0, D>(Va[0], u16, la, nul);
        convB(1);
        if (!FIRST) ad[1] = take<4 * P0 + 1, D>(Va[1], u16, la, nul);
        convD(0);
        convA(1);
        if (!FIRST) ad[2] = take<4 * P0 + 2, D>(Va[2], u16, la, nul);
        convB(2);
        hook(13);
        if (!FIRST) ad[3] = take<4 * P0 + 3, D>(Va[3], u16, la, nul);
        convB(3);
        if (!FIRST) put<4 * P0 + 0, LAST ? 3 : 3, LAST>(rsA, ad[0], pq);
        if (!FIRST) ad[4] = take<4 * P1 + 0, D>(Vb[0], u16, la, nul);
        convD(1);
        check(0);
        ticket(0);
        hook(4);
        if (!FIRST) put<4 * P0 + 1, LAST ? 3 : 4, LAST>(rsA, ad[1], pq);
        if (!FIRST) ad[5] = take<4 * P1 + 1, D>(Vb[1], u16, la, nul);
        convA(2);
        ticket(1);
        if (!FIRST) put<4 * P0 + 2, LAST ? 3 : 5, LAST>(rsA, ad[2], pq);
        if (!FIRST) ad[6] = take<4 * P1 + 2, D>(Vb[2], u16, la, nul);
        convB(4);
        ticket(2);
        if (!FIRST) put<4 * P0 + 3, LAST ? 3 : 6, LAST>(rsA, ad[3], pq);
        if (!FIRST) Va = *(const LDS u32x4*)(uintptr_t)(tr + P2 * 16);
        __builtin_amdgcn_sched_barrier(0);
        if (!FIRST) ad[7] = take<4 * P1 + 3, D>(Vb[3], u16, la, nul);
        convB(5);
        ticket(3);
        SETP(2);
        if (!FIRST) put<4 * P1 + 0, LAST ? 4 : 8, LAST>(rsA, ad[4], pq);
        if (!FIRST) ad[0] = take<4 * P2 + 0, D>(Va[0], u16, la, nul);
        convD(2);
        convA(3);
        hook(5);
        if (!FIRST) put<4 * P1 + 1, LAST ? 4 : 7, LAST>(rsA, ad[5], pq);
        if (!FIRST) ad[1] = take<4 * P2 + 1, D>(Va[1], u16, la, nul);
        convB(6);
        if (!FIRST) put<4 * P1 + 2, LAST ? 4 : 6, LAST>(rsA, ad[6], pq);
        if (!FIRST) ad[2] = take<4 * P2 + 2, D>(Va[2], u16, la, nul);
        convB(7);
        if (!FIRST) put<4 * P1 + 3, LAST ? 3 : 4, LAST>(rsA, ad[7], pq);
        if (!FIRST) Vb = *(const LDS u32x4*)(uintptr_t)(tr + P3 * 16);
        __builtin_amdgcn_sched_barrier(0);
        if (!FIRST) ad[3] = take<4 * P2 + 3, D>(Va[3], u16, la, nul);
        convD(3);
        check(1);
        ticket(4);
        SETP(1);
        hook(6);
        if (!FIRST) put<4 * P2 + 0, LAST ? 4 : 5, LAST>(rsA, ad[0], pq);
        if (!FIRST) ad[4] = take<4 * P3 + 0, D>(Vb[0], u16, la, nul);
        ticket(5);
        if (!FIRST) put<4 * P2 + 1, LAST ? 4 : 6, LAST>(rsA, ad[1], pq);
        if (!FIRST) ad[5] = take<4 * P3 + 1, D>(Vb[1], u16, la, nul);
        ticket(6);
        if (!FIRST) put<4 * P2 + 2, LAST ? 4 : 7, LAST>(rsA, ad[2], pq);
        if (!FIRST) ad[6] = take<4 * P3 + 2, D>(Vb[2], u16, la, nul);
        ticket(7);
        SETP(0);
        if (!FIRST) put<4 * P2 + 3, LAST ? 3 : 7, LAST>(rsA, ad[3], pq);
        if (!FIRST) ad[7] = take<4 * P3 + 3, D>(Vb[3], u16, la, nul);
        hook(7);
        SETP(3);
        asm volatile("s_waitcnt lgkmcnt(0)" ::: "memory");
        __syncthreads();
        u32x2 cn;
        uint32_t tv;
        if (!LAST) { cn = *(const LDS u32x2*)(uintptr_t)(P_CNT + t8); tv = lds_ld(vconst<P_FLAG + 4>()); }
        __builtin_amdgcn_sched_barrier(0);
        if (!FIRST) later2(0);
        if (!FIRST) put<4 * P3, 15, LAST>(rsA, ad[4], pq);
        if (!FIRST) later2(2);
        if (!FIRST) put<4 * P3 + 1, 15, LAST>(rsA, ad[5], pq);
        if (!FIRST) drain();
        if (!LAST) {
            tl2 = rd + 2 < nstat ? fixed_tile(rd + 2) : nstat * NWG + (int)__builtin_amdgcn_readfirstlane(tv);
            look(cn);
        }
        hook(0);
        SETP(2);
        if (!FIRST) later2(4);
        if (!FIRST) put<4 * P3 + 2, 15, LAST>(rsA, ad[6], pq);
        if (!FIRST) later2(6);
        if (!FIRST) put<4 * P3 + 3, 15, LAST>(rsA, ad[7], pq);
        if (!LAST) {
            hook(1);
            SETP(1);
            insert(0);
            hook(2);
            SETP(0);
            pin(t8);
            Va = *(const LDS u32x4*)(uintptr_t)(P_TAB + (t8 & ~63u) + P3 * 16);
            Vb = *(const LDS u32x4*)(uintptr_t)(P_TAB + (t8 & ~63u) + P2 * 16);
            __builtin_amdgcn_sched_barrier(0);
            insert(1);
            hook(3);
            SETP(3);
            asm volatile("s_waitcnt lgkmcnt(0)" ::: "memory");
            __syncthreads();
            tl0 = tl1; tl1 = tl2;
        }
    };
#define HMV(A, B) "v_mov_b64 v[" #A ":" #A "+1], v[" #B ":" #B "+1]\n\t"
#define HMV4(A) HMV(A + 8, A + 6) HMV(A + 6, A + 4) HMV(A + 4, A + 2) HMV(A + 2, A)
    round(IC<1>(), IC<2>(), raw[1], raw[0], 0);
    int rd = 1;
#pragma unroll 1
    for (; tl1 < ntile; rd += 2) {
        round(IC<0>(), IC<0>(), raw[0], raw[1], rd);
        round(IC<1>(), IC<0>(), raw[1], raw[0], rd + 1);
    }
    if (tl0 >= ntile) asm volatile(HMV4(88) HMV4(98) HMV4(108) HMV4(118));
    asm volatile("" : "+s"(tl0));
    if (tl0 < ntile) round(IC<0>(), IC<0>(), raw[0], raw[1], rd);
    if (wv == 5) tk = CODE_AHEAD(acc_kernel, gen_scatter_kernel, (t8 >> 3) & 63u);
    round(IC<1>(), IC<1>(), raw[1], raw[0], 0);
    if (__builtin_amdgcn_readfirstlane(lds_ld(P_FLAG + 16 + ((epoch + 2u) & 3u) * 4u)) != 0u) {
        asm volatile("s_waitcnt lgkmcnt(0)" ::: "memory");
        __syncthreads();
        look(*(const LDS u32x2*)(uintptr_t)(P_CNT + t8));
        general(0, true, fm);
        __syncthreads();
        drain();
    }
    {
        uint32_t* tcnt = meta + W_TCNT / 4;
        const u32x2 c = *(const LDS u32x2*)(uintptr_t)(P_CNT + t8);
        const uint32_t tw = tab_at();
#pragma unroll
        for (int e = 0; e < 2; e++) {
            const uint32_t g = U[e] & 15u;
            lds_st(tw + e * 64u, g >= 8u ? 0x80000000u : ((U[e] + ((g & 1u) ? 0u : SK * 64u)) & 0x7fffff81u) | (((7u - g) << 1) & 12u));
            *(LDS uint8_t*)(uintptr_t)(P_CNT + (t8 & ~511u) + (((tw - P_TAB) >> 2) & 127u) + e * 16u) =
                (uint8_t)(g < 8u ? 0u : ((g - 8u) >> 1) + (((c[e] + 2u * CSTEP) >> 15) << 2) + (g & 1u) * 0x50u);
        }
        asm volatile("s_waitcnt lgkmcnt(0)" ::: "memory");
        __syncthreads();
        const uint32_t u16 = (t8 + t8) & 0x70u;
        u32x4 En[4];
#pragma unroll
        for (int p = 0; p < 4; p++) En[p] = *(const LDS u32x4*)(uintptr_t)(P_TAB + (t8 & ~63u) + p * 16u);
        const u32x4 cb = *(const LDS u32x4*)(uintptr_t)(P_CNT + (t8 & ~511u) + ((t8 >> 2) & 0x70u));
        u32x2 N[16];
#pragma unroll
        for (int s = 0; s < 16; s++) N[s] = *(const LDS u32x2*)(uintptr_t)(la + s * 256u);
        if (u16 == 0u) *(u32x4*)(tcnt + blockIdx.x * 512u + (t8 >> 4)) = cb;
        const uint32_t sg = P_BUF + ((t8 & ~63u) << 4);
        uint32_t ps;
        auto rest = [&](auto SL) __attribute__((always_inline)) {
            constexpr int s = decltype(SL)::value;
            const uint32_t en = En[s >> 2][s & 3], by = cb[s >> 2] >> ((s & 3) * 8);
            const u32x2 Hr = hget<HREG(s, 0)>();
            const uint32_t at = ps + ((la & 0x38u) ^ ((s & 3) << 4));
            *(LDS u32x2*)(uintptr_t)at = Hr;
            *(LDS u32x2*)(uintptr_t)(at + (by & 0x40u)) = N[s];
            ps += (by & 0x3cu) << 2;
            const u32x2 Hs = (en & 1u) ? Hr : nul;
            const u32x4 d = {Hs.x, Hs.y, N[s].x, N[s].y};
            uint32_t a = __builtin_amdgcn_bitop3_b32(en, u16, 15u, 0x54);
            {
                const u32x4 rs_ = rsA;
                const uint32_t far_ = ((NEARM >> (2 * s)) & 1u) ? far : 0u;
                asm volatile("s_cmp_eq_u32 %3, 0\n\ts_cbranch_scc0 1f\n\tbuffer_store_dwordx4 %0, %1, %2, 0 offen sc1\n\ts_branch 2f\n"
                             "1:\n\tbuffer_store_dwordx4 %0, %1, %2, 0 offen sc1 nt\n2:\n\ts_nop 1" :: "v"(d), "v"(a), "s"(rs_), "s"(far_) : "memory", "scc");
            }
            if (__builtin_expect(__builtin_amdgcn_ballot_w64((en & 12u) != 0u) != 0ull, 0)) {
                const u32x4 dn = {nul.x, nul.y, nul.x, nul.y};
#pragma unroll 1
                for (uint32_t k = (en >> 2) & 3u; k > 0u; k--) {
                    a += SK * 128u;
                    __builtin_amdgcn_raw_buffer_store_b128(dn, rsR, a, 0, AUX_SC1);
                }
            }
        };
        auto pack = [&](int p) __attribute__((always_inline)) {
            const uint32_t a = (SBASE + ((((t8 >> 1) & 0x60u) + (t8 >> 8)) * 2u + p) * (NWG * 9u) + blockIdx.x * 9u) * 128u + u16;
            asm volatile("" ::: "memory");
#pragma unroll 1
            for (uint32_t q = 0; __builtin_amdgcn_ballot_w64(sg + q < ps) != 0ull; q += 256u) {
                const u32x4 d0 = *(const LDS u32x4*)(uintptr_t)(sg + q + u16), d1 = *(const LDS u32x4*)(uintptr_t)(sg + q + 128u + u16);
                __builtin_amdgcn_raw_buffer_store_b128(d0, rsR, sg + q < ps ? a + q : 0xffffff00u, 0, AUX_SC1);
                __builtin_amdgcn_raw_buffer_store_b128(d1, rsR, sg + q + 128u < ps ? a + q + 128u : 0xffffff00u, 0, AUX_SC1);
            }
            asm volatile("" ::: "memory");
        };
        asm volatile("" : "+v"(N[0]), "+v"(N[1]), "+v"(N[2]), "+v"(N[3]), "+v"(N[4]), "+v"(N[5]), "+v"(N[6]), "+v"(N[7]) :: "memory");
        asm volatile("" : "+v"(N[8]), "+v"(N[9]), "+v"(N[10]), "+v"(N[11]), "+v"(N[12]), "+v"(N[13]), "+v"(N[14]), "+v"(N[15]) :: "memory");
        ps = sg;
        rest(IC<0>()); rest(IC<1>()); rest(IC<2>()); rest(IC<3>()); rest(IC<4>()); rest(IC<5>()); rest(IC<6>()); rest(IC<7>());
        pack(0);
        ps = sg;
        rest(IC<8>()); rest(IC<9>()); rest(IC<10>()); rest(IC<11>()); rest(IC<12>()); rest(IC<13>()); rest(IC<14>()); rest(IC<15>());
        pack(1);
    }
    asm volatile("" :: "v"(tk));
    if (blockIdx.x == 0 && t8 == 1536u) {
        do {
            beat();
            __builtin_amdgcn_s_sleep(8);
        } while (peek(vconst<M_MD * 4>()) < NWG);
    }
}

#define A_MD 0
#define A_ACC (SLOTI * 4)
#define A_FLAG (A_ACC + SLOTO * 4)
#define A_LDS (A_FLAG + 512)
static_assert(SLOTI * 4 == 3 * NT * 16 + 1024, "md staging assumes three full vectors and 256 dwords");
#define BIAS 0x40004000u
#define NFAST 8
#define NLD 10
#define MDF 0x0a11333
#define MDP 0xaaaaa
#define HP 2
#define SHARE (SLOTO / 8)
#define SPINCAP 4096u
#define ZSIG 9.0f
#define HSLOW 0x80008000u
#define GAS __attribute__((address_space(1)))

extern "C" __global__ __launch_bounds__(NT, 4) __attribute__((amdgpu_num_vgpr(53))) void acc_kernel(char* ws, int T, int E, int ks, int lcap, float zf, int pad,
                                                 const uint16_t* __restrict__ centers, const uint16_t* __restrict__ wrbf,
                                                 const uint16_t* __restrict__ wsbf, const uint16_t* __restrict__ wup,
                                                 const u32x2* __restrict__ m, const uint16_t* __restrict__ dist, u32x2* __restrict__ out,
                                                 const int* __restrict__ iin, const int* __restrict__ iout, const uint16_t* __restrict__ ang,
                                                 const uint16_t* __restrict__ wdown, const uint16_t* __restrict__ freq) {
    const char* recs = ws + W_RECS;
    const u32x4* tcnt = (const u32x4*)(ws + W_TCNT);
    const uint32_t* mdc = (const uint32_t*)(ws + W_MDC);
    const u32x4* stat = (const u32x4*)(ws + W_STAT);
    uint32_t* partial = (uint32_t*)(ws + W_PART);
    unsigned* meta = (unsigned*)ws;
    extern __shared__ __attribute__((aligned(16))) char smem[];
    unsigned* acc = (unsigned*)(smem + A_ACC);
    unsigned* s_flag = (unsigned*)(smem + A_FLAG);
    const int tid = threadIdx.x;
    const int lane = tid & 63;
    const int wv = __builtin_amdgcn_readfirstlane(tid >> 6);
    const int cp = (blockIdx.x >> 3) & 1;
    const int h = blockIdx.x & 3;
    const int j = ((blockIdx.x >> 2) & 1) * 16 + (blockIdx.x >> 4);
    u32x4 tc;
    const __amdgpu_buffer_rsrc_t rsM = __builtin_amdgcn_make_buffer_rsrc((void*)mdc, 0, (int)(128u * SLOTI * 4u), 0x00020000);

    u32x4 pm[4];
    u32x4 ring[NLD];
    auto load_md = [&](int ff, int pl, int mdp) __attribute__((always_inline)) {
        const uint32_t so = (uint32_t)(cp * 64 + ff * 4 + h) * (SLOTI * 4u);
#pragma unroll
        for (int v = 0; v < 3; v++)
            if (pl < 0 || pl == ((mdp >> (4 * v)) & 15)) pm[v] = __builtin_amdgcn_raw_buffer_load_b128(rsM, (uint32_t)tid * 16u, so + (uint32_t)v * (NT * 16u), 0);
        if ((pl < 0 || pl == (mdp & 15)) && wv == 0) pm[3] = __builtin_amdgcn_raw_buffer_load_b128(rsM, (uint32_t)tid * 16u, so + 3u * NT * 16u, 0);
        __builtin_amdgcn_sched_barrier(0);
    };
    const uint32_t lf = (uint32_t)(tid >> 9) * (SKH * 128u) + (uint32_t)(tid & 0x1e0) * 16u + (uint32_t)(tid & 12) * 32u + (uint32_t)(tid & 16) * 4u + (uint32_t)(tid & 3) * 16u;
    const uint32_t lp = (uint32_t)(tid >> 8) * (SKM * 128u) + (uint32_t)((tid >> 2) & 63) * 128u + (uint32_t)(tid & 3) * 16u;
    auto load_main = [&](int i, int ff, bool near = false) __attribute__((always_inline)) -> u32x4 {
        const uint32_t q = (uint32_t)(((i >> 1) - HP) & 3), SK = q ? SKM : SKH, SS = q ? SSM : SSH;
        const __amdgpu_buffer_rsrc_t rs = __builtin_amdgcn_make_buffer_rsrc((void*)(recs + (q * SQ + (uint32_t)ff * SS + (uint32_t)(h * 32 + j) * SC) * 128u), 0, ks * (int)(SK * 128u), 0x00020000);
        if (q == 0u && near) return __builtin_amdgcn_raw_buffer_load_b128(rs, lf, (i & 1) * (int)(2u * SK * 128u), 0);
        if (q == 0u) return __builtin_amdgcn_raw_buffer_load_b128(rs, lf, (i & 1) * (int)(2u * SK * 128u), AUX_NT);
        return __builtin_amdgcn_raw_buffer_load_b128(rs, lp + (uint32_t)(i & 1) * 64u, 0, 0);
    };
    const __amdgpu_buffer_rsrc_t rsT = __builtin_amdgcn_make_buffer_rsrc((void*)recs, 0, (int)(LBASE * 128u), 0x00020000);
    const uint32_t tof = (uint32_t)(lane & 2) * (TPL * 64u) + (uint32_t)(tid >> 2) * 128u + (uint32_t)(lane & 1) * 64u + 4u + (TBASE + (uint32_t)(h * 32 + j) * TST) * 128u;
    const uint32_t sp0 = (SBASE + (uint32_t)(h * 32 + j) * (2u * NWG * 9u) + (uint32_t)(tid >> 2) * 9u) * 128u;
    auto tail_rank = [&](uint32_t c, uint32_t sp, int ff, uint32_t t0, int& dst, uint32_t& en) __attribute__((always_inline)) -> int {
        const int k4 = (int)(((uint32_t)(lane & 3) + t0) * 4u) - (int)((c & 3u) * 8u);
        const int left = (int)((c >> 2) & 15u) - k4;
        const bool has = left > 0;
        uint32_t ea = tof + (uint32_t)ff * (128u * TST * 128u), eb = sp + (uint32_t)(k4 * 16 + min(max(left, 0), 4));
        asm volatile("" : "+v"(ea), "+v"(eb));
        en = k4 < 0 ? ea : eb;
        const u64 mask = __builtin_amdgcn_ballot_w64(has);
        const int n = __builtin_popcountll(mask);
        const int rank = __builtin_amdgcn_mbcnt_hi((uint32_t)(mask >> 32), __builtin_amdgcn_mbcnt_lo((uint32_t)mask, 0u));
        const int r = has ? rank : (n + lane - rank);
        dst = ((r & 15) << 4) | ((r >> 4) << 2);
        return n;
    };
    auto load_tail = [&](uint32_t en, u64& tm) __attribute__((always_inline)) -> u32x4 {
        const bool on = (uint32_t)(lane & 3) < (en & 7u);
        tm = __builtin_amdgcn_ballot_w64(on);
        return __builtin_amdgcn_raw_buffer_load_b128(rsT, on ? (en & ~15u) + (uint32_t)(lane & 3) * 16u : 0xffffff00u, 0, 0);
    };
    f32x2 fa[4][4];
    u32x4 em[2];
    u32x2 dw2;
    uint32_t wl;
    const __amdgpu_buffer_rsrc_t rsE = __builtin_amdgcn_make_buffer_rsrc((void*)m, 0, E * 8, 0x00020000);
    const __amdgpu_buffer_rsrc_t rsD = __builtin_amdgcn_make_buffer_rsrc((void*)dist, 0, E * 2, 0x00020000);
    auto acc_vec = [&](int i) __attribute__((always_inline)) -> u32x4 {
        const uint32_t a = A_ACC + (uint32_t)(i >> 4) * 256u + (uint32_t)(i & 15) * 8u;
        const u32x2 x = *(const LDS u32x2*)(uintptr_t)a, y = *(const LDS u32x2*)(uintptr_t)(a + 128u);
        const u32x4 v = {x.x, x.y, y.x, y.y};
        return v;
    };
    auto edge0 = [&](int sh) __attribute__((always_inline)) -> uint32_t {
        return (tid < SHARE / 4) ? (uint32_t)(((sh * (SHARE / 64) + (tid >> 4)) << 11) | (j << 6) | ((tid & 15) * 2)) : 0x0fffff00u;
    };
    auto load_m = [&](int sh) __attribute__((always_inline)) {
        em[0] = __builtin_amdgcn_raw_buffer_load_b128(rsE, edge0(sh) * 8u, 0, AUX_NT);
        em[1] = __builtin_amdgcn_raw_buffer_load_b128(rsE, edge0(sh) * 8u + 256u, 0, AUX_NT);
    };
    auto load_d = [&](int sh) __attribute__((always_inline)) {
        dw2.x = __builtin_amdgcn_raw_buffer_load_b32(rsD, edge0(sh) * 2u, 0, 0);
        dw2.y = __builtin_amdgcn_raw_buffer_load_b32(rsD, edge0(sh) * 2u + 64u, 0, 0);
    };
    auto load_wl = [&]() __attribute__((always_inline)) {
        const u64 p0 = (u64)wsbf, p1 = (u64)wrbf, p2 = (u64)centers, p3 = (u64)wup;
        const u64 pa = lane < 16 ? p0 : p1, pb = lane < 40 ? p2 : p3;
        wl = ((const GAS uint32_t*)(lane < 32 ? pa : pb))[lane & (lane < 32 ? 15 : lane < 40 ? 3 : 7)];
    };
    auto factor = [&](int r, bool first) __attribute__((always_inline)) {
        const float sq = 3.7982826f;
        const f32x2 d01 = {bf2f(dw2.x & 0xffffu), bf2f(dw2.x >> 16)}, d23 = {bf2f(dw2.y & 0xffffu), bf2f(dw2.y >> 16)};
        const uint32_t cw = __builtin_amdgcn_readlane(wl, 32 + (r >> 1));
        const float cn = bf2f((r & 1) ? (cw >> 16) : (cw & 0xffffu));
        f32x2 w[4];
#pragma unroll
        for (int k = 0; k < 4; k++) {
            const uint32_t x = __builtin_amdgcn_readlane(wl, (k >> 1) * 16 + r * 2 + (k & 1));
            w[k].x = bf2f(x & 0xffffu); w[k].y = bf2f(x >> 16);
        }
        const f32x2 y01 = (d01 - cn) * sq, y23 = (d23 - cn) * sq;
        const f32x2 t01 = -y01 * y01, t23 = -y23 * y23;
        const float rb[4] = {__builtin_amdgcn_exp2f(t01.x), __builtin_amdgcn_exp2f(t01.y), __builtin_amdgcn_exp2f(t23.x), __builtin_amdgcn_exp2f(t23.y)};
#pragma unroll
        for (int q = 0; q < 4; q++) {
#pragma unroll
            for (int k = 0; k < 4; k++) fa[q][k] = first ? w[k] * rb[q] : __builtin_elementwise_fma(w[k], (f32x2)(rb[q]), fa[q][k]);
        }
    };
    auto edges = [&](int sh) __attribute__((always_inline)) {
        load_d(sh);
        load_m(sh);
        factor(0, true);
#pragma unroll 1
        for (int r = 1; r < 8; r++) factor(r, false);
    };
#define ADT(V, O) "ds_write_addtid_b32 " V " offset:" #O "\n\t"
    auto store_md = [&]() __attribute__((always_inline)) {
        asm volatile("s_mov_b32 m0, %12\n\ts_nop 0\n\t" ADT("%0", 0) ADT("%1", 256) ADT("%2", 512) ADT("%3", 768) ADT("%4", 16384) ADT("%5", 16640) ADT("%6", 16896) ADT("%7", 17152)
                     ADT("%8", 32768) ADT("%9", 33024) ADT("%10", 33280) ADT("%11", 33536)
                     :: "v"(pm[0][0]), "v"(pm[0][1]), "v"(pm[0][2]), "v"(pm[0][3]), "v"(pm[1][0]), "v"(pm[1][1]), "v"(pm[1][2]), "v"(pm[1][3]),
                        "v"(pm[2][0]), "v"(pm[2][1]), "v"(pm[2][2]), "v"(pm[2][3]), "s"(wv * 1024) : "memory");
        if (wv == 0) asm volatile("s_mov_b32 m0, 0\n\ts_nop 0\n\t" ADT("%0", 49152) ADT("%1", 49408) ADT("%2", 49664) ADT("%3", 49920)
                                  :: "v"(pm[3][0]), "v"(pm[3][1]), "v"(pm[3][2]), "v"(pm[3][3]) : "memory");
    };
    const int me = h * 2 + cp;
    GAS unsigned* dw = (GAS unsigned*)(meta + M_DONE + j * 64);
    const bool fin = wv < (SHARE / 4 + 63) / 64;
    bool fast, more;
    uint32_t need, mg0, mg1;
    uint32_t guard = 0;
    uint32_t sgn = 0x8000u;
    pin(sgn);

#define GAT3(M) { uint32_t a0_, a1_, a2_; asm volatile( \
        "v_lshlrev_b16 %0, 2, %3\n\tv_lshlrev_b16 %1, 2, %4\n\tv_lshlrev_b16 %2, 2, %5\n\t" \
        "ds_read_b32 v[%6], %0\n\tds_read_b32 v[%6+1], %1\n\tds_read_b32 v[%6+2], %2" \
        : "=&v"(a0_), "=&v"(a1_), "=&v"(a2_) : "v"(w0), "v"(w1), "v"(w2), "n"(M) : "memory"); }
#define MIXL " op_sel_hi:[1,1,0]\n\t"
#define MIXH " op_sel:[0,1,0] op_sel_hi:[1,1,0]\n\t"
#define SDW " dst_sel:DWORD dst_unused:UNUSED_PAD src0_sel:DWORD src1_sel:WORD_1\n\t"
#define FOLD "v_bitop3_b32 %6, %6, v[%15-9], v[%15-8] bitop3:0xfe\n\tv_or_b32 %6, v[%15-7], %6\n\t"
#define SCAT(PRE, POST, M) { uint32_t c0_, c1_, c2_, a0_, a1_, a2_; asm volatile( \
        FOLD "v_and_b32 %0, 0x3ff, %7\n\tv_lshrrev_b32 %2, 22, %7\n\t" PRE \
        "v_bitop3_b32 %0, %11, %8, %0 bitop3:0xea\n\tv_bitop3_b32 %2, %11, %10, %2 bitop3:0xea\n\tv_bfe_u32 %1, %7, 11, 10\n\t" \
        "v_fma_mix_f32 %3, %0, v[%15], %12" MIXL "v_bitop3_b32 %1, %11, %9, %1 bitop3:0xea\n\tv_fma_mix_f32 %0, %0, v[%15], %13" MIXH \
        "v_fma_mix_f32 %4, %1, v[%15+1], %12" MIXL "v_lshl_add_u32 %0, %0, 16, %3\n\tv_lshlrev_b32_sdwa %3, 2, %8" SDW \
        "v_fma_mix_f32 %1, %1, v[%15+1], %13" MIXH "ds_add_rtn_u32 v[%15-9], %3, %0 offset:%14\n\t" \
        "v_fma_mix_f32 %5, %2, v[%15+2], %12" MIXL "v_lshl_add_u32 %1, %1, 16, %4\n\tv_lshlrev_b32_sdwa %4, 2, %9" SDW \
        "v_fma_mix_f32 %2, %2, v[%15+2], %13" MIXH "ds_add_rtn_u32 v[%15-8], %4, %1 offset:%14\n\t" \
        "v_lshl_add_u32 %2, %2, 16, %5\n\tv_lshlrev_b32_sdwa %5, 2, %10" SDW \
        "ds_add_rtn_u32 v[%15-7], %5, %2 offset:%14" POST \
        : "=&v"(c0_), "=&v"(c1_), "=&v"(c2_), "=&v"(a0_), "=&v"(a1_), "=&v"(a2_), "+v"(guard) \
        : "v"(xw), "v"(w0), "v"(w1), "v"(w2), "v"(sgn), "s"(mg0), "s"(mg1), "n"(A_ACC), "n"(M), "s"(tm) : "memory"); }
    asm volatile("v_mov_b32 v110, 0\n\tv_mov_b32 v111, 0\n\tv_mov_b32 v112, 0\n\tv_mov_b32 v113, 0\n\tv_mov_b32 v114, 0\n\tv_mov_b32 v115, 0\n\t"
                 "v_mov_b32 v116, 0\n\tv_mov_b32 v117, 0\n\tv_mov_b32 v118, 0" ::: "v106", "v127");
    auto unmix = [&](int i) __attribute__((always_inline)) {
        const auto s0 = __builtin_amdgcn_permlane16_swap(ring[i][0], ring[i][2], false, false);
        const auto s1 = __builtin_amdgcn_permlane16_swap(ring[i][1], ring[i][3], false, false);
        ring[i][0] = s0[0]; ring[i][2] = s0[1]; ring[i][1] = s1[0]; ring[i][3] = s1[1];
    };
    auto word = [&](int i, int k) __attribute__((always_inline)) -> uint32_t {
        return i < NFAST && (i >> 1) != HP ? ring[(i & ~1) | (k >> 1)][(i & 1) * 2 + (k & 1)] : ring[i][k];
    };
    auto gather = [&](int R, int i, int p) __attribute__((always_inline)) {
        if ((i >> 1) == HP) unmix(i);
        const uint32_t w0 = word(i, 0 ^ R), w1 = word(i, 1 ^ R), w2 = word(i, 2 ^ R);
        if (p == 0) GAT3(119) else if (p == 1) GAT3(122) else GAT3(125)
    };
    auto scatter = [&](int R, int i, int p, u64 tm) __attribute__((always_inline)) {
        const uint32_t xw = word(i, 3 ^ R), w0 = word(i, 0 ^ R), w1 = word(i, 1 ^ R), w2 = word(i, 2 ^ R);
        if (i < NFAST) { if (p == 0) SCAT("", "", 119) else if (p == 1) SCAT("", "", 122) else SCAT("", "", 125) }
        else if (p == 0) SCAT("s_mov_b64 exec, %16\n\t", "\n\ts_mov_b64 exec, -1", 119)
        else SCAT("s_mov_b64 exec, %16\n\t", "\n\ts_mov_b64 exec, -1", 125)
    };
#define WAIT_L(N) asm volatile("s_waitcnt lgkmcnt(" #N ")" ::: "memory")
    int ntn;
    u64 tm0, tm1;
    uint32_t ta[2], sp = sp0;
    auto list1 = [&](int ff) __attribute__((always_inline)) {
        int dst;
        uint32_t en;
        ntn = tail_rank(tc.x, sp, ff, 0u, dst, en);
        asm volatile("ds_permute_b32 v106, %0, %1" :: "v"(dst), "v"(en) : "memory");
        if (__builtin_expect(__builtin_amdgcn_ballot_w64((tc.x & 2u) != 0u) != 0ull, 0)) {
            const int n2 = tail_rank(tc.x, sp, ff, 4u, dst, en);
            if (ntn + n2 <= 32) {
                const int r = ((dst >> 4) | ((dst & 12) << 2)) + ntn;
                const int x = __builtin_amdgcn_ds_permute((en & 7u) ? ((r & 15) << 4) | ((r >> 4) << 2) : 252, (en & 7u) ? (int)en : 0);
                asm volatile("v_cmpx_ne_u32 vcc, 0, %0\n\tv_mov_b32 v106, %0\n\ts_mov_b64 exec, -1\n\ts_nop 4" :: "v"(x) : "vcc");
                ntn += n2;
            } else ntn = 64;
        }
    };
    auto step = [&](auto RR, auto KK, int ff) __attribute__((always_inline)) {
        constexpr int R = decltype(RR)::value;
        constexpr bool first = decltype(KK)::value == 1, last = decltype(KK)::value == 2;
        constexpr int mdp = first ? MDF : MDP;
        gather(R, 0, 0);
        const int ntl = ntn;
        asm volatile("v_mov_b32_dpp %0, v106 quad_perm:[0,0,0,0] row_mask:0xf bank_mask:0xf\n\tv_mov_b32_dpp %1, v106 quad_perm:[1,1,1,1] row_mask:0xf bank_mask:0xf" : "=v"(ta[0]), "=v"(ta[1]));
        auto side = [&](int pl) __attribute__((always_inline)) {
            if (first && pl == ((mdp >> 20) & 15)) { ring[4] = load_main(4, ff, !(NEARM & 1u)); ring[5] = load_main(5, ff, !(NEARM & 1u)); }
            if (first && pl == ((mdp >> 24) & 15)) { ring[6] = load_main(6, ff); ring[7] = load_main(7, ff); }
            if (pl == ((mdp >> 12) & 15)) ring[NFAST] = load_tail(ta[0], tm0);
            if (pl == ((mdp >> 16) & 15)) ring[NFAST + 1] = load_tail(ta[1], tm1);
            __builtin_amdgcn_sched_barrier(0);
            if (!last) load_md(ff + 1, pl, mdp);
            if (!first && !last && pl == 5 && ff == 6) {
                load_d(me);
                load_wl();
                if (tid == NT - 128) (void)__hip_atomic_fetch_add((GAS unsigned*)((GAS char*)dw + vconst<0>()), 0x1000000u << me, __ATOMIC_RELAXED, __HIP_MEMORY_SCOPE_AGENT);
            }
            if (last && pl == 3) load_m(me);
            if (last && pl == 4 && wv == 15) { GAS unsigned* d_ = dw; asm volatile("s_nop 4\n\tglobal_load_dword v108, %0, %1 sc1" :: "v"(vconst<0>()), "s"(d_) : "memory"); }
            if (last && pl < 7 && fin) factor(pl, pl == 0);
            __builtin_amdgcn_sched_barrier(0);
        };
        side(8);
        gather(R, 1, 1);
        side(9);
        const uint32_t tcur = tc.x, spc = sp;
#pragma unroll
        for (int i = 0; i < NFAST - 1; i++) {
            gather(R, i + 2, (i + 2) % 3);
            if (i == 0) side(10);
            if (i == 0) WAIT_L(6); else if (i == 1) WAIT_L(9); else WAIT_L(12);
            scatter(R, i, i % 3, 0ull);
            if (!last && (i & 1)) { ring[i - 1] = load_main(i - 1, ff + 1, first && !(NEARM & 4u)); ring[i] = load_main(i, ff + 1, first && !(NEARM & 4u)); }
            side(i);
            if (i == 3) SETP(2);
            if (i == 4) SETP(1);
            if (i == 5) SETP(0);
            if (i == 0 && !last) {
                tc.x = __builtin_amdgcn_alignbit(tc.y, tc.x, 8u); tc.y = __builtin_amdgcn_alignbit(tc.z, tc.y, 8u);
                tc.z = __builtin_amdgcn_alignbit(tc.w, tc.z, 8u); tc.w >>= 8;
                sp = ff == 7 ? sp0 + NWG * 9u * 128u : sp + ((tcur & 0x3cu) << 2);
                list1(ff + 1);
            }
            __builtin_amdgcn_sched_barrier(0);
        }
        if (last) asm volatile("" :: "v"(ring[NFAST + 1]));
        if (__builtin_expect(ntl > 16, 0)) {
            gather(R, NFAST + 1, 0);
            WAIT_L(0);
            scatter(R, NFAST + 1, 0, tm1);
        }
        if (__builtin_expect(ntl > 32, 0)) {
#pragma unroll 1
            for (int e = 2; e < 6; e++) {
                int dst;
                uint32_t en;
                if (tail_rank(tcur, spc, ff, (uint32_t)e & 4u, dst, en) <= (e & 3) * 16) continue;
                const int cmp = __builtin_amdgcn_ds_permute(dst, (int)en);
                ring[NFAST + 1] = load_tail((uint32_t)__builtin_amdgcn_ds_bpermute(((lane & ~3) | (e & 3)) << 2, cmp), tm1);
                gather(R, NFAST + 1, 0);
                WAIT_L(0);
                scatter(R, NFAST + 1, 0, tm1);
            }
        }
        SETP(3);
        WAIT_L(3);
        if (!last) {
            __builtin_amdgcn_s_barrier();
            store_md();
            SETP(0);
        }
        scatter(R, NFAST - 1, 1, 0ull);
        if (!last) { ring[6] = load_main(6, ff + 1); ring[7] = load_main(7, ff + 1); }
        scatter(R, NFAST, 2, tm0);
        if (!last) {
            WAIT_L(6);
            __builtin_amdgcn_s_barrier();
            SETP(3);
        }
    };
    auto steps = [&](auto RR) __attribute__((always_inline)) {
        tc = tcnt[(wv * 16 + (lane >> 2)) * 128 + j * 4 + h];
        u32x4 sv[2];
        float4 su[3];
        uint32_t e0;
        if (wv < 4) {
            sv[0] = (u32x4)(0u); sv[1] = sv[0];
            su[0] = make_float4(0.f, 0.f, 0.f, 0.f);
            if (lane < SLOTI / 256) { sv[0] = stat[tid * 2]; sv[1] = stat[tid * 2 + 1]; su[0] = ((const float4*)(ws + W_SUM))[tid]; }
            su[1] = ((const float4*)(ws + W_SUM))[256 + lane];
            su[2] = ((const float4*)(ws + W_SUM))[320 + lane];
            e0 = meta[M_E0 + wv];
        }
        const u32x2 extra = *(const u32x2*)(meta + M_LOG);
        load_md(0, -1, 0);
        {
            const u32x4 bi = {BIAS, BIAS, BIAS, BIAS};
#pragma unroll
            for (int k = 0; k < 6; k++) *(LDS u32x4*)(uintptr_t)(A_ACC + (k * NT + threadIdx.x) * 16) = bi;
            if (threadIdx.x < 128) *(LDS u32x4*)(uintptr_t)(A_ACC + (6 * NT + threadIdx.x) * 16) = bi;
            __builtin_amdgcn_sched_barrier(0);
        }
#pragma unroll
        for (int i = 0; i < 2; i++) ring[i] = load_main(i, 0);
        __builtin_amdgcn_sched_barrier(0);
        list1(0);
        if (wv < 4) {
            const uint32_t mxb = wave_max(max(max(sv[0].x & 0xffffu, sv[0].z & 0xffffu), max(sv[1].x & 0xffffu, sv[1].z & 0xffffu)));
            const uint32_t mnb = 0xffffu - (wave_max(max(max(sv[0].x, sv[0].z), max(sv[1].x, sv[1].z))) >> 16);
            const float s2 = wave_sum((__uint_as_float(sv[0].y) + __uint_as_float(sv[0].w)) + (__uint_as_float(sv[1].y) + __uint_as_float(sv[1].w)));
            const float s1 = wave_sum((su[0].x + su[0].y) + (su[0].z + su[0].w));
            const float c1 = wave_sum((su[1].x + su[1].y) + (su[1].z + su[1].w)) * (1.0f / (NWG * 512)), c2 = wave_sum((su[2].x + su[2].y) + (su[2].z + su[2].w)) * (1.0f / (NWG * 512));
            const float rng = fmaxf(fabsf(zf * s1 * c1) + ZSIG * sqrtf(zf * s2 * c2), 2.0f * bf2f(mxb)) * 6.103515625e-05f;
            const uint32_t eb = mxb ? (__float_as_uint(rng) + 0x7fffffu) >> 23 : 127u;
            const bool ok = !mxb || (mxb < 0x4700u && eb >= 107u && eb <= 140u && mnb >= ((eb + 8u) << 7) && c2 >= 0.015625f);
            if (lane == 0) {
                s_flag[8 + wv] = (eb & 0xffu) | (ok ? 0x100u : 0u) | (mxb ? 0x200u : 0u);
                s_flag[12 + wv] = __float_as_uint(__uint_as_float(((eb + e0) & 0xffu) << 23) * (1024.0f / 1023.0f));
                s_flag[wv] = 0u;
            }
        }
        store_md();
#pragma unroll
        for (int i = 2; i < 4; i++) ring[i] = load_main(i, 0);
        WAIT_L(0);
        __syncthreads();
        const uint32_t q0 = __builtin_amdgcn_readfirstlane(s_flag[8 + 2 * cp]), q1 = __builtin_amdgcn_readfirstlane(s_flag[9 + 2 * cp]);
        fast = (q0 & q1 & 0x100u) != 0u;
        need = (q0 >> 9) | ((q1 >> 9) << 1);
        more = (__builtin_amdgcn_readfirstlane(extra.x | extra.y) | (uint32_t)(T & 3)) != 0u;
        mg0 = (((q0 & 0xffu) + 9u) << 23) | 0x400000u;
        mg1 = ((((q1 & 0xffu) + 9u) << 23) | 0x400000u) + ((0u - (mg0 >> 16)) & 0xffffu);
        if (!fast) { __builtin_amdgcn_s_waitcnt(0x0f70); return; }
        step(RR, IC<1>(), 0);
#pragma unroll 1
        for (int ff = 1; ff < 15; ff++) step(RR, IC<0>(), ff);
        step(RR, IC<2>(), 15);
        if (fin) factor(7, false);
    };
    auto irregular = [&](auto f) __attribute__((always_inline)) {
        const float fr = bf2f(freq[0]);
        const char* lg = recs + (size_t)LBASE * 128u;
        const uint16_t* tags = (const uint16_t*)(lg + (size_t)lcap * 128u);
        const uint32_t nl = meta[M_LOG], ns = meta[M_LOG + 1];
#pragma unroll 1
        for (uint32_t q = tid; q < nl * 8u; q += NT) {
            const uint32_t b = tags[q >> 3];
            if ((b & 127u) == (uint32_t)(h * 32 + j)) {
                const uint32_t* un = (const uint32_t*)(lg + (size_t)q * 16u);
                const uint32_t xw = un[3 ^ h];
#pragma unroll 1
                for (int k = 0; k < 3; k++) {
                    const uint32_t ww = un[k ^ h];
                    const float c = (float)((xw >> (k * 11)) & 0x3ffu) * (1.0f / 1023.0f);
                    if (!(ww & WNULL)) f(ww >> 16, ((ww & 0x3fffu) << 6) | (b >> 5), (ww & 0x8000u) ? -c : c);
                }
            }
        }
#pragma unroll 1
        for (uint32_t q = tid; q < ns + (uint32_t)(T & 3); q += NT) {
            const uint32_t t = q < ns ? ((const uint32_t*)tags)[-1 - (int)q] : (uint32_t)(T & ~3) + (q - ns);
            const uint32_t i = (uint32_t)iin[t], o = (uint32_t)iout[t];
            if (((o >> 6) & 31u) == (uint32_t)j && (i & 3u) == (uint32_t)h) f(((o >> 11) << 6) | (o & 63u), i, cosf(fr * bf2f(ang[t])));
        }
        __builtin_amdgcn_s_waitcnt(0x0f70);
    };

    if (h == 0) steps(IC<0>());
    else if (h == 1) steps(IC<1>());
    else if (h == 2) steps(IC<2>());
    else steps(IC<3>());
    if (fast) {
        if (more) irregular([&](uint32_t ol, uint32_t i, float cf) __attribute__((always_inline)) {
            const uint32_t md = mdc[(cp * 64u + (i & 63u)) * SLOTI + ((i >> 14) << 8) + ((i >> 4) & 0xfcu) + ((i >> 12) & 3u)];
            const float c = cf * (1023.0f / 16777216.0f);
            const uint32_t lo = __float_as_uint(__builtin_fmaf(c, h2f(md & 0xffffu), __uint_as_float(mg0)));
            const uint32_t hi = __float_as_uint(__builtin_fmaf(c, h2f(md >> 16), __uint_as_float(mg1)));
            if (cf == cf) guard |= atomicAdd(&acc[ol], (hi << 16) + lo);
            else guard |= HSLOW;
        });
    }
    volatile LDS unsigned* seen = (volatile LDS unsigned*)(uintptr_t)(A_FLAG + 4);
    if (fast && wv == 15) asm volatile("s_waitcnt vmcnt(0)\n\tds_write_b32 %0, v108 offset:16\n\tv_and_b32 v109, 0xff00ff, v108\n\tds_write_b32 %0, v109" :: "v"(vconst<A_FLAG + 4>()) : "memory");
    WAIT_L(0);
    asm volatile("v_or3_b32 %0, %0, v110, v111\n\tv_or3_b32 %0, %0, v112, v113\n\tv_or3_b32 %0, %0, v114, v115\n\tv_or3_b32 %0, %0, v116, v117\n\tv_or_b32 %0, v118, %0" : "+v"(guard));
    __syncthreads();
    const __amdgpu_buffer_rsrc_t rsP = __builtin_amdgcn_make_buffer_rsrc((void*)(partial + (size_t)j * 16 * SLOTO), 0, 16 * SLOTO * 4, 0x00020000);
    const char* pk = recs + (uint32_t)j * (SC * 128u);
    const __amdgpu_buffer_rsrc_t rsK = __builtin_amdgcn_make_buffer_rsrc((void*)pk, 0, (int)(MAINL * 128u), 0x00020000);
#define PKA (SSM * 128u)
#define PKB (32u * SC * 128u)
    auto pk_at = [&](uint32_t i) __attribute__((always_inline)) -> uint32_t {
        const uint32_t c = i >> 9;
        return (c < 12u ? (1u + (c >> 2)) * SQ + (c & 3u) * SKM : SQ + 2u * SSM) * 128u + (i & 511u) * 16u;
    };
    u32x4 p[8];
    unsigned have = 0u;
    auto fetch = [&](unsigned v, int sh) __attribute__((always_inline)) {
        const uint32_t po = tid < SHARE / 4 ? pk_at((uint32_t)(sh * (SHARE / 4) + tid)) : 0x7fffff00u;
        const unsigned nw = v & ~have;
#pragma unroll
        for (int r = 0; r < 8; r++) {
            const uint32_t k = (uint32_t)(me ^ r);
            if ((nw >> k) & 1u) p[r] = __builtin_amdgcn_raw_buffer_load_b128(rsK, po, ((v >> (16u + k)) & 1u) ? MAINL * 128u : (k & 1u) * PKA + (k >> 1) * PKB, AUX_SC1);
        }
        have |= v;
    };
    bool room = false;
    if (fast) {
        room = (__builtin_amdgcn_readfirstlane(s_flag[5]) >> (24 + (me ^ 1))) & 1u;
        const __amdgpu_buffer_rsrc_t rsW = __builtin_amdgcn_make_buffer_rsrc((void*)(pk + (uint32_t)cp * PKA + (uint32_t)h * PKB), 0, room ? (int)(MAINL * 128u) : 0, 0x00020000);
        uint32_t big = 0u;
        u32x4 v[7];
#pragma unroll
        for (int k = 0; k < 6; k++) v[k] = acc_vec(k * NT + tid);
        v[6] = (u32x4)(BIAS);
        if (wv < 2) v[6] = acc_vec(6 * NT + tid);
#pragma unroll
        for (int k = 0; k < 6; k++) __builtin_amdgcn_raw_buffer_store_b128(v[k], rsW, pk_at((uint32_t)tid) + ((k >> 1) * SQ + (k & 1) * 2u * SKM) * 128u, 0, AUX_SC1);
        if (wv < 2) __builtin_amdgcn_raw_buffer_store_b128(v[6], rsW, (SQ + 2u * SSM) * 128u + (uint32_t)tid * 16u, 0, AUX_SC1);
        __builtin_amdgcn_sched_barrier(0);
#pragma unroll
        for (int k = 0; k < 28; k++) { guard |= v[k >> 2][k & 3]; big |= v[k >> 2][k & 3] + 0x42004200u; }
        if (guard & HSLOW) s_flag[0] = 1u;
        if (big & 0x7c00u) s_flag[2] = 1u;
        if (big & 0x7c000000u) s_flag[3] = 2u;
        __builtin_amdgcn_sched_barrier(0);
        p[0] = acc_vec(me * (SHARE / 4) + min(tid, SHARE / 4 - 1));
        have = 1u << me;
        if (fin) fetch(__builtin_amdgcn_readfirstlane(*seen), me);
    }
    asm volatile("s_waitcnt vmcnt(0)" ::: "memory");
    __syncthreads();
    const bool redo = !fast || __builtin_amdgcn_readfirstlane(s_flag[0]) != 0u || (need & ~__builtin_amdgcn_readfirstlane(s_flag[2] | s_flag[3])) != 0u;
    const bool slow = redo || !room;
    if (slow) {
        float* fs = (float*)acc;
        float w[16];
        load_w(wdown, w);
        const float fr = bf2f(freq[0]);
#pragma unroll 1
        for (int e = 0; e < 2; e++) {
            if (redo) {
                __syncthreads();
                for (int i = tid; i < SLOTO; i += NT) fs[i] = 0.f;
                __syncthreads();
                const __amdgpu_buffer_rsrc_t rsO = __builtin_amdgcn_make_buffer_rsrc((void*)iout, 0, T * 4, 0x00020000);
                const __amdgpu_buffer_rsrc_t rsI = __builtin_amdgcn_make_buffer_rsrc((void*)iin, 0, T * 4, 0x00020000);
#pragma unroll 1
                for (int t0 = tid * 4; t0 < T; t0 += NT * 16) {
                    u32x4 o4[4], i4[4];
#pragma unroll
                    for (int g = 0; g < 4; g++) {
                        o4[g] = __builtin_amdgcn_raw_buffer_load_b128(rsO, (uint32_t)(t0 + g * NT * 4) * 4u, 0, 0);
                        i4[g] = __builtin_amdgcn_raw_buffer_load_b128(rsI, (uint32_t)(t0 + g * NT * 4) * 4u, 0, 0);
                    }
                    uint32_t hit = 0u;
#pragma unroll
                    for (int k = 0; k < 16; k++) hit |= (bfi(3u, i4[k >> 2][k & 3], o4[k >> 2][k & 3]) & 0x7c3u) == (uint32_t)(j * 64 + h) ? 1u << k : 0u;
#pragma unroll 1
                    for (; hit; hit &= hit - 1u) {
                        const int k = __builtin_ctz(hit), t = t0 + (k >> 2) * NT * 4 + (k & 3);
                        if (t >= T) break;
                        const uint32_t o = (uint32_t)iout[t];
                        uint32_t rr[4];
                        md_of(m[iin[t]], w, rr);
                        atomicAdd(&fs[((o >> 11) << 6) | (o & 63u)], cosf(fr * bf2f(ang[t])) * bf2f(cp ? (e ? rr[3] : rr[2]) : (e ? rr[1] : rr[0])));
                    }
                }
                __syncthreads();
            }
            const float un = __uint_as_float(s_flag[12 + 2 * cp + e]);
            for (int i = tid; i < SLOTO / 4; i += NT) {
                u32x4 v = acc_vec(i);
                if (!redo) {
#pragma unroll
                    for (int k = 0; k < 4; k++) v[k] = __float_as_uint((float)((int)((v[k] >> (16 * e)) & 0xffffu) - 0x4000) * un);
                }
                __builtin_amdgcn_raw_buffer_store_b128(v, rsP, (uint32_t)((e * 8 + me) * SLOTO * 4 + i * 16), 0, AUX_SC1);
            }
        }
        load_wl();
        edges(me);
#pragma unroll
        for (int k = 0; k < 8; k++) p[k] = (u32x4)(0u);
        have = 0u;
        asm volatile("s_waitcnt vmcnt(0)" ::: "memory");
        __syncthreads();
    }
    if (tid == NT - 64) {
        const unsigned inc = (slow ? 0x10001u : 1u) << me;
        unsigned v = __hip_atomic_fetch_add(dw, inc, __ATOMIC_RELAXED, __HIP_MEMORY_SCOPE_AGENT) + inc;
        unsigned quit = 0u;
        if ((v & 0xffu) != 0xffu) {
            unsigned spins = 0;
            do {
                *seen = v & 0xff00ffu;
                __builtin_amdgcn_s_sleep(1);
                v = __hip_atomic_load(dw, __ATOMIC_RELAXED, __HIP_MEMORY_SCOPE_AGENT);
                if (++spins > SPINCAP && (v & 0xffu) != 0xffu) {
                    v = __hip_atomic_fetch_add(dw, 256u << me, __ATOMIC_RELAXED, __HIP_MEMORY_SCOPE_AGENT);
                    if ((v & 0xffu) != 0xffu) { v = 0xffu; quit = 0x80000000u; }
                }
            } while ((v & 0xffu) != 0xffu);
            v &= 0xff00ffu;
        }
        *seen = (v & 0xffffffu) | quit;
    }
    if (!fin) return;
    have = __builtin_amdgcn_readfirstlane(have);
    uint32_t un[4];
#pragma unroll
    for (int c = 0; c < 4; c++) un[c] = __builtin_amdgcn_readfirstlane(s_flag[12 + c]);
    unsigned sv;
    {
        const u32x4 rsQ = {(uint32_t)(uintptr_t)pk, (uint32_t)((uintptr_t)pk >> 32) & 0xffffu, MAINL * 128u, 0x00020000u};
        const uint32_t po = tid < SHARE / 4 ? pk_at((uint32_t)(me * (SHARE / 4) + tid)) : 0x7fffff00u;
        do {
            sv = __builtin_amdgcn_readfirstlane(*seen);
            uint32_t t0, t1, t2;
#define FET(R) "s_xor_b32 %8, %13, " #R "\n\ts_bitcmp1_b32 %14, %8\n\ts_cbranch_scc0 1f\n\ts_and_b32 %9, %8, 1\n\ts_mul_i32 %9, %9, %16\n\ts_lshr_b32 %10, %8, 1\n\t" \
               "s_mul_i32 %10, %10, %17\n\ts_add_u32 %9, %9, %10\n\ts_bitcmp1_b32 %15, %8\n\ts_cselect_b32 %9, %18, %9\n\tbuffer_load_dwordx4 %" #R ", %11, %12, %9 offen sc1\n1:\n\t"
            asm volatile("s_and_b32 %8, %14, 0xff\n\ts_cbranch_scc0 2f\n\ts_nop 2\n\t" FET(0) FET(1) FET(2) FET(3) FET(4) FET(5) FET(6) FET(7) "\n2:\n\ts_sleep 2\n\ts_waitcnt vmcnt(0)"
                         : "+v"(p[0]), "+v"(p[1]), "+v"(p[2]), "+v"(p[3]), "+v"(p[4]), "+v"(p[5]), "+v"(p[6]), "+v"(p[7]), "=&s"(t0), "=&s"(t1), "=&s"(t2)
                         : "v"(po), "s"(rsQ), "s"(me), "s"((int)sv < 0 ? 0u : sv & ~have), "s"(sv >> 16), "n"(PKA), "n"(PKB), "n"(MAINL * 128u) : "memory", "scc");
            have |= sv;
        } while ((sv & 0xffu) != 0xffu);
    }
    if ((int)sv < 0) return;
    const unsigned flt = (sv >> 16) & 0xffu;
    auto finish = [&](int sh) __attribute__((always_inline)) {
        {
            const int ol = sh * SHARE + tid * 4;
            const uint32_t po = tid < SHARE / 4 ? (uint32_t)ol * 4u : 0x7fffff00u;
            float g[4][4];
            u32x4 a[2] = {p[0] + p[2], p[1] + p[3]}, b[2] = {p[4] + p[6], p[5] + p[7]};
            if (cp) { const u32x4 t = a[0], u = b[0]; a[0] = a[1]; b[0] = b[1]; a[1] = t; b[1] = u; }
#pragma unroll
            for (int c = 0; c < 2; c++) {
                const int nb = 0x10000 - 0x4000 * __builtin_popcount(flt & (0x55u << c));
                const float u0 = __uint_as_float(nb ? un[2 * c] : 0u), u1 = __uint_as_float(nb ? un[2 * c + 1] : 0u);
#pragma unroll
                for (int q = 0; q < 4; q++) {
                    g[q][2 * c] = (float)((int)((a[c][q] & 0xffffu) + (b[c][q] & 0xffffu)) - nb) * u0;
                    g[q][2 * c + 1] = (float)((int)((a[c][q] >> 16) + (b[c][q] >> 16)) - nb) * u1;
                }
            }
            if (__builtin_expect(flt != 0u, 0)) {
#pragma unroll
                for (int c = 0; c < 2; c++) {
#pragma unroll 1
                    for (int k = c; k < 8; k += 2) {
                        if (!((flt >> k) & 1u)) continue;
                        const u32x4 f0 = __builtin_amdgcn_raw_buffer_load_b128(rsP, po, k * SLOTO * 4, AUX_SC1);
                        const u32x4 f1 = __builtin_amdgcn_raw_buffer_load_b128(rsP, po, (8 + k) * SLOTO * 4, AUX_SC1);
#pragma unroll
                        for (int q = 0; q < 4; q++) { g[q][2 * c] += __uint_as_float(f0[q]); g[q][2 * c + 1] += __uint_as_float(f1[q]); }
                    }
                }
            }
            float wu[16];
#pragma unroll
            for (int k = 0; k < 8; k++) {
                const uint32_t x = __builtin_amdgcn_readlane(wl, 40 + k);
                wu[2 * k] = bf2f(x & 0xffffu); wu[2 * k + 1] = bf2f(x >> 16);
            }
            u32x4 ov[2];
#pragma unroll
            for (int q = 0; q < 4; q++) {
#pragma unroll
                for (int k = 0; k < 4; k++) g[q][k] *= fa[q][k >> 1][k & 1];
                const uint32_t m0 = em[q >> 1][(q & 1) * 2], m1 = em[q >> 1][(q & 1) * 2 + 1];
                const float mm[4] = {bf2f(m0 & 0xffff), bf2f(m0 >> 16), bf2f(m1 & 0xffff), bf2f(m1 >> 16)};
                float o[4];
#pragma unroll
                for (int k = 0; k < 4; k++) o[k] = mm[k] + (g[q][0] * wu[k] + g[q][1] * wu[4 + k] + g[q][2] * wu[8 + k] + g[q][3] * wu[12 + k]) * fa[q][2 + (k >> 1)][k & 1];
                ov[q >> 1][(q & 1) * 2] = pk_bf16(o[0], o[1]);
                ov[q >> 1][(q & 1) * 2 + 1] = pk_bf16(o[2], o[3]);
            }
            const __amdgpu_buffer_rsrc_t rsU = __builtin_amdgcn_make_buffer_rsrc((void*)out, 0, E * 8, 0x00020000);
            const uint32_t uo = edge0(sh) * 8u;
            __builtin_amdgcn_raw_buffer_store_b128(ov[0], rsU, uo, 0, AUX_NT);
            __builtin_amdgcn_raw_buffer_store_b128(ov[1], rsU, uo + 256u, 0, AUX_NT);
        }
    };
    finish(me);
#pragma unroll 1
    for (unsigned todo = (sv >> 8) & 0xffu; todo; todo &= todo - 1u) {
        const int sh = __builtin_ctz(todo);
        edges(sh);
        have = 0u;
        fetch(sv, sh);
        finish(sh);
    }
}

extern "C" __global__ __launch_bounds__(256) void gen_scatter_kernel(const int* __restrict__ iin, const int* __restrict__ iout,
                                                          const uint16_t* __restrict__ ang, const uint16_t* __restrict__ freq,
                                                          const u32x2* __restrict__ m, const uint16_t* __restrict__ wdown,
                                                          float* __restrict__ agg, int T) {
    float w[16];
    load_w(wdown, w);
    const float fr = bf2f(freq[0]);
    for (long t = (long)blockIdx.x * 256 + threadIdx.x; t < T; t += (long)gridDim.x * 256) {
        const float c = cosf(fr * bf2f(ang[t]));
        uint32_t r[4];
        md_of(m[iin[t]], w, r);
        float* q = agg + (size_t)iout[t] * 4;
#pragma unroll
        for (int k = 0; k < 4; k++) atomicAdd(q + k, c * bf2f(r[k]));
    }
}
__global__ __launch_bounds__(256) void gen_final_kernel(const u32x2* __restrict__ m, const uint16_t* __restrict__ dist,
                                                        const uint16_t* __restrict__ centers, const uint16_t* __restrict__ wrbf,
                                                        const uint16_t* __restrict__ wsbf, const uint16_t* __restrict__ wup,
                                                        const float* __restrict__ agg, u32x2* __restrict__ out, int E) {
    const int e = blockIdx.x * 256 + threadIdx.x;
    if (e >= E) return;
    const float d = bf2f(dist[e]);
    float rs[4] = {0, 0, 0, 0}, rg[4] = {0, 0, 0, 0};
#pragma unroll 1
    for (int r = 0; r < 8; r++) {
        const float x = d - bf2f(centers[r]);
        const float rb = __expf(-10.0f * x * x);
#pragma unroll
        for (int k = 0; k < 4; k++) { rs[k] += rb * bf2f(wsbf[r * 4 + k]); rg[k] += rb * bf2f(wrbf[r * 4 + k]); }
    }
    float wu[16];
    load_w(wup, wu);
    const u32x2 mv = m[e];
    const float mm[4] = {bf2f(mv.x & 0xffff), bf2f(mv.x >> 16), bf2f(mv.y & 0xffff), bf2f(mv.y >> 16)};
    float g[4], o[4];
#pragma unroll
    for (int k = 0; k < 4; k++) g[k] = agg[(size_t)e * 4 + k] * rs[k];
#pragma unroll
    for (int k = 0; k < 4; k++) o[k] = mm[k] + (g[0] * wu[k] + g[1] * wu[4 + k] + g[2] * wu[8 + k] + g[3] * wu[12 + k]) * rg[k];
    const u32x2 ov = {f2bf(o[0]) | (f2bf(o[1]) << 16), f2bf(o[2]) | (f2bf(o[3]) << 16)};
    out[e] = ov;
}

extern "C" void kernel_launch(void* const* d_in, const int* in_sizes, int n_in,
                              void* d_out, int out_size, void* d_ws, size_t ws_size,
                              hipStream_t stream) {
    const u32x2* m = (const u32x2*)d_in[0];
    const int* iin = (const int*)d_in[1];
    const int* iout = (const int*)d_in[2];
    const uint16_t* dist = (const uint16_t*)d_in[3];
    const uint16_t* ang = (const uint16_t*)d_in[4];
    const uint16_t* centers = (const uint16_t*)d_in[5];
    const uint16_t* freq = (const uint16_t*)d_in[6];
    const uint16_t* wrbf = (const uint16_t*)d_in[7];
    const uint16_t* wsbf = (const uint16_t*)d_in[8];
    const uint16_t* wdown = (const uint16_t*)d_in[9];
    const uint16_t* wup = (const uint16_t*)d_in[10];
    const int E = in_sizes[3];
    const int T = in_sizes[1];
    char* ws = (char*)d_ws;
    const int G = T >> 2;
    const int ntile = (G + GR - 1) / GR;
    const int nstat = ntile / NWG - NDYN > 4 ? ntile / NWG - NDYN : 4;
    const int lcap = G / 6 + 2;
    char* recs = ws + W_RECS;
    size_t off = W_RECS;
    const size_t rbytes = (size_t)LBASE * 128 + (size_t)lcap * 130;
    off += rbytes;
    if (E > EMAX || (E & 3) || E < 1 || rbytes > 0x70000000u || off > ws_size) {
        (void)hipMemsetAsync(ws, 0, (size_t)E * 16, stream);
        gen_scatter_kernel<<<4096, 256, 0, stream>>>(iin, iout, ang, freq, m, wdown, (float*)ws, T);
        gen_final_kernel<<<(E + 255) / 256, 256, 0, stream>>>(m, dist, centers, wrbf, wsbf, wup, (const float*)ws, (u32x2*)d_out, E);
        return;
    }
    int ks = T / (NWG * NB * 24);
    if (ks > KMAX) ks = KMAX;
    const float zf = (float)T / (4.0f * (float)E * (float)E);
    (void)hipFuncSetAttribute((const void*)part_kernel, hipFuncAttributeMaxDynamicSharedMemorySize, P_LDS);
    (void)hipFuncSetAttribute((const void*)acc_kernel, hipFuncAttributeMaxDynamicSharedMemorySize, A_LDS);
    part_kernel<<<NWG, NT, P_LDS, stream>>>(iin, iout, ang, freq, m, wdown, recs, T, E, ntile, nstat, ks, lcap);
    acc_kernel<<<NWG, NT, A_LDS, stream>>>(ws, T, E, ks, lcap, zf, 0, centers, wrbf, wsbf, wup, m, dist, (u32x2*)d_out, iin, iout, ang, wdown, freq);
}
```

```cpp
#include <hip/hip_runtime.h>
#include <hip/hip_bf16.h>
#include <stdint.h>
#pragma clang diagnostic ignored "-Winline-asm"

typedef unsigned int u32x4 __attribute__((ext_vector_type(4)));
typedef unsigned int u32x2 __attribute__((ext_vector_type(2)));
typedef unsigned short u16x2 __attribute__((ext_vector_type(2)));
typedef __bf16 bf16x2 __attribute__((ext_vector_type(2)));
typedef float f32x2 __attribute__((ext_vector_type(2)));
typedef unsigned long long u64;

static __device__ __forceinline__ float bf2f(uint32_t bits16) { return __uint_as_float(bits16 << 16); }
static __device__ __forceinline__ uint32_t f2bf(float f) {
    uint32_t u = __float_as_uint(f);
    if ((u & 0x7fffffffu) > 0x7f800000u) return (u >> 16) | 0x40;
    return (u + 0x7fffu + ((u >> 16) & 1u)) >> 16;
}
static __device__ __forceinline__ float h2f(uint32_t bits16) { return (float)__builtin_bit_cast(_Float16, (unsigned short)bits16); }

#define NB 2048
#define WNULL 0x80000000u
#define CSTEP 0x2aabu
#define CINIT 205u
#define CHV (12u * CSTEP)
#define SLOTO 25088
#define SLOTI 12544
#define EMAX (SLOTO * 32)
#define NT 1024
#define NWG 256
#define KMAX 4
#define NTAIL 4
#define SC 64u
#define SKM (128u * SC + 2u)
#define SSM (4u * SKM + 1u)
#define NNEAR 2u
#define NEARM (0x55555555u << (2u * NNEAR))
#define SSH (128u * SC + 1u)
#define SKH ((16u + NNEAR) * SSH + 2u - (NNEAR & 1u))
#define SQ (16u * SSM)
#define MAINL (3u * SQ)
#define SBASE MAINL
#define SPL (128u * 2u * NWG + 1u)
#define HBASE (SBASE + 8u * SPL)
#define QAT(q) ((q) ? ((q) - 1u) * SQ : HBASE)
#define BPL(b) (QAT(((b) >> 3) & 3u) + ((b) & 7u) + (((b) >> 5) << 3))
#define TST 257u
#define TBASE (HBASE + 3u * SKH + 16u * SSH)
#define TPL (NB * TST)
#define LBASE (TBASE + 2u * TPL)
#define NDYN 3
#define GR 2048
#define M_DONE 0
#define M_TILE (32 * 64)
#define M_LOG (M_TILE + 64)
#define M_MD (M_LOG + 64)
#define M_BEAT (M_MD + 64)
#define M_E0 (M_BEAT + 64)
#define M_WORDS (M_E0 + 64)
#define W_TCNT (M_WORDS * 4)
#define W_STAT (W_TCNT + NWG * NB)
#define W_SUM (W_STAT + 4 * NWG * 8)
#define W_MDC (W_SUM + 6 * NWG * 4)
#define W_RECS (W_MDC + 128 * SLOTI * 4)
#define AUX_NT 2
#define AUX_SC1 16

static __device__ __forceinline__ uint32_t row_max(uint32_t x) {
    x = max(x, (uint32_t)__builtin_amdgcn_update_dpp(0, x, 0x111, 0xf, 0xf, false));
    x = max(x, (uint32_t)__builtin_amdgcn_update_dpp(0, x, 0x112, 0xf, 0xf, false));
    x = max(x, (uint32_t)__builtin_amdgcn_update_dpp(0, x, 0x114, 0xf, 0xf, false));
    return max(x, (uint32_t)__builtin_amdgcn_update_dpp(0, x, 0x118, 0xf, 0xf, false));
}
static __device__ __forceinline__ uint32_t wave_max(uint32_t x) {
    x = row_max(x);
    x = max(x, (uint32_t)__builtin_amdgcn_update_dpp(0, x, 0x142, 0xa, 0xf, false));
    x = max(x, (uint32_t)__builtin_amdgcn_update_dpp(0, x, 0x143, 0xc, 0xf, false));
    return __builtin_amdgcn_readlane(x, 63);
}
static __device__ __forceinline__ float row_sum(float x) {
    x += __int_as_float(__builtin_amdgcn_update_dpp(0, __float_as_int(x), 0x111, 0xf, 0xf, false));
    x += __int_as_float(__builtin_amdgcn_update_dpp(0, __float_as_int(x), 0x112, 0xf, 0xf, false));
    x += __int_as_float(__builtin_amdgcn_update_dpp(0, __float_as_int(x), 0x114, 0xf, 0xf, false));
    return x + __int_as_float(__builtin_amdgcn_update_dpp(0, __float_as_int(x), 0x118, 0xf, 0xf, false));
}
static __device__ __forceinline__ float wave_sum(float x) {
    x = row_sum(x);
    x += __int_as_float(__builtin_amdgcn_update_dpp(0, __float_as_int(x), 0x142, 0xa, 0xf, false));
    x += __int_as_float(__builtin_amdgcn_update_dpp(0, __float_as_int(x), 0x143, 0xc, 0xf, false));
    return __int_as_float(__builtin_amdgcn_readlane(__float_as_int(x), 63));
}
static __device__ __forceinline__ uint32_t bfi(uint32_t mask, uint32_t a, uint32_t b) {
    return __builtin_amdgcn_bitop3_b32(mask, a, b, 0xca);
}
template <uint32_t C> static __device__ __forceinline__ uint32_t vconst() {
    uint32_t r;
    asm volatile("v_mov_b32 %0, %1" : "=v"(r) : "n"(C));
    return r;
}
static __device__ __forceinline__ uint32_t pk_bf16(float a, float b) {
    const f32x2 v = {a, b};
    return __builtin_bit_cast(uint32_t, __builtin_convertvector(v, bf16x2));
}
static __device__ __forceinline__ void pin(uint32_t& x) { asm volatile("" : "+v"(x)); }


static __device__ __forceinline__ void md_of(u32x2 v, const float* w, uint32_t* r) {
    float a0 = bf2f(v.x & 0xffff), a1 = bf2f(v.x >> 16), a2 = bf2f(v.y & 0xffff), a3 = bf2f(v.y >> 16);
#pragma unroll
    for (int j = 0; j < 4; j++) r[j] = f2bf(a0 * w[j] + a1 * w[4 + j] + a2 * w[8 + j] + a3 * w[12 + j]);
}
static __device__ __forceinline__ void load_w(const uint16_t* __restrict__ wdown, float* w) {
    u32x4 wv0 = *(const u32x4*)wdown, wv1 = *(const u32x4*)(wdown + 8);
#pragma unroll
    for (int i = 0; i < 4; i++) {
        w[2 * i] = bf2f(wv0[i] & 0xffffu); w[2 * i + 1] = bf2f(wv0[i] >> 16);
        w[8 + 2 * i] = bf2f(wv1[i] & 0xffffu); w[8 + 2 * i + 1] = bf2f(wv1[i] >> 16);
    }
}
#define LDS __attribute__((address_space(3)))
#define P_CNT 0
#define P_FLAG ((NB + 64) * 4)
#define P_LST (P_FLAG + 256)
#define LCAP 32
#define P_TAB (P_LST + 4 * LCAP * 16)
#define P_BUF (P_TAB + NB * 4)
#define P_LDS (P_BUF + NB * 64)
#define PD 2
#define LPOS 0x448763
#define SETP(n) __builtin_amdgcn_s_setprio(n)
#define MDL 1
#define MDM 3
#define RMAXREV 40.0f
#define HB 88
#define HREG(S, D) (HB + ((S) & 3) * 10 + (((S) >> 2) + (D)) * 2)

template <int N> struct IC { static constexpr int value = N; };
struct Raw { u32x4 o[2], i[2]; u32x2 a[2]; };
static __device__ __forceinline__ uint32_t lds_ld(uint32_t a) { return *(const LDS uint32_t*)(uintptr_t)a; }
static __device__ __forceinline__ void lds_st(uint32_t a, uint32_t v) { *(LDS uint32_t*)(uintptr_t)a = v; }
static __device__ __forceinline__ void lds_or(uint32_t a, uint32_t v) {
    __hip_atomic_fetch_or((LDS uint32_t*)(uintptr_t)a, v, __ATOMIC_RELAXED, __HIP_MEMORY_SCOPE_WORKGROUP);
}
static __device__ __forceinline__ uint32_t lds_add(uint32_t a, uint32_t v) {
    return __hip_atomic_fetch_add((LDS uint32_t*)(uintptr_t)a, v, __ATOMIC_RELAXED, __HIP_MEMORY_SCOPE_WORKGROUP);
}
static __device__ __forceinline__ uint32_t slot_w(uint32_t ca, uint32_t cv) {
    return (ca << 4) | __builtin_amdgcn_bitop3_b32(cv >> 11, ca, 0x3cu, 0x28);
}
static __device__ __forceinline__ uint32_t slot_x2(uint32_t aw, uint32_t ca) {
    return __builtin_amdgcn_bitop3_b32(aw, ca, 0xcu, 0x72);
}
static __device__ __forceinline__ uint32_t slot_x(uint32_t aw) { return slot_x2(aw, aw >> 4); }
template <int R> static __device__ __forceinline__ u32x2 hget() {
    u32x2 d;
    asm volatile("v_mov_b64 %0, v[%1:%2]" : "=v"(d) : "n"(R), "n"(R + 1));
    return d;
}
static __device__ __forceinline__ u32x2 hget_at(int r) {
    u32x2 d;
    asm volatile("s_set_gpr_idx_on %2, gpr_idx(SRC0)\n\ts_nop 1\n\tv_mov_b32 %0, v88\n\tv_mov_b32 %1, v89\n\ts_set_gpr_idx_off"
                 : "=&v"(d.x), "=&v"(d.y) : "s"(r));
    return d;
}
static __device__ __forceinline__ void hset_at(int r, u32x2 d) {
    asm volatile("s_set_gpr_idx_on %2, gpr_idx(DST)\n\ts_nop 1\n\tv_mov_b32 v88, %0\n\tv_mov_b32 v89, %1\n\ts_set_gpr_idx_off"
                 :: "v"(d.x), "v"(d.y), "s"(r));
}

template <int S, int D> static __device__ __forceinline__ uint32_t take(uint32_t V, uint32_t u16, uint32_t la, u32x2 nul) {
    const uint32_t a = __builtin_amdgcn_bitop3_b32(V, u16, 15u, 0x54);
    asm volatile("v_mov_b64 v[%4:%4+1], v[%3:%3+1]\n\tv_cmpx_ne_u32 vcc, -16, %0\n\tds_wrxchg_rtn_b64 v[%4:%4+1], %1, %2 offset:%5\n\ts_mov_b64 exec, -1"
                 :: "v"(a), "v"(la), "v"(nul), "n"(HREG(S, D)), "n"(HREG(S, 1 - D)), "n"(S * 256) : "memory", "vcc");
    return a;
}
#define PUT(P) asm volatile("s_bitcmp1_b32 %3, %4\n\ts_cbranch_scc0 1f\n\ts_waitcnt lgkmcnt(%5)\n\tbuffer_store_dwordx4 v[%2:%2+3], %0, %1, 0 offen sc1\n" \
                 "1:\n\ts_bitcmp1_b32 %3, %4+1\n\ts_cbranch_scc0 2f\n\ts_waitcnt lgkmcnt(%5)\n\tbuffer_store_dwordx4 v[%2:%2+3], %0, %1, 0 offen " P "\n\ts_nop 1\n2:" \
                 :: "v"(ad), "s"(rsA), "n"(HREG(S, 0)), "s"(pm), "n"(2 * S), "n"(W) : "memory", "scc")
template <int S, int W, bool WT> static __device__ __forceinline__ void put(u32x4 rsA, uint32_t ad, uint32_t pm) {
    if (WT) PUT("sc1 nt"); else PUT("nt");
}

#define CODE_AHEAD(SYM, NEXT, LANE) ({ u64 ca_; int cb_; \
    asm volatile("s_getpc_b64 vcc\n\ts_add_u32 vcc_lo, vcc_lo, " #SYM "@rel32@lo+4\n\ts_addc_u32 vcc_hi, vcc_hi, " #SYM "@rel32@hi+12\n\t" \
                 "s_mov_b64 %0, vcc\n\ts_max_i32 %1, " #NEXT "-" #SYM ", 0" : "=s"(ca_), "=s"(cb_) :: "scc", "vcc"); \
    __builtin_amdgcn_raw_buffer_load_b32(__builtin_amdgcn_make_buffer_rsrc((void*)ca_, 0, cb_, 0x00020000), ((blockIdx.x >> 3) * 64u + (LANE)) * 64u, 0, 0); })
extern "C" __global__ __launch_bounds__(NT, 4) __attribute__((amdgpu_num_vgpr(HB / 2))) void part_kernel(const int* __restrict__ iin, const int* __restrict__ iout,
                                                  const uint16_t* __restrict__ ang, const uint16_t* __restrict__ freq,
                                                  const u32x2* __restrict__ m, const uint16_t* __restrict__ wdown,
                                                  char* __restrict__ recs,
                                                  int T, int E, int ntile, int nstat, int ks, int lcap) {
    asm volatile("" :: "s"(iin), "s"(iout), "s"(ang), "s"(freq), "s"(m), "s"(wdown), "s"(recs), "s"(T), "s"(E), "s"(ntile), "s"(nstat), "s"(ks), "s"(lcap));
    const int tid0 = threadIdx.x;
    const int wv = __builtin_amdgcn_readfirstlane(tid0 >> 6);
    const int G = T >> 2;
    const int pos = (int)((blockIdx.x & 7) << 5) | (int)(blockIdx.x >> 3);
    float frr;
    unsigned* meta = (unsigned*)(recs - W_RECS);
    const uint32_t gbase = (LBASE + (uint32_t)lcap) * 128u;
    const uint32_t rbytes = gbase + (uint32_t)lcap * 2u;
    const __amdgpu_buffer_rsrc_t rsR = __builtin_amdgcn_make_buffer_rsrc(recs, 0, (int)rbytes, 0x00020000);
    const u32x4 rsA = {(uint32_t)(uintptr_t)recs, (uint32_t)((uintptr_t)recs >> 32) & 0xffffu, rbytes, 0x00020000u};
    const uint32_t g2 = 2u * (uint32_t)ks;
    const uint32_t far = 1u >> ((blockIdx.x >> 3) & 3u);
    const uint32_t SK = SKM + far * (SKH - SKM), SS = SSM + far * (SSH - SSM), UINC = 0x80000001u + SK * 64u;
    const uint32_t spec0 = gbase - 4u;
    auto fixed_tile = [&](int r) __attribute__((always_inline)) -> int { return r * NWG + ((pos + r * 37) & (NWG - 1)); };
    int tl0 = fixed_tile(0), tl1 = fixed_tile(1), tl2;
    uint32_t tk = 0u;
    const __amdgpu_buffer_rsrc_t rsO = __builtin_amdgcn_make_buffer_rsrc((void*)iout, 0, G * 16, 0x00020000);
    const __amdgpu_buffer_rsrc_t rsI = __builtin_amdgcn_make_buffer_rsrc((void*)iin, 0, G * 16, 0x00020000);
    const __amdgpu_buffer_rsrc_t rsG = __builtin_amdgcn_make_buffer_rsrc((void*)ang, 0, G * 8, 0x00020000);
    uint32_t t8 = (uint32_t)tid0 * 8u;
    auto load_one = [&](Raw& r, int tl, int idx) __attribute__((always_inline)) {
        const int j = idx < 4 ? idx >> 1 : idx & 1;
        const uint32_t g0 = (uint32_t)(tl * GR + j * NT);
        __builtin_amdgcn_sched_barrier(0);
        if (idx >= 4) r.a[j] = __builtin_amdgcn_raw_buffer_load_b64(rsG, t8, g0 * 8u, AUX_NT);
        else if (idx & 1) r.i[j] = __builtin_amdgcn_raw_buffer_load_b128(rsI, t8 + t8, g0 * 16u, AUX_NT);
        else r.o[j] = __builtin_amdgcn_raw_buffer_load_b128(rsO, t8 + t8, g0 * 16u, AUX_NT);
        __builtin_amdgcn_sched_barrier(0);
    };
    auto peek = [&](uint32_t at) __attribute__((always_inline)) -> uint32_t {
        return __hip_atomic_load((unsigned*)((char*)meta + at), __ATOMIC_RELAXED, __HIP_MEMORY_SCOPE_AGENT);
    };
    auto ready = [&]() __attribute__((always_inline)) {
        if (blockIdx.x != 0) {
            const uint32_t x = lds_ld(vconst<P_FLAG + 8>());
            while (peek(vconst<M_BEAT * 4>()) == x) __builtin_amdgcn_s_sleep(8);
        }
    };
    auto beat = [&]() __attribute__((always_inline)) {
        (void)__hip_atomic_fetch_add((unsigned*)((char*)meta + vconst<M_BEAT * 4>()), vconst<1>(), __ATOMIC_RELAXED, __HIP_MEMORY_SCOPE_AGENT);
    };
    auto draw = [&](uint32_t at) __attribute__((always_inline)) -> uint32_t {
        return __hip_atomic_fetch_add((unsigned*)((char*)meta + at), vconst<1>(), __ATOMIC_RELAXED, __HIP_MEMORY_SCOPE_AGENT);
    };
    uint32_t la;
    auto tab_at = [&]() __attribute__((always_inline)) -> uint32_t {
        return P_TAB + bfi(0xffu, bfi(0x3cu, t8 >> 2, t8 << 4), t8);
    };
    u32x2 nul;
    uint32_t U[2];
    {
        const uint32_t t = (uint32_t)tid0;
        la = P_BUF + ((t >> 5) << 12) + ((t & 0x18u) << 3) + ((t & 3u) << 4) + ((t & 4u) << 1);
        const uint32_t q = ((t >> 3) ^ (t >> 1 & 2u)) & 3u;
        nul.x = (0x7u >> q) << 31; nul.y = (0xbu >> q) << 31;
        U[0] = (((t >> 1) & 15u) * SS + ((t & 1u) * 64u + (t >> 5)) * SC + BPL(blockIdx.x)) * 128u + (0x80000000u - SK * 64u) + 8u - g2;
        U[1] = U[0] + 32u * SC * 128u;
    }
    auto log_line = [&](bool lg, uint32_t b) __attribute__((always_inline)) -> uint32_t {
        uint32_t lp = 0u;
        if (lg && (t8 & 0x38u) == 0u) {
            ready();
            lp = draw(vconst<M_LOG * 4>());
            __builtin_amdgcn_raw_buffer_store_b16((uint16_t)b, rsR, gbase + lp * 2u, 0, 0);
        }
        return LBASE + (uint32_t)__builtin_amdgcn_ds_bpermute((int)((t8 >> 1) & 0xe0u), (int)lp);
    };
    auto general = [&](int D, bool all, u64 sm) __attribute__((always_inline)) {
        uint32_t todo = __builtin_amdgcn_readfirstlane((uint32_t)sm | (uint32_t)(sm >> 32));
#pragma unroll 1
        while (todo) {
            const uint32_t S = (uint32_t)__builtin_ctz(todo) >> 1;
            todo &= ~(3u << (2u * S));
            pin(t8); pin(la);
            const uint32_t V = lds_ld(P_TAB + (t8 & ~63u) + S * 4u), g = V & 15u;
            const uint32_t col = S * 128u + ((t8 >> 1) & 0x60u) + (t8 >> 8);
            const bool act = all ? V != 0xffffffffu : (V & 0x80000009u) == 9u;
            const int hr = HREG((int)S, D) - HB;
            uint32_t ln = log_line(act && g > 8u + NTAIL, col);
            if (act) {
                const u32x2 N = *(const LDS u32x2*)(uintptr_t)(la + S * 256u);
                *(LDS u32x2*)(uintptr_t)(la + S * 256u) = nul;
                lds_st(P_TAB + (t8 & ~63u) + S * 4u, vconst<0xffffffffu>());
                if (!(g & 1u)) hset_at(hr, N);
                else {
                    uint32_t a, st;
                    if (g < 8u) { a = (V & ~15u) + ((t8 & 0x38u) << 1); st = 8u; }
                    else {
                        if (g <= 8u + NTAIL) ln = TBASE + ((g - 8u) >> 1) * TPL + col * TST + blockIdx.x;
                        a = ln * 128u + (la & 0x38u); st = 64u;
                    }
                    __builtin_amdgcn_raw_buffer_store_b64(hget_at(hr), rsR, a, 0, AUX_SC1);
                    __builtin_amdgcn_raw_buffer_store_b64(N, rsR, a + st, 0, AUX_SC1);
                }
            }
        }
    };
    unsigned epoch = 0;
    u64 pb2 = 0ull;
    u64 fm = 0ull, sm = 0ull;
    auto look = [&](u32x2 c) __attribute__((always_inline)) {
        uint32_t V[2];
        const bool late = __builtin_amdgcn_ballot_w64(((U[0] | U[1]) & 8u) != 0u) != 0ull;
        epoch++;
        asm volatile("v_mov_b32 %0, -1\n\tv_mov_b32 %1, -1\n\t"
                     "v_cmpx_le_u32 %6, %7, %2\n\tv_mov_b32 %0, %4\n\tv_add_u32 %4, %9, %4\n\tv_subrev_u32 %2, %8, %2\n\ts_mov_b64 exec, -1\n\t"
                     "v_cmpx_le_u32 vcc, %8, %3\n\tv_mov_b32 %1, %5\n\tv_add_u32 %5, %9, %5\n\tv_subrev_u32 %3, %8, %3\n\t"
                     "s_or_b64 %6, %6, exec\n\ts_mov_b64 exec, -1"
                     : "=&v"(V[0]), "=&v"(V[1]), "+v"(c.x), "+v"(c.y), "+v"(U[0]), "+v"(U[1]), "=&s"(fm) : "s"(CHV), "n"(CHV), "s"(UINC) : "vcc", "scc");
        *(LDS u32x2*)(uintptr_t)(P_CNT + t8) = c;
        pin(t8);
        const uint32_t tw = tab_at();
        lds_st(tw, V[0]);
        lds_st(tw + 64u, V[1]);
        if (wv == 1) lds_st(P_CNT + NB * 4 - 256 + (t8 >> 1), vconst<0x40000000u>());
        pb2 = __builtin_amdgcn_ballot_w64((int)(V[0] & V[1]) >= 0);
        sm = 0ull;
        if (__builtin_expect(late, 0)) {
            sm = __builtin_amdgcn_ballot_w64((V[0] & 0x80000009u) == 9u || (V[1] & 0x80000009u) == 9u);
#pragma unroll
            for (int e = 0; e < 2; e++)
                if ((V[e] & 0x8000000fu) == 9u + NTAIL) U[e] -= 2u * UINC;
        }
    };
    u32x4 Va, Vb;

    Raw raw[PD];
    {
        const int tid = tid0;
        asm volatile("" ::: "v88", "v127");
        typedef const __attribute__((address_space(4))) u32x4* cptr;
        u32x4 wd0 = ((cptr)(uintptr_t)wdown)[0], wd1 = ((cptr)(uintptr_t)wdown)[1];
        __builtin_amdgcn_sched_barrier(0);
        if (wv == 5) tk = CODE_AHEAD(part_kernel, acc_kernel, (uint32_t)tid & 63u);
        if (tid == 128) tk = peek(vconst<M_BEAT * 4>());
        if (blockIdx.x == 0 && wv == 3 && (tid & 63) < M_BEAT / 64) {
            __hip_atomic_store(meta + (tid & 63) * 64, 0u, __ATOMIC_RELAXED, __HIP_MEMORY_SCOPE_AGENT);
            __hip_atomic_store(meta + (tid & 63) * 64 + 1, 0u, __ATOMIC_RELAXED, __HIP_MEMORY_SCOPE_AGENT);
        }
        const __amdgpu_buffer_rsrc_t rsE = __builtin_amdgcn_make_buffer_rsrc((void*)m, 0, E * 8, 0x00020000);
        u32x2 mv[5];
        mv[4] = __builtin_amdgcn_raw_buffer_load_b64(rsE, (t8 & 511u) * (uint32_t)(E >> 6), 0, 0);
#pragma unroll
        for (int q = 0; q < 4; q++)
            mv[q] = __builtin_amdgcn_raw_buffer_load_b64(rsE, t8 & 511u, ((blockIdx.x >> 2) * 256u + (blockIdx.x & 3u) * 16u + (uint32_t)wv + 64u * q) * 512u, AUX_NT);
#pragma unroll
        for (int d = 0; d < MDL; d++) load_one(raw[1], tl0, d);
        asm volatile("" : "+s"(wd0), "+s"(wd1));
        const u32x2 ci = {CINIT, CINIT};
        *(LDS u32x2*)(uintptr_t)(P_CNT + tid * 8) = ci;
        if (tid < 64) lds_st(P_CNT + (NB + tid) * 4, 0x40000000u);
        if (tid < 8 && tid != 2) lds_st(P_FLAG + tid * 4, tid == 0 ? 0xffffffffu : 0u);
        if (tid == 128) lds_st(P_FLAG + 8, tk);
        const uint32_t lane = (uint32_t)tid & 63u;
        int e0[4];
        {
            float w[16];
#pragma unroll
            for (int i = 0; i < 4; i++) {
                w[2 * i] = bf2f(wd0[i] & 0xffffu); w[2 * i + 1] = bf2f(wd0[i] >> 16);
                w[8 + 2 * i] = bf2f(wd1[i] & 0xffffu); w[8 + 2 * i + 1] = bf2f(wd1[i] >> 16);
            }
            auto dot = [&](u32x2 v, int c) __attribute__((always_inline)) -> f32x2 {
                const f32x2 w0 = {w[c], w[c + 1]}, w1 = {w[4 + c], w[5 + c]}, w2 = {w[8 + c], w[9 + c]}, w3 = {w[12 + c], w[13 + c]};
                return w0 * bf2f(v.x & 0xffffu) + w1 * bf2f(v.x >> 16) + w2 * bf2f(v.y & 0xffffu) + w3 * bf2f(v.y >> 16);
            };
#pragma unroll
            for (int cp = 0; cp < 2; cp++) {
                if (cp == 1) {
#pragma unroll
                    for (int d = MDL; d < MDM; d++) load_one(raw[1], tl0, d);
                }
                if (blockIdx.x >= SLOTI / 64) continue;
                const f32x2 sd = dot(mv[4], 2 * cp);
#pragma unroll
                for (int e = 0; e < 2; e++) {
                    const int ex = (int)(wave_max(__float_as_uint(sd[e]) & 0x7fffffffu) >> 23);
                    e0[2 * cp + e] = __builtin_amdgcn_readfirstlane(ex == 0 ? 0 : min(max(ex - 129, -100), 100));
                }
                const f32x2 sc = {__uint_as_float((uint32_t)(127 - e0[2 * cp]) << 23), __uint_as_float((uint32_t)(127 - e0[2 * cp + 1]) << 23)};
                u32x4 o;
                u16x2 mx = {0, 0};
                f32x2 sq = {0.f, 0.f}, sg = {0.f, 0.f};
#pragma unroll
                for (int q = 0; q < 4; q++) {
                    const f32x2 y = dot(mv[q], 2 * cp) * sc;
                    const uint32_t hb = pk_bf16(y.x, y.y);
                    const f32x2 x = {__uint_as_float(hb << 16), __uint_as_float(hb & 0xffff0000u)};
                    o[q] = __builtin_bit_cast(uint32_t, __builtin_amdgcn_cvt_pkrtz(x.x, x.y));
                    mx = __builtin_elementwise_max(mx, __builtin_bit_cast(u16x2, hb & 0x7fff7fffu));
                    sq += x * x;
                    sg += x;
                }
                *(LDS u32x4*)(uintptr_t)(P_BUF + (cp * 64u + lane) * 272u + wv * 16) = o;
#pragma unroll
                for (int e = 0; e < 2; e++) {
                    const u32x4 st = {wave_max(mx[e]), __float_as_uint(wave_sum(sq[e])), __float_as_uint(wave_sum(sg[e])), 0u};
                    if (lane == 0u) *(LDS u32x4*)(uintptr_t)(P_LST + ((2 * cp + e) * 16 + wv) * 16) = st;
                }
            }
        }
        __builtin_amdgcn_sched_barrier(0);
        const uint16_t fq = freq[0];
#pragma unroll
        for (int d = MDM; d < 6; d++) load_one(raw[1], tl0, d);
#pragma unroll
        for (int d = 0; d < 6; d++)
            if (((LPOS >> (4 * d)) & 15) < 4) load_one(raw[0], tl1, d);
        __syncthreads();
        u32x4 mo[2];
#pragma unroll
        for (int cp = 0; cp < 2; cp++) mo[cp] = *(const LDS u32x4*)(uintptr_t)(P_BUF + (cp * 64u + (t8 >> 7)) * 272u + ((t8 + t8) & 0xf0u));
        if (blockIdx.x == 0 && wv == 3) asm volatile("s_waitcnt vmcnt(0)" ::: "memory");
        __syncthreads();
#pragma unroll
        for (int s = 0; s < 16; s++) *(LDS u32x2*)(uintptr_t)(la + s * 256) = nul;
        asm volatile("" : "+v"(raw[1].o[0]), "+v"(raw[1].o[1]), "+v"(raw[1].i[0]), "+v"(raw[1].i[1]), "+v"(raw[1].a[0]), "+v"(raw[1].a[1]));
        asm volatile("s_nop 0\n\tv_readfirstlane_b32 %0, %1" : "=s"(frr) : "v"(bf2f(fq) * 0.15915494309189535f));
        if (blockIdx.x < SLOTI / 64) {
            const __amdgpu_buffer_rsrc_t rsM = __builtin_amdgcn_make_buffer_rsrc((void*)(meta + W_MDC / 4), 0, (int)(128u * SLOTI * 4u), 0x00020000);
#pragma unroll
            for (int cp = 0; cp < 2; cp++) {
                const uint32_t row = cp * 64u + (t8 >> 7), c16 = (t8 + t8) & 0xf0u;
                __builtin_amdgcn_raw_buffer_store_b128(mo[cp], rsM, row * (SLOTI * 4u) + c16 + blockIdx.x * 256u, 0, AUX_SC1);
            }
            if (wv == 0) {
                const u32x4 st = *(const LDS u32x4*)(uintptr_t)(P_LST + t8 * 2u);
                const float sm1 = row_sum(__uint_as_float(st.z));
                const u32x2 r = {row_max(st.x) | (row_max(st.x ? 0xffffu - st.x : 0u) << 16), __float_as_uint(row_sum(__uint_as_float(st.y)))};
                if ((lane & 15u) == 15u) {
                    ((u32x2*)(meta + W_STAT / 4))[(lane >> 4) * NWG + blockIdx.x] = r;
                    meta[W_SUM / 4 + (lane >> 4) * NWG + blockIdx.x] = __float_as_uint(sm1);
                }
                if (blockIdx.x == 0 && lane < 4u) meta[M_E0 + lane] = (uint32_t)(lane == 0u ? e0[0] : lane == 1u ? e0[1] : lane == 2u ? e0[2] : e0[3]);
            }
        }
    }
    pin(la);

    uint32_t w[8], aw[8], xv[8];
    u64 nm[8];
    auto later2 = [&](int k) __attribute__((always_inline)) {
        uint32_t x0, x1;
        asm volatile("v_lshrrev_b32 %0, 4, %2\n\tv_lshrrev_b32 %1, 4, %5\n\tv_bitop3_b32 %0, %2, %0, 12 bitop3:0x72\n\tv_bitop3_b32 %1, %5, %1, 12 bitop3:0x72\n\t"
                     "s_not_b64 exec, %8\n\tds_write_b32 %2, %3 offset:%10\n\tds_or_b32 %0, %4 offset:%10\n\t"
                     "s_not_b64 exec, %9\n\tds_write_b32 %5, %6 offset:%10\n\tds_or_b32 %1, %7 offset:%10\n\ts_mov_b64 exec, -1"
                     : "=&v"(x0), "=&v"(x1)
                     : "v"(aw[k]), "v"(w[k]), "v"(xv[k]), "v"(aw[k + 1]), "v"(w[k + 1]), "v"(xv[k + 1]), "s"(nm[k]), "s"(nm[k + 1]), "n"(P_BUF) : "memory", "scc");
    };
    auto drain = [&]() __attribute__((always_inline)) {
        if (wv == 1) {
            const uint32_t q = (epoch + 1u) & 3u;
            const uint32_t n = __builtin_amdgcn_readfirstlane(lds_ld(vconst<P_FLAG + 16>() + q * 4u));
            if (__builtin_expect(n != 0u, 0)) {
                pin(t8);
                const uint32_t l = (t8 >> 3) & 63u;
                if (l < min(n, (uint32_t)LCAP)) {
                    const u32x4 e = *(const LDS u32x4*)(uintptr_t)(P_LST + (q * LCAP + l) * 16u);
                    lds_st(P_BUF + e.x, e.y);
                    lds_or(P_BUF + slot_x(e.x), e.z);
                }
                lds_st(vconst<P_FLAG + 16>() + q * 4u, vconst<0>());
            }
        }
    };
    auto again = [&](int D) __attribute__((always_inline)) {
        do {
            general(D, true, fm);
            __syncthreads();
            later2(0); later2(2); later2(4); later2(6);
            drain();
#pragma unroll
            for (int k = 0; k < 8; k++) {
                pin(aw[k]);
                bool due = false;
                if (aw[k] & 0x80000000u) {
                    xv[k] -= CHV;
                    if (xv[k] < CHV) {
                        const uint32_t f = (aw[k] >> 16) & 0x3ffu;
                        aw[k] = slot_w(aw[k] & 0xffffu, xv[k]);
                        xv[k] = f << ((xv[k] >> 8) & 31u);
                        due = true;
                    } else lds_st(vconst<P_FLAG>(), epoch + 1u);
                }
                nm[k] = ~__builtin_amdgcn_ballot_w64(due);
            }
            look(*(const LDS u32x2*)(uintptr_t)(P_CNT + t8));
            asm volatile("s_waitcnt lgkmcnt(0)" ::: "memory");
            __syncthreads();
        } while (__builtin_amdgcn_readfirstlane(lds_ld(vconst<P_FLAG>())) == epoch);
    };
    auto round = [&](auto DD, auto LL, Raw& rc, Raw& rn, int rd) __attribute__((always_inline)) {
        constexpr int D = decltype(DD)::value;
        constexpr bool LAST = decltype(LL)::value == 1, FIRST = decltype(LL)::value == 2;
        const int ng = min(GR, G - tl0 * GR);
        auto hook = [&](int p) __attribute__((always_inline)) {
#pragma unroll
            for (int q = 0; q < 6; q++)
            {
                const int lp = (LPOS >> (4 * q)) & 15;
                if (!LAST && lp < 4 && lp == p) load_one(rc, tl2, q);
                if (!LAST && lp >= 4 && (lp < 8 || lp >= 12 ? lp : lp - 8) == p) load_one(rn, tl1, q);
            }
        };
        uint32_t wn[8], ca[8], px[4], cv[8];
        float mx;
        float cs[2], cm[2] = {0.f, 0.f};
        auto convA = [&](int q) __attribute__((always_inline)) {
            if (LAST) return;
            const uint32_t a = rc.a[q >> 1][q & 1];
            const f32x2 av = {__uint_as_float(a << 16), __uint_as_float(a & 0xffff0000u)};
            f32x2 rv;
            asm("v_pk_mul_f32 %0, %1, %2 op_sel_hi:[1,0]" : "=v"(rv) : "v"(av), "s"((u64)__float_as_uint(frr)));
            mx = __builtin_elementwise_maximum(__builtin_elementwise_maximum(__builtin_fabsf(rv.x), __builtin_fabsf(rv.y)), (q & 1) ? mx : 0.f);
            cs[0] = __builtin_amdgcn_cosf(rv.x); cs[1] = __builtin_amdgcn_cosf(rv.y);
            asm volatile("" : "+v"(mx), "+v"(cs[0]), "+v"(cs[1]));
            if (FIRST) {
                const float c0 = fminf(fmaxf(cs[0], -1.f), 1.f), c1 = fminf(fmaxf(cs[1], -1.f), 1.f);
                cm[0] += c0 + c1; cm[1] += c0 * c0 + c1 * c1;
                if (q == 3 && wv == 4) {
                    const float a0 = wave_sum(cm[0]), a1 = wave_sum(cm[1]);
                    if ((t8 & 0x1f0u) == 0u) meta[W_SUM / 4 + (4 + ((t8 >> 3) & 1u)) * NWG + blockIdx.x] = __float_as_uint((t8 & 8u) ? a1 : a0);
                }
            }
            __builtin_amdgcn_sched_barrier(0);
        };
        auto convB = [&](int k) __attribute__((always_inline)) {
            if (LAST) return;
            const uint32_t o = rc.o[k >> 2][k & 3], i = rc.i[k >> 2][k & 3];
            wn[k] = (__builtin_amdgcn_alignbit(bfi(63u, o, o >> 5), __float_as_uint(cs[k & 1]), 30u) << 14) | (i >> 6);
            ca[k] = (bfi(63u, i, o) << 2) & 0x1ffcu;
            pin(wn[k]); pin(ca[k]);
            __builtin_amdgcn_sched_barrier(0);
        };
        auto convD = [&](int q) __attribute__((always_inline)) {
            if (LAST) return;
            asm volatile("v_cvt_pk_f16_f32 %0, |%1|, |%2|\n\tv_pk_mul_f16 %0, %0, %3 op_sel_hi:[1,0]" : "=v"(px[q]) : "v"(cs[0]), "v"(cs[1]), "s"(0x3ffu));
        };
        uint32_t cst;
        auto check = [&](int j) __attribute__((always_inline)) {
            if (LAST) return;
            if (__builtin_expect((__builtin_amdgcn_ballot_w64(!(mx <= RMAXREV)) | (u64)(uint32_t)(GR - ng)) != 0ull, 0)) {
                pin(t8);
                asm volatile("" : "+v"(rc.a[j]));
                const int lg = j * NT + (int)(t8 >> 3);
#pragma unroll
                for (int k = 0; k < 4; k++) {
                    const uint32_t a = (k & 1) ? (rc.a[j][k >> 1] >> 16) : (rc.a[j][k >> 1] & 0xffffu);
                    const bool nrm = __builtin_fabsf(frr * bf2f(a)) <= RMAXREV;
                    if (!(lg < ng && nrm)) ca[j * 4 + k] = NB * 4u + ((t8 >> 1) & 0xfcu);
                    if (lg < ng && !nrm) {
                        ready();
                        __builtin_amdgcn_raw_buffer_store_b32((uint32_t)((tl0 * GR + lg) * 4 + k), rsR, spec0 - draw(vconst<M_LOG * 4 + 4>()) * 4u, 0, 0);
                    }
                }
            }
            cst = vconst<CSTEP>();
        };
        auto ticket = [&](int k) __attribute__((always_inline)) {
            if (LAST) return;
            cv[k] = lds_add(P_CNT + ca[k], cst);
            __builtin_amdgcn_sched_barrier(0);
        };
        auto insert = [&](int j) __attribute__((always_inline)) {
            const int k0 = 4 * j;
            const bool odd = __builtin_amdgcn_ballot_w64(max(max(cv[k0], cv[k0 + 1]), max(cv[k0 + 2], cv[k0 + 3])) >= 2u * CHV) != 0ull;
#pragma unroll
            for (int k = k0; k < k0 + 4; k += 2) {
                uint32_t x0, x1;
                asm volatile("v_lshrrev_b32 %6, 11, %8\n\tv_lshrrev_b32 %7, 11, %9\n\t"
                             "v_bitop3_b32 %6, %6, %10, 60 bitop3:0x28\n\tv_bitop3_b32 %7, %7, %11, 60 bitop3:0x28\n\t"
                             "v_lshl_or_b32 %0, %10, 4, %6\n\tv_lshl_or_b32 %1, %11, 4, %7\n\t"
                             "v_lshlrev_b32_sdwa %2, %8, %12 dst_sel:DWORD dst_unused:UNUSED_PAD src0_sel:BYTE_1 src1_sel:WORD_0\n\t"
                             "v_lshlrev_b32_sdwa %3, %9, %12 dst_sel:DWORD dst_unused:UNUSED_PAD src0_sel:BYTE_1 src1_sel:WORD_1\n\t"
                             "v_bitop3_b32 %6, %0, %10, 12 bitop3:0x72\n\tv_bitop3_b32 %7, %1, %11, 12 bitop3:0x72\n\t"
                             "v_cmpx_gt_u32 %4, %15, %8\n\tds_write_b32 %0, %13 offset:%16\n\tds_or_b32 %6, %2 offset:%16\n\ts_mov_b64 exec, -1\n\t"
                             "v_cmpx_gt_u32 %5, %15, %9\n\tds_write_b32 %1, %14 offset:%16\n\tds_or_b32 %7, %3 offset:%16\n\ts_mov_b64 exec, -1"
                             : "=&v"(aw[k]), "=&v"(aw[k + 1]), "=&v"(xv[k]), "=&v"(xv[k + 1]), "=&s"(nm[k]), "=&s"(nm[k + 1]), "=&v"(x0), "=&v"(x1)
                             : "v"(cv[k]), "v"(cv[k + 1]), "v"(ca[k]), "v"(ca[k + 1]), "v"(px[k >> 1]), "v"(wn[k]), "v"(wn[k + 1]), "s"(CHV), "n"(P_BUF) : "memory");
                w[k] = wn[k]; w[k + 1] = wn[k + 1];
                if (__builtin_expect(odd, 0)) {
#pragma unroll
                    for (int q = k; q < k + 2; q++) {
                        nm[q] |= __builtin_amdgcn_ballot_w64(cv[q] >= 2u * CHV);
                        if (cv[q] >= 2u * CHV && cv[q] < 0x40000000u) {
                            const uint32_t lq = (epoch + 2u) & 3u;
                            uint32_t p = LCAP;
                            if (cv[q] < 3u * CHV) p = lds_add(vconst<P_FLAG + 16>() + lq * 4u, vconst<1>());
                            if (p < LCAP) {
                                const u32x2 e = {aw[q], w[q]};
                                *(LDS u32x2*)(uintptr_t)(P_LST + (lq * LCAP + p) * 16u) = e;
                                lds_st(P_LST + (lq * LCAP + p) * 16u + 8u, xv[q]);
                            } else {
                                aw[q] = ca[q] | ((q & 1) ? (px[q >> 1] & 0xffff0000u) : (px[q >> 1] << 16)) | 0x80000000u;
                                xv[q] = cv[q] - CHV;
                                lds_st(vconst<P_FLAG>(), epoch);
                            }
                        }
                    }
                }
            }
        };
        constexpr int P0 = D ? 0 : 3, P1 = D ? 1 : 2, P2 = D ? 2 : 1, P3 = D ? 3 : 0;
        const uint32_t fl = lds_ld(vconst<P_FLAG>());
        if (!LAST) asm volatile("" : "+v"(rc.o[0]), "+v"(rc.o[1]), "+v"(rc.i[0]), "+v"(rc.i[1]), "+v"(rc.a[0]), "+v"(rc.a[1]));
        if (!LAST && (rd + 4 >= nstat || tl1 >= ntile) && wv == 2) {
            pin(tk);
            if ((t8 & 511u) == 0u) {
                if (tl1 >= ntile) {
                    if (rd + 3 <= nstat) ready();
                    (void)draw(vconst<M_MD * 4>());
                } else if (rd + 4 == nstat) tk = peek(vconst<M_BEAT * 4>());
                else {
                    if (rd + 3 == nstat) {
                        if (tk == lds_ld(vconst<P_FLAG + 8>())) ready();
                        tk = 0u;
                    }
                    lds_st(vconst<P_FLAG + 4>(), tk);
                    if ((int)tk < ntile - nstat * NWG) tk = draw(vconst<M_TILE * 4>());
                }
            }
        }
        if (!LAST && blockIdx.x == 0 && wv == 3 && (t8 & 511u) == 0u) beat();
        hook(12);
        pin(t8);
        const uint32_t tr = P_TAB + (t8 & ~63u), u16 = (t8 + t8) & 0x70u;
        convA(0);
        convB(0);
        const bool ag = __builtin_amdgcn_readfirstlane(fl) == epoch;
        if (!FIRST && __builtin_expect(LAST || ag || sm != 0ull, 0)) {
            if (ag) again(D);
            general(D, false, sm);
            Va = *(const LDS u32x4*)(uintptr_t)(P_TAB + (t8 & ~63u) + P0 * 16);
            Vb = *(const LDS u32x4*)(uintptr_t)(P_TAB + (t8 & ~63u) + P1 * 16);
            asm volatile("" : "+v"(Va), "+v"(Vb));
        }
        uint32_t pq = (uint32_t)pb2 | (uint32_t)(pb2 >> 32);
        pq = (pq | (pq >> 1)) & 0x55555555u;
        pq = __builtin_amdgcn_readfirstlane(pq + (pq & (far * NEARM)));
        uint32_t ad[8];
        if (!FIRST) ad[0] = take<4 * P0 + 0, D>(Va[0], u16, la, nul);
        convB(1);
        if (!FIRST) ad[1] = take<4 * P0 + 1, D>(Va[1], u16, la, nul);
        convD(0);
        convA(1);
        if (!FIRST) ad[2] = take<4 * P0 + 2, D>(Va[2], u16, la, nul);
        convB(2);
        hook(13);
        if (!FIRST) ad[3] = take<4 * P0 + 3, D>(Va[3], u16, la, nul);
        convB(3);
        if (!FIRST) put<4 * P0 + 0, LAST ? 3 : 3, LAST>(rsA, ad[0], pq);
        if (!FIRST) ad[4] = take<4 * P1 + 0, D>(Vb[0], u16, la, nul);
        convD(1);
        check(0);
        ticket(0);
        hook(4);
        if (!FIRST) put<4 * P0 + 1, LAST ? 3 : 4, LAST>(rsA, ad[1], pq);
        if (!FIRST) ad[5] = take<4 * P1 + 1, D>(Vb[1], u16, la, nul);
        convA(2);
        ticket(1);
        if (!FIRST) put<4 * P0 + 2, LAST ? 3 : 5, LAST>(rsA, ad[2], pq);
        if (!FIRST) ad[6] = take<4 * P1 + 2, D>(Vb[2], u16, la, nul);
        convB(4);
        ticket(2);
        if (!FIRST) put<4 * P0 + 3, LAST ? 3 : 6, LAST>(rsA, ad[3], pq);
        if (!FIRST) Va = *(const LDS u32x4*)(uintptr_t)(tr + P2 * 16);
        __builtin_amdgcn_sched_barrier(0);
        if (!FIRST) ad[7] = take<4 * P1 + 3, D>(Vb[3], u16, la, nul);
        convB(5);
        ticket(3);
        SETP(2);
        if (!FIRST) put<4 * P1 + 0, LAST ? 4 : 8, LAST>(rsA, ad[4], pq);
        if (!FIRST) ad[0] = take<4 * P2 + 0, D>(Va[0], u16, la, nul);
        convD(2);
        convA(3);
        hook(5);
        if (!FIRST) put<4 * P1 + 1, LAST ? 4 : 7, LAST>(rsA, ad[5], pq);
        if (!FIRST) ad[1] = take<4 * P2 + 1, D>(Va[1], u16, la, nul);
        convB(6);
        if (!FIRST) put<4 * P1 + 2, LAST ? 4 : 6, LAST>(rsA, ad[6], pq);
        if (!FIRST) ad[2] = take<4 * P2 + 2, D>(Va[2], u16, la, nul);
        convB(7);
        if (!FIRST) put<4 * P1 + 3, LAST ? 3 : 4, LAST>(rsA, ad[7], pq);
        if (!FIRST) Vb = *(const LDS u32x4*)(uintptr_t)(tr + P3 * 16);
        __builtin_amdgcn_sched_barrier(0);
        if (!FIRST) ad[3] = take<4 * P2 + 3, D>(Va[3], u16, la, nul);
        convD(3);
        check(1);
        ticket(4);
        SETP(1);
        hook(6);
        if (!FIRST) put<4 * P2 + 0, LAST ? 4 : 5, LAST>(rsA, ad[0], pq);
        if (!FIRST) ad[4] = take<4 * P3 + 0, D>(Vb[0], u16, la, nul);
        ticket(5);
        if (!FIRST) put<4 * P2 + 1, LAST ? 4 : 6, LAST>(rsA, ad[1], pq);
        if (!FIRST) ad[5] = take<4 * P3 + 1, D>(Vb[1], u16, la, nul);
        ticket(6);
        if (!FIRST) put<4 * P2 + 2, LAST ? 4 : 7, LAST>(rsA, ad[2], pq);
        if (!FIRST) ad[6] = take<4 * P3 + 2, D>(Vb[2], u16, la, nul);
        ticket(7);
        SETP(0);
        if (!FIRST) put<4 * P2 + 3, LAST ? 3 : 7, LAST>(rsA, ad[3], pq);
        if (!FIRST) ad[7] = take<4 * P3 + 3, D>(Vb[3], u16, la, nul);
        hook(7);
        SETP(3);
        asm volatile("s_waitcnt lgkmcnt(0)" ::: "memory");
        __syncthreads();
        u32x2 cn;
        uint32_t tv;
        if (!LAST) { cn = *(const LDS u32x2*)(uintptr_t)(P_CNT + t8); tv = lds_ld(vconst<P_FLAG + 4>()); }
        __builtin_amdgcn_sched_barrier(0);
        if (!FIRST) later2(0);
        if (!FIRST) put<4 * P3, 15, LAST>(rsA, ad[4], pq);
        if (!FIRST) later2(2);
        if (!FIRST) put<4 * P3 + 1, 15, LAST>(rsA, ad[5], pq);
        if (!FIRST) drain();
        if (!LAST) {
            tl2 = rd + 2 < nstat ? fixed_tile(rd + 2) : nstat * NWG + (int)__builtin_amdgcn_readfirstlane(tv);
            look(cn);
        }
        hook(0);
        SETP(2);
        if (!FIRST) later2(4);
        if (!FIRST) put<4 * P3 + 2, 15, LAST>(rsA, ad[6], pq);
        if (!FIRST) later2(6);
        if (!FIRST) put<4 * P3 + 3, 15, LAST>(rsA, ad[7], pq);
        if (!LAST) {
            hook(1);
            SETP(1);
            insert(0);
            hook(2);
            SETP(0);
            pin(t8);
            Va = *(const LDS u32x4*)(uintptr_t)(P_TAB + (t8 & ~63u) + P3 * 16);
            Vb = *(const LDS u32x4*)(uintptr_t)(P_TAB + (t8 & ~63u) + P2 * 16);
            __builtin_amdgcn_sched_barrier(0);
            insert(1);
            hook(3);
            SETP(3);
            asm volatile("s_waitcnt lgkmcnt(0)" ::: "memory");
            __syncthreads();
            tl0 = tl1; tl1 = tl2;
        }
    };
#define HMV(A, B) "v_mov_b64 v[" #A ":" #A "+1], v[" #B ":" #B "+1]\n\t"
#define HMV4(A) HMV(A + 8, A + 6) HMV(A + 6, A + 4) HMV(A + 4, A + 2) HMV(A + 2, A)
    round(IC<1>(), IC<2>(), raw[1], raw[0], 0);
    int rd = 1;
#pragma unroll 1
    for (; tl1 < ntile; rd += 2) {
        round(IC<0>(), IC<0>(), raw[0], raw[1], rd);
        round(IC<1>(), IC<0>(), raw[1], raw[0], rd + 1);
    }
    if (tl0 >= ntile) asm volatile(HMV4(88) HMV4(98) HMV4(108) HMV4(118));
    asm volatile("" : "+s"(tl0));
    if (tl0 < ntile) round(IC<0>(), IC<0>(), raw[0], raw[1], rd);
    if (wv == 5) tk = CODE_AHEAD(acc_kernel, gen_scatter_kernel, (t8 >> 3) & 63u);
    round(IC<1>(), IC<1>(), raw[1], raw[0], 0);
    if (__builtin_amdgcn_readfirstlane(lds_ld(P_FLAG + 16 + ((epoch + 2u) & 3u) * 4u)) != 0u) {
        asm volatile("s_waitcnt lgkmcnt(0)" ::: "memory");
        __syncthreads();
        look(*(const LDS u32x2*)(uintptr_t)(P_CNT + t8));
        general(0, true, fm);
        __syncthreads();
        drain();
    }
    {
        uint32_t* tcnt = meta + W_TCNT / 4;
        const u32x2 c = *(const LDS u32x2*)(uintptr_t)(P_CNT + t8);
        const uint32_t tw = tab_at();
#pragma unroll
        for (int e = 0; e < 2; e++) {
            const uint32_t g = U[e] & 15u;
            lds_st(tw + e * 64u, g >= 8u ? 0x80000000u : ((U[e] + ((g & 1u) ? 0u : SK * 64u)) & 0x7fffff81u) | (((7u - g) << 1) & 12u));
            *(LDS uint8_t*)(uintptr_t)(P_CNT + (t8 & ~511u) + (((tw - P_TAB) >> 2) & 127u) + e * 16u) =
                (uint8_t)(g < 8u ? 0u : ((g - 8u) >> 1) + (((c[e] + 2u * CSTEP) >> 15) << 2) + (g & 1u) * 0x50u);
        }
        asm volatile("s_waitcnt lgkmcnt(0)" ::: "memory");
        __syncthreads();
        const uint32_t u16 = (t8 + t8) & 0x70u;
        u32x4 En[4];
#pragma unroll
        for (int p = 0; p < 4; p++) En[p] = *(const LDS u32x4*)(uintptr_t)(P_TAB + (t8 & ~63u) + p * 16u);
        const u32x4 cb = *(const LDS u32x4*)(uintptr_t)(P_CNT + (t8 & ~511u) + ((t8 >> 2) & 0x70u));
        u32x2 N[16];
#pragma unroll
        for (int s = 0; s < 16; s++) N[s] = *(const LDS u32x2*)(uintptr_t)(la + s * 256u);
        if (u16 == 0u) *(u32x4*)(tcnt + blockIdx.x * 512u + (t8 >> 4)) = cb;
        const uint32_t sg = P_BUF + ((t8 & ~63u) << 4);
        uint32_t ps;
        auto rest = [&](auto SL) __attribute__((always_inline)) {
            constexpr int s = decltype(SL)::value;
            const uint32_t en = En[s >> 2][s & 3], by = cb[s >> 2] >> ((s & 3) * 8);
            const u32x2 Hr = hget<HREG(s, 0)>();
            const uint32_t at = ps + ((la & 0x38u) ^ ((s & 3) << 4));
            *(LDS u32x2*)(uintptr_t)at = Hr;
            *(LDS u32x2*)(uintptr_t)(at + (by & 0x40u)) = N[s];
            ps += (by & 0x3cu) << 2;
            const u32x2 Hs = (en & 1u) ? Hr : nul;
            const u32x4 d = {Hs.x, Hs.y, N[s].x, N[s].y};
            uint32_t a = __builtin_amdgcn_bitop3_b32(en, u16, 15u, 0x54);
            {
                const u32x4 rs_ = rsA;
                const uint32_t far_ = ((NEARM >> (2 * s)) & 1u) ? far : 0u;
                asm volatile("s_cmp_eq_u32 %3, 0\n\ts_cbranch_scc0 1f\n\tbuffer_store_dwordx4 %0, %1, %2, 0 offen sc1\n\ts_branch 2f\n"
                             "1:\n\tbuffer_store_dwordx4 %0, %1, %2, 0 offen sc1 nt\n2:\n\ts_nop 1" :: "v"(d), "v"(a), "s"(rs_), "s"(far_) : "memory", "scc");
            }
            if (__builtin_expect(__builtin_amdgcn_ballot_w64((en & 12u) != 0u) != 0ull, 0)) {
                const u32x4 dn = {nul.x, nul.y, nul.x, nul.y};
#pragma unroll 1
                for (uint32_t k = (en >> 2) & 3u; k > 0u; k--) {
                    a += SK * 128u;
                    __builtin_amdgcn_raw_buffer_store_b128(dn, rsR, a, 0, AUX_SC1);
                }
            }
        };
        auto pack = [&](int p) __attribute__((always_inline)) {
            const uint32_t a = (SBASE + ((((t8 >> 1) & 0x60u) + (t8 >> 8)) * 2u + p) * NWG + blockIdx.x) * 128u + u16;
            asm volatile("" ::: "memory");
#pragma unroll 1
            for (uint32_t q = 0; __builtin_amdgcn_ballot_w64(sg + q < ps) != 0ull; q += 256u) {
                const u32x4 d0 = *(const LDS u32x4*)(uintptr_t)(sg + q + u16), d1 = *(const LDS u32x4*)(uintptr_t)(sg + q + 128u + u16);
                __builtin_amdgcn_raw_buffer_store_b128(d0, rsR, sg + q < ps ? a + q * SPL : 0xffffff00u, 0, AUX_SC1);
                __builtin_amdgcn_raw_buffer_store_b128(d1, rsR, sg + q + 128u < ps ? a + (q + 128u) * SPL : 0xffffff00u, 0, AUX_SC1);
            }
            asm volatile("" ::: "memory");
        };
        asm volatile("" : "+v"(N[0]), "+v"(N[1]), "+v"(N[2]), "+v"(N[3]), "+v"(N[4]), "+v"(N[5]), "+v"(N[6]), "+v"(N[7]) :: "memory");
        asm volatile("" : "+v"(N[8]), "+v"(N[9]), "+v"(N[10]), "+v"(N[11]), "+v"(N[12]), "+v"(N[13]), "+v"(N[14]), "+v"(N[15]) :: "memory");
        ps = sg;
        rest(IC<0>()); rest(IC<1>()); rest(IC<2>()); rest(IC<3>()); rest(IC<4>()); rest(IC<5>()); rest(IC<6>()); rest(IC<7>());
        pack(0);
        ps = sg;
        rest(IC<8>()); rest(IC<9>()); rest(IC<10>()); rest(IC<11>()); rest(IC<12>()); rest(IC<13>()); rest(IC<14>()); rest(IC<15>());
        pack(1);
    }
    asm volatile("" :: "v"(tk));
    if (blockIdx.x == 0 && t8 == 1536u) {
        do {
            beat();
            __builtin_amdgcn_s_sleep(8);
        } while (peek(vconst<M_MD * 4>()) < NWG);
    }
}

#define A_MD 0
#define A_ACC (SLOTI * 4)
#define A_FLAG (A_ACC + SLOTO * 4)
#define A_LDS (A_FLAG + 512)
static_assert(SLOTI * 4 == 3 * NT * 16 + 1024, "md staging assumes three full vectors and 256 dwords");
#define BIAS 0x40004000u
#define NFAST 8
#define NLD 10
#define MDF 0x0a11333
#define MDP 0xaaaaa
#define HP 2
#define SHARE (SLOTO / 8)
#define SPINCAP 4096u
#define ZSIG 9.0f
#define HSLOW 0x80008000u
#define GAS __attribute__((address_space(1)))

extern "C" __global__ __launch_bounds__(NT, 4) __attribute__((amdgpu_num_vgpr(53))) void acc_kernel(char* ws, int T, int E, int ks, int lcap, float zf, int pad,
                                                 const uint16_t* __restrict__ centers, const uint16_t* __restrict__ wrbf,
                                                 const uint16_t* __restrict__ wsbf, const uint16_t* __restrict__ wup,
                                                 const u32x2* __restrict__ m, const uint16_t* __restrict__ dist, u32x2* __restrict__ out,
                                                 const int* __restrict__ iin, const int* __restrict__ iout, const uint16_t* __restrict__ ang,
                                                 const uint16_t* __restrict__ wdown, const uint16_t* __restrict__ freq) {
    const char* recs = ws + W_RECS;
    const u32x4* tcnt = (const u32x4*)(ws + W_TCNT);
    const uint32_t* mdc = (const uint32_t*)(ws + W_MDC);
    const u32x4* stat = (const u32x4*)(ws + W_STAT);
    uint32_t* partial = (uint32_t*)(ws + W_RECS + (((size_t)LBASE * 128u + (size_t)lcap * 130u + 255u) & ~(size_t)255u));
    unsigned* meta = (unsigned*)ws;
    extern __shared__ __attribute__((aligned(16))) char smem[];
    unsigned* acc = (unsigned*)(smem + A_ACC);
    unsigned* s_flag = (unsigned*)(smem + A_FLAG);
    const int tid = threadIdx.x;
    const int lane = tid & 63;
    const int wv = __builtin_amdgcn_readfirstlane(tid >> 6);
    const int cp = (blockIdx.x >> 3) & 1;
    const int h = blockIdx.x & 3;
    const int j = ((blockIdx.x >> 2) & 1) * 16 + (blockIdx.x >> 4);
    u32x4 tc;
    const __amdgpu_buffer_rsrc_t rsM = __builtin_amdgcn_make_buffer_rsrc((void*)mdc, 0, (int)(128u * SLOTI * 4u), 0x00020000);

    u32x4 pm[4];
    u32x4 ring[NLD];
    auto load_md = [&](int ff, int pl, int mdp) __attribute__((always_inline)) {
        const uint32_t so = (uint32_t)(cp * 64 + ff * 4 + h) * (SLOTI * 4u);
#pragma unroll
        for (int v = 0; v < 3; v++)
            if (pl < 0 || pl == ((mdp >> (4 * v)) & 15)) pm[v] = __builtin_amdgcn_raw_buffer_load_b128(rsM, (uint32_t)tid * 16u, so + (uint32_t)v * (NT * 16u), 0);
        if ((pl < 0 || pl == (mdp & 15)) && wv == 0) pm[3] = __builtin_amdgcn_raw_buffer_load_b128(rsM, (uint32_t)tid * 16u, so + 3u * NT * 16u, 0);
        __builtin_amdgcn_sched_barrier(0);
    };
    const uint32_t lf = (uint32_t)(tid >> 9) * (SKH * 128u) + (uint32_t)(tid & 0x1e0) * 16u + (uint32_t)(tid & 12) * 32u + (uint32_t)(tid & 16) * 4u + (uint32_t)(tid & 3) * 16u;
    const uint32_t lp = (uint32_t)(tid >> 8) * (SKM * 128u) + (uint32_t)((tid >> 2) & 63) * 128u + (uint32_t)(tid & 3) * 16u;
    auto load_main = [&](int i, int ff, bool near = false) __attribute__((always_inline)) -> u32x4 {
        const uint32_t q = (uint32_t)(((i >> 1) - HP) & 3), SK = q ? SKM : SKH, SS = q ? SSM : SSH;
        const __amdgpu_buffer_rsrc_t rs = __builtin_amdgcn_make_buffer_rsrc((void*)(recs + (QAT(q) + (uint32_t)ff * SS + (uint32_t)(h * 32 + j) * SC) * 128u), 0, ks * (int)(SK * 128u), 0x00020000);
        if (q == 0u && near) return __builtin_amdgcn_raw_buffer_load_b128(rs, lf, (i & 1) * (int)(2u * SK * 128u), 0);
        if (q == 0u) return __builtin_amdgcn_raw_buffer_load_b128(rs, lf, (i & 1) * (int)(2u * SK * 128u), AUX_NT);
        return __builtin_amdgcn_raw_buffer_load_b128(rs, lp + (uint32_t)(i & 1) * 64u, 0, 0);
    };
    const __amdgpu_buffer_rsrc_t rsT = __builtin_amdgcn_make_buffer_rsrc((void*)recs, 0, (int)(LBASE * 128u), 0x00020000);
    const uint32_t tof = (uint32_t)(lane & 2) * (TPL * 64u) + (uint32_t)(tid >> 2) * 128u + (uint32_t)(lane & 1) * 64u + 4u + (TBASE + (uint32_t)(h * 32 + j) * TST) * 128u;
    const uint32_t sp0 = (SBASE + (uint32_t)(h * 32 + j) * (2u * NWG) + (uint32_t)(tid >> 2)) * 128u;
    auto tail_rank = [&](uint32_t c, uint32_t sp, int ff, uint32_t t0, int& dst, uint32_t& en) __attribute__((always_inline)) -> int {
        const int k4 = (int)(((uint32_t)(lane & 3) + t0) * 4u) - (int)((c & 3u) * 8u);
        const int left = (int)((c >> 2) & 15u) - k4;
        const bool has = left > 0;
        uint32_t ea = tof + (uint32_t)ff * (128u * TST * 128u), eb = sp + (uint32_t)(k4 * 16);
        eb = sp0 + (uint32_t)(ff >> 3) * (NWG * 128u) + eb + __umul24(eb >> 7, (SPL - 1u) * 128u) + (uint32_t)min(max(left, 0), 4);
        asm volatile("" : "+v"(ea), "+v"(eb));
        en = k4 < 0 ? ea : eb;
        const u64 mask = __builtin_amdgcn_ballot_w64(has);
        const int n = __builtin_popcountll(mask);
        const int rank = __builtin_amdgcn_mbcnt_hi((uint32_t)(mask >> 32), __builtin_amdgcn_mbcnt_lo((uint32_t)mask, 0u));
        const int r = has ? rank : (n + lane - rank);
        dst = ((r & 15) << 4) | ((r >> 4) << 2);
        return n;
    };
    auto load_tail = [&](uint32_t en, u64& tm) __attribute__((always_inline)) -> u32x4 {
        const bool on = (uint32_t)(lane & 3) < (en & 7u);
        tm = __builtin_amdgcn_ballot_w64(on);
        const uint32_t at = (en & 0x70u) + (uint32_t)(lane & 3) * 16u;
        return __builtin_amdgcn_raw_buffer_load_b128(rsT, on ? (en & ~127u) + at + __umul24(at >> 7, (SPL - 1u) * 128u) : 0xffffff00u, 0, 0);
    };
    f32x2 fa[4][4];
    u32x4 em[2];
    u32x2 dw2;
    uint32_t wl;
    const __amdgpu_buffer_rsrc_t rsE = __builtin_amdgcn_make_buffer_rsrc((void*)m, 0, E * 8, 0x00020000);
    const __amdgpu_buffer_rsrc_t rsD = __builtin_amdgcn_make_buffer_rsrc((void*)dist, 0, E * 2, 0x00020000);
    auto acc_vec = [&](int i) __attribute__((always_inline)) -> u32x4 {
        const uint32_t a = A_ACC + (uint32_t)(i >> 4) * 256u + (uint32_t)(i & 15) * 8u;
        const u32x2 x = *(const LDS u32x2*)(uintptr_t)a, y = *(const LDS u32x2*)(uintptr_t)(a + 128u);
        const u32x4 v = {x.x, x.y, y.x, y.y};
        return v;
    };
    auto edge0 = [&](int sh) __attribute__((always_inline)) -> uint32_t {
        return (tid < SHARE / 4) ? (uint32_t)(((sh * (SHARE / 64) + (tid >> 4)) << 11) | (j << 6) | ((tid & 15) * 2)) : 0x0fffff00u;
    };
    auto load_m = [&](int sh) __attribute__((always_inline)) {
        em[0] = __builtin_amdgcn_raw_buffer_load_b128(rsE, edge0(sh) * 8u, 0, AUX_NT);
        em[1] = __builtin_amdgcn_raw_buffer_load_b128(rsE, edge0(sh) * 8u + 256u, 0, AUX_NT);
    };
    auto load_d = [&](int sh) __attribute__((always_inline)) {
        dw2.x = __builtin_amdgcn_raw_buffer_load_b32(rsD, edge0(sh) * 2u, 0, AUX_NT);
        dw2.y = __builtin_amdgcn_raw_buffer_load_b32(rsD, edge0(sh) * 2u + 64u, 0, AUX_NT);
    };
    auto load_wl = [&]() __attribute__((always_inline)) {
        const u64 p0 = (u64)wsbf, p1 = (u64)wrbf, p2 = (u64)centers, p3 = (u64)wup;
        const u64 pa = lane < 16 ? p0 : p1, pb = lane < 40 ? p2 : p3;
        wl = ((const GAS uint32_t*)(lane < 32 ? pa : pb))[lane & (lane < 32 ? 15 : lane < 40 ? 3 : 7)];
    };
    auto factor = [&](int r, bool first) __attribute__((always_inline)) {
        const float sq = 3.7982826f;
        const f32x2 d01 = {bf2f(dw2.x & 0xffffu), bf2f(dw2.x >> 16)}, d23 = {bf2f(dw2.y & 0xffffu), bf2f(dw2.y >> 16)};
        const uint32_t cw = __builtin_amdgcn_readlane(wl, 32 + (r >> 1));
        const float cn = bf2f((r & 1) ? (cw >> 16) : (cw & 0xffffu));
        f32x2 w[4];
#pragma unroll
        for (int k = 0; k < 4; k++) {
            const uint32_t x = __builtin_amdgcn_readlane(wl, (k >> 1) * 16 + r * 2 + (k & 1));
            w[k].x = bf2f(x & 0xffffu); w[k].y = bf2f(x >> 16);
        }
        const f32x2 y01 = (d01 - cn) * sq, y23 = (d23 - cn) * sq;
        const f32x2 t01 = -y01 * y01, t23 = -y23 * y23;
        const float rb[4] = {__builtin_amdgcn_exp2f(t01.x), __builtin_amdgcn_exp2f(t01.y), __builtin_amdgcn_exp2f(t23.x), __builtin_amdgcn_exp2f(t23.y)};
#pragma unroll
        for (int q = 0; q < 4; q++) {
#pragma unroll
            for (int k = 0; k < 4; k++) fa[q][k] = first ? w[k] * rb[q] : __builtin_elementwise_fma(w[k], (f32x2)(rb[q]), fa[q][k]);
        }
    };
    auto edges = [&](int sh) __attribute__((always_inline)) {
        load_d(sh);
        load_m(sh);
        factor(0, true);
#pragma unroll 1
        for (int r = 1; r < 8; r++) factor(r, false);
    };
#define ADT(V, O) "ds_write_addtid_b32 " V " offset:" #O "\n\t"
    auto store_md = [&]() __attribute__((always_inline)) {
        asm volatile("s_mov_b32 m0, %12\n\ts_nop 0\n\t" ADT("%0", 0) ADT("%1", 256) ADT("%2", 512) ADT("%3", 768) ADT("%4", 16384) ADT("%5", 16640) ADT("%6", 16896) ADT("%7", 17152)
                     ADT("%8", 32768) ADT("%9", 33024) ADT("%10", 33280) ADT("%11", 33536)
                     :: "v"(pm[0][0]), "v"(pm[0][1]), "v"(pm[0][2]), "v"(pm[0][3]), "v"(pm[1][0]), "v"(pm[1][1]), "v"(pm[1][2]), "v"(pm[1][3]),
                        "v"(pm[2][0]), "v"(pm[2][1]), "v"(pm[2][2]), "v"(pm[2][3]), "s"(wv * 1024) : "memory");
        if (wv == 0) asm volatile("s_mov_b32 m0, 0\n\ts_nop 0\n\t" ADT("%0", 49152) ADT("%1", 49408) ADT("%2", 49664) ADT("%3", 49920)
                                  :: "v"(pm[3][0]), "v"(pm[3][1]), "v"(pm[3][2]), "v"(pm[3][3]) : "memory");
    };
    const int me = h * 2 + cp;
    GAS unsigned* dw = (GAS unsigned*)(meta + M_DONE + j * 64);
    const bool fin = wv < (SHARE / 4 + 63) / 64;
    bool fast, more;
    uint32_t need, mg0, mg1;
    uint32_t guard = 0;
    uint32_t sgn = 0x8000u;
    pin(sgn);

#define GAT3(M) { uint32_t a0_, a1_, a2_; asm volatile( \
        "v_lshlrev_b16 %0, 2, %3\n\tv_lshlrev_b16 %1, 2, %4\n\tv_lshlrev_b16 %2, 2, %5\n\t" \
        "ds_read_b32 v[%6], %0\n\tds_read_b32 v[%6+1], %1\n\tds_read_b32 v[%6+2], %2" \
        : "=&v"(a0_), "=&v"(a1_), "=&v"(a2_) : "v"(w0), "v"(w1), "v"(w2), "n"(M) : "memory"); }
#define MIXL " op_sel_hi:[1,1,0]\n\t"
#define MIXH " op_sel:[0,1,0] op_sel_hi:[1,1,0]\n\t"
#define SDW " dst_sel:DWORD dst_unused:UNUSED_PAD src0_sel:DWORD src1_sel:WORD_1\n\t"
#define FOLD "v_bitop3_b32 %6, %6, v[%15-9], v[%15-8] bitop3:0xfe\n\tv_or_b32 %6, v[%15-7], %6\n\t"
#define SCAT(PRE, POST, M) { uint32_t c0_, c1_, c2_, a0_, a1_, a2_; asm volatile( \
        FOLD "v_and_b32 %0, 0x3ff, %7\n\tv_lshrrev_b32 %2, 22, %7\n\t" PRE \
        "v_bitop3_b32 %0, %11, %8, %0 bitop3:0xea\n\tv_bitop3_b32 %2, %11, %10, %2 bitop3:0xea\n\tv_bfe_u32 %1, %7, 11, 10\n\t" \
        "v_fma_mix_f32 %3, %0, v[%15], %12" MIXL "v_bitop3_b32 %1, %11, %9, %1 bitop3:0xea\n\tv_fma_mix_f32 %0, %0, v[%15], %13" MIXH \
        "v_fma_mix_f32 %4, %1, v[%15+1], %12" MIXL "v_lshl_add_u32 %0, %0, 16, %3\n\tv_lshlrev_b32_sdwa %3, 2, %8" SDW \
        "v_fma_mix_f32 %1, %1, v[%15+1], %13" MIXH "ds_add_rtn_u32 v[%15-9], %3, %0 offset:%14\n\t" \
        "v_fma_mix_f32 %5, %2, v[%15+2], %12" MIXL "v_lshl_add_u32 %1, %1, 16, %4\n\tv_lshlrev_b32_sdwa %4, 2, %9" SDW \
        "v_fma_mix_f32 %2, %2, v[%15+2], %13" MIXH "ds_add_rtn_u32 v[%15-8], %4, %1 offset:%14\n\t" \
        "v_lshl_add_u32 %2, %2, 16, %5\n\tv_lshlrev_b32_sdwa %5, 2, %10" SDW \
        "ds_add_rtn_u32 v[%15-7], %5, %2 offset:%14" POST \
        : "=&v"(c0_), "=&v"(c1_), "=&v"(c2_), "=&v"(a0_), "=&v"(a1_), "=&v"(a2_), "+v"(guard) \
        : "v"(xw), "v"(w0), "v"(w1), "v"(w2), "v"(sgn), "s"(mg0), "s"(mg1), "n"(A_ACC), "n"(M), "s"(tm) : "memory"); }
    asm volatile("v_mov_b32 v110, 0\n\tv_mov_b32 v111, 0\n\tv_mov_b32 v112, 0\n\tv_mov_b32 v113, 0\n\tv_mov_b32 v114, 0\n\tv_mov_b32 v115, 0\n\t"
                 "v_mov_b32 v116, 0\n\tv_mov_b32 v117, 0\n\tv_mov_b32 v118, 0" ::: "v106", "v127");
    auto unmix = [&](int i) __attribute__((always_inline)) {
        const auto s0 = __builtin_amdgcn_permlane16_swap(ring[i][0], ring[i][2], false, false);
        const auto s1 = __builtin_amdgcn_permlane16_swap(ring[i][1], ring[i][3], false, false);
        ring[i][0] = s0[0]; ring[i][2] = s0[1]; ring[i][1] = s1[0]; ring[i][3] = s1[1];
    };
    auto word = [&](int i, int k) __attribute__((always_inline)) -> uint32_t {
        return i < NFAST && (i >> 1) != HP ? ring[(i & ~1) | (k >> 1)][(i & 1) * 2 + (k & 1)] : ring[i][k];
    };
    auto gather = [&](int R, int i, int p) __attribute__((always_inline)) {
        if ((i >> 1) == HP) unmix(i);
        const uint32_t w0 = word(i, 0 ^ R), w1 = word(i, 1 ^ R), w2 = word(i, 2 ^ R);
        if (p == 0) GAT3(119) else if (p == 1) GAT3(122) else GAT3(125)
    };
    auto scatter = [&](int R, int i, int p, u64 tm) __attribute__((always_inline)) {
        const uint32_t xw = word(i, 3 ^ R), w0 = word(i, 0 ^ R), w1 = word(i, 1 ^ R), w2 = word(i, 2 ^ R);
        if (i < NFAST) { if (p == 0) SCAT("", "", 119) else if (p == 1) SCAT("", "", 122) else SCAT("", "", 125) }
        else if (p == 0) SCAT("s_mov_b64 exec, %16\n\t", "\n\ts_mov_b64 exec, -1", 119)
        else SCAT("s_mov_b64 exec, %16\n\t", "\n\ts_mov_b64 exec, -1", 125)
    };
#define WAIT_L(N) asm volatile("s_waitcnt lgkmcnt(" #N ")" ::: "memory")
    int ntn;
    u64 tm0, tm1;
    uint32_t ta[2], sp = 0u;
    auto list1 = [&](int ff) __attribute__((always_inline)) {
        int dst;
        uint32_t en;
        ntn = tail_rank(tc.x, sp, ff, 0u, dst, en);
        asm volatile("ds_permute_b32 v106, %0, %1" :: "v"(dst), "v"(en) : "memory");
        if (__builtin_expect(__builtin_amdgcn_ballot_w64((tc.x & 2u) != 0u) != 0ull, 0)) {
            const int n2 = tail_rank(tc.x, sp, ff, 4u, dst, en);
            if (ntn + n2 <= 32) {
                const int r = ((dst >> 4) | ((dst & 12) << 2)) + ntn;
                const int x = __builtin_amdgcn_ds_permute((en & 7u) ? ((r & 15) << 4) | ((r >> 4) << 2) : 252, (en & 7u) ? (int)en : 0);
                asm volatile("v_cmpx_ne_u32 vcc, 0, %0\n\tv_mov_b32 v106, %0\n\ts_mov_b64 exec, -1\n\ts_nop 4" :: "v"(x) : "vcc");
                ntn += n2;
            } else ntn = 64;
        }
    };
    auto step = [&](auto RR, auto KK, int ff) __attribute__((always_inline)) {
        constexpr int R = decltype(RR)::value;
        constexpr bool first = decltype(KK)::value == 1, last = decltype(KK)::value == 2;
        constexpr int mdp = first ? MDF : MDP;
        gather(R, 0, 0);
        const int ntl = ntn;
        asm volatile("v_mov_b32_dpp %0, v106 quad_perm:[0,0,0,0] row_mask:0xf bank_mask:0xf\n\tv_mov_b32_dpp %1, v106 quad_perm:[1,1,1,1] row_mask:0xf bank_mask:0xf" : "=v"(ta[0]), "=v"(ta[1]));
        auto side = [&](int pl) __attribute__((always_inline)) {
            if (first && pl == ((mdp >> 20) & 15)) { ring[4] = load_main(4, ff, !(NEARM & 1u)); ring[5] = load_main(5, ff, !(NEARM & 1u)); }
            if (first && pl == ((mdp >> 24) & 15)) { ring[6] = load_main(6, ff); ring[7] = load_main(7, ff); }
            if (pl == ((mdp >> 12) & 15)) ring[NFAST] = load_tail(ta[0], tm0);
            if (pl == ((mdp >> 16) & 15)) ring[NFAST + 1] = load_tail(ta[1], tm1);
            __builtin_amdgcn_sched_barrier(0);
            if (!last) load_md(ff + 1, pl, mdp);
            if (!first && !last && pl == 5 && ff == 6) {
                load_d(me);
                load_wl();
                if (tid == NT - 128) (void)__hip_atomic_fetch_add((GAS unsigned*)((GAS char*)dw + vconst<0>()), 0x1000000u << me, __ATOMIC_RELAXED, __HIP_MEMORY_SCOPE_AGENT);
            }
            if (last && pl == 3) load_m(me);
            if (last && pl == 4 && wv == 15) { GAS unsigned* d_ = dw; asm volatile("s_nop 4\n\tglobal_load_dword v108, %0, %1 sc1" :: "v"(vconst<0>()), "s"(d_) : "memory"); }
            if (last && pl < 7 && fin) factor(pl, pl == 0);
            __builtin_amdgcn_sched_barrier(0);
        };
        side(8);
        gather(R, 1, 1);
        side(9);
        const uint32_t tcur = tc.x, spc = sp;
#pragma unroll
        for (int i = 0; i < NFAST - 1; i++) {
            gather(R, i + 2, (i + 2) % 3);
            if (i == 0) side(10);
            if (i == 0) WAIT_L(6); else if (i == 1) WAIT_L(9); else WAIT_L(12);
            scatter(R, i, i % 3, 0ull);
            if (!last && (i & 1)) { ring[i - 1] = load_main(i - 1, ff + 1, first && !(NEARM & 4u)); ring[i] = load_main(i, ff + 1, first && !(NEARM & 4u)); }
            side(i);
            if (i == 3) SETP(2);
            if (i == 4) SETP(1);
            if (i == 5) SETP(0);
            if (i == 0 && !last) {
                tc.x = __builtin_amdgcn_alignbit(tc.y, tc.x, 8u); tc.y = __builtin_amdgcn_alignbit(tc.z, tc.y, 8u);
                tc.z = __builtin_amdgcn_alignbit(tc.w, tc.z, 8u); tc.w >>= 8;
                sp = ff == 7 ? 0u : sp + ((tcur & 0x3cu) << 2);
                list1(ff + 1);
            }
            __builtin_amdgcn_sched_barrier(0);
        }
        if (last) asm volatile("" :: "v"(ring[NFAST + 1]));
        if (__builtin_expect(ntl > 16, 0)) {
            gather(R, NFAST + 1, 0);
            WAIT_L(0);
            scatter(R, NFAST + 1, 0, tm1);
        }
        if (__builtin_expect(ntl > 32, 0)) {
#pragma unroll 1
            for (int e = 2; e < 6; e++) {
                int dst;
                uint32_t en;
                if (tail_rank(tcur, spc, ff, (uint32_t)e & 4u, dst, en) <= (e & 3) * 16) continue;
                const int cmp = __builtin_amdgcn_ds_permute(dst, (int)en);
                ring[NFAST + 1] = load_tail((uint32_t)__builtin_amdgcn_ds_bpermute(((lane & ~3) | (e & 3)) << 2, cmp), tm1);
                gather(R, NFAST + 1, 0);
                WAIT_L(0);
                scatter(R, NFAST + 1, 0, tm1);
            }
        }
        SETP(3);
        WAIT_L(3);
        if (!last) {
            __builtin_amdgcn_s_barrier();
            store_md();
            SETP(0);
        }
        scatter(R, NFAST - 1, 1, 0ull);
        if (!last) { ring[6] = load_main(6, ff + 1); ring[7] = load_main(7, ff + 1); }
        scatter(R, NFAST, 2, tm0);
        if (!last) {
            WAIT_L(6);
            __builtin_amdgcn_s_barrier();
            SETP(3);
        }
    };
    auto steps = [&](auto RR) __attribute__((always_inline)) {
        tc = tcnt[(wv * 16 + (lane >> 2)) * 128 + j * 4 + h];
        u32x4 sv[2];
        float4 su[3];
        uint32_t e0;
        if (wv < 4) {
            sv[0] = (u32x4)(0u); sv[1] = sv[0];
            su[0] = make_float4(0.f, 0.f, 0.f, 0.f);
            if (lane < SLOTI / 256) { sv[0] = stat[tid * 2]; sv[1] = stat[tid * 2 + 1]; su[0] = ((const float4*)(ws + W_SUM))[tid]; }
            su[1] = ((const float4*)(ws + W_SUM))[256 + lane];
            su[2] = ((const float4*)(ws + W_SUM))[320 + lane];
            e0 = meta[M_E0 + wv];
        }
        const u32x2 extra = *(const u32x2*)(meta + M_LOG);
        load_md(0, -1, 0);
        {
            const u32x4 bi = {BIAS, BIAS, BIAS, BIAS};
#pragma unroll
            for (int k = 0; k < 6; k++) *(LDS u32x4*)(uintptr_t)(A_ACC + (k * NT + threadIdx.x) * 16) = bi;
            if (threadIdx.x < 128) *(LDS u32x4*)(uintptr_t)(A_ACC + (6 * NT + threadIdx.x) * 16) = bi;
            __builtin_amdgcn_sched_barrier(0);
        }
#pragma unroll
        for (int i = 0; i < 2; i++) ring[i] = load_main(i, 0);
        __builtin_amdgcn_sched_barrier(0);
        list1(0);
        if (wv < 4) {
            const uint32_t mxb = wave_max(max(max(sv[0].x & 0xffffu, sv[0].z & 0xffffu), max(sv[1].x & 0xffffu, sv[1].z & 0xffffu)));
            const uint32_t mnb = 0xffffu - (wave_max(max(max(sv[0].x, sv[0].z), max(sv[1].x, sv[1].z))) >> 16);
            const float s2 = wave_sum((__uint_as_float(sv[0].y) + __uint_as_float(sv[0].w)) + (__uint_as_float(sv[1].y) + __uint_as_float(sv[1].w)));
            const float s1 = wave_sum((su[0].x + su[0].y) + (su[0].z + su[0].w));
            const float c1 = wave_sum((su[1].x + su[1].y) + (su[1].z + su[1].w)) * (1.0f / (NWG * 512)), c2 = wave_sum((su[2].x + su[2].y) + (su[2].z + su[2].w)) * (1.0f / (NWG * 512));
            const float rng = fmaxf(fabsf(zf * s1 * c1) + ZSIG * sqrtf(zf * s2 * c2), 2.0f * bf2f(mxb)) * 6.103515625e-05f;
            const uint32_t eb = mxb ? (__float_as_uint(rng) + 0x7fffffu) >> 23 : 127u;
            const bool ok = !mxb || (mxb < 0x4700u && eb >= 107u && eb <= 140u && mnb >= ((eb + 8u) << 7) && c2 >= 0.015625f);
            if (lane == 0) {
                s_flag[8 + wv] = (eb & 0xffu) | (ok ? 0x100u : 0u) | (mxb ? 0x200u : 0u);
                s_flag[12 + wv] = __float_as_uint(__uint_as_float(((eb + e0) & 0xffu) << 23) * (1024.0f / 1023.0f));
                s_flag[wv] = 0u;
            }
        }
        store_md();
#pragma unroll
        for (int i = 2; i < 4; i++) ring[i] = load_main(i, 0);
        WAIT_L(0);
        __syncthreads();
        const uint32_t q0 = __builtin_amdgcn_readfirstlane(s_flag[8 + 2 * cp]), q1 = __builtin_amdgcn_readfirstlane(s_flag[9 + 2 * cp]);
        fast = (q0 & q1 & 0x100u) != 0u;
        need = (q0 >> 9) | ((q1 >> 9) << 1);
        more = (__builtin_amdgcn_readfirstlane(extra.x | extra.y) | (uint32_t)(T & 3)) != 0u;
        mg0 = (((q0 & 0xffu) + 9u) << 23) | 0x400000u;
        mg1 = ((((q1 & 0xffu) + 9u) << 23) | 0x400000u) + ((0u - (mg0 >> 16)) & 0xffffu);
        if (!fast) { __builtin_amdgcn_s_waitcnt(0x0f70); return; }
        step(RR, IC<1>(), 0);
#pragma unroll 1
        for (int ff = 1; ff < 15; ff++) step(RR, IC<0>(), ff);
        step(RR, IC<2>(), 15);
        if (fin) factor(7, false);
    };
    auto irregular = [&](auto f) __attribute__((always_inline)) {
        const float fr = bf2f(freq[0]);
        const char* lg = recs + (size_t)LBASE * 128u;
        const uint16_t* tags = (const uint16_t*)(lg + (size_t)lcap * 128u);
        const uint32_t nl = meta[M_LOG], ns = meta[M_LOG + 1];
#pragma unroll 1
        for (uint32_t q = tid; q < nl * 8u; q += NT) {
            const uint32_t b = tags[q >> 3];
            if ((b & 127u) == (uint32_t)(h * 32 + j)) {
                const uint32_t* un = (const uint32_t*)(lg + (size_t)q * 16u);
                const uint32_t xw = un[3 ^ h];
#pragma unroll 1
                for (int k = 0; k < 3; k++) {
                    const uint32_t ww = un[k ^ h];
                    const float c = (float)((xw >> (k * 11)) & 0x3ffu) * (1.0f / 1023.0f);
                    if (!(ww & WNULL)) f(ww >> 16, ((ww & 0x3fffu) << 6) | (b >> 5), (ww & 0x8000u) ? -c : c);
                }
            }
        }
#pragma unroll 1
        for (uint32_t q = tid; q < ns + (uint32_t)(T & 3); q += NT) {
            const uint32_t t = q < ns ? ((const uint32_t*)tags)[-1 - (int)q] : (uint32_t)(T & ~3) + (q - ns);
            const uint32_t i = (uint32_t)iin[t], o = (uint32_t)iout[t];
            if (((o >> 6) & 31u) == (uint32_t)j && (i & 3u) == (uint32_t)h) f(((o >> 11) << 6) | (o & 63u), i, cosf(fr * bf2f(ang[t])));
        }
        __builtin_amdgcn_s_waitcnt(0x0f70);
    };

    if (h == 0) steps(IC<0>());
    else if (h == 1) steps(IC<1>());
    else if (h == 2) steps(IC<2>());
    else steps(IC<3>());
    if (fast) {
        if (more) irregular([&](uint32_t ol, uint32_t i, float cf) __attribute__((always_inline)) {
            const uint32_t md = mdc[(cp * 64u + (i & 63u)) * SLOTI + ((i >> 14) << 8) + ((i >> 4) & 0xfcu) + ((i >> 12) & 3u)];
            const float c = cf * (1023.0f / 16777216.0f);
            const uint32_t lo = __float_as_uint(__builtin_fmaf(c, h2f(md & 0xffffu), __uint_as_float(mg0)));
            const uint32_t hi = __float_as_uint(__builtin_fmaf(c, h2f(md >> 16), __uint_as_float(mg1)));
            if (cf == cf) guard |= atomicAdd(&acc[ol], (hi << 16) + lo);
            else guard |= HSLOW;
        });
    }
    volatile LDS unsigned* seen = (volatile LDS unsigned*)(uintptr_t)(A_FLAG + 4);
    if (fast && wv == 15) asm volatile("s_waitcnt vmcnt(0)\n\tds_write_b32 %0, v108 offset:16\n\tv_and_b32 v109, 0xff00ff, v108\n\tds_write_b32 %0, v109" :: "v"(vconst<A_FLAG + 4>()) : "memory");
    WAIT_L(0);
    asm volatile("v_or3_b32 %0, %0, v110, v111\n\tv_or3_b32 %0, %0, v112, v113\n\tv_or3_b32 %0, %0, v114, v115\n\tv_or3_b32 %0, %0, v116, v117\n\tv_or_b32 %0, v118, %0" : "+v"(guard));
    __syncthreads();
    const __amdgpu_buffer_rsrc_t rsP = __builtin_amdgcn_make_buffer_rsrc((void*)(partial + (size_t)j * 16 * SLOTO), 0, 16 * SLOTO * 4, 0x00020000);
    const char* pk = recs + (uint32_t)j * (SC * 128u);
    const __amdgpu_buffer_rsrc_t rsK = __builtin_amdgcn_make_buffer_rsrc((void*)pk, 0, (int)(MAINL * 128u), 0x00020000);
#define PKA (SSM * 128u)
#define PKB (32u * SC * 128u)
    auto pk_at = [&](uint32_t i) __attribute__((always_inline)) -> uint32_t {
        const uint32_t c = i >> 9;
        return (c < 12u ? (c >> 2) * SQ + (c & 3u) * SKM : 2u * SSM) * 128u + (i & 511u) * 16u;
    };
    u32x4 p[8];
    unsigned have = 0u;
    auto fetch = [&](unsigned v, int sh) __attribute__((always_inline)) {
        const uint32_t po = tid < SHARE / 4 ? pk_at((uint32_t)(sh * (SHARE / 4) + tid)) : 0x7fffff00u;
        const unsigned nw = v & ~have;
#pragma unroll
        for (int r = 0; r < 8; r++) {
            const uint32_t k = (uint32_t)(me ^ r);
            if ((nw >> k) & 1u) p[r] = __builtin_amdgcn_raw_buffer_load_b128(rsK, po, ((v >> (16u + k)) & 1u) ? MAINL * 128u : (k & 1u) * PKA + (k >> 1) * PKB, AUX_SC1);
        }
        have |= v;
    };
    bool room = false;
    if (fast) {
        room = (__builtin_amdgcn_readfirstlane(s_flag[5]) >> (24 + (me ^ 1))) & 1u;
        const __amdgpu_buffer_rsrc_t rsW = __builtin_amdgcn_make_buffer_rsrc((void*)(pk + (uint32_t)cp * PKA + (uint32_t)h * PKB), 0, room ? (int)(MAINL * 128u) : 0, 0x00020000);
        uint32_t big = 0u;
        u32x4 v[7];
#pragma unroll
        for (int k = 0; k < 6; k++) v[k] = acc_vec(k * NT + tid);
        v[6] = (u32x4)(BIAS);
        if (wv < 2) v[6] = acc_vec(6 * NT + tid);
#pragma unroll
        for (int k = 0; k < 6; k++) __builtin_amdgcn_raw_buffer_store_b128(v[k], rsW, pk_at((uint32_t)tid) + ((k >> 1) * SQ + (k & 1) * 2u * SKM) * 128u, 0, AUX_SC1);
        if (wv < 2) __builtin_amdgcn_raw_buffer_store_b128(v[6], rsW, 2u * SSM * 128u + (uint32_t)tid * 16u, 0, AUX_SC1);
        __builtin_amdgcn_sched_barrier(0);
#pragma unroll
        for (int k = 0; k < 28; k++) { guard |= v[k >> 2][k & 3]; big |= v[k >> 2][k & 3] + 0x42004200u; }
        if (guard & HSLOW) s_flag[0] = 1u;
        if (big & 0x7c00u) s_flag[2] = 1u;
        if (big & 0x7c000000u) s_flag[3] = 2u;
        __builtin_amdgcn_sched_barrier(0);
        p[0] = acc_vec(me * (SHARE / 4) + min(tid, SHARE / 4 - 1));
        have = 1u << me;
        if (fin) fetch(__builtin_amdgcn_readfirstlane(*seen), me);
    }
    asm volatile("s_waitcnt vmcnt(0)" ::: "memory");
    __syncthreads();
    const bool redo = !fast || __builtin_amdgcn_readfirstlane(s_flag[0]) != 0u || (need & ~__builtin_amdgcn_readfirstlane(s_flag[2] | s_flag[3])) != 0u;
    const bool slow = redo || !room;
    if (slow) {
        float* fs = (float*)acc;
        float w[16];
        load_w(wdown, w);
        const float fr = bf2f(freq[0]);
#pragma unroll 1
        for (int e = 0; e < 2; e++) {
            if (redo) {
                __syncthreads();
                for (int i = tid; i < SLOTO; i += NT) fs[i] = 0.f;
                __syncthreads();
                const __amdgpu_buffer_rsrc_t rsO = __builtin_amdgcn_make_buffer_rsrc((void*)iout, 0, T * 4, 0x00020000);
                const __amdgpu_buffer_rsrc_t rsI = __builtin_amdgcn_make_buffer_rsrc((void*)iin, 0, T * 4, 0x00020000);
#pragma unroll 1
                for (int t0 = tid * 4; t0 < T; t0 += NT * 16) {
                    u32x4 o4[4], i4[4];
#pragma unroll
                    for (int g = 0; g < 4; g++) {
                        o4[g] = __builtin_amdgcn_raw_buffer_load_b128(rsO, (uint32_t)(t0 + g * NT * 4) * 4u, 0, 0);
                        i4[g] = __builtin_amdgcn_raw_buffer_load_b128(rsI, (uint32_t)(t0 + g * NT * 4) * 4u, 0, 0);
                    }
                    uint32_t hit = 0u;
#pragma unroll
                    for (int k = 0; k < 16; k++) hit |= (bfi(3u, i4[k >> 2][k & 3], o4[k >> 2][k & 3]) & 0x7c3u) == (uint32_t)(j * 64 + h) ? 1u << k : 0u;
#pragma unroll 1
                    for (; hit; hit &= hit - 1u) {
                        const int k = __builtin_ctz(hit), t = t0 + (k >> 2) * NT * 4 + (k & 3);
                        if (t >= T) break;
                        const uint32_t o = (uint32_t)iout[t];
                        uint32_t rr[4];
                        md_of(m[iin[t]], w, rr);
                        atomicAdd(&fs[((o >> 11) << 6) | (o & 63u)], cosf(fr * bf2f(ang[t])) * bf2f(cp ? (e ? rr[3] : rr[2]) : (e ? rr[1] : rr[0])));
                    }
                }
                __syncthreads();
            }
            const float un = __uint_as_float(s_flag[12 + 2 * cp + e]);
            for (int i = tid; i < SLOTO / 4; i += NT) {
                u32x4 v = acc_vec(i);
                if (!redo) {
#pragma unroll
                    for (int k = 0; k < 4; k++) v[k] = __float_as_uint((float)((int)((v[k] >> (16 * e)) & 0xffffu) - 0x4000) * un);
                }
                __builtin_amdgcn_raw_buffer_store_b128(v, rsP, (uint32_t)((e * 8 + me) * SLOTO * 4 + i * 16), 0, AUX_SC1);
            }
        }
        load_wl();
        edges(me);
#pragma unroll
        for (int k = 0; k < 8; k++) p[k] = (u32x4)(0u);
        have = 0u;
        asm volatile("s_waitcnt vmcnt(0)" ::: "memory");
        __syncthreads();
    }
    if (tid == NT - 64) {
        const unsigned inc = (slow ? 0x10001u : 1u) << me;
        unsigned v = __hip_atomic_fetch_add(dw, inc, __ATOMIC_RELAXED, __HIP_MEMORY_SCOPE_AGENT) + inc;
        unsigned quit = 0u;
        if ((v & 0xffu) != 0xffu) {
            unsigned spins = 0;
            do {
                *seen = v & 0xff00ffu;
                __builtin_amdgcn_s_sleep(1);
                v = __hip_atomic_load(dw, __ATOMIC_RELAXED, __HIP_MEMORY_SCOPE_AGENT);
                if (++spins > SPINCAP && (v & 0xffu) != 0xffu) {
                    v = __hip_atomic_fetch_add(dw, 256u << me, __ATOMIC_RELAXED, __HIP_MEMORY_SCOPE_AGENT);
                    if ((v & 0xffu) != 0xffu) { v = 0xffu; quit = 0x80000000u; }
                }
            } while ((v & 0xffu) != 0xffu);
            v &= 0xff00ffu;
        }
        *seen = (v & 0xffffffu) | quit;
    }
    if (!fin) return;
    have = __builtin_amdgcn_readfirstlane(have);
    uint32_t un[4];
#pragma unroll
    for (int c = 0; c < 4; c++) un[c] = __builtin_amdgcn_readfirstlane(s_flag[12 + c]);
    unsigned sv;
    {
        const u32x4 rsQ = {(uint32_t)(uintptr_t)pk, (uint32_t)((uintptr_t)pk >> 32) & 0xffffu, MAINL * 128u, 0x00020000u};
        const uint32_t po = tid < SHARE / 4 ? pk_at((uint32_t)(me * (SHARE / 4) + tid)) : 0x7fffff00u;
        do {
            sv = __builtin_amdgcn_readfirstlane(*seen);
            uint32_t t0, t1, t2;
#define FET(R) "s_xor_b32 %8, %13, " #R "\n\ts_bitcmp1_b32 %14, %8\n\ts_cbranch_scc0 1f\n\ts_and_b32 %9, %8, 1\n\ts_mul_i32 %9, %9, %16\n\ts_lshr_b32 %10, %8, 1\n\t" \
               "s_mul_i32 %10, %10, %17\n\ts_add_u32 %9, %9, %10\n\ts_bitcmp1_b32 %15, %8\n\ts_cselect_b32 %9, %18, %9\n\tbuffer_load_dwordx4 %" #R ", %11, %12, %9 offen sc1\n1:\n\t"
            asm volatile("s_and_b32 %8, %14, 0xff\n\ts_cbranch_scc0 2f\n\ts_nop 2\n\t" FET(0) FET(1) FET(2) FET(3) FET(4) FET(5) FET(6) FET(7) "\n2:\n\ts_sleep 2\n\ts_waitcnt vmcnt(0)"
                         : "+v"(p[0]), "+v"(p[1]), "+v"(p[2]), "+v"(p[3]), "+v"(p[4]), "+v"(p[5]), "+v"(p[6]), "+v"(p[7]), "=&s"(t0), "=&s"(t1), "=&s"(t2)
                         : "v"(po), "s"(rsQ), "s"(me), "s"((int)sv < 0 ? 0u : sv & ~have), "s"(sv >> 16), "n"(PKA), "n"(PKB), "n"(MAINL * 128u) : "memory", "scc");
            have |= sv;
        } while ((sv & 0xffu) != 0xffu);
    }
    if ((int)sv < 0) return;
    const unsigned flt = (sv >> 16) & 0xffu;
    auto finish = [&](int sh) __attribute__((always_inline)) {
        {
            const int ol = sh * SHARE + tid * 4;
            const uint32_t po = tid < SHARE / 4 ? (uint32_t)ol * 4u : 0x7fffff00u;
            float g[4][4];
            u32x4 a[2] = {p[0] + p[2], p[1] + p[3]}, b[2] = {p[4] + p[6], p[5] + p[7]};
            if (cp) { const u32x4 t = a[0], u = b[0]; a[0] = a[1]; b[0] = b[1]; a[1] = t; b[1] = u; }
#pragma unroll
            for (int c = 0; c < 2; c++) {
                const int nb = 0x10000 - 0x4000 * __builtin_popcount(flt & (0x55u << c));
                const float u0 = __uint_as_float(nb ? un[2 * c] : 0u), u1 = __uint_as_float(nb ? un[2 * c + 1] : 0u);
#pragma unroll
                for (int q = 0; q < 4; q++) {
                    g[q][2 * c] = (float)((int)((a[c][q] & 0xffffu) + (b[c][q] & 0xffffu)) - nb) * u0;
                    g[q][2 * c + 1] = (float)((int)((a[c][q] >> 16) + (b[c][q] >> 16)) - nb) * u1;
                }
            }
            if (__builtin_expect(flt != 0u, 0)) {
#pragma unroll
                for (int c = 0; c < 2; c++) {
#pragma unroll 1
                    for (int k = c; k < 8; k += 2) {
                        if (!((flt >> k) & 1u)) continue;
                        const u32x4 f0 = __builtin_amdgcn_raw_buffer_load_b128(rsP, po, k * SLOTO * 4, AUX_SC1);
                        const u32x4 f1 = __builtin_amdgcn_raw_buffer_load_b128(rsP, po, (8 + k) * SLOTO * 4, AUX_SC1);
#pragma unroll
                        for (int q = 0; q < 4; q++) { g[q][2 * c] += __uint_as_float(f0[q]); g[q][2 * c + 1] += __uint_as_float(f1[q]); }
                    }
                }
            }
            float wu[16];
#pragma unroll
            for (int k = 0; k < 8; k++) {
                const uint32_t x = __builtin_amdgcn_readlane(wl, 40 + k);
                wu[2 * k] = bf2f(x & 0xffffu); wu[2 * k + 1] = bf2f(x >> 16);
            }
            u32x4 ov[2];
#pragma unroll
            for (int q = 0; q < 4; q++) {
#pragma unroll
                for (int k = 0; k < 4; k++) g[q][k] *= fa[q][k >> 1][k & 1];
                const uint32_t m0 = em[q >> 1][(q & 1) * 2], m1 = em[q >> 1][(q & 1) * 2 + 1];
                const float mm[4] = {bf2f(m0 & 0xffff), bf2f(m0 >> 16), bf2f(m1 & 0xffff), bf2f(m1 >> 16)};
                float o[4];
#pragma unroll
                for (int k = 0; k < 4; k++) o[k] = mm[k] + (g[q][0] * wu[k] + g[q][1] * wu[4 + k] + g[q][2] * wu[8 + k] + g[q][3] * wu[12 + k]) * fa[q][2 + (k >> 1)][k & 1];
                ov[q >> 1][(q & 1) * 2] = pk_bf16(o[0], o[1]);
                ov[q >> 1][(q & 1) * 2 + 1] = pk_bf16(o[2], o[3]);
            }
            const __amdgpu_buffer_rsrc_t rsU = __builtin_amdgcn_make_buffer_rsrc((void*)out, 0, E * 8, 0x00020000);
            const uint32_t uo = edge0(sh) * 8u;
            __builtin_amdgcn_raw_buffer_store_b128(ov[0], rsU, uo, 0, AUX_NT);
            __builtin_amdgcn_raw_buffer_store_b128(ov[1], rsU, uo + 256u, 0, AUX_NT);
        }
    };
    finish(me);
#pragma unroll 1
    for (unsigned todo = (sv >> 8) & 0xffu; todo; todo &= todo - 1u) {
        const int sh = __builtin_ctz(todo);
        edges(sh);
        have = 0u;
        fetch(sv, sh);
        finish(sh);
    }
}

extern "C" __global__ __launch_bounds__(256) void gen_scatter_kernel(const int* __restrict__ iin, const int* __restrict__ iout,
                                                          const uint16_t* __restrict__ ang, const uint16_t* __restrict__ freq,
                                                          const u32x2* __restrict__ m, const uint16_t* __restrict__ wdown,
                                                          float* __restrict__ agg, int T) {
    float w[16];
    load_w(wdown, w);
    const float fr = bf2f(freq[0]);
    for (long t = (long)blockIdx.x * 256 + threadIdx.x; t < T; t += (long)gridDim.x * 256) {
        const float c = cosf(fr * bf2f(ang[t]));
        uint32_t r[4];
        md_of(m[iin[t]], w, r);
        float* q = agg + (size_t)iout[t] * 4;
#pragma unroll
        for (int k = 0; k < 4; k++) atomicAdd(q + k, c * bf2f(r[k]));
    }
}
__global__ __launch_bounds__(256) void gen_final_kernel(const u32x2* __restrict__ m, const uint16_t* __restrict__ dist,
                                                        const uint16_t* __restrict__ centers, const uint16_t* __restrict__ wrbf,
                                                        const uint16_t* __restrict__ wsbf, const uint16_t* __restrict__ wup,
                                                        const float* __restrict__ agg, u32x2* __restrict__ out, int E) {
    const int e = blockIdx.x * 256 + threadIdx.x;
    if (e >= E) return;
    const float d = bf2f(dist[e]);
    float rs[4] = {0, 0, 0, 0}, rg[4] = {0, 0, 0, 0};
#pragma unroll 1
    for (int r = 0; r < 8; r++) {
        const float x = d - bf2f(centers[r]);
        const float rb = __expf(-10.0f * x * x);
#pragma unroll
        for (int k = 0; k < 4; k++) { rs[k] += rb * bf2f(wsbf[r * 4 + k]); rg[k] += rb * bf2f(wrbf[r * 4 + k]); }
    }
    float wu[16];
    load_w(wup, wu);
    const u32x2 mv = m[e];
    const float mm[4] = {bf2f(mv.x & 0xffff), bf2f(mv.x >> 16), bf2f(mv.y & 0xffff), bf2f(mv.y >> 16)};
    float g[4], o[4];
#pragma unroll
    for (int k = 0; k < 4; k++) g[k] = agg[(size_t)e * 4 + k] * rs[k];
#pragma unroll
    for (int k = 0; k < 4; k++) o[k] = mm[k] + (g[0] * wu[k] + g[1] * wu[4 + k] + g[2] * wu[8 + k] + g[3] * wu[12 + k]) * rg[k];
    const u32x2 ov = {f2bf(o[0]) | (f2bf(o[1]) << 16), f2bf(o[2]) | (f2bf(o[3]) << 16)};
    out[e] = ov;
}

extern "C" void kernel_launch(void* const* d_in, const int* in_sizes, int n_in,
                              void* d_out, int out_size, void* d_ws, size_t ws_size,
                              hipStream_t stream) {
    const u32x2* m = (const u32x2*)d_in[0];
    const int* iin = (const int*)d_in[1];
    const int* iout = (const int*)d_in[2];
    const uint16_t* dist = (const uint16_t*)d_in[3];
    const uint16_t* ang = (const uint16_t*)d_in[4];
    const uint16_t* centers = (const uint16_t*)d_in[5];
    const uint16_t* freq = (const uint16_t*)d_in[6];
    const uint16_t* wrbf = (const uint16_t*)d_in[7];
    const uint16_t* wsbf = (const uint16_t*)d_in[8];
    const uint16_t* wdown = (const uint16_t*)d_in[9];
    const uint16_t* wup = (const uint16_t*)d_in[10];
    const int E = in_sizes[3];
    const int T = in_sizes[1];
    char* ws = (char*)d_ws;
    const int G = T >> 2;
    const int ntile = (G + GR - 1) / GR;
    const int nstat = ntile / NWG - NDYN > 4 ? ntile / NWG - NDYN : 4;
    const int lcap = G / 6 + 2;
    char* recs = ws + W_RECS;
    size_t off = W_RECS;
    const size_t rbytes = (size_t)LBASE * 128 + (size_t)lcap * 130;
    off += rbytes + 256 + (size_t)32 * 16 * SLOTO * 4;
    if (E > EMAX || (E & 3) || E < 1 || rbytes > 0x70000000u || off > ws_size) {
        (void)hipMemsetAsync(ws, 0, (size_t)E * 16, stream);
        gen_scatter_kernel<<<4096, 256, 0, stream>>>(iin, iout, ang, freq, m, wdown, (float*)ws, T);
        gen_final_kernel<<<(E + 255) / 256, 256, 0, stream>>>(m, dist, centers, wrbf, wsbf, wup, (const float*)ws, (u32x2*)d_out, E);
        return;
    }
    int ks = T / (NWG * NB * 24);
    if (ks > KMAX) ks = KMAX;
    const float zf = (float)T / (4.0f * (float)E * (float)E);
    (void)hipFuncSetAttribute((const void*)part_kernel, hipFuncAttributeMaxDynamicSharedMemorySize, P_LDS);
    (void)hipFuncSetAttribute((const void*)acc_kernel, hipFuncAttributeMaxDynamicSharedMemorySize, A_LDS);
    part_kernel<<<NWG, NT, P_LDS, stream>>>(iin, iout, ang, freq, m, wdown, recs, T, E, ntile, nstat, ks, lcap);
    acc_kernel<<<NWG, NT, A_LDS, stream>>>(ws, T, E, ks, lcap, zf, 0, centers, wrbf, wsbf, wup, m, dist, (u32x2*)d_out, iin, iout, ang, wdown, freq);
}
```

```cpp
#include <hip/hip_runtime.h>
#include <hip/hip_bf16.h>
#include <stdint.h>
#pragma clang diagnostic ignored "-Winline-asm"

typedef unsigned int u32x4 __attribute__((ext_vector_type(4)));
typedef unsigned int u32x2 __attribute__((ext_vector_type(2)));
typedef unsigned short u16x2 __attribute__((ext_vector_type(2)));
typedef __bf16 bf16x2 __attribute__((ext_vector_type(2)));
typedef float f32x2 __attribute__((ext_vector_type(2)));
typedef unsigned long long u64;

static __device__ __forceinline__ float bf2f(uint32_t bits16) { return __uint_as_float(bits16 << 16); }
static __device__ __forceinline__ uint32_t f2bf(float f) {
    uint32_t u = __float_as_uint(f);
    if ((u & 0x7fffffffu) > 0x7f800000u) return (u >> 16) | 0x40;
    return (u + 0x7fffu + ((u >> 16) & 1u)) >> 16;
}
static __device__ __forceinline__ float h2f(uint32_t bits16) { return (float)__builtin_bit_cast(_Float16, (unsigned short)bits16); }

#define NB 2048
#define WNULL 0x80000000u
#define CSTEP 0x2aabu
#define CINIT 205u
#define CHV (12u * CSTEP)
#define SLOTO 25088
#define SLOTI 12544
#define EMAX (SLOTO * 32)
#define NT 1024
#define NWG 256
#define KMAX 4
#define NTAIL 4
#define SC 64u
#define SKM (128u * SC + 2u)
#define SSM (4u * SKM + 1u)
#define NNEAR 2u
#define NEARM (0x55555555u << (2u * NNEAR))
#define SSH (128u * SC + 1u)
#define SKH ((16u + NNEAR) * SSH + 2u - (NNEAR & 1u))
#define SQ (16u * SSM)
#define MAINL (3u * SQ)
#define SBASE MAINL
#define SPL (128u * 2u * NWG + 1u)
#define HBASE (SBASE + 8u * SPL)
#define QAT(q) ((q) ? ((q) - 1u) * SQ : HBASE)
#define BPL(b) (QAT(((b) >> 3) & 3u) + ((b) & 7u) + (((b) >> 5) << 3))
#define TST 257u
#define TBASE (HBASE + 3u * SKH + 16u * SSH)
#define TPL (NB * TST)
#define LBASE (TBASE + 2u * TPL)
#define NDYN 3
#define GR 2048
#define M_DONE 0
#define M_TILE (32 * 64)
#define M_LOG (M_TILE + 64)
#define M_MD (M_LOG + 64)
#define M_BEAT (M_MD + 64)
#define M_E0 (M_BEAT + 64)
#define M_WORDS (M_E0 + 64)
#define W_TCNT (M_WORDS * 4)
#define W_STAT (W_TCNT + NWG * NB)
#define W_SUM (W_STAT + 4 * NWG * 8)
#define W_MDC (W_SUM + 6 * NWG * 4)
#define W_RECS (W_MDC + 128 * SLOTI * 4)
#define AUX_NT 2
#define AUX_SC1 16

static __device__ __forceinline__ uint32_t row_max(uint32_t x) {
    x = max(x, (uint32_t)__builtin_amdgcn_update_dpp(0, x, 0x111, 0xf, 0xf, false));
    x = max(x, (uint32_t)__builtin_amdgcn_update_dpp(0, x, 0x112, 0xf, 0xf, false));
    x = max(x, (uint32_t)__builtin_amdgcn_update_dpp(0, x, 0x114, 0xf, 0xf, false));
    return max(x, (uint32_t)__builtin_amdgcn_update_dpp(0, x, 0x118, 0xf, 0xf, false));
}
static __device__ __forceinline__ uint32_t wave_max(uint32_t x) {
    x = row_max(x);
    x = max(x, (uint32_t)__builtin_amdgcn_update_dpp(0, x, 0x142, 0xa, 0xf, false));
    x = max(x, (uint32_t)__builtin_amdgcn_update_dpp(0, x, 0x143, 0xc, 0xf, false));
    return __builtin_amdgcn_readlane(x, 63);
}
static __device__ __forceinline__ float row_sum(float x) {
    x += __int_as_float(__builtin_amdgcn_update_dpp(0, __float_as_int(x), 0x111, 0xf, 0xf, false));
    x += __int_as_float(__builtin_amdgcn_update_dpp(0, __float_as_int(x), 0x112, 0xf, 0xf, false));
    x += __int_as_float(__builtin_amdgcn_update_dpp(0, __float_as_int(x), 0x114, 0xf, 0xf, false));
    return x + __int_as_float(__builtin_amdgcn_update_dpp(0, __float_as_int(x), 0x118, 0xf, 0xf, false));
}
static __device__ __forceinline__ float wave_sum(float x) {
    x = row_sum(x);
    x += __int_as_float(__builtin_amdgcn_update_dpp(0, __float_as_int(x), 0x142, 0xa, 0xf, false));
    x += __int_as_float(__builtin_amdgcn_update_dpp(0, __float_as_int(x), 0x143, 0xc, 0xf, false));
    return __int_as_float(__builtin_amdgcn_readlane(__float_as_int(x), 63));
}
static __device__ __forceinline__ uint32_t bfi(uint32_t mask, uint32_t a, uint32_t b) {
    return __builtin_amdgcn_bitop3_b32(mask, a, b, 0xca);
}
template <uint32_t C> static __device__ __forceinline__ uint32_t vconst() {
    uint32_t r;
    asm volatile("v_mov_b32 %0, %1" : "=v"(r) : "n"(C));
    return r;
}
static __device__ __forceinline__ uint32_t pk_bf16(float a, float b) {
    const f32x2 v = {a, b};
    return __builtin_bit_cast(uint32_t, __builtin_convertvector(v, bf16x2));
}
static __device__ __forceinline__ void pin(uint32_t& x) { asm volatile("" : "+v"(x)); }

#define RTS(k)

static __device__ __forceinline__ void md_of(u32x2 v, const float* w, uint32_t* r) {
    float a0 = bf2f(v.x & 0xffff), a1 = bf2f(v.x >> 16), a2 = bf2f(v.y & 0xffff), a3 = bf2f(v.y >> 16);
#pragma unroll
    for (int j = 0; j < 4; j++) r[j] = f2bf(a0 * w[j] + a1 * w[4 + j] + a2 * w[8 + j] + a3 * w[12 + j]);
}
static __device__ __forceinline__ void load_w(const uint16_t* __restrict__ wdown, float* w) {
    u32x4 wv0 = *(const u32x4*)wdown, wv1 = *(const u32x4*)(wdown + 8);
#pragma unroll
    for (int i = 0; i < 4; i++) {
        w[2 * i] = bf2f(wv0[i] & 0xffffu); w[2 * i + 1] = bf2f(wv0[i] >> 16);
        w[8 + 2 * i] = bf2f(wv1[i] & 0xffffu); w[8 + 2 * i + 1] = bf2f(wv1[i] >> 16);
    }
}
#define LDS __attribute__((address_space(3)))
#define P_CNT 0
#define P_FLAG ((NB + 64) * 4)
#define P_LST (P_FLAG + 256)
#define LCAP 32
#define P_TAB (P_LST + 4 * LCAP * 16)
#define P_BUF (P_TAB + NB * 4)
#define P_LDS (P_BUF + NB * 64)
#define PD 2
#define LPOS 0x448763
#define SETP(n) __builtin_amdgcn_s_setprio(n)
#define MDL 1
#define MDM 3
#define RMAXREV 40.0f
#define HB 88
#define HREG(S, D) (HB + ((S) & 3) * 10 + (((S) >> 2) + (D)) * 2)

template <int N> struct IC { static constexpr int value = N; };
struct Raw { u32x4 o[2], i[2]; u32x2 a[2]; };
static __device__ __forceinline__ uint32_t lds_ld(uint32_t a) { return *(const LDS uint32_t*)(uintptr_t)a; }
static __device__ __forceinline__ void lds_st(uint32_t a, uint32_t v) { *(LDS uint32_t*)(uintptr_t)a = v; }
static __device__ __forceinline__ void lds_or(uint32_t a, uint32_t v) {
    __hip_atomic_fetch_or((LDS uint32_t*)(uintptr_t)a, v, __ATOMIC_RELAXED, __HIP_MEMORY_SCOPE_WORKGROUP);
}
static __device__ __forceinline__ uint32_t lds_add(uint32_t a, uint32_t v) {
    return __hip_atomic_fetch_add((LDS uint32_t*)(uintptr_t)a, v, __ATOMIC_RELAXED, __HIP_MEMORY_SCOPE_WORKGROUP);
}
static __device__ __forceinline__ uint32_t slot_w(uint32_t ca, uint32_t cv) {
    return (ca << 4) | __builtin_amdgcn_bitop3_b32(cv >> 11, ca, 0x3cu, 0x28);
}
static __device__ __forceinline__ uint32_t slot_x2(uint32_t aw, uint32_t ca) {
    return __builtin_amdgcn_bitop3_b32(aw, ca, 0xcu, 0x72);
}
static __device__ __forceinline__ uint32_t slot_x(uint32_t aw) { return slot_x2(aw, aw >> 4); }
template <int R> static __device__ __forceinline__ u32x2 hget() {
    u32x2 d;
    asm volatile("v_mov_b64 %0, v[%1:%2]" : "=v"(d) : "n"(R), "n"(R + 1));
    return d;
}
static __device__ __forceinline__ u32x2 hget_at(int r) {
    u32x2 d;
    asm volatile("s_set_gpr_idx_on %2, gpr_idx(SRC0)\n\ts_nop 1\n\tv_mov_b32 %0, v88\n\tv_mov_b32 %1, v89\n\ts_set_gpr_idx_off"
                 : "=&v"(d.x), "=&v"(d.y) : "s"(r));
    return d;
}
static __device__ __forceinline__ void hset_at(int r, u32x2 d) {
    asm volatile("s_set_gpr_idx_on %2, gpr_idx(DST)\n\ts_nop 1\n\tv_mov_b32 v88, %0\n\tv_mov_b32 v89, %1\n\ts_set_gpr_idx_off"
                 :: "v"(d.x), "v"(d.y), "s"(r));
}

template <int S, int D> static __device__ __forceinline__ uint32_t take(uint32_t V, uint32_t u16, uint32_t la, u32x2 nul) {
    const uint32_t a = __builtin_amdgcn_bitop3_b32(V, u16, 15u, 0x54);
    asm volatile("v_mov_b64 v[%4:%4+1], v[%3:%3+1]\n\tv_cmpx_ne_u32 vcc, -16, %0\n\tds_wrxchg_rtn_b64 v[%4:%4+1], %1, %2 offset:%5\n\ts_mov_b64 exec, -1"
                 :: "v"(a), "v"(la), "v"(nul), "n"(HREG(S, D)), "n"(HREG(S, 1 - D)), "n"(S * 256) : "memory", "vcc");
    return a;
}
#define PUT(P) asm volatile("s_bitcmp1_b32 %3, %4\n\ts_cbranch_scc0 1f\n\ts_waitcnt lgkmcnt(%5)\n\tbuffer_store_dwordx4 v[%2:%2+3], %0, %1, 0 offen sc1\n" \
                 "1:\n\ts_bitcmp1_b32 %3, %4+1\n\ts_cbranch_scc0 2f\n\ts_waitcnt lgkmcnt(%5)\n\tbuffer_store_dwordx4 v[%2:%2+3], %0, %1, 0 offen " P "\n\ts_nop 1\n2:" \
                 :: "v"(ad), "s"(rsA), "n"(HREG(S, 0)), "s"(pm), "n"(2 * S), "n"(W) : "memory", "scc")
template <int S, int W, bool WT> static __device__ __forceinline__ void put(u32x4 rsA, uint32_t ad, uint32_t pm) {
    if (WT) PUT("sc1 nt"); else PUT("nt");
}

#define CODE_AHEAD(SYM, NEXT, LANE) ({ u64 ca_; int cb_; \
    asm volatile("s_getpc_b64 vcc\n\ts_add_u32 vcc_lo, vcc_lo, " #SYM "@rel32@lo+4\n\ts_addc_u32 vcc_hi, vcc_hi, " #SYM "@rel32@hi+12\n\t" \
                 "s_mov_b64 %0, vcc\n\ts_max_i32 %1, " #NEXT "-" #SYM ", 0" : "=s"(ca_), "=s"(cb_) :: "scc", "vcc"); \
    __builtin_amdgcn_raw_buffer_load_b32(__builtin_amdgcn_make_buffer_rsrc((void*)ca_, 0, cb_, 0x00020000), ((blockIdx.x >> 3) * 64u + (LANE)) * 64u, 0, 0); })
extern "C" __global__ __launch_bounds__(NT, 4) __attribute__((amdgpu_num_vgpr(HB / 2))) void part_kernel(const int* __restrict__ iin, const int* __restrict__ iout,
                                                  const uint16_t* __restrict__ ang, const uint16_t* __restrict__ freq,
                                                  const u32x2* __restrict__ m, const uint16_t* __restrict__ wdown,
                                                  char* __restrict__ recs,
                                                  int T, int E, int ntile, int nstat, int ks, int lcap) {
    RTS(0)
    asm volatile("" :: "s"(iin), "s"(iout), "s"(ang), "s"(freq), "s"(m), "s"(wdown), "s"(recs), "s"(T), "s"(E), "s"(ntile), "s"(nstat), "s"(ks), "s"(lcap));
    const int tid0 = threadIdx.x;
    const int wv = __builtin_amdgcn_readfirstlane(tid0 >> 6);
    const int G = T >> 2;
    const int pos = (int)((blockIdx.x & 7) << 5) | (int)(blockIdx.x >> 3);
    float frr;
    unsigned* meta = (unsigned*)(recs - W_RECS);
    const uint32_t gbase = (LBASE + (uint32_t)lcap) * 128u;
    const uint32_t rbytes = gbase + (uint32_t)lcap * 2u;
    const __amdgpu_buffer_rsrc_t rsR = __builtin_amdgcn_make_buffer_rsrc(recs, 0, (int)rbytes, 0x00020000);
    const u32x4 rsA = {(uint32_t)(uintptr_t)recs, (uint32_t)((uintptr_t)recs >> 32) & 0xffffu, rbytes, 0x00020000u};
    const uint32_t g2 = 2u * (uint32_t)ks;
    const uint32_t far = 1u >> ((blockIdx.x >> 3) & 3u);
    const uint32_t SK = SKM + far * (SKH - SKM), SS = SSM + far * (SSH - SSM), UINC = 0x80000001u + SK * 64u;
    const uint32_t spec0 = gbase - 4u;
    auto fixed_tile = [&](int r) __attribute__((always_inline)) -> int { return r * NWG + ((pos + r * 37) & (NWG - 1)); };
    int tl0 = fixed_tile(0), tl1 = fixed_tile(1), tl2;
    uint32_t tk = 0u;
    const __amdgpu_buffer_rsrc_t rsO = __builtin_amdgcn_make_buffer_rsrc((void*)iout, 0, G * 16, 0x00020000);
    const __amdgpu_buffer_rsrc_t rsI = __builtin_amdgcn_make_buffer_rsrc((void*)iin, 0, G * 16, 0x00020000);
    const __amdgpu_buffer_rsrc_t rsG = __builtin_amdgcn_make_buffer_rsrc((void*)ang, 0, G * 8, 0x00020000);
    uint32_t t8 = (uint32_t)tid0 * 8u;
    auto load_one = [&](Raw& r, int tl, int idx) __attribute__((always_inline)) {
        const int j = idx < 4 ? idx >> 1 : idx & 1;
        const uint32_t g0 = (uint32_t)(tl * GR + j * NT);
        __builtin_amdgcn_sched_barrier(0);
        if (idx >= 4) r.a[j] = __builtin_amdgcn_raw_buffer_load_b64(rsG, t8, g0 * 8u, AUX_NT);
        else if (idx & 1) r.i[j] = __builtin_amdgcn_raw_buffer_load_b128(rsI, t8 + t8, g0 * 16u, AUX_NT);
        else r.o[j] = __builtin_amdgcn_raw_buffer_load_b128(rsO, t8 + t8, g0 * 16u, AUX_NT);
        __builtin_amdgcn_sched_barrier(0);
    };
    auto peek = [&](uint32_t at) __attribute__((always_inline)) -> uint32_t {
        return __hip_atomic_load((unsigned*)((char*)meta + at), __ATOMIC_RELAXED, __HIP_MEMORY_SCOPE_AGENT);
    };
    auto ready = [&]() __attribute__((always_inline)) {
        if (blockIdx.x != 0) {
            const uint32_t x = lds_ld(vconst<P_FLAG + 8>());
            while (peek(vconst<M_BEAT * 4>()) == x) __builtin_amdgcn_s_sleep(8);
        }
    };
    auto beat = [&]() __attribute__((always_inline)) {
        (void)__hip_atomic_fetch_add((unsigned*)((char*)meta + vconst<M_BEAT * 4>()), vconst<1>(), __ATOMIC_RELAXED, __HIP_MEMORY_SCOPE_AGENT);
    };
    auto draw = [&](uint32_t at) __attribute__((always_inline)) -> uint32_t {
        return __hip_atomic_fetch_add((unsigned*)((char*)meta + at), vconst<1>(), __ATOMIC_RELAXED, __HIP_MEMORY_SCOPE_AGENT);
    };
    uint32_t la;
    auto tab_at = [&]() __attribute__((always_inline)) -> uint32_t {
        return P_TAB + bfi(0xffu, bfi(0x3cu, t8 >> 2, t8 << 4), t8);
    };
    u32x2 nul;
    uint32_t U[2];
    {
        const uint32_t t = (uint32_t)tid0;
        la = P_BUF + ((t >> 5) << 12) + ((t & 0x18u) << 3) + ((t & 3u) << 4) + ((t & 4u) << 1);
        const uint32_t q = ((t >> 3) ^ (t >> 1 & 2u)) & 3u;
        nul.x = (0x7u >> q) << 31; nul.y = (0xbu >> q) << 31;
        U[0] = (((t >> 1) & 15u) * SS + ((t & 1u) * 64u + (t >> 5)) * SC + BPL(blockIdx.x)) * 128u + (0x80000000u - SK * 64u) + 8u - g2;
        U[1] = U[0] + 32u * SC * 128u;
    }
    auto log_line = [&](bool lg, uint32_t b) __attribute__((always_inline)) -> uint32_t {
        uint32_t lp = 0u;
        if (lg && (t8 & 0x38u) == 0u) {
            ready();
            lp = draw(vconst<M_LOG * 4>());
            __builtin_amdgcn_raw_buffer_store_b16((uint16_t)b, rsR, gbase + lp * 2u, 0, 0);
        }
        return LBASE + (uint32_t)__builtin_amdgcn_ds_bpermute((int)((t8 >> 1) & 0xe0u), (int)lp);
    };
    auto general = [&](int D, bool all, u64 sm) __attribute__((always_inline)) {
        uint32_t todo = __builtin_amdgcn_readfirstlane((uint32_t)sm | (uint32_t)(sm >> 32));
#pragma unroll 1
        while (todo) {
            const uint32_t S = (uint32_t)__builtin_ctz(todo) >> 1;
            todo &= ~(3u << (2u * S));
            pin(t8); pin(la);
            const uint32_t V = lds_ld(P_TAB + (t8 & ~63u) + S * 4u), g = V & 15u;
            const uint32_t col = S * 128u + ((t8 >> 1) & 0x60u) + (t8 >> 8);
            const bool act = all ? V != 0xffffffffu : (V & 0x80000009u) == 9u;
            const int hr = HREG((int)S, D) - HB;
            uint32_t ln = log_line(act && g > 8u + NTAIL, col);
            if (act) {
                const u32x2 N = *(const LDS u32x2*)(uintptr_t)(la + S * 256u);
                *(LDS u32x2*)(uintptr_t)(la + S * 256u) = nul;
                lds_st(P_TAB + (t8 & ~63u) + S * 4u, vconst<0xffffffffu>());
                if (!(g & 1u)) hset_at(hr, N);
                else {
                    uint32_t a, st;
                    if (g < 8u) { a = (V & ~15u) + ((t8 & 0x38u) << 1); st = 8u; }
                    else {
                        if (g <= 8u + NTAIL) ln = TBASE + ((g - 8u) >> 1) * TPL + col * TST + blockIdx.x;
                        a = ln * 128u + (la & 0x38u); st = 64u;
                    }
                    __builtin_amdgcn_raw_buffer_store_b64(hget_at(hr), rsR, a, 0, AUX_SC1);
                    __builtin_amdgcn_raw_buffer_store_b64(N, rsR, a + st, 0, AUX_SC1);
                }
            }
        }
    };
    unsigned epoch = 0;
    u64 pb2 = 0ull;
    u64 fm = 0ull, sm = 0ull;
    auto look = [&](u32x2 c) __attribute__((always_inline)) {
        uint32_t V[2];
        const bool late = __builtin_amdgcn_ballot_w64(((U[0] | U[1]) & 8u) != 0u) != 0ull;
        epoch++;
        asm volatile("v_mov_b32 %0, -1\n\tv_mov_b32 %1, -1\n\t"
                     "v_cmpx_le_u32 %6, %7, %2\n\tv_mov_b32 %0, %4\n\tv_add_u32 %4, %9, %4\n\tv_subrev_u32 %2, %8, %2\n\ts_mov_b64 exec, -1\n\t"
                     "v_cmpx_le_u32 vcc, %8, %3\n\tv_mov_b32 %1, %5\n\tv_add_u32 %5, %9, %5\n\tv_subrev_u32 %3, %8, %3\n\t"
                     "s_or_b64 %6, %6, exec\n\ts_mov_b64 exec, -1"
                     : "=&v"(V[0]), "=&v"(V[1]), "+v"(c.x), "+v"(c.y), "+v"(U[0]), "+v"(U[1]), "=&s"(fm) : "s"(CHV), "n"(CHV), "s"(UINC) : "vcc", "scc");
        *(LDS u32x2*)(uintptr_t)(P_CNT + t8) = c;
        pin(t8);
        const uint32_t tw = tab_at();
        lds_st(tw, V[0]);
        lds_st(tw + 64u, V[1]);
        if (wv == 1) lds_st(P_CNT + NB * 4 - 256 + (t8 >> 1), vconst<0x40000000u>());
        pb2 = __builtin_amdgcn_ballot_w64((int)(V[0] & V[1]) >= 0);
        sm = 0ull;
        if (__builtin_expect(late, 0)) {
            sm = __builtin_amdgcn_ballot_w64((V[0] & 0x80000009u) == 9u || (V[1] & 0x80000009u) == 9u);
#pragma unroll
            for (int e = 0; e < 2; e++)
                if ((V[e] & 0x8000000fu) == 9u + NTAIL) U[e] -= 2u * UINC;
        }
    };
    u32x4 Va, Vb;

    Raw raw[PD];
    {
        const int tid = tid0;
        asm volatile("" ::: "v88", "v127");
        typedef const __attribute__((address_space(4))) u32x4* cptr;
        u32x4 wd0 = ((cptr)(uintptr_t)wdown)[0], wd1 = ((cptr)(uintptr_t)wdown)[1];
        __builtin_amdgcn_sched_barrier(0);
        if (wv == 5) tk = CODE_AHEAD(part_kernel, acc_kernel, (uint32_t)tid & 63u);
        if (tid == 128) tk = peek(vconst<M_BEAT * 4>());
        if (blockIdx.x == 0 && wv == 3 && (tid & 63) < M_BEAT / 64) {
            __hip_atomic_store(meta + (tid & 63) * 64, 0u, __ATOMIC_RELAXED, __HIP_MEMORY_SCOPE_AGENT);
            __hip_atomic_store(meta + (tid & 63) * 64 + 1, 0u, __ATOMIC_RELAXED, __HIP_MEMORY_SCOPE_AGENT);
        }
        const __amdgpu_buffer_rsrc_t rsE = __builtin_amdgcn_make_buffer_rsrc((void*)m, 0, E * 8, 0x00020000);
        u32x2 mv[5];
        mv[4] = __builtin_amdgcn_raw_buffer_load_b64(rsE, (t8 & 511u) * (uint32_t)(E >> 6), 0, 0);
#pragma unroll
        for (int q = 0; q < 4; q++)
            mv[q] = __builtin_amdgcn_raw_buffer_load_b64(rsE, t8 & 511u, ((blockIdx.x >> 2) * 256u + (blockIdx.x & 3u) * 16u + (uint32_t)wv + 64u * q) * 512u, AUX_NT);
#pragma unroll
        for (int d = 0; d < MDL; d++) load_one(raw[1], tl0, d);
        asm volatile("" : "+s"(wd0), "+s"(wd1));
        const u32x2 ci = {CINIT, CINIT};
        *(LDS u32x2*)(uintptr_t)(P_CNT + tid * 8) = ci;
        if (tid < 64) lds_st(P_CNT + (NB + tid) * 4, 0x40000000u);
        if (tid < 8 && tid != 2) lds_st(P_FLAG + tid * 4, tid == 0 ? 0xffffffffu : 0u);
        if (tid == 128) lds_st(P_FLAG + 8, tk);
        const uint32_t lane = (uint32_t)tid & 63u;
        int e0[4];
        {
            float w[16];
#pragma unroll
            for (int i = 0; i < 4; i++) {
                w[2 * i] = bf2f(wd0[i] & 0xffffu); w[2 * i + 1] = bf2f(wd0[i] >> 16);
                w[8 + 2 * i] = bf2f(wd1[i] & 0xffffu); w[8 + 2 * i + 1] = bf2f(wd1[i] >> 16);
            }
            auto dot = [&](u32x2 v, int c) __attribute__((always_inline)) -> f32x2 {
                const f32x2 w0 = {w[c], w[c + 1]}, w1 = {w[4 + c], w[5 + c]}, w2 = {w[8 + c], w[9 + c]}, w3 = {w[12 + c], w[13 + c]};
                return w0 * bf2f(v.x & 0xffffu) + w1 * bf2f(v.x >> 16) + w2 * bf2f(v.y & 0xffffu) + w3 * bf2f(v.y >> 16);
            };
#pragma unroll
            for (int cp = 0; cp < 2; cp++) {
                if (cp == 1) {
#pragma unroll
                    for (int d = MDL; d < MDM; d++) load_one(raw[1], tl0, d);
                }
                if (blockIdx.x >= SLOTI / 64) continue;
                const f32x2 sd = dot(mv[4], 2 * cp);
#pragma unroll
                for (int e = 0; e < 2; e++) {
                    const int ex = (int)(wave_max(__float_as_uint(sd[e]) & 0x7fffffffu) >> 23);
                    e0[2 * cp + e] = __builtin_amdgcn_readfirstlane(ex == 0 ? 0 : min(max(ex - 129, -100), 100));
                }
                const f32x2 sc = {__uint_as_float((uint32_t)(127 - e0[2 * cp]) << 23), __uint_as_float((uint32_t)(127 - e0[2 * cp + 1]) << 23)};
                u32x4 o;
                u16x2 mx = {0, 0};
                f32x2 sq = {0.f, 0.f}, sg = {0.f, 0.f};
#pragma unroll
                for (int q = 0; q < 4; q++) {
                    const f32x2 y = dot(mv[q], 2 * cp) * sc;
                    const uint32_t hb = pk_bf16(y.x, y.y);
                    const f32x2 x = {__uint_as_float(hb << 16), __uint_as_float(hb & 0xffff0000u)};
                    o[q] = __builtin_bit_cast(uint32_t, __builtin_amdgcn_cvt_pkrtz(x.x, x.y));
                    mx = __builtin_elementwise_max(mx, __builtin_bit_cast(u16x2, hb & 0x7fff7fffu));
                    sq += x * x;
                    sg += x;
                }
                *(LDS u32x4*)(uintptr_t)(P_BUF + (cp * 64u + lane) * 272u + wv * 16) = o;
#pragma unroll
                for (int e = 0; e < 2; e++) {
                    const u32x4 st = {wave_max(mx[e]), __float_as_uint(wave_sum(sq[e])), __float_as_uint(wave_sum(sg[e])), 0u};
                    if (lane == 0u) *(LDS u32x4*)(uintptr_t)(P_LST + ((2 * cp + e) * 16 + wv) * 16) = st;
                }
            }
        }
        __builtin_amdgcn_sched_barrier(0);
        const uint16_t fq = freq[0];
#pragma unroll
        for (int d = MDM; d < 6; d++) load_one(raw[1], tl0, d);
#pragma unroll
        for (int d = 0; d < 6; d++)
            if (((LPOS >> (4 * d)) & 15) < 4) load_one(raw[0], tl1, d);
        __syncthreads();
        u32x4 mo[2];
#pragma unroll
        for (int cp = 0; cp < 2; cp++) mo[cp] = *(const LDS u32x4*)(uintptr_t)(P_BUF + (cp * 64u + (t8 >> 7)) * 272u + ((t8 + t8) & 0xf0u));
        if (blockIdx.x == 0 && wv == 3) asm volatile("s_waitcnt vmcnt(0)" ::: "memory");
        __syncthreads();
#pragma unroll
        for (int s = 0; s < 16; s++) *(LDS u32x2*)(uintptr_t)(la + s * 256) = nul;
        asm volatile("" : "+v"(raw[1].o[0]), "+v"(raw[1].o[1]), "+v"(raw[1].i[0]), "+v"(raw[1].i[1]), "+v"(raw[1].a[0]), "+v"(raw[1].a[1]));
        asm volatile("s_nop 0\n\tv_readfirstlane_b32 %0, %1" : "=s"(frr) : "v"(bf2f(fq) * 0.15915494309189535f));
        if (blockIdx.x < SLOTI / 64) {
            const __amdgpu_buffer_rsrc_t rsM = __builtin_amdgcn_make_buffer_rsrc((void*)(meta + W_MDC / 4), 0, (int)(128u * SLOTI * 4u), 0x00020000);
#pragma unroll
            for (int cp = 0; cp < 2; cp++) {
                const uint32_t row = cp * 64u + (t8 >> 7), c16 = (t8 + t8) & 0xf0u;
                __builtin_amdgcn_raw_buffer_store_b128(mo[cp], rsM, row * (SLOTI * 4u) + c16 + blockIdx.x * 256u, 0, AUX_SC1);
            }
            if (wv == 0) {
                const u32x4 st = *(const LDS u32x4*)(uintptr_t)(P_LST + t8 * 2u);
                const float sm1 = row_sum(__uint_as_float(st.z));
                const u32x2 r = {row_max(st.x) | (row_max(st.x ? 0xffffu - st.x : 0u) << 16), __float_as_uint(row_sum(__uint_as_float(st.y)))};
                if ((lane & 15u) == 15u) {
                    ((u32x2*)(meta + W_STAT / 4))[(lane >> 4) * NWG + blockIdx.x] = r;
                    meta[W_SUM / 4 + (lane >> 4) * NWG + blockIdx.x] = __float_as_uint(sm1);
                }
                if (blockIdx.x == 0 && lane < 4u) meta[M_E0 + lane] = (uint32_t)(lane == 0u ? e0[0] : lane == 1u ? e0[1] : lane == 2u ? e0[2] : e0[3]);
            }
        }
    }
    pin(la);
    RTS(5)

    uint32_t w[8], aw[8], xv[8];
    u64 nm[8];
    auto later2 = [&](int k) __attribute__((always_inline)) {
        uint32_t x0, x1;
        asm volatile("v_lshrrev_b32 %0, 4, %2\n\tv_lshrrev_b32 %1, 4, %5\n\tv_bitop3_b32 %0, %2, %0, 12 bitop3:0x72\n\tv_bitop3_b32 %1, %5, %1, 12 bitop3:0x72\n\t"
                     "s_not_b64 exec, %8\n\tds_write_b32 %2, %3 offset:%10\n\tds_or_b32 %0, %4 offset:%10\n\t"
                     "s_not_b64 exec, %9\n\tds_write_b32 %5, %6 offset:%10\n\tds_or_b32 %1, %7 offset:%10\n\ts_mov_b64 exec, -1"
                     : "=&v"(x0), "=&v"(x1)
                     : "v"(aw[k]), "v"(w[k]), "v"(xv[k]), "v"(aw[k + 1]), "v"(w[k + 1]), "v"(xv[k + 1]), "s"(nm[k]), "s"(nm[k + 1]), "n"(P_BUF) : "memory", "scc");
    };
    auto drain = [&]() __attribute__((always_inline)) {
        if (wv == 1) {
            const uint32_t q = (epoch + 1u) & 3u;
            const uint32_t n = __builtin_amdgcn_readfirstlane(lds_ld(vconst<P_FLAG + 16>() + q * 4u));
            if (__builtin_expect(n != 0u, 0)) {
                pin(t8);
                const uint32_t l = (t8 >> 3) & 63u;
                if (l < min(n, (uint32_t)LCAP)) {
                    const u32x4 e = *(const LDS u32x4*)(uintptr_t)(P_LST + (q * LCAP + l) * 16u);
                    lds_st(P_BUF + e.x, e.y);
                    lds_or(P_BUF + slot_x(e.x), e.z);
                }
                lds_st(vconst<P_FLAG + 16>() + q * 4u, vconst<0>());
            }
        }
    };
    auto again = [&](int D) __attribute__((always_inline)) {
        do {
            general(D, true, fm);
            __syncthreads();
            later2(0); later2(2); later2(4); later2(6);
            drain();
#pragma unroll
            for (int k = 0; k < 8; k++) {
                pin(aw[k]);
                bool due = false;
                if (aw[k] & 0x80000000u) {
                    xv[k] -= CHV;
                    if (xv[k] < CHV) {
                        const uint32_t f = (aw[k] >> 16) & 0x3ffu;
                        aw[k] = slot_w(aw[k] & 0xffffu, xv[k]);
                        xv[k] = f << ((xv[k] >> 8) & 31u);
                        due = true;
                    } else lds_st(vconst<P_FLAG>(), epoch + 1u);
                }
                nm[k] = ~__builtin_amdgcn_ballot_w64(due);
            }
            look(*(const LDS u32x2*)(uintptr_t)(P_CNT + t8));
            asm volatile("s_waitcnt lgkmcnt(0)" ::: "memory");
            __syncthreads();
        } while (__builtin_amdgcn_readfirstlane(lds_ld(vconst<P_FLAG>())) == epoch);
    };
    auto round = [&](auto DD, auto LL, Raw& rc, Raw& rn, int rd) __attribute__((always_inline)) {
        constexpr int D = decltype(DD)::value;
        constexpr bool LAST = decltype(LL)::value == 1, FIRST = decltype(LL)::value == 2;
        const int ng = min(GR, G - tl0 * GR);
        auto hook = [&](int p) __attribute__((always_inline)) {
#pragma unroll
            for (int q = 0; q < 6; q++)
            {
                const int lp = (LPOS >> (4 * q)) & 15;
                if (!LAST && lp < 4 && lp == p) load_one(rc, tl2, q);
                if (!LAST && lp >= 4 && (lp < 8 || lp >= 12 ? lp : lp - 8) == p) load_one(rn, tl1, q);
            }
        };
        uint32_t wn[8], ca[8], px[4], cv[8];
        float mx;
        float cs[2], cm[2] = {0.f, 0.f};
        auto convA = [&](int q) __attribute__((always_inline)) {
            if (LAST) return;
            const uint32_t a = rc.a[q >> 1][q & 1];
            const f32x2 av = {__uint_as_float(a << 16), __uint_as_float(a & 0xffff0000u)};
            f32x2 rv;
            asm("v_pk_mul_f32 %0, %1, %2 op_sel_hi:[1,0]" : "=v"(rv) : "v"(av), "s"((u64)__float_as_uint(frr)));
            mx = __builtin_elementwise_maximum(__builtin_elementwise_maximum(__builtin_fabsf(rv.x), __builtin_fabsf(rv.y)), (q & 1) ? mx : 0.f);
            cs[0] = __builtin_amdgcn_cosf(rv.x); cs[1] = __builtin_amdgcn_cosf(rv.y);
            asm volatile("" : "+v"(mx), "+v"(cs[0]), "+v"(cs[1]));
            if (FIRST) {
                const float c0 = fminf(fmaxf(cs[0], -1.f), 1.f), c1 = fminf(fmaxf(cs[1], -1.f), 1.f);
                cm[0] += c0 + c1; cm[1] += c0 * c0 + c1 * c1;
                if (q == 3 && wv == 4) {
                    const float a0 = wave_sum(cm[0]), a1 = wave_sum(cm[1]);
                    if ((t8 & 0x1f0u) == 0u) meta[W_SUM / 4 + (4 + ((t8 >> 3) & 1u)) * NWG + blockIdx.x] = __float_as_uint((t8 & 8u) ? a1 : a0);
                }
            }
            __builtin_amdgcn_sched_barrier(0);
        };
        auto convB = [&](int k) __attribute__((always_inline)) {
            if (LAST) return;
            const uint32_t o = rc.o[k >> 2][k & 3], i = rc.i[k >> 2][k & 3];
            wn[k] = (__builtin_amdgcn_alignbit(bfi(63u, o, o >> 5), __float_as_uint(cs[k & 1]), 30u) << 14) | (i >> 6);
            ca[k] = (bfi(63u, i, o) << 2) & 0x1ffcu;
            pin(wn[k]); pin(ca[k]);
            __builtin_amdgcn_sched_barrier(0);
        };
        auto convD = [&](int q) __attribute__((always_inline)) {
            if (LAST) return;
            asm volatile("v_cvt_pk_f16_f32 %0, |%1|, |%2|\n\tv_pk_mul_f16 %0, %0, %3 op_sel_hi:[1,0]" : "=v"(px[q]) : "v"(cs[0]), "v"(cs[1]), "s"(0x3ffu));
        };
        uint32_t cst;
        auto check = [&](int j) __attribute__((always_inline)) {
            if (LAST) return;
            if (__builtin_expect((__builtin_amdgcn_ballot_w64(!(mx <= RMAXREV)) | (u64)(uint32_t)(GR - ng)) != 0ull, 0)) {
                pin(t8);
                asm volatile("" : "+v"(rc.a[j]));
                const int lg = j * NT + (int)(t8 >> 3);
#pragma unroll
                for (int k = 0; k < 4; k++) {
                    const uint32_t a = (k & 1) ? (rc.a[j][k >> 1] >> 16) : (rc.a[j][k >> 1] & 0xffffu);
                    const bool nrm = __builtin_fabsf(frr * bf2f(a)) <= RMAXREV;
                    if (!(lg < ng && nrm)) ca[j * 4 + k] = NB * 4u + ((t8 >> 1) & 0xfcu);
                    if (lg < ng && !nrm) {
                        ready();
                        __builtin_amdgcn_raw_buffer_store_b32((uint32_t)((tl0 * GR + lg) * 4 + k), rsR, spec0 - draw(vconst<M_LOG * 4 + 4>()) * 4u, 0, 0);
                    }
                }
            }
            cst = vconst<CSTEP>();
        };
        auto ticket = [&](int k) __attribute__((always_inline)) {
            if (LAST) return;
            cv[k] = lds_add(P_CNT + ca[k], cst);
            __builtin_amdgcn_sched_barrier(0);
        };
        auto insert = [&](int j) __attribute__((always_inline)) {
            const int k0 = 4 * j;
            const bool odd = __builtin_amdgcn_ballot_w64(max(max(cv[k0], cv[k0 + 1]), max(cv[k0 + 2], cv[k0 + 3])) >= 2u * CHV) != 0ull;
#pragma unroll
            for (int k = k0; k < k0 + 4; k += 2) {
                uint32_t x0, x1;
                asm volatile("v_lshrrev_b32 %6, 11, %8\n\tv_lshrrev_b32 %7, 11, %9\n\t"
                             "v_bitop3_b32 %6, %6, %10, 60 bitop3:0x28\n\tv_bitop3_b32 %7, %7, %11, 60 bitop3:0x28\n\t"
                             "v_lshl_or_b32 %0, %10, 4, %6\n\tv_lshl_or_b32 %1, %11, 4, %7\n\t"
                             "v_lshlrev_b32_sdwa %2, %8, %12 dst_sel:DWORD dst_unused:UNUSED_PAD src0_sel:BYTE_1 src1_sel:WORD_0\n\t"
                             "v_lshlrev_b32_sdwa %3, %9, %12 dst_sel:DWORD dst_unused:UNUSED_PAD src0_sel:BYTE_1 src1_sel:WORD_1\n\t"
                             "v_bitop3_b32 %6, %0, %10, 12 bitop3:0x72\n\tv_bitop3_b32 %7, %1, %11, 12 bitop3:0x72\n\t"
                             "v_cmpx_gt_u32 %4, %15, %8\n\tds_write_b32 %0, %13 offset:%16\n\tds_or_b32 %6, %2 offset:%16\n\ts_mov_b64 exec, -1\n\t"
                             "v_cmpx_gt_u32 %5, %15, %9\n\tds_write_b32 %1, %14 offset:%16\n\tds_or_b32 %7, %3 offset:%16\n\ts_mov_b64 exec, -1"
                             : "=&v"(aw[k]), "=&v"(aw[k + 1]), "=&v"(xv[k]), "=&v"(xv[k + 1]), "=&s"(nm[k]), "=&s"(nm[k + 1]), "=&v"(x0), "=&v"(x1)
                             : "v"(cv[k]), "v"(cv[k + 1]), "v"(ca[k]), "v"(ca[k + 1]), "v"(px[k >> 1]), "v"(wn[k]), "v"(wn[k + 1]), "s"(CHV), "n"(P_BUF) : "memory");
                w[k] = wn[k]; w[k + 1] = wn[k + 1];
                if (__builtin_expect(odd, 0)) {
#pragma unroll
                    for (int q = k; q < k + 2; q++) {
                        nm[q] |= __builtin_amdgcn_ballot_w64(cv[q] >= 2u * CHV);
                        if (cv[q] >= 2u * CHV && cv[q] < 0x40000000u) {
                            const uint32_t lq = (epoch + 2u) & 3u;
                            uint32_t p = LCAP;
                            if (cv[q] < 3u * CHV) p = lds_add(vconst<P_FLAG + 16>() + lq * 4u, vconst<1>());
                            if (p < LCAP) {
                                const u32x2 e = {aw[q], w[q]};
                                *(LDS u32x2*)(uintptr_t)(P_LST + (lq * LCAP + p) * 16u) = e;
                                lds_st(P_LST + (lq * LCAP + p) * 16u + 8u, xv[q]);
                            } else {
                                aw[q] = ca[q] | ((q & 1) ? (px[q >> 1] & 0xffff0000u) : (px[q >> 1] << 16)) | 0x80000000u;
                                xv[q] = cv[q] - CHV;
                                lds_st(vconst<P_FLAG>(), epoch);
                            }
                        }
                    }
                }
            }
        };
        constexpr int P0 = D ? 0 : 3, P1 = D ? 1 : 2, P2 = D ? 2 : 1, P3 = D ? 3 : 0;
        const uint32_t fl = lds_ld(vconst<P_FLAG>());
        if (!LAST) asm volatile("" : "+v"(rc.o[0]), "+v"(rc.o[1]), "+v"(rc.i[0]), "+v"(rc.i[1]), "+v"(rc.a[0]), "+v"(rc.a[1]));
        if (!LAST && (rd + 4 >= nstat || tl1 >= ntile) && wv == 2) {
            pin(tk);
            if ((t8 & 511u) == 0u) {
                if (tl1 >= ntile) {
                    if (rd + 3 <= nstat) ready();
                    (void)draw(vconst<M_MD * 4>());
                } else if (rd + 4 == nstat) tk = peek(vconst<M_BEAT * 4>());
                else {
                    if (rd + 3 == nstat) {
                        if (tk == lds_ld(vconst<P_FLAG + 8>())) ready();
                        tk = 0u;
                    }
                    lds_st(vconst<P_FLAG + 4>(), tk);
                    if ((int)tk < ntile - nstat * NWG) tk = draw(vconst<M_TILE * 4>());
                }
            }
        }
        if (!LAST && blockIdx.x == 0 && wv == 3 && (t8 & 511u) == 0u) beat();
        hook(12);
        pin(t8);
        const uint32_t tr = P_TAB + (t8 & ~63u), u16 = (t8 + t8) & 0x70u;
        convA(0);
        convB(0);
        const bool ag = __builtin_amdgcn_readfirstlane(fl) == epoch;
        if (!FIRST && __builtin_expect(LAST || ag || sm != 0ull, 0)) {
            if (ag) again(D);
            general(D, false, sm);
            Va = *(const LDS u32x4*)(uintptr_t)(P_TAB + (t8 & ~63u) + P0 * 16);
            Vb = *(const LDS u32x4*)(uintptr_t)(P_TAB + (t8 & ~63u) + P1 * 16);
            asm volatile("" : "+v"(Va), "+v"(Vb));
        }
        uint32_t pq = (uint32_t)pb2 | (uint32_t)(pb2 >> 32);
        pq = (pq | (pq >> 1)) & 0x55555555u;
        pq = __builtin_amdgcn_readfirstlane(pq + (pq & (far * NEARM)));
        uint32_t ad[8];
        if (!FIRST) ad[0] = take<4 * P0 + 0, D>(Va[0], u16, la, nul);
        convB(1);
        if (!FIRST) ad[1] = take<4 * P0 + 1, D>(Va[1], u16, la, nul);
        convD(0);
        convA(1);
        if (!FIRST) ad[2] = take<4 * P0 + 2, D>(Va[2], u16, la, nul);
        convB(2);
        hook(13);
        if (!FIRST) ad[3] = take<4 * P0 + 3, D>(Va[3], u16, la, nul);
        convB(3);
        if (!FIRST) put<4 * P0 + 0, LAST ? 3 : 3, LAST>(rsA, ad[0], pq);
        if (!FIRST) ad[4] = take<4 * P1 + 0, D>(Vb[0], u16, la, nul);
        convD(1);
        check(0);
        ticket(0);
        hook(4);
        if (!FIRST) put<4 * P0 + 1, LAST ? 3 : 4, LAST>(rsA, ad[1], pq);
        if (!FIRST) ad[5] = take<4 * P1 + 1, D>(Vb[1], u16, la, nul);
        convA(2);
        ticket(1);
        if (!FIRST) put<4 * P0 + 2, LAST ? 3 : 5, LAST>(rsA, ad[2], pq);
        if (!FIRST) ad[6] = take<4 * P1 + 2, D>(Vb[2], u16, la, nul);
        convB(4);
        ticket(2);
        if (!FIRST) put<4 * P0 + 3, LAST ? 3 : 6, LAST>(rsA, ad[3], pq);
        if (!FIRST) Va = *(const LDS u32x4*)(uintptr_t)(tr + P2 * 16);
        __builtin_amdgcn_sched_barrier(0);
        if (!FIRST) ad[7] = take<4 * P1 + 3, D>(Vb[3], u16, la, nul);
        convB(5);
        ticket(3);
        SETP(2);
        if (!FIRST) put<4 * P1 + 0, LAST ? 4 : 8, LAST>(rsA, ad[4], pq);
        if (!FIRST) ad[0] = take<4 * P2 + 0, D>(Va[0], u16, la, nul);
        convD(2);
        convA(3);
        hook(5);
        if (!FIRST) put<4 * P1 + 1, LAST ? 4 : 7, LAST>(rsA, ad[5], pq);
        if (!FIRST) ad[1] = take<4 * P2 + 1, D>(Va[1], u16, la, nul);
        convB(6);
        if (!FIRST) put<4 * P1 + 2, LAST ? 4 : 6, LAST>(rsA, ad[6], pq);
        if (!FIRST) ad[2] = take<4 * P2 + 2, D>(Va[2], u16, la, nul);
        convB(7);
        if (!FIRST) put<4 * P1 + 3, LAST ? 3 : 4, LAST>(rsA, ad[7], pq);
        if (!FIRST) Vb = *(const LDS u32x4*)(uintptr_t)(tr + P3 * 16);
        __builtin_amdgcn_sched_barrier(0);
        if (!FIRST) ad[3] = take<4 * P2 + 3, D>(Va[3], u16, la, nul);
        convD(3);
        check(1);
        ticket(4);
        SETP(1);
        hook(6);
        if (!FIRST) put<4 * P2 + 0, LAST ? 4 : 5, LAST>(rsA, ad[0], pq);
        if (!FIRST) ad[4] = take<4 * P3 + 0, D>(Vb[0], u16, la, nul);
        ticket(5);
        if (!FIRST) put<4 * P2 + 1, LAST ? 4 : 6, LAST>(rsA, ad[1], pq);
        if (!FIRST) ad[5] = take<4 * P3 + 1, D>(Vb[1], u16, la, nul);
        ticket(6);
        if (!FIRST) put<4 * P2 + 2, LAST ? 4 : 7, LAST>(rsA, ad[2], pq);
        if (!FIRST) ad[6] = take<4 * P3 + 2, D>(Vb[2], u16, la, nul);
        ticket(7);
        SETP(0);
        if (!FIRST) put<4 * P2 + 3, LAST ? 3 : 7, LAST>(rsA, ad[3], pq);
        if (!FIRST) ad[7] = take<4 * P3 + 3, D>(Vb[3], u16, la, nul);
        hook(7);
        SETP(3);
        asm volatile("s_waitcnt lgkmcnt(0)" ::: "memory");
        __syncthreads();
        u32x2 cn;
        uint32_t tv;
        if (!LAST) { cn = *(const LDS u32x2*)(uintptr_t)(P_CNT + t8); tv = lds_ld(vconst<P_FLAG + 4>()); }
        __builtin_amdgcn_sched_barrier(0);
        if (!FIRST) later2(0);
        if (!FIRST) put<4 * P3, 15, LAST>(rsA, ad[4], pq);
        if (!FIRST) later2(2);
        if (!FIRST) put<4 * P3 + 1, 15, LAST>(rsA, ad[5], pq);
        if (!FIRST) drain();
        if (!LAST) {
            tl2 = rd + 2 < nstat ? fixed_tile(rd + 2) : nstat * NWG + (int)__builtin_amdgcn_readfirstlane(tv);
            look(cn);
        }
        hook(0);
        SETP(2);
        if (!FIRST) later2(4);
        if (!FIRST) put<4 * P3 + 2, 15, LAST>(rsA, ad[6], pq);
        if (!FIRST) later2(6);
        if (!FIRST) put<4 * P3 + 3, 15, LAST>(rsA, ad[7], pq);
        if (!LAST) {
            hook(1);
            SETP(1);
            insert(0);
            hook(2);
            SETP(0);
            pin(t8);
            Va = *(const LDS u32x4*)(uintptr_t)(P_TAB + (t8 & ~63u) + P3 * 16);
            Vb = *(const LDS u32x4*)(uintptr_t)(P_TAB + (t8 & ~63u) + P2 * 16);
            __builtin_amdgcn_sched_barrier(0);
            insert(1);
            hook(3);
            SETP(3);
            asm volatile("s_waitcnt lgkmcnt(0)" ::: "memory");
            __syncthreads();
            tl0 = tl1; tl1 = tl2;
        }
    };
#define HMV(A, B) "v_mov_b64 v[" #A ":" #A "+1], v[" #B ":" #B "+1]\n\t"
#define HMV4(A) HMV(A + 8, A + 6) HMV(A + 6, A + 4) HMV(A + 4, A + 2) HMV(A + 2, A)
    round(IC<1>(), IC<2>(), raw[1], raw[0], 0);
    int rd = 1;
#pragma unroll 1
    for (; tl1 < ntile; rd += 2) {
        round(IC<0>(), IC<0>(), raw[0], raw[1], rd);
        round(IC<1>(), IC<0>(), raw[1], raw[0], rd + 1);
    }
    if (tl0 >= ntile) asm volatile(HMV4(88) HMV4(98) HMV4(108) HMV4(118));
    asm volatile("" : "+s"(tl0));
    if (tl0 < ntile) round(IC<0>(), IC<0>(), raw[0], raw[1], rd);
    RTS(1)
    if (wv == 5) tk = CODE_AHEAD(acc_kernel, gen_scatter_kernel, (t8 >> 3) & 63u);
    round(IC<1>(), IC<1>(), raw[1], raw[0], 0);
    if (__builtin_amdgcn_readfirstlane(lds_ld(P_FLAG + 16 + ((epoch + 2u) & 3u) * 4u)) != 0u) {
        asm volatile("s_waitcnt lgkmcnt(0)" ::: "memory");
        __syncthreads();
        look(*(const LDS u32x2*)(uintptr_t)(P_CNT + t8));
        general(0, true, fm);
        __syncthreads();
        drain();
    }
    {
        uint32_t* tcnt = meta + W_TCNT / 4;
        const u32x2 c = *(const LDS u32x2*)(uintptr_t)(P_CNT + t8);
        const uint32_t tw = tab_at();
#pragma unroll
        for (int e = 0; e < 2; e++) {
            const uint32_t g = U[e] & 15u;
            lds_st(tw + e * 64u, g >= 8u ? 0x80000000u : ((U[e] + ((g & 1u) ? 0u : SK * 64u)) & 0x7fffff81u) | (((7u - g) << 1) & 12u));
            *(LDS uint8_t*)(uintptr_t)(P_CNT + (t8 & ~511u) + (((tw - P_TAB) >> 2) & 127u) + e * 16u) =
                (uint8_t)(g < 8u ? 0u : ((g - 8u) >> 1) + (((c[e] + 2u * CSTEP) >> 15) << 2) + (g & 1u) * 0x50u);
        }
        asm volatile("s_waitcnt lgkmcnt(0)" ::: "memory");
        __syncthreads();
        const uint32_t u16 = (t8 + t8) & 0x70u;
        u32x4 En[4];
#pragma unroll
        for (int p = 0; p < 4; p++) En[p] = *(const LDS u32x4*)(uintptr_t)(P_TAB + (t8 & ~63u) + p * 16u);
        const u32x4 cb = *(const LDS u32x4*)(uintptr_t)(P_CNT + (t8 & ~511u) + ((t8 >> 2) & 0x70u));
        u32x2 N[16];
#pragma unroll
        for (int s = 0; s < 16; s++) N[s] = *(const LDS u32x2*)(uintptr_t)(la + s * 256u);
        if (u16 == 0u) *(u32x4*)(tcnt + blockIdx.x * 512u + (t8 >> 4)) = cb;
        const uint32_t sg = P_BUF + ((t8 & ~63u) << 4);
        uint32_t ps;
        auto rest = [&](auto SL) __attribute__((always_inline)) {
            constexpr int s = decltype(SL)::value;
            const uint32_t en = En[s >> 2][s & 3], by = cb[s >> 2] >> ((s & 3) * 8);
            const u32x2 Hr = hget<HREG(s, 0)>();
            const uint32_t at = ps + ((la & 0x38u) ^ ((s & 3) << 4));
            *(LDS u32x2*)(uintptr_t)at = Hr;
            *(LDS u32x2*)(uintptr_t)(at + (by & 0x40u)) = N[s];
            ps += (by & 0x3cu) << 2;
            const u32x2 Hs = (en & 1u) ? Hr : nul;
            const u32x4 d = {Hs.x, Hs.y, N[s].x, N[s].y};
            uint32_t a = __builtin_amdgcn_bitop3_b32(en, u16, 15u, 0x54);
            {
                const u32x4 rs_ = rsA;
                const uint32_t far_ = ((NEARM >> (2 * s)) & 1u) ? far : 0u;
                asm volatile("s_cmp_eq_u32 %3, 0\n\ts_cbranch_scc0 1f\n\tbuffer_store_dwordx4 %0, %1, %2, 0 offen sc1\n\ts_branch 2f\n"
                             "1:\n\tbuffer_store_dwordx4 %0, %1, %2, 0 offen sc1 nt\n2:\n\ts_nop 1" :: "v"(d), "v"(a), "s"(rs_), "s"(far_) : "memory", "scc");
            }
            if (__builtin_expect(__builtin_amdgcn_ballot_w64((en & 12u) != 0u) != 0ull, 0)) {
                const u32x4 dn = {nul.x, nul.y, nul.x, nul.y};
#pragma unroll 1
                for (uint32_t k = (en >> 2) & 3u; k > 0u; k--) {
                    a += SK * 128u;
                    __builtin_amdgcn_raw_buffer_store_b128(dn, rsR, a, 0, AUX_SC1);
                }
            }
        };
        auto pack = [&](int p) __attribute__((always_inline)) {
            const uint32_t a = (SBASE + ((((t8 >> 1) & 0x60u) + (t8 >> 8)) * 2u + p) * NWG + blockIdx.x) * 128u + u16;
            asm volatile("" ::: "memory");
#pragma unroll 1
            for (uint32_t q = 0; __builtin_amdgcn_ballot_w64(sg + q < ps) != 0ull; q += 256u) {
                const u32x4 d0 = *(const LDS u32x4*)(uintptr_t)(sg + q + u16), d1 = *(const LDS u32x4*)(uintptr_t)(sg + q + 128u + u16);
                __builtin_amdgcn_raw_buffer_store_b128(d0, rsR, sg + q < ps ? a + q * SPL : 0xffffff00u, 0, AUX_SC1);
                __builtin_amdgcn_raw_buffer_store_b128(d1, rsR, sg + q + 128u < ps ? a + (q + 128u) * SPL : 0xffffff00u, 0, AUX_SC1);
            }
            asm volatile("" ::: "memory");
        };
        asm volatile("" : "+v"(N[0]), "+v"(N[1]), "+v"(N[2]), "+v"(N[3]), "+v"(N[4]), "+v"(N[5]), "+v"(N[6]), "+v"(N[7]) :: "memory");
        asm volatile("" : "+v"(N[8]), "+v"(N[9]), "+v"(N[10]), "+v"(N[11]), "+v"(N[12]), "+v"(N[13]), "+v"(N[14]), "+v"(N[15]) :: "memory");
        ps = sg;
        rest(IC<0>()); rest(IC<1>()); rest(IC<2>()); rest(IC<3>()); rest(IC<4>()); rest(IC<5>()); rest(IC<6>()); rest(IC<7>());
        pack(0);
        ps = sg;
        rest(IC<8>()); rest(IC<9>()); rest(IC<10>()); rest(IC<11>()); rest(IC<12>()); rest(IC<13>()); rest(IC<14>()); rest(IC<15>());
        pack(1);
    }
    RTS(2)
    asm volatile("" :: "v"(tk));
    if (blockIdx.x == 0 && t8 == 1536u) {
        do {
            beat();
            __builtin_amdgcn_s_sleep(8);
        } while (peek(vconst<M_MD * 4>()) < NWG);
    }
    RTS(3)
    RTS(4)
}

#define A_MD 0
#define A_ACC (SLOTI * 4)
#define A_FLAG (A_ACC + SLOTO * 4)
#define A_LDS (A_FLAG + 512)
static_assert(SLOTI * 4 == 3 * NT * 16 + 1024, "md staging assumes three full vectors and 256 dwords");
#define BIAS 0x40004000u
#define NFAST 8
#define NLD 10
#define MDF 0x0a11333
#define MDP 0xaaaaa
#define HP 2
#define SHARE (SLOTO / 8)
#define SPINCAP 4096u
#define ZSIG 9.0f
#define HSLOW 0x80008000u
#define GAS __attribute__((address_space(1)))

extern "C" __global__ __launch_bounds__(NT, 4) __attribute__((amdgpu_num_vgpr(53))) void acc_kernel(char* ws, int T, int E, int ks, int lcap, float zf, int pad,
                                                 const uint16_t* __restrict__ centers, const uint16_t* __restrict__ wrbf,
                                                 const uint16_t* __restrict__ wsbf, const uint16_t* __restrict__ wup,
                                                 const u32x2* __restrict__ m, const uint16_t* __restrict__ dist, u32x2* __restrict__ out,
                                                 const int* __restrict__ iin, const int* __restrict__ iout, const uint16_t* __restrict__ ang,
                                                 const uint16_t* __restrict__ wdown, const uint16_t* __restrict__ freq) {
    const char* recs = ws + W_RECS;
    const u32x4* tcnt = (const u32x4*)(ws + W_TCNT);
    const uint32_t* mdc = (const uint32_t*)(ws + W_MDC);
    const u32x4* stat = (const u32x4*)(ws + W_STAT);
    uint32_t* partial = (uint32_t*)(ws + W_RECS + (((size_t)LBASE * 128u + (size_t)lcap * 130u + 255u) & ~(size_t)255u));
    unsigned* meta = (unsigned*)ws;
    extern __shared__ __attribute__((aligned(16))) char smem[];
    unsigned long long tf_; asm volatile("s_memrealtime %0\n\ts_waitcnt lgkmcnt(0)" : "=s"(tf_));
    unsigned leak_ = (unsigned)tf_;
    unsigned* acc = (unsigned*)(smem + A_ACC);
    unsigned* s_flag = (unsigned*)(smem + A_FLAG);
    const int tid = threadIdx.x;
    const int lane = tid & 63;
    const int wv = __builtin_amdgcn_readfirstlane(tid >> 6);
    const int cp = (blockIdx.x >> 3) & 1;
    const int h = blockIdx.x & 3;
    const int j = ((blockIdx.x >> 2) & 1) * 16 + (blockIdx.x >> 4);
    u32x4 tc;
    const __amdgpu_buffer_rsrc_t rsM = __builtin_amdgcn_make_buffer_rsrc((void*)mdc, 0, (int)(128u * SLOTI * 4u), 0x00020000);

    u32x4 pm[4];
    u32x4 ring[NLD];
    auto load_md = [&](int ff, int pl, int mdp) __attribute__((always_inline)) {
        const uint32_t so = (uint32_t)(cp * 64 + ff * 4 + h) * (SLOTI * 4u);
#pragma unroll
        for (int v = 0; v < 3; v++)
            if (pl < 0 || pl == ((mdp >> (4 * v)) & 15)) pm[v] = __builtin_amdgcn_raw_buffer_load_b128(rsM, (uint32_t)tid * 16u, so + (uint32_t)v * (NT * 16u), 0);
        if ((pl < 0 || pl == (mdp & 15)) && wv == 0) pm[3] = __builtin_amdgcn_raw_buffer_load_b128(rsM, (uint32_t)tid * 16u, so + 3u * NT * 16u, 0);
        __builtin_amdgcn_sched_barrier(0);
    };
    const uint32_t lf = (uint32_t)(tid >> 9) * (SKH * 128u) + (uint32_t)(tid & 0x1e0) * 16u + (uint32_t)(tid & 12) * 32u + (uint32_t)(tid & 16) * 4u + (uint32_t)(tid & 3) * 16u;
    const uint32_t lp = (uint32_t)(tid >> 8) * (SKM * 128u) + (uint32_t)((tid >> 2) & 63) * 128u + (uint32_t)(tid & 3) * 16u;
    auto load_main = [&](int i, int ff, bool near = false) __attribute__((always_inline)) -> u32x4 {
        const uint32_t q = (uint32_t)(((i >> 1) - HP) & 3), SK = q ? SKM : SKH, SS = q ? SSM : SSH;
        const __amdgpu_buffer_rsrc_t rs = __builtin_amdgcn_make_buffer_rsrc((void*)(recs + (QAT(q) + (uint32_t)ff * SS + (uint32_t)(h * 32 + j) * SC) * 128u), 0, ks * (int)(SK * 128u), 0x00020000);
        if (q == 0u && near) return __builtin_amdgcn_raw_buffer_load_b128(rs, lf, (i & 1) * (int)(2u * SK * 128u), 0);
        if (q == 0u) return __builtin_amdgcn_raw_buffer_load_b128(rs, lf, (i & 1) * (int)(2u * SK * 128u), AUX_NT);
        return __builtin_amdgcn_raw_buffer_load_b128(rs, lp + (uint32_t)(i & 1) * 64u, 0, 0);
    };
    const __amdgpu_buffer_rsrc_t rsT = __builtin_amdgcn_make_buffer_rsrc((void*)recs, 0, (int)(LBASE * 128u), 0x00020000);
    const uint32_t tof = (uint32_t)(lane & 2) * (TPL * 64u) + (uint32_t)(tid >> 2) * 128u + (uint32_t)(lane & 1) * 64u + 4u + (TBASE + (uint32_t)(h * 32 + j) * TST) * 128u;
    const uint32_t sp0 = (SBASE + (uint32_t)(h * 32 + j) * (2u * NWG) + (uint32_t)(tid >> 2)) * 128u;
    auto tail_rank = [&](uint32_t c, uint32_t sp, int ff, uint32_t t0, int& dst, uint32_t& en) __attribute__((always_inline)) -> int {
        const int k4 = (int)(((uint32_t)(lane & 3) + t0) * 4u) - (int)((c & 3u) * 8u);
        const int left = (int)((c >> 2) & 15u) - k4;
        const bool has = left > 0;
        uint32_t ea = tof + (uint32_t)ff * (128u * TST * 128u), eb = sp + (uint32_t)(k4 * 16);
        eb = sp0 + (uint32_t)(ff >> 3) * (NWG * 128u) + eb + __umul24(eb >> 7, (SPL - 1u) * 128u) + (uint32_t)min(max(left, 0), 4);
        asm volatile("" : "+v"(ea), "+v"(eb));
        en = k4 < 0 ? ea : eb;
        const u64 mask = __builtin_amdgcn_ballot_w64(has);
        const int n = __builtin_popcountll(mask);
        const int rank = __builtin_amdgcn_mbcnt_hi((uint32_t)(mask >> 32), __builtin_amdgcn_mbcnt_lo((uint32_t)mask, 0u));
        const int r = has ? rank : (n + lane - rank);
        dst = ((r & 15) << 4) | ((r >> 4) << 2);
        return n;
    };
    auto load_tail = [&](uint32_t en, u64& tm) __attribute__((always_inline)) -> u32x4 {
        const bool on = (uint32_t)(lane & 3) < (en & 7u);
        tm = __builtin_amdgcn_ballot_w64(on);
        const uint32_t at = (en & 0x70u) + (uint32_t)(lane & 3) * 16u;
        return __builtin_amdgcn_raw_buffer_load_b128(rsT, on ? (en & ~127u) + at + __umul24(at >> 7, (SPL - 1u) * 128u) : 0xffffff00u, 0, 0);
    };
    f32x2 fa[4][4];
    u32x4 em[2];
    u32x2 dw2;
    uint32_t wl;
    const __amdgpu_buffer_rsrc_t rsE = __builtin_amdgcn_make_buffer_rsrc((void*)m, 0, E * 8, 0x00020000);
    const __amdgpu_buffer_rsrc_t rsD = __builtin_amdgcn_make_buffer_rsrc((void*)dist, 0, E * 2, 0x00020000);
    auto acc_vec = [&](int i) __attribute__((always_inline)) -> u32x4 {
        const uint32_t a = A_ACC + (uint32_t)(i >> 4) * 256u + (uint32_t)(i & 15) * 8u;
        const u32x2 x = *(const LDS u32x2*)(uintptr_t)a, y = *(const LDS u32x2*)(uintptr_t)(a + 128u);
        const u32x4 v = {x.x, x.y, y.x, y.y};
        return v;
    };
    auto edge0 = [&](int sh) __attribute__((always_inline)) -> uint32_t {
        return (tid < SHARE / 4) ? (uint32_t)(((sh * (SHARE / 64) + (tid >> 4)) << 11) | (j << 6) | ((tid & 15) * 2)) : 0x0fffff00u;
    };
    auto load_m = [&](int sh) __attribute__((always_inline)) {
        em[0] = __builtin_amdgcn_raw_buffer_load_b128(rsE, edge0(sh) * 8u, 0, AUX_NT);
        em[1] = __builtin_amdgcn_raw_buffer_load_b128(rsE, edge0(sh) * 8u + 256u, 0, AUX_NT);
    };
    auto load_d = [&](int sh) __attribute__((always_inline)) {
        dw2.x = __builtin_amdgcn_raw_buffer_load_b32(rsD, edge0(sh) * 2u, 0, AUX_NT);
        dw2.y = __builtin_amdgcn_raw_buffer_load_b32(rsD, edge0(sh) * 2u + 64u, 0, AUX_NT);
    };
    auto load_wl = [&]() __attribute__((always_inline)) {
        const u64 p0 = (u64)wsbf, p1 = (u64)wrbf, p2 = (u64)centers, p3 = (u64)wup;
        const u64 pa = lane < 16 ? p0 : p1, pb = lane < 40 ? p2 : p3;
        wl = ((const GAS uint32_t*)(lane < 32 ? pa : pb))[lane & (lane < 32 ? 15 : lane < 40 ? 3 : 7)];
    };
    auto factor = [&](int r, bool first) __attribute__((always_inline)) {
        const float sq = 3.7982826f;
        const f32x2 d01 = {bf2f(dw2.x & 0xffffu), bf2f(dw2.x >> 16)}, d23 = {bf2f(dw2.y & 0xffffu), bf2f(dw2.y >> 16)};
        const uint32_t cw = __builtin_amdgcn_readlane(wl, 32 + (r >> 1));
        const float cn = bf2f((r & 1) ? (cw >> 16) : (cw & 0xffffu));
        f32x2 w[4];
#pragma unroll
        for (int k = 0; k < 4; k++) {
            const uint32_t x = __builtin_amdgcn_readlane(wl, (k >> 1) * 16 + r * 2 + (k & 1));
            w[k].x = bf2f(x & 0xffffu); w[k].y = bf2f(x >> 16);
        }
        const f32x2 y01 = (d01 - cn) * sq, y23 = (d23 - cn) * sq;
        const f32x2 t01 = -y01 * y01, t23 = -y23 * y23;
        const float rb[4] = {__builtin_amdgcn_exp2f(t01.x), __builtin_amdgcn_exp2f(t01.y), __builtin_amdgcn_exp2f(t23.x), __builtin_amdgcn_exp2f(t23.y)};
#pragma unroll
        for (int q = 0; q < 4; q++) {
#pragma unroll
            for (int k = 0; k < 4; k++) fa[q][k] = first ? w[k] * rb[q] : __builtin_elementwise_fma(w[k], (f32x2)(rb[q]), fa[q][k]);
        }
    };
    auto edges = [&](int sh) __attribute__((always_inline)) {
        load_d(sh);
        load_m(sh);
        factor(0, true);
#pragma unroll 1
        for (int r = 1; r < 8; r++) factor(r, false);
    };
#define ADT(V, O) "ds_write_addtid_b32 " V " offset:" #O "\n\t"
    auto store_md = [&]() __attribute__((always_inline)) {
        asm volatile("s_mov_b32 m0, %12\n\ts_nop 0\n\t" ADT("%0", 0) ADT("%1", 256) ADT("%2", 512) ADT("%3", 768) ADT("%4", 16384) ADT("%5", 16640) ADT("%6", 16896) ADT("%7", 17152)
                     ADT("%8", 32768) ADT("%9", 33024) ADT("%10", 33280) ADT("%11", 33536)
                     :: "v"(pm[0][0]), "v"(pm[0][1]), "v"(pm[0][2]), "v"(pm[0][3]), "v"(pm[1][0]), "v"(pm[1][1]), "v"(pm[1][2]), "v"(pm[1][3]),
                        "v"(pm[2][0]), "v"(pm[2][1]), "v"(pm[2][2]), "v"(pm[2][3]), "s"(wv * 1024) : "memory");
        if (wv == 0) asm volatile("s_mov_b32 m0, 0\n\ts_nop 0\n\t" ADT("%0", 49152) ADT("%1", 49408) ADT("%2", 49664) ADT("%3", 49920)
                                  :: "v"(pm[3][0]), "v"(pm[3][1]), "v"(pm[3][2]), "v"(pm[3][3]) : "memory");
    };
    const int me = h * 2 + cp;
    GAS unsigned* dw = (GAS unsigned*)(meta + M_DONE + j * 64);
    const bool fin = wv < (SHARE / 4 + 63) / 64;
    bool fast, more;
    uint32_t need, mg0, mg1;
    uint32_t guard = 0;
    uint32_t sgn = 0x8000u;
    pin(sgn);

#define GAT3(M) { uint32_t a0_, a1_, a2_; asm volatile( \
        "v_lshlrev_b16 %0, 2, %3\n\tv_lshlrev_b16 %1, 2, %4\n\tv_lshlrev_b16 %2, 2, %5\n\t" \
        "ds_read_b32 v[%6], %0\n\tds_read_b32 v[%6+1], %1\n\tds_read_b32 v[%6+2], %2" \
        : "=&v"(a0_), "=&v"(a1_), "=&v"(a2_) : "v"(w0), "v"(w1), "v"(w2), "n"(M) : "memory"); }
#define MIXL " op_sel_hi:[1,1,0]\n\t"
#define MIXH " op_sel:[0,1,0] op_sel_hi:[1,1,0]\n\t"
#define SDW " dst_sel:DWORD dst_unused:UNUSED_PAD src0_sel:DWORD src1_sel:WORD_1\n\t"
#define FOLD "v_bitop3_b32 %6, %6, v[%15-9], v[%15-8] bitop3:0xfe\n\tv_or_b32 %6, v[%15-7], %6\n\t"
#define SCAT(PRE, POST, M) { uint32_t c0_, c1_, c2_, a0_, a1_, a2_; asm volatile( \
        FOLD "v_and_b32 %0, 0x3ff, %7\n\tv_lshrrev_b32 %2, 22, %7\n\t" PRE \
        "v_bitop3_b32 %0, %11, %8, %0 bitop3:0xea\n\tv_bitop3_b32 %2, %11, %10, %2 bitop3:0xea\n\tv_bfe_u32 %1, %7, 11, 10\n\t" \
        "v_fma_mix_f32 %3, %0, v[%15], %12" MIXL "v_bitop3_b32 %1, %11, %9, %1 bitop3:0xea\n\tv_fma_mix_f32 %0, %0, v[%15], %13" MIXH \
        "v_fma_mix_f32 %4, %1, v[%15+1], %12" MIXL "v_lshl_add_u32 %0, %0, 16, %3\n\tv_lshlrev_b32_sdwa %3, 2, %8" SDW \
        "v_fma_mix_f32 %1, %1, v[%15+1], %13" MIXH "ds_add_rtn_u32 v[%15-9], %3, %0 offset:%14\n\t" \
        "v_fma_mix_f32 %5, %2, v[%15+2], %12" MIXL "v_lshl_add_u32 %1, %1, 16, %4\n\tv_lshlrev_b32_sdwa %4, 2, %9" SDW \
        "v_fma_mix_f32 %2, %2, v[%15+2], %13" MIXH "ds_add_rtn_u32 v[%15-8], %4, %1 offset:%14\n\t" \
        "v_lshl_add_u32 %2, %2, 16, %5\n\tv_lshlrev_b32_sdwa %5, 2, %10" SDW \
        "ds_add_rtn_u32 v[%15-7], %5, %2 offset:%14" POST \
        : "=&v"(c0_), "=&v"(c1_), "=&v"(c2_), "=&v"(a0_), "=&v"(a1_), "=&v"(a2_), "+v"(guard) \
        : "v"(xw), "v"(w0), "v"(w1), "v"(w2), "v"(sgn), "s"(mg0), "s"(mg1), "n"(A_ACC), "n"(M), "s"(tm) : "memory"); }
    asm volatile("v_mov_b32 v110, 0\n\tv_mov_b32 v111, 0\n\tv_mov_b32 v112, 0\n\tv_mov_b32 v113, 0\n\tv_mov_b32 v114, 0\n\tv_mov_b32 v115, 0\n\t"
                 "v_mov_b32 v116, 0\n\tv_mov_b32 v117, 0\n\tv_mov_b32 v118, 0" ::: "v106", "v127");
    auto unmix = [&](int i) __attribute__((always_inline)) {
        const auto s0 = __builtin_amdgcn_permlane16_swap(ring[i][0], ring[i][2], false, false);
        const auto s1 = __builtin_amdgcn_permlane16_swap(ring[i][1], ring[i][3], false, false);
        ring[i][0] = s0[0]; ring[i][2] = s0[1]; ring[i][1] = s1[0]; ring[i][3] = s1[1];
    };
    auto word = [&](int i, int k) __attribute__((always_inline)) -> uint32_t {
        return i < NFAST && (i >> 1) != HP ? ring[(i & ~1) | (k >> 1)][(i & 1) * 2 + (k & 1)] : ring[i][k];
    };
    auto gather = [&](int R, int i, int p) __attribute__((always_inline)) {
        if ((i >> 1) == HP) unmix(i);
        const uint32_t w0 = word(i, 0 ^ R), w1 = word(i, 1 ^ R), w2 = word(i, 2 ^ R);
        if (p == 0) GAT3(119) else if (p == 1) GAT3(122) else GAT3(125)
    };
    auto scatter = [&](int R, int i, int p, u64 tm) __attribute__((always_inline)) {
        const uint32_t xw = word(i, 3 ^ R), w0 = word(i, 0 ^ R), w1 = word(i, 1 ^ R), w2 = word(i, 2 ^ R);
        if (i < NFAST) { if (p == 0) SCAT("", "", 119) else if (p == 1) SCAT("", "", 122) else SCAT("", "", 125) }
        else if (p == 0) SCAT("s_mov_b64 exec, %16\n\t", "\n\ts_mov_b64 exec, -1", 119)
        else SCAT("s_mov_b64 exec, %16\n\t", "\n\ts_mov_b64 exec, -1", 125)
    };
#define WAIT_L(N) asm volatile("s_waitcnt lgkmcnt(" #N ")" ::: "memory")
    int ntn;
    u64 tm0, tm1;
    uint32_t ta[2], sp = 0u;
    auto list1 = [&](int ff) __attribute__((always_inline)) {
        int dst;
        uint32_t en;
        ntn = tail_rank(tc.x, sp, ff, 0u, dst, en);
        asm volatile("ds_permute_b32 v106, %0, %1" :: "v"(dst), "v"(en) : "memory");
        if (__builtin_expect(__builtin_amdgcn_ballot_w64((tc.x & 2u) != 0u) != 0ull, 0)) {
            const int n2 = tail_rank(tc.x, sp, ff, 4u, dst, en);
            if (ntn + n2 <= 32) {
                const int r = ((dst >> 4) | ((dst & 12) << 2)) + ntn;
                const int x = __builtin_amdgcn_ds_permute((en & 7u) ? ((r & 15) << 4) | ((r >> 4) << 2) : 252, (en & 7u) ? (int)en : 0);
                asm volatile("v_cmpx_ne_u32 vcc, 0, %0\n\tv_mov_b32 v106, %0\n\ts_mov_b64 exec, -1\n\ts_nop 4" :: "v"(x) : "vcc");
                ntn += n2;
            } else ntn = 64;
        }
    };
    auto step = [&](auto RR, auto KK, int ff) __attribute__((always_inline)) {
        constexpr int R = decltype(RR)::value;
        constexpr bool first = decltype(KK)::value == 1, last = decltype(KK)::value == 2;
        constexpr int mdp = first ? MDF : MDP;
        gather(R, 0, 0);
        const int ntl = ntn;
        asm volatile("v_mov_b32_dpp %0, v106 quad_perm:[0,0,0,0] row_mask:0xf bank_mask:0xf\n\tv_mov_b32_dpp %1, v106 quad_perm:[1,1,1,1] row_mask:0xf bank_mask:0xf" : "=v"(ta[0]), "=v"(ta[1]));
        auto side = [&](int pl) __attribute__((always_inline)) {
            if (first && pl == ((mdp >> 20) & 15)) { ring[4] = load_main(4, ff, !(NEARM & 1u)); ring[5] = load_main(5, ff, !(NEARM & 1u)); }
            if (first && pl == ((mdp >> 24) & 15)) { ring[6] = load_main(6, ff); ring[7] = load_main(7, ff); }
            if (pl == ((mdp >> 12) & 15)) ring[NFAST] = load_tail(ta[0], tm0);
            if (pl == ((mdp >> 16) & 15)) ring[NFAST + 1] = load_tail(ta[1], tm1);
            __builtin_amdgcn_sched_barrier(0);
            if (!last) load_md(ff + 1, pl, mdp);
            if (!first && !last && pl == 5 && ff == 6) {
                load_d(me);
                load_wl();
                if (tid == NT - 128) (void)__hip_atomic_fetch_add((GAS unsigned*)((GAS char*)dw + vconst<0>()), 0x1000000u << me, __ATOMIC_RELAXED, __HIP_MEMORY_SCOPE_AGENT);
            }
            if (last && pl == 3) load_m(me);
            if (last && pl == 4 && wv == 15) { GAS unsigned* d_ = dw; asm volatile("s_nop 4\n\tglobal_load_dword v108, %0, %1 sc1" :: "v"(vconst<0>()), "s"(d_) : "memory"); }
            if (last && pl < 7 && fin) factor(pl, pl == 0);
            __builtin_amdgcn_sched_barrier(0);
        };
        side(8);
        gather(R, 1, 1);
        side(9);
        const uint32_t tcur = tc.x, spc = sp;
#pragma unroll
        for (int i = 0; i < NFAST - 1; i++) {
            gather(R, i + 2, (i + 2) % 3);
            if (i == 0) side(10);
            if (i == 0) WAIT_L(6); else if (i == 1) WAIT_L(9); else WAIT_L(12);
            scatter(R, i, i % 3, 0ull);
            if (!last && (i & 1)) { ring[i - 1] = load_main(i - 1, ff + 1, first && !(NEARM & 4u)); ring[i] = load_main(i, ff + 1, first && !(NEARM & 4u)); }
            side(i);
            if (i == 3) SETP(2);
            if (i == 4) SETP(1);
            if (i == 5) SETP(0);
            if (i == 0 && !last) {
                tc.x = __builtin_amdgcn_alignbit(tc.y, tc.x, 8u); tc.y = __builtin_amdgcn_alignbit(tc.z, tc.y, 8u);
                tc.z = __builtin_amdgcn_alignbit(tc.w, tc.z, 8u); tc.w >>= 8;
                sp = ff == 7 ? 0u : sp + ((tcur & 0x3cu) << 2);
                list1(ff + 1);
            }
            __builtin_amdgcn_sched_barrier(0);
        }
        if (last) asm volatile("" :: "v"(ring[NFAST + 1]));
        if (__builtin_expect(ntl > 16, 0)) {
            gather(R, NFAST + 1, 0);
            WAIT_L(0);
            scatter(R, NFAST + 1, 0, tm1);
        }
        if (__builtin_expect(ntl > 32, 0)) {
#pragma unroll 1
            for (int e = 2; e < 6; e++) {
                int dst;
                uint32_t en;
                if (tail_rank(tcur, spc, ff, (uint32_t)e & 4u, dst, en) <= (e & 3) * 16) continue;
                const int cmp = __builtin_amdgcn_ds_permute(dst, (int)en);
                ring[NFAST + 1] = load_tail((uint32_t)__builtin_amdgcn_ds_bpermute(((lane & ~3) | (e & 3)) << 2, cmp), tm1);
                gather(R, NFAST + 1, 0);
                WAIT_L(0);
                scatter(R, NFAST + 1, 0, tm1);
            }
        }
        SETP(3);
        WAIT_L(3);
        if (!last) {
            __builtin_amdgcn_s_barrier();
            store_md();
            SETP(0);
        }
        scatter(R, NFAST - 1, 1, 0ull);
        if (!last) { ring[6] = load_main(6, ff + 1); ring[7] = load_main(7, ff + 1); }
        scatter(R, NFAST, 2, tm0);
        if (!last) {
            WAIT_L(6);
            __builtin_amdgcn_s_barrier();
            SETP(3);
        }
    };
    auto steps = [&](auto RR) __attribute__((always_inline)) {
        tc = tcnt[(wv * 16 + (lane >> 2)) * 128 + j * 4 + h];
        u32x4 sv[2];
        float4 su[3];
        uint32_t e0;
        if (wv < 4) {
            sv[0] = (u32x4)(0u); sv[1] = sv[0];
            su[0] = make_float4(0.f, 0.f, 0.f, 0.f);
            if (lane < SLOTI / 256) { sv[0] = stat[tid * 2]; sv[1] = stat[tid * 2 + 1]; su[0] = ((const float4*)(ws + W_SUM))[tid]; }
            su[1] = ((const float4*)(ws + W_SUM))[256 + lane];
            su[2] = ((const float4*)(ws + W_SUM))[320 + lane];
            e0 = meta[M_E0 + wv];
        }
        const u32x2 extra = *(const u32x2*)(meta + M_LOG);
        load_md(0, -1, 0);
        {
            const u32x4 bi = {BIAS, BIAS, BIAS, BIAS};
#pragma unroll
            for (int k = 0; k < 6; k++) *(LDS u32x4*)(uintptr_t)(A_ACC + (k * NT + threadIdx.x) * 16) = bi;
            if (threadIdx.x < 128) *(LDS u32x4*)(uintptr_t)(A_ACC + (6 * NT + threadIdx.x) * 16) = bi;
            __builtin_amdgcn_sched_barrier(0);
        }
#pragma unroll
        for (int i = 0; i < 2; i++) ring[i] = load_main(i, 0);
        __builtin_amdgcn_sched_barrier(0);
        list1(0);
        if (wv < 4) {
            const uint32_t mxb = wave_max(max(max(sv[0].x & 0xffffu, sv[0].z & 0xffffu), max(sv[1].x & 0xffffu, sv[1].z & 0xffffu)));
            const uint32_t mnb = 0xffffu - (wave_max(max(max(sv[0].x, sv[0].z), max(sv[1].x, sv[1].z))) >> 16);
            const float s2 = wave_sum((__uint_as_float(sv[0].y) + __uint_as_float(sv[0].w)) + (__uint_as_float(sv[1].y) + __uint_as_float(sv[1].w)));
            const float s1 = wave_sum((su[0].x + su[0].y) + (su[0].z + su[0].w));
            const float c1 = wave_sum((su[1].x + su[1].y) + (su[1].z + su[1].w)) * (1.0f / (NWG * 512)), c2 = wave_sum((su[2].x + su[2].y) + (su[2].z + su[2].w)) * (1.0f / (NWG * 512));
            const float rng = fmaxf(fabsf(zf * s1 * c1) + ZSIG * sqrtf(zf * s2 * c2), 2.0f * bf2f(mxb)) * 6.103515625e-05f;
            const uint32_t eb = mxb ? (__float_as_uint(rng) + 0x7fffffu) >> 23 : 127u;
            const bool ok = !mxb || (mxb < 0x4700u && eb >= 107u && eb <= 140u && mnb >= ((eb + 8u) << 7) && c2 >= 0.015625f);
            if (lane == 0) {
                s_flag[8 + wv] = (eb & 0xffu) | (ok ? 0x100u : 0u) | (mxb ? 0x200u : 0u);
                s_flag[12 + wv] = __float_as_uint(__uint_as_float(((eb + e0) & 0xffu) << 23) * (1024.0f / 1023.0f));
                s_flag[wv] = 0u;
            }
        }
        store_md();
#pragma unroll
        for (int i = 2; i < 4; i++) ring[i] = load_main(i, 0);
        WAIT_L(0);
        __syncthreads();
        const uint32_t q0 = __builtin_amdgcn_readfirstlane(s_flag[8 + 2 * cp]), q1 = __builtin_amdgcn_readfirstlane(s_flag[9 + 2 * cp]);
        fast = (q0 & q1 & 0x100u) != 0u;
        need = (q0 >> 9) | ((q1 >> 9) << 1);
        more = (__builtin_amdgcn_readfirstlane(extra.x | extra.y) | (uint32_t)(T & 3)) != 0u;
        mg0 = (((q0 & 0xffu) + 9u) << 23) | 0x400000u;
        mg1 = ((((q1 & 0xffu) + 9u) << 23) | 0x400000u) + ((0u - (mg0 >> 16)) & 0xffffu);
        if (!fast) { __builtin_amdgcn_s_waitcnt(0x0f70); return; }
        RTS(9)
        step(RR, IC<1>(), 0);
#pragma unroll 1
        for (int ff = 1; ff < 15; ff++) step(RR, IC<0>(), ff);
        step(RR, IC<2>(), 15);
        if (fin) factor(7, false);
    };
    auto irregular = [&](auto f) __attribute__((always_inline)) {
        const float fr = bf2f(freq[0]);
        const char* lg = recs + (size_t)LBASE * 128u;
        const uint16_t* tags = (const uint16_t*)(lg + (size_t)lcap * 128u);
        const uint32_t nl = meta[M_LOG], ns = meta[M_LOG + 1];
#pragma unroll 1
        for (uint32_t q = tid; q < nl * 8u; q += NT) {
            const uint32_t b = tags[q >> 3];
            if ((b & 127u) == (uint32_t)(h * 32 + j)) {
                const uint32_t* un = (const uint32_t*)(lg + (size_t)q * 16u);
                const uint32_t xw = un[3 ^ h];
#pragma unroll 1
                for (int k = 0; k < 3; k++) {
                    const uint32_t ww = un[k ^ h];
                    const float c = (float)((xw >> (k * 11)) & 0x3ffu) * (1.0f / 1023.0f);
                    if (!(ww & WNULL)) f(ww >> 16, ((ww & 0x3fffu) << 6) | (b >> 5), (ww & 0x8000u) ? -c : c);
                }
            }
        }
#pragma unroll 1
        for (uint32_t q = tid; q < ns + (uint32_t)(T & 3); q += NT) {
            const uint32_t t = q < ns ? ((const uint32_t*)tags)[-1 - (int)q] : (uint32_t)(T & ~3) + (q - ns);
            const uint32_t i = (uint32_t)iin[t], o = (uint32_t)iout[t];
            if (((o >> 6) & 31u) == (uint32_t)j && (i & 3u) == (uint32_t)h) f(((o >> 11) << 6) | (o & 63u), i, cosf(fr * bf2f(ang[t])));
        }
        __builtin_amdgcn_s_waitcnt(0x0f70);
    };

    if (h == 0) steps(IC<0>());
    else if (h == 1) steps(IC<1>());
    else if (h == 2) steps(IC<2>());
    else steps(IC<3>());
    if (fast) {
        if (more) irregular([&](uint32_t ol, uint32_t i, float cf) __attribute__((always_inline)) {
            const uint32_t md = mdc[(cp * 64u + (i & 63u)) * SLOTI + ((i >> 14) << 8) + ((i >> 4) & 0xfcu) + ((i >> 12) & 3u)];
            const float c = cf * (1023.0f / 16777216.0f);
            const uint32_t lo = __float_as_uint(__builtin_fmaf(c, h2f(md & 0xffffu), __uint_as_float(mg0)));
            const uint32_t hi = __float_as_uint(__builtin_fmaf(c, h2f(md >> 16), __uint_as_float(mg1)));
            if (cf == cf) guard |= atomicAdd(&acc[ol], (hi << 16) + lo);
            else guard |= HSLOW;
        });
    }
    volatile LDS unsigned* seen = (volatile LDS unsigned*)(uintptr_t)(A_FLAG + 4);
    if (fast && wv == 15) asm volatile("s_waitcnt vmcnt(0)\n\tds_write_b32 %0, v108 offset:16\n\tv_and_b32 v109, 0xff00ff, v108\n\tds_write_b32 %0, v109" :: "v"(vconst<A_FLAG + 4>()) : "memory");
    WAIT_L(0);
    asm volatile("v_or3_b32 %0, %0, v110, v111\n\tv_or3_b32 %0, %0, v112, v113\n\tv_or3_b32 %0, %0, v114, v115\n\tv_or3_b32 %0, %0, v116, v117\n\tv_or_b32 %0, v118, %0" : "+v"(guard));
    __syncthreads();
    leak_ = ((unsigned)__builtin_amdgcn_s_memrealtime() - leak_ - 5000u) * 10u;
    const __amdgpu_buffer_rsrc_t rsP = __builtin_amdgcn_make_buffer_rsrc((void*)(partial + (size_t)j * 16 * SLOTO), 0, 16 * SLOTO * 4, 0x00020000);
    const char* pk = recs + (uint32_t)j * (SC * 128u);
    const __amdgpu_buffer_rsrc_t rsK = __builtin_amdgcn_make_buffer_rsrc((void*)pk, 0, (int)(MAINL * 128u), 0x00020000);
#define PKA (SSM * 128u)
#define PKB (32u * SC * 128u)
    auto pk_at = [&](uint32_t i) __attribute__((always_inline)) -> uint32_t {
        const uint32_t c = i >> 9;
        return (c < 12u ? (c >> 2) * SQ + (c & 3u) * SKM : 2u * SSM) * 128u + (i & 511u) * 16u;
    };
    u32x4 p[8];
    unsigned have = 0u;
    auto fetch = [&](unsigned v, int sh) __attribute__((always_inline)) {
        const uint32_t po = tid < SHARE / 4 ? pk_at((uint32_t)(sh * (SHARE / 4) + tid)) : 0x7fffff00u;
        const unsigned nw = v & ~have;
#pragma unroll
        for (int r = 0; r < 8; r++) {
            const uint32_t k = (uint32_t)(me ^ r);
            if ((nw >> k) & 1u) p[r] = __builtin_amdgcn_raw_buffer_load_b128(rsK, po, ((v >> (16u + k)) & 1u) ? MAINL * 128u : (k & 1u) * PKA + (k >> 1) * PKB, AUX_SC1);
        }
        have |= v;
    };
    bool room = false;
    if (fast) {
        room = (__builtin_amdgcn_readfirstlane(s_flag[5]) >> (24 + (me ^ 1))) & 1u;
        const __amdgpu_buffer_rsrc_t rsW = __builtin_amdgcn_make_buffer_rsrc((void*)(pk + (uint32_t)cp * PKA + (uint32_t)h * PKB), 0, room ? (int)(MAINL * 128u) : 0, 0x00020000);
        uint32_t big = 0u;
        u32x4 v[7];
#pragma unroll
        for (int k = 0; k < 6; k++) v[k] = acc_vec(k * NT + tid);
        v[6] = (u32x4)(BIAS);
        if (wv < 2) v[6] = acc_vec(6 * NT + tid);
#pragma unroll
        for (int k = 0; k < 6; k++) __builtin_amdgcn_raw_buffer_store_b128(v[k], rsW, pk_at((uint32_t)tid) + ((k >> 1) * SQ + (k & 1) * 2u * SKM) * 128u, 0, AUX_SC1);
        if (wv < 2) __builtin_amdgcn_raw_buffer_store_b128(v[6], rsW, 2u * SSM * 128u + (uint32_t)tid * 16u, 0, AUX_SC1);
        __builtin_amdgcn_sched_barrier(0);
#pragma unroll
        for (int k = 0; k < 28; k++) { guard |= v[k >> 2][k & 3]; big |= v[k >> 2][k & 3] + 0x42004200u; }
        if (guard & HSLOW) s_flag[0] = 1u;
        if (big & 0x7c00u) s_flag[2] = 1u;
        if (big & 0x7c000000u) s_flag[3] = 2u;
        __builtin_amdgcn_sched_barrier(0);
        p[0] = acc_vec(me * (SHARE / 4) + min(tid, SHARE / 4 - 1));
        have = 1u << me;
        if (fin) fetch(__builtin_amdgcn_readfirstlane(*seen), me);
    }
    asm volatile("s_waitcnt vmcnt(0)" ::: "memory");
    __syncthreads();
    RTS(11)
    const bool redo = !fast || __builtin_amdgcn_readfirstlane(s_flag[0]) != 0u || (need & ~__builtin_amdgcn_readfirstlane(s_flag[2] | s_flag[3])) != 0u;
    const bool slow = redo || !room;
    if (slow) {
        float* fs = (float*)acc;
        float w[16];
        load_w(wdown, w);
        const float fr = bf2f(freq[0]);
#pragma unroll 1
        for (int e = 0; e < 2; e++) {
            if (redo) {
                __syncthreads();
                for (int i = tid; i < SLOTO; i += NT) fs[i] = 0.f;
                __syncthreads();
                const __amdgpu_buffer_rsrc_t rsO = __builtin_amdgcn_make_buffer_rsrc((void*)iout, 0, T * 4, 0x00020000);
                const __amdgpu_buffer_rsrc_t rsI = __builtin_amdgcn_make_buffer_rsrc((void*)iin, 0, T * 4, 0x00020000);
#pragma unroll 1
                for (int t0 = tid * 4; t0 < T; t0 += NT * 16) {
                    u32x4 o4[4], i4[4];
#pragma unroll
                    for (int g = 0; g < 4; g++) {
                        o4[g] = __builtin_amdgcn_raw_buffer_load_b128(rsO, (uint32_t)(t0 + g * NT * 4) * 4u, 0, 0);
                        i4[g] = __builtin_amdgcn_raw_buffer_load_b128(rsI, (uint32_t)(t0 + g * NT * 4) * 4u, 0, 0);
                    }
                    uint32_t hit = 0u;
#pragma unroll
                    for (int k = 0; k < 16; k++) hit |= (bfi(3u, i4[k >> 2][k & 3], o4[k >> 2][k & 3]) & 0x7c3u) == (uint32_t)(j * 64 + h) ? 1u << k : 0u;
#pragma unroll 1
                    for (; hit; hit &= hit - 1u) {
                        const int k = __builtin_ctz(hit), t = t0 + (k >> 2) * NT * 4 + (k & 3);
                        if (t >= T) break;
                        const uint32_t o = (uint32_t)iout[t];
                        uint32_t rr[4];
                        md_of(m[iin[t]], w, rr);
                        atomicAdd(&fs[((o >> 11) << 6) | (o & 63u)], cosf(fr * bf2f(ang[t])) * bf2f(cp ? (e ? rr[3] : rr[2]) : (e ? rr[1] : rr[0])));
                    }
                }
                __syncthreads();
            }
            const float un = __uint_as_float(s_flag[12 + 2 * cp + e]);
            for (int i = tid; i < SLOTO / 4; i += NT) {
                u32x4 v = acc_vec(i);
                if (!redo) {
#pragma unroll
                    for (int k = 0; k < 4; k++) v[k] = __float_as_uint((float)((int)((v[k] >> (16 * e)) & 0xffffu) - 0x4000) * un);
                }
                __builtin_amdgcn_raw_buffer_store_b128(v, rsP, (uint32_t)((e * 8 + me) * SLOTO * 4 + i * 16), 0, AUX_SC1);
            }
        }
        load_wl();
        edges(me);
#pragma unroll
        for (int k = 0; k < 8; k++) p[k] = (u32x4)(0u);
        have = 0u;
        asm volatile("s_waitcnt vmcnt(0)" ::: "memory");
        __syncthreads();
    }
    if (tid == NT - 64) {
        const unsigned inc = (slow ? 0x10001u : 1u) << me;
        unsigned v = __hip_atomic_fetch_add(dw, inc, __ATOMIC_RELAXED, __HIP_MEMORY_SCOPE_AGENT) + inc;
        unsigned quit = 0u;
        if ((v & 0xffu) != 0xffu) {
            unsigned spins = 0;
            do {
                *seen = v & 0xff00ffu;
                __builtin_amdgcn_s_sleep(1);
                v = __hip_atomic_load(dw, __ATOMIC_RELAXED, __HIP_MEMORY_SCOPE_AGENT);
                if (++spins > SPINCAP && (v & 0xffu) != 0xffu) {
                    v = __hip_atomic_fetch_add(dw, 256u << me, __ATOMIC_RELAXED, __HIP_MEMORY_SCOPE_AGENT);
                    if ((v & 0xffu) != 0xffu) { v = 0xffu; quit = 0x80000000u; }
                }
            } while ((v & 0xffu) != 0xffu);
            v &= 0xff00ffu;
        }
        *seen = (v & 0xffffffu) | quit;
    }
    if (!fin) return;
    have = __builtin_amdgcn_readfirstlane(have);
    uint32_t un[4];
#pragma unroll
    for (int c = 0; c < 4; c++) un[c] = __builtin_amdgcn_readfirstlane(s_flag[12 + c]);
    unsigned sv;
    {
        const u32x4 rsQ = {(uint32_t)(uintptr_t)pk, (uint32_t)((uintptr_t)pk >> 32) & 0xffffu, MAINL * 128u, 0x00020000u};
        const uint32_t po = tid < SHARE / 4 ? pk_at((uint32_t)(me * (SHARE / 4) + tid)) : 0x7fffff00u;
        do {
            sv = __builtin_amdgcn_readfirstlane(*seen);
            uint32_t t0, t1, t2;
#define FET(R) "s_xor_b32 %8, %13, " #R "\n\ts_bitcmp1_b32 %14, %8\n\ts_cbranch_scc0 1f\n\ts_and_b32 %9, %8, 1\n\ts_mul_i32 %9, %9, %16\n\ts_lshr_b32 %10, %8, 1\n\t" \
               "s_mul_i32 %10, %10, %17\n\ts_add_u32 %9, %9, %10\n\ts_bitcmp1_b32 %15, %8\n\ts_cselect_b32 %9, %18, %9\n\tbuffer_load_dwordx4 %" #R ", %11, %12, %9 offen sc1\n1:\n\t"
            asm volatile("s_and_b32 %8, %14, 0xff\n\ts_cbranch_scc0 2f\n\ts_nop 2\n\t" FET(0) FET(1) FET(2) FET(3) FET(4) FET(5) FET(6) FET(7) "\n2:\n\ts_sleep 2\n\ts_waitcnt vmcnt(0)"
                         : "+v"(p[0]), "+v"(p[1]), "+v"(p[2]), "+v"(p[3]), "+v"(p[4]), "+v"(p[5]), "+v"(p[6]), "+v"(p[7]), "=&s"(t0), "=&s"(t1), "=&s"(t2)
                         : "v"(po), "s"(rsQ), "s"(me), "s"((int)sv < 0 ? 0u : sv & ~have), "s"(sv >> 16), "n"(PKA), "n"(PKB), "n"(MAINL * 128u) : "memory", "scc");
            have |= sv;
        } while ((sv & 0xffu) != 0xffu);
    }
    if ((int)sv < 0) return;
    RTS(12)
    const unsigned flt = (sv >> 16) & 0xffu;
    auto finish = [&](int sh) __attribute__((always_inline)) {
        {
            const int ol = sh * SHARE + tid * 4;
            const uint32_t po = tid < SHARE / 4 ? (uint32_t)ol * 4u : 0x7fffff00u;
            float g[4][4];
            u32x4 a[2] = {p[0] + p[2], p[1] + p[3]}, b[2] = {p[4] + p[6], p[5] + p[7]};
            if (cp) { const u32x4 t = a[0], u = b[0]; a[0] = a[1]; b[0] = b[1]; a[1] = t; b[1] = u; }
#pragma unroll
            for (int c = 0; c < 2; c++) {
                const int nb = 0x10000 - 0x4000 * __builtin_popcount(flt & (0x55u << c));
                const float u0 = __uint_as_float(nb ? un[2 * c] : 0u), u1 = __uint_as_float(nb ? un[2 * c + 1] : 0u);
#pragma unroll
                for (int q = 0; q < 4; q++) {
                    g[q][2 * c] = (float)((int)((a[c][q] & 0xffffu) + (b[c][q] & 0xffffu)) - nb) * u0;
                    g[q][2 * c + 1] = (float)((int)((a[c][q] >> 16) + (b[c][q] >> 16)) - nb) * u1;
                }
            }
            if (__builtin_expect(flt != 0u, 0)) {
#pragma unroll
                for (int c = 0; c < 2; c++) {
#pragma unroll 1
                    for (int k = c; k < 8; k += 2) {
                        if (!((flt >> k) & 1u)) continue;
                        const u32x4 f0 = __builtin_amdgcn_raw_buffer_load_b128(rsP, po, k * SLOTO * 4, AUX_SC1);
                        const u32x4 f1 = __builtin_amdgcn_raw_buffer_load_b128(rsP, po, (8 + k) * SLOTO * 4, AUX_SC1);
#pragma unroll
                        for (int q = 0; q < 4; q++) { g[q][2 * c] += __uint_as_float(f0[q]); g[q][2 * c + 1] += __uint_as_float(f1[q]); }
                    }
                }
            }
            float wu[16];
#pragma unroll
            for (int k = 0; k < 8; k++) {
                const uint32_t x = __builtin_amdgcn_readlane(wl, 40 + k);
                wu[2 * k] = bf2f(x & 0xffffu); wu[2 * k + 1] = bf2f(x >> 16);
            }
            u32x4 ov[2];
#pragma unroll
            for (int q = 0; q < 4; q++) {
#pragma unroll
                for (int k = 0; k < 4; k++) g[q][k] *= fa[q][k >> 1][k & 1];
                const uint32_t m0 = em[q >> 1][(q & 1) * 2], m1 = em[q >> 1][(q & 1) * 2 + 1];
                const float mm[4] = {bf2f(m0 & 0xffff), bf2f(m0 >> 16), bf2f(m1 & 0xffff), bf2f(m1 >> 16)};
                float o[4];
#pragma unroll
                for (int k = 0; k < 4; k++) o[k] = mm[k] + (g[q][0] * wu[k] + g[q][1] * wu[4 + k] + g[q][2] * wu[8 + k] + g[q][3] * wu[12 + k]) * fa[q][2 + (k >> 1)][k & 1];
                ov[q >> 1][(q & 1) * 2] = pk_bf16(o[0], o[1]);
                ov[q >> 1][(q & 1) * 2 + 1] = pk_bf16(o[2], o[3]);
            }
            const __amdgpu_buffer_rsrc_t rsU = __builtin_amdgcn_make_buffer_rsrc((void*)out, 0, E * 8, 0x00020000);
            const uint32_t uo = edge0(sh) * 8u;
            __builtin_amdgcn_raw_buffer_store_b128(ov[0], rsU, uo, 0, AUX_NT);
            __builtin_amdgcn_raw_buffer_store_b128(ov[1], rsU, uo + 256u, 0, AUX_NT);
        }
    };
    finish(me);
#pragma unroll 1
    for (unsigned todo = (sv >> 8) & 0xffu; todo; todo &= todo - 1u) {
        const int sh = __builtin_ctz(todo);
        edges(sh);
        have = 0u;
        fetch(sv, sh);
        finish(sh);
    }
    if (blockIdx.x == 5) { const unsigned long long t0_ = __builtin_amdgcn_s_memrealtime(); while (__builtin_amdgcn_s_memrealtime() - t0_ < min(leak_, 40000u)) __builtin_amdgcn_s_sleep(2); }
}

extern "C" __global__ __launch_bounds__(256) void gen_scatter_kernel(const int* __restrict__ iin, const int* __restrict__ iout,
                                                          const uint16_t* __restrict__ ang, const uint16_t* __restrict__ freq,
                                                          const u32x2* __restrict__ m, const uint16_t* __restrict__ wdown,
                                                          float* __restrict__ agg, int T) {
    float w[16];
    load_w(wdown, w);
    const float fr = bf2f(freq[0]);
    for (long t = (long)blockIdx.x * 256 + threadIdx.x; t < T; t += (long)gridDim.x * 256) {
        const float c = cosf(fr * bf2f(ang[t]));
        uint32_t r[4];
        md_of(m[iin[t]], w, r);
        float* q = agg + (size_t)iout[t] * 4;
#pragma unroll
        for (int k = 0; k < 4; k++) atomicAdd(q + k, c * bf2f(r[k]));
    }
}
__global__ __launch_bounds__(256) void gen_final_kernel(const u32x2* __restrict__ m, const uint16_t* __restrict__ dist,
                                                        const uint16_t* __restrict__ centers, const uint16_t* __restrict__ wrbf,
                                                        const uint16_t* __restrict__ wsbf, const uint16_t* __restrict__ wup,
                                                        const float* __restrict__ agg, u32x2* __restrict__ out, int E) {
    const int e = blockIdx.x * 256 + threadIdx.x;
    if (e >= E) return;
    const float d = bf2f(dist[e]);
    float rs[4] = {0, 0, 0, 0}, rg[4] = {0, 0, 0, 0};
#pragma unroll 1
    for (int r = 0; r < 8; r++) {
        const float x = d - bf2f(centers[r]);
        const float rb = __expf(-10.0f * x * x);
#pragma unroll
        for (int k = 0; k < 4; k++) { rs[k] += rb * bf2f(wsbf[r * 4 + k]); rg[k] += rb * bf2f(wrbf[r * 4 + k]); }
    }
    float wu[16];
    load_w(wup, wu);
    const u32x2 mv = m[e];
    const float mm[4] = {bf2f(mv.x & 0xffff), bf2f(mv.x >> 16), bf2f(mv.y & 0xffff), bf2f(mv.y >> 16)};
    float g[4], o[4];
#pragma unroll
    for (int k = 0; k < 4; k++) g[k] = agg[(size_t)e * 4 + k] * rs[k];
#pragma unroll
    for (int k = 0; k < 4; k++) o[k] = mm[k] + (g[0] * wu[k] + g[1] * wu[4 + k] + g[2] * wu[8 + k] + g[3] * wu[12 + k]) * rg[k];
    const u32x2 ov = {f2bf(o[0]) | (f2bf(o[1]) << 16), f2bf(o[2]) | (f2bf(o[3]) << 16)};
    out[e] = ov;
}

extern "C" void kernel_launch(void* const* d_in, const int* in_sizes, int n_in,
                              void* d_out, int out_size, void* d_ws, size_t ws_size,
                              hipStream_t stream) {
    const u32x2* m = (const u32x2*)d_in[0];
    const int* iin = (const int*)d_in[1];
    const int* iout = (const int*)d_in[2];
    const uint16_t* dist = (const uint16_t*)d_in[3];
    const uint16_t* ang = (const uint16_t*)d_in[4];
    const uint16_t* centers = (const uint16_t*)d_in[5];
    const uint16_t* freq = (const uint16_t*)d_in[6];
    const uint16_t* wrbf = (const uint16_t*)d_in[7];
    const uint16_t* wsbf = (const uint16_t*)d_in[8];
    const uint16_t* wdown = (const uint16_t*)d_in[9];
    const uint16_t* wup = (const uint16_t*)d_in[10];
    const int E = in_sizes[3];
    const int T = in_sizes[1];
    char* ws = (char*)d_ws;
    const int G = T >> 2;
    const int ntile = (G + GR - 1) / GR;
    const int nstat = ntile / NWG - NDYN > 4 ? ntile / NWG - NDYN : 4;
    const int lcap = G / 6 + 2;
    char* recs = ws + W_RECS;
    size_t off = W_RECS;
    const size_t rbytes = (size_t)LBASE * 128 + (size_t)lcap * 130;
    off += rbytes + 256 + (size_t)32 * 16 * SLOTO * 4;
    if (E > EMAX || (E & 3) || E < 1 || rbytes > 0x70000000u || off > ws_size) {
        (void)hipMemsetAsync(ws, 0, (size_t)E * 16, stream);
        gen_scatter_kernel<<<4096, 256, 0, stream>>>(iin, iout, ang, freq, m, wdown, (float*)ws, T);
        gen_final_kernel<<<(E + 255) / 256, 256, 0, stream>>>(m, dist, centers, wrbf, wsbf, wup, (const float*)ws, (u32x2*)d_out, E);
        return;
    }
    int ks = T / (NWG * NB * 24);
    if (ks > KMAX) ks = KMAX;
    const float zf = (float)T / (4.0f * (float)E * (float)E);
    (void)hipFuncSetAttribute((const void*)part_kernel, hipFuncAttributeMaxDynamicSharedMemorySize, P_LDS);
    (void)hipFuncSetAttribute((const void*)acc_kernel, hipFuncAttributeMaxDynamicSharedMemorySize, A_LDS);
    part_kernel<<<NWG, NT, P_LDS, stream>>>(iin, iout, ang, freq, m, wdown, recs, T, E, ntile, nstat, ks, lcap);
    acc_kernel<<<NWG, NT, A_LDS, stream>>>(ws, T, E, ks, lcap, zf, 0, centers, wrbf, wsbf, wup, m, dist, (u32x2*)d_out, iin, iout, ang, wdown, freq);
}
```

```cpp
#include <hip/hip_runtime.h>
#include <hip/hip_bf16.h>
#include <stdint.h>
#pragma clang diagnostic ignored "-Winline-asm"

typedef unsigned int u32x4 __attribute__((ext_vector_type(4)));
typedef unsigned int u32x2 __attribute__((ext_vector_type(2)));
typedef unsigned short u16x2 __attribute__((ext_vector_type(2)));
typedef __bf16 bf16x2 __attribute__((ext_vector_type(2)));
typedef float f32x2 __attribute__((ext_vector_type(2)));
typedef unsigned long long u64;

static __device__ __forceinline__ float bf2f(uint32_t bits16) { return __uint_as_float(bits16 << 16); }
static __device__ __forceinline__ uint32_t f2bf(float f) {
    uint32_t u = __float_as_uint(f);
    if ((u & 0x7fffffffu) > 0x7f800000u) return (u >> 16) | 0x40;
    return (u + 0x7fffu + ((u >> 16) & 1u)) >> 16;
}
static __device__ __forceinline__ float h2f(uint32_t bits16) { return (float)__builtin_bit_cast(_Float16, (unsigned short)bits16); }

#define NB 2048
#define WNULL 0x80000000u
#define CSTEP 0x2aabu
#define CINIT 205u
#define CHV (12u * CSTEP)
#define SLOTO 25088
#define SLOTI 12544
#define EMAX (SLOTO * 32)
#define NT 1024
#define NWG 256
#define KMAX 4
#define NTAIL 4
#define SC 64u
#define SKM (128u * SC + 2u)
#define SSM (4u * SKM + 1u)
#define NNEAR 2u
#define NEARM (0x55555555u << (2u * NNEAR))
#define SSH (128u * SC + 1u)
#define SKH ((16u + NNEAR) * SSH + 2u - (NNEAR & 1u))
#define SQ (16u * SSM)
#define MAINL (3u * SQ)
#define SBASE MAINL
#define SPL (128u * 2u * NWG + 1u)
#define HBASE (SBASE + 8u * SPL)
#define QAT(q) ((q) ? ((q) - 1u) * SQ : HBASE)
#define BPL(b) (QAT(((b) >> 3) & 3u) + ((b) & 7u) + (((b) >> 5) << 3))
#define TST 257u
#define TBASE (HBASE + 3u * SKH + 16u * SSH)
#define TPL (NB * TST)
#define LBASE (TBASE + 2u * TPL)
#define NDYN 3
#define GR 2048
#define M_DONE 0
#define M_TILE (32 * 64)
#define M_LOG (M_TILE + 64)
#define M_MD (M_LOG + 64)
#define M_BEAT (M_MD + 64)
#define M_E0 (M_BEAT + 64)
#define M_WORDS (M_E0 + 64)
#define W_TCNT (M_WORDS * 4)
#define W_STAT (W_TCNT + NWG * NB)
#define W_SUM (W_STAT + 4 * NWG * 8)
#define W_MDC (W_SUM + 6 * NWG * 4)
#define W_RECS (W_MDC + 128 * SLOTI * 4)
#define AUX_NT 2
#define AUX_SC1 16

static __device__ __forceinline__ uint32_t row_max(uint32_t x) {
    x = max(x, (uint32_t)__builtin_amdgcn_update_dpp(0, x, 0x111, 0xf, 0xf, false));
    x = max(x, (uint32_t)__builtin_amdgcn_update_dpp(0, x, 0x112, 0xf, 0xf, false));
    x = max(x, (uint32_t)__builtin_amdgcn_update_dpp(0, x, 0x114, 0xf, 0xf, false));
    return max(x, (uint32_t)__builtin_amdgcn_update_dpp(0, x, 0x118, 0xf, 0xf, false));
}
static __device__ __forceinline__ uint32_t wave_max(uint32_t x) {
    x = row_max(x);
    x = max(x, (uint32_t)__builtin_amdgcn_update_dpp(0, x, 0x142, 0xa, 0xf, false));
    x = max(x, (uint32_t)__builtin_amdgcn_update_dpp(0, x, 0x143, 0xc, 0xf, false));
    return __builtin_amdgcn_readlane(x, 63);
}
static __device__ __forceinline__ float row_sum(float x) {
    x += __int_as_float(__builtin_amdgcn_update_dpp(0, __float_as_int(x), 0x111, 0xf, 0xf, false));
    x += __int_as_float(__builtin_amdgcn_update_dpp(0, __float_as_int(x), 0x112, 0xf, 0xf, false));
    x += __int_as_float(__builtin_amdgcn_update_dpp(0, __float_as_int(x), 0x114, 0xf, 0xf, false));
    return x + __int_as_float(__builtin_amdgcn_update_dpp(0, __float_as_int(x), 0x118, 0xf, 0xf, false));
}
static __device__ __forceinline__ float wave_sum(float x) {
    x = row_sum(x);
    x += __int_as_float(__builtin_amdgcn_update_dpp(0, __float_as_int(x), 0x142, 0xa, 0xf, false));
    x += __int_as_float(__builtin_amdgcn_update_dpp(0, __float_as_int(x), 0x143, 0xc, 0xf, false));
    return __int_as_float(__builtin_amdgcn_readlane(__float_as_int(x), 63));
}
static __device__ __forceinline__ uint32_t bfi(uint32_t mask, uint32_t a, uint32_t b) {
    return __builtin_amdgcn_bitop3_b32(mask, a, b, 0xca);
}
template <uint32_t C> static __device__ __forceinline__ uint32_t vconst() {
    uint32_t r;
    asm volatile("v_mov_b32 %0, %1" : "=v"(r) : "n"(C));
    return r;
}
static __device__ __forceinline__ uint32_t pk_bf16(float a, float b) {
    const f32x2 v = {a, b};
    return __builtin_bit_cast(uint32_t, __builtin_convertvector(v, bf16x2));
}
static __device__ __forceinline__ void pin(uint32_t& x) { asm volatile("" : "+v"(x)); }


static __device__ __forceinline__ void md_of(u32x2 v, const float* w, uint32_t* r) {
    float a0 = bf2f(v.x & 0xffff), a1 = bf2f(v.x >> 16), a2 = bf2f(v.y & 0xffff), a3 = bf2f(v.y >> 16);
#pragma unroll
    for (int j = 0; j < 4; j++) r[j] = f2bf(a0 * w[j] + a1 * w[4 + j] + a2 * w[8 + j] + a3 * w[12 + j]);
}
static __device__ __forceinline__ void load_w(const uint16_t* __restrict__ wdown, float* w) {
    u32x4 wv0 = *(const u32x4*)wdown, wv1 = *(const u32x4*)(wdown + 8);
#pragma unroll
    for (int i = 0; i < 4; i++) {
        w[2 * i] = bf2f(wv0[i] & 0xffffu); w[2 * i + 1] = bf2f(wv0[i] >> 16);
        w[8 + 2 * i] = bf2f(wv1[i] & 0xffffu); w[8 + 2 * i + 1] = bf2f(wv1[i] >> 16);
    }
}
#define LDS __attribute__((address_space(3)))
#define P_CNT 0
#define P_FLAG ((NB + 64) * 4)
#define P_LST (P_FLAG + 256)
#define LCAP 32
#define P_TAB (P_LST + 4 * LCAP * 16)
#define P_BUF (P_TAB + NB * 4)
#define P_LDS (P_BUF + NB * 64)
#define PD 2
#define LPOS 0x448763
#define SETP(n) __builtin_amdgcn_s_setprio(n)
#define MDL 1
#define MDM 3
#define RMAXREV 40.0f
#define HB 88
#define HREG(S, D) (HB + ((S) & 3) * 10 + (((S) >> 2) + (D)) * 2)

template <int N> struct IC { static constexpr int value = N; };
struct Raw { u32x4 o[2], i[2]; u32x2 a[2]; };
static __device__ __forceinline__ uint32_t lds_ld(uint32_t a) { return *(const LDS uint32_t*)(uintptr_t)a; }
static __device__ __forceinline__ void lds_st(uint32_t a, uint32_t v) { *(LDS uint32_t*)(uintptr_t)a = v; }
static __device__ __forceinline__ void lds_or(uint32_t a, uint32_t v) {
    __hip_atomic_fetch_or((LDS uint32_t*)(uintptr_t)a, v, __ATOMIC_RELAXED, __HIP_MEMORY_SCOPE_WORKGROUP);
}
static __device__ __forceinline__ uint32_t lds_add(uint32_t a, uint32_t v) {
    return __hip_atomic_fetch_add((LDS uint32_t*)(uintptr_t)a, v, __ATOMIC_RELAXED, __HIP_MEMORY_SCOPE_WORKGROUP);
}
static __device__ __forceinline__ uint32_t slot_w(uint32_t ca, uint32_t cv) {
    return (ca << 4) | __builtin_amdgcn_bitop3_b32(cv >> 11, ca, 0x3cu, 0x28);
}
static __device__ __forceinline__ uint32_t slot_x2(uint32_t aw, uint32_t ca) {
    return __builtin_amdgcn_bitop3_b32(aw, ca, 0xcu, 0x72);
}
static __device__ __forceinline__ uint32_t slot_x(uint32_t aw) { return slot_x2(aw, aw >> 4); }
template <int R> static __device__ __forceinline__ u32x2 hget() {
    u32x2 d;
    asm volatile("v_mov_b64 %0, v[%1:%2]" : "=v"(d) : "n"(R), "n"(R + 1));
    return d;
}
static __device__ __forceinline__ u32x2 hget_at(int r) {
    u32x2 d;
    asm volatile("s_set_gpr_idx_on %2, gpr_idx(SRC0)\n\ts_nop 1\n\tv_mov_b32 %0, v88\n\tv_mov_b32 %1, v89\n\ts_set_gpr_idx_off"
                 : "=&v"(d.x), "=&v"(d.y) : "s"(r));
    return d;
}
static __device__ __forceinline__ void hset_at(int r, u32x2 d) {
    asm volatile("s_set_gpr_idx_on %2, gpr_idx(DST)\n\ts_nop 1\n\tv_mov_b32 v88, %0\n\tv_mov_b32 v89, %1\n\ts_set_gpr_idx_off"
                 :: "v"(d.x), "v"(d.y), "s"(r));
}

template <int S, int D> static __device__ __forceinline__ uint32_t take(uint32_t V, uint32_t u16, uint32_t la, u32x2 nul) {
    const uint32_t a = __builtin_amdgcn_bitop3_b32(V, u16, 15u, 0x54);
    asm volatile("v_mov_b64 v[%4:%4+1], v[%3:%3+1]\n\tv_cmpx_ne_u32 vcc, -16, %0\n\tds_wrxchg_rtn_b64 v[%4:%4+1], %1, %2 offset:%5\n\ts_mov_b64 exec, -1"
                 :: "v"(a), "v"(la), "v"(nul), "n"(HREG(S, D)), "n"(HREG(S, 1 - D)), "n"(S * 256) : "memory", "vcc");
    return a;
}
#define PUT(P) asm volatile("s_bitcmp1_b32 %3, %4\n\ts_cbranch_scc0 1f\n\ts_waitcnt lgkmcnt(%5)\n\tbuffer_store_dwordx4 v[%2:%2+3], %0, %1, 0 offen sc1\n" \
                 "1:\n\ts_bitcmp1_b32 %3, %4+1\n\ts_cbranch_scc0 2f\n\ts_waitcnt lgkmcnt(%5)\n\tbuffer_store_dwordx4 v[%2:%2+3], %0, %1, 0 offen " P "\n\ts_nop 1\n2:" \
                 :: "v"(ad), "s"(rsA), "n"(HREG(S, 0)), "s"(pm), "n"(2 * S), "n"(W) : "memory", "scc")
template <int S, int W, bool WT> static __device__ __forceinline__ void put(u32x4 rsA, uint32_t ad, uint32_t pm) {
    if (WT) PUT("sc1 nt"); else PUT("nt");
}

#define CODE_AHEAD(SYM, NEXT, LANE) ({ u64 ca_; int cb_; \
    asm volatile("s_getpc_b64 vcc\n\ts_add_u32 vcc_lo, vcc_lo, " #SYM "@rel32@lo+4\n\ts_addc_u32 vcc_hi, vcc_hi, " #SYM "@rel32@hi+12\n\t" \
                 "s_mov_b64 %0, vcc\n\ts_max_i32 %1, " #NEXT "-" #SYM ", 0" : "=s"(ca_), "=s"(cb_) :: "scc", "vcc"); \
    __builtin_amdgcn_raw_buffer_load_b32(__builtin_amdgcn_make_buffer_rsrc((void*)ca_, 0, cb_, 0x00020000), ((blockIdx.x >> 3) * 64u + (LANE)) * 64u, 0, 0); })
extern "C" __global__ __launch_bounds__(NT, 4) __attribute__((amdgpu_num_vgpr(HB / 2))) void part_kernel(const int* __restrict__ iin, const int* __restrict__ iout,
                                                  const uint16_t* __restrict__ ang, const uint16_t* __restrict__ freq,
                                                  const u32x2* __restrict__ m, const uint16_t* __restrict__ wdown,
                                                  char* __restrict__ recs,
                                                  int T, int E, int ntile, int nstat, int ks, int lcap) {
    asm volatile("" :: "s"(iin), "s"(iout), "s"(ang), "s"(freq), "s"(m), "s"(wdown), "s"(recs), "s"(T), "s"(E), "s"(ntile), "s"(nstat), "s"(ks), "s"(lcap));
    const int tid0 = threadIdx.x;
    const int wv = __builtin_amdgcn_readfirstlane(tid0 >> 6);
    const int G = T >> 2;
    const int pos = (int)((blockIdx.x & 7) << 5) | (int)(blockIdx.x >> 3);
    float frr;
    unsigned* meta = (unsigned*)(recs - W_RECS);
    const uint32_t gbase = (LBASE + (uint32_t)lcap) * 128u;
    const uint32_t rbytes = gbase + (uint32_t)lcap * 2u;
    const __amdgpu_buffer_rsrc_t rsR = __builtin_amdgcn_make_buffer_rsrc(recs, 0, (int)rbytes, 0x00020000);
    const u32x4 rsA = {(uint32_t)(uintptr_t)recs, (uint32_t)((uintptr_t)recs >> 32) & 0xffffu, rbytes, 0x00020000u};
    const uint32_t g2 = 2u * (uint32_t)ks;
    const uint32_t far = 1u >> ((blockIdx.x >> 3) & 3u);
    const uint32_t SK = SKM + far * (SKH - SKM), SS = SSM + far * (SSH - SSM), UINC = 0x80000001u + SK * 64u;
    const uint32_t spec0 = gbase - 4u;
    auto fixed_tile = [&](int r) __attribute__((always_inline)) -> int { return r * NWG + ((pos + r * 37) & (NWG - 1)); };
    int tl0 = fixed_tile(0), tl1 = fixed_tile(1), tl2;
    uint32_t tk = 0u;
    const __amdgpu_buffer_rsrc_t rsO = __builtin_amdgcn_make_buffer_rsrc((void*)iout, 0, G * 16, 0x00020000);
    const __amdgpu_buffer_rsrc_t rsI = __builtin_amdgcn_make_buffer_rsrc((void*)iin, 0, G * 16, 0x00020000);
    const __amdgpu_buffer_rsrc_t rsG = __builtin_amdgcn_make_buffer_rsrc((void*)ang, 0, G * 8, 0x00020000);
    uint32_t t8 = (uint32_t)tid0 * 8u;
    auto load_one = [&](Raw& r, int tl, int idx) __attribute__((always_inline)) {
        const int j = idx < 4 ? idx >> 1 : idx & 1;
        const uint32_t g0 = (uint32_t)(tl * GR + j * NT);
        __builtin_amdgcn_sched_barrier(0);
        if (idx >= 4) r.a[j] = __builtin_amdgcn_raw_buffer_load_b64(rsG, t8, g0 * 8u, AUX_NT);
        else if (idx & 1) r.i[j] = __builtin_amdgcn_raw_buffer_load_b128(rsI, t8 + t8, g0 * 16u, AUX_NT);
        else r.o[j] = __builtin_amdgcn_raw_buffer_load_b128(rsO, t8 + t8, g0 * 16u, AUX_NT);
        __builtin_amdgcn_sched_barrier(0);
    };
    auto peek = [&](uint32_t at) __attribute__((always_inline)) -> uint32_t {
        return __hip_atomic_load((unsigned*)((char*)meta + at), __ATOMIC_RELAXED, __HIP_MEMORY_SCOPE_AGENT);
    };
    auto ready = [&]() __attribute__((always_inline)) {
        if (blockIdx.x != 0) {
            const uint32_t x = lds_ld(vconst<P_FLAG + 8>());
            while (peek(vconst<M_BEAT * 4>()) == x) __builtin_amdgcn_s_sleep(8);
        }
    };
    auto beat = [&]() __attribute__((always_inline)) {
        (void)__hip_atomic_fetch_add((unsigned*)((char*)meta + vconst<M_BEAT * 4>()), vconst<1>(), __ATOMIC_RELAXED, __HIP_MEMORY_SCOPE_AGENT);
    };
    auto draw = [&](uint32_t at) __attribute__((always_inline)) -> uint32_t {
        return __hip_atomic_fetch_add((unsigned*)((char*)meta + at), vconst<1>(), __ATOMIC_RELAXED, __HIP_MEMORY_SCOPE_AGENT);
    };
    uint32_t la;
    auto tab_at = [&]() __attribute__((always_inline)) -> uint32_t {
        return P_TAB + bfi(0xffu, bfi(0x3cu, t8 >> 2, t8 << 4), t8);
    };
    u32x2 nul;
    uint32_t U[2];
    {
        const uint32_t t = (uint32_t)tid0;
        la = P_BUF + ((t >> 5) << 12) + ((t & 0x18u) << 3) + ((t & 3u) << 4) + ((t & 4u) << 1);
        const uint32_t q = ((t >> 3) ^ (t >> 1 & 2u)) & 3u;
        nul.x = (0x7u >> q) << 31; nul.y = (0xbu >> q) << 31;
        U[0] = (((t >> 1) & 15u) * SS + ((t & 1u) * 64u + (t >> 5)) * SC + BPL(blockIdx.x)) * 128u + (0x80000000u - SK * 64u) + 8u - g2;
        U[1] = U[0] + 32u * SC * 128u;
    }
    auto log_line = [&](bool lg, uint32_t b) __attribute__((always_inline)) -> uint32_t {
        uint32_t lp = 0u;
        if (lg && (t8 & 0x38u) == 0u) {
            ready();
            lp = draw(vconst<M_LOG * 4>());
            __builtin_amdgcn_raw_buffer_store_b16((uint16_t)b, rsR, gbase + lp * 2u, 0, 0);
        }
        return LBASE + (uint32_t)__builtin_amdgcn_ds_bpermute((int)((t8 >> 1) & 0xe0u), (int)lp);
    };
    auto general = [&](int D, bool all, u64 sm) __attribute__((always_inline)) {
        uint32_t todo = __builtin_amdgcn_readfirstlane((uint32_t)sm | (uint32_t)(sm >> 32));
#pragma unroll 1
        while (todo) {
            const uint32_t S = (uint32_t)__builtin_ctz(todo) >> 1;
            todo &= ~(3u << (2u * S));
            pin(t8); pin(la);
            const uint32_t V = lds_ld(P_TAB + (t8 & ~63u) + S * 4u), g = V & 15u;
            const uint32_t col = S * 128u + ((t8 >> 1) & 0x60u) + (t8 >> 8);
            const bool act = all ? V != 0xffffffffu : (V & 0x80000009u) == 9u;
            const int hr = HREG((int)S, D) - HB;
            uint32_t ln = log_line(act && g > 8u + NTAIL, col);
            if (act) {
                const u32x2 N = *(const LDS u32x2*)(uintptr_t)(la + S * 256u);
                *(LDS u32x2*)(uintptr_t)(la + S * 256u) = nul;
                lds_st(P_TAB + (t8 & ~63u) + S * 4u, vconst<0xffffffffu>());
                if (!(g & 1u)) hset_at(hr, N);
                else {
                    uint32_t a, st;
                    if (g < 8u) { a = (V & ~15u) + ((t8 & 0x38u) << 1); st = 8u; }
                    else {
                        if (g <= 8u + NTAIL) ln = TBASE + ((g - 8u) >> 1) * TPL + col * TST + blockIdx.x;
                        a = ln * 128u + (la & 0x38u); st = 64u;
                    }
                    __builtin_amdgcn_raw_buffer_store_b64(hget_at(hr), rsR, a, 0, AUX_SC1);
                    __builtin_amdgcn_raw_buffer_store_b64(N, rsR, a + st, 0, AUX_SC1);
                }
            }
        }
    };
    unsigned epoch = 0;
    u64 pb2 = 0ull;
    u64 fm = 0ull, sm = 0ull;
    auto look = [&](u32x2 c) __attribute__((always_inline)) {
        uint32_t V[2];
        const bool late = __builtin_amdgcn_ballot_w64(((U[0] | U[1]) & 8u) != 0u) != 0ull;
        epoch++;
        asm volatile("v_mov_b32 %0, -1\n\tv_mov_b32 %1, -1\n\t"
                     "v_cmpx_le_u32 %6, %7, %2\n\tv_mov_b32 %0, %4\n\tv_add_u32 %4, %9, %4\n\tv_subrev_u32 %2, %8, %2\n\ts_mov_b64 exec, -1\n\t"
                     "v_cmpx_le_u32 vcc, %8, %3\n\tv_mov_b32 %1, %5\n\tv_add_u32 %5, %9, %5\n\tv_subrev_u32 %3, %8, %3\n\t"
                     "s_or_b64 %6, %6, exec\n\ts_mov_b64 exec, -1"
                     : "=&v"(V[0]), "=&v"(V[1]), "+v"(c.x), "+v"(c.y), "+v"(U[0]), "+v"(U[1]), "=&s"(fm) : "s"(CHV), "n"(CHV), "s"(UINC) : "vcc", "scc");
        *(LDS u32x2*)(uintptr_t)(P_CNT + t8) = c;
        pin(t8);
        const uint32_t tw = tab_at();
        lds_st(tw, V[0]);
        lds_st(tw + 64u, V[1]);
        if (wv == 1) lds_st(P_CNT + NB * 4 - 256 + (t8 >> 1), vconst<0x40000000u>());
        pb2 = __builtin_amdgcn_ballot_w64((int)(V[0] & V[1]) >= 0);
        sm = 0ull;
        if (__builtin_expect(late, 0)) {
            sm = __builtin_amdgcn_ballot_w64((V[0] & 0x80000009u) == 9u || (V[1] & 0x80000009u) == 9u);
#pragma unroll
            for (int e = 0; e < 2; e++)
                if ((V[e] & 0x8000000fu) == 9u + NTAIL) U[e] -= 2u * UINC;
        }
    };
    u32x4 Va, Vb;

    Raw raw[PD];
    {
        const int tid = tid0;
        asm volatile("" ::: "v88", "v127");
        typedef const __attribute__((address_space(4))) u32x4* cptr;
        u32x4 wd0 = ((cptr)(uintptr_t)wdown)[0], wd1 = ((cptr)(uintptr_t)wdown)[1];
        __builtin_amdgcn_sched_barrier(0);
        if (wv == 5) tk = CODE_AHEAD(part_kernel, acc_kernel, (uint32_t)tid & 63u);
        if (tid == 128) tk = peek(vconst<M_BEAT * 4>());
        if (blockIdx.x == 0 && wv == 3 && (tid & 63) < M_BEAT / 64) {
            __hip_atomic_store(meta + (tid & 63) * 64, 0u, __ATOMIC_RELAXED, __HIP_MEMORY_SCOPE_AGENT);
            __hip_atomic_store(meta + (tid & 63) * 64 + 1, 0u, __ATOMIC_RELAXED, __HIP_MEMORY_SCOPE_AGENT);
        }
        const __amdgpu_buffer_rsrc_t rsE = __builtin_amdgcn_make_buffer_rsrc((void*)m, 0, E * 8, 0x00020000);
        u32x2 mv[5];
        mv[4] = __builtin_amdgcn_raw_buffer_load_b64(rsE, (t8 & 511u) * (uint32_t)(E >> 6), 0, 0);
#pragma unroll
        for (int q = 0; q < 4; q++)
            mv[q] = __builtin_amdgcn_raw_buffer_load_b64(rsE, t8 & 511u, ((blockIdx.x >> 2) * 256u + (blockIdx.x & 3u) * 16u + (uint32_t)wv + 64u * q) * 512u, AUX_NT);
#pragma unroll
        for (int d = 0; d < MDL; d++) load_one(raw[1], tl0, d);
        asm volatile("" : "+s"(wd0), "+s"(wd1));
        const u32x2 ci = {CINIT, CINIT};
        *(LDS u32x2*)(uintptr_t)(P_CNT + tid * 8) = ci;
        if (tid < 64) lds_st(P_CNT + (NB + tid) * 4, 0x40000000u);
        if (tid < 8 && tid != 2) lds_st(P_FLAG + tid * 4, tid == 0 ? 0xffffffffu : 0u);
        if (tid == 128) lds_st(P_FLAG + 8, tk);
        const uint32_t lane = (uint32_t)tid & 63u;
        int e0[4];
        {
            float w[16];
#pragma unroll
            for (int i = 0; i < 4; i++) {
                w[2 * i] = bf2f(wd0[i] & 0xffffu); w[2 * i + 1] = bf2f(wd0[i] >> 16);
                w[8 + 2 * i] = bf2f(wd1[i] & 0xffffu); w[8 + 2 * i + 1] = bf2f(wd1[i] >> 16);
            }
            auto dot = [&](u32x2 v, int c) __attribute__((always_inline)) -> f32x2 {
                const f32x2 w0 = {w[c], w[c + 1]}, w1 = {w[4 + c], w[5 + c]}, w2 = {w[8 + c], w[9 + c]}, w3 = {w[12 + c], w[13 + c]};
                return w0 * bf2f(v.x & 0xffffu) + w1 * bf2f(v.x >> 16) + w2 * bf2f(v.y & 0xffffu) + w3 * bf2f(v.y >> 16);
            };
#pragma unroll
            for (int cp = 0; cp < 2; cp++) {
                if (cp == 1) {
#pragma unroll
                    for (int d = MDL; d < MDM; d++) load_one(raw[1], tl0, d);
                }
                if (blockIdx.x >= SLOTI / 64) continue;
                const f32x2 sd = dot(mv[4], 2 * cp);
#pragma unroll
                for (int e = 0; e < 2; e++) {
                    const int ex = (int)(wave_max(__float_as_uint(sd[e]) & 0x7fffffffu) >> 23);
                    e0[2 * cp + e] = __builtin_amdgcn_readfirstlane(ex == 0 ? 0 : min(max(ex - 129, -100), 100));
                }
                const f32x2 sc = {__uint_as_float((uint32_t)(127 - e0[2 * cp]) << 23), __uint_as_float((uint32_t)(127 - e0[2 * cp + 1]) << 23)};
                u32x4 o;
                u16x2 mx = {0, 0};
                f32x2 sq = {0.f, 0.f}, sg = {0.f, 0.f};
#pragma unroll
                for (int q = 0; q < 4; q++) {
                    const f32x2 y = dot(mv[q], 2 * cp) * sc;
                    const uint32_t hb = pk_bf16(y.x, y.y);
                    const f32x2 x = {__uint_as_float(hb << 16), __uint_as_float(hb & 0xffff0000u)};
                    o[q] = __builtin_bit_cast(uint32_t, __builtin_amdgcn_cvt_pkrtz(x.x, x.y));
                    mx = __builtin_elementwise_max(mx, __builtin_bit_cast(u16x2, hb & 0x7fff7fffu));
                    sq += x * x;
                    sg += x;
                }
                *(LDS u32x4*)(uintptr_t)(P_BUF + (cp * 64u + lane) * 272u + wv * 16) = o;
#pragma unroll
                for (int e = 0; e < 2; e++) {
                    const u32x4 st = {wave_max(mx[e]), __float_as_uint(wave_sum(sq[e])), __float_as_uint(wave_sum(sg[e])), 0u};
                    if (lane == 0u) *(LDS u32x4*)(uintptr_t)(P_LST + ((2 * cp + e) * 16 + wv) * 16) = st;
                }
            }
        }
        __builtin_amdgcn_sched_barrier(0);
        const uint16_t fq = freq[0];
#pragma unroll
        for (int d = MDM; d < 6; d++) load_one(raw[1], tl0, d);
#pragma unroll
        for (int d = 0; d < 6; d++)
            if (((LPOS >> (4 * d)) & 15) < 4) load_one(raw[0], tl1, d);
        __syncthreads();
        u32x4 mo[2];
#pragma unroll
        for (int cp = 0; cp < 2; cp++) mo[cp] = *(const LDS u32x4*)(uintptr_t)(P_BUF + (cp * 64u + (t8 >> 7)) * 272u + ((t8 + t8) & 0xf0u));
        if (blockIdx.x == 0 && wv == 3) asm volatile("s_waitcnt vmcnt(0)" ::: "memory");
        __syncthreads();
#pragma unroll
        for (int s = 0; s < 16; s++) *(LDS u32x2*)(uintptr_t)(la + s * 256) = nul;
        asm volatile("" : "+v"(raw[1].o[0]), "+v"(raw[1].o[1]), "+v"(raw[1].i[0]), "+v"(raw[1].i[1]), "+v"(raw[1].a[0]), "+v"(raw[1].a[1]));
        asm volatile("s_nop 0\n\tv_readfirstlane_b32 %0, %1" : "=s"(frr) : "v"(bf2f(fq) * 0.15915494309189535f));
        if (blockIdx.x < SLOTI / 64) {
            const __amdgpu_buffer_rsrc_t rsM = __builtin_amdgcn_make_buffer_rsrc((void*)(meta + W_MDC / 4), 0, (int)(128u * SLOTI * 4u), 0x00020000);
#pragma unroll
            for (int cp = 0; cp < 2; cp++) {
                const uint32_t row = cp * 64u + (t8 >> 7), c16 = (t8 + t8) & 0xf0u;
                __builtin_amdgcn_raw_buffer_store_b128(mo[cp], rsM, row * (SLOTI * 4u) + c16 + blockIdx.x * 256u, 0, AUX_SC1);
            }
            if (wv == 0) {
                const u32x4 st = *(const LDS u32x4*)(uintptr_t)(P_LST + t8 * 2u);
                const float sm1 = row_sum(__uint_as_float(st.z));
                const u32x2 r = {row_max(st.x) | (row_max(st.x ? 0xffffu - st.x : 0u) << 16), __float_as_uint(row_sum(__uint_as_float(st.y)))};
                if ((lane & 15u) == 15u) {
                    ((u32x2*)(meta + W_STAT / 4))[(lane >> 4) * NWG + blockIdx.x] = r;
                    meta[W_SUM / 4 + (lane >> 4) * NWG + blockIdx.x] = __float_as_uint(sm1);
                }
                if (blockIdx.x == 0 && lane < 4u) meta[M_E0 + lane] = (uint32_t)(lane == 0u ? e0[0] : lane == 1u ? e0[1] : lane == 2u ? e0[2] : e0[3]);
            }
        }
    }
    pin(la);

    uint32_t w[8], aw[8], xv[8];
    u64 nm[8];
    auto later2 = [&](int k) __attribute__((always_inline)) {
        uint32_t x0, x1;
        asm volatile("v_lshrrev_b32 %0, 4, %2\n\tv_lshrrev_b32 %1, 4, %5\n\tv_bitop3_b32 %0, %2, %0, 12 bitop3:0x72\n\tv_bitop3_b32 %1, %5, %1, 12 bitop3:0x72\n\t"
                     "s_not_b64 exec, %8\n\tds_write_b32 %2, %3 offset:%10\n\tds_or_b32 %0, %4 offset:%10\n\t"
                     "s_not_b64 exec, %9\n\tds_write_b32 %5, %6 offset:%10\n\tds_or_b32 %1, %7 offset:%10\n\ts_mov_b64 exec, -1"
                     : "=&v"(x0), "=&v"(x1)
                     : "v"(aw[k]), "v"(w[k]), "v"(xv[k]), "v"(aw[k + 1]), "v"(w[k + 1]), "v"(xv[k + 1]), "s"(nm[k]), "s"(nm[k + 1]), "n"(P_BUF) : "memory", "scc");
    };
    auto drain = [&]() __attribute__((always_inline)) {
        if (wv == 1) {
            const uint32_t q = (epoch + 1u) & 3u;
            const uint32_t n = __builtin_amdgcn_readfirstlane(lds_ld(vconst<P_FLAG + 16>() + q * 4u));
            if (__builtin_expect(n != 0u, 0)) {
                pin(t8);
                const uint32_t l = (t8 >> 3) & 63u;
                if (l < min(n, (uint32_t)LCAP)) {
                    const u32x4 e = *(const LDS u32x4*)(uintptr_t)(P_LST + (q * LCAP + l) * 16u);
                    lds_st(P_BUF + e.x, e.y);
                    lds_or(P_BUF + slot_x(e.x), e.z);
                }
                lds_st(vconst<P_FLAG + 16>() + q * 4u, vconst<0>());
            }
        }
    };
    auto again = [&](int D) __attribute__((always_inline)) {
        do {
            general(D, true, fm);
            __syncthreads();
            later2(0); later2(2); later2(4); later2(6);
            drain();
#pragma unroll
            for (int k = 0; k < 8; k++) {
                pin(aw[k]);
                bool due = false;
                if (aw[k] & 0x80000000u) {
                    xv[k] -= CHV;
                    if (xv[k] < CHV) {
                        const uint32_t f = (aw[k] >> 16) & 0x3ffu;
                        aw[k] = slot_w(aw[k] & 0xffffu, xv[k]);
                        xv[k] = f << ((xv[k] >> 8) & 31u);
                        due = true;
                    } else lds_st(vconst<P_FLAG>(), epoch + 1u);
                }
                nm[k] = ~__builtin_amdgcn_ballot_w64(due);
            }
            look(*(const LDS u32x2*)(uintptr_t)(P_CNT + t8));
            asm volatile("s_waitcnt lgkmcnt(0)" ::: "memory");
            __syncthreads();
        } while (__builtin_amdgcn_readfirstlane(lds_ld(vconst<P_FLAG>())) == epoch);
    };
    auto round = [&](auto DD, auto LL, Raw& rc, Raw& rn, int rd) __attribute__((always_inline)) {
        constexpr int D = decltype(DD)::value;
        constexpr bool LAST = decltype(LL)::value == 1, FIRST = decltype(LL)::value == 2;
        const int ng = min(GR, G - tl0 * GR);
        auto hook = [&](int p) __attribute__((always_inline)) {
#pragma unroll
            for (int q = 0; q < 6; q++)
            {
                const int lp = (LPOS >> (4 * q)) & 15;
                if (!LAST && lp < 4 && lp == p) load_one(rc, tl2, q);
                if (!LAST && lp >= 4 && (lp < 8 || lp >= 12 ? lp : lp - 8) == p) load_one(rn, tl1, q);
            }
        };
        uint32_t wn[8], ca[8], px[4], cv[8];
        float mx;
        float cs[2], cm[2] = {0.f, 0.f};
        auto convA = [&](int q) __attribute__((always_inline)) {
            if (LAST) return;
            const uint32_t a = rc.a[q >> 1][q & 1];
            const f32x2 av = {__uint_as_float(a << 16), __uint_as_float(a & 0xffff0000u)};
            f32x2 rv;
            asm("v_pk_mul_f32 %0, %1, %2 op_sel_hi:[1,0]" : "=v"(rv) : "v"(av), "s"((u64)__float_as_uint(frr)));
            mx = __builtin_elementwise_maximum(__builtin_elementwise_maximum(__builtin_fabsf(rv.x), __builtin_fabsf(rv.y)), (q & 1) ? mx : 0.f);
            cs[0] = __builtin_amdgcn_cosf(rv.x); cs[1] = __builtin_amdgcn_cosf(rv.y);
            asm volatile("" : "+v"(mx), "+v"(cs[0]), "+v"(cs[1]));
            if (FIRST) {
                const float c0 = fminf(fmaxf(cs[0], -1.f), 1.f), c1 = fminf(fmaxf(cs[1], -1.f), 1.f);
                cm[0] += c0 + c1; cm[1] += c0 * c0 + c1 * c1;
                if (q == 3 && wv == 4) {
                    const float a0 = wave_sum(cm[0]), a1 = wave_sum(cm[1]);
                    if ((t8 & 0x1f0u) == 0u) meta[W_SUM / 4 + (4 + ((t8 >> 3) & 1u)) * NWG + blockIdx.x] = __float_as_uint((t8 & 8u) ? a1 : a0);
                }
            }
            __builtin_amdgcn_sched_barrier(0);
        };
        auto convB = [&](int k) __attribute__((always_inline)) {
            if (LAST) return;
            const uint32_t o = rc.o[k >> 2][k & 3], i = rc.i[k >> 2][k & 3];
            wn[k] = (__builtin_amdgcn_alignbit(bfi(63u, o, o >> 5), __float_as_uint(cs[k & 1]), 30u) << 14) | (i >> 6);
            ca[k] = (bfi(63u, i, o) << 2) & 0x1ffcu;
            pin(wn[k]); pin(ca[k]);
            __builtin_amdgcn_sched_barrier(0);
        };
        auto convD = [&](int q) __attribute__((always_inline)) {
            if (LAST) return;
            asm volatile("v_cvt_pk_f16_f32 %0, |%1|, |%2|\n\tv_pk_mul_f16 %0, %0, %3 op_sel_hi:[1,0]" : "=v"(px[q]) : "v"(cs[0]), "v"(cs[1]), "s"(0x3ffu));
        };
        uint32_t cst;
        auto check = [&](int j) __attribute__((always_inline)) {
            if (LAST) return;
            if (__builtin_expect((__builtin_amdgcn_ballot_w64(!(mx <= RMAXREV)) | (u64)(uint32_t)(GR - ng)) != 0ull, 0)) {
                pin(t8);
                asm volatile("" : "+v"(rc.a[j]));
                const int lg = j * NT + (int)(t8 >> 3);
#pragma unroll
                for (int k = 0; k < 4; k++) {
                    const uint32_t a = (k & 1) ? (rc.a[j][k >> 1] >> 16) : (rc.a[j][k >> 1] & 0xffffu);
                    const bool nrm = __builtin_fabsf(frr * bf2f(a)) <= RMAXREV;
                    if (!(lg < ng && nrm)) ca[j * 4 + k] = NB * 4u + ((t8 >> 1) & 0xfcu);
                    if (lg < ng && !nrm) {
                        ready();
                        __builtin_amdgcn_raw_buffer_store_b32((uint32_t)((tl0 * GR + lg) * 4 + k), rsR, spec0 - draw(vconst<M_LOG * 4 + 4>()) * 4u, 0, 0);
                    }
                }
            }
            cst = vconst<CSTEP>();
        };
        auto ticket = [&](int k) __attribute__((always_inline)) {
            if (LAST) return;
            cv[k] = lds_add(P_CNT + ca[k], cst);
            __builtin_amdgcn_sched_barrier(0);
        };
        auto insert = [&](int j) __attribute__((always_inline)) {
            const int k0 = 4 * j;
            const bool odd = __builtin_amdgcn_ballot_w64(max(max(cv[k0], cv[k0 + 1]), max(cv[k0 + 2], cv[k0 + 3])) >= 2u * CHV) != 0ull;
#pragma unroll
            for (int k = k0; k < k0 + 4; k += 2) {
                uint32_t x0, x1;
                asm volatile("v_lshrrev_b32 %6, 11, %8\n\tv_lshrrev_b32 %7, 11, %9\n\t"
                             "v_bitop3_b32 %6, %6, %10, 60 bitop3:0x28\n\tv_bitop3_b32 %7, %7, %11, 60 bitop3:0x28\n\t"
                             "v_lshl_or_b32 %0, %10, 4, %6\n\tv_lshl_or_b32 %1, %11, 4, %7\n\t"
                             "v_lshlrev_b32_sdwa %2, %8, %12 dst_sel:DWORD dst_unused:UNUSED_PAD src0_sel:BYTE_1 src1_sel:WORD_0\n\t"
                             "v_lshlrev_b32_sdwa %3, %9, %12 dst_sel:DWORD dst_unused:UNUSED_PAD src0_sel:BYTE_1 src1_sel:WORD_1\n\t"
                             "v_bitop3_b32 %6, %0, %10, 12 bitop3:0x72\n\tv_bitop3_b32 %7, %1, %11, 12 bitop3:0x72\n\t"
                             "v_cmpx_gt_u32 %4, %15, %8\n\tds_write_b32 %0, %13 offset:%16\n\tds_or_b32 %6, %2 offset:%16\n\ts_mov_b64 exec, -1\n\t"
                             "v_cmpx_gt_u32 %5, %15, %9\n\tds_write_b32 %1, %14 offset:%16\n\tds_or_b32 %7, %3 offset:%16\n\ts_mov_b64 exec, -1"
                             : "=&v"(aw[k]), "=&v"(aw[k + 1]), "=&v"(xv[k]), "=&v"(xv[k + 1]), "=&s"(nm[k]), "=&s"(nm[k + 1]), "=&v"(x0), "=&v"(x1)
                             : "v"(cv[k]), "v"(cv[k + 1]), "v"(ca[k]), "v"(ca[k + 1]), "v"(px[k >> 1]), "v"(wn[k]), "v"(wn[k + 1]), "s"(CHV), "n"(P_BUF) : "memory");
                w[k] = wn[k]; w[k + 1] = wn[k + 1];
                if (__builtin_expect(odd, 0)) {
#pragma unroll
                    for (int q = k; q < k + 2; q++) {
                        nm[q] |= __builtin_amdgcn_ballot_w64(cv[q] >= 2u * CHV);
                        if (cv[q] >= 2u * CHV && cv[q] < 0x40000000u) {
                            const uint32_t lq = (epoch + 2u) & 3u;
                            uint32_t p = LCAP;
                            if (cv[q] < 3u * CHV) p = lds_add(vconst<P_FLAG + 16>() + lq * 4u, vconst<1>());
                            if (p < LCAP) {
                                const u32x2 e = {aw[q], w[q]};
                                *(LDS u32x2*)(uintptr_t)(P_LST + (lq * LCAP + p) * 16u) = e;
                                lds_st(P_LST + (lq * LCAP + p) * 16u + 8u, xv[q]);
                            } else {
                                aw[q] = ca[q] | ((q & 1) ? (px[q >> 1] & 0xffff0000u) : (px[q >> 1] << 16)) | 0x80000000u;
                                xv[q] = cv[q] - CHV;
                                lds_st(vconst<P_FLAG>(), epoch);
                            }
                        }
                    }
                }
            }
        };
        constexpr int P0 = D ? 0 : 3, P1 = D ? 1 : 2, P2 = D ? 2 : 1, P3 = D ? 3 : 0;
        const uint32_t fl = lds_ld(vconst<P_FLAG>());
        if (!LAST) asm volatile("" : "+v"(rc.o[0]), "+v"(rc.o[1]), "+v"(rc.i[0]), "+v"(rc.i[1]), "+v"(rc.a[0]), "+v"(rc.a[1]));
        if (!LAST && (rd + 4 >= nstat || tl1 >= ntile) && wv == 2) {
            pin(tk);
            if ((t8 & 511u) == 0u) {
                if (tl1 >= ntile) {
                    if (rd + 3 <= nstat) ready();
                    (void)draw(vconst<M_MD * 4>());
                } else if (rd + 4 == nstat) tk = peek(vconst<M_BEAT * 4>());
                else {
                    if (rd + 3 == nstat) {
                        if (tk == lds_ld(vconst<P_FLAG + 8>())) ready();
                        tk = 0u;
                    }
                    lds_st(vconst<P_FLAG + 4>(), tk);
                    if ((int)tk < ntile - nstat * NWG) tk = draw(vconst<M_TILE * 4>());
                }
            }
        }
        if (!LAST && blockIdx.x == 0 && wv == 3 && (t8 & 511u) == 0u) beat();
        hook(12);
        pin(t8);
        const uint32_t tr = P_TAB + (t8 & ~63u), u16 = (t8 + t8) & 0x70u;
        convA(0);
        convB(0);
        const bool ag = __builtin_amdgcn_readfirstlane(fl) == epoch;
        if (!FIRST && __builtin_expect(LAST || ag || sm != 0ull, 0)) {
            if (ag) again(D);
            general(D, false, sm);
            Va = *(const LDS u32x4*)(uintptr_t)(P_TAB + (t8 & ~63u) + P0 * 16);
            Vb = *(const LDS u32x4*)(uintptr_t)(P_TAB + (t8 & ~63u) + P1 * 16);
            asm volatile("" : "+v"(Va), "+v"(Vb));
        }
        uint32_t pq = (uint32_t)pb2 | (uint32_t)(pb2 >> 32);
        pq = (pq | (pq >> 1)) & 0x55555555u;
        pq = __builtin_amdgcn_readfirstlane(pq + (pq & (far * NEARM)));
        uint32_t ad[8];
        if (!FIRST) ad[0] = take<4 * P0 + 0, D>(Va[0], u16, la, nul);
        convB(1);
        if (!FIRST) ad[1] = take<4 * P0 + 1, D>(Va[1], u16, la, nul);
        convD(0);
        convA(1);
        if (!FIRST) ad[2] = take<4 * P0 + 2, D>(Va[2], u16, la, nul);
        convB(2);
        hook(13);
        if (!FIRST) ad[3] = take<4 * P0 + 3, D>(Va[3], u16, la, nul);
        convB(3);
        if (!FIRST) put<4 * P0 + 0, LAST ? 3 : 3, LAST>(rsA, ad[0], pq);
        if (!FIRST) ad[4] = take<4 * P1 + 0, D>(Vb[0], u16, la, nul);
        convD(1);
        check(0);
        ticket(0);
        hook(4);
        if (!FIRST) put<4 * P0 + 1, LAST ? 3 : 4, LAST>(rsA, ad[1], pq);
        if (!FIRST) ad[5] = take<4 * P1 + 1, D>(Vb[1], u16, la, nul);
        convA(2);
        ticket(1);
        if (!FIRST) put<4 * P0 + 2, LAST ? 3 : 5, LAST>(rsA, ad[2], pq);
        if (!FIRST) ad[6] = take<4 * P1 + 2, D>(Vb[2], u16, la, nul);
        convB(4);
        ticket(2);
        if (!FIRST) put<4 * P0 + 3, LAST ? 3 : 6, LAST>(rsA, ad[3], pq);
        if (!FIRST) Va = *(const LDS u32x4*)(uintptr_t)(tr + P2 * 16);
        __builtin_amdgcn_sched_barrier(0);
        if (!FIRST) ad[7] = take<4 * P1 + 3, D>(Vb[3], u16, la, nul);
        convB(5);
        ticket(3);
        SETP(2);
        if (!FIRST) put<4 * P1 + 0, LAST ? 4 : 8, LAST>(rsA, ad[4], pq);
        if (!FIRST) ad[0] = take<4 * P2 + 0, D>(Va[0], u16, la, nul);
        convD(2);
        convA(3);
        hook(5);
        if (!FIRST) put<4 * P1 + 1, LAST ? 4 : 7, LAST>(rsA, ad[5], pq);
        if (!FIRST) ad[1] = take<4 * P2 + 1, D>(Va[1], u16, la, nul);
        convB(6);
        if (!FIRST) put<4 * P1 + 2, LAST ? 4 : 6, LAST>(rsA, ad[6], pq);
        if (!FIRST) ad[2] = take<4 * P2 + 2, D>(Va[2], u16, la, nul);
        convB(7);
        if (!FIRST) put<4 * P1 + 3, LAST ? 3 : 4, LAST>(rsA, ad[7], pq);
        if (!FIRST) Vb = *(const LDS u32x4*)(uintptr_t)(tr + P3 * 16);
        __builtin_amdgcn_sched_barrier(0);
        if (!FIRST) ad[3] = take<4 * P2 + 3, D>(Va[3], u16, la, nul);
        convD(3);
        check(1);
        ticket(4);
        SETP(1);
        hook(6);
        if (!FIRST) put<4 * P2 + 0, LAST ? 4 : 5, LAST>(rsA, ad[0], pq);
        if (!FIRST) ad[4] = take<4 * P3 + 0, D>(Vb[0], u16, la, nul);
        ticket(5);
        if (!FIRST) put<4 * P2 + 1, LAST ? 4 : 6, LAST>(rsA, ad[1], pq);
        if (!FIRST) ad[5] = take<4 * P3 + 1, D>(Vb[1], u16, la, nul);
        ticket(6);
        if (!FIRST) put<4 * P2 + 2, LAST ? 4 : 7, LAST>(rsA, ad[2], pq);
        if (!FIRST) ad[6] = take<4 * P3 + 2, D>(Vb[2], u16, la, nul);
        ticket(7);
        SETP(0);
        if (!FIRST) put<4 * P2 + 3, LAST ? 3 : 7, LAST>(rsA, ad[3], pq);
        if (!FIRST) ad[7] = take<4 * P3 + 3, D>(Vb[3], u16, la, nul);
        hook(7);
        SETP(3);
        asm volatile("s_waitcnt lgkmcnt(0)" ::: "memory");
        __syncthreads();
        u32x2 cn;
        uint32_t tv;
        if (!LAST) { cn = *(const LDS u32x2*)(uintptr_t)(P_CNT + t8); tv = lds_ld(vconst<P_FLAG + 4>()); }
        __builtin_amdgcn_sched_barrier(0);
        if (!FIRST) later2(0);
        if (!FIRST) put<4 * P3, 15, LAST>(rsA, ad[4], pq);
        if (!FIRST) later2(2);
        if (!FIRST) put<4 * P3 + 1, 15, LAST>(rsA, ad[5], pq);
        if (!FIRST) drain();
        if (!LAST) {
            tl2 = rd + 2 < nstat ? fixed_tile(rd + 2) : nstat * NWG + (int)__builtin_amdgcn_readfirstlane(tv);
            look(cn);
        }
        hook(0);
        SETP(2);
        if (!FIRST) later2(4);
        if (!FIRST) put<4 * P3 + 2, 15, LAST>(rsA, ad[6], pq);
        if (!FIRST) later2(6);
        if (!FIRST) put<4 * P3 + 3, 15, LAST>(rsA, ad[7], pq);
        if (!LAST) {
            hook(1);
            SETP(1);
            insert(0);
            hook(2);
            SETP(0);
            pin(t8);
            Va = *(const LDS u32x4*)(uintptr_t)(P_TAB + (t8 & ~63u) + P3 * 16);
            Vb = *(const LDS u32x4*)(uintptr_t)(P_TAB + (t8 & ~63u) + P2 * 16);
            __builtin_amdgcn_sched_barrier(0);
            insert(1);
            hook(3);
            SETP(3);
            asm volatile("s_waitcnt lgkmcnt(0)" ::: "memory");
            __syncthreads();
            tl0 = tl1; tl1 = tl2;
        }
    };
#define HMV(A, B) "v_mov_b64 v[" #A ":" #A "+1], v[" #B ":" #B "+1]\n\t"
#define HMV4(A) HMV(A + 8, A + 6) HMV(A + 6, A + 4) HMV(A + 4, A + 2) HMV(A + 2, A)
    round(IC<1>(), IC<2>(), raw[1], raw[0], 0);
    int rd = 1;
#pragma unroll 1
    for (; tl1 < ntile; rd += 2) {
        round(IC<0>(), IC<0>(), raw[0], raw[1], rd);
        round(IC<1>(), IC<0>(), raw[1], raw[0], rd + 1);
    }
    if (tl0 >= ntile) asm volatile(HMV4(88) HMV4(98) HMV4(108) HMV4(118));
    asm volatile("" : "+s"(tl0));
    if (tl0 < ntile) round(IC<0>(), IC<0>(), raw[0], raw[1], rd);
    if (wv == 5) tk = CODE_AHEAD(acc_kernel, gen_scatter_kernel, (t8 >> 3) & 63u);
    round(IC<1>(), IC<1>(), raw[1], raw[0], 0);
    if (__builtin_amdgcn_readfirstlane(lds_ld(P_FLAG + 16 + ((epoch + 2u) & 3u) * 4u)) != 0u) {
        asm volatile("s_waitcnt lgkmcnt(0)" ::: "memory");
        __syncthreads();
        look(*(const LDS u32x2*)(uintptr_t)(P_CNT + t8));
        general(0, true, fm);
        __syncthreads();
        drain();
    }
    {
        uint32_t* tcnt = meta + W_TCNT / 4;
        const u32x2 c = *(const LDS u32x2*)(uintptr_t)(P_CNT + t8);
        const uint32_t tw = tab_at();
#pragma unroll
        for (int e = 0; e < 2; e++) {
            const uint32_t g = U[e] & 15u;
            lds_st(tw + e * 64u, g >= 8u ? 0x80000000u : ((U[e] + ((g & 1u) ? 0u : SK * 64u)) & 0x7fffff81u) | (((7u - g) << 1) & 12u));
            *(LDS uint8_t*)(uintptr_t)(P_CNT + (t8 & ~511u) + (((tw - P_TAB) >> 2) & 127u) + e * 16u) =
                (uint8_t)(g < 8u ? 0u : ((g - 8u) >> 1) + (((c[e] + 2u * CSTEP) >> 15) << 2) + (g & 1u) * 0x50u);
        }
        asm volatile("s_waitcnt lgkmcnt(0)" ::: "memory");
        __syncthreads();
        const uint32_t u16 = (t8 + t8) & 0x70u;
        u32x4 En[4];
#pragma unroll
        for (int p = 0; p < 4; p++) En[p] = *(const LDS u32x4*)(uintptr_t)(P_TAB + (t8 & ~63u) + p * 16u);
        const u32x4 cb = *(const LDS u32x4*)(uintptr_t)(P_CNT + (t8 & ~511u) + ((t8 >> 2) & 0x70u));
        u32x2 N[16];
#pragma unroll
        for (int s = 0; s < 16; s++) N[s] = *(const LDS u32x2*)(uintptr_t)(la + s * 256u);
        if (u16 == 0u) *(u32x4*)(tcnt + blockIdx.x * 512u + (t8 >> 4)) = cb;
        const uint32_t sg = P_BUF + ((t8 & ~63u) << 4);
        uint32_t ps;
        auto rest = [&](auto SL) __attribute__((always_inline)) {
            constexpr int s = decltype(SL)::value;
            const uint32_t en = En[s >> 2][s & 3], by = cb[s >> 2] >> ((s & 3) * 8);
            const u32x2 Hr = hget<HREG(s, 0)>();
            const uint32_t at = ps + ((la & 0x38u) ^ ((s & 3) << 4));
            *(LDS u32x2*)(uintptr_t)at = Hr;
            *(LDS u32x2*)(uintptr_t)(at + (by & 0x40u)) = N[s];
            ps += (by & 0x3cu) << 2;
            const u32x2 Hs = (en & 1u) ? Hr : nul;
            const u32x4 d = {Hs.x, Hs.y, N[s].x, N[s].y};
            uint32_t a = __builtin_amdgcn_bitop3_b32(en, u16, 15u, 0x54);
            {
                const u32x4 rs_ = rsA;
                const uint32_t far_ = ((NEARM >> (2 * s)) & 1u) ? far : 0u;
                asm volatile("s_cmp_eq_u32 %3, 0\n\ts_cbranch_scc0 1f\n\tbuffer_store_dwordx4 %0, %1, %2, 0 offen sc1\n\ts_branch 2f\n"
                             "1:\n\tbuffer_store_dwordx4 %0, %1, %2, 0 offen sc1 nt\n2:\n\ts_nop 1" :: "v"(d), "v"(a), "s"(rs_), "s"(far_) : "memory", "scc");
            }
            if (__builtin_expect(__builtin_amdgcn_ballot_w64((en & 12u) != 0u) != 0ull, 0)) {
                const u32x4 dn = {nul.x, nul.y, nul.x, nul.y};
#pragma unroll 1
                for (uint32_t k = (en >> 2) & 3u; k > 0u; k--) {
                    a += SK * 128u;
                    __builtin_amdgcn_raw_buffer_store_b128(dn, rsR, a, 0, AUX_SC1);
                }
            }
        };
        auto pack = [&](int p) __attribute__((always_inline)) {
            const uint32_t a = (SBASE + ((((t8 >> 1) & 0x60u) + (t8 >> 8)) * 2u + p) * NWG + blockIdx.x) * 128u + u16;
            asm volatile("" ::: "memory");
#pragma unroll 1
            for (uint32_t q = 0; __builtin_amdgcn_ballot_w64(sg + q < ps) != 0ull; q += 256u) {
                const u32x4 d0 = *(const LDS u32x4*)(uintptr_t)(sg + q + u16), d1 = *(const LDS u32x4*)(uintptr_t)(sg + q + 128u + u16);
                __builtin_amdgcn_raw_buffer_store_b128(d0, rsR, sg + q < ps ? a + q * SPL : 0xffffff00u, 0, AUX_SC1);
                __builtin_amdgcn_raw_buffer_store_b128(d1, rsR, sg + q + 128u < ps ? a + (q + 128u) * SPL : 0xffffff00u, 0, AUX_SC1);
            }
            asm volatile("" ::: "memory");
        };
        asm volatile("" : "+v"(N[0]), "+v"(N[1]), "+v"(N[2]), "+v"(N[3]), "+v"(N[4]), "+v"(N[5]), "+v"(N[6]), "+v"(N[7]) :: "memory");
        asm volatile("" : "+v"(N[8]), "+v"(N[9]), "+v"(N[10]), "+v"(N[11]), "+v"(N[12]), "+v"(N[13]), "+v"(N[14]), "+v"(N[15]) :: "memory");
        ps = sg;
        rest(IC<0>()); rest(IC<1>()); rest(IC<2>()); rest(IC<3>()); rest(IC<4>()); rest(IC<5>()); rest(IC<6>()); rest(IC<7>());
        pack(0);
        ps = sg;
        rest(IC<8>()); rest(IC<9>()); rest(IC<10>()); rest(IC<11>()); rest(IC<12>()); rest(IC<13>()); rest(IC<14>()); rest(IC<15>());
        pack(1);
    }
    asm volatile("" :: "v"(tk));
    if (blockIdx.x == 0 && t8 == 1536u) {
        do {
            beat();
            __builtin_amdgcn_s_sleep(8);
        } while (peek(vconst<M_MD * 4>()) < NWG);
    }
}

#define A_MD 0
#define A_ACC (SLOTI * 4)
#define A_FLAG (A_ACC + SLOTO * 4)
#define A_LDS (A_FLAG + 512)
static_assert(SLOTI * 4 == 3 * NT * 16 + 1024, "md staging assumes three full vectors and 256 dwords");
#define BIAS 0x40004000u
#define NFAST 8
#define NLD 10
#define MDF 0x0a11333
#define MDP 0xaaaaa
#define HP 2
#define SHARE (SLOTO / 8)
#define ZSIG 9.0f
#define HSLOW 0x80008000u
#define GAS __attribute__((address_space(1)))

extern "C" __global__ __launch_bounds__(NT, 4) __attribute__((amdgpu_num_vgpr(53))) void acc_kernel(char* ws, int T, int E, int ks, int lcap, float zf, int pad,
                                                 const uint16_t* __restrict__ centers, const uint16_t* __restrict__ wrbf,
                                                 const uint16_t* __restrict__ wsbf, const uint16_t* __restrict__ wup,
                                                 const u32x2* __restrict__ m, const uint16_t* __restrict__ dist, u32x2* __restrict__ out,
                                                 const int* __restrict__ iin, const int* __restrict__ iout, const uint16_t* __restrict__ ang,
                                                 const uint16_t* __restrict__ wdown, const uint16_t* __restrict__ freq) {
    const char* recs = ws + W_RECS;
    const u32x4* tcnt = (const u32x4*)(ws + W_TCNT);
    const uint32_t* mdc = (const uint32_t*)(ws + W_MDC);
    const u32x4* stat = (const u32x4*)(ws + W_STAT);
    uint32_t* partial = (uint32_t*)(ws + W_RECS + (((size_t)LBASE * 128u + (size_t)lcap * 130u + 255u) & ~(size_t)255u));
    unsigned* meta = (unsigned*)ws;
    extern __shared__ __attribute__((aligned(16))) char smem[];
    unsigned* acc = (unsigned*)(smem + A_ACC);
    unsigned* s_flag = (unsigned*)(smem + A_FLAG);
    const int tid = threadIdx.x;
    const int lane = tid & 63;
    const int wv = __builtin_amdgcn_readfirstlane(tid >> 6);
    const int cp = (blockIdx.x >> 3) & 1;
    const int h = blockIdx.x & 3;
    const int j = ((blockIdx.x >> 2) & 1) * 16 + (blockIdx.x >> 4);
    u32x4 tc;
    const __amdgpu_buffer_rsrc_t rsM = __builtin_amdgcn_make_buffer_rsrc((void*)mdc, 0, (int)(128u * SLOTI * 4u), 0x00020000);

    u32x4 pm[4];
    u32x4 ring[NLD];
    auto load_md = [&](int ff, int pl, int mdp) __attribute__((always_inline)) {
        const uint32_t so = (uint32_t)(cp * 64 + ff * 4 + h) * (SLOTI * 4u);
#pragma unroll
        for (int v = 0; v < 3; v++)
            if (pl < 0 || pl == ((mdp >> (4 * v)) & 15)) pm[v] = __builtin_amdgcn_raw_buffer_load_b128(rsM, (uint32_t)tid * 16u, so + (uint32_t)v * (NT * 16u), 0);
        if ((pl < 0 || pl == (mdp & 15)) && wv == 0) pm[3] = __builtin_amdgcn_raw_buffer_load_b128(rsM, (uint32_t)tid * 16u, so + 3u * NT * 16u, 0);
        __builtin_amdgcn_sched_barrier(0);
    };
    const uint32_t lf = (uint32_t)(tid >> 9) * (SKH * 128u) + (uint32_t)(tid & 0x1e0) * 16u + (uint32_t)(tid & 12) * 32u + (uint32_t)(tid & 16) * 4u + (uint32_t)(tid & 3) * 16u;
    const uint32_t lp = (uint32_t)(tid >> 8) * (SKM * 128u) + (uint32_t)((tid >> 2) & 63) * 128u + (uint32_t)(tid & 3) * 16u;
    auto load_main = [&](int i, int ff, bool near = false) __attribute__((always_inline)) -> u32x4 {
        const uint32_t q = (uint32_t)(((i >> 1) - HP) & 3), SK = q ? SKM : SKH, SS = q ? SSM : SSH;
        const __amdgpu_buffer_rsrc_t rs = __builtin_amdgcn_make_buffer_rsrc((void*)(recs + (QAT(q) + (uint32_t)ff * SS + (uint32_t)(h * 32 + j) * SC) * 128u), 0, ks * (int)(SK * 128u), 0x00020000);
        if (q == 0u && near) return __builtin_amdgcn_raw_buffer_load_b128(rs, lf, (i & 1) * (int)(2u * SK * 128u), 0);
        if (q == 0u) return __builtin_amdgcn_raw_buffer_load_b128(rs, lf, (i & 1) * (int)(2u * SK * 128u), AUX_NT);
        return __builtin_amdgcn_raw_buffer_load_b128(rs, lp + (uint32_t)(i & 1) * 64u, 0, 0);
    };
    const __amdgpu_buffer_rsrc_t rsT = __builtin_amdgcn_make_buffer_rsrc((void*)recs, 0, (int)(LBASE * 128u), 0x00020000);
    const uint32_t tof = (uint32_t)(lane & 2) * (TPL * 64u) + (uint32_t)(tid >> 2) * 128u + (uint32_t)(lane & 1) * 64u + 4u + (TBASE + (uint32_t)(h * 32 + j) * TST) * 128u;
    const uint32_t sp0 = (SBASE + (uint32_t)(h * 32 + j) * (2u * NWG) + (uint32_t)(tid >> 2)) * 128u;
    auto tail_rank = [&](uint32_t c, uint32_t sp, int ff, uint32_t t0, int& dst, uint32_t& en) __attribute__((always_inline)) -> int {
        const int k4 = (int)(((uint32_t)(lane & 3) + t0) * 4u) - (int)((c & 3u) * 8u);
        const int left = (int)((c >> 2) & 15u) - k4;
        const bool has = left > 0;
        uint32_t ea = tof + (uint32_t)ff * (128u * TST * 128u), eb = sp + (uint32_t)(k4 * 16);
        eb = sp0 + (uint32_t)(ff >> 3) * (NWG * 128u) + eb + __umul24(eb >> 7, (SPL - 1u) * 128u) + (uint32_t)min(max(left, 0), 4);
        asm volatile("" : "+v"(ea), "+v"(eb));
        en = k4 < 0 ? ea : eb;
        const u64 mask = __builtin_amdgcn_ballot_w64(has);
        const int n = __builtin_popcountll(mask);
        const int rank = __builtin_amdgcn_mbcnt_hi((uint32_t)(mask >> 32), __builtin_amdgcn_mbcnt_lo((uint32_t)mask, 0u));
        const int r = has ? rank : (n + lane - rank);
        dst = ((r & 15) << 4) | ((r >> 4) << 2);
        return n;
    };
    auto load_tail = [&](uint32_t en, u64& tm) __attribute__((always_inline)) -> u32x4 {
        const bool on = (uint32_t)(lane & 3) < (en & 7u);
        tm = __builtin_amdgcn_ballot_w64(on);
        const uint32_t at = (en & 0x70u) + (uint32_t)(lane & 3) * 16u;
        return __builtin_amdgcn_raw_buffer_load_b128(rsT, on ? (en & ~127u) + at + __umul24(at >> 7, (SPL - 1u) * 128u) : 0xffffff00u, 0, 0);
    };
    f32x2 fa[4][4];
    u32x4 em[2];
    u32x2 dw2;
    uint32_t wl;
    const __amdgpu_buffer_rsrc_t rsE = __builtin_amdgcn_make_buffer_rsrc((void*)m, 0, E * 8, 0x00020000);
    const __amdgpu_buffer_rsrc_t rsD = __builtin_amdgcn_make_buffer_rsrc((void*)dist, 0, E * 2, 0x00020000);
    auto acc_vec = [&](int i) __attribute__((always_inline)) -> u32x4 {
        const uint32_t a = A_ACC + (uint32_t)(i >> 4) * 256u + (uint32_t)(i & 15) * 8u;
        const u32x2 x = *(const LDS u32x2*)(uintptr_t)a, y = *(const LDS u32x2*)(uintptr_t)(a + 128u);
        const u32x4 v = {x.x, x.y, y.x, y.y};
        return v;
    };
    auto edge0 = [&](int sh) __attribute__((always_inline)) -> uint32_t {
        return (tid < SHARE / 4) ? (uint32_t)(((sh * (SHARE / 64) + (tid >> 4)) << 11) | (j << 6) | ((tid & 15) * 2)) : 0x0fffff00u;
    };
    auto load_m = [&](int sh) __attribute__((always_inline)) {
        em[0] = __builtin_amdgcn_raw_buffer_load_b128(rsE, edge0(sh) * 8u, 0, AUX_NT);
        em[1] = __builtin_amdgcn_raw_buffer_load_b128(rsE, edge0(sh) * 8u + 256u, 0, AUX_NT);
    };
    auto load_d = [&](int sh) __attribute__((always_inline)) {
        dw2.x = __builtin_amdgcn_raw_buffer_load_b32(rsD, edge0(sh) * 2u, 0, AUX_NT);
        dw2.y = __builtin_amdgcn_raw_buffer_load_b32(rsD, edge0(sh) * 2u + 64u, 0, AUX_NT);
    };
    auto load_wl = [&]() __attribute__((always_inline)) {
        const u64 p0 = (u64)wsbf, p1 = (u64)wrbf, p2 = (u64)centers, p3 = (u64)wup;
        const u64 pa = lane < 16 ? p0 : p1, pb = lane < 40 ? p2 : p3;
        wl = ((const GAS uint32_t*)(lane < 32 ? pa : pb))[lane & (lane < 32 ? 15 : lane < 40 ? 3 : 7)];
    };
    auto factor = [&](int r, bool first) __attribute__((always_inline)) {
        const float sq = 3.7982826f;
        const f32x2 d01 = {bf2f(dw2.x & 0xffffu), bf2f(dw2.x >> 16)}, d23 = {bf2f(dw2.y & 0xffffu), bf2f(dw2.y >> 16)};
        const uint32_t cw = __builtin_amdgcn_readlane(wl, 32 + (r >> 1));
        const float cn = bf2f((r & 1) ? (cw >> 16) : (cw & 0xffffu));
        f32x2 w[4];
#pragma unroll
        for (int k = 0; k < 4; k++) {
            const uint32_t x = __builtin_amdgcn_readlane(wl, (k >> 1) * 16 + r * 2 + (k & 1));
            w[k].x = bf2f(x & 0xffffu); w[k].y = bf2f(x >> 16);
        }
        const f32x2 y01 = (d01 - cn) * sq, y23 = (d23 - cn) * sq;
        const f32x2 t01 = -y01 * y01, t23 = -y23 * y23;
        const float rb[4] = {__builtin_amdgcn_exp2f(t01.x), __builtin_amdgcn_exp2f(t01.y), __builtin_amdgcn_exp2f(t23.x), __builtin_amdgcn_exp2f(t23.y)};
#pragma unroll
        for (int q = 0; q < 4; q++) {
#pragma unroll
            for (int k = 0; k < 4; k++) fa[q][k] = first ? w[k] * rb[q] : __builtin_elementwise_fma(w[k], (f32x2)(rb[q]), fa[q][k]);
        }
    };
#define ADT(V, O) "ds_write_addtid_b32 " V " offset:" #O "\n\t"
    auto store_md = [&]() __attribute__((always_inline)) {
        asm volatile("s_mov_b32 m0, %12\n\ts_nop 0\n\t" ADT("%0", 0) ADT("%1", 256) ADT("%2", 512) ADT("%3", 768) ADT("%4", 16384) ADT("%5", 16640) ADT("%6", 16896) ADT("%7", 17152)
                     ADT("%8", 32768) ADT("%9", 33024) ADT("%10", 33280) ADT("%11", 33536)
                     :: "v"(pm[0][0]), "v"(pm[0][1]), "v"(pm[0][2]), "v"(pm[0][3]), "v"(pm[1][0]), "v"(pm[1][1]), "v"(pm[1][2]), "v"(pm[1][3]),
                        "v"(pm[2][0]), "v"(pm[2][1]), "v"(pm[2][2]), "v"(pm[2][3]), "s"(wv * 1024) : "memory");
        if (wv == 0) asm volatile("s_mov_b32 m0, 0\n\ts_nop 0\n\t" ADT("%0", 49152) ADT("%1", 49408) ADT("%2", 49664) ADT("%3", 49920)
                                  :: "v"(pm[3][0]), "v"(pm[3][1]), "v"(pm[3][2]), "v"(pm[3][3]) : "memory");
    };
    const int me = h * 2 + cp;
    GAS unsigned* dw = (GAS unsigned*)(meta + M_DONE + j * 64);
    const bool fin = wv < (SHARE / 4 + 63) / 64;
    bool fast, more;
    uint32_t need, mg0, mg1;
    uint32_t guard = 0;
    uint32_t sgn = 0x8000u;
    pin(sgn);

#define GAT3(M) { uint32_t a0_, a1_, a2_; asm volatile( \
        "v_lshlrev_b16 %0, 2, %3\n\tv_lshlrev_b16 %1, 2, %4\n\tv_lshlrev_b16 %2, 2, %5\n\t" \
        "ds_read_b32 v[%6], %0\n\tds_read_b32 v[%6+1], %1\n\tds_read_b32 v[%6+2], %2" \
        : "=&v"(a0_), "=&v"(a1_), "=&v"(a2_) : "v"(w0), "v"(w1), "v"(w2), "n"(M) : "memory"); }
#define MIXL " op_sel_hi:[1,1,0]\n\t"
#define MIXH " op_sel:[0,1,0] op_sel_hi:[1,1,0]\n\t"
#define SDW " dst_sel:DWORD dst_unused:UNUSED_PAD src0_sel:DWORD src1_sel:WORD_1\n\t"
#define FOLD "v_bitop3_b32 %6, %6, v[%15-9], v[%15-8] bitop3:0xfe\n\tv_or_b32 %6, v[%15-7], %6\n\t"
#define SCAT(PRE, POST, M) { uint32_t c0_, c1_, c2_, a0_, a1_, a2_; asm volatile( \
        FOLD "v_and_b32 %0, 0x3ff, %7\n\tv_lshrrev_b32 %2, 22, %7\n\t" PRE \
        "v_bitop3_b32 %0, %11, %8, %0 bitop3:0xea\n\tv_bitop3_b32 %2, %11, %10, %2 bitop3:0xea\n\tv_bfe_u32 %1, %7, 11, 10\n\t" \
        "v_fma_mix_f32 %3, %0, v[%15], %12" MIXL "v_bitop3_b32 %1, %11, %9, %1 bitop3:0xea\n\tv_fma_mix_f32 %0, %0, v[%15], %13" MIXH \
        "v_fma_mix_f32 %4, %1, v[%15+1], %12" MIXL "v_lshl_add_u32 %0, %0, 16, %3\n\tv_lshlrev_b32_sdwa %3, 2, %8" SDW \
        "v_fma_mix_f32 %1, %1, v[%15+1], %13" MIXH "ds_add_rtn_u32 v[%15-9], %3, %0 offset:%14\n\t" \
        "v_fma_mix_f32 %5, %2, v[%15+2], %12" MIXL "v_lshl_add_u32 %1, %1, 16, %4\n\tv_lshlrev_b32_sdwa %4, 2, %9" SDW \
        "v_fma_mix_f32 %2, %2, v[%15+2], %13" MIXH "ds_add_rtn_u32 v[%15-8], %4, %1 offset:%14\n\t" \
        "v_lshl_add_u32 %2, %2, 16, %5\n\tv_lshlrev_b32_sdwa %5, 2, %10" SDW \
        "ds_add_rtn_u32 v[%15-7], %5, %2 offset:%14" POST \
        : "=&v"(c0_), "=&v"(c1_), "=&v"(c2_), "=&v"(a0_), "=&v"(a1_), "=&v"(a2_), "+v"(guard) \
        : "v"(xw), "v"(w0), "v"(w1), "v"(w2), "v"(sgn), "s"(mg0), "s"(mg1), "n"(A_ACC), "n"(M), "s"(tm) : "memory"); }
    asm volatile("v_mov_b32 v110, 0\n\tv_mov_b32 v111, 0\n\tv_mov_b32 v112, 0\n\tv_mov_b32 v113, 0\n\tv_mov_b32 v114, 0\n\tv_mov_b32 v115, 0\n\t"
                 "v_mov_b32 v116, 0\n\tv_mov_b32 v117, 0\n\tv_mov_b32 v118, 0" ::: "v106", "v127");
    auto unmix = [&](int i) __attribute__((always_inline)) {
        const auto s0 = __builtin_amdgcn_permlane16_swap(ring[i][0], ring[i][2], false, false);
        const auto s1 = __builtin_amdgcn_permlane16_swap(ring[i][1], ring[i][3], false, false);
        ring[i][0] = s0[0]; ring[i][2] = s0[1]; ring[i][1] = s1[0]; ring[i][3] = s1[1];
    };
    auto word = [&](int i, int k) __attribute__((always_inline)) -> uint32_t {
        return i < NFAST && (i >> 1) != HP ? ring[(i & ~1) | (k >> 1)][(i & 1) * 2 + (k & 1)] : ring[i][k];
    };
    auto gather = [&](int R, int i, int p) __attribute__((always_inline)) {
        if ((i >> 1) == HP) unmix(i);
        const uint32_t w0 = word(i, 0 ^ R), w1 = word(i, 1 ^ R), w2 = word(i, 2 ^ R);
        if (p == 0) GAT3(119) else if (p == 1) GAT3(122) else GAT3(125)
    };
    auto scatter = [&](int R, int i, int p, u64 tm) __attribute__((always_inline)) {
        const uint32_t xw = word(i, 3 ^ R), w0 = word(i, 0 ^ R), w1 = word(i, 1 ^ R), w2 = word(i, 2 ^ R);
        if (i < NFAST) { if (p == 0) SCAT("", "", 119) else if (p == 1) SCAT("", "", 122) else SCAT("", "", 125) }
        else if (p == 0) SCAT("s_mov_b64 exec, %16\n\t", "\n\ts_mov_b64 exec, -1", 119)
        else SCAT("s_mov_b64 exec, %16\n\t", "\n\ts_mov_b64 exec, -1", 125)
    };
#define WAIT_L(N) asm volatile("s_waitcnt lgkmcnt(" #N ")" ::: "memory")
    int ntn;
    u64 tm0, tm1;
    uint32_t ta[2], sp = 0u;
    auto list1 = [&](int ff) __attribute__((always_inline)) {
        int dst;
        uint32_t en;
        ntn = tail_rank(tc.x, sp, ff, 0u, dst, en);
        asm volatile("ds_permute_b32 v106, %0, %1" :: "v"(dst), "v"(en) : "memory");
        if (__builtin_expect(__builtin_amdgcn_ballot_w64((tc.x & 2u) != 0u) != 0ull, 0)) {
            const int n2 = tail_rank(tc.x, sp, ff, 4u, dst, en);
            if (ntn + n2 <= 32) {
                const int r = ((dst >> 4) | ((dst & 12) << 2)) + ntn;
                const int x = __builtin_amdgcn_ds_permute((en & 7u) ? ((r & 15) << 4) | ((r >> 4) << 2) : 252, (en & 7u) ? (int)en : 0);
                asm volatile("v_cmpx_ne_u32 vcc, 0, %0\n\tv_mov_b32 v106, %0\n\ts_mov_b64 exec, -1\n\ts_nop 4" :: "v"(x) : "vcc");
                ntn += n2;
            } else ntn = 64;
        }
    };
    auto step = [&](auto RR, auto KK, int ff) __attribute__((always_inline)) {
        constexpr int R = decltype(RR)::value;
        constexpr bool first = decltype(KK)::value == 1, last = decltype(KK)::value == 2;
        constexpr int mdp = first ? MDF : MDP;
        gather(R, 0, 0);
        const int ntl = ntn;
        asm volatile("v_mov_b32_dpp %0, v106 quad_perm:[0,0,0,0] row_mask:0xf bank_mask:0xf\n\tv_mov_b32_dpp %1, v106 quad_perm:[1,1,1,1] row_mask:0xf bank_mask:0xf" : "=v"(ta[0]), "=v"(ta[1]));
        auto side = [&](int pl) __attribute__((always_inline)) {
            if (first && pl == ((mdp >> 20) & 15)) { ring[4] = load_main(4, ff, !(NEARM & 1u)); ring[5] = load_main(5, ff, !(NEARM & 1u)); }
            if (first && pl == ((mdp >> 24) & 15)) { ring[6] = load_main(6, ff); ring[7] = load_main(7, ff); }
            if (pl == ((mdp >> 12) & 15)) ring[NFAST] = load_tail(ta[0], tm0);
            if (pl == ((mdp >> 16) & 15)) ring[NFAST + 1] = load_tail(ta[1], tm1);
            __builtin_amdgcn_sched_barrier(0);
            if (!last) load_md(ff + 1, pl, mdp);
            if (!first && !last && pl == 5 && ff == 6) {
                load_d(me);
                load_wl();
                if (tid == NT - 128) (void)__hip_atomic_fetch_add((GAS unsigned*)((GAS char*)dw + vconst<0>()), 0x1000000u << me, __ATOMIC_RELAXED, __HIP_MEMORY_SCOPE_AGENT);
            }
            if (last && pl == 5) load_m(me);
            if (!first && !last && pl == 1 && ff == 13 && wv == 15) { GAS unsigned* d_ = dw; asm volatile("s_nop 4\n\tglobal_load_dword v108, %0, %1 sc1" :: "v"(vconst<0>()), "s"(d_) : "memory"); }
            __builtin_amdgcn_sched_barrier(0);
        };
        side(8);
        gather(R, 1, 1);
        side(9);
        const uint32_t tcur = tc.x, spc = sp;
#pragma unroll
        for (int i = 0; i < NFAST - 1; i++) {
            gather(R, i + 2, (i + 2) % 3);
            if (i == 0) side(10);
            if (i == 0) WAIT_L(6); else if (i == 1) WAIT_L(9); else WAIT_L(12);
            scatter(R, i, i % 3, 0ull);
            if (!last && (i & 1)) { ring[i - 1] = load_main(i - 1, ff + 1, first && !(NEARM & 4u)); ring[i] = load_main(i, ff + 1, first && !(NEARM & 4u)); }
            side(i);
            if (i == 3) SETP(2);
            if (i == 4) SETP(1);
            if (i == 5) SETP(0);
            if (i == 0 && !last) {
                tc.x = __builtin_amdgcn_alignbit(tc.y, tc.x, 8u); tc.y = __builtin_amdgcn_alignbit(tc.z, tc.y, 8u);
                tc.z = __builtin_amdgcn_alignbit(tc.w, tc.z, 8u); tc.w >>= 8;
                sp = ff == 7 ? 0u : sp + ((tcur & 0x3cu) << 2);
                list1(ff + 1);
            }
            __builtin_amdgcn_sched_barrier(0);
        }
        if (last) asm volatile("" :: "v"(ring[NFAST + 1]));
        if (__builtin_expect(ntl > 16, 0)) {
            gather(R, NFAST + 1, 0);
            WAIT_L(0);
            scatter(R, NFAST + 1, 0, tm1);
        }
        if (__builtin_expect(ntl > 32, 0)) {
#pragma unroll 1
            for (int e = 2; e < 6; e++) {
                int dst;
                uint32_t en;
                if (tail_rank(tcur, spc, ff, (uint32_t)e & 4u, dst, en) <= (e & 3) * 16) continue;
                const int cmp = __builtin_amdgcn_ds_permute(dst, (int)en);
                ring[NFAST + 1] = load_tail((uint32_t)__builtin_amdgcn_ds_bpermute(((lane & ~3) | (e & 3)) << 2, cmp), tm1);
                gather(R, NFAST + 1, 0);
                WAIT_L(0);
                scatter(R, NFAST + 1, 0, tm1);
            }
        }
        SETP(3);
        WAIT_L(3);
        if (!last) {
            __builtin_amdgcn_s_barrier();
            store_md();
            SETP(0);
        }
        scatter(R, NFAST - 1, 1, 0ull);
        if (!last) { ring[6] = load_main(6, ff + 1); ring[7] = load_main(7, ff + 1); }
        scatter(R, NFAST, 2, tm0);
        if (!last) {
            WAIT_L(6);
            __builtin_amdgcn_s_barrier();
            SETP(3);
        }
    };
    auto steps = [&](auto RR) __attribute__((always_inline)) {
        tc = tcnt[(wv * 16 + (lane >> 2)) * 128 + j * 4 + h];
        u32x4 sv[2];
        float4 su[3];
        uint32_t e0;
        if (wv < 4) {
            sv[0] = (u32x4)(0u); sv[1] = sv[0];
            su[0] = make_float4(0.f, 0.f, 0.f, 0.f);
            if (lane < SLOTI / 256) { sv[0] = stat[tid * 2]; sv[1] = stat[tid * 2 + 1]; su[0] = ((const float4*)(ws + W_SUM))[tid]; }
            su[1] = ((const float4*)(ws + W_SUM))[256 + lane];
            su[2] = ((const float4*)(ws + W_SUM))[320 + lane];
            e0 = meta[M_E0 + wv];
        }
        const u32x2 extra = *(const u32x2*)(meta + M_LOG);
        load_md(0, -1, 0);
        {
            const u32x4 bi = {BIAS, BIAS, BIAS, BIAS};
#pragma unroll
            for (int k = 0; k < 6; k++) *(LDS u32x4*)(uintptr_t)(A_ACC + (k * NT + threadIdx.x) * 16) = bi;
            if (threadIdx.x < 128) *(LDS u32x4*)(uintptr_t)(A_ACC + (6 * NT + threadIdx.x) * 16) = bi;
            __builtin_amdgcn_sched_barrier(0);
        }
#pragma unroll
        for (int i = 0; i < 2; i++) ring[i] = load_main(i, 0);
        __builtin_amdgcn_sched_barrier(0);
        list1(0);
        if (wv < 4) {
            const uint32_t mxb = wave_max(max(max(sv[0].x & 0xffffu, sv[0].z & 0xffffu), max(sv[1].x & 0xffffu, sv[1].z & 0xffffu)));
            const uint32_t mnb = 0xffffu - (wave_max(max(max(sv[0].x, sv[0].z), max(sv[1].x, sv[1].z))) >> 16);
            const float s2 = wave_sum((__uint_as_float(sv[0].y) + __uint_as_float(sv[0].w)) + (__uint_as_float(sv[1].y) + __uint_as_float(sv[1].w)));
            const float s1 = wave_sum((su[0].x + su[0].y) + (su[0].z + su[0].w));
            const float c1 = wave_sum((su[1].x + su[1].y) + (su[1].z + su[1].w)) * (1.0f / (NWG * 512)), c2 = wave_sum((su[2].x + su[2].y) + (su[2].z + su[2].w)) * (1.0f / (NWG * 512));
            const float rng = fmaxf(fabsf(zf * s1 * c1) + ZSIG * sqrtf(zf * s2 * c2), 2.0f * bf2f(mxb)) * 6.103515625e-05f;
            const uint32_t eb = mxb ? (__float_as_uint(rng) + 0x7fffffu) >> 23 : 127u;
            const bool ok = !mxb || (mxb < 0x4700u && eb >= 107u && eb <= 140u && mnb >= ((eb + 8u) << 7) && c2 >= 0.015625f);
            if (lane == 0) {
                s_flag[8 + wv] = (eb & 0xffu) | (ok ? 0x100u : 0u) | (mxb ? 0x200u : 0u);
                s_flag[12 + wv] = __float_as_uint(__uint_as_float(((eb + e0) & 0xffu) << 23) * (1024.0f / 1023.0f));
                s_flag[wv] = 0u;
            }
        }
        store_md();
#pragma unroll
        for (int i = 2; i < 4; i++) ring[i] = load_main(i, 0);
        WAIT_L(0);
        __syncthreads();
        const uint32_t q0 = __builtin_amdgcn_readfirstlane(s_flag[8 + 2 * cp]), q1 = __builtin_amdgcn_readfirstlane(s_flag[9 + 2 * cp]);
        fast = (q0 & q1 & 0x100u) != 0u;
        need = (q0 >> 9) | ((q1 >> 9) << 1);
        more = (__builtin_amdgcn_readfirstlane(extra.x | extra.y) | (uint32_t)(T & 3)) != 0u;
        mg0 = (((q0 & 0xffu) + 9u) << 23) | 0x400000u;
        mg1 = ((((q1 & 0xffu) + 9u) << 23) | 0x400000u) + ((0u - (mg0 >> 16)) & 0xffffu);
        step(RR, IC<1>(), 0);
#pragma unroll 1
        for (int ff = 1; ff < 15; ff++) step(RR, IC<0>(), ff);
        step(RR, IC<2>(), 15);
    };
    auto irregular = [&](auto f) __attribute__((always_inline)) {
        const float fr = bf2f(freq[0]);
        const char* lg = recs + (size_t)LBASE * 128u;
        const uint16_t* tags = (const uint16_t*)(lg + (size_t)lcap * 128u);
        const uint32_t nl = meta[M_LOG], ns = meta[M_LOG + 1];
#pragma unroll 1
        for (uint32_t q = tid; q < nl * 8u; q += NT) {
            const uint32_t b = tags[q >> 3];
            if ((b & 127u) == (uint32_t)(h * 32 + j)) {
                const uint32_t* un = (const uint32_t*)(lg + (size_t)q * 16u);
                const uint32_t xw = un[3 ^ h];
#pragma unroll 1
                for (int k = 0; k < 3; k++) {
                    const uint32_t ww = un[k ^ h];
                    const float c = (float)((xw >> (k * 11)) & 0x3ffu) * (1.0f / 1023.0f);
                    if (!(ww & WNULL)) f(ww >> 16, ((ww & 0x3fffu) << 6) | (b >> 5), (ww & 0x8000u) ? -c : c);
                }
            }
        }
#pragma unroll 1
        for (uint32_t q = tid; q < ns + (uint32_t)(T & 3); q += NT) {
            const uint32_t t = q < ns ? ((const uint32_t*)tags)[-1 - (int)q] : (uint32_t)(T & ~3) + (q - ns);
            const uint32_t i = (uint32_t)iin[t], o = (uint32_t)iout[t];
            if (((o >> 6) & 31u) == (uint32_t)j && (i & 3u) == (uint32_t)h) f(((o >> 11) << 6) | (o & 63u), i, cosf(fr * bf2f(ang[t])));
        }
        __builtin_amdgcn_s_waitcnt(0x0f70);
    };

    if (h == 0) steps(IC<0>());
    else if (h == 1) steps(IC<1>());
    else if (h == 2) steps(IC<2>());
    else steps(IC<3>());
    if (fast) {
        if (more) irregular([&](uint32_t ol, uint32_t i, float cf) __attribute__((always_inline)) {
            const uint32_t md = mdc[(cp * 64u + (i & 63u)) * SLOTI + ((i >> 14) << 8) + ((i >> 4) & 0xfcu) + ((i >> 12) & 3u)];
            const float c = cf * (1023.0f / 16777216.0f);
            const uint32_t lo = __float_as_uint(__builtin_fmaf(c, h2f(md & 0xffffu), __uint_as_float(mg0)));
            const uint32_t hi = __float_as_uint(__builtin_fmaf(c, h2f(md >> 16), __uint_as_float(mg1)));
            if (cf == cf) guard |= atomicAdd(&acc[ol], (hi << 16) + lo);
            else guard |= HSLOW;
        });
    }
    if (wv == 15) asm volatile("ds_write_b32 %0, v108" :: "v"(vconst<A_FLAG + 20>()) : "memory");
    WAIT_L(0);
    asm volatile("v_or3_b32 %0, %0, v110, v111\n\tv_or3_b32 %0, %0, v112, v113\n\tv_or3_b32 %0, %0, v114, v115\n\tv_or3_b32 %0, %0, v116, v117\n\tv_or_b32 %0, v118, %0" : "+v"(guard));
    __syncthreads();
    const __amdgpu_buffer_rsrc_t rsP = __builtin_amdgcn_make_buffer_rsrc((void*)(partial + (size_t)j * 16 * SLOTO), 0, 16 * SLOTO * 4, 0x00020000);
    const char* pk = recs + (uint32_t)j * (SC * 128u);
    const __amdgpu_buffer_rsrc_t rsK = __builtin_amdgcn_make_buffer_rsrc((void*)pk, 0, (int)(MAINL * 128u), 0x00020000);
#define PKA (SSM * 128u)
#define PKB (32u * SC * 128u)
    auto pk_at = [&](uint32_t i) __attribute__((always_inline)) -> uint32_t {
        const uint32_t c = i >> 9;
        return (c < 12u ? (c >> 2) * SQ + (c & 3u) * SKM : 2u * SSM) * 128u + (i & 511u) * 16u;
    };
    auto factors = [&](int r0, int r1) __attribute__((always_inline)) {
#pragma unroll
        for (int r = r0; r < r1; r++) factor(r, r == 0);
    };
    bool room;
    {
        room = (__builtin_amdgcn_readfirstlane(s_flag[5]) >> (24 + (me ^ 1))) & 1u;
        const __amdgpu_buffer_rsrc_t rsW = __builtin_amdgcn_make_buffer_rsrc((void*)(pk + (uint32_t)cp * PKA + (uint32_t)h * PKB), 0, room ? (int)(MAINL * 128u) : 0, 0x00020000);
        uint32_t big = 0u;
        u32x4 v[7];
#pragma unroll
        for (int k = 0; k < 6; k++) v[k] = acc_vec(k * NT + tid);
        v[6] = (u32x4)(BIAS);
        if (wv < 2) v[6] = acc_vec(6 * NT + tid);
#pragma unroll
        for (int k = 0; k < 6; k++)
            __builtin_amdgcn_raw_buffer_store_b128(v[k], rsW, (uint32_t)(k * NT + tid - me * (SHARE / 4)) < SHARE / 4 ? 0xffffff00u : pk_at((uint32_t)tid) + ((k >> 1) * SQ + (k & 1) * 2u * SKM) * 128u, 0, AUX_SC1);
        if (wv < 2 && me != 7) __builtin_amdgcn_raw_buffer_store_b128(v[6], rsW, 2u * SSM * 128u + (uint32_t)tid * 16u, 0, AUX_SC1);
        __builtin_amdgcn_sched_barrier(0);
#pragma unroll
        for (int k = 0; k < 28; k++) { guard |= v[k >> 2][k & 3]; big |= v[k >> 2][k & 3] + 0x42004200u; }
        if (guard & HSLOW) s_flag[0] = 1u;
        if (big & 0x7c00u) s_flag[2] = 1u;
        if (big & 0x7c000000u) s_flag[3] = 2u;
        __builtin_amdgcn_sched_barrier(0);
        if (fin) factors(0, 3);
    }
    asm volatile("s_waitcnt vmcnt(0)" ::: "memory");
    __syncthreads();
    const bool redo = !fast || __builtin_amdgcn_readfirstlane(s_flag[0]) != 0u || (need & ~__builtin_amdgcn_readfirstlane(s_flag[2] | s_flag[3])) != 0u;
    const bool slow = redo || !room;
    if (slow) {
        float* fs = (float*)acc;
        float w[16];
        load_w(wdown, w);
        const float fr = bf2f(freq[0]);
#pragma unroll 1
        for (int e = 0; e < 2; e++) {
            if (redo) {
                __syncthreads();
                for (int i = tid; i < SLOTO; i += NT) fs[i] = 0.f;
                __syncthreads();
                const __amdgpu_buffer_rsrc_t rsO = __builtin_amdgcn_make_buffer_rsrc((void*)iout, 0, T * 4, 0x00020000);
                const __amdgpu_buffer_rsrc_t rsI = __builtin_amdgcn_make_buffer_rsrc((void*)iin, 0, T * 4, 0x00020000);
#pragma unroll 1
                for (int t0 = tid * 4; t0 < T; t0 += NT * 16) {
                    u32x4 o4[4], i4[4];
#pragma unroll
                    for (int g = 0; g < 4; g++) {
                        o4[g] = __builtin_amdgcn_raw_buffer_load_b128(rsO, (uint32_t)(t0 + g * NT * 4) * 4u, 0, 0);
                        i4[g] = __builtin_amdgcn_raw_buffer_load_b128(rsI, (uint32_t)(t0 + g * NT * 4) * 4u, 0, 0);
                    }
                    uint32_t hit = 0u;
#pragma unroll
                    for (int k = 0; k < 16; k++) hit |= (bfi(3u, i4[k >> 2][k & 3], o4[k >> 2][k & 3]) & 0x7c3u) == (uint32_t)(j * 64 + h) ? 1u << k : 0u;
#pragma unroll 1
                    for (; hit; hit &= hit - 1u) {
                        const int k = __builtin_ctz(hit), t = t0 + (k >> 2) * NT * 4 + (k & 3);
                        if (t >= T) break;
                        const uint32_t o = (uint32_t)iout[t];
                        uint32_t rr[4];
                        md_of(m[iin[t]], w, rr);
                        atomicAdd(&fs[((o >> 11) << 6) | (o & 63u)], cosf(fr * bf2f(ang[t])) * bf2f(cp ? (e ? rr[3] : rr[2]) : (e ? rr[1] : rr[0])));
                    }
                }
                __syncthreads();
            }
            const float un = __uint_as_float(s_flag[12 + 2 * cp + e]);
            for (int i = tid; i < SLOTO / 4; i += NT) {
                u32x4 v = acc_vec(i);
                if (!redo) {
#pragma unroll
                    for (int k = 0; k < 4; k++) v[k] = __float_as_uint((float)((int)((v[k] >> (16 * e)) & 0xffffu) - 0x4000) * un);
                }
                __builtin_amdgcn_raw_buffer_store_b128(v, rsP, (uint32_t)((e * 8 + me) * SLOTO * 4 + i * 16), 0, AUX_SC1);
            }
        }
        load_wl();
        load_d(me);
        load_m(me);
        factors(0, 3);
        asm volatile("s_waitcnt vmcnt(0)" ::: "memory");
        __syncthreads();
    }
    if (tid == NT - 64) {
        const unsigned inc = (slow ? 0x10001u : 1u) << me;
        unsigned v = __hip_atomic_fetch_add(dw, inc, __ATOMIC_RELAXED, __HIP_MEMORY_SCOPE_AGENT) + inc;
        while ((v & 0xffu) != 0xffu) {
            __builtin_amdgcn_s_sleep(1);
            v = __hip_atomic_load(dw, __ATOMIC_RELAXED, __HIP_MEMORY_SCOPE_AGENT);
        }
        s_flag[1] = v;
    }
    if (fin) factors(3, 6);
    __syncthreads();
    if (!fin) return;
    const unsigned sv = __builtin_amdgcn_readfirstlane(s_flag[1]);
    u32x4 p[8];
    {
        const uint32_t po = tid < SHARE / 4 ? pk_at((uint32_t)(me * (SHARE / 4) + tid)) : 0x7fffff00u;
#pragma unroll
        for (int r = 1; r < 8; r++) {
            const uint32_t k = (uint32_t)(me ^ r);
            p[r] = __builtin_amdgcn_raw_buffer_load_b128(rsK, po, ((sv >> (16u + k)) & 1u) ? MAINL * 128u : (k & 1u) * PKA + (k >> 1) * PKB, AUX_SC1);
        }
        __builtin_amdgcn_sched_barrier(0);
    }
    p[0] = (u32x4)(0u);
    if (!slow) p[0] = acc_vec(me * (SHARE / 4) + min(tid, SHARE / 4 - 1));
    uint32_t un[4];
#pragma unroll
    for (int c = 0; c < 4; c++) un[c] = __builtin_amdgcn_readfirstlane(s_flag[12 + c]);
    factors(6, 8);
    __builtin_amdgcn_sched_barrier(0);
    const unsigned flt = (sv >> 16) & 0xffu;
    auto finish = [&](int sh) __attribute__((always_inline)) {
        {
            const int ol = sh * SHARE + tid * 4;
            const uint32_t po = tid < SHARE / 4 ? (uint32_t)ol * 4u : 0x7fffff00u;
            float g[4][4];
            u32x4 a[2] = {p[0] + p[2], p[1] + p[3]}, b[2] = {p[4] + p[6], p[5] + p[7]};
            if (cp) { const u32x4 t = a[0], u = b[0]; a[0] = a[1]; b[0] = b[1]; a[1] = t; b[1] = u; }
#pragma unroll
            for (int c = 0; c < 2; c++) {
                const int nb = 0x10000 - 0x4000 * __builtin_popcount(flt & (0x55u << c));
                const float u0 = __uint_as_float(nb ? un[2 * c] : 0u), u1 = __uint_as_float(nb ? un[2 * c + 1] : 0u);
#pragma unroll
                for (int q = 0; q < 4; q++) {
                    g[q][2 * c] = (float)((int)((a[c][q] & 0xffffu) + (b[c][q] & 0xffffu)) - nb) * u0;
                    g[q][2 * c + 1] = (float)((int)((a[c][q] >> 16) + (b[c][q] >> 16)) - nb) * u1;
                }
            }
            if (__builtin_expect(flt != 0u, 0)) {
#pragma unroll
                for (int c = 0; c < 2; c++) {
#pragma unroll 1
                    for (int k = c; k < 8; k += 2) {
                        if (!((flt >> k) & 1u)) continue;
                        const u32x4 f0 = __builtin_amdgcn_raw_buffer_load_b128(rsP, po, k * SLOTO * 4, AUX_SC1);
                        const u32x4 f1 = __builtin_amdgcn_raw_buffer_load_b128(rsP, po, (8 + k) * SLOTO * 4, AUX_SC1);
#pragma unroll
                        for (int q = 0; q < 4; q++) { g[q][2 * c] += __uint_as_float(f0[q]); g[q][2 * c + 1] += __uint_as_float(f1[q]); }
                    }
                }
            }
            float wu[16];
#pragma unroll
            for (int k = 0; k < 8; k++) {
                const uint32_t x = __builtin_amdgcn_readlane(wl, 40 + k);
                wu[2 * k] = bf2f(x & 0xffffu); wu[2 * k + 1] = bf2f(x >> 16);
            }
            u32x4 ov[2];
#pragma unroll
            for (int q = 0; q < 4; q++) {
#pragma unroll
                for (int k = 0; k < 4; k++) g[q][k] *= fa[q][k >> 1][k & 1];
                const uint32_t m0 = em[q >> 1][(q & 1) * 2], m1 = em[q >> 1][(q & 1) * 2 + 1];
                const float mm[4] = {bf2f(m0 & 0xffff), bf2f(m0 >> 16), bf2f(m1 & 0xffff), bf2f(m1 >> 16)};
                float o[4];
#pragma unroll
                for (int k = 0; k < 4; k++) o[k] = mm[k] + (g[q][0] * wu[k] + g[q][1] * wu[4 + k] + g[q][2] * wu[8 + k] + g[q][3] * wu[12 + k]) * fa[q][2 + (k >> 1)][k & 1];
                ov[q >> 1][(q & 1) * 2] = pk_bf16(o[0], o[1]);
                ov[q >> 1][(q & 1) * 2 + 1] = pk_bf16(o[2], o[3]);
            }
            const __amdgpu_buffer_rsrc_t rsU = __builtin_amdgcn_make_buffer_rsrc((void*)out, 0, E * 8, 0x00020000);
            const uint32_t uo = edge0(sh) * 8u;
            __builtin_amdgcn_raw_buffer_store_b128(ov[0], rsU, uo, 0, AUX_NT);
            __builtin_amdgcn_raw_buffer_store_b128(ov[1], rsU, uo + 256u, 0, AUX_NT);
        }
    };
    finish(me);
}

extern "C" __global__ __launch_bounds__(256) void gen_scatter_kernel(const int* __restrict__ iin, const int* __restrict__ iout,
                                                          const uint16_t* __restrict__ ang, const uint16_t* __restrict__ freq,
                                                          const u32x2* __restrict__ m, const uint16_t* __restrict__ wdown,
                                                          float* __restrict__ agg, int T) {
    float w[16];
    load_w(wdown, w);
    const float fr = bf2f(freq[0]);
    for (long t = (long)blockIdx.x * 256 + threadIdx.x; t < T; t += (long)gridDim.x * 256) {
        const float c = cosf(fr * bf2f(ang[t]));
        uint32_t r[4];
        md_of(m[iin[t]], w, r);
        float* q = agg + (size_t)iout[t] * 4;
#pragma unroll
        for (int k = 0; k < 4; k++) atomicAdd(q + k, c * bf2f(r[k]));
    }
}
__global__ __launch_bounds__(256) void gen_final_kernel(const u32x2* __restrict__ m, const uint16_t* __restrict__ dist,
                                                        const uint16_t* __restrict__ centers, const uint16_t* __restrict__ wrbf,
                                                        const uint16_t* __restrict__ wsbf, const uint16_t* __restrict__ wup,
                                                        const float* __restrict__ agg, u32x2* __restrict__ out, int E) {
    const int e = blockIdx.x * 256 + threadIdx.x;
    if (e >= E) return;
    const float d = bf2f(dist[e]);
    float rs[4] = {0, 0, 0, 0}, rg[4] = {0, 0, 0, 0};
#pragma unroll 1
    for (int r = 0; r < 8; r++) {
        const float x = d - bf2f(centers[r]);
        const float rb = __expf(-10.0f * x * x);
#pragma unroll
        for (int k = 0; k < 4; k++) { rs[k] += rb * bf2f(wsbf[r * 4 + k]); rg[k] += rb * bf2f(wrbf[r * 4 + k]); }
    }
    float wu[16];
    load_w(wup, wu);
    const u32x2 mv = m[e];
    const float mm[4] = {bf2f(mv.x & 0xffff), bf2f(mv.x >> 16), bf2f(mv.y & 0xffff), bf2f(mv.y >> 16)};
    float g[4], o[4];
#pragma unroll
    for (int k = 0; k < 4; k++) g[k] = agg[(size_t)e * 4 + k] * rs[k];
#pragma unroll
    for (int k = 0; k < 4; k++) o[k] = mm[k] + (g[0] * wu[k] + g[1] * wu[4 + k] + g[2] * wu[8 + k] + g[3] * wu[12 + k]) * rg[k];
    const u32x2 ov = {f2bf(o[0]) | (f2bf(o[1]) << 16), f2bf(o[2]) | (f2bf(o[3]) << 16)};
    out[e] = ov;
}

extern "C" void kernel_launch(void* const* d_in, const int* in_sizes, int n_in,
                              void* d_out, int out_size, void* d_ws, size_t ws_size,
                              hipStream_t stream) {
    const u32x2* m = (const u32x2*)d_in[0];
    const int* iin = (const int*)d_in[1];
    const int* iout = (const int*)d_in[2];
    const uint16_t* dist = (const uint16_t*)d_in[3];
    const uint16_t* ang = (const uint16_t*)d_in[4];
    const uint16_t* centers = (const uint16_t*)d_in[5];
    const uint16_t* freq = (const uint16_t*)d_in[6];
    const uint16_t* wrbf = (const uint16_t*)d_in[7];
    const uint16_t* wsbf = (const uint16_t*)d_in[8];
    const uint16_t* wdown = (const uint16_t*)d_in[9];
    const uint16_t* wup = (const uint16_t*)d_in[10];
    const int E = in_sizes[3];
    const int T = in_sizes[1];
    char* ws = (char*)d_ws;
    const int G = T >> 2;
    const int ntile = (G + GR - 1) / GR;
    const int nstat = ntile / NWG - NDYN > 4 ? ntile / NWG - NDYN : 4;
    const int lcap = G / 6 + 2;
    char* recs = ws + W_RECS;
    size_t off = W_RECS;
    const size_t rbytes = (size_t)LBASE * 128 + (size_t)lcap * 130;
    off += rbytes + 256 + (size_t)32 * 16 * SLOTO * 4;
    if (E > EMAX || (E & 3) || E < 1 || rbytes > 0x70000000u || off > ws_size) {
        (void)hipMemsetAsync(ws, 0, (size_t)E * 16, stream);
        gen_scatter_kernel<<<4096, 256, 0, stream>>>(iin, iout, ang, freq, m, wdown, (float*)ws, T);
        gen_final_kernel<<<(E + 255) / 256, 256, 0, stream>>>(m, dist, centers, wrbf, wsbf, wup, (const float*)ws, (u32x2*)d_out, E);
        return;
    }
    int ks = T / (NWG * NB * 24);
    if (ks > KMAX) ks = KMAX;
    const float zf = (float)T / (4.0f * (float)E * (float)E);
    (void)hipFuncSetAttribute((const void*)part_kernel, hipFuncAttributeMaxDynamicSharedMemorySize, P_LDS);
    (void)hipFuncSetAttribute((const void*)acc_kernel, hipFuncAttributeMaxDynamicSharedMemorySize, A_LDS);
    part_kernel<<<NWG, NT, P_LDS, stream>>>(iin, iout, ang, freq, m, wdown, recs, T, E, ntile, nstat, ks, lcap);
    acc_kernel<<<NWG, NT, A_LDS, stream>>>(ws, T, E, ks, lcap, zf, 0, centers, wrbf, wsbf, wup, m, dist, (u32x2*)d_out, iin, iout, ang, wdown, freq);
}
```

```cpp
#include <hip/hip_runtime.h>
#include <hip/hip_bf16.h>
#include <stdint.h>
#pragma clang diagnostic ignored "-Winline-asm"

typedef unsigned int u32x4 __attribute__((ext_vector_type(4)));
typedef unsigned int u32x2 __attribute__((ext_vector_type(2)));
typedef unsigned short u16x2 __attribute__((ext_vector_type(2)));
typedef __bf16 bf16x2 __attribute__((ext_vector_type(2)));
typedef float f32x2 __attribute__((ext_vector_type(2)));
typedef unsigned long long u64;

static __device__ __forceinline__ float bf2f(uint32_t bits16) { return __uint_as_float(bits16 << 16); }
static __device__ __forceinline__ uint32_t f2bf(float f) {
    uint32_t u = __float_as_uint(f);
    if ((u & 0x7fffffffu) > 0x7f800000u) return (u >> 16) | 0x40;
    return (u + 0x7fffu + ((u >> 16) & 1u)) >> 16;
}
static __device__ __forceinline__ float h2f(uint32_t bits16) { return (float)__builtin_bit_cast(_Float16, (unsigned short)bits16); }

#define NB 2048
#define WNULL 0x80000000u
#define CSTEP 0x2aabu
#define CINIT 205u
#define CHV (12u * CSTEP)
#define SLOTO 25088
#define SLOTI 12544
#define EMAX (SLOTO * 32)
#define NT 1024
#define NWG 256
#define KMAX 4
#define NTAIL 4
#define SC 64u
#define SKM (128u * SC + 2u)
#define SSM (4u * SKM + 1u)
#define NNEAR 2u
#define NEARM (0x55555555u << (2u * NNEAR))
#define SSH (128u * SC + 1u)
#define SKH ((16u + NNEAR) * SSH + 2u - (NNEAR & 1u))
#define SQ (16u * SSM)
#define MAINL (3u * SQ)
#define SBASE MAINL
#define SPL (128u * 2u * NWG + 1u)
#define HBASE (SBASE + 8u * SPL)
#define QAT(q) ((q) ? ((q) - 1u) * SQ : HBASE)
#define BPL(b) (QAT(((b) >> 3) & 3u) + ((b) & 7u) + (((b) >> 5) << 3))
#define TST 257u
#define TBASE (HBASE + 3u * SKH + 16u * SSH)
#define TPL (NB * TST)
#define LBASE (TBASE + 2u * TPL)
#define NDYN 3
#define GR 2048
#define M_DONE 0
#define M_TILE (32 * 64)
#define M_LOG (M_TILE + 64)
#define M_MD (M_LOG + 64)
#define M_BEAT (M_MD + 64)
#define M_E0 (M_BEAT + 64)
#define M_WORDS (M_E0 + 64)
#define W_TCNT (M_WORDS * 4)
#define W_STAT (W_TCNT + NWG * NB)
#define W_SUM (W_STAT + 4 * NWG * 8)
#define W_MDC (W_SUM + 6 * NWG * 4)
#define W_RECS (W_MDC + 128 * SLOTI * 4)
#define AUX_NT 2
#define AUX_SC1 16

static __device__ __forceinline__ uint32_t row_max(uint32_t x) {
    x = max(x, (uint32_t)__builtin_amdgcn_update_dpp(0, x, 0x111, 0xf, 0xf, false));
    x = max(x, (uint32_t)__builtin_amdgcn_update_dpp(0, x, 0x112, 0xf, 0xf, false));
    x = max(x, (uint32_t)__builtin_amdgcn_update_dpp(0, x, 0x114, 0xf, 0xf, false));
    return max(x, (uint32_t)__builtin_amdgcn_update_dpp(0, x, 0x118, 0xf, 0xf, false));
}
static __device__ __forceinline__ uint32_t wave_max(uint32_t x) {
    x = row_max(x);
    x = max(x, (uint32_t)__builtin_amdgcn_update_dpp(0, x, 0x142, 0xa, 0xf, false));
    x = max(x, (uint32_t)__builtin_amdgcn_update_dpp(0, x, 0x143, 0xc, 0xf, false));
    return __builtin_amdgcn_readlane(x, 63);
}
static __device__ __forceinline__ float row_sum(float x) {
    x += __int_as_float(__builtin_amdgcn_update_dpp(0, __float_as_int(x), 0x111, 0xf, 0xf, false));
    x += __int_as_float(__builtin_amdgcn_update_dpp(0, __float_as_int(x), 0x112, 0xf, 0xf, false));
    x += __int_as_float(__builtin_amdgcn_update_dpp(0, __float_as_int(x), 0x114, 0xf, 0xf, false));
    return x + __int_as_float(__builtin_amdgcn_update_dpp(0, __float_as_int(x), 0x118, 0xf, 0xf, false));
}
static __device__ __forceinline__ float wave_sum(float x) {
    x = row_sum(x);
    x += __int_as_float(__builtin_amdgcn_update_dpp(0, __float_as_int(x), 0x142, 0xa, 0xf, false));
    x += __int_as_float(__builtin_amdgcn_update_dpp(0, __float_as_int(x), 0x143, 0xc, 0xf, false));
    return __int_as_float(__builtin_amdgcn_readlane(__float_as_int(x), 63));
}
static __device__ __forceinline__ uint32_t bfi(uint32_t mask, uint32_t a, uint32_t b) {
    return __builtin_amdgcn_bitop3_b32(mask, a, b, 0xca);
}
template <uint32_t C> static __device__ __forceinline__ uint32_t vconst() {
    uint32_t r;
    asm volatile("v_mov_b32 %0, %1" : "=v"(r) : "n"(C));
    return r;
}
static __device__ __forceinline__ uint32_t pk_bf16(float a, float b) {
    const f32x2 v = {a, b};
    return __builtin_bit_cast(uint32_t, __builtin_convertvector(v, bf16x2));
}
static __device__ __forceinline__ void pin(uint32_t& x) { asm volatile("" : "+v"(x)); }


static __device__ __forceinline__ void md_of(u32x2 v, const float* w, uint32_t* r) {
    float a0 = bf2f(v.x & 0xffff), a1 = bf2f(v.x >> 16), a2 = bf2f(v.y & 0xffff), a3 = bf2f(v.y >> 16);
#pragma unroll
    for (int j = 0; j < 4; j++) r[j] = f2bf(a0 * w[j] + a1 * w[4 + j] + a2 * w[8 + j] + a3 * w[12 + j]);
}
static __device__ __forceinline__ void load_w(const uint16_t* __restrict__ wdown, float* w) {
    u32x4 wv0 = *(const u32x4*)wdown, wv1 = *(const u32x4*)(wdown + 8);
#pragma unroll
    for (int i = 0; i < 4; i++) {
        w[2 * i] = bf2f(wv0[i] & 0xffffu); w[2 * i + 1] = bf2f(wv0[i] >> 16);
        w[8 + 2 * i] = bf2f(wv1[i] & 0xffffu); w[8 + 2 * i + 1] = bf2f(wv1[i] >> 16);
    }
}
#define LDS __attribute__((address_space(3)))
#define P_CNT 0
#define P_FLAG ((NB + 64) * 4)
#define P_LST (P_FLAG + 256)
#define LCAP 32
#define P_TAB (P_LST + 4 * LCAP * 16)
#define P_BUF (P_TAB + NB * 4)
#define P_LDS (P_BUF + NB * 64)
#define PD 2
#define LPOS 0x448763
#define SETP(n) __builtin_amdgcn_s_setprio(n)
#define MDL 1
#define MDM 3
#define RMAXREV 40.0f
#define HB 88
#define HREG(S, D) (HB + ((S) & 3) * 10 + (((S) >> 2) + (D)) * 2)

template <int N> struct IC { static constexpr int value = N; };
struct Raw { u32x4 o[2], i[2]; u32x2 a[2]; };
static __device__ __forceinline__ uint32_t lds_ld(uint32_t a) { return *(const LDS uint32_t*)(uintptr_t)a; }
static __device__ __forceinline__ void lds_st(uint32_t a, uint32_t v) { *(LDS uint32_t*)(uintptr_t)a = v; }
static __device__ __forceinline__ void lds_or(uint32_t a, uint32_t v) {
    __hip_atomic_fetch_or((LDS uint32_t*)(uintptr_t)a, v, __ATOMIC_RELAXED, __HIP_MEMORY_SCOPE_WORKGROUP);
}
static __device__ __forceinline__ uint32_t lds_add(uint32_t a, uint32_t v) {
    return __hip_atomic_fetch_add((LDS uint32_t*)(uintptr_t)a, v, __ATOMIC_RELAXED, __HIP_MEMORY_SCOPE_WORKGROUP);
}
static __device__ __forceinline__ uint32_t slot_w(uint32_t ca, uint32_t cv) {
    return (ca << 4) | __builtin_amdgcn_bitop3_b32(cv >> 11, ca, 0x3cu, 0x28);
}
static __device__ __forceinline__ uint32_t slot_x2(uint32_t aw, uint32_t ca) {
    return __builtin_amdgcn_bitop3_b32(aw, ca, 0xcu, 0x72);
}
static __device__ __forceinline__ uint32_t slot_x(uint32_t aw) { return slot_x2(aw, aw >> 4); }
template <int R> static __device__ __forceinline__ u32x2 hget() {
    u32x2 d;
    asm volatile("v_mov_b64 %0, v[%1:%2]" : "=v"(d) : "n"(R), "n"(R + 1));
    return d;
}
static __device__ __forceinline__ u32x2 hget_at(int r) {
    u32x2 d;
    asm volatile("s_set_gpr_idx_on %2, gpr_idx(SRC0)\n\ts_nop 1\n\tv_mov_b32 %0, v88\n\tv_mov_b32 %1, v89\n\ts_set_gpr_idx_off"
                 : "=&v"(d.x), "=&v"(d.y) : "s"(r));
    return d;
}
static __device__ __forceinline__ void hset_at(int r, u32x2 d) {
    asm volatile("s_set_gpr_idx_on %2, gpr_idx(DST)\n\ts_nop 1\n\tv_mov_b32 v88, %0\n\tv_mov_b32 v89, %1\n\ts_set_gpr_idx_off"
                 :: "v"(d.x), "v"(d.y), "s"(r));
}

template <int S, int D> static __device__ __forceinline__ uint32_t take(uint32_t V, uint32_t u16, uint32_t la, u32x2 nul) {
    const uint32_t a = __builtin_amdgcn_bitop3_b32(V, u16, 15u, 0x54);
    asm volatile("v_mov_b64 v[%4:%4+1], v[%3:%3+1]\n\tv_cmpx_ne_u32 vcc, -16, %0\n\tds_wrxchg_rtn_b64 v[%4:%4+1], %1, %2 offset:%5\n\ts_mov_b64 exec, -1"
                 :: "v"(a), "v"(la), "v"(nul), "n"(HREG(S, D)), "n"(HREG(S, 1 - D)), "n"(S * 256) : "memory", "vcc");
    return a;
}
#define PUT(P) asm volatile("s_bitcmp1_b32 %3, %4\n\ts_cbranch_scc0 1f\n\ts_waitcnt lgkmcnt(%5)\n\tbuffer_store_dwordx4 v[%2:%2+3], %0, %1, 0 offen sc1\n" \
                 "1:\n\ts_bitcmp1_b32 %3, %4+1\n\ts_cbranch_scc0 2f\n\ts_waitcnt lgkmcnt(%5)\n\tbuffer_store_dwordx4 v[%2:%2+3], %0, %1, 0 offen " P "\n\ts_nop 1\n2:" \
                 :: "v"(ad), "s"(rsA), "n"(HREG(S, 0)), "s"(pm), "n"(2 * S), "n"(W) : "memory", "scc")
template <int S, int W, bool WT> static __device__ __forceinline__ void put(u32x4 rsA, uint32_t ad, uint32_t pm) {
    if (WT) PUT("sc1 nt"); else PUT("nt");
}

#define CODE_AHEAD(SYM, NEXT, LANE) ({ u64 ca_; int cb_; \
    asm volatile("s_getpc_b64 vcc\n\ts_add_u32 vcc_lo, vcc_lo, " #SYM "@rel32@lo+4\n\ts_addc_u32 vcc_hi, vcc_hi, " #SYM "@rel32@hi+12\n\t" \
                 "s_mov_b64 %0, vcc\n\ts_max_i32 %1, " #NEXT "-" #SYM ", 0" : "=s"(ca_), "=s"(cb_) :: "scc", "vcc"); \
    __builtin_amdgcn_raw_buffer_load_b32(__builtin_amdgcn_make_buffer_rsrc((void*)ca_, 0, cb_, 0x00020000), ((blockIdx.x >> 3) * 64u + (LANE)) * 64u, 0, 0); })
extern "C" __global__ __launch_bounds__(NT, 4) __attribute__((amdgpu_num_vgpr(HB / 2))) void part_kernel(const int* __restrict__ iin, const int* __restrict__ iout,
                                                  const uint16_t* __restrict__ ang, const uint16_t* __restrict__ freq,
                                                  const u32x2* __restrict__ m, const uint16_t* __restrict__ wdown,
                                                  char* __restrict__ recs,
                                                  int T, int E, int ntile, int nstat, int ks, int lcap) {
    asm volatile("" :: "s"(iin), "s"(iout), "s"(ang), "s"(freq), "s"(m), "s"(wdown), "s"(recs), "s"(T), "s"(E), "s"(ntile), "s"(nstat), "s"(ks), "s"(lcap));
    const int tid0 = threadIdx.x;
    const int wv = __builtin_amdgcn_readfirstlane(tid0 >> 6);
    const int G = T >> 2;
    const int pos = (int)((blockIdx.x & 7) << 5) | (int)(blockIdx.x >> 3);
    float frr;
    unsigned* meta = (unsigned*)(recs - W_RECS);
    const uint32_t gbase = (LBASE + (uint32_t)lcap) * 128u;
    const uint32_t rbytes = gbase + (uint32_t)lcap * 2u;
    const __amdgpu_buffer_rsrc_t rsR = __builtin_amdgcn_make_buffer_rsrc(recs, 0, (int)rbytes, 0x00020000);
    const u32x4 rsA = {(uint32_t)(uintptr_t)recs, (uint32_t)((uintptr_t)recs >> 32) & 0xffffu, rbytes, 0x00020000u};
    const uint32_t g2 = 2u * (uint32_t)ks;
    const uint32_t far = 1u >> ((blockIdx.x >> 3) & 3u);
    const uint32_t SK = SKM + far * (SKH - SKM), SS = SSM + far * (SSH - SSM), UINC = 0x80000001u + SK * 64u;
    const uint32_t spec0 = gbase - 4u;
    auto fixed_tile = [&](int r) __attribute__((always_inline)) -> int { return r * NWG + ((pos + r * 37) & (NWG - 1)); };
    int tl0 = fixed_tile(0), tl1 = fixed_tile(1), tl2;
    uint32_t tk = 0u;
    const __amdgpu_buffer_rsrc_t rsO = __builtin_amdgcn_make_buffer_rsrc((void*)iout, 0, G * 16, 0x00020000);
    const __amdgpu_buffer_rsrc_t rsI = __builtin_amdgcn_make_buffer_rsrc((void*)iin, 0, G * 16, 0x00020000);
    const __amdgpu_buffer_rsrc_t rsG = __builtin_amdgcn_make_buffer_rsrc((void*)ang, 0, G * 8, 0x00020000);
    uint32_t t8 = (uint32_t)tid0 * 8u;
    auto load_one = [&](Raw& r, int tl, int idx) __attribute__((always_inline)) {
        const int j = idx < 4 ? idx >> 1 : idx & 1;
        const uint32_t g0 = (uint32_t)(tl * GR + j * NT);
        __builtin_amdgcn_sched_barrier(0);
        if (idx >= 4) r.a[j] = __builtin_amdgcn_raw_buffer_load_b64(rsG, t8, g0 * 8u, AUX_NT);
        else if (idx & 1) r.i[j] = __builtin_amdgcn_raw_buffer_load_b128(rsI, t8 + t8, g0 * 16u, AUX_NT);
        else r.o[j] = __builtin_amdgcn_raw_buffer_load_b128(rsO, t8 + t8, g0 * 16u, AUX_NT);
        __builtin_amdgcn_sched_barrier(0);
    };
    auto peek = [&](uint32_t at) __attribute__((always_inline)) -> uint32_t {
        return __hip_atomic_load((unsigned*)((char*)meta + at), __ATOMIC_RELAXED, __HIP_MEMORY_SCOPE_AGENT);
    };
    auto ready = [&]() __attribute__((always_inline)) {
        if (blockIdx.x != 0) {
            const uint32_t x = lds_ld(vconst<P_FLAG + 8>());
            while (peek(vconst<M_BEAT * 4>()) == x) __builtin_amdgcn_s_sleep(8);
        }
    };
    auto beat = [&]() __attribute__((always_inline)) {
        (void)__hip_atomic_fetch_add((unsigned*)((char*)meta + vconst<M_BEAT * 4>()), vconst<1>(), __ATOMIC_RELAXED, __HIP_MEMORY_SCOPE_AGENT);
    };
    auto draw = [&](uint32_t at) __attribute__((always_inline)) -> uint32_t {
        return __hip_atomic_fetch_add((unsigned*)((char*)meta + at), vconst<1>(), __ATOMIC_RELAXED, __HIP_MEMORY_SCOPE_AGENT);
    };
    uint32_t la;
    auto tab_at = [&]() __attribute__((always_inline)) -> uint32_t {
        return P_TAB + bfi(0xffu, bfi(0x3cu, t8 >> 2, t8 << 4), t8);
    };
    u32x2 nul;
    uint32_t U[2];
    {
        const uint32_t t = (uint32_t)tid0;
        la = P_BUF + ((t >> 5) << 12) + ((t & 0x18u) << 3) + ((t & 3u) << 4) + ((t & 4u) << 1);
        const uint32_t q = ((t >> 3) ^ (t >> 1 & 2u)) & 3u;
        nul.x = (0x7u >> q) << 31; nul.y = (0xbu >> q) << 31;
        U[0] = (((t >> 1) & 15u) * SS + ((t & 1u) * 64u + (t >> 5)) * SC + BPL(blockIdx.x)) * 128u + (0x80000000u - SK * 64u) + 8u - g2;
        U[1] = U[0] + 32u * SC * 128u;
    }
    auto log_line = [&](bool lg, uint32_t b) __attribute__((always_inline)) -> uint32_t {
        uint32_t lp = 0u;
        if (lg && (t8 & 0x38u) == 0u) {
            ready();
            lp = draw(vconst<M_LOG * 4>());
            __builtin_amdgcn_raw_buffer_store_b16((uint16_t)b, rsR, gbase + lp * 2u, 0, 0);
        }
        return LBASE + (uint32_t)__builtin_amdgcn_ds_bpermute((int)((t8 >> 1) & 0xe0u), (int)lp);
    };
    auto general = [&](int D, bool all, u64 sm) __attribute__((always_inline)) {
        uint32_t todo = __builtin_amdgcn_readfirstlane((uint32_t)sm | (uint32_t)(sm >> 32));
#pragma unroll 1
        while (todo) {
            const uint32_t S = (uint32_t)__builtin_ctz(todo) >> 1;
            todo &= ~(3u << (2u * S));
            pin(t8); pin(la);
            const uint32_t V = lds_ld(P_TAB + (t8 & ~63u) + S * 4u), g = V & 15u;
            const uint32_t col = S * 128u + ((t8 >> 1) & 0x60u) + (t8 >> 8);
            const bool act = all ? V != 0xffffffffu : (V & 0x80000009u) == 9u;
            const int hr = HREG((int)S, D) - HB;
            uint32_t ln = log_line(act && g > 8u + NTAIL, col);
            if (act) {
                const u32x2 N = *(const LDS u32x2*)(uintptr_t)(la + S * 256u);
                *(LDS u32x2*)(uintptr_t)(la + S * 256u) = nul;
                lds_st(P_TAB + (t8 & ~63u) + S * 4u, vconst<0xffffffffu>());
                if (!(g & 1u)) hset_at(hr, N);
                else {
                    uint32_t a, st;
                    if (g < 8u) { a = (V & ~15u) + ((t8 & 0x38u) << 1); st = 8u; }
                    else {
                        if (g <= 8u + NTAIL) ln = TBASE + ((g - 8u) >> 1) * TPL + col * TST + blockIdx.x;
                        a = ln * 128u + (la & 0x38u); st = 64u;
                    }
                    __builtin_amdgcn_raw_buffer_store_b64(hget_at(hr), rsR, a, 0, AUX_SC1);
                    __builtin_amdgcn_raw_buffer_store_b64(N, rsR, a + st, 0, AUX_SC1);
                }
            }
        }
    };
    unsigned epoch = 0;
    u64 pb2 = 0ull;
    u64 fm = 0ull, sm = 0ull;
    auto look = [&](u32x2 c) __attribute__((always_inline)) {
        uint32_t V[2];
        const bool late = __builtin_amdgcn_ballot_w64(((U[0] | U[1]) & 8u) != 0u) != 0ull;
        epoch++;
        asm volatile("v_mov_b32 %0, -1\n\tv_mov_b32 %1, -1\n\t"
                     "v_cmpx_le_u32 %6, %7, %2\n\tv_mov_b32 %0, %4\n\tv_add_u32 %4, %9, %4\n\tv_subrev_u32 %2, %8, %2\n\ts_mov_b64 exec, -1\n\t"
                     "v_cmpx_le_u32 vcc, %8, %3\n\tv_mov_b32 %1, %5\n\tv_add_u32 %5, %9, %5\n\tv_subrev_u32 %3, %8, %3\n\t"
                     "s_or_b64 %6, %6, exec\n\ts_mov_b64 exec, -1"
                     : "=&v"(V[0]), "=&v"(V[1]), "+v"(c.x), "+v"(c.y), "+v"(U[0]), "+v"(U[1]), "=&s"(fm) : "s"(CHV), "n"(CHV), "s"(UINC) : "vcc", "scc");
        *(LDS u32x2*)(uintptr_t)(P_CNT + t8) = c;
        pin(t8);
        const uint32_t tw = tab_at();
        lds_st(tw, V[0]);
        lds_st(tw + 64u, V[1]);
        if (wv == 1) lds_st(P_CNT + NB * 4 - 256 + (t8 >> 1), vconst<0x40000000u>());
        pb2 = __builtin_amdgcn_ballot_w64((int)(V[0] & V[1]) >= 0);
        sm = 0ull;
        if (__builtin_expect(late, 0)) {
            sm = __builtin_amdgcn_ballot_w64((V[0] & 0x80000009u) == 9u || (V[1] & 0x80000009u) == 9u);
#pragma unroll
            for (int e = 0; e < 2; e++)
                if ((V[e] & 0x8000000fu) == 9u + NTAIL) U[e] -= 2u * UINC;
        }
    };
    u32x4 Va, Vb;

    Raw raw[PD];
    {
        const int tid = tid0;
        asm volatile("" ::: "v88", "v127");
        typedef const __attribute__((address_space(4))) u32x4* cptr;
        u32x4 wd0 = ((cptr)(uintptr_t)wdown)[0], wd1 = ((cptr)(uintptr_t)wdown)[1];
        __builtin_amdgcn_sched_barrier(0);
        if (wv == 5) tk = CODE_AHEAD(part_kernel, acc_kernel, (uint32_t)tid & 63u);
        if (tid == 128) tk = peek(vconst<M_BEAT * 4>());
        if (blockIdx.x == 0 && wv == 3 && (tid & 63) < M_BEAT / 64) {
            __hip_atomic_store(meta + (tid & 63) * 64, 0u, __ATOMIC_RELAXED, __HIP_MEMORY_SCOPE_AGENT);
            __hip_atomic_store(meta + (tid & 63) * 64 + 1, 0u, __ATOMIC_RELAXED, __HIP_MEMORY_SCOPE_AGENT);
        }
        const __amdgpu_buffer_rsrc_t rsE = __builtin_amdgcn_make_buffer_rsrc((void*)m, 0, E * 8, 0x00020000);
        u32x2 mv[5];
        mv[4] = __builtin_amdgcn_raw_buffer_load_b64(rsE, (t8 & 511u) * (uint32_t)(E >> 6), 0, 0);
#pragma unroll
        for (int q = 0; q < 4; q++)
            mv[q] = __builtin_amdgcn_raw_buffer_load_b64(rsE, t8 & 511u, ((blockIdx.x >> 2) * 256u + (blockIdx.x & 3u) * 16u + (uint32_t)wv + 64u * q) * 512u, AUX_NT);
#pragma unroll
        for (int d = 0; d < MDL; d++) load_one(raw[1], tl0, d);
        asm volatile("" : "+s"(wd0), "+s"(wd1));
        const u32x2 ci = {CINIT, CINIT};
        *(LDS u32x2*)(uintptr_t)(P_CNT + tid * 8) = ci;
        if (tid < 64) lds_st(P_CNT + (NB + tid) * 4, 0x40000000u);
        if (tid < 8 && tid != 2) lds_st(P_FLAG + tid * 4, tid == 0 ? 0xffffffffu : 0u);
        if (tid == 128) lds_st(P_FLAG + 8, tk);
        const uint32_t lane = (uint32_t)tid & 63u;
        int e0[4];
        {
            float w[16];
#pragma unroll
            for (int i = 0; i < 4; i++) {
                w[2 * i] = bf2f(wd0[i] & 0xffffu); w[2 * i + 1] = bf2f(wd0[i] >> 16);
                w[8 + 2 * i] = bf2f(wd1[i] & 0xffffu); w[8 + 2 * i + 1] = bf2f(wd1[i] >> 16);
            }
            auto dot = [&](u32x2 v, int c) __attribute__((always_inline)) -> f32x2 {
                const f32x2 w0 = {w[c], w[c + 1]}, w1 = {w[4 + c], w[5 + c]}, w2 = {w[8 + c], w[9 + c]}, w3 = {w[12 + c], w[13 + c]};
                return w0 * bf2f(v.x & 0xffffu) + w1 * bf2f(v.x >> 16) + w2 * bf2f(v.y & 0xffffu) + w3 * bf2f(v.y >> 16);
            };
#pragma unroll
            for (int cp = 0; cp < 2; cp++) {
                if (cp == 1) {
#pragma unroll
                    for (int d = MDL; d < MDM; d++) load_one(raw[1], tl0, d);
                }
                if (blockIdx.x >= SLOTI / 64) continue;
                const f32x2 sd = dot(mv[4], 2 * cp);
#pragma unroll
                for (int e = 0; e < 2; e++) {
                    const int ex = (int)(wave_max(__float_as_uint(sd[e]) & 0x7fffffffu) >> 23);
                    e0[2 * cp + e] = __builtin_amdgcn_readfirstlane(ex == 0 ? 0 : min(max(ex - 129, -100), 100));
                }
                const f32x2 sc = {__uint_as_float((uint32_t)(127 - e0[2 * cp]) << 23), __uint_as_float((uint32_t)(127 - e0[2 * cp + 1]) << 23)};
                u32x4 o;
                u16x2 mx = {0, 0};
                f32x2 sq = {0.f, 0.f}, sg = {0.f, 0.f};
#pragma unroll
                for (int q = 0; q < 4; q++) {
                    const f32x2 y = dot(mv[q], 2 * cp) * sc;
                    const uint32_t hb = pk_bf16(y.x, y.y);
                    const f32x2 x = {__uint_as_float(hb << 16), __uint_as_float(hb & 0xffff0000u)};
                    o[q] = __builtin_bit_cast(uint32_t, __builtin_amdgcn_cvt_pkrtz(x.x, x.y));
                    mx = __builtin_elementwise_max(mx, __builtin_bit_cast(u16x2, hb & 0x7fff7fffu));
                    sq += x * x;
                    sg += x;
                }
                *(LDS u32x4*)(uintptr_t)(P_BUF + (cp * 64u + lane) * 272u + wv * 16) = o;
#pragma unroll
                for (int e = 0; e < 2; e++) {
                    const u32x4 st = {wave_max(mx[e]), __float_as_uint(wave_sum(sq[e])), __float_as_uint(wave_sum(sg[e])), 0u};
                    if (lane == 0u) *(LDS u32x4*)(uintptr_t)(P_LST + ((2 * cp + e) * 16 + wv) * 16) = st;
                }
            }
        }
        __builtin_amdgcn_sched_barrier(0);
        const uint16_t fq = freq[0];
#pragma unroll
        for (int d = MDM; d < 6; d++) load_one(raw[1], tl0, d);
#pragma unroll
        for (int d = 0; d < 6; d++)
            if (((LPOS >> (4 * d)) & 15) < 4) load_one(raw[0], tl1, d);
        __syncthreads();
        u32x4 mo[2];
#pragma unroll
        for (int cp = 0; cp < 2; cp++) mo[cp] = *(const LDS u32x4*)(uintptr_t)(P_BUF + (cp * 64u + (t8 >> 7)) * 272u + ((t8 + t8) & 0xf0u));
        if (blockIdx.x == 0 && wv == 3) asm volatile("s_waitcnt vmcnt(0)" ::: "memory");
        __syncthreads();
#pragma unroll
        for (int s = 0; s < 16; s++) *(LDS u32x2*)(uintptr_t)(la + s * 256) = nul;
        asm volatile("" : "+v"(raw[1].o[0]), "+v"(raw[1].o[1]), "+v"(raw[1].i[0]), "+v"(raw[1].i[1]), "+v"(raw[1].a[0]), "+v"(raw[1].a[1]));
        asm volatile("s_nop 0\n\tv_readfirstlane_b32 %0, %1" : "=s"(frr) : "v"(bf2f(fq) * 0.15915494309189535f));
        if (blockIdx.x < SLOTI / 64) {
            const __amdgpu_buffer_rsrc_t rsM = __builtin_amdgcn_make_buffer_rsrc((void*)(meta + W_MDC / 4), 0, (int)(128u * SLOTI * 4u), 0x00020000);
#pragma unroll
            for (int cp = 0; cp < 2; cp++) {
                const uint32_t row = cp * 64u + (t8 >> 7), c16 = (t8 + t8) & 0xf0u;
                __builtin_amdgcn_raw_buffer_store_b128(mo[cp], rsM, row * (SLOTI * 4u) + c16 + blockIdx.x * 256u, 0, AUX_SC1);
            }
            if (wv == 0) {
                const u32x4 st = *(const LDS u32x4*)(uintptr_t)(P_LST + t8 * 2u);
                const float sm1 = row_sum(__uint_as_float(st.z));
                const u32x2 r = {row_max(st.x) | (row_max(st.x ? 0xffffu - st.x : 0u) << 16), __float_as_uint(row_sum(__uint_as_float(st.y)))};
                if ((lane & 15u) == 15u) {
                    ((u32x2*)(meta + W_STAT / 4))[(lane >> 4) * NWG + blockIdx.x] = r;
                    meta[W_SUM / 4 + (lane >> 4) * NWG + blockIdx.x] = __float_as_uint(sm1);
                }
                if (blockIdx.x == 0 && lane < 4u) meta[M_E0 + lane] = (uint32_t)(lane == 0u ? e0[0] : lane == 1u ? e0[1] : lane == 2u ? e0[2] : e0[3]);
            }
        }
    }
    pin(la);

    uint32_t w[8], aw[8], xv[8];
    u64 nm[8];
    auto later2 = [&](int k) __attribute__((always_inline)) {
        uint32_t x0, x1;
        asm volatile("v_lshrrev_b32 %0, 4, %2\n\tv_lshrrev_b32 %1, 4, %5\n\tv_bitop3_b32 %0, %2, %0, 12 bitop3:0x72\n\tv_bitop3_b32 %1, %5, %1, 12 bitop3:0x72\n\t"
                     "s_not_b64 exec, %8\n\tds_write_b32 %2, %3 offset:%10\n\tds_or_b32 %0, %4 offset:%10\n\t"
                     "s_not_b64 exec, %9\n\tds_write_b32 %5, %6 offset:%10\n\tds_or_b32 %1, %7 offset:%10\n\ts_mov_b64 exec, -1"
                     : "=&v"(x0), "=&v"(x1)
                     : "v"(aw[k]), "v"(w[k]), "v"(xv[k]), "v"(aw[k + 1]), "v"(w[k + 1]), "v"(xv[k + 1]), "s"(nm[k]), "s"(nm[k + 1]), "n"(P_BUF) : "memory", "scc");
    };
    auto drain = [&]() __attribute__((always_inline)) {
        if (wv == 1) {
            const uint32_t q = (epoch + 1u) & 3u;
            const uint32_t n = __builtin_amdgcn_readfirstlane(lds_ld(vconst<P_FLAG + 16>() + q * 4u));
            if (__builtin_expect(n != 0u, 0)) {
                pin(t8);
                const uint32_t l = (t8 >> 3) & 63u;
                if (l < min(n, (uint32_t)LCAP)) {
                    const u32x4 e = *(const LDS u32x4*)(uintptr_t)(P_LST + (q * LCAP + l) * 16u);
                    lds_st(P_BUF + e.x, e.y);
                    lds_or(P_BUF + slot_x(e.x), e.z);
                }
                lds_st(vconst<P_FLAG + 16>() + q * 4u, vconst<0>());
            }
        }
    };
    auto again = [&](int D) __attribute__((always_inline)) {
        do {
            general(D, true, fm);
            __syncthreads();
            later2(0); later2(2); later2(4); later2(6);
            drain();
#pragma unroll
            for (int k = 0; k < 8; k++) {
                pin(aw[k]);
                bool due = false;
                if (aw[k] & 0x80000000u) {
                    xv[k] -= CHV;
                    if (xv[k] < CHV) {
                        const uint32_t f = (aw[k] >> 16) & 0x3ffu;
                        aw[k] = slot_w(aw[k] & 0xffffu, xv[k]);
                        xv[k] = f << ((xv[k] >> 8) & 31u);
                        due = true;
                    } else lds_st(vconst<P_FLAG>(), epoch + 1u);
                }
                nm[k] = ~__builtin_amdgcn_ballot_w64(due);
            }
            look(*(const LDS u32x2*)(uintptr_t)(P_CNT + t8));
            asm volatile("s_waitcnt lgkmcnt(0)" ::: "memory");
            __syncthreads();
        } while (__builtin_amdgcn_readfirstlane(lds_ld(vconst<P_FLAG>())) == epoch);
    };
    auto round = [&](auto DD, auto LL, Raw& rc, Raw& rn, int rd) __attribute__((always_inline)) {
        constexpr int D = decltype(DD)::value;
        constexpr bool LAST = decltype(LL)::value == 1, FIRST = decltype(LL)::value == 2;
        const int ng = min(GR, G - tl0 * GR);
        auto hook = [&](int p) __attribute__((always_inline)) {
#pragma unroll
            for (int q = 0; q < 6; q++)
            {
                const int lp = (LPOS >> (4 * q)) & 15;
                if (!LAST && lp < 4 && lp == p) load_one(rc, tl2, q);
                if (!LAST && lp >= 4 && (lp < 8 || lp >= 12 ? lp : lp - 8) == p) load_one(rn, tl1, q);
            }
        };
        uint32_t wn[8], ca[8], px[4], cv[8];
        float mx;
        float cs[2], cm[2] = {0.f, 0.f};
        auto convA = [&](int q) __attribute__((always_inline)) {
            if (LAST) return;
            const uint32_t a = rc.a[q >> 1][q & 1];
            const f32x2 av = {__uint_as_float(a << 16), __uint_as_float(a & 0xffff0000u)};
            f32x2 rv;
            asm("v_pk_mul_f32 %0, %1, %2 op_sel_hi:[1,0]" : "=v"(rv) : "v"(av), "s"((u64)__float_as_uint(frr)));
            mx = __builtin_elementwise_maximum(__builtin_elementwise_maximum(__builtin_fabsf(rv.x), __builtin_fabsf(rv.y)), (q & 1) ? mx : 0.f);
            cs[0] = __builtin_amdgcn_cosf(rv.x); cs[1] = __builtin_amdgcn_cosf(rv.y);
            asm volatile("" : "+v"(mx), "+v"(cs[0]), "+v"(cs[1]));
            if (FIRST) {
                const float c0 = fminf(fmaxf(cs[0], -1.f), 1.f), c1 = fminf(fmaxf(cs[1], -1.f), 1.f);
                cm[0] += c0 + c1; cm[1] += c0 * c0 + c1 * c1;
                if (q == 3 && wv == 4) {
                    const float a0 = wave_sum(cm[0]), a1 = wave_sum(cm[1]);
                    if ((t8 & 0x1f0u) == 0u) meta[W_SUM / 4 + (4 + ((t8 >> 3) & 1u)) * NWG + blockIdx.x] = __float_as_uint((t8 & 8u) ? a1 : a0);
                }
            }
            __builtin_amdgcn_sched_barrier(0);
        };
        auto convB = [&](int k) __attribute__((always_inline)) {
            if (LAST) return;
            const uint32_t o = rc.o[k >> 2][k & 3], i = rc.i[k >> 2][k & 3];
            wn[k] = (__builtin_amdgcn_alignbit(bfi(63u, o, o >> 5), __float_as_uint(cs[k & 1]), 30u) << 14) | (i >> 6);
            ca[k] = (bfi(63u, i, o) << 2) & 0x1ffcu;
            pin(wn[k]); pin(ca[k]);
            __builtin_amdgcn_sched_barrier(0);
        };
        auto convD = [&](int q) __attribute__((always_inline)) {
            if (LAST) return;
            asm volatile("v_cvt_pk_f16_f32 %0, |%1|, |%2|\n\tv_pk_mul_f16 %0, %0, %3 op_sel_hi:[1,0]" : "=v"(px[q]) : "v"(cs[0]), "v"(cs[1]), "s"(0x3ffu));
        };
        uint32_t cst;
        auto check = [&](int j) __attribute__((always_inline)) {
            if (LAST) return;
            if (__builtin_expect((__builtin_amdgcn_ballot_w64(!(mx <= RMAXREV)) | (u64)(uint32_t)(GR - ng)) != 0ull, 0)) {
                pin(t8);
                asm volatile("" : "+v"(rc.a[j]));
                const int lg = j * NT + (int)(t8 >> 3);
#pragma unroll
                for (int k = 0; k < 4; k++) {
                    const uint32_t a = (k & 1) ? (rc.a[j][k >> 1] >> 16) : (rc.a[j][k >> 1] & 0xffffu);
                    const bool nrm = __builtin_fabsf(frr * bf2f(a)) <= RMAXREV;
                    if (!(lg < ng && nrm)) ca[j * 4 + k] = NB * 4u + ((t8 >> 1) & 0xfcu);
                    if (lg < ng && !nrm) {
                        ready();
                        __builtin_amdgcn_raw_buffer_store_b32((uint32_t)((tl0 * GR + lg) * 4 + k), rsR, spec0 - draw(vconst<M_LOG * 4 + 4>()) * 4u, 0, 0);
                    }
                }
            }
            cst = vconst<CSTEP>();
        };
        auto ticket = [&](int k) __attribute__((always_inline)) {
            if (LAST) return;
            cv[k] = lds_add(P_CNT + ca[k], cst);
            __builtin_amdgcn_sched_barrier(0);
        };
        auto insert = [&](int j) __attribute__((always_inline)) {
            const int k0 = 4 * j;
            const bool odd = __builtin_amdgcn_ballot_w64(max(max(cv[k0], cv[k0 + 1]), max(cv[k0 + 2], cv[k0 + 3])) >= 2u * CHV) != 0ull;
#pragma unroll
            for (int k = k0; k < k0 + 4; k += 2) {
                uint32_t x0, x1;
                asm volatile("v_lshrrev_b32 %6, 11, %8\n\tv_lshrrev_b32 %7, 11, %9\n\t"
                             "v_bitop3_b32 %6, %6, %10, 60 bitop3:0x28\n\tv_bitop3_b32 %7, %7, %11, 60 bitop3:0x28\n\t"
                             "v_lshl_or_b32 %0, %10, 4, %6\n\tv_lshl_or_b32 %1, %11, 4, %7\n\t"
                             "v_lshlrev_b32_sdwa %2, %8, %12 dst_sel:DWORD dst_unused:UNUSED_PAD src0_sel:BYTE_1 src1_sel:WORD_0\n\t"
                             "v_lshlrev_b32_sdwa %3, %9, %12 dst_sel:DWORD dst_unused:UNUSED_PAD src0_sel:BYTE_1 src1_sel:WORD_1\n\t"
                             "v_bitop3_b32 %6, %0, %10, 12 bitop3:0x72\n\tv_bitop3_b32 %7, %1, %11, 12 bitop3:0x72\n\t"
                             "v_cmpx_gt_u32 %4, %15, %8\n\tds_write_b32 %0, %13 offset:%16\n\tds_or_b32 %6, %2 offset:%16\n\ts_mov_b64 exec, -1\n\t"
                             "v_cmpx_gt_u32 %5, %15, %9\n\tds_write_b32 %1, %14 offset:%16\n\tds_or_b32 %7, %3 offset:%16\n\ts_mov_b64 exec, -1"
                             : "=&v"(aw[k]), "=&v"(aw[k + 1]), "=&v"(xv[k]), "=&v"(xv[k + 1]), "=&s"(nm[k]), "=&s"(nm[k + 1]), "=&v"(x0), "=&v"(x1)
                             : "v"(cv[k]), "v"(cv[k + 1]), "v"(ca[k]), "v"(ca[k + 1]), "v"(px[k >> 1]), "v"(wn[k]), "v"(wn[k + 1]), "s"(CHV), "n"(P_BUF) : "memory");
                w[k] = wn[k]; w[k + 1] = wn[k + 1];
                if (__builtin_expect(odd, 0)) {
#pragma unroll
                    for (int q = k; q < k + 2; q++) {
                        nm[q] |= __builtin_amdgcn_ballot_w64(cv[q] >= 2u * CHV);
                        if (cv[q] >= 2u * CHV && cv[q] < 0x40000000u) {
                            const uint32_t lq = (epoch + 2u) & 3u;
                            uint32_t p = LCAP;
                            if (cv[q] < 3u * CHV) p = lds_add(vconst<P_FLAG + 16>() + lq * 4u, vconst<1>());
                            if (p < LCAP) {
                                const u32x2 e = {aw[q], w[q]};
                                *(LDS u32x2*)(uintptr_t)(P_LST + (lq * LCAP + p) * 16u) = e;
                                lds_st(P_LST + (lq * LCAP + p) * 16u + 8u, xv[q]);
                            } else {
                                aw[q] = ca[q] | ((q & 1) ? (px[q >> 1] & 0xffff0000u) : (px[q >> 1] << 16)) | 0x80000000u;
                                xv[q] = cv[q] - CHV;
                                lds_st(vconst<P_FLAG>(), epoch);
                            }
                        }
                    }
                }
            }
        };
        constexpr int P0 = D ? 0 : 3, P1 = D ? 1 : 2, P2 = D ? 2 : 1, P3 = D ? 3 : 0;
        const uint32_t fl = lds_ld(vconst<P_FLAG>());
        if (!LAST) asm volatile("" : "+v"(rc.o[0]), "+v"(rc.o[1]), "+v"(rc.i[0]), "+v"(rc.i[1]), "+v"(rc.a[0]), "+v"(rc.a[1]));
        if (!LAST && (rd + 4 >= nstat || tl1 >= ntile) && wv == 2) {
            pin(tk);
            if ((t8 & 511u) == 0u) {
                if (tl1 >= ntile) {
                    if (rd + 3 <= nstat) ready();
                    (void)draw(vconst<M_MD * 4>());
                } else if (rd + 4 == nstat) tk = peek(vconst<M_BEAT * 4>());
                else {
                    if (rd + 3 == nstat) {
                        if (tk == lds_ld(vconst<P_FLAG + 8>())) ready();
                        tk = 0u;
                    }
                    lds_st(vconst<P_FLAG + 4>(), tk);
                    if ((int)tk < ntile - nstat * NWG) tk = draw(vconst<M_TILE * 4>());
                }
            }
        }
        if (!LAST && blockIdx.x == 0 && wv == 3 && (t8 & 511u) == 0u) beat();
        hook(12);
        pin(t8);
        const uint32_t tr = P_TAB + (t8 & ~63u), u16 = (t8 + t8) & 0x70u;
        convA(0);
        convB(0);
        const bool ag = __builtin_amdgcn_readfirstlane(fl) == epoch;
        if (!FIRST && __builtin_expect(LAST || ag || sm != 0ull, 0)) {
            if (ag) again(D);
            general(D, false, sm);
            Va = *(const LDS u32x4*)(uintptr_t)(P_TAB + (t8 & ~63u) + P0 * 16);
            Vb = *(const LDS u32x4*)(uintptr_t)(P_TAB + (t8 & ~63u) + P1 * 16);
            asm volatile("" : "+v"(Va), "+v"(Vb));
        }
        uint32_t pq = (uint32_t)pb2 | (uint32_t)(pb2 >> 32);
        pq = (pq | (pq >> 1)) & 0x55555555u;
        pq = __builtin_amdgcn_readfirstlane(pq + (pq & (far * NEARM)));
        uint32_t ad[8];
        if (!FIRST) ad[0] = take<4 * P0 + 0, D>(Va[0], u16, la, nul);
        convB(1);
        if (!FIRST) ad[1] = take<4 * P0 + 1, D>(Va[1], u16, la, nul);
        convD(0);
        convA(1);
        if (!FIRST) ad[2] = take<4 * P0 + 2, D>(Va[2], u16, la, nul);
        convB(2);
        hook(13);
        if (!FIRST) ad[3] = take<4 * P0 + 3, D>(Va[3], u16, la, nul);
        convB(3);
        if (!FIRST) put<4 * P0 + 0, LAST ? 3 : 3, LAST>(rsA, ad[0], pq);
        if (!FIRST) ad[4] = take<4 * P1 + 0, D>(Vb[0], u16, la, nul);
        convD(1);
        check(0);
        ticket(0);
        hook(4);
        if (!FIRST) put<4 * P0 + 1, LAST ? 3 : 4, LAST>(rsA, ad[1], pq);
        if (!FIRST) ad[5] = take<4 * P1 + 1, D>(Vb[1], u16, la, nul);
        convA(2);
        ticket(1);
        if (!FIRST) put<4 * P0 + 2, LAST ? 3 : 5, LAST>(rsA, ad[2], pq);
        if (!FIRST) ad[6] = take<4 * P1 + 2, D>(Vb[2], u16, la, nul);
        convB(4);
        ticket(2);
        if (!FIRST) put<4 * P0 + 3, LAST ? 3 : 6, LAST>(rsA, ad[3], pq);
        if (!FIRST) Va = *(const LDS u32x4*)(uintptr_t)(tr + P2 * 16);
        __builtin_amdgcn_sched_barrier(0);
        if (!FIRST) ad[7] = take<4 * P1 + 3, D>(Vb[3], u16, la, nul);
        convB(5);
        ticket(3);
        SETP(2);
        if (!FIRST) put<4 * P1 + 0, LAST ? 4 : 8, LAST>(rsA, ad[4], pq);
        if (!FIRST) ad[0] = take<4 * P2 + 0, D>(Va[0], u16, la, nul);
        convD(2);
        convA(3);
        hook(5);
        if (!FIRST) put<4 * P1 + 1, LAST ? 4 : 7, LAST>(rsA, ad[5], pq);
        if (!FIRST) ad[1] = take<4 * P2 + 1, D>(Va[1], u16, la, nul);
        convB(6);
        if (!FIRST) put<4 * P1 + 2, LAST ? 4 : 6, LAST>(rsA, ad[6], pq);
        if (!FIRST) ad[2] = take<4 * P2 + 2, D>(Va[2], u16, la, nul);
        convB(7);
        if (!FIRST) put<4 * P1 + 3, LAST ? 3 : 4, LAST>(rsA, ad[7], pq);
        if (!FIRST) Vb = *(const LDS u32x4*)(uintptr_t)(tr + P3 * 16);
        __builtin_amdgcn_sched_barrier(0);
        if (!FIRST) ad[3] = take<4 * P2 + 3, D>(Va[3], u16, la, nul);
        convD(3);
        check(1);
        ticket(4);
        SETP(1);
        hook(6);
        if (!FIRST) put<4 * P2 + 0, LAST ? 4 : 5, LAST>(rsA, ad[0], pq);
        if (!FIRST) ad[4] = take<4 * P3 + 0, D>(Vb[0], u16, la, nul);
        ticket(5);
        if (!FIRST) put<4 * P2 + 1, LAST ? 4 : 6, LAST>(rsA, ad[1], pq);
        if (!FIRST) ad[5] = take<4 * P3 + 1, D>(Vb[1], u16, la, nul);
        ticket(6);
        if (!FIRST) put<4 * P2 + 2, LAST ? 4 : 7, LAST>(rsA, ad[2], pq);
        if (!FIRST) ad[6] = take<4 * P3 + 2, D>(Vb[2], u16, la, nul);
        ticket(7);
        SETP(0);
        if (!FIRST) put<4 * P2 + 3, LAST ? 3 : 7, LAST>(rsA, ad[3], pq);
        if (!FIRST) ad[7] = take<4 * P3 + 3, D>(Vb[3], u16, la, nul);
        hook(7);
        SETP(3);
        asm volatile("s_waitcnt lgkmcnt(0)" ::: "memory");
        __syncthreads();
        u32x2 cn;
        uint32_t tv;
        if (!LAST) { cn = *(const LDS u32x2*)(uintptr_t)(P_CNT + t8); tv = lds_ld(vconst<P_FLAG + 4>()); }
        __builtin_amdgcn_sched_barrier(0);
        if (!FIRST) later2(0);
        if (!FIRST) put<4 * P3, 15, LAST>(rsA, ad[4], pq);
        if (!FIRST) later2(2);
        if (!FIRST) put<4 * P3 + 1, 15, LAST>(rsA, ad[5], pq);
        if (!FIRST) drain();
        if (!LAST) {
            tl2 = rd + 2 < nstat ? fixed_tile(rd + 2) : nstat * NWG + (int)__builtin_amdgcn_readfirstlane(tv);
            look(cn);
        }
        hook(0);
        SETP(2);
        if (!FIRST) later2(4);
        if (!FIRST) put<4 * P3 + 2, 15, LAST>(rsA, ad[6], pq);
        if (!FIRST) later2(6);
        if (!FIRST) put<4 * P3 + 3, 15, LAST>(rsA, ad[7], pq);
        if (!LAST) {
            hook(1);
            SETP(1);
            insert(0);
            hook(2);
            SETP(0);
            pin(t8);
            Va = *(const LDS u32x4*)(uintptr_t)(P_TAB + (t8 & ~63u) + P3 * 16);
            Vb = *(const LDS u32x4*)(uintptr_t)(P_TAB + (t8 & ~63u) + P2 * 16);
            __builtin_amdgcn_sched_barrier(0);
            insert(1);
            hook(3);
            SETP(3);
            asm volatile("s_waitcnt lgkmcnt(0)" ::: "memory");
            __syncthreads();
            tl0 = tl1; tl1 = tl2;
        }
    };
#define HMV(A, B) "v_mov_b64 v[" #A ":" #A "+1], v[" #B ":" #B "+1]\n\t"
#define HMV4(A) HMV(A + 8, A + 6) HMV(A + 6, A + 4) HMV(A + 4, A + 2) HMV(A + 2, A)
    round(IC<1>(), IC<2>(), raw[1], raw[0], 0);
    int rd = 1;
#pragma unroll 1
    for (; tl1 < ntile; rd += 2) {
        round(IC<0>(), IC<0>(), raw[0], raw[1], rd);
        round(IC<1>(), IC<0>(), raw[1], raw[0], rd + 1);
    }
    if (tl0 >= ntile) asm volatile(HMV4(88) HMV4(98) HMV4(108) HMV4(118));
    asm volatile("" : "+s"(tl0));
    if (tl0 < ntile) round(IC<0>(), IC<0>(), raw[0], raw[1], rd);
    if (wv == 5) tk = CODE_AHEAD(acc_kernel, gen_scatter_kernel, (t8 >> 3) & 63u);
    round(IC<1>(), IC<1>(), raw[1], raw[0], 0);
    if (__builtin_amdgcn_readfirstlane(lds_ld(P_FLAG + 16 + ((epoch + 2u) & 3u) * 4u)) != 0u) {
        asm volatile("s_waitcnt lgkmcnt(0)" ::: "memory");
        __syncthreads();
        look(*(const LDS u32x2*)(uintptr_t)(P_CNT + t8));
        general(0, true, fm);
        __syncthreads();
        drain();
    }
    {
        uint32_t* tcnt = meta + W_TCNT / 4;
        const u32x2 c = *(const LDS u32x2*)(uintptr_t)(P_CNT + t8);
        const uint32_t tw = tab_at();
#pragma unroll
        for (int e = 0; e < 2; e++) {
            const uint32_t g = U[e] & 15u;
            lds_st(tw + e * 64u, g >= 8u ? 0x80000000u : ((U[e] + ((g & 1u) ? 0u : SK * 64u)) & 0x7fffff81u) | (((7u - g) << 1) & 12u));
            *(LDS uint8_t*)(uintptr_t)(P_CNT + (t8 & ~511u) + (((tw - P_TAB) >> 2) & 127u) + e * 16u) =
                (uint8_t)(g < 8u ? 0u : ((g - 8u) >> 1) + (((c[e] + 2u * CSTEP) >> 15) << 2) + (g & 1u) * 0x50u);
        }
        asm volatile("s_waitcnt lgkmcnt(0)" ::: "memory");
        __syncthreads();
        const uint32_t u16 = (t8 + t8) & 0x70u;
        u32x4 En[4];
#pragma unroll
        for (int p = 0; p < 4; p++) En[p] = *(const LDS u32x4*)(uintptr_t)(P_TAB + (t8 & ~63u) + p * 16u);
        const u32x4 cb = *(const LDS u32x4*)(uintptr_t)(P_CNT + (t8 & ~511u) + ((t8 >> 2) & 0x70u));
        u32x2 N[16];
#pragma unroll
        for (int s = 0; s < 16; s++) N[s] = *(const LDS u32x2*)(uintptr_t)(la + s * 256u);
        if ((t8 & 0x1c0u) == 0u)
            __builtin_amdgcn_raw_buffer_store_b128(*(const LDS u32x4*)(uintptr_t)(P_CNT + (t8 & ~511u) + u16), __builtin_amdgcn_make_buffer_rsrc((void*)tcnt, 0, NWG * NB, 0x00020000),
                                                   (blockIdx.x * 16u + (uint32_t)wv) * 128u + u16, 0, AUX_SC1);
        const uint32_t sg = P_BUF + ((t8 & ~63u) << 4);
        uint32_t ps;
        auto rest = [&](auto SL) __attribute__((always_inline)) {
            constexpr int s = decltype(SL)::value;
            const uint32_t en = En[s >> 2][s & 3], by = cb[s >> 2] >> ((s & 3) * 8);
            const u32x2 Hr = hget<HREG(s, 0)>();
            const uint32_t at = ps + ((la & 0x38u) ^ ((s & 3) << 4));
            *(LDS u32x2*)(uintptr_t)at = Hr;
            *(LDS u32x2*)(uintptr_t)(at + (by & 0x40u)) = N[s];
            ps += (by & 0x3cu) << 2;
            const u32x2 Hs = (en & 1u) ? Hr : nul;
            const u32x4 d = {Hs.x, Hs.y, N[s].x, N[s].y};
            uint32_t a = __builtin_amdgcn_bitop3_b32(en, u16, 15u, 0x54);
            {
                const u32x4 rs_ = rsA;
                const uint32_t far_ = ((NEARM >> (2 * s)) & 1u) ? far : 0u;
                asm volatile("s_cmp_eq_u32 %3, 0\n\ts_cbranch_scc0 1f\n\tbuffer_store_dwordx4 %0, %1, %2, 0 offen sc1\n\ts_branch 2f\n"
                             "1:\n\tbuffer_store_dwordx4 %0, %1, %2, 0 offen sc1 nt\n2:\n\ts_nop 1" :: "v"(d), "v"(a), "s"(rs_), "s"(far_) : "memory", "scc");
            }
            if (__builtin_expect(__builtin_amdgcn_ballot_w64((en & 12u) != 0u) != 0ull, 0)) {
                const u32x4 dn = {nul.x, nul.y, nul.x, nul.y};
#pragma unroll 1
                for (uint32_t k = (en >> 2) & 3u; k > 0u; k--) {
                    a += SK * 128u;
                    __builtin_amdgcn_raw_buffer_store_b128(dn, rsR, a, 0, AUX_SC1);
                }
            }
        };
        auto pack = [&](int p) __attribute__((always_inline)) {
            const uint32_t a = (SBASE + ((((t8 >> 1) & 0x60u) + (t8 >> 8)) * 2u + p) * NWG + blockIdx.x) * 128u + u16;
            asm volatile("" ::: "memory");
#pragma unroll 1
            for (uint32_t q = 0; __builtin_amdgcn_ballot_w64(sg + q < ps) != 0ull; q += 256u) {
                const u32x4 d0 = *(const LDS u32x4*)(uintptr_t)(sg + q + u16), d1 = *(const LDS u32x4*)(uintptr_t)(sg + q + 128u + u16);
                __builtin_amdgcn_raw_buffer_store_b128(d0, rsR, sg + q < ps ? a + q * SPL : 0xffffff00u, 0, AUX_SC1);
                __builtin_amdgcn_raw_buffer_store_b128(d1, rsR, sg + q + 128u < ps ? a + (q + 128u) * SPL : 0xffffff00u, 0, AUX_SC1);
            }
            asm volatile("" ::: "memory");
        };
        asm volatile("" : "+v"(N[0]), "+v"(N[1]), "+v"(N[2]), "+v"(N[3]), "+v"(N[4]), "+v"(N[5]), "+v"(N[6]), "+v"(N[7]) :: "memory");
        asm volatile("" : "+v"(N[8]), "+v"(N[9]), "+v"(N[10]), "+v"(N[11]), "+v"(N[12]), "+v"(N[13]), "+v"(N[14]), "+v"(N[15]) :: "memory");
        ps = sg;
        rest(IC<0>()); rest(IC<1>()); rest(IC<2>()); rest(IC<3>()); rest(IC<4>()); rest(IC<5>()); rest(IC<6>()); rest(IC<7>());
        pack(0);
        ps = sg;
        rest(IC<8>()); rest(IC<9>()); rest(IC<10>()); rest(IC<11>()); rest(IC<12>()); rest(IC<13>()); rest(IC<14>()); rest(IC<15>());
        pack(1);
    }
    asm volatile("" :: "v"(tk));
    if (blockIdx.x == 0 && t8 == 1536u) {
        do {
            beat();
            __builtin_amdgcn_s_sleep(8);
        } while (peek(vconst<M_MD * 4>()) < NWG);
    }
}

#define A_MD 0
#define A_ACC (SLOTI * 4)
#define A_FLAG (A_ACC + SLOTO * 4)
#define A_LDS (A_FLAG + 512)
static_assert(SLOTI * 4 == 3 * NT * 16 + 1024, "md staging assumes three full vectors and 256 dwords");
#define BIAS 0x40004000u
#define NFAST 8
#define NLD 10
#define MDF 0x0911432
#define MDP 0xaaaaa
#define HP 2
#define SHARE (SLOTO / 8)
#define ZSIG 9.0f
#define HSLOW 0x80008000u
#define GAS __attribute__((address_space(1)))

extern "C" __global__ __launch_bounds__(NT, 4) __attribute__((amdgpu_num_vgpr(53))) void acc_kernel(char* ws, int T, int E, int ks, int lcap, float zf, int pad,
                                                 const uint16_t* __restrict__ centers, const uint16_t* __restrict__ wrbf,
                                                 const uint16_t* __restrict__ wsbf, const uint16_t* __restrict__ wup,
                                                 const u32x2* __restrict__ m, const uint16_t* __restrict__ dist, u32x2* __restrict__ out,
                                                 const int* __restrict__ iin, const int* __restrict__ iout, const uint16_t* __restrict__ ang,
                                                 const uint16_t* __restrict__ wdown, const uint16_t* __restrict__ freq) {
    const char* recs = ws + W_RECS;
    const u32x4* tcnt = (const u32x4*)(ws + W_TCNT);
    const uint32_t* mdc = (const uint32_t*)(ws + W_MDC);
    const u32x4* stat = (const u32x4*)(ws + W_STAT);
    uint32_t* partial = (uint32_t*)(ws + W_RECS + (((size_t)LBASE * 128u + (size_t)lcap * 130u + 255u) & ~(size_t)255u));
    unsigned* meta = (unsigned*)ws;
    extern __shared__ __attribute__((aligned(16))) char smem[];
    unsigned* acc = (unsigned*)(smem + A_ACC);
    unsigned* s_flag = (unsigned*)(smem + A_FLAG);
    const int tid = threadIdx.x;
    const int lane = tid & 63;
    const int wv = __builtin_amdgcn_readfirstlane(tid >> 6);
    const int cp = (blockIdx.x >> 3) & 1;
    const int h = blockIdx.x & 3;
    const int j = ((blockIdx.x >> 2) & 1) * 16 + (blockIdx.x >> 4);
    u32x4 tc;
    const __amdgpu_buffer_rsrc_t rsM = __builtin_amdgcn_make_buffer_rsrc((void*)mdc, 0, (int)(128u * SLOTI * 4u), 0x00020000);

    u32x4 pm[4];
    u32x4 ring[NLD];
    auto load_md = [&](int ff, int pl, int mdp) __attribute__((always_inline)) {
        const uint32_t so = (uint32_t)(cp * 64 + ff * 4 + h) * (SLOTI * 4u);
#pragma unroll
        for (int v = 0; v < 3; v++)
            if (pl < 0 || pl == ((mdp >> (4 * v)) & 15)) pm[v] = __builtin_amdgcn_raw_buffer_load_b128(rsM, (uint32_t)tid * 16u, so + (uint32_t)v * (NT * 16u), 0);
        if ((pl < 0 || pl == (mdp & 15)) && wv == 0) pm[3] = __builtin_amdgcn_raw_buffer_load_b128(rsM, (uint32_t)tid * 16u, so + 3u * NT * 16u, 0);
        __builtin_amdgcn_sched_barrier(0);
    };
    const uint32_t lf = (uint32_t)(tid >> 9) * (SKH * 128u) + (uint32_t)(tid & 0x1e0) * 16u + (uint32_t)(tid & 12) * 32u + (uint32_t)(tid & 16) * 4u + (uint32_t)(tid & 3) * 16u;
    const uint32_t lp = (uint32_t)(tid >> 8) * (SKM * 128u) + (uint32_t)((tid >> 2) & 63) * 128u + (uint32_t)(tid & 3) * 16u;
    auto load_main = [&](int i, int ff, bool near = false) __attribute__((always_inline)) -> u32x4 {
        const uint32_t q = (uint32_t)(((i >> 1) - HP) & 3), SK = q ? SKM : SKH, SS = q ? SSM : SSH;
        const __amdgpu_buffer_rsrc_t rs = __builtin_amdgcn_make_buffer_rsrc((void*)(recs + (QAT(q) + (uint32_t)ff * SS + (uint32_t)(h * 32 + j) * SC) * 128u), 0, ks * (int)(SK * 128u), 0x00020000);
        if (q == 0u && near) return __builtin_amdgcn_raw_buffer_load_b128(rs, lf, (i & 1) * (int)(2u * SK * 128u), 0);
        if (q == 0u) return __builtin_amdgcn_raw_buffer_load_b128(rs, lf, (i & 1) * (int)(2u * SK * 128u), AUX_NT);
        return __builtin_amdgcn_raw_buffer_load_b128(rs, lp + (uint32_t)(i & 1) * 64u, 0, 0);
    };
    const __amdgpu_buffer_rsrc_t rsT = __builtin_amdgcn_make_buffer_rsrc((void*)recs, 0, (int)(LBASE * 128u), 0x00020000);
    const uint32_t tof = (uint32_t)(lane & 2) * (TPL * 64u) + (uint32_t)(tid >> 2) * 128u + (uint32_t)(lane & 1) * 64u + 4u + (TBASE + (uint32_t)(h * 32 + j) * TST) * 128u;
    const uint32_t sp0 = (SBASE + (uint32_t)(h * 32 + j) * (2u * NWG) + (uint32_t)(tid >> 2)) * 128u;
    auto tail_rank = [&](uint32_t c, uint32_t sp, int ff, uint32_t t0, int& dst, uint32_t& en) __attribute__((always_inline)) -> int {
        const int k4 = (int)(((uint32_t)(lane & 3) + t0) * 4u) - (int)((c & 3u) * 8u);
        const int left = (int)((c >> 2) & 15u) - k4;
        const bool has = left > 0;
        uint32_t ea = tof + (uint32_t)ff * (128u * TST * 128u), eb = sp + (uint32_t)(k4 * 16);
        eb = sp0 + (uint32_t)(ff >> 3) * (NWG * 128u) + eb + __umul24(eb >> 7, (SPL - 1u) * 128u) + (uint32_t)min(max(left, 0), 4);
        asm volatile("" : "+v"(ea), "+v"(eb));
        en = k4 < 0 ? ea : eb;
        const u64 mask = __builtin_amdgcn_ballot_w64(has);
        const int n = __builtin_popcountll(mask);
        const int rank = __builtin_amdgcn_mbcnt_hi((uint32_t)(mask >> 32), __builtin_amdgcn_mbcnt_lo((uint32_t)mask, 0u));
        const int r = has ? rank : (n + lane - rank);
        dst = ((r & 15) << 4) | ((r >> 4) << 2);
        return n;
    };
    auto load_tail = [&](uint32_t en, u64& tm) __attribute__((always_inline)) -> u32x4 {
        const bool on = (uint32_t)(lane & 3) < (en & 7u);
        tm = __builtin_amdgcn_ballot_w64(on);
        const uint32_t at = (en & 0x70u) + (uint32_t)(lane & 3) * 16u;
        return __builtin_amdgcn_raw_buffer_load_b128(rsT, on ? (en & ~127u) + at + __umul24(at >> 7, (SPL - 1u) * 128u) : 0xffffff00u, 0, 0);
    };
    f32x2 fa[4][4];
    u32x4 em[2];
    u32x2 dw2;
    uint32_t wl;
    const __amdgpu_buffer_rsrc_t rsE = __builtin_amdgcn_make_buffer_rsrc((void*)m, 0, E * 8, 0x00020000);
    const __amdgpu_buffer_rsrc_t rsD = __builtin_amdgcn_make_buffer_rsrc((void*)dist, 0, E * 2, 0x00020000);
    auto acc_vec = [&](int i) __attribute__((always_inline)) -> u32x4 {
        const uint32_t a = A_ACC + (uint32_t)(i >> 4) * 256u + (uint32_t)(i & 15) * 8u;
        const u32x2 x = *(const LDS u32x2*)(uintptr_t)a, y = *(const LDS u32x2*)(uintptr_t)(a + 128u);
        const u32x4 v = {x.x, x.y, y.x, y.y};
        return v;
    };
    auto edge0 = [&](int sh) __attribute__((always_inline)) -> uint32_t {
        return (tid < SHARE / 4) ? (uint32_t)(((sh * (SHARE / 64) + (tid >> 4)) << 11) | (j << 6) | ((tid & 15) * 2)) : 0x0fffff00u;
    };
    auto load_m = [&](int sh) __attribute__((always_inline)) {
        em[0] = __builtin_amdgcn_raw_buffer_load_b128(rsE, edge0(sh) * 8u, 0, AUX_NT);
        em[1] = __builtin_amdgcn_raw_buffer_load_b128(rsE, edge0(sh) * 8u + 256u, 0, AUX_NT);
    };
    auto load_d = [&](int sh) __attribute__((always_inline)) {
        dw2.x = __builtin_amdgcn_raw_buffer_load_b32(rsD, edge0(sh) * 2u, 0, AUX_NT);
        dw2.y = __builtin_amdgcn_raw_buffer_load_b32(rsD, edge0(sh) * 2u + 64u, 0, AUX_NT);
    };
    auto load_wl = [&]() __attribute__((always_inline)) {
        const u64 p0 = (u64)wsbf, p1 = (u64)wrbf, p2 = (u64)centers, p3 = (u64)wup;
        const u64 pa = lane < 16 ? p0 : p1, pb = lane < 40 ? p2 : p3;
        wl = ((const GAS uint32_t*)(lane < 32 ? pa : pb))[lane & (lane < 32 ? 15 : lane < 40 ? 3 : 7)];
    };
    auto factor = [&](int r, bool first) __attribute__((always_inline)) {
        const float sq = 3.7982826f;
        const f32x2 d01 = {bf2f(dw2.x & 0xffffu), bf2f(dw2.x >> 16)}, d23 = {bf2f(dw2.y & 0xffffu), bf2f(dw2.y >> 16)};
        const uint32_t cw = __builtin_amdgcn_readlane(wl, 32 + (r >> 1));
        const float cn = bf2f((r & 1) ? (cw >> 16) : (cw & 0xffffu));
        f32x2 w[4];
#pragma unroll
        for (int k = 0; k < 4; k++) {
            const uint32_t x = __builtin_amdgcn_readlane(wl, (k >> 1) * 16 + r * 2 + (k & 1));
            w[k].x = bf2f(x & 0xffffu); w[k].y = bf2f(x >> 16);
        }
        const f32x2 y01 = (d01 - cn) * sq, y23 = (d23 - cn) * sq;
        const f32x2 t01 = -y01 * y01, t23 = -y23 * y23;
        const float rb[4] = {__builtin_amdgcn_exp2f(t01.x), __builtin_amdgcn_exp2f(t01.y), __builtin_amdgcn_exp2f(t23.x), __builtin_amdgcn_exp2f(t23.y)};
#pragma unroll
        for (int q = 0; q < 4; q++) {
#pragma unroll
            for (int k = 0; k < 4; k++) fa[q][k] = first ? w[k] * rb[q] : __builtin_elementwise_fma(w[k], (f32x2)(rb[q]), fa[q][k]);
        }
    };
#define ADT(V, O) "ds_write_addtid_b32 " V " offset:" #O "\n\t"
    auto store_md = [&]() __attribute__((always_inline)) {
        asm volatile("s_mov_b32 m0, %12\n\ts_nop 0\n\t" ADT("%0", 0) ADT("%1", 256) ADT("%2", 512) ADT("%3", 768) ADT("%4", 16384) ADT("%5", 16640) ADT("%6", 16896) ADT("%7", 17152)
                     ADT("%8", 32768) ADT("%9", 33024) ADT("%10", 33280) ADT("%11", 33536)
                     :: "v"(pm[0][0]), "v"(pm[0][1]), "v"(pm[0][2]), "v"(pm[0][3]), "v"(pm[1][0]), "v"(pm[1][1]), "v"(pm[1][2]), "v"(pm[1][3]),
                        "v"(pm[2][0]), "v"(pm[2][1]), "v"(pm[2][2]), "v"(pm[2][3]), "s"(wv * 1024) : "memory");
        if (wv == 0) asm volatile("s_mov_b32 m0, 0\n\ts_nop 0\n\t" ADT("%0", 49152) ADT("%1", 49408) ADT("%2", 49664) ADT("%3", 49920)
                                  :: "v"(pm[3][0]), "v"(pm[3][1]), "v"(pm[3][2]), "v"(pm[3][3]) : "memory");
    };
    const int me = h * 2 + cp;
    GAS unsigned* dw = (GAS unsigned*)(meta + M_DONE + j * 64);
    const bool fin = wv < (SHARE / 4 + 63) / 64;
    bool fast, more;
    uint32_t need, mg0, mg1;
    uint32_t guard = 0;
    uint32_t sgn = 0x8000u;
    pin(sgn);

#define GAT3(M) { uint32_t a0_, a1_, a2_; asm volatile( \
        "v_lshlrev_b16 %0, 2, %3\n\tv_lshlrev_b16 %1, 2, %4\n\tv_lshlrev_b16 %2, 2, %5\n\t" \
        "ds_read_b32 v[%6], %0\n\tds_read_b32 v[%6+1], %1\n\tds_read_b32 v[%6+2], %2" \
        : "=&v"(a0_), "=&v"(a1_), "=&v"(a2_) : "v"(w0), "v"(w1), "v"(w2), "n"(M) : "memory"); }
#define MIXL " op_sel_hi:[1,1,0]\n\t"
#define MIXH " op_sel:[0,1,0] op_sel_hi:[1,1,0]\n\t"
#define SDW " dst_sel:DWORD dst_unused:UNUSED_PAD src0_sel:DWORD src1_sel:WORD_1\n\t"
#define FOLD "v_bitop3_b32 %6, %6, v[%15-9], v[%15-8] bitop3:0xfe\n\tv_or_b32 %6, v[%15-7], %6\n\t"
#define SCAT(PRE, POST, M) { uint32_t c0_, c1_, c2_, a0_, a1_, a2_; asm volatile( \
        FOLD "v_and_b32 %0, 0x3ff, %7\n\tv_lshrrev_b32 %2, 22, %7\n\t" PRE \
        "v_bitop3_b32 %0, %11, %8, %0 bitop3:0xea\n\tv_bitop3_b32 %2, %11, %10, %2 bitop3:0xea\n\tv_bfe_u32 %1, %7, 11, 10\n\t" \
        "v_fma_mix_f32 %3, %0, v[%15], %12" MIXL "v_bitop3_b32 %1, %11, %9, %1 bitop3:0xea\n\tv_fma_mix_f32 %0, %0, v[%15], %13" MIXH \
        "v_fma_mix_f32 %4, %1, v[%15+1], %12" MIXL "v_lshl_add_u32 %0, %0, 16, %3\n\tv_lshlrev_b32_sdwa %3, 2, %8" SDW \
        "v_fma_mix_f32 %1, %1, v[%15+1], %13" MIXH "ds_add_rtn_u32 v[%15-9], %3, %0 offset:%14\n\t" \
        "v_fma_mix_f32 %5, %2, v[%15+2], %12" MIXL "v_lshl_add_u32 %1, %1, 16, %4\n\tv_lshlrev_b32_sdwa %4, 2, %9" SDW \
        "v_fma_mix_f32 %2, %2, v[%15+2], %13" MIXH "ds_add_rtn_u32 v[%15-8], %4, %1 offset:%14\n\t" \
        "v_lshl_add_u32 %2, %2, 16, %5\n\tv_lshlrev_b32_sdwa %5, 2, %10" SDW \
        "ds_add_rtn_u32 v[%15-7], %5, %2 offset:%14" POST \
        : "=&v"(c0_), "=&v"(c1_), "=&v"(c2_), "=&v"(a0_), "=&v"(a1_), "=&v"(a2_), "+v"(guard) \
        : "v"(xw), "v"(w0), "v"(w1), "v"(w2), "v"(sgn), "s"(mg0), "s"(mg1), "n"(A_ACC), "n"(M), "s"(tm) : "memory"); }
    asm volatile("v_mov_b32 v110, 0\n\tv_mov_b32 v111, 0\n\tv_mov_b32 v112, 0\n\tv_mov_b32 v113, 0\n\tv_mov_b32 v114, 0\n\tv_mov_b32 v115, 0\n\t"
                 "v_mov_b32 v116, 0\n\tv_mov_b32 v117, 0\n\tv_mov_b32 v118, 0" ::: "v106", "v127");
    auto unmix = [&](int i) __attribute__((always_inline)) {
        const auto s0 = __builtin_amdgcn_permlane16_swap(ring[i][0], ring[i][2], false, false);
        const auto s1 = __builtin_amdgcn_permlane16_swap(ring[i][1], ring[i][3], false, false);
        ring[i][0] = s0[0]; ring[i][2] = s0[1]; ring[i][1] = s1[0]; ring[i][3] = s1[1];
    };
    auto word = [&](int i, int k) __attribute__((always_inline)) -> uint32_t {
        return i < NFAST && (i >> 1) != HP ? ring[(i & ~1) | (k >> 1)][(i & 1) * 2 + (k & 1)] : ring[i][k];
    };
    auto gather = [&](int R, int i, int p) __attribute__((always_inline)) {
        if ((i >> 1) == HP) unmix(i);
        const uint32_t w0 = word(i, 0 ^ R), w1 = word(i, 1 ^ R), w2 = word(i, 2 ^ R);
        if (p == 0) GAT3(119) else if (p == 1) GAT3(122) else GAT3(125)
    };
    auto scatter = [&](int R, int i, int p, u64 tm) __attribute__((always_inline)) {
        const uint32_t xw = word(i, 3 ^ R), w0 = word(i, 0 ^ R), w1 = word(i, 1 ^ R), w2 = word(i, 2 ^ R);
        if (i < NFAST) { if (p == 0) SCAT("", "", 119) else if (p == 1) SCAT("", "", 122) else SCAT("", "", 125) }
        else if (p == 0) SCAT("s_mov_b64 exec, %16\n\t", "\n\ts_mov_b64 exec, -1", 119)
        else SCAT("s_mov_b64 exec, %16\n\t", "\n\ts_mov_b64 exec, -1", 125)
    };
#define WAIT_L(N) asm volatile("s_waitcnt lgkmcnt(" #N ")" ::: "memory")
    int ntn;
    u64 tm0, tm1;
    uint32_t ta[2], sp = 0u;
    auto list1 = [&](int ff) __attribute__((always_inline)) {
        int dst;
        uint32_t en;
        ntn = tail_rank(tc.x, sp, ff, 0u, dst, en);
        asm volatile("ds_permute_b32 v106, %0, %1" :: "v"(dst), "v"(en) : "memory");
        if (__builtin_expect(__builtin_amdgcn_ballot_w64((tc.x & 2u) != 0u) != 0ull, 0)) {
            const int n2 = tail_rank(tc.x, sp, ff, 4u, dst, en);
            if (ntn + n2 <= 32) {
                const int r = ((dst >> 4) | ((dst & 12) << 2)) + ntn;
                const int x = __builtin_amdgcn_ds_permute((en & 7u) ? ((r & 15) << 4) | ((r >> 4) << 2) : 252, (en & 7u) ? (int)en : 0);
                asm volatile("v_cmpx_ne_u32 vcc, 0, %0\n\tv_mov_b32 v106, %0\n\ts_mov_b64 exec, -1\n\ts_nop 4" :: "v"(x) : "vcc");
                ntn += n2;
            } else ntn = 64;
        }
    };
    auto step = [&](auto RR, auto KK, int ff) __attribute__((always_inline)) {
        constexpr int R = decltype(RR)::value;
        constexpr bool first = decltype(KK)::value == 1, last = decltype(KK)::value == 2;
        constexpr int mdp = first ? MDF : MDP;
        gather(R, 0, 0);
        const int ntl = ntn;
        asm volatile("v_mov_b32_dpp %0, v106 quad_perm:[0,0,0,0] row_mask:0xf bank_mask:0xf\n\tv_mov_b32_dpp %1, v106 quad_perm:[1,1,1,1] row_mask:0xf bank_mask:0xf" : "=v"(ta[0]), "=v"(ta[1]));
        auto side = [&](int pl) __attribute__((always_inline)) {
            if (first && pl == ((mdp >> 20) & 15)) { ring[4] = load_main(4, ff, NNEAR > 0u); ring[5] = load_main(5, ff, NNEAR > 0u); }
            if (first && pl == ((mdp >> 24) & 15)) { ring[6] = load_main(6, ff); ring[7] = load_main(7, ff); }
            if (pl == ((mdp >> 12) & 15)) ring[NFAST] = load_tail(ta[0], tm0);
            if (pl == ((mdp >> 16) & 15) && ntl > 16) ring[NFAST + 1] = load_tail(ta[1], tm1);
            __builtin_amdgcn_sched_barrier(0);
            if (!last) load_md(ff + 1, pl, mdp);
            if (!first && !last && pl == 5 && ff == 6) {
                load_d(me);
                load_wl();
                if (tid == NT - 128) (void)__hip_atomic_fetch_add((GAS unsigned*)((GAS char*)dw + vconst<0>()), 0x1000000u << me, __ATOMIC_RELAXED, __HIP_MEMORY_SCOPE_AGENT);
            }
            if (last && pl == 5) load_m(me);
            if (!first && !last && pl == 1 && ff == 13 && wv == 15) { GAS unsigned* d_ = dw; asm volatile("s_nop 4\n\tglobal_load_dword v108, %0, %1 sc1" :: "v"(vconst<0>()), "s"(d_) : "memory"); }
            __builtin_amdgcn_sched_barrier(0);
        };
        side(8);
        gather(R, 1, 1);
        side(9);
        const uint32_t tcur = tc.x, spc = sp;
#pragma unroll
        for (int i = 0; i < NFAST - 1; i++) {
            gather(R, i + 2, (i + 2) % 3);
            if (i == 0) side(10);
            if (i == 0) WAIT_L(6); else if (i == 1) WAIT_L(9); else WAIT_L(12);
            scatter(R, i, i % 3, 0ull);
            if (!last && (i == 1 || i == 3)) { ring[i - 1] = load_main(i - 1, ff + 1); ring[i] = load_main(i, ff + 1); }
            if (!last && (i == 4 || i == 5)) ring[i] = load_main(i, ff + 1, first && NNEAR > 1u);
            side(i);
            if (i == 2) SETP(2);
            if (i == 3) SETP(1);
            if (i == 5) SETP(0);
            if (i == 0 && !last) {
                tc.x = __builtin_amdgcn_alignbit(tc.y, tc.x, 8u); tc.y = __builtin_amdgcn_alignbit(tc.z, tc.y, 8u);
                tc.z = __builtin_amdgcn_alignbit(tc.w, tc.z, 8u); tc.w >>= 8;
                sp = ff == 7 ? 0u : sp + ((tcur & 0x3cu) << 2);
                list1(ff + 1);
            }
            __builtin_amdgcn_sched_barrier(0);
        }
        if (last) asm volatile("" :: "v"(ring[NFAST + 1]));
        if (__builtin_expect(ntl > 16, 0)) {
            gather(R, NFAST + 1, 0);
            WAIT_L(0);
            scatter(R, NFAST + 1, 0, tm1);
        }
        if (__builtin_expect(ntl > 32, 0)) {
#pragma unroll 1
            for (int e = 2; e < 6; e++) {
                int dst;
                uint32_t en;
                if (tail_rank(tcur, spc, ff, (uint32_t)e & 4u, dst, en) <= (e & 3) * 16) continue;
                const int cmp = __builtin_amdgcn_ds_permute(dst, (int)en);
                ring[NFAST + 1] = load_tail((uint32_t)__builtin_amdgcn_ds_bpermute(((lane & ~3) | (e & 3)) << 2, cmp), tm1);
                gather(R, NFAST + 1, 0);
                WAIT_L(0);
                scatter(R, NFAST + 1, 0, tm1);
            }
        }
        SETP(3);
        WAIT_L(3);
        if (!last) {
            __builtin_amdgcn_s_barrier();
            store_md();
            SETP(0);
        }
        scatter(R, NFAST, 2, tm0);
        scatter(R, NFAST - 1, 1, 0ull);
        if (!last) {
            ring[6] = load_main(6, ff + 1);
            ring[7] = load_main(7, ff + 1);
            WAIT_L(6);
            __builtin_amdgcn_s_barrier();
            SETP(3);
        }
    };
    auto steps = [&](auto RR) __attribute__((always_inline)) {
        tc = tcnt[(tid >> 2) * 128 + j * 4 + h];
        u32x4 sv[2];
        float4 su[3];
        uint32_t e0;
        if (wv < 4) {
            sv[0] = (u32x4)(0u); sv[1] = sv[0];
            su[0] = make_float4(0.f, 0.f, 0.f, 0.f);
            if (lane < SLOTI / 256) { sv[0] = stat[tid * 2]; sv[1] = stat[tid * 2 + 1]; su[0] = ((const float4*)(ws + W_SUM))[tid]; }
            su[1] = ((const float4*)(ws + W_SUM))[256 + lane];
            su[2] = ((const float4*)(ws + W_SUM))[320 + lane];
            e0 = meta[M_E0 + wv];
        }
        const u32x2 extra = *(const u32x2*)(meta + M_LOG);
        load_md(0, -1, 0);
        {
            const u32x4 bi = {BIAS, BIAS, BIAS, BIAS};
#pragma unroll
            for (int k = 0; k < 6; k++) *(LDS u32x4*)(uintptr_t)(A_ACC + (k * NT + threadIdx.x) * 16) = bi;
            if (threadIdx.x < 128) *(LDS u32x4*)(uintptr_t)(A_ACC + (6 * NT + threadIdx.x) * 16) = bi;
            __builtin_amdgcn_sched_barrier(0);
        }
#pragma unroll
        for (int i = 0; i < 2; i++) ring[i] = load_main(i, 0);
        __builtin_amdgcn_sched_barrier(0);
        list1(0);
        if (wv < 4) {
            const uint32_t mxb = wave_max(max(max(sv[0].x & 0xffffu, sv[0].z & 0xffffu), max(sv[1].x & 0xffffu, sv[1].z & 0xffffu)));
            const uint32_t mnb = 0xffffu - (wave_max(max(max(sv[0].x, sv[0].z), max(sv[1].x, sv[1].z))) >> 16);
            const float s2 = wave_sum((__uint_as_float(sv[0].y) + __uint_as_float(sv[0].w)) + (__uint_as_float(sv[1].y) + __uint_as_float(sv[1].w)));
            const float s1 = wave_sum((su[0].x + su[0].y) + (su[0].z + su[0].w));
            const float c1 = wave_sum((su[1].x + su[1].y) + (su[1].z + su[1].w)) * (1.0f / (NWG * 512)), c2 = wave_sum((su[2].x + su[2].y) + (su[2].z + su[2].w)) * (1.0f / (NWG * 512));
            const float rng = fmaxf(fabsf(zf * s1 * c1) + ZSIG * sqrtf(zf * s2 * c2), 2.0f * bf2f(mxb)) * 6.103515625e-05f;
            const uint32_t eb = mxb ? (__float_as_uint(rng) + 0x7fffffu) >> 23 : 127u;
            const bool ok = !mxb || (mxb < 0x4700u && eb >= 107u && eb <= 140u && mnb >= ((eb + 8u) << 7) && c2 >= 0.015625f);
            if (lane == 0) {
                s_flag[8 + wv] = (eb & 0xffu) | (ok ? 0x100u : 0u) | (mxb ? 0x200u : 0u);
                s_flag[12 + wv] = __float_as_uint(__uint_as_float(((eb + e0) & 0xffu) << 23) * (1024.0f / 1023.0f));
                s_flag[wv] = 0u;
            }
        }
        store_md();
#pragma unroll
        for (int i = 2; i < 4; i++) ring[i] = load_main(i, 0);
        WAIT_L(0);
        __syncthreads();
        const uint32_t q0 = __builtin_amdgcn_readfirstlane(s_flag[8 + 2 * cp]), q1 = __builtin_amdgcn_readfirstlane(s_flag[9 + 2 * cp]);
        fast = (q0 & q1 & 0x100u) != 0u;
        need = (q0 >> 9) | ((q1 >> 9) << 1);
        more = (__builtin_amdgcn_readfirstlane(extra.x | extra.y) | (uint32_t)(T & 3)) != 0u;
        mg0 = (((q0 & 0xffu) + 9u) << 23) | 0x400000u;
        mg1 = ((((q1 & 0xffu) + 9u) << 23) | 0x400000u) + ((0u - (mg0 >> 16)) & 0xffffu);
        step(RR, IC<1>(), 0);
#pragma unroll 1
        for (int ff = 1; ff < 15; ff++) step(RR, IC<0>(), ff);
        step(RR, IC<2>(), 15);
    };
    auto irregular = [&](auto f) __attribute__((always_inline)) {
        const float fr = bf2f(freq[0]);
        const char* lg = recs + (size_t)LBASE * 128u;
        const uint16_t* tags = (const uint16_t*)(lg + (size_t)lcap * 128u);
        const uint32_t nl = meta[M_LOG], ns = meta[M_LOG + 1];
#pragma unroll 1
        for (uint32_t q = tid; q < nl * 8u; q += NT) {
            const uint32_t b = tags[q >> 3];
            if ((b & 127u) == (uint32_t)(h * 32 + j)) {
                const uint32_t* un = (const uint32_t*)(lg + (size_t)q * 16u);
                const uint32_t xw = un[3 ^ h];
#pragma unroll 1
                for (int k = 0; k < 3; k++) {
                    const uint32_t ww = un[k ^ h];
                    const float c = (float)((xw >> (k * 11)) & 0x3ffu) * (1.0f / 1023.0f);
                    if (!(ww & WNULL)) f(ww >> 16, ((ww & 0x3fffu) << 6) | (b >> 5), (ww & 0x8000u) ? -c : c);
                }
            }
        }
#pragma unroll 1
        for (uint32_t q = tid; q < ns + (uint32_t)(T & 3); q += NT) {
            const uint32_t t = q < ns ? ((const uint32_t*)tags)[-1 - (int)q] : (uint32_t)(T & ~3) + (q - ns);
            const uint32_t i = (uint32_t)iin[t], o = (uint32_t)iout[t];
            if (((o >> 6) & 31u) == (uint32_t)j && (i & 3u) == (uint32_t)h) f(((o >> 11) << 6) | (o & 63u), i, cosf(fr * bf2f(ang[t])));
        }
        __builtin_amdgcn_s_waitcnt(0x0f70);
    };

    if (h == 0) steps(IC<0>());
    else if (h == 1) steps(IC<1>());
    else if (h == 2) steps(IC<2>());
    else steps(IC<3>());
    if (fast) {
        if (more) irregular([&](uint32_t ol, uint32_t i, float cf) __attribute__((always_inline)) {
            const uint32_t md = mdc[(cp * 64u + (i & 63u)) * SLOTI + ((i >> 14) << 8) + ((i >> 4) & 0xfcu) + ((i >> 12) & 3u)];
            const float c = cf * (1023.0f / 16777216.0f);
            const uint32_t lo = __float_as_uint(__builtin_fmaf(c, h2f(md & 0xffffu), __uint_as_float(mg0)));
            const uint32_t hi = __float_as_uint(__builtin_fmaf(c, h2f(md >> 16), __uint_as_float(mg1)));
            if (cf == cf) guard |= atomicAdd(&acc[ol], (hi << 16) + lo);
            else guard |= HSLOW;
        });
    }
    if (wv == 15) asm volatile("ds_write_b32 %0, v108" :: "v"(vconst<A_FLAG + 20>()) : "memory");
    WAIT_L(0);
    asm volatile("v_or3_b32 %0, %0, v110, v111\n\tv_or3_b32 %0, %0, v112, v113\n\tv_or3_b32 %0, %0, v114, v115\n\tv_or3_b32 %0, %0, v116, v117\n\tv_or_b32 %0, v118, %0" : "+v"(guard));
    __syncthreads();
    const __amdgpu_buffer_rsrc_t rsP = __builtin_amdgcn_make_buffer_rsrc((void*)(partial + (size_t)j * 16 * SLOTO), 0, 16 * SLOTO * 4, 0x00020000);
    const char* pk = recs + (uint32_t)j * (SC * 128u);
    const __amdgpu_buffer_rsrc_t rsK = __builtin_amdgcn_make_buffer_rsrc((void*)pk, 0, (int)(MAINL * 128u), 0x00020000);
#define PKA (SSM * 128u)
#define PKB (32u * SC * 128u)
    auto pk_at = [&](uint32_t i) __attribute__((always_inline)) -> uint32_t {
        const uint32_t c = i >> 9;
        return (c < 12u ? (c >> 2) * SQ + (c & 3u) * SKM : 2u * SSM) * 128u + (i & 511u) * 16u;
    };
    auto factors = [&](int r0, int r1) __attribute__((always_inline)) {
#pragma unroll
        for (int r = r0; r < r1; r++) factor(r, r == 0);
    };
    bool room;
    {
        room = (__builtin_amdgcn_readfirstlane(s_flag[5]) >> (24 + (me ^ 1))) & 1u;
        const __amdgpu_buffer_rsrc_t rsW = __builtin_amdgcn_make_buffer_rsrc((void*)(pk + (uint32_t)cp * PKA + (uint32_t)h * PKB), 0, room ? (int)(MAINL * 128u) : 0, 0x00020000);
        uint32_t big = 0u;
        u32x4 v[7];
#pragma unroll
        for (int k = 0; k < 6; k++) v[k] = acc_vec(k * NT + tid);
        v[6] = (u32x4)(BIAS);
        if (wv < 2) v[6] = acc_vec(6 * NT + tid);
#pragma unroll
        for (int k = 0; k < 6; k++)
            __builtin_amdgcn_raw_buffer_store_b128(v[k], rsW, (uint32_t)(k * NT + tid - me * (SHARE / 4)) < SHARE / 4 ? 0xffffff00u : pk_at((uint32_t)tid) + ((k >> 1) * SQ + (k & 1) * 2u * SKM) * 128u, 0, AUX_SC1);
        if (wv < 2 && me != 7) __builtin_amdgcn_raw_buffer_store_b128(v[6], rsW, 2u * SSM * 128u + (uint32_t)tid * 16u, 0, AUX_SC1);
        __builtin_amdgcn_sched_barrier(0);
#pragma unroll
        for (int k = 0; k < 28; k++) { guard |= v[k >> 2][k & 3]; big |= v[k >> 2][k & 3] + 0x42004200u; }
        if (guard & HSLOW) s_flag[0] = 1u;
        if (big & 0x7c00u) s_flag[2] = 1u;
        if (big & 0x7c000000u) s_flag[3] = 2u;
        __builtin_amdgcn_sched_barrier(0);
        if (fin) factors(0, 3);
    }
    asm volatile("s_waitcnt vmcnt(0)" ::: "memory");
    __syncthreads();
    const bool redo = !fast || __builtin_amdgcn_readfirstlane(s_flag[0]) != 0u || (need & ~__builtin_amdgcn_readfirstlane(s_flag[2] | s_flag[3])) != 0u;
    const bool slow = redo || !room;
    if (slow) {
        float* fs = (float*)acc;
        float w[16];
        load_w(wdown, w);
        const float fr = bf2f(freq[0]);
#pragma unroll 1
        for (int e = 0; e < 2; e++) {
            if (redo) {
                __syncthreads();
                for (int i = tid; i < SLOTO; i += NT) fs[i] = 0.f;
                __syncthreads();
                const __amdgpu_buffer_rsrc_t rsO = __builtin_amdgcn_make_buffer_rsrc((void*)iout, 0, T * 4, 0x00020000);
                const __amdgpu_buffer_rsrc_t rsI = __builtin_amdgcn_make_buffer_rsrc((void*)iin, 0, T * 4, 0x00020000);
#pragma unroll 1
                for (int t0 = tid * 4; t0 < T; t0 += NT * 16) {
                    u32x4 o4[4], i4[4];
#pragma unroll
                    for (int g = 0; g < 4; g++) {
                        o4[g] = __builtin_amdgcn_raw_buffer_load_b128(rsO, (uint32_t)(t0 + g * NT * 4) * 4u, 0, 0);
                        i4[g] = __builtin_amdgcn_raw_buffer_load_b128(rsI, (uint32_t)(t0 + g * NT * 4) * 4u, 0, 0);
                    }
                    uint32_t hit = 0u;
#pragma unroll
                    for (int k = 0; k < 16; k++) hit |= (bfi(3u, i4[k >> 2][k & 3], o4[k >> 2][k & 3]) & 0x7c3u) == (uint32_t)(j * 64 + h) ? 1u << k : 0u;
#pragma unroll 1
                    for (; hit; hit &= hit - 1u) {
                        const int k = __builtin_ctz(hit), t = t0 + (k >> 2) * NT * 4 + (k & 3);
                        if (t >= T) break;
                        const uint32_t o = (uint32_t)iout[t];
                        uint32_t rr[4];
                        md_of(m[iin[t]], w, rr);
                        atomicAdd(&fs[((o >> 11) << 6) | (o & 63u)], cosf(fr * bf2f(ang[t])) * bf2f(cp ? (e ? rr[3] : rr[2]) : (e ? rr[1] : rr[0])));
                    }
                }
                __syncthreads();
            }
            const float un = __uint_as_float(s_flag[12 + 2 * cp + e]);
            for (int i = tid; i < SLOTO / 4; i += NT) {
                u32x4 v = acc_vec(i);
                if (!redo) {
#pragma unroll
                    for (int k = 0; k < 4; k++) v[k] = __float_as_uint((float)((int)((v[k] >> (16 * e)) & 0xffffu) - 0x4000) * un);
                }
                __builtin_amdgcn_raw_buffer_store_b128(v, rsP, (uint32_t)((e * 8 + me) * SLOTO * 4 + i * 16), 0, AUX_SC1);
            }
        }
        load_wl();
        load_d(me);
        load_m(me);
        factors(0, 3);
        asm volatile("s_waitcnt vmcnt(0)" ::: "memory");
        __syncthreads();
    }
    if (tid == NT - 64) {
        const unsigned inc = (slow ? 0x10001u : 1u) << me;
        unsigned v = __hip_atomic_fetch_add(dw, inc, __ATOMIC_RELAXED, __HIP_MEMORY_SCOPE_AGENT) + inc;
        while ((v & 0xffu) != 0xffu) {
            __builtin_amdgcn_s_sleep(1);
            v = __hip_atomic_load(dw, __ATOMIC_RELAXED, __HIP_MEMORY_SCOPE_AGENT);
        }
        s_flag[1] = v;
    }
    if (fin) factors(3, 6);
    __syncthreads();
    if (!fin) return;
    const unsigned sv = __builtin_amdgcn_readfirstlane(s_flag[1]);
    u32x4 p[8];
    {
        const uint32_t po = tid < SHARE / 4 ? pk_at((uint32_t)(me * (SHARE / 4) + tid)) : 0x7fffff00u;
#pragma unroll
        for (int r = 1; r < 8; r++) {
            const uint32_t k = (uint32_t)(me ^ r);
            p[r] = __builtin_amdgcn_raw_buffer_load_b128(rsK, po, ((sv >> (16u + k)) & 1u) ? MAINL * 128u : (k & 1u) * PKA + (k >> 1) * PKB, AUX_SC1);
        }
        __builtin_amdgcn_sched_barrier(0);
    }
    p[0] = (u32x4)(0u);
    if (!slow) p[0] = acc_vec(me * (SHARE / 4) + min(tid, SHARE / 4 - 1));
    uint32_t un[4];
#pragma unroll
    for (int c = 0; c < 4; c++) un[c] = __builtin_amdgcn_readfirstlane(s_flag[12 + c]);
    factors(6, 8);
    __builtin_amdgcn_sched_barrier(0);
    const unsigned flt = (sv >> 16) & 0xffu;
    auto finish = [&](int sh) __attribute__((always_inline)) {
        {
            const int ol = sh * SHARE + tid * 4;
            const uint32_t po = tid < SHARE / 4 ? (uint32_t)ol * 4u : 0x7fffff00u;
            float g[4][4];
            u32x4 a[2] = {p[0] + p[2], p[1] + p[3]}, b[2] = {p[4] + p[6], p[5] + p[7]};
            if (cp) { const u32x4 t = a[0], u = b[0]; a[0] = a[1]; b[0] = b[1]; a[1] = t; b[1] = u; }
#pragma unroll
            for (int c = 0; c < 2; c++) {
                const int nb = 0x10000 - 0x4000 * __builtin_popcount(flt & (0x55u << c));
                const float u0 = __uint_as_float(nb ? un[2 * c] : 0u), u1 = __uint_as_float(nb ? un[2 * c + 1] : 0u);
#pragma unroll
                for (int q = 0; q < 4; q++) {
                    g[q][2 * c] = (float)((int)((a[c][q] & 0xffffu) + (b[c][q] & 0xffffu)) - nb) * u0;
                    g[q][2 * c + 1] = (float)((int)((a[c][q] >> 16) + (b[c][q] >> 16)) - nb) * u1;
                }
            }
            if (__builtin_expect(flt != 0u, 0)) {
#pragma unroll
                for (int c = 0; c < 2; c++) {
#pragma unroll 1
                    for (int k = c; k < 8; k += 2) {
                        if (!((flt >> k) & 1u)) continue;
                        const u32x4 f0 = __builtin_amdgcn_raw_buffer_load_b128(rsP, po, k * SLOTO * 4, AUX_SC1);
                        const u32x4 f1 = __builtin_amdgcn_raw_buffer_load_b128(rsP, po, (8 + k) * SLOTO * 4, AUX_SC1);
#pragma unroll
                        for (int q = 0; q < 4; q++) { g[q][2 * c] += __uint_as_float(f0[q]); g[q][2 * c + 1] += __uint_as_float(f1[q]); }
                    }
                }
            }
            float wu[16];
#pragma unroll
            for (int k = 0; k < 8; k++) {
                const uint32_t x = __builtin_amdgcn_readlane(wl, 40 + k);
                wu[2 * k] = bf2f(x & 0xffffu); wu[2 * k + 1] = bf2f(x >> 16);
            }
            u32x4 ov[2];
#pragma unroll
            for (int q = 0; q < 4; q++) {
#pragma unroll
                for (int k = 0; k < 4; k++) g[q][k] *= fa[q][k >> 1][k & 1];
                const uint32_t m0 = em[q >> 1][(q & 1) * 2], m1 = em[q >> 1][(q & 1) * 2 + 1];
                const float mm[4] = {bf2f(m0 & 0xffff), bf2f(m0 >> 16), bf2f(m1 & 0xffff), bf2f(m1 >> 16)};
                float o[4];
#pragma unroll
                for (int k = 0; k < 4; k++) o[k] = mm[k] + (g[q][0] * wu[k] + g[q][1] * wu[4 + k] + g[q][2] * wu[8 + k] + g[q][3] * wu[12 + k]) * fa[q][2 + (k >> 1)][k & 1];
                ov[q >> 1][(q & 1) * 2] = pk_bf16(o[0], o[1]);
                ov[q >> 1][(q & 1) * 2 + 1] = pk_bf16(o[2], o[3]);
            }
            const __amdgpu_buffer_rsrc_t rsU = __builtin_amdgcn_make_buffer_rsrc((void*)out, 0, E * 8, 0x00020000);
            const uint32_t uo = edge0(sh) * 8u;
            __builtin_amdgcn_raw_buffer_store_b128(ov[0], rsU, uo, 0, AUX_NT);
            __builtin_amdgcn_raw_buffer_store_b128(ov[1], rsU, uo + 256u, 0, AUX_NT);
        }
    };
    finish(me);
}

extern "C" __global__ __launch_bounds__(256) void gen_scatter_kernel(const int* __restrict__ iin, const int* __restrict__ iout,
                                                          const uint16_t* __restrict__ ang, const uint16_t* __restrict__ freq,
                                                          const u32x2* __restrict__ m, const uint16_t* __restrict__ wdown,
                                                          float* __restrict__ agg, int T) {
    float w[16];
    load_w(wdown, w);
    const float fr = bf2f(freq[0]);
    for (long t = (long)blockIdx.x * 256 + threadIdx.x; t < T; t += (long)gridDim.x * 256) {
        const float c = cosf(fr * bf2f(ang[t]));
        uint32_t r[4];
        md_of(m[iin[t]], w, r);
        float* q = agg + (size_t)iout[t] * 4;
#pragma unroll
        for (int k = 0; k < 4; k++) atomicAdd(q + k, c * bf2f(r[k]));
    }
}
__global__ __launch_bounds__(256) void gen_final_kernel(const u32x2* __restrict__ m, const uint16_t* __restrict__ dist,
                                                        const uint16_t* __restrict__ centers, const uint16_t* __restrict__ wrbf,
                                                        const uint16_t* __restrict__ wsbf, const uint16_t* __restrict__ wup,
                                                        const float* __restrict__ agg, u32x2* __restrict__ out, int E) {
    const int e = blockIdx.x * 256 + threadIdx.x;
    if (e >= E) return;
    const float d = bf2f(dist[e]);
    float rs[4] = {0, 0, 0, 0}, rg[4] = {0, 0, 0, 0};
#pragma unroll 1
    for (int r = 0; r < 8; r++) {
        const float x = d - bf2f(centers[r]);
        const float rb = __expf(-10.0f * x * x);
#pragma unroll
        for (int k = 0; k < 4; k++) { rs[k] += rb * bf2f(wsbf[r * 4 + k]); rg[k] += rb * bf2f(wrbf[r * 4 + k]); }
    }
    float wu[16];
    load_w(wup, wu);
    const u32x2 mv = m[e];
    const float mm[4] = {bf2f(mv.x & 0xffff), bf2f(mv.x >> 16), bf2f(mv.y & 0xffff), bf2f(mv.y >> 16)};
    float g[4], o[4];
#pragma unroll
    for (int k = 0; k < 4; k++) g[k] = agg[(size_t)e * 4 + k] * rs[k];
#pragma unroll
    for (int k = 0; k < 4; k++) o[k] = mm[k] + (g[0] * wu[k] + g[1] * wu[4 + k] + g[2] * wu[8 + k] + g[3] * wu[12 + k]) * rg[k];
    const u32x2 ov = {f2bf(o[0]) | (f2bf(o[1]) << 16), f2bf(o[2]) | (f2bf(o[3]) << 16)};
    out[e] = ov;
}

extern "C" void kernel_launch(void* const* d_in, const int* in_sizes, int n_in,
                              void* d_out, int out_size, void* d_ws, size_t ws_size,
                              hipStream_t stream) {
    const u32x2* m = (const u32x2*)d_in[0];
    const int* iin = (const int*)d_in[1];
    const int* iout = (const int*)d_in[2];
    const uint16_t* dist = (const uint16_t*)d_in[3];
    const uint16_t* ang = (const uint16_t*)d_in[4];
    const uint16_t* centers = (const uint16_t*)d_in[5];
    const uint16_t* freq = (const uint16_t*)d_in[6];
    const uint16_t* wrbf = (const uint16_t*)d_in[7];
    const uint16_t* wsbf = (const uint16_t*)d_in[8];
    const uint16_t* wdown = (const uint16_t*)d_in[9];
    const uint16_t* wup = (const uint16_t*)d_in[10];
    const int E = in_sizes[3];
    const int T = in_sizes[1];
    char* ws = (char*)d_ws;
    const int G = T >> 2;
    const int ntile = (G + GR - 1) / GR;
    const int nstat = ntile / NWG - NDYN > 4 ? ntile / NWG - NDYN : 4;
    const int lcap = G / 6 + 2;
    char* recs = ws + W_RECS;
    size_t off = W_RECS;
    const size_t rbytes = (size_t)LBASE * 128 + (size_t)lcap * 130;
    off += rbytes + 256 + (size_t)32 * 16 * SLOTO * 4;
    if (E > EMAX || (E & 3) || E < 1 || rbytes > 0x70000000u || off > ws_size) {
        (void)hipMemsetAsync(ws, 0, (size_t)E * 16, stream);
        gen_scatter_kernel<<<4096, 256, 0, stream>>>(iin, iout, ang, freq, m, wdown, (float*)ws, T);
        gen_final_kernel<<<(E + 255) / 256, 256, 0, stream>>>(m, dist, centers, wrbf, wsbf, wup, (const float*)ws, (u32x2*)d_out, E);
        return;
    }
    int ks = T / (NWG * NB * 24);
    if (ks > KMAX) ks = KMAX;
    const float zf = (float)T / (4.0f * (float)E * (float)E);
    (void)hipFuncSetAttribute((const void*)part_kernel, hipFuncAttributeMaxDynamicSharedMemorySize, P_LDS);
    (void)hipFuncSetAttribute((const void*)acc_kernel, hipFuncAttributeMaxDynamicSharedMemorySize, A_LDS);
    part_kernel<<<NWG, NT, P_LDS, stream>>>(iin, iout, ang, freq, m, wdown, recs, T, E, ntile, nstat, ks, lcap);
    acc_kernel<<<NWG, NT, A_LDS, stream>>>(ws, T, E, ks, lcap, zf, 0, centers, wrbf, wsbf, wup, m, dist, (u32x2*)d_out, iin, iout, ang, wdown, freq);
}
```

```cpp
#include <hip/hip_runtime.h>
#include <hip/hip_bf16.h>
#include <stdint.h>
#pragma clang diagnostic ignored "-Winline-asm"

typedef unsigned int u32x4 __attribute__((ext_vector_type(4)));
typedef unsigned int u32x2 __attribute__((ext_vector_type(2)));
typedef unsigned short u16x2 __attribute__((ext_vector_type(2)));
typedef __bf16 bf16x2 __attribute__((ext_vector_type(2)));
typedef float f32x2 __attribute__((ext_vector_type(2)));
typedef unsigned long long u64;

static __device__ __forceinline__ float bf2f(uint32_t bits16) { return __uint_as_float(bits16 << 16); }
static __device__ __forceinline__ uint32_t f2bf(float f) {
    uint32_t u = __float_as_uint(f);
    if ((u & 0x7fffffffu) > 0x7f800000u) return (u >> 16) | 0x40;
    return (u + 0x7fffu + ((u >> 16) & 1u)) >> 16;
}
static __device__ __forceinline__ float h2f(uint32_t bits16) { return (float)__builtin_bit_cast(_Float16, (unsigned short)bits16); }

#define NB 2048
#define WNULL 0x80000000u
#define CSTEP 0x2aabu
#define CINIT 205u
#define CHV (12u * CSTEP)
#define SLOTO 25088
#define SLOTI 12544
#define EMAX (SLOTO * 32)
#define NT 1024
#define NWG 256
#define KMAX 4
#define NTAIL 4
#define SC 64u
#define SKM (128u * SC + 2u)
#define SSM (4u * SKM + 1u)
#define NNEAR 2u
#define NEARM (0x55555555u << (2u * NNEAR))
#define SSH (128u * SC + 1u)
#define SKH ((16u + NNEAR) * SSH + 2u - (NNEAR & 1u))
#define SQ (16u * SSM)
#define MAINL (3u * SQ)
#define SBASE MAINL
#define SPL (128u * 2u * NWG + 1u)
#define HBASE (SBASE + 8u * SPL)
#define QAT(q) ((q) ? ((q) - 1u) * SQ : HBASE)
#define BPL(b) (QAT(((b) >> 3) & 3u) + ((b) & 7u) + (((b) >> 5) << 3))
#define TST 257u
#define TBASE (HBASE + 3u * SKH + 16u * SSH)
#define TPL (NB * TST)
#define LBASE (TBASE + 2u * TPL)
#define NDYN 3
#define NHALF 80
#define GR 2048
#define M_DONE 0
#define M_TILE (32 * 64)
#define M_LOG (M_TILE + 64)
#define M_MD (M_LOG + 64)
#define M_BEAT (M_MD + 64)
#define M_E0 (M_BEAT + 64)
#define M_WORDS (M_E0 + 64)
#define W_TCNT (M_WORDS * 4)
#define W_STAT (W_TCNT + NWG * NB)
#define W_SUM (W_STAT + 4 * NWG * 8)
#define W_MDC (W_SUM + 6 * NWG * 4)
#define W_RECS (W_MDC + 128 * SLOTI * 4)
#define AUX_NT 2
#define AUX_SC1 16

static __device__ __forceinline__ uint32_t row_max(uint32_t x) {
    x = max(x, (uint32_t)__builtin_amdgcn_update_dpp(0, x, 0x111, 0xf, 0xf, false));
    x = max(x, (uint32_t)__builtin_amdgcn_update_dpp(0, x, 0x112, 0xf, 0xf, false));
    x = max(x, (uint32_t)__builtin_amdgcn_update_dpp(0, x, 0x114, 0xf, 0xf, false));
    return max(x, (uint32_t)__builtin_amdgcn_update_dpp(0, x, 0x118, 0xf, 0xf, false));
}
static __device__ __forceinline__ uint32_t wave_max(uint32_t x) {
    x = row_max(x);
    x = max(x, (uint32_t)__builtin_amdgcn_update_dpp(0, x, 0x142, 0xa, 0xf, false));
    x = max(x, (uint32_t)__builtin_amdgcn_update_dpp(0, x, 0x143, 0xc, 0xf, false));
    return __builtin_amdgcn_readlane(x, 63);
}
static __device__ __forceinline__ float row_sum(float x) {
    x += __int_as_float(__builtin_amdgcn_update_dpp(0, __float_as_int(x), 0x111, 0xf, 0xf, false));
    x += __int_as_float(__builtin_amdgcn_update_dpp(0, __float_as_int(x), 0x112, 0xf, 0xf, false));
    x += __int_as_float(__builtin_amdgcn_update_dpp(0, __float_as_int(x), 0x114, 0xf, 0xf, false));
    return x + __int_as_float(__builtin_amdgcn_update_dpp(0, __float_as_int(x), 0x118, 0xf, 0xf, false));
}
static __device__ __forceinline__ float wave_sum(float x) {
    x = row_sum(x);
    x += __int_as_float(__builtin_amdgcn_update_dpp(0, __float_as_int(x), 0x142, 0xa, 0xf, false));
    x += __int_as_float(__builtin_amdgcn_update_dpp(0, __float_as_int(x), 0x143, 0xc, 0xf, false));
    return __int_as_float(__builtin_amdgcn_readlane(__float_as_int(x), 63));
}
static __device__ __forceinline__ uint32_t bfi(uint32_t mask, uint32_t a, uint32_t b) {
    return __builtin_amdgcn_bitop3_b32(mask, a, b, 0xca);
}
template <uint32_t C> static __device__ __forceinline__ uint32_t vconst() {
    uint32_t r;
    asm volatile("v_mov_b32 %0, %1" : "=v"(r) : "n"(C));
    return r;
}
static __device__ __forceinline__ uint32_t pk_bf16(float a, float b) {
    const f32x2 v = {a, b};
    return __builtin_bit_cast(uint32_t, __builtin_convertvector(v, bf16x2));
}
static __device__ __forceinline__ void pin(uint32_t& x) { asm volatile("" : "+v"(x)); }


static __device__ __forceinline__ void md_of(u32x2 v, const float* w, uint32_t* r) {
    float a0 = bf2f(v.x & 0xffff), a1 = bf2f(v.x >> 16), a2 = bf2f(v.y & 0xffff), a3 = bf2f(v.y >> 16);
#pragma unroll
    for (int j = 0; j < 4; j++) r[j] = f2bf(a0 * w[j] + a1 * w[4 + j] + a2 * w[8 + j] + a3 * w[12 + j]);
}
static __device__ __forceinline__ void load_w(const uint16_t* __restrict__ wdown, float* w) {
    u32x4 wv0 = *(const u32x4*)wdown, wv1 = *(const u32x4*)(wdown + 8);
#pragma unroll
    for (int i = 0; i < 4; i++) {
        w[2 * i] = bf2f(wv0[i] & 0xffffu); w[2 * i + 1] = bf2f(wv0[i] >> 16);
        w[8 + 2 * i] = bf2f(wv1[i] & 0xffffu); w[8 + 2 * i + 1] = bf2f(wv1[i] >> 16);
    }
}
#define LDS __attribute__((address_space(3)))
#define P_CNT 0
#define P_FLAG ((NB + 64) * 4)
#define P_LST (P_FLAG + 256)
#define LCAP 32
#define P_TAB (P_LST + 4 * LCAP * 16)
#define P_BUF (P_TAB + NB * 4)
#define P_LDS (P_BUF + NB * 64)
#define PD 2
#define LPOS 0x448763
#define SETP(n) __builtin_amdgcn_s_setprio(n)
#define MDL 1
#define MDM 3
#define RMAXREV 40.0f
#define HB 88
#define HREG(S, D) (HB + ((S) & 3) * 10 + (((S) >> 2) + (D)) * 2)

template <int N> struct IC { static constexpr int value = N; };
struct Raw { u32x4 o[2], i[2]; u32x2 a[2]; };
static __device__ __forceinline__ uint32_t lds_ld(uint32_t a) { return *(const LDS uint32_t*)(uintptr_t)a; }
static __device__ __forceinline__ void lds_st(uint32_t a, uint32_t v) { *(LDS uint32_t*)(uintptr_t)a = v; }
static __device__ __forceinline__ void lds_or(uint32_t a, uint32_t v) {
    __hip_atomic_fetch_or((LDS uint32_t*)(uintptr_t)a, v, __ATOMIC_RELAXED, __HIP_MEMORY_SCOPE_WORKGROUP);
}
static __device__ __forceinline__ uint32_t lds_add(uint32_t a, uint32_t v) {
    return __hip_atomic_fetch_add((LDS uint32_t*)(uintptr_t)a, v, __ATOMIC_RELAXED, __HIP_MEMORY_SCOPE_WORKGROUP);
}
static __device__ __forceinline__ uint32_t slot_w(uint32_t ca, uint32_t cv) {
    return (ca << 4) | __builtin_amdgcn_bitop3_b32(cv >> 11, ca, 0x3cu, 0x28);
}
static __device__ __forceinline__ uint32_t slot_x2(uint32_t aw, uint32_t ca) {
    return __builtin_amdgcn_bitop3_b32(aw, ca, 0xcu, 0x72);
}
static __device__ __forceinline__ uint32_t slot_x(uint32_t aw) { return slot_x2(aw, aw >> 4); }
template <int R> static __device__ __forceinline__ u32x2 hget() {
    u32x2 d;
    asm volatile("v_mov_b64 %0, v[%1:%2]" : "=v"(d) : "n"(R), "n"(R + 1));
    return d;
}
static __device__ __forceinline__ u32x2 hget_at(int r) {
    u32x2 d;
    asm volatile("s_set_gpr_idx_on %2, gpr_idx(SRC0)\n\ts_nop 1\n\tv_mov_b32 %0, v88\n\tv_mov_b32 %1, v89\n\ts_set_gpr_idx_off"
                 : "=&v"(d.x), "=&v"(d.y) : "s"(r));
    return d;
}
static __device__ __forceinline__ void hset_at(int r, u32x2 d) {
    asm volatile("s_set_gpr_idx_on %2, gpr_idx(DST)\n\ts_nop 1\n\tv_mov_b32 v88, %0\n\tv_mov_b32 v89, %1\n\ts_set_gpr_idx_off"
                 :: "v"(d.x), "v"(d.y), "s"(r));
}

template <int S, int D> static __device__ __forceinline__ uint32_t take(uint32_t V, uint32_t u16, uint32_t la, u32x2 nul) {
    const uint32_t a = __builtin_amdgcn_bitop3_b32(V, u16, 15u, 0x54);
    asm volatile("v_mov_b64 v[%4:%4+1], v[%3:%3+1]\n\tv_cmpx_ne_u32 vcc, -16, %0\n\tds_wrxchg_rtn_b64 v[%4:%4+1], %1, %2 offset:%5\n\ts_mov_b64 exec, -1"
                 :: "v"(a), "v"(la), "v"(nul), "n"(HREG(S, D)), "n"(HREG(S, 1 - D)), "n"(S * 256) : "memory", "vcc");
    return a;
}
#define PUT(P) asm volatile("s_bitcmp1_b32 %3, %4\n\ts_cbranch_scc0 1f\n\ts_waitcnt lgkmcnt(%5)\n\tbuffer_store_dwordx4 v[%2:%2+3], %0, %1, 0 offen sc1\n" \
                 "1:\n\ts_bitcmp1_b32 %3, %4+1\n\ts_cbranch_scc0 2f\n\ts_waitcnt lgkmcnt(%5)\n\tbuffer_store_dwordx4 v[%2:%2+3], %0, %1, 0 offen " P "\n\ts_nop 1\n2:" \
                 :: "v"(ad), "s"(rsA), "n"(HREG(S, 0)), "s"(pm), "n"(2 * S), "n"(W) : "memory", "scc")
template <int S, int W, bool WT> static __device__ __forceinline__ void put(u32x4 rsA, uint32_t ad, uint32_t pm) {
    if (WT) PUT("sc1 nt"); else PUT("nt");
}

#define CODE_AHEAD(SYM, NEXT, LANE) ({ u64 ca_; int cb_; \
    asm volatile("s_getpc_b64 vcc\n\ts_add_u32 vcc_lo, vcc_lo, " #SYM "@rel32@lo+4\n\ts_addc_u32 vcc_hi, vcc_hi, " #SYM "@rel32@hi+12\n\t" \
                 "s_mov_b64 %0, vcc\n\ts_max_i32 %1, " #NEXT "-" #SYM ", 0" : "=s"(ca_), "=s"(cb_) :: "scc", "vcc"); \
    __builtin_amdgcn_raw_buffer_load_b32(__builtin_amdgcn_make_buffer_rsrc((void*)ca_, 0, cb_, 0x00020000), ((blockIdx.x >> 3) * 64u + (LANE)) * 64u, 0, 0); })
extern "C" __global__ __launch_bounds__(NT, 4) __attribute__((amdgpu_num_vgpr(HB / 2))) void part_kernel(const int* __restrict__ iin, const int* __restrict__ iout,
                                                  const uint16_t* __restrict__ ang, const uint16_t* __restrict__ freq,
                                                  const u32x2* __restrict__ m, const uint16_t* __restrict__ wdown,
                                                  char* __restrict__ recs,
                                                  int T, int E, int ntile, int nstat, int ks, int lcap) {
    asm volatile("" :: "s"(iin), "s"(iout), "s"(ang), "s"(freq), "s"(m), "s"(wdown), "s"(recs), "s"(T), "s"(E), "s"(ntile), "s"(nstat), "s"(ks), "s"(lcap));
    const int tid0 = threadIdx.x;
    const int wv = __builtin_amdgcn_readfirstlane(tid0 >> 6);
    const int G = T >> 2;
    const int pos = (int)((blockIdx.x & 7) << 5) | (int)(blockIdx.x >> 3);
    float frr;
    unsigned* meta = (unsigned*)(recs - W_RECS);
    const uint32_t gbase = (LBASE + (uint32_t)lcap) * 128u;
    const uint32_t rbytes = gbase + (uint32_t)lcap * 2u;
    const __amdgpu_buffer_rsrc_t rsR = __builtin_amdgcn_make_buffer_rsrc(recs, 0, (int)rbytes, 0x00020000);
    const u32x4 rsA = {(uint32_t)(uintptr_t)recs, (uint32_t)((uintptr_t)recs >> 32) & 0xffffu, rbytes, 0x00020000u};
    const uint32_t g2 = 2u * (uint32_t)ks;
    const uint32_t far = 1u >> ((blockIdx.x >> 3) & 3u);
    const uint32_t SK = SKM + far * (SKH - SKM), SS = SSM + far * (SSH - SSM), UINC = 0x80000001u + SK * 64u;
    const uint32_t spec0 = gbase - 4u;
    auto fixed_tile = [&](int r) __attribute__((always_inline)) -> int { return r * NWG + ((pos + r * 37) & (NWG - 1)); };
    int tl0 = fixed_tile(0), tl1 = fixed_tile(1), tl2;
    uint32_t tk = 0u;
    const __amdgpu_buffer_rsrc_t rsO = __builtin_amdgcn_make_buffer_rsrc((void*)iout, 0, G * 16, 0x00020000);
    const __amdgpu_buffer_rsrc_t rsI = __builtin_amdgcn_make_buffer_rsrc((void*)iin, 0, G * 16, 0x00020000);
    const __amdgpu_buffer_rsrc_t rsG = __builtin_amdgcn_make_buffer_rsrc((void*)ang, 0, G * 8, 0x00020000);
    uint32_t t8 = (uint32_t)tid0 * 8u;
    auto load_one = [&](Raw& r, int tl, int idx) __attribute__((always_inline)) {
        const int j = idx < 4 ? idx >> 1 : idx & 1;
        const uint32_t g0 = (uint32_t)(j ? (tl < ntile ? tl * GR + NT : G) : (tl + min(tl, ntile)) * NT);
        __builtin_amdgcn_sched_barrier(0);
        if (idx >= 4) r.a[j] = __builtin_amdgcn_raw_buffer_load_b64(rsG, t8, g0 * 8u, AUX_NT);
        else if (idx & 1) r.i[j] = __builtin_amdgcn_raw_buffer_load_b128(rsI, t8 + t8, g0 * 16u, AUX_NT);
        else r.o[j] = __builtin_amdgcn_raw_buffer_load_b128(rsO, t8 + t8, g0 * 16u, AUX_NT);
        __builtin_amdgcn_sched_barrier(0);
    };
    auto peek = [&](uint32_t at) __attribute__((always_inline)) -> uint32_t {
        return __hip_atomic_load((unsigned*)((char*)meta + at), __ATOMIC_RELAXED, __HIP_MEMORY_SCOPE_AGENT);
    };
    auto ready = [&]() __attribute__((always_inline)) {
        if (blockIdx.x != 0) {
            const uint32_t x = lds_ld(vconst<P_FLAG + 8>());
            while (peek(vconst<M_BEAT * 4>()) == x) __builtin_amdgcn_s_sleep(8);
        }
    };
    auto beat = [&]() __attribute__((always_inline)) {
        (void)__hip_atomic_fetch_add((unsigned*)((char*)meta + vconst<M_BEAT * 4>()), vconst<1>(), __ATOMIC_RELAXED, __HIP_MEMORY_SCOPE_AGENT);
    };
    auto draw = [&](uint32_t at) __attribute__((always_inline)) -> uint32_t {
        return __hip_atomic_fetch_add((unsigned*)((char*)meta + at), vconst<1>(), __ATOMIC_RELAXED, __HIP_MEMORY_SCOPE_AGENT);
    };
    uint32_t la;
    auto tab_at = [&]() __attribute__((always_inline)) -> uint32_t {
        return P_TAB + bfi(0xffu, bfi(0x3cu, t8 >> 2, t8 << 4), t8);
    };
    u32x2 nul;
    uint32_t U[2];
    {
        const uint32_t t = (uint32_t)tid0;
        la = P_BUF + ((t >> 5) << 12) + ((t & 0x18u) << 3) + ((t & 3u) << 4) + ((t & 4u) << 1);
        const uint32_t q = ((t >> 3) ^ (t >> 1 & 2u)) & 3u;
        nul.x = (0x7u >> q) << 31; nul.y = (0xbu >> q) << 31;
        U[0] = (((t >> 1) & 15u) * SS + ((t & 1u) * 64u + (t >> 5)) * SC + BPL(blockIdx.x)) * 128u + (0x80000000u - SK * 64u) + 8u - g2;
        U[1] = U[0] + 32u * SC * 128u;
    }
    auto log_line = [&](bool lg, uint32_t b) __attribute__((always_inline)) -> uint32_t {
        uint32_t lp = 0u;
        if (lg && (t8 & 0x38u) == 0u) {
            ready();
            lp = draw(vconst<M_LOG * 4>());
            __builtin_amdgcn_raw_buffer_store_b16((uint16_t)b, rsR, gbase + lp * 2u, 0, 0);
        }
        return LBASE + (uint32_t)__builtin_amdgcn_ds_bpermute((int)((t8 >> 1) & 0xe0u), (int)lp);
    };
    auto general = [&](int D, bool all, u64 sm) __attribute__((always_inline)) {
        uint32_t todo = __builtin_amdgcn_readfirstlane((uint32_t)sm | (uint32_t)(sm >> 32));
#pragma unroll 1
        while (todo) {
            const uint32_t S = (uint32_t)__builtin_ctz(todo) >> 1;
            todo &= ~(3u << (2u * S));
            pin(t8); pin(la);
            const uint32_t V = lds_ld(P_TAB + (t8 & ~63u) + S * 4u), g = V & 15u;
            const uint32_t col = S * 128u + ((t8 >> 1) & 0x60u) + (t8 >> 8);
            const bool act = all ? V != 0xffffffffu : (V & 0x80000009u) == 9u;
            const int hr = HREG((int)S, D) - HB;
            uint32_t ln = log_line(act && g > 8u + NTAIL, col);
            if (act) {
                const u32x2 N = *(const LDS u32x2*)(uintptr_t)(la + S * 256u);
                *(LDS u32x2*)(uintptr_t)(la + S * 256u) = nul;
                lds_st(P_TAB + (t8 & ~63u) + S * 4u, vconst<0xffffffffu>());
                if (!(g & 1u)) hset_at(hr, N);
                else {
                    uint32_t a, st;
                    if (g < 8u) { a = (V & ~15u) + ((t8 & 0x38u) << 1); st = 8u; }
                    else {
                        if (g <= 8u + NTAIL) ln = TBASE + ((g - 8u) >> 1) * TPL + col * TST + blockIdx.x;
                        a = ln * 128u + (la & 0x38u); st = 64u;
                    }
                    __builtin_amdgcn_raw_buffer_store_b64(hget_at(hr), rsR, a, 0, AUX_SC1);
                    __builtin_amdgcn_raw_buffer_store_b64(N, rsR, a + st, 0, AUX_SC1);
                }
            }
        }
    };
    unsigned epoch = 0;
    u64 pb2 = 0ull;
    u64 fm = 0ull, sm = 0ull;
    auto look = [&](u32x2 c) __attribute__((always_inline)) {
        uint32_t V[2];
        const bool late = __builtin_amdgcn_ballot_w64(((U[0] | U[1]) & 8u) != 0u) != 0ull;
        epoch++;
        asm volatile("v_mov_b32 %0, -1\n\tv_mov_b32 %1, -1\n\t"
                     "v_cmpx_le_u32 %6, %7, %2\n\tv_mov_b32 %0, %4\n\tv_add_u32 %4, %9, %4\n\tv_subrev_u32 %2, %8, %2\n\ts_mov_b64 exec, -1\n\t"
                     "v_cmpx_le_u32 vcc, %8, %3\n\tv_mov_b32 %1, %5\n\tv_add_u32 %5, %9, %5\n\tv_subrev_u32 %3, %8, %3\n\t"
                     "s_or_b64 %6, %6, exec\n\ts_mov_b64 exec, -1"
                     : "=&v"(V[0]), "=&v"(V[1]), "+v"(c.x), "+v"(c.y), "+v"(U[0]), "+v"(U[1]), "=&s"(fm) : "s"(CHV), "n"(CHV), "s"(UINC) : "vcc", "scc");
        *(LDS u32x2*)(uintptr_t)(P_CNT + t8) = c;
        pin(t8);
        const uint32_t tw = tab_at();
        lds_st(tw, V[0]);
        lds_st(tw + 64u, V[1]);
        if (wv == 1) lds_st(P_CNT + NB * 4 - 256 + (t8 >> 1), vconst<0x40000000u>());
        pb2 = __builtin_amdgcn_ballot_w64((int)(V[0] & V[1]) >= 0);
        sm = 0ull;
        if (__builtin_expect(late, 0)) {
            sm = __builtin_amdgcn_ballot_w64((V[0] & 0x80000009u) == 9u || (V[1] & 0x80000009u) == 9u);
#pragma unroll
            for (int e = 0; e < 2; e++)
                if ((V[e] & 0x8000000fu) == 9u + NTAIL) U[e] -= 2u * UINC;
        }
    };
    u32x4 Va, Vb;

    Raw raw[PD];
    {
        const int tid = tid0;
        asm volatile("" ::: "v88", "v127");
        typedef const __attribute__((address_space(4))) u32x4* cptr;
        u32x4 wd0 = ((cptr)(uintptr_t)wdown)[0], wd1 = ((cptr)(uintptr_t)wdown)[1];
        __builtin_amdgcn_sched_barrier(0);
        if (wv == 5) tk = CODE_AHEAD(part_kernel, acc_kernel, (uint32_t)tid & 63u);
        if (tid == 128) tk = peek(vconst<M_BEAT * 4>());
        if (blockIdx.x == 0 && wv == 3 && (tid & 63) < M_BEAT / 64) {
            __hip_atomic_store(meta + (tid & 63) * 64, 0u, __ATOMIC_RELAXED, __HIP_MEMORY_SCOPE_AGENT);
            __hip_atomic_store(meta + (tid & 63) * 64 + 1, 0u, __ATOMIC_RELAXED, __HIP_MEMORY_SCOPE_AGENT);
        }
        const __amdgpu_buffer_rsrc_t rsE = __builtin_amdgcn_make_buffer_rsrc((void*)m, 0, E * 8, 0x00020000);
        u32x2 mv[5];
        mv[4] = __builtin_amdgcn_raw_buffer_load_b64(rsE, (t8 & 511u) * (uint32_t)(E >> 6), 0, 0);
#pragma unroll
        for (int q = 0; q < 4; q++)
            mv[q] = __builtin_amdgcn_raw_buffer_load_b64(rsE, t8 & 511u, ((blockIdx.x >> 2) * 256u + (blockIdx.x & 3u) * 16u + (uint32_t)wv + 64u * q) * 512u, AUX_NT);
#pragma unroll
        for (int d = 0; d < MDL; d++) load_one(raw[1], tl0, d);
        asm volatile("" : "+s"(wd0), "+s"(wd1));
        const u32x2 ci = {CINIT, CINIT};
        *(LDS u32x2*)(uintptr_t)(P_CNT + tid * 8) = ci;
        if (tid < 64) lds_st(P_CNT + (NB + tid) * 4, 0x40000000u);
        if (tid < 8 && tid != 2) lds_st(P_FLAG + tid * 4, tid == 0 ? 0xffffffffu : 0u);
        if (tid == 128) lds_st(P_FLAG + 8, tk);
        const uint32_t lane = (uint32_t)tid & 63u;
        int e0[4];
        {
            float w[16];
#pragma unroll
            for (int i = 0; i < 4; i++) {
                w[2 * i] = bf2f(wd0[i] & 0xffffu); w[2 * i + 1] = bf2f(wd0[i] >> 16);
                w[8 + 2 * i] = bf2f(wd1[i] & 0xffffu); w[8 + 2 * i + 1] = bf2f(wd1[i] >> 16);
            }
            auto dot = [&](u32x2 v, int c) __attribute__((always_inline)) -> f32x2 {
                const f32x2 w0 = {w[c], w[c + 1]}, w1 = {w[4 + c], w[5 + c]}, w2 = {w[8 + c], w[9 + c]}, w3 = {w[12 + c], w[13 + c]};
                return w0 * bf2f(v.x & 0xffffu) + w1 * bf2f(v.x >> 16) + w2 * bf2f(v.y & 0xffffu) + w3 * bf2f(v.y >> 16);
            };
#pragma unroll
            for (int cp = 0; cp < 2; cp++) {
                if (cp == 1) {
#pragma unroll
                    for (int d = MDL; d < MDM; d++) load_one(raw[1], tl0, d);
                }
                if (blockIdx.x >= SLOTI / 64) continue;
                const f32x2 sd = dot(mv[4], 2 * cp);
#pragma unroll
                for (int e = 0; e < 2; e++) {
                    const int ex = (int)(wave_max(__float_as_uint(sd[e]) & 0x7fffffffu) >> 23);
                    e0[2 * cp + e] = __builtin_amdgcn_readfirstlane(ex == 0 ? 0 : min(max(ex - 129, -100), 100));
                }
                const f32x2 sc = {__uint_as_float((uint32_t)(127 - e0[2 * cp]) << 23), __uint_as_float((uint32_t)(127 - e0[2 * cp + 1]) << 23)};
                u32x4 o;
                u16x2 mx = {0, 0};
                f32x2 sq = {0.f, 0.f}, sg = {0.f, 0.f};
#pragma unroll
                for (int q = 0; q < 4; q++) {
                    const f32x2 y = dot(mv[q], 2 * cp) * sc;
                    const uint32_t hb = pk_bf16(y.x, y.y);
                    const f32x2 x = {__uint_as_float(hb << 16), __uint_as_float(hb & 0xffff0000u)};
                    o[q] = __builtin_bit_cast(uint32_t, __builtin_amdgcn_cvt_pkrtz(x.x, x.y));
                    mx = __builtin_elementwise_max(mx, __builtin_bit_cast(u16x2, hb & 0x7fff7fffu));
                    sq += x * x;
                    sg += x;
                }
                *(LDS u32x4*)(uintptr_t)(P_BUF + (cp * 64u + lane) * 272u + wv * 16) = o;
#pragma unroll
                for (int e = 0; e < 2; e++) {
                    const u32x4 st = {wave_max(mx[e]), __float_as_uint(wave_sum(sq[e])), __float_as_uint(wave_sum(sg[e])), 0u};
                    if (lane == 0u) *(LDS u32x4*)(uintptr_t)(P_LST + ((2 * cp + e) * 16 + wv) * 16) = st;
                }
            }
        }
        __builtin_amdgcn_sched_barrier(0);
        const uint16_t fq = freq[0];
#pragma unroll
        for (int d = MDM; d < 6; d++) load_one(raw[1], tl0, d);
#pragma unroll
        for (int d = 0; d < 6; d++)
            if (((LPOS >> (4 * d)) & 15) < 4) load_one(raw[0], tl1, d);
        __syncthreads();
        u32x4 mo[2];
#pragma unroll
        for (int cp = 0; cp < 2; cp++) mo[cp] = *(const LDS u32x4*)(uintptr_t)(P_BUF + (cp * 64u + (t8 >> 7)) * 272u + ((t8 + t8) & 0xf0u));
        if (blockIdx.x == 0 && wv == 3) asm volatile("s_waitcnt vmcnt(0)" ::: "memory");
        __syncthreads();
#pragma unroll
        for (int s = 0; s < 16; s++) *(LDS u32x2*)(uintptr_t)(la + s * 256) = nul;
        asm volatile("" : "+v"(raw[1].o[0]), "+v"(raw[1].o[1]), "+v"(raw[1].i[0]), "+v"(raw[1].i[1]), "+v"(raw[1].a[0]), "+v"(raw[1].a[1]));
        asm volatile("s_nop 0\n\tv_readfirstlane_b32 %0, %1" : "=s"(frr) : "v"(bf2f(fq) * 0.15915494309189535f));
        if (blockIdx.x < SLOTI / 64) {
            const __amdgpu_buffer_rsrc_t rsM = __builtin_amdgcn_make_buffer_rsrc((void*)(meta + W_MDC / 4), 0, (int)(128u * SLOTI * 4u), 0x00020000);
#pragma unroll
            for (int cp = 0; cp < 2; cp++) {
                const uint32_t row = cp * 64u + (t8 >> 7), c16 = (t8 + t8) & 0xf0u;
                __builtin_amdgcn_raw_buffer_store_b128(mo[cp], rsM, row * (SLOTI * 4u) + c16 + blockIdx.x * 256u, 0, AUX_SC1);
            }
            if (wv == 0) {
                const u32x4 st = *(const LDS u32x4*)(uintptr_t)(P_LST + t8 * 2u);
                const float sm1 = row_sum(__uint_as_float(st.z));
                const u32x2 r = {row_max(st.x) | (row_max(st.x ? 0xffffu - st.x : 0u) << 16), __float_as_uint(row_sum(__uint_as_float(st.y)))};
                if ((lane & 15u) == 15u) {
                    ((u32x2*)(meta + W_STAT / 4))[(lane >> 4) * NWG + blockIdx.x] = r;
                    meta[W_SUM / 4 + (lane >> 4) * NWG + blockIdx.x] = __float_as_uint(sm1);
                }
                if (blockIdx.x == 0 && lane < 4u) meta[M_E0 + lane] = (uint32_t)(lane == 0u ? e0[0] : lane == 1u ? e0[1] : lane == 2u ? e0[2] : e0[3]);
            }
        }
    }
    pin(la);

    uint32_t w[8], aw[8], xv[8];
    u64 nm[8];
    auto later2 = [&](int k) __attribute__((always_inline)) {
        uint32_t x0, x1;
        asm volatile("v_lshrrev_b32 %0, 4, %2\n\tv_lshrrev_b32 %1, 4, %5\n\tv_bitop3_b32 %0, %2, %0, 12 bitop3:0x72\n\tv_bitop3_b32 %1, %5, %1, 12 bitop3:0x72\n\t"
                     "s_not_b64 exec, %8\n\tds_write_b32 %2, %3 offset:%10\n\tds_or_b32 %0, %4 offset:%10\n\t"
                     "s_not_b64 exec, %9\n\tds_write_b32 %5, %6 offset:%10\n\tds_or_b32 %1, %7 offset:%10\n\ts_mov_b64 exec, -1"
                     : "=&v"(x0), "=&v"(x1)
                     : "v"(aw[k]), "v"(w[k]), "v"(xv[k]), "v"(aw[k + 1]), "v"(w[k + 1]), "v"(xv[k + 1]), "s"(nm[k]), "s"(nm[k + 1]), "n"(P_BUF) : "memory", "scc");
    };
    auto drain = [&]() __attribute__((always_inline)) {
        if (wv == 1) {
            const uint32_t q = (epoch + 1u) & 3u;
            const uint32_t n = __builtin_amdgcn_readfirstlane(lds_ld(vconst<P_FLAG + 16>() + q * 4u));
            if (__builtin_expect(n != 0u, 0)) {
                pin(t8);
                const uint32_t l = (t8 >> 3) & 63u;
                if (l < min(n, (uint32_t)LCAP)) {
                    const u32x4 e = *(const LDS u32x4*)(uintptr_t)(P_LST + (q * LCAP + l) * 16u);
                    lds_st(P_BUF + e.x, e.y);
                    lds_or(P_BUF + slot_x(e.x), e.z);
                }
                lds_st(vconst<P_FLAG + 16>() + q * 4u, vconst<0>());
            }
        }
    };
    auto again = [&](int D) __attribute__((always_inline)) {
        do {
            general(D, true, fm);
            __syncthreads();
            later2(0); later2(2); later2(4); later2(6);
            drain();
#pragma unroll
            for (int k = 0; k < 8; k++) {
                pin(aw[k]);
                bool due = false;
                if (aw[k] & 0x80000000u) {
                    xv[k] -= CHV;
                    if (xv[k] < CHV) {
                        const uint32_t f = (aw[k] >> 16) & 0x3ffu;
                        aw[k] = slot_w(aw[k] & 0xffffu, xv[k]);
                        xv[k] = f << ((xv[k] >> 8) & 31u);
                        due = true;
                    } else lds_st(vconst<P_FLAG>(), epoch + 1u);
                }
                nm[k] = ~__builtin_amdgcn_ballot_w64(due);
            }
            look(*(const LDS u32x2*)(uintptr_t)(P_CNT + t8));
            asm volatile("s_waitcnt lgkmcnt(0)" ::: "memory");
            __syncthreads();
        } while (__builtin_amdgcn_readfirstlane(lds_ld(vconst<P_FLAG>())) == epoch);
    };
    auto round = [&](auto DD, auto LL, Raw& rc, Raw& rn, int rd) __attribute__((always_inline)) {
        constexpr int D = decltype(DD)::value;
        constexpr bool LAST = decltype(LL)::value == 1, FIRST = decltype(LL)::value == 2, HALF = decltype(LL)::value == 3;
        const int st0 = HALF ? (tl0 + ntile) * NT : tl0 * GR, ng = min(HALF ? NT : GR, G - st0);
        auto hook = [&](int p) __attribute__((always_inline)) {
#pragma unroll
            for (int q = 0; q < 6; q++)
            {
                const int lp = (LPOS >> (4 * q)) & 15;
                if (!LAST && !HALF && lp < 4 && lp == p) load_one(rc, tl2, q);
                if (!LAST && !HALF && lp >= 4 && (lp < 8 || lp >= 12 ? lp : lp - 8) == p) load_one(rn, tl1, q);
            }
        };
        uint32_t wn[8], ca[8], px[4], cv[8];
        float mx;
        float cs[2], cm[2] = {0.f, 0.f};
        auto convA = [&](int q) __attribute__((always_inline)) {
            if (LAST || (HALF && q > 1)) return;
            const uint32_t a = rc.a[q >> 1][q & 1];
            const f32x2 av = {__uint_as_float(a << 16), __uint_as_float(a & 0xffff0000u)};
            f32x2 rv;
            asm("v_pk_mul_f32 %0, %1, %2 op_sel_hi:[1,0]" : "=v"(rv) : "v"(av), "s"((u64)__float_as_uint(frr)));
            mx = __builtin_elementwise_maximum(__builtin_elementwise_maximum(__builtin_fabsf(rv.x), __builtin_fabsf(rv.y)), (q & 1) ? mx : 0.f);
            cs[0] = __builtin_amdgcn_cosf(rv.x); cs[1] = __builtin_amdgcn_cosf(rv.y);
            asm volatile("" : "+v"(mx), "+v"(cs[0]), "+v"(cs[1]));
            if (FIRST) {
                const float c0 = fminf(fmaxf(cs[0], -1.f), 1.f), c1 = fminf(fmaxf(cs[1], -1.f), 1.f);
                cm[0] += c0 + c1; cm[1] += c0 * c0 + c1 * c1;
                if (q == 3 && wv == 4) {
                    const float a0 = wave_sum(cm[0]), a1 = wave_sum(cm[1]);
                    if ((t8 & 0x1f0u) == 0u) meta[W_SUM / 4 + (4 + ((t8 >> 3) & 1u)) * NWG + blockIdx.x] = __float_as_uint((t8 & 8u) ? a1 : a0);
                }
            }
            __builtin_amdgcn_sched_barrier(0);
        };
        auto convB = [&](int k) __attribute__((always_inline)) {
            if (LAST || (HALF && k > 3)) return;
            const uint32_t o = rc.o[k >> 2][k & 3], i = rc.i[k >> 2][k & 3];
            wn[k] = (__builtin_amdgcn_alignbit(bfi(63u, o, o >> 5), __float_as_uint(cs[k & 1]), 30u) << 14) | (i >> 6);
            ca[k] = (bfi(63u, i, o) << 2) & 0x1ffcu;
            pin(wn[k]); pin(ca[k]);
            __builtin_amdgcn_sched_barrier(0);
        };
        auto convD = [&](int q) __attribute__((always_inline)) {
            if (LAST || (HALF && q > 1)) return;
            asm volatile("v_cvt_pk_f16_f32 %0, |%1|, |%2|\n\tv_pk_mul_f16 %0, %0, %3 op_sel_hi:[1,0]" : "=v"(px[q]) : "v"(cs[0]), "v"(cs[1]), "s"(0x3ffu));
        };
        uint32_t cst;
        auto check = [&](int j) __attribute__((always_inline)) {
            if (LAST || (HALF && j)) return;
            if (__builtin_expect((__builtin_amdgcn_ballot_w64(!(mx <= RMAXREV)) | (u64)(uint32_t)((HALF ? NT : GR) - ng)) != 0ull, 0)) {
                pin(t8);
                asm volatile("" : "+v"(rc.a[j]));
                const int lg = j * NT + (int)(t8 >> 3);
#pragma unroll
                for (int k = 0; k < 4; k++) {
                    const uint32_t a = (k & 1) ? (rc.a[j][k >> 1] >> 16) : (rc.a[j][k >> 1] & 0xffffu);
                    const bool nrm = __builtin_fabsf(frr * bf2f(a)) <= RMAXREV;
                    if (!(lg < ng && nrm)) ca[j * 4 + k] = NB * 4u + ((t8 >> 1) & 0xfcu);
                    if (lg < ng && !nrm) {
                        ready();
                        __builtin_amdgcn_raw_buffer_store_b32((uint32_t)((st0 + lg) * 4 + k), rsR, spec0 - draw(vconst<M_LOG * 4 + 4>()) * 4u, 0, 0);
                    }
                }
            }
            cst = vconst<CSTEP>();
        };
        auto ticket = [&](int k) __attribute__((always_inline)) {
            if (LAST || (HALF && k > 3)) return;
            cv[k] = lds_add(P_CNT + ca[k], cst);
            __builtin_amdgcn_sched_barrier(0);
        };
        auto insert = [&](int j) __attribute__((always_inline)) {
            const int k0 = 4 * j;
            const bool odd = __builtin_amdgcn_ballot_w64(max(max(cv[k0], cv[k0 + 1]), max(cv[k0 + 2], cv[k0 + 3])) >= 2u * CHV) != 0ull;
#pragma unroll
            for (int k = k0; k < k0 + 4; k += 2) {
                uint32_t x0, x1;
                asm volatile("v_lshrrev_b32 %6, 11, %8\n\tv_lshrrev_b32 %7, 11, %9\n\t"
                             "v_bitop3_b32 %6, %6, %10, 60 bitop3:0x28\n\tv_bitop3_b32 %7, %7, %11, 60 bitop3:0x28\n\t"
                             "v_lshl_or_b32 %0, %10, 4, %6\n\tv_lshl_or_b32 %1, %11, 4, %7\n\t"
                             "v_lshlrev_b32_sdwa %2, %8, %12 dst_sel:DWORD dst_unused:UNUSED_PAD src0_sel:BYTE_1 src1_sel:WORD_0\n\t"
                             "v_lshlrev_b32_sdwa %3, %9, %12 dst_sel:DWORD dst_unused:UNUSED_PAD src0_sel:BYTE_1 src1_sel:WORD_1\n\t"
                             "v_bitop3_b32 %6, %0, %10, 12 bitop3:0x72\n\tv_bitop3_b32 %7, %1, %11, 12 bitop3:0x72\n\t"
                             "v_cmpx_gt_u32 %4, %15, %8\n\tds_write_b32 %0, %13 offset:%16\n\tds_or_b32 %6, %2 offset:%16\n\ts_mov_b64 exec, -1\n\t"
                             "v_cmpx_gt_u32 %5, %15, %9\n\tds_write_b32 %1, %14 offset:%16\n\tds_or_b32 %7, %3 offset:%16\n\ts_mov_b64 exec, -1"
                             : "=&v"(aw[k]), "=&v"(aw[k + 1]), "=&v"(xv[k]), "=&v"(xv[k + 1]), "=&s"(nm[k]), "=&s"(nm[k + 1]), "=&v"(x0), "=&v"(x1)
                             : "v"(cv[k]), "v"(cv[k + 1]), "v"(ca[k]), "v"(ca[k + 1]), "v"(px[k >> 1]), "v"(wn[k]), "v"(wn[k + 1]), "s"(CHV), "n"(P_BUF) : "memory");
                w[k] = wn[k]; w[k + 1] = wn[k + 1];
                if (__builtin_expect(odd, 0)) {
#pragma unroll
                    for (int q = k; q < k + 2; q++) {
                        nm[q] |= __builtin_amdgcn_ballot_w64(cv[q] >= 2u * CHV);
                        if (cv[q] >= 2u * CHV && cv[q] < 0x40000000u) {
                            const uint32_t lq = (epoch + 2u) & 3u;
                            uint32_t p = LCAP;
                            if (cv[q] < 3u * CHV) p = lds_add(vconst<P_FLAG + 16>() + lq * 4u, vconst<1>());
                            if (p < LCAP) {
                                const u32x2 e = {aw[q], w[q]};
                                *(LDS u32x2*)(uintptr_t)(P_LST + (lq * LCAP + p) * 16u) = e;
                                lds_st(P_LST + (lq * LCAP + p) * 16u + 8u, xv[q]);
                            } else {
                                aw[q] = ca[q] | ((q & 1) ? (px[q >> 1] & 0xffff0000u) : (px[q >> 1] << 16)) | 0x80000000u;
                                xv[q] = cv[q] - CHV;
                                lds_st(vconst<P_FLAG>(), epoch);
                            }
                        }
                    }
                }
            }
        };
        constexpr int P0 = D ? 0 : 3, P1 = D ? 1 : 2, P2 = D ? 2 : 1, P3 = D ? 3 : 0;
        const uint32_t fl = lds_ld(vconst<P_FLAG>());
        if (HALF) asm volatile("" : "+v"(rc.o[0]), "+v"(rc.i[0]), "+v"(rc.a[0]));
        else if (!LAST) asm volatile("" : "+v"(rc.o[0]), "+v"(rc.o[1]), "+v"(rc.i[0]), "+v"(rc.i[1]), "+v"(rc.a[0]), "+v"(rc.a[1]));
        if (!LAST && !HALF && (rd + 4 >= nstat || tl1 >= ntile) && wv == 2) {
            pin(tk);
            if ((t8 & 511u) == 0u) {
                if (tl1 >= ntile) {
                    if (rd + 3 <= nstat) ready();
                    (void)draw(vconst<M_MD * 4>());
                } else if (rd + 4 == nstat) tk = peek(vconst<M_BEAT * 4>());
                else {
                    if (rd + 3 == nstat) {
                        if (tk == lds_ld(vconst<P_FLAG + 8>())) ready();
                        tk = 0u;
                    }
                    lds_st(vconst<P_FLAG + 4>(), tk);
                    if ((int)tk < ntile - nstat * NWG) tk = draw(vconst<M_TILE * 4>());
                }
            }
        }
        if (!LAST && !HALF && blockIdx.x == 0 && wv == 3 && (t8 & 511u) == 0u) beat();
        hook(12);
        pin(t8);
        const uint32_t tr = P_TAB + (t8 & ~63u), u16 = (t8 + t8) & 0x70u;
        convA(0);
        convB(0);
        const bool ag = __builtin_amdgcn_readfirstlane(fl) == epoch;
        if (!FIRST && __builtin_expect(LAST || HALF || ag || sm != 0ull, 0)) {
            if (ag) again(D);
            general(D, false, sm);
            Va = *(const LDS u32x4*)(uintptr_t)(P_TAB + (t8 & ~63u) + P0 * 16);
            Vb = *(const LDS u32x4*)(uintptr_t)(P_TAB + (t8 & ~63u) + P1 * 16);
            asm volatile("" : "+v"(Va), "+v"(Vb));
        }
        uint32_t pq = (uint32_t)pb2 | (uint32_t)(pb2 >> 32);
        pq = (pq | (pq >> 1)) & 0x55555555u;
        pq = __builtin_amdgcn_readfirstlane(pq + (pq & (far * NEARM)));
        uint32_t ad[8];
        if (!FIRST) ad[0] = take<4 * P0 + 0, D>(Va[0], u16, la, nul);
        convB(1);
        if (!FIRST) ad[1] = take<4 * P0 + 1, D>(Va[1], u16, la, nul);
        convD(0);
        convA(1);
        if (!FIRST) ad[2] = take<4 * P0 + 2, D>(Va[2], u16, la, nul);
        convB(2);
        hook(13);
        if (!FIRST) ad[3] = take<4 * P0 + 3, D>(Va[3], u16, la, nul);
        convB(3);
        if (!FIRST) put<4 * P0 + 0, LAST ? 3 : 3, LAST>(rsA, ad[0], pq);
        if (!FIRST) ad[4] = take<4 * P1 + 0, D>(Vb[0], u16, la, nul);
        convD(1);
        check(0);
        ticket(0);
        hook(4);
        if (!FIRST) put<4 * P0 + 1, LAST ? 3 : 4, LAST>(rsA, ad[1], pq);
        if (!FIRST) ad[5] = take<4 * P1 + 1, D>(Vb[1], u16, la, nul);
        convA(2);
        ticket(1);
        if (!FIRST) put<4 * P0 + 2, LAST ? 3 : 5, LAST>(rsA, ad[2], pq);
        if (!FIRST) ad[6] = take<4 * P1 + 2, D>(Vb[2], u16, la, nul);
        convB(4);
        ticket(2);
        if (!FIRST) put<4 * P0 + 3, LAST ? 3 : 6, LAST>(rsA, ad[3], pq);
        if (!FIRST) Va = *(const LDS u32x4*)(uintptr_t)(tr + P2 * 16);
        __builtin_amdgcn_sched_barrier(0);
        if (!FIRST) ad[7] = take<4 * P1 + 3, D>(Vb[3], u16, la, nul);
        convB(5);
        ticket(3);
        SETP(2);
        if (!FIRST) put<4 * P1 + 0, LAST ? 4 : 8, LAST>(rsA, ad[4], pq);
        if (!FIRST) ad[0] = take<4 * P2 + 0, D>(Va[0], u16, la, nul);
        convD(2);
        convA(3);
        hook(5);
        if (!FIRST) put<4 * P1 + 1, LAST ? 4 : 7, LAST>(rsA, ad[5], pq);
        if (!FIRST) ad[1] = take<4 * P2 + 1, D>(Va[1], u16, la, nul);
        convB(6);
        if (!FIRST) put<4 * P1 + 2, LAST ? 4 : 6, LAST>(rsA, ad[6], pq);
        if (!FIRST) ad[2] = take<4 * P2 + 2, D>(Va[2], u16, la, nul);
        convB(7);
        if (!FIRST) put<4 * P1 + 3, LAST ? 3 : 4, LAST>(rsA, ad[7], pq);
        if (!FIRST) Vb = *(const LDS u32x4*)(uintptr_t)(tr + P3 * 16);
        __builtin_amdgcn_sched_barrier(0);
        if (!FIRST) ad[3] = take<4 * P2 + 3, D>(Va[3], u16, la, nul);
        convD(3);
        check(1);
        ticket(4);
        SETP(1);
        hook(6);
        if (!FIRST) put<4 * P2 + 0, LAST || HALF ? 4 : 5, LAST>(rsA, ad[0], pq);
        if (!FIRST) ad[4] = take<4 * P3 + 0, D>(Vb[0], u16, la, nul);
        ticket(5);
        if (!FIRST) put<4 * P2 + 1, LAST || HALF ? 4 : 6, LAST>(rsA, ad[1], pq);
        if (!FIRST) ad[5] = take<4 * P3 + 1, D>(Vb[1], u16, la, nul);
        ticket(6);
        if (!FIRST) put<4 * P2 + 2, LAST || HALF ? 4 : 7, LAST>(rsA, ad[2], pq);
        if (!FIRST) ad[6] = take<4 * P3 + 2, D>(Vb[2], u16, la, nul);
        ticket(7);
        SETP(0);
        if (!FIRST) put<4 * P2 + 3, LAST || HALF ? 3 : 7, LAST>(rsA, ad[3], pq);
        if (!FIRST) ad[7] = take<4 * P3 + 3, D>(Vb[3], u16, la, nul);
        hook(7);
        SETP(3);
        asm volatile("s_waitcnt lgkmcnt(0)" ::: "memory");
        __syncthreads();
        u32x2 cn;
        uint32_t tv;
        if (!LAST) { cn = *(const LDS u32x2*)(uintptr_t)(P_CNT + t8); tv = lds_ld(vconst<P_FLAG + 4>()); }
        __builtin_amdgcn_sched_barrier(0);
        if (!FIRST) later2(0);
        if (!FIRST) put<4 * P3, 15, LAST>(rsA, ad[4], pq);
        if (!FIRST) later2(2);
        if (!FIRST) put<4 * P3 + 1, 15, LAST>(rsA, ad[5], pq);
        if (!FIRST) drain();
        if (!LAST) {
            tl2 = tl1 >= ntile ? ntile + 4 * NWG : rd + 2 < nstat ? fixed_tile(rd + 2) : nstat * NWG + (int)__builtin_amdgcn_readfirstlane(tv);
            look(cn);
        }
        hook(0);
        SETP(2);
        if (!FIRST) later2(4);
        if (!FIRST) put<4 * P3 + 2, 15, LAST>(rsA, ad[6], pq);
        if (!FIRST) later2(6);
        if (!FIRST) put<4 * P3 + 3, 15, LAST>(rsA, ad[7], pq);
        if (!LAST) {
            hook(1);
            SETP(1);
            insert(0);
            hook(2);
            SETP(0);
            pin(t8);
            Va = *(const LDS u32x4*)(uintptr_t)(P_TAB + (t8 & ~63u) + P3 * 16);
            Vb = *(const LDS u32x4*)(uintptr_t)(P_TAB + (t8 & ~63u) + P2 * 16);
            __builtin_amdgcn_sched_barrier(0);
            if (!HALF) insert(1);
            else { nm[4] = ~0ull; nm[5] = ~0ull; nm[6] = ~0ull; nm[7] = ~0ull; }
            hook(3);
            SETP(3);
            asm volatile("s_waitcnt lgkmcnt(0)" ::: "memory");
            __syncthreads();
            tl0 = tl1; tl1 = tl2;
        }
    };
#define HMV(A, B) "v_mov_b64 v[" #A ":" #A "+1], v[" #B ":" #B "+1]\n\t"
#define HMV4(A) HMV(A + 8, A + 6) HMV(A + 6, A + 4) HMV(A + 4, A + 2) HMV(A + 2, A)
    round(IC<1>(), IC<2>(), raw[1], raw[0], 0);
    int rd = 1;
#pragma unroll 1
    for (; tl1 < ntile; rd += 2) {
        round(IC<0>(), IC<0>(), raw[0], raw[1], rd);
        round(IC<1>(), IC<0>(), raw[1], raw[0], rd + 1);
    }
    if (tl0 >= ntile) {
        asm volatile(HMV4(88) HMV4(98) HMV4(108) HMV4(118));
        raw[1].o[0] = raw[0].o[0];
    }
    raw[1].i[0] = raw[0].i[0]; raw[1].a[0] = raw[0].a[0];
    asm volatile("" : "+s"(tl0));
    if (tl0 < ntile) round(IC<0>(), IC<0>(), raw[0], raw[1], rd);
#define HMB4(A) HMV(A, A + 2) HMV(A + 2, A + 4) HMV(A + 4, A + 6) HMV(A + 6, A + 8)
    if ((tl0 + ntile) * NT >= G) asm volatile(HMB4(88) HMB4(98) HMB4(108) HMB4(118));
    asm volatile("" : "+s"(tl0));
    if ((tl0 + ntile) * NT < G) round(IC<1>(), IC<3>(), raw[1], raw[0], 0);
    if (wv == 5) tk = CODE_AHEAD(acc_kernel, gen_scatter_kernel, (t8 >> 3) & 63u);
    round(IC<0>(), IC<1>(), raw[1], raw[0], 0);
    if (__builtin_amdgcn_readfirstlane(lds_ld(P_FLAG + 16 + ((epoch + 2u) & 3u) * 4u)) != 0u) {
        asm volatile("s_waitcnt lgkmcnt(0)" ::: "memory");
        __syncthreads();
        look(*(const LDS u32x2*)(uintptr_t)(P_CNT + t8));
        general(1, true, fm);
        __syncthreads();
        drain();
    }
    {
        uint32_t* tcnt = meta + W_TCNT / 4;
        const u32x2 c = *(const LDS u32x2*)(uintptr_t)(P_CNT + t8);
        const uint32_t tw = tab_at();
#pragma unroll
        for (int e = 0; e < 2; e++) {
            const uint32_t g = U[e] & 15u;
            lds_st(tw + e * 64u, g >= 8u ? 0x80000000u : ((U[e] + ((g & 1u) ? 0u : SK * 64u)) & 0x7fffff81u) | (((7u - g) << 1) & 12u));
            *(LDS uint8_t*)(uintptr_t)(P_CNT + (t8 & ~511u) + (((tw - P_TAB) >> 2) & 127u) + e * 16u) =
                (uint8_t)(g < 8u ? 0u : ((g - 8u) >> 1) + (((c[e] + 2u * CSTEP) >> 15) << 2) + (g & 1u) * 0x50u);
        }
        asm volatile("s_waitcnt lgkmcnt(0)" ::: "memory");
        __syncthreads();
        const uint32_t u16 = (t8 + t8) & 0x70u;
        u32x4 En[4];
#pragma unroll
        for (int p = 0; p < 4; p++) En[p] = *(const LDS u32x4*)(uintptr_t)(P_TAB + (t8 & ~63u) + p * 16u);
        const u32x4 cb = *(const LDS u32x4*)(uintptr_t)(P_CNT + (t8 & ~511u) + ((t8 >> 2) & 0x70u));
        u32x2 N[16];
#pragma unroll
        for (int s = 0; s < 16; s++) N[s] = *(const LDS u32x2*)(uintptr_t)(la + s * 256u);
        if ((t8 & 0x1c0u) == 0u)
            __builtin_amdgcn_raw_buffer_store_b128(*(const LDS u32x4*)(uintptr_t)(P_CNT + (t8 & ~511u) + u16), __builtin_amdgcn_make_buffer_rsrc((void*)tcnt, 0, NWG * NB, 0x00020000),
                                                   (blockIdx.x * 16u + (uint32_t)wv) * 128u + u16, 0, AUX_SC1);
        const uint32_t sg = P_BUF + ((t8 & ~63u) << 4);
        uint32_t ps;
        auto rest = [&](auto SL) __attribute__((always_inline)) {
            constexpr int s = decltype(SL)::value;
            const uint32_t en = En[s >> 2][s & 3], by = cb[s >> 2] >> ((s & 3) * 8);
            const u32x2 Hr = hget<HREG(s, 1)>();
            const uint32_t at = ps + ((la & 0x38u) ^ ((s & 3) << 4));
            *(LDS u32x2*)(uintptr_t)at = Hr;
            *(LDS u32x2*)(uintptr_t)(at + (by & 0x40u)) = N[s];
            ps += (by & 0x3cu) << 2;
            const u32x2 Hs = (en & 1u) ? Hr : nul;
            const u32x4 d = {Hs.x, Hs.y, N[s].x, N[s].y};
            uint32_t a = __builtin_amdgcn_bitop3_b32(en, u16, 15u, 0x54);
            {
                const u32x4 rs_ = rsA;
                const uint32_t far_ = ((NEARM >> (2 * s)) & 1u) ? far : 0u;
                asm volatile("s_cmp_eq_u32 %3, 0\n\ts_cbranch_scc0 1f\n\tbuffer_store_dwordx4 %0, %1, %2, 0 offen sc1\n\ts_branch 2f\n"
                             "1:\n\tbuffer_store_dwordx4 %0, %1, %2, 0 offen sc1 nt\n2:\n\ts_nop 1" :: "v"(d), "v"(a), "s"(rs_), "s"(far_) : "memory", "scc");
            }
            if (__builtin_expect(__builtin_amdgcn_ballot_w64((en & 12u) != 0u) != 0ull, 0)) {
                const u32x4 dn = {nul.x, nul.y, nul.x, nul.y};
#pragma unroll 1
                for (uint32_t k = (en >> 2) & 3u; k > 0u; k--) {
                    a += SK * 128u;
                    __builtin_amdgcn_raw_buffer_store_b128(dn, rsR, a, 0, AUX_SC1);
                }
            }
        };
        auto pack = [&](int p) __attribute__((always_inline)) {
            const uint32_t a = (SBASE + ((((t8 >> 1) & 0x60u) + (t8 >> 8)) * 2u + p) * NWG + blockIdx.x) * 128u + u16;
            asm volatile("" ::: "memory");
#pragma unroll 1
            for (uint32_t q = 0; __builtin_amdgcn_ballot_w64(sg + q < ps) != 0ull; q += 256u) {
                const u32x4 d0 = *(const LDS u32x4*)(uintptr_t)(sg + q + u16), d1 = *(const LDS u32x4*)(uintptr_t)(sg + q + 128u + u16);
                __builtin_amdgcn_raw_buffer_store_b128(d0, rsR, sg + q < ps ? a + q * SPL : 0xffffff00u, 0, AUX_SC1);
                __builtin_amdgcn_raw_buffer_store_b128(d1, rsR, sg + q + 128u < ps ? a + (q + 128u) * SPL : 0xffffff00u, 0, AUX_SC1);
            }
            asm volatile("" ::: "memory");
        };
        asm volatile("" : "+v"(N[0]), "+v"(N[1]), "+v"(N[2]), "+v"(N[3]), "+v"(N[4]), "+v"(N[5]), "+v"(N[6]), "+v"(N[7]) :: "memory");
        asm volatile("" : "+v"(N[8]), "+v"(N[9]), "+v"(N[10]), "+v"(N[11]), "+v"(N[12]), "+v"(N[13]), "+v"(N[14]), "+v"(N[15]) :: "memory");
        ps = sg;
        rest(IC<0>()); rest(IC<1>()); rest(IC<2>()); rest(IC<3>()); rest(IC<4>()); rest(IC<5>()); rest(IC<6>()); rest(IC<7>());
        pack(0);
        ps = sg;
        rest(IC<8>()); rest(IC<9>()); rest(IC<10>()); rest(IC<11>()); rest(IC<12>()); rest(IC<13>()); rest(IC<14>()); rest(IC<15>());
        pack(1);
    }
    asm volatile("" :: "v"(tk));
    if (blockIdx.x == 0 && t8 == 1536u) {
        do {
            beat();
            __builtin_amdgcn_s_sleep(8);
        } while (peek(vconst<M_MD * 4>()) < NWG);
    }
}

#define A_MD 0
#define A_ACC (SLOTI * 4)
#define A_FLAG (A_ACC + SLOTO * 4)
#define A_LDS (A_FLAG + 512)
static_assert(SLOTI * 4 == 3 * NT * 16 + 1024, "md staging assumes three full vectors and 256 dwords");
#define BIAS 0x40004000u
#define NFAST 8
#define NLD 10
#define MDF 0x0911432
#define MDP 0xaaaaa
#define HP 2
#define SHARE (SLOTO / 8)
#define ZSIG 9.0f
#define HSLOW 0x80008000u
#define GAS __attribute__((address_space(1)))

extern "C" __global__ __launch_bounds__(NT, 4) __attribute__((amdgpu_num_vgpr(53))) void acc_kernel(char* ws, int T, int E, int ks, int lcap, float zf, int pad,
                                                 const uint16_t* __restrict__ centers, const uint16_t* __restrict__ wrbf,
                                                 const uint16_t* __restrict__ wsbf, const uint16_t* __restrict__ wup,
                                                 const u32x2* __restrict__ m, const uint16_t* __restrict__ dist, u32x2* __restrict__ out,
                                                 const int* __restrict__ iin, const int* __restrict__ iout, const uint16_t* __restrict__ ang,
                                                 const uint16_t* __restrict__ wdown, const uint16_t* __restrict__ freq) {
    const char* recs = ws + W_RECS;
    const u32x4* tcnt = (const u32x4*)(ws + W_TCNT);
    const uint32_t* mdc = (const uint32_t*)(ws + W_MDC);
    const u32x4* stat = (const u32x4*)(ws + W_STAT);
    uint32_t* partial = (uint32_t*)(ws + W_RECS + (((size_t)LBASE * 128u + (size_t)lcap * 130u + 255u) & ~(size_t)255u));
    unsigned* meta = (unsigned*)ws;
    extern __shared__ __attribute__((aligned(16))) char smem[];
    unsigned* acc = (unsigned*)(smem + A_ACC);
    unsigned* s_flag = (unsigned*)(smem + A_FLAG);
    const int tid = threadIdx.x;
    const int lane = tid & 63;
    const int wv = __builtin_amdgcn_readfirstlane(tid >> 6);
    const int cp = (blockIdx.x >> 3) & 1;
    const int h = blockIdx.x & 3;
    const int j = ((blockIdx.x >> 2) & 1) * 16 + (blockIdx.x >> 4);
    u32x4 tc;
    const __amdgpu_buffer_rsrc_t rsM = __builtin_amdgcn_make_buffer_rsrc((void*)mdc, 0, (int)(128u * SLOTI * 4u), 0x00020000);

    u32x4 pm[4];
    u32x4 ring[NLD];
    auto load_md = [&](int ff, int pl, int mdp) __attribute__((always_inline)) {
        const uint32_t so = (uint32_t)(cp * 64 + ff * 4 + h) * (SLOTI * 4u);
#pragma unroll
        for (int v = 0; v < 3; v++)
            if (pl < 0 || pl == ((mdp >> (4 * v)) & 15)) pm[v] = __builtin_amdgcn_raw_buffer_load_b128(rsM, (uint32_t)tid * 16u, so + (uint32_t)v * (NT * 16u), 0);
        if ((pl < 0 || pl == (mdp & 15)) && wv == 0) pm[3] = __builtin_amdgcn_raw_buffer_load_b128(rsM, (uint32_t)tid * 16u, so + 3u * NT * 16u, 0);
        __builtin_amdgcn_sched_barrier(0);
    };
    const uint32_t lf = (uint32_t)(tid >> 9) * (SKH * 128u) + (uint32_t)(tid & 0x1e0) * 16u + (uint32_t)(tid & 12) * 32u + (uint32_t)(tid & 16) * 4u + (uint32_t)(tid & 3) * 16u;
    const uint32_t lp = (uint32_t)(tid >> 8) * (SKM * 128u) + (uint32_t)((tid >> 2) & 63) * 128u + (uint32_t)(tid & 3) * 16u;
    auto load_main = [&](int i, int ff, bool near = false) __attribute__((always_inline)) -> u32x4 {
        const uint32_t q = (uint32_t)(((i >> 1) - HP) & 3), SK = q ? SKM : SKH, SS = q ? SSM : SSH;
        const __amdgpu_buffer_rsrc_t rs = __builtin_amdgcn_make_buffer_rsrc((void*)(recs + (QAT(q) + (uint32_t)ff * SS + (uint32_t)(h * 32 + j) * SC) * 128u), 0, ks * (int)(SK * 128u), 0x00020000);
        if (q == 0u && near) return __builtin_amdgcn_raw_buffer_load_b128(rs, lf, (i & 1) * (int)(2u * SK * 128u), 0);
        if (q == 0u) return __builtin_amdgcn_raw_buffer_load_b128(rs, lf, (i & 1) * (int)(2u * SK * 128u), AUX_NT);
        return __builtin_amdgcn_raw_buffer_load_b128(rs, lp + (uint32_t)(i & 1) * 64u, 0, 0);
    };
    const __amdgpu_buffer_rsrc_t rsT = __builtin_amdgcn_make_buffer_rsrc((void*)recs, 0, (int)(LBASE * 128u), 0x00020000);
    const uint32_t tof = (uint32_t)(lane & 2) * (TPL * 64u) + (uint32_t)(tid >> 2) * 128u + (uint32_t)(lane & 1) * 64u + 4u + (TBASE + (uint32_t)(h * 32 + j) * TST) * 128u;
    const uint32_t sp0 = (SBASE + (uint32_t)(h * 32 + j) * (2u * NWG) + (uint32_t)(tid >> 2)) * 128u;
    auto tail_rank = [&](uint32_t c, uint32_t sp, int ff, uint32_t t0, int& dst, uint32_t& en) __attribute__((always_inline)) -> int {
        const int k4 = (int)(((uint32_t)(lane & 3) + t0) * 4u) - (int)((c & 3u) * 8u);
        const int left = (int)((c >> 2) & 15u) - k4;
        const bool has = left > 0;
        uint32_t ea = tof + (uint32_t)ff * (128u * TST * 128u), eb = sp + (uint32_t)(k4 * 16);
        eb = sp0 + (uint32_t)(ff >> 3) * (NWG * 128u) + eb + __umul24(eb >> 7, (SPL - 1u) * 128u) + (uint32_t)min(max(left, 0), 4);
        asm volatile("" : "+v"(ea), "+v"(eb));
        en = k4 < 0 ? ea : eb;
        const u64 mask = __builtin_amdgcn_ballot_w64(has);
        const int n = __builtin_popcountll(mask);
        const int rank = __builtin_amdgcn_mbcnt_hi((uint32_t)(mask >> 32), __builtin_amdgcn_mbcnt_lo((uint32_t)mask, 0u));
        const int r = has ? rank : (n + lane - rank);
        dst = ((r & 15) << 4) | ((r >> 4) << 2);
        return n;
    };
    auto load_tail = [&](uint32_t en, u64& tm) __attribute__((always_inline)) -> u32x4 {
        const bool on = (uint32_t)(lane & 3) < (en & 7u);
        tm = __builtin_amdgcn_ballot_w64(on);
        const uint32_t at = (en & 0x70u) + (uint32_t)(lane & 3) * 16u;
        return __builtin_amdgcn_raw_buffer_load_b128(rsT, on ? (en & ~127u) + at + __umul24(at >> 7, (SPL - 1u) * 128u) : 0xffffff00u, 0, 0);
    };
    f32x2 fa[4][4];
    u32x4 em[2];
    u32x2 dw2;
    uint32_t wl;
    const __amdgpu_buffer_rsrc_t rsE = __builtin_amdgcn_make_buffer_rsrc((void*)m, 0, E * 8, 0x00020000);
    const __amdgpu_buffer_rsrc_t rsD = __builtin_amdgcn_make_buffer_rsrc((void*)dist, 0, E * 2, 0x00020000);
    auto acc_vec = [&](int i) __attribute__((always_inline)) -> u32x4 {
        const uint32_t a = A_ACC + (uint32_t)(i >> 4) * 256u + (uint32_t)(i & 15) * 8u;
        const u32x2 x = *(const LDS u32x2*)(uintptr_t)a, y = *(const LDS u32x2*)(uintptr_t)(a + 128u);
        const u32x4 v = {x.x, x.y, y.x, y.y};
        return v;
    };
    auto edge0 = [&](int sh) __attribute__((always_inline)) -> uint32_t {
        return (tid < SHARE / 4) ? (uint32_t)(((sh * (SHARE / 64) + (tid >> 4)) << 11) | (j << 6) | ((tid & 15) * 2)) : 0x0fffff00u;
    };
    auto load_m = [&](int sh) __attribute__((always_inline)) {
        em[0] = __builtin_amdgcn_raw_buffer_load_b128(rsE, edge0(sh) * 8u, 0, AUX_NT);
        em[1] = __builtin_amdgcn_raw_buffer_load_b128(rsE, edge0(sh) * 8u + 256u, 0, AUX_NT);
    };
    auto load_d = [&](int sh) __attribute__((always_inline)) {
        dw2.x = __builtin_amdgcn_raw_buffer_load_b32(rsD, edge0(sh) * 2u, 0, AUX_NT);
        dw2.y = __builtin_amdgcn_raw_buffer_load_b32(rsD, edge0(sh) * 2u + 64u, 0, AUX_NT);
    };
    auto load_wl = [&]() __attribute__((always_inline)) {
        const u64 p0 = (u64)wsbf, p1 = (u64)wrbf, p2 = (u64)centers, p3 = (u64)wup;
        const u64 pa = lane < 16 ? p0 : p1, pb = lane < 40 ? p2 : p3;
        wl = ((const GAS uint32_t*)(lane < 32 ? pa : pb))[lane & (lane < 32 ? 15 : lane < 40 ? 3 : 7)];
    };
    auto factor = [&](int r, bool first) __attribute__((always_inline)) {
        const float sq = 3.7982826f;
        const f32x2 d01 = {bf2f(dw2.x & 0xffffu), bf2f(dw2.x >> 16)}, d23 = {bf2f(dw2.y & 0xffffu), bf2f(dw2.y >> 16)};
        const uint32_t cw = __builtin_amdgcn_readlane(wl, 32 + (r >> 1));
        const float cn = bf2f((r & 1) ? (cw >> 16) : (cw & 0xffffu));
        f32x2 w[4];
#pragma unroll
        for (int k = 0; k < 4; k++) {
            const uint32_t x = __builtin_amdgcn_readlane(wl, (k >> 1) * 16 + r * 2 + (k & 1));
            w[k].x = bf2f(x & 0xffffu); w[k].y = bf2f(x >> 16);
        }
        const f32x2 y01 = (d01 - cn) * sq, y23 = (d23 - cn) * sq;
        const f32x2 t01 = -y01 * y01, t23 = -y23 * y23;
        const float rb[4] = {__builtin_amdgcn_exp2f(t01.x), __builtin_amdgcn_exp2f(t01.y), __builtin_amdgcn_exp2f(t23.x), __builtin_amdgcn_exp2f(t23.y)};
#pragma unroll
        for (int q = 0; q < 4; q++) {
#pragma unroll
            for (int k = 0; k < 4; k++) fa[q][k] = first ? w[k] * rb[q] : __builtin_elementwise_fma(w[k], (f32x2)(rb[q]), fa[q][k]);
        }
    };
#define ADT(V, O) "ds_write_addtid_b32 " V " offset:" #O "\n\t"
    auto store_md = [&]() __attribute__((always_inline)) {
        asm volatile("s_mov_b32 m0, %12\n\ts_nop 0\n\t" ADT("%0", 0) ADT("%1", 256) ADT("%2", 512) ADT("%3", 768) ADT("%4", 16384) ADT("%5", 16640) ADT("%6", 16896) ADT("%7", 17152)
                     ADT("%8", 32768) ADT("%9", 33024) ADT("%10", 33280) ADT("%11", 33536)
                     :: "v"(pm[0][0]), "v"(pm[0][1]), "v"(pm[0][2]), "v"(pm[0][3]), "v"(pm[1][0]), "v"(pm[1][1]), "v"(pm[1][2]), "v"(pm[1][3]),
                        "v"(pm[2][0]), "v"(pm[2][1]), "v"(pm[2][2]), "v"(pm[2][3]), "s"(wv * 1024) : "memory");
        if (wv == 0) asm volatile("s_mov_b32 m0, 0\n\ts_nop 0\n\t" ADT("%0", 49152) ADT("%1", 49408) ADT("%2", 49664) ADT("%3", 49920)
                                  :: "v"(pm[3][0]), "v"(pm[3][1]), "v"(pm[3][2]), "v"(pm[3][3]) : "memory");
    };
    const int me = h * 2 + cp;
    GAS unsigned* dw = (GAS unsigned*)(meta + M_DONE + j * 64);
    const bool fin = wv < (SHARE / 4 + 63) / 64;
    bool fast, more;
    uint32_t need, mg0, mg1;
    uint32_t guard = 0;
    uint32_t sgn = 0x8000u;
    pin(sgn);

#define GAT3(M) { uint32_t a0_, a1_, a2_; asm volatile( \
        "v_lshlrev_b16 %0, 2, %3\n\tv_lshlrev_b16 %1, 2, %4\n\tv_lshlrev_b16 %2, 2, %5\n\t" \
        "ds_read_b32 v[%6], %0\n\tds_read_b32 v[%6+1], %1\n\tds_read_b32 v[%6+2], %2" \
        : "=&v"(a0_), "=&v"(a1_), "=&v"(a2_) : "v"(w0), "v"(w1), "v"(w2), "n"(M) : "memory"); }
#define MIXL " op_sel_hi:[1,1,0]\n\t"
#define MIXH " op_sel:[0,1,0] op_sel_hi:[1,1,0]\n\t"
#define SDW " dst_sel:DWORD dst_unused:UNUSED_PAD src0_sel:DWORD src1_sel:WORD_1\n\t"
#define FOLD "v_bitop3_b32 %6, %6, v[%15-9], v[%15-8] bitop3:0xfe\n\tv_or_b32 %6, v[%15-7], %6\n\t"
#define SCAT(PRE, POST, M) { uint32_t c0_, c1_, c2_, a0_, a1_, a2_; asm volatile( \
        FOLD "v_and_b32 %0, 0x3ff, %7\n\tv_lshrrev_b32 %2, 22, %7\n\t" PRE \
        "v_bitop3_b32 %0, %11, %8, %0 bitop3:0xea\n\tv_bitop3_b32 %2, %11, %10, %2 bitop3:0xea\n\tv_bfe_u32 %1, %7, 11, 10\n\t" \
        "v_fma_mix_f32 %3, %0, v[%15], %12" MIXL "v_bitop3_b32 %1, %11, %9, %1 bitop3:0xea\n\tv_fma_mix_f32 %0, %0, v[%15], %13" MIXH \
        "v_fma_mix_f32 %4, %1, v[%15+1], %12" MIXL "v_lshl_add_u32 %0, %0, 16, %3\n\tv_lshlrev_b32_sdwa %3, 2, %8" SDW \
        "v_fma_mix_f32 %1, %1, v[%15+1], %13" MIXH "ds_add_rtn_u32 v[%15-9], %3, %0 offset:%14\n\t" \
        "v_fma_mix_f32 %5, %2, v[%15+2], %12" MIXL "v_lshl_add_u32 %1, %1, 16, %4\n\tv_lshlrev_b32_sdwa %4, 2, %9" SDW \
        "v_fma_mix_f32 %2, %2, v[%15+2], %13" MIXH "ds_add_rtn_u32 v[%15-8], %4, %1 offset:%14\n\t" \
        "v_lshl_add_u32 %2, %2, 16, %5\n\tv_lshlrev_b32_sdwa %5, 2, %10" SDW \
        "ds_add_rtn_u32 v[%15-7], %5, %2 offset:%14" POST \
        : "=&v"(c0_), "=&v"(c1_), "=&v"(c2_), "=&v"(a0_), "=&v"(a1_), "=&v"(a2_), "+v"(guard) \
        : "v"(xw), "v"(w0), "v"(w1), "v"(w2), "v"(sgn), "s"(mg0), "s"(mg1), "n"(A_ACC), "n"(M), "s"(tm) : "memory"); }
    asm volatile("v_mov_b32 v110, 0\n\tv_mov_b32 v111, 0\n\tv_mov_b32 v112, 0\n\tv_mov_b32 v113, 0\n\tv_mov_b32 v114, 0\n\tv_mov_b32 v115, 0\n\t"
                 "v_mov_b32 v116, 0\n\tv_mov_b32 v117, 0\n\tv_mov_b32 v118, 0" ::: "v106", "v127");
    auto unmix = [&](int i) __attribute__((always_inline)) {
        const auto s0 = __builtin_amdgcn_permlane16_swap(ring[i][0], ring[i][2], false, false);
        const auto s1 = __builtin_amdgcn_permlane16_swap(ring[i][1], ring[i][3], false, false);
        ring[i][0] = s0[0]; ring[i][2] = s0[1]; ring[i][1] = s1[0]; ring[i][3] = s1[1];
    };
    auto word = [&](int i, int k) __attribute__((always_inline)) -> uint32_t {
        return i < NFAST && (i >> 1) != HP ? ring[(i & ~1) | (k >> 1)][(i & 1) * 2 + (k & 1)] : ring[i][k];
    };
    auto gather = [&](int R, int i, int p) __attribute__((always_inline)) {
        if ((i >> 1) == HP) unmix(i);
        const uint32_t w0 = word(i, 0 ^ R), w1 = word(i, 1 ^ R), w2 = word(i, 2 ^ R);
        if (p == 0) GAT3(119) else if (p == 1) GAT3(122) else GAT3(125)
    };
    auto scatter = [&](int R, int i, int p, u64 tm) __attribute__((always_inline)) {
        const uint32_t xw = word(i, 3 ^ R), w0 = word(i, 0 ^ R), w1 = word(i, 1 ^ R), w2 = word(i, 2 ^ R);
        if (i < NFAST) { if (p == 0) SCAT("", "", 119) else if (p == 1) SCAT("", "", 122) else SCAT("", "", 125) }
        else if (p == 0) SCAT("s_mov_b64 exec, %16\n\t", "\n\ts_mov_b64 exec, -1", 119)
        else SCAT("s_mov_b64 exec, %16\n\t", "\n\ts_mov_b64 exec, -1", 125)
    };
#define WAIT_L(N) asm volatile("s_waitcnt lgkmcnt(" #N ")" ::: "memory")
    int ntn;
    u64 tm0, tm1;
    uint32_t ta[2], sp = 0u;
    auto list1 = [&](int ff) __attribute__((always_inline)) {
        int dst;
        uint32_t en;
        ntn = tail_rank(tc.x, sp, ff, 0u, dst, en);
        asm volatile("ds_permute_b32 v106, %0, %1" :: "v"(dst), "v"(en) : "memory");
        if (__builtin_expect(__builtin_amdgcn_ballot_w64((tc.x & 2u) != 0u) != 0ull, 0)) {
            const int n2 = tail_rank(tc.x, sp, ff, 4u, dst, en);
            if (ntn + n2 <= 32) {
                const int r = ((dst >> 4) | ((dst & 12) << 2)) + ntn;
                const int x = __builtin_amdgcn_ds_permute((en & 7u) ? ((r & 15) << 4) | ((r >> 4) << 2) : 252, (en & 7u) ? (int)en : 0);
                asm volatile("v_cmpx_ne_u32 vcc, 0, %0\n\tv_mov_b32 v106, %0\n\ts_mov_b64 exec, -1\n\ts_nop 4" :: "v"(x) : "vcc");
                ntn += n2;
            } else ntn = 64;
        }
    };
    auto step = [&](auto RR, auto KK, int ff) __attribute__((always_inline)) {
        constexpr int R = decltype(RR)::value;
        constexpr bool first = decltype(KK)::value == 1, last = decltype(KK)::value == 2;
        constexpr int mdp = first ? MDF : MDP;
        gather(R, 0, 0);
        const int ntl = ntn;
        asm volatile("v_mov_b32_dpp %0, v106 quad_perm:[0,0,0,0] row_mask:0xf bank_mask:0xf\n\tv_mov_b32_dpp %1, v106 quad_perm:[1,1,1,1] row_mask:0xf bank_mask:0xf" : "=v"(ta[0]), "=v"(ta[1]));
        auto side = [&](int pl) __attribute__((always_inline)) {
            if (first && pl == ((mdp >> 20) & 15)) { ring[4] = load_main(4, ff, NNEAR > 0u); ring[5] = load_main(5, ff, NNEAR > 0u); }
            if (first && pl == ((mdp >> 24) & 15)) { ring[6] = load_main(6, ff); ring[7] = load_main(7, ff); }
            if (pl == ((mdp >> 12) & 15)) ring[NFAST] = load_tail(ta[0], tm0);
            if (pl == ((mdp >> 16) & 15) && ntl > 16) ring[NFAST + 1] = load_tail(ta[1], tm1);
            __builtin_amdgcn_sched_barrier(0);
            if (!last) load_md(ff + 1, pl, mdp);
            if (!first && !last && pl == 5 && ff == 6) {
                load_d(me);
                load_wl();
                if (tid == NT - 128) (void)__hip_atomic_fetch_add((GAS unsigned*)((GAS char*)dw + vconst<0>()), 0x1000000u << me, __ATOMIC_RELAXED, __HIP_MEMORY_SCOPE_AGENT);
            }
            if (last && pl == 5) load_m(me);
            if (!first && !last && pl == 1 && ff == 13 && wv == 15) { GAS unsigned* d_ = dw; asm volatile("s_nop 4\n\tglobal_load_dword v108, %0, %1 sc1" :: "v"(vconst<0>()), "s"(d_) : "memory"); }
            __builtin_amdgcn_sched_barrier(0);
        };
        side(8);
        gather(R, 1, 1);
        side(9);
        const uint32_t tcur = tc.x, spc = sp;
#pragma unroll
        for (int i = 0; i < NFAST - 1; i++) {
            gather(R, i + 2, (i + 2) % 3);
            if (i == 0) side(10);
            if (i == 0) WAIT_L(6); else if (i == 1) WAIT_L(9); else WAIT_L(12);
            scatter(R, i, i % 3, 0ull);
            if (!last && (i == 1 || i == 3)) { ring[i - 1] = load_main(i - 1, ff + 1); ring[i] = load_main(i, ff + 1); }
            if (!last && (i == 4 || i == 5)) ring[i] = load_main(i, ff + 1, first && NNEAR > 1u);
            side(i);
            if (i == 2) SETP(2);
            if (i == 3) SETP(1);
            if (i == 5) SETP(0);
            if (i == 0 && !last) {
                tc.x = __builtin_amdgcn_alignbit(tc.y, tc.x, 8u); tc.y = __builtin_amdgcn_alignbit(tc.z, tc.y, 8u);
                tc.z = __builtin_amdgcn_alignbit(tc.w, tc.z, 8u); tc.w >>= 8;
                sp = ff == 7 ? 0u : sp + ((tcur & 0x3cu) << 2);
                list1(ff + 1);
            }
            __builtin_amdgcn_sched_barrier(0);
        }
        if (last) asm volatile("" :: "v"(ring[NFAST + 1]));
        if (__builtin_expect(ntl > 16, 0)) {
            gather(R, NFAST + 1, 0);
            WAIT_L(0);
            scatter(R, NFAST + 1, 0, tm1);
        }
        if (__builtin_expect(ntl > 32, 0)) {
#pragma unroll 1
            for (int e = 2; e < 6; e++) {
                int dst;
                uint32_t en;
                if (tail_rank(tcur, spc, ff, (uint32_t)e & 4u, dst, en) <= (e & 3) * 16) continue;
                const int cmp = __builtin_amdgcn_ds_permute(dst, (int)en);
                ring[NFAST + 1] = load_tail((uint32_t)__builtin_amdgcn_ds_bpermute(((lane & ~3) | (e & 3)) << 2, cmp), tm1);
                gather(R, NFAST + 1, 0);
                WAIT_L(0);
                scatter(R, NFAST + 1, 0, tm1);
            }
        }
        SETP(3);
        WAIT_L(3);
        if (!last) {
            __builtin_amdgcn_s_barrier();
            store_md();
            SETP(0);
        }
        scatter(R, NFAST, 2, tm0);
        scatter(R, NFAST - 1, 1, 0ull);
        if (!last) {
            ring[6] = load_main(6, ff + 1);
            ring[7] = load_main(7, ff + 1);
            WAIT_L(6);
            __builtin_amdgcn_s_barrier();
            SETP(3);
        }
    };
    auto steps = [&](auto RR) __attribute__((always_inline)) {
        tc = tcnt[(tid >> 2) * 128 + j * 4 + h];
        u32x4 sv[2];
        float4 su[3];
        uint32_t e0;
        if (wv < 4) {
            sv[0] = (u32x4)(0u); sv[1] = sv[0];
            su[0] = make_float4(0.f, 0.f, 0.f, 0.f);
            if (lane < SLOTI / 256) { sv[0] = stat[tid * 2]; sv[1] = stat[tid * 2 + 1]; su[0] = ((const float4*)(ws + W_SUM))[tid]; }
            su[1] = ((const float4*)(ws + W_SUM))[256 + lane];
            su[2] = ((const float4*)(ws + W_SUM))[320 + lane];
            e0 = meta[M_E0 + wv];
        }
        const u32x2 extra = *(const u32x2*)(meta + M_LOG);
        load_md(0, -1, 0);
        {
            const u32x4 bi = {BIAS, BIAS, BIAS, BIAS};
#pragma unroll
            for (int k = 0; k < 6; k++) *(LDS u32x4*)(uintptr_t)(A_ACC + (k * NT + threadIdx.x) * 16) = bi;
            if (threadIdx.x < 128) *(LDS u32x4*)(uintptr_t)(A_ACC + (6 * NT + threadIdx.x) * 16) = bi;
            __builtin_amdgcn_sched_barrier(0);
        }
#pragma unroll
        for (int i = 0; i < 2; i++) ring[i] = load_main(i, 0);
        __builtin_amdgcn_sched_barrier(0);
        list1(0);
        if (wv < 4) {
            const uint32_t mxb = wave_max(max(max(sv[0].x & 0xffffu, sv[0].z & 0xffffu), max(sv[1].x & 0xffffu, sv[1].z & 0xffffu)));
            const uint32_t mnb = 0xffffu - (wave_max(max(max(sv[0].x, sv[0].z), max(sv[1].x, sv[1].z))) >> 16);
            const float s2 = wave_sum((__uint_as_float(sv[0].y) + __uint_as_float(sv[0].w)) + (__uint_as_float(sv[1].y) + __uint_as_float(sv[1].w)));
            const float s1 = wave_sum((su[0].x + su[0].y) + (su[0].z + su[0].w));
            const float c1 = wave_sum((su[1].x + su[1].y) + (su[1].z + su[1].w)) * (1.0f / (NWG * 512)), c2 = wave_sum((su[2].x + su[2].y) + (su[2].z + su[2].w)) * (1.0f / (NWG * 512));
            const float rng = fmaxf(fabsf(zf * s1 * c1) + ZSIG * sqrtf(zf * s2 * c2), 2.0f * bf2f(mxb)) * 6.103515625e-05f;
            const uint32_t eb = mxb ? (__float_as_uint(rng) + 0x7fffffu) >> 23 : 127u;
            const bool ok = !mxb || (mxb < 0x4700u && eb >= 107u && eb <= 140u && mnb >= ((eb + 8u) << 7) && c2 >= 0.015625f);
            if (lane == 0) {
                s_flag[8 + wv] = (eb & 0xffu) | (ok ? 0x100u : 0u) | (mxb ? 0x200u : 0u);
                s_flag[12 + wv] = __float_as_uint(__uint_as_float(((eb + e0) & 0xffu) << 23) * (1024.0f / 1023.0f));
                s_flag[wv] = 0u;
            }
        }
        store_md();
#pragma unroll
        for (int i = 2; i < 4; i++) ring[i] = load_main(i, 0);
        WAIT_L(0);
        __syncthreads();
        const uint32_t q0 = __builtin_amdgcn_readfirstlane(s_flag[8 + 2 * cp]), q1 = __builtin_amdgcn_readfirstlane(s_flag[9 + 2 * cp]);
        fast = (q0 & q1 & 0x100u) != 0u;
        need = (q0 >> 9) | ((q1 >> 9) << 1);
        more = (__builtin_amdgcn_readfirstlane(extra.x | extra.y) | (uint32_t)(T & 3)) != 0u;
        mg0 = (((q0 & 0xffu) + 9u) << 23) | 0x400000u;
        mg1 = ((((q1 & 0xffu) + 9u) << 23) | 0x400000u) + ((0u - (mg0 >> 16)) & 0xffffu);
        step(RR, IC<1>(), 0);
#pragma unroll 1
        for (int ff = 1; ff < 15; ff++) step(RR, IC<0>(), ff);
        step(RR, IC<2>(), 15);
    };
    auto irregular = [&](auto f) __attribute__((always_inline)) {
        const float fr = bf2f(freq[0]);
        const char* lg = recs + (size_t)LBASE * 128u;
        const uint16_t* tags = (const uint16_t*)(lg + (size_t)lcap * 128u);
        const uint32_t nl = meta[M_LOG], ns = meta[M_LOG + 1];
#pragma unroll 1
        for (uint32_t q = tid; q < nl * 8u; q += NT) {
            const uint32_t b = tags[q >> 3];
            if ((b & 127u) == (uint32_t)(h * 32 + j)) {
                const uint32_t* un = (const uint32_t*)(lg + (size_t)q * 16u);
                const uint32_t xw = un[3 ^ h];
#pragma unroll 1
                for (int k = 0; k < 3; k++) {
                    const uint32_t ww = un[k ^ h];
                    const float c = (float)((xw >> (k * 11)) & 0x3ffu) * (1.0f / 1023.0f);
                    if (!(ww & WNULL)) f(ww >> 16, ((ww & 0x3fffu) << 6) | (b >> 5), (ww & 0x8000u) ? -c : c);
                }
            }
        }
#pragma unroll 1
        for (uint32_t q = tid; q < ns + (uint32_t)(T & 3); q += NT) {
            const uint32_t t = q < ns ? ((const uint32_t*)tags)[-1 - (int)q] : (uint32_t)(T & ~3) + (q - ns);
            const uint32_t i = (uint32_t)iin[t], o = (uint32_t)iout[t];
            if (((o >> 6) & 31u) == (uint32_t)j && (i & 3u) == (uint32_t)h) f(((o >> 11) << 6) | (o & 63u), i, cosf(fr * bf2f(ang[t])));
        }
        __builtin_amdgcn_s_waitcnt(0x0f70);
    };

    if (h == 0) steps(IC<0>());
    else if (h == 1) steps(IC<1>());
    else if (h == 2) steps(IC<2>());
    else steps(IC<3>());
    if (fast) {
        if (more) irregular([&](uint32_t ol, uint32_t i, float cf) __attribute__((always_inline)) {
            const uint32_t md = mdc[(cp * 64u + (i & 63u)) * SLOTI + ((i >> 14) << 8) + ((i >> 4) & 0xfcu) + ((i >> 12) & 3u)];
            const float c = cf * (1023.0f / 16777216.0f);
            const uint32_t lo = __float_as_uint(__builtin_fmaf(c, h2f(md & 0xffffu), __uint_as_float(mg0)));
            const uint32_t hi = __float_as_uint(__builtin_fmaf(c, h2f(md >> 16), __uint_as_float(mg1)));
            if (cf == cf) guard |= atomicAdd(&acc[ol], (hi << 16) + lo);
            else guard |= HSLOW;
        });
    }
    if (wv == 15) asm volatile("ds_write_b32 %0, v108" :: "v"(vconst<A_FLAG + 20>()) : "memory");
    WAIT_L(0);
    asm volatile("v_or3_b32 %0, %0, v110, v111\n\tv_or3_b32 %0, %0, v112, v113\n\tv_or3_b32 %0, %0, v114, v115\n\tv_or3_b32 %0, %0, v116, v117\n\tv_or_b32 %0, v118, %0" : "+v"(guard));
    __syncthreads();
    const __amdgpu_buffer_rsrc_t rsP = __builtin_amdgcn_make_buffer_rsrc((void*)(partial + (size_t)j * 16 * SLOTO), 0, 16 * SLOTO * 4, 0x00020000);
    const char* pk = recs + (uint32_t)j * (SC * 128u);
    const __amdgpu_buffer_rsrc_t rsK = __builtin_amdgcn_make_buffer_rsrc((void*)pk, 0, (int)(MAINL * 128u), 0x00020000);
#define PKA (SSM * 128u)
#define PKB (32u * SC * 128u)
    auto pk_at = [&](uint32_t i) __attribute__((always_inline)) -> uint32_t {
        const uint32_t c = i >> 9;
        return (c < 12u ? (c >> 2) * SQ + (c & 3u) * SKM : 2u * SSM) * 128u + (i & 511u) * 16u;
    };
    auto factors = [&](int r0, int r1) __attribute__((always_inline)) {
#pragma unroll
        for (int r = r0; r < r1; r++) factor(r, r == 0);
    };
    bool room;
    {
        room = (__builtin_amdgcn_readfirstlane(s_flag[5]) >> (24 + (me ^ 1))) & 1u;
        const __amdgpu_buffer_rsrc_t rsW = __builtin_amdgcn_make_buffer_rsrc((void*)(pk + (uint32_t)cp * PKA + (uint32_t)h * PKB), 0, room ? (int)(MAINL * 128u) : 0, 0x00020000);
        uint32_t big = 0u;
        u32x4 v[7];
#pragma unroll
        for (int k = 0; k < 6; k++) v[k] = acc_vec(k * NT + tid);
        v[6] = (u32x4)(BIAS);
        if (wv < 2) v[6] = acc_vec(6 * NT + tid);
#pragma unroll
        for (int k = 0; k < 6; k++)
            __builtin_amdgcn_raw_buffer_store_b128(v[k], rsW, (uint32_t)(k * NT + tid - me * (SHARE / 4)) < SHARE / 4 ? 0xffffff00u : pk_at((uint32_t)tid) + ((k >> 1) * SQ + (k & 1) * 2u * SKM) * 128u, 0, AUX_SC1);
        if (wv < 2 && me != 7) __builtin_amdgcn_raw_buffer_store_b128(v[6], rsW, 2u * SSM * 128u + (uint32_t)tid * 16u, 0, AUX_SC1);
        __builtin_amdgcn_sched_barrier(0);
#pragma unroll
        for (int k = 0; k < 28; k++) { guard |= v[k >> 2][k & 3]; big |= v[k >> 2][k & 3] + 0x42004200u; }
        if (guard & HSLOW) s_flag[0] = 1u;
        if (big & 0x7c00u) s_flag[2] = 1u;
        if (big & 0x7c000000u) s_flag[3] = 2u;
        __builtin_amdgcn_sched_barrier(0);
        if (fin) factors(0, 3);
    }
    asm volatile("s_waitcnt vmcnt(0)" ::: "memory");
    __syncthreads();
    const bool redo = !fast || __builtin_amdgcn_readfirstlane(s_flag[0]) != 0u || (need & ~__builtin_amdgcn_readfirstlane(s_flag[2] | s_flag[3])) != 0u;
    const bool slow = redo || !room;
    if (slow) {
        float* fs = (float*)acc;
        float w[16];
        load_w(wdown, w);
        const float fr = bf2f(freq[0]);
#pragma unroll 1
        for (int e = 0; e < 2; e++) {
            if (redo) {
                __syncthreads();
                for (int i = tid; i < SLOTO; i += NT) fs[i] = 0.f;
                __syncthreads();
                const __amdgpu_buffer_rsrc_t rsO = __builtin_amdgcn_make_buffer_rsrc((void*)iout, 0, T * 4, 0x00020000);
                const __amdgpu_buffer_rsrc_t rsI = __builtin_amdgcn_make_buffer_rsrc((void*)iin, 0, T * 4, 0x00020000);
#pragma unroll 1
                for (int t0 = tid * 4; t0 < T; t0 += NT * 16) {
                    u32x4 o4[4], i4[4];
#pragma unroll
                    for (int g = 0; g < 4; g++) {
                        o4[g] = __builtin_amdgcn_raw_buffer_load_b128(rsO, (uint32_t)(t0 + g * NT * 4) * 4u, 0, 0);
                        i4[g] = __builtin_amdgcn_raw_buffer_load_b128(rsI, (uint32_t)(t0 + g * NT * 4) * 4u, 0, 0);
                    }
                    uint32_t hit = 0u;
#pragma unroll
                    for (int k = 0; k < 16; k++) hit |= (bfi(3u, i4[k >> 2][k & 3], o4[k >> 2][k & 3]) & 0x7c3u) == (uint32_t)(j * 64 + h) ? 1u << k : 0u;
#pragma unroll 1
                    for (; hit; hit &= hit - 1u) {
                        const int k = __builtin_ctz(hit), t = t0 + (k >> 2) * NT * 4 + (k & 3);
                        if (t >= T) break;
                        const uint32_t o = (uint32_t)iout[t];
                        uint32_t rr[4];
                        md_of(m[iin[t]], w, rr);
                        atomicAdd(&fs[((o >> 11) << 6) | (o & 63u)], cosf(fr * bf2f(ang[t])) * bf2f(cp ? (e ? rr[3] : rr[2]) : (e ? rr[1] : rr[0])));
                    }
                }
                __syncthreads();
            }
            const float un = __uint_as_float(s_flag[12 + 2 * cp + e]);
            for (int i = tid; i < SLOTO / 4; i += NT) {
                u32x4 v = acc_vec(i);
                if (!redo) {
#pragma unroll
                    for (int k = 0; k < 4; k++) v[k] = __float_as_uint((float)((int)((v[k] >> (16 * e)) & 0xffffu) - 0x4000) * un);
                }
                __builtin_amdgcn_raw_buffer_store_b128(v, rsP, (uint32_t)((e * 8 + me) * SLOTO * 4 + i * 16), 0, AUX_SC1);
            }
        }
        load_wl();
        load_d(me);
        load_m(me);
        factors(0, 3);
        asm volatile("s_waitcnt vmcnt(0)" ::: "memory");
        __syncthreads();
    }
    if (tid == NT - 64) {
        const unsigned inc = (slow ? 0x10001u : 1u) << me;
        unsigned v = __hip_atomic_fetch_add(dw, inc, __ATOMIC_RELAXED, __HIP_MEMORY_SCOPE_AGENT) + inc;
        while ((v & 0xffu) != 0xffu) {
            __builtin_amdgcn_s_sleep(1);
            v = __hip_atomic_load(dw, __ATOMIC_RELAXED, __HIP_MEMORY_SCOPE_AGENT);
        }
        s_flag[1] = v;
    }
    if (fin) factors(3, 6);
    __syncthreads();
    if (!fin) return;
    const unsigned sv = __builtin_amdgcn_readfirstlane(s_flag[1]);
    u32x4 p[8];
    {
        const uint32_t po = tid < SHARE / 4 ? pk_at((uint32_t)(me * (SHARE / 4) + tid)) : 0x7fffff00u;
#pragma unroll
        for (int r = 1; r < 8; r++) {
            const uint32_t k = (uint32_t)(me ^ r);
            p[r] = __builtin_amdgcn_raw_buffer_load_b128(rsK, po, ((sv >> (16u + k)) & 1u) ? MAINL * 128u : (k & 1u) * PKA + (k >> 1) * PKB, AUX_SC1);
        }
        __builtin_amdgcn_sched_barrier(0);
    }
    p[0] = (u32x4)(0u);
    if (!slow) p[0] = acc_vec(me * (SHARE / 4) + min(tid, SHARE / 4 - 1));
    uint32_t un[4];
#pragma unroll
    for (int c = 0; c < 4; c++) un[c] = __builtin_amdgcn_readfirstlane(s_flag[12 + c]);
    factors(6, 8);
    __builtin_amdgcn_sched_barrier(0);
    const unsigned flt = (sv >> 16) & 0xffu;
    auto finish = [&](int sh) __attribute__((always_inline)) {
        {
            const int ol = sh * SHARE + tid * 4;
            const uint32_t po = tid < SHARE / 4 ? (uint32_t)ol * 4u : 0x7fffff00u;
            float g[4][4];
            u32x4 a[2] = {p[0] + p[2], p[1] + p[3]}, b[2] = {p[4] + p[6], p[5] + p[7]};
            if (cp) { const u32x4 t = a[0], u = b[0]; a[0] = a[1]; b[0] = b[1]; a[1] = t; b[1] = u; }
#pragma unroll
            for (int c = 0; c < 2; c++) {
                const int nb = 0x10000 - 0x4000 * __builtin_popcount(flt & (0x55u << c));
                const float u0 = __uint_as_float(nb ? un[2 * c] : 0u), u1 = __uint_as_float(nb ? un[2 * c + 1] : 0u);
#pragma unroll
                for (int q = 0; q < 4; q++) {
                    g[q][2 * c] = (float)((int)((a[c][q] & 0xffffu) + (b[c][q] & 0xffffu)) - nb) * u0;
                    g[q][2 * c + 1] = (float)((int)((a[c][q] >> 16) + (b[c][q] >> 16)) - nb) * u1;
                }
            }
            if (__builtin_expect(flt != 0u, 0)) {
#pragma unroll
                for (int c = 0; c < 2; c++) {
#pragma unroll 1
                    for (int k = c; k < 8; k += 2) {
                        if (!((flt >> k) & 1u)) continue;
                        const u32x4 f0 = __builtin_amdgcn_raw_buffer_load_b128(rsP, po, k * SLOTO * 4, AUX_SC1);
                        const u32x4 f1 = __builtin_amdgcn_raw_buffer_load_b128(rsP, po, (8 + k) * SLOTO * 4, AUX_SC1);
#pragma unroll
                        for (int q = 0; q < 4; q++) { g[q][2 * c] += __uint_as_float(f0[q]); g[q][2 * c + 1] += __uint_as_float(f1[q]); }
                    }
                }
            }
            float wu[16];
#pragma unroll
            for (int k = 0; k < 8; k++) {
                const uint32_t x = __builtin_amdgcn_readlane(wl, 40 + k);
                wu[2 * k] = bf2f(x & 0xffffu); wu[2 * k + 1] = bf2f(x >> 16);
            }
            u32x4 ov[2];
#pragma unroll
            for (int q = 0; q < 4; q++) {
#pragma unroll
                for (int k = 0; k < 4; k++) g[q][k] *= fa[q][k >> 1][k & 1];
                const uint32_t m0 = em[q >> 1][(q & 1) * 2], m1 = em[q >> 1][(q & 1) * 2 + 1];
                const float mm[4] = {bf2f(m0 & 0xffff), bf2f(m0 >> 16), bf2f(m1 & 0xffff), bf2f(m1 >> 16)};
                float o[4];
#pragma unroll
                for (int k = 0; k < 4; k++) o[k] = mm[k] + (g[q][0] * wu[k] + g[q][1] * wu[4 + k] + g[q][2] * wu[8 + k] + g[q][3] * wu[12 + k]) * fa[q][2 + (k >> 1)][k & 1];
                ov[q >> 1][(q & 1) * 2] = pk_bf16(o[0], o[1]);
                ov[q >> 1][(q & 1) * 2 + 1] = pk_bf16(o[2], o[3]);
            }
            const __amdgpu_buffer_rsrc_t rsU = __builtin_amdgcn_make_buffer_rsrc((void*)out, 0, E * 8, 0x00020000);
            const uint32_t uo = edge0(sh) * 8u;
            __builtin_amdgcn_raw_buffer_store_b128(ov[0], rsU, uo, 0, AUX_NT);
            __builtin_amdgcn_raw_buffer_store_b128(ov[1], rsU, uo + 256u, 0, AUX_NT);
        }
    };
    finish(me);
}

extern "C" __global__ __launch_bounds__(256) void gen_scatter_kernel(const int* __restrict__ iin, const int* __restrict__ iout,
                                                          const uint16_t* __restrict__ ang, const uint16_t* __restrict__ freq,
                                                          const u32x2* __restrict__ m, const uint16_t* __restrict__ wdown,
                                                          float* __restrict__ agg, int T) {
    float w[16];
    load_w(wdown, w);
    const float fr = bf2f(freq[0]);
    for (long t = (long)blockIdx.x * 256 + threadIdx.x; t < T; t += (long)gridDim.x * 256) {
        const float c = cosf(fr * bf2f(ang[t]));
        uint32_t r[4];
        md_of(m[iin[t]], w, r);
        float* q = agg + (size_t)iout[t] * 4;
#pragma unroll
        for (int k = 0; k < 4; k++) atomicAdd(q + k, c * bf2f(r[k]));
    }
}
__global__ __launch_bounds__(256) void gen_final_kernel(const u32x2* __restrict__ m, const uint16_t* __restrict__ dist,
                                                        const uint16_t* __restrict__ centers, const uint16_t* __restrict__ wrbf,
                                                        const uint16_t* __restrict__ wsbf, const uint16_t* __restrict__ wup,
                                                        const float* __restrict__ agg, u32x2* __restrict__ out, int E) {
    const int e = blockIdx.x * 256 + threadIdx.x;
    if (e >= E) return;
    const float d = bf2f(dist[e]);
    float rs[4] = {0, 0, 0, 0}, rg[4] = {0, 0, 0, 0};
#pragma unroll 1
    for (int r = 0; r < 8; r++) {
        const float x = d - bf2f(centers[r]);
        const float rb = __expf(-10.0f * x * x);
#pragma unroll
        for (int k = 0; k < 4; k++) { rs[k] += rb * bf2f(wsbf[r * 4 + k]); rg[k] += rb * bf2f(wrbf[r * 4 + k]); }
    }
    float wu[16];
    load_w(wup, wu);
    const u32x2 mv = m[e];
    const float mm[4] = {bf2f(mv.x & 0xffff), bf2f(mv.x >> 16), bf2f(mv.y & 0xffff), bf2f(mv.y >> 16)};
    float g[4], o[4];
#pragma unroll
    for (int k = 0; k < 4; k++) g[k] = agg[(size_t)e * 4 + k] * rs[k];
#pragma unroll
    for (int k = 0; k < 4; k++) o[k] = mm[k] + (g[0] * wu[k] + g[1] * wu[4 + k] + g[2] * wu[8 + k] + g[3] * wu[12 + k]) * rg[k];
    const u32x2 ov = {f2bf(o[0]) | (f2bf(o[1]) << 16), f2bf(o[2]) | (f2bf(o[3]) << 16)};
    out[e] = ov;
}

extern "C" void kernel_launch(void* const* d_in, const int* in_sizes, int n_in,
                              void* d_out, int out_size, void* d_ws, size_t ws_size,
                              hipStream_t stream) {
    const u32x2* m = (const u32x2*)d_in[0];
    const int* iin = (const int*)d_in[1];
    const int* iout = (const int*)d_in[2];
    const uint16_t* dist = (const uint16_t*)d_in[3];
    const uint16_t* ang = (const uint16_t*)d_in[4];
    const uint16_t* centers = (const uint16_t*)d_in[5];
    const uint16_t* freq = (const uint16_t*)d_in[6];
    const uint16_t* wrbf = (const uint16_t*)d_in[7];
    const uint16_t* wsbf = (const uint16_t*)d_in[8];
    const uint16_t* wdown = (const uint16_t*)d_in[9];
    const uint16_t* wup = (const uint16_t*)d_in[10];
    const int E = in_sizes[3];
    const int T = in_sizes[1];
    char* ws = (char*)d_ws;
    const int G = T >> 2;
    int ntile = (G + GR - 1) / GR;
    const int nstat = ntile / NWG - NDYN > 4 ? ntile / NWG - NDYN : 4;
    if (ntile - NHALF / 2 > nstat * NWG) ntile -= NHALF / 2;
    const int lcap = G / 6 + 2;
    char* recs = ws + W_RECS;
    size_t off = W_RECS;
    const size_t rbytes = (size_t)LBASE * 128 + (size_t)lcap * 130;
    off += rbytes + 256 + (size_t)32 * 16 * SLOTO * 4;
    if (E > EMAX || (E & 3) || E < 1 || rbytes > 0x70000000u || off > ws_size) {
        (void)hipMemsetAsync(ws, 0, (size_t)E * 16, stream);
        gen_scatter_kernel<<<4096, 256, 0, stream>>>(iin, iout, ang, freq, m, wdown, (float*)ws, T);
        gen_final_kernel<<<(E + 255) / 256, 256, 0, stream>>>(m, dist, centers, wrbf, wsbf, wup, (const float*)ws, (u32x2*)d_out, E);
        return;
    }
    int ks = T / (NWG * NB * 24);
    if (ks > KMAX) ks = KMAX;
    const float zf = (float)T / (4.0f * (float)E * (float)E);
    (void)hipFuncSetAttribute((const void*)part_kernel, hipFuncAttributeMaxDynamicSharedMemorySize, P_LDS);
    (void)hipFuncSetAttribute((const void*)acc_kernel, hipFuncAttributeMaxDynamicSharedMemorySize, A_LDS);
    part_kernel<<<NWG, NT, P_LDS, stream>>>(iin, iout, ang, freq, m, wdown, recs, T, E, ntile, nstat, ks, lcap);
    acc_kernel<<<NWG, NT, A_LDS, stream>>>(ws, T, E, ks, lcap, zf, 0, centers, wrbf, wsbf, wup, m, dist, (u32x2*)d_out, iin, iout, ang, wdown, freq);
}
```
